```python
import torch

def _softmax(x, axis=-1):
    return torch.softmax(x, dim=axis)
def _gelu(x):
    return torch.nn.functional.gelu(x, approximate="tanh")
def _silu(x):
    return torch.nn.functional.silu(x)
def _sigmoid(x):
    return torch.sigmoid(x)
def _layernorm(x, axis=-1, eps=1e-5):
    m = x.mean(dim=axis, keepdim=True)
    v = x.var(dim=axis, keepdim=True, unbiased=False)
    return (x - m) / torch.sqrt(v + eps)
def _scatter_add(tgt, idx, src, axis=0):
    return tgt.clone().index_add_(axis, idx, src)
def _segment_sum(src, seg, n):
    out = torch.zeros((n,) + src.shape[1:], dtype=src.dtype)
    return out.index_add_(0, seg, src)
def _pad(x, pw):
    flat = tuple(v for pair in reversed(pw) for v in pair)
    return torch.nn.functional.pad(x, flat)

kernel_name = 'standalone__deep_cross_xdeepfm__recsys'

def setup_inputs():

    _w = (lambda a: (lambda t: t.to(torch.bfloat16) if t.is_floating_point() else t)(torch.as_tensor(a)))
    B, F, D, L = 4096, 26, 64, 3
    V = min(1000000, 100000)
    Din = F * D
    return {
        "idx": _w((lambda lo, hi, *s: torch.randint(lo, hi, s))(0, V, B, F).to(torch.int64)),
        "table": _w((lambda *s: torch.randn(*s, dtype=torch.bfloat16) * 0.8)(V, D)),
        "ws_cin": _w((lambda *s: torch.randn(*s, dtype=torch.bfloat16) * 0.2)(L, F, F, F) * (1.0 / F)),
        "w_cin_out": _w((lambda *s: torch.randn(*s, dtype=torch.bfloat16) * 0.2)(L * F) * ((L * F) ** -0.5)),
        "w_dnn1": _w((lambda *s: torch.randn(*s, dtype=torch.bfloat16) * 0.8)(Din, Din) * (Din ** -0.5)),
        "b_dnn1": _w((lambda *s: torch.randn(*s, dtype=torch.bfloat16) * 1.6)(Din) * 0.1),
        "w_dnn2": _w((lambda *s: torch.randn(*s, dtype=torch.bfloat16) * 0.2)(Din) * (Din ** -0.5)),
        "b_out": _w((lambda *s: torch.randn(*s, dtype=torch.bfloat16) * 0.2)(1)),
    }

def reference(idx, table, ws_cin, w_cin_out, w_dnn1, b_dnn1, w_dnn2, b_out):
    x0 = table[idx]
    B = x0.shape[0]
    L = ws_cin.shape[0]
    xk = x0
    pooled = []
    for l in range(L):
        xk = torch.einsum('bid,bjd,hij->bhd', xk, x0, ws_cin[l])
        pooled.append(xk.sum(axis=-1))
    p_plus = torch.stack(pooled, axis=1).reshape(B, -1)
    logit_cin = p_plus @ w_cin_out
    h = x0.reshape(B, -1)
    h = _gelu(h @ w_dnn1 + b_dnn1)
    logit_dnn = h @ w_dnn2
    return logit_cin + logit_dnn + b_out
```

```cpp
#define AMP_A 0
#define AMP_B 15
#define AMP_K 0
#define AMP_BLK 100
#include <hip/hip_runtime.h>
#include <hip/hip_bf16.h>
#include <stdint.h>
#include <utility>

typedef __attribute__((ext_vector_type(8))) short bf16x8;
typedef __attribute__((ext_vector_type(4))) short s16x4;
typedef __attribute__((ext_vector_type(4))) float f32x4;
typedef __attribute__((ext_vector_type(2))) float f32x2;
typedef __attribute__((ext_vector_type(2))) __bf16 bf16x2;
typedef __attribute__((ext_vector_type(16))) float f32x16;
typedef unsigned short u16;
typedef __attribute__((ext_vector_type(2))) unsigned u32x2;
typedef __attribute__((ext_vector_type(4))) unsigned u32x4;

constexpr int NB = 4096, NF = 26, ND = 64, DIN = 1664;

#define STAMP(k,i)
__device__ __forceinline__ unsigned now32() { unsigned long long t; asm volatile("s_memrealtime %0\n\ts_waitcnt lgkmcnt(0)" : "=s"(t) :: "memory"); return (unsigned)t; }
#define WSTAMP(i) do { if ((i) == 0) asm volatile("s_memrealtime %0" : "=s"(amp_0) :: "memory"); if ((i) == 15) { asm volatile("" : "+s"(amp_0)); if (AMP_A == 0) amp_a = (unsigned)amp_0; } \
  if ((i) == AMP_A && (i) != 0) amp_a = now32(); if ((i) == AMP_B) amp_b = now32(); \
  if ((i) == 10 && AMP_K && blockIdx.x == AMP_BLK && threadIdx.x < 64) { unsigned te = now32() + AMP_K * (amp_b - amp_a); while ((int)(now32() - te) < 0) __builtin_amdgcn_s_sleep(2); } } while (0)
template <class F, int... I>
__device__ __forceinline__ void static_for(F& f, std::integer_sequence<int, I...>) { (f(std::integral_constant<int, I>{}), ...); }
template <int CTRL, int RM = 0xf>
__device__ __forceinline__ float dpp(float v) { return __builtin_bit_cast(float, __builtin_amdgcn_update_dpp(0, __builtin_bit_cast(int, v), CTRL, RM, 0xf, false)); }
__device__ __forceinline__ unsigned pk2(float a, float b) { return __builtin_bit_cast(unsigned, __builtin_convertvector((f32x2){a, b}, bf16x2)); }

constexpr int BM = 128, BN = 208;
constexpr int A_BYTES = BM * 128;
constexpr int BROW = 448;
constexpr int W_BYTES = 64 * BROW;
constexpr int STAGE_BYTES = A_BYTES + W_BYTES;
constexpr int G_LDS = 163840;
constexpr int MB = G_LDS - 42 * 1024, MC = 41 * 1024;
constexpr int IDX_OFF = 3 * STAGE_BYTES;
constexpr int IP = 112;
constexpr int W2S = IDX_OFF + BM * IP, B1_OFF = W2S + NF * 8 * 64;
constexpr int XP = 160, XR = 28 * XP, XE = 106496, CS = XE + 2 * XR;
constexpr int W3S = 2 * STAGE_BYTES, U_OFF = W3S + 35840, NC_OFF = U_OFF + 2048, GT_OFF = NC_OFF + 1024, WO_OFF = GT_OFF + 2048, DP = 4352;
static_assert(B1_OFF + 832 <= G_LDS && CS + 2560 <= MB && WO_OFF + 256 <= IDX_OFF, "lds");
constexpr int NS = 5, NSR = 2, NCL = 4, NP = 8 - NS;
constexpr int WS_RESP = 4096, WS_POOL = 32768, WS_GRAN = 65536, WS_M = 1 << 20;

struct PI { int a, b; };
constexpr PI prod(int n) {
  int m = n - 2;
  for (int jj = 0; jj < 6; ++jj) {
    const int j = jj ? 6 - jj : 0;
    if (m < 2 + 2 * j) return m == 0 ? PI{2 * j, 2 * j} : m == 1 ? PI{2 * j, 1} : PI{(m - 2) & ~1, 2 * j + (m & 1)};
    m -= 2 + 2 * j;
  }
  return PI{0, 0};
}
constexpr int xfield(int k, int h, int g) { return k == 1 ? 24 + h : 4 * (k >> 1) + ((g + 2 * (k & 1) + h) & 3); }
constexpr int pqfield(int q, int sl, int g) { return g < 3 ? 8 * g + 4 * (sl >> 1) + (sl & 1) + (((g ^ q) & 1) << 1) : sl == 3 ? 26 : 24 + ((sl + q) >> 1); }
constexpr unsigned short slot_code(int f, int t, int g, int h) {
  if (f == 0) {
    const int sl = 2 * (t & 1) + h, q = t >> 1;
    return g == 3 && (q || (sl & 1)) ? 0x8000 : 0x4000 | 6 << 10 | (26 * 33 + pqfield(q, sl, g));
  }
  const int n = 4 * (f - 1) + t;
  const int p = n < 2 ? pqfield(0, 2 * n + h, g) : xfield(prod(n).a, 0, g), q = n < 2 ? pqfield(1, 2 * n + h, g) : xfield(prod(n).b, h, g);
  return (p == 26 ? 0x4000 | 10 << 10 : p == q ? 1 << 10 : 2 << 10) | (p * 33 + q);
}
struct GT { unsigned short t[2][64][8]; };
constexpr GT make_gt() {
  GT r{};
  for (int l = 0; l < 64; ++l)
    for (int e = 0; e < 8; ++e) {
      r.t[0][l][e] = l < 48 ? slot_code(l >> 2, e >> 1, l & 3, e & 1) : 0x8000;
      r.t[1][l][e] = l < 48 ? slot_code(2 * (l >> 3) + ((l >> 1) & 1), 2 * ((e >> 1) & 1) + ((l >> 2) & 1), 2 * (l & 1) + (e >> 2), e & 1) : 0x8000;
    }
  return r;
}
static __device__ const __attribute__((aligned(16))) GT g_tab = make_gt();

#define OJ(n) ((n) / 4)
#define OM(n) (((n) / 4) & 1 ? 3 - (n) % 4 : (n) % 4)
struct Frag { bf16x8 a[4]; s16x4 lo[7], hi[7]; };

__global__ __launch_bounds__(512) void dnn_kernel(const int* __restrict__ idx, const u16* __restrict__ table,
                                                  const u16* __restrict__ ws_cin, const u16* __restrict__ w1,
                                                  const u16* __restrict__ b1, const u16* __restrict__ w2,
                                                  const u16* __restrict__ wout, const u16* __restrict__ b_out,
                                                  char* __restrict__ ws, u16* __restrict__ out) {
  __shared__ __attribute__((aligned(1024))) char shm[G_LDS];
  const int tid = threadIdx.x, lane = tid & 63;
  const int w = __builtin_amdgcn_readfirstlane(tid >> 6);
  const int ntile = blockIdx.x >> 5, mtile = (blockIdx.x & 7) * 4 + ((blockIdx.x >> 3) & 3);
  const int b0 = mtile * BM, n0 = ntile * BN;
  const unsigned x8 = blockIdx.x & 7, nt = ntile;
  const int rot = 2 + ((((x8 * 3) >> 1) + 3 * (nt >> 1)) % 12) * 2, rt = ((rot - 2) & ~3) + 2;
  unsigned amp_a = 0, amp_b = 0; unsigned long long amp_0;
  WSTAMP(0);
#define SB __builtin_amdgcn_sched_barrier(0)
#define PIN(x) asm volatile("" : "+v"(x))
#define BAR asm volatile("s_barrier" ::: "memory")
  unsigned tch = 0, toff = ((blockIdx.x >> 3) * 16 + (tid & 15)) * 128;
  {
    unsigned long long pc;
    asm volatile("s_cmp_lg_u32 %3, 7\n\ts_cbranch_scc1 .Lxd_pc1\n\ts_getpc_b64 %1\n.Lxd_pc0:\n\tv_min_u32 %2, .Lxd_end-.Lxd_pc0-4, %2\n\tglobal_load_dword %0, %2, %1\n.Lxd_pc1:" : "+v"(tch), "=&s"(pc), "+v"(toff) : "s"(w) : "memory", "scc");
  }
  asm volatile("" :: "s"(idx), "s"(table), "s"(ws_cin), "s"(w1), "s"(b1), "s"(w2), "s"(wout), "s"(b_out), "s"(ws), "s"(out));
  WSTAMP(15);
  unsigned nonce, bo;
  asm volatile("s_load_dword %0, %2, %3\n\ts_load_dword %1, %4, 0" : "=&s"(nonce), "=&s"(bo) : "s"(ws), "s"(blockIdx.x * 4), "s"(b_out) : "memory");
  const unsigned sb = (unsigned)(size_t)(__attribute__((address_space(3))) char*)shm;
  auto set_m0 = [&](unsigned v) { asm volatile("" :: "{m0}"(v)); };
  const int g = lane >> 4, fr = lane & 15, hf = lane >> 5, l31 = lane & 31;
  constexpr unsigned TX = 0xA5C30F96u;
  auto chk = [](unsigned t) -> unsigned { return (t * 0x9E3779B1u) >> 24; };
  typedef __attribute__((ext_vector_type(4))) int i32x4;
  i32x4 a3[2];
  {
    unsigned io[2];
#pragma unroll
    for (int k = 0; k < 2; ++k) {
      const unsigned c = lane + 48 * k, r = (c * 9363u) >> 16, f = rt + 4 * (c - 7 * r);
      io[k] = (r * NF + min(f, f - NF)) * 4;
    }
    asm volatile("s_mov_b32 m0, %3\n\ts_nop 0\n\tglobal_load_lds_dwordx4 %0, %2\n\tglobal_load_lds_dwordx4 %1, %4 offset:768"
                 :: "v"(io[0]), "v"(io[1]), "s"(idx + (b0 + w * 16) * NF), "s"(sb + IDX_OFF + w * 16 * IP), "s"(idx + (b0 + w * 16) * NF - 192) : "memory");
  }
  const bool bld = (blockIdx.x & 31) == ntile, rblk = ntile >= 4;
  const int fi = (ntile & 3) * 8 + w, mi_ = min(fi, NF - 1);
  const unsigned tc = sb + (g & 1) * 32 + (fr & 3) * 8, h0 = 8 * (g >> 1) + (fr >> 2);
  unsigned ma_, ur[2], uw[6], fa[2][2];
  if (__builtin_expect(bld, 0)) {
    if (rblk) {
      const unsigned o3 = min(w * 4480, NF * NF * NF * 2 - 4480);
      asm volatile("s_mov_b32 m0, %2\n\ts_nop 0\n\tglobal_load_lds_dwordx4 %0, %1\n\tglobal_load_lds_dwordx4 %0, %1 offset:1024\n\tglobal_load_lds_dwordx4 %0, %1 offset:2048\n\t"
                   "global_load_lds_dwordx4 %0, %1 offset:3072\n\tglobal_load_lds_dwordx4 %0, %1 offset:3456"
                   :: "v"(lane * 16), "s"((const char*)(ws_cin + 2 * NF * NF * NF) + o3), "s"(sb + W3S + o3) : "memory");
      const unsigned rr = (ntile & 3) * 8 + ((lane >> 2) & 7);
      const unsigned vo = (hf * NF * NF + min(rr, 25u) * NF) * 2 + (lane & 3) * 16;
#pragma unroll
      for (int m = 0; m < 2; ++m) {
        const unsigned p = min(w + 8 * m, 12);
        asm volatile("s_mov_b32 m0, %2\n\ts_nop 0\n\tglobal_load_lds_dwordx4 %0, %1" :: "v"(vo), "s"(ws_cin + (NF + 2 * p) * NF * NF), "s"(sb + W2S + p * 1024) : "memory");
      }
      asm volatile("ds_write_b32 %0, %1" :: "v"(sb + U_OFF + tid * 4), "v"(0) : "memory");
    } else {
#pragma unroll
      for (int s = 0; s < 2; ++s)
        asm volatile("s_mov_b32 m0, %2\n\ts_nop 0\n\tglobal_load_lds_dwordx4 %0, %1" :: "v"(min(l31, NF - 1) * NF * 2 + hf * 16), "s"(ws_cin + mi_ * NF * NF + s * 16), "s"(sb + W3S + w * DP + s * 1024) : "memory");
    }
    if (w < 3) asm volatile("s_mov_b32 m0, %2\n\ts_nop 0\n\tglobal_load_lds_dwordx4 %0, %1 sc1" :: "v"(lane * 16), "s"(w ? (const char*)&g_tab + (w - 1) * 1024 : ws), "s"(sb + NC_OFF + w * 1024) : "memory");
    if (w == 3) asm volatile("s_mov_b32 m0, %2\n\ts_nop 0\n\tglobal_load_lds_dword %0, %1" :: "v"(min(lane, 38) * 4), "s"(wout), "s"(sb + WO_OFF) : "memory");
    {
      const int ty = ntile >> 2, f = lane >> 2;
      const bool sp = fi >= 24;
      int c0 = sp ? 5 : fi < 16 ? ty - 1 : 1, c1 = sp ? 16 : 3072, rho = sp ? 4 * (fi - 24) + 2 * ty : fi < 16 ? fi : 2 * (fi & 6) + (fi & 1) + 2 * ty;
      asm volatile("" : "+s"(c0), "+s"(c1), "+s"(rho));
      int t = (7 * (f >> 1) + c0) * 1024 + (f & 1) * c1 + ((lane & 3) * 16 + rho) * 16;
      PIN(t);
      ma_ = min((unsigned)t | (fi < 28 ? 0u : ~0u), 41u * 1024 + 1024);
      ma_ = lane < 48 ? ma_ : 43008;
    }
#pragma unroll
    for (int p = 0; p < 2; ++p) {
      ur[p] = sb + W3S + min(8 * g + 4 * p + (fr >> 2), NF - 1) * (NF * NF * 2) + w * 32 + (fr & 3) * 8;
      fa[0][p] = tc + W2S + ((h0 + 4 * p) * 8 + w) * 64; fa[1][p] = tc + W2S + (min(h0 + 16 + 4 * p, 25u) * 8 + w) * 64;
      PIN(ur[p]); PIN(fa[0][p]); PIN(fa[1][p]);
    }
#pragma unroll
    for (int m = 0; m < 6; ++m) {
      const unsigned e = (w + 8 * m) * 16 + fr;
      uw[m] = sb + U_OFF + e * 2 + ((e * 2521u) >> 16) * 12;
      PIN(uw[m]);
      if (m == 5) uw[m] = e < NF * NF ? uw[m] : 0x40000u;
    }
    PIN(uw[5]); PIN(ma_);
  }
  SB;

  const int mh = w & 1, kh = w >> 2, nh = ((w >> 1) ^ kh) & 1;
  int achunk[2];
  int boff[4];
#pragma unroll
  for (int r = 0; r < 4; ++r) {
    const int up = lane >= 28, c = (lane - 28 * up) ^ ((w & 1) << 1);
    boff[r] = ((8 * w + 2 * r + up) * DIN + n0 + min(c, 25) * 8) * 2 - r * 896;
    PIN(boff[r]);
  }
  const unsigned long long wmask = (w & 1) ? 0x00cffffffcffffffull : 0x003ffffff3ffffffull;
  constexpr unsigned WSTR = 64 * DIN * 2;
  const unsigned mA = sb + w * 2048, mW = sb + A_BYTES + w * 3584;
  const char* tab1 = (const char*)table - 1024;
  auto dmaA = [&](int id, int r) {
    const unsigned off = (unsigned)id * (ND * 2) + achunk[r];
    asm volatile("global_load_lds_dwordx4 %0, %1 offset:%2" :: "v"(off), "s"(r ? tab1 : (const char*)table), "i"(r * 1024) : "memory");
  };
  auto dmaW = [&](unsigned long long wb, int r) {
    asm volatile("s_mov_b64 exec, %3\n\tglobal_load_lds_dwordx4 %0, %1 offset:%2\n\ts_mov_b64 exec, -1" :: "v"(boff[r]), "s"(wb), "i"(r * 896), "s"(wmask) : "memory");
  };
  auto stageW = [&](int f) __attribute__((always_inline)) {
    const unsigned long long wb = (unsigned long long)(size_t)w1 + min((unsigned)(rot + f), (unsigned)(rot + f - NF)) * WSTR;
    asm volatile("s_mov_b32 m0, %0\n\ts_nop 0" :: "s"(mW + f * STAGE_BYTES) : "memory");
    dmaW(wb, 0); dmaW(wb, 1); dmaW(wb, 2); dmaW(wb, 3);
  };
  auto stageA = [&](int f) __attribute__((always_inline)) {
    asm volatile("s_mov_b32 m0, %0\n\ts_nop 0" :: "s"(mA + f * STAGE_BYTES) : "memory");
    dmaA(a3[0][f], 0); dmaA(a3[1][f], 1);
  };
  stageW(0); SB;
  unsigned aidx = sb + IDX_OFF + (w * 16 + (lane >> 3)) * IP + (rot - rt) * 4;
  PIN(aidx);
#pragma unroll
  for (int r = 0; r < 2; ++r) { achunk[r] = ((lane & 7) ^ ((4 * r + (lane >> 4)) & 7)) * 16; PIN(achunk[r]); }
  SB;
  asm volatile("s_waitcnt vmcnt(4)" : "+v"(tch) :: "memory");
  asm volatile("ds_read2_b64 %0, %4 offset1:1\n\tds_read2_b64 %1, %4 offset0:%5 offset1:%6\n\ts_waitcnt lgkmcnt(0)" : "=&v"(a3[0]), "=&v"(a3[1]), "+s"(nonce), "+s"(bo)
               : "v"(aidx), "i"(IP), "i"(IP + 1) : "memory");
  WSTAMP(1);
  stageA(0); SB;
  asm volatile("s_cmp_lg_u32 %3, 1\n\ts_cbranch_scc1 .Lb1%=\n\ts_mov_b32 m0, %2\n\ts_mov_b64 exec, 0x3ffffff\n\tglobal_load_lds_dwordx4 %0, %1\n\ts_mov_b64 exec, -1\n.Lb1%=:"
               :: "v"(lane * 16), "s"(b1 + n0), "s"(sb + B1_OFF), "s"(w) : "memory", "scc");
  unsigned w2r;
  asm volatile("global_load_ushort %0, %1, %2" : "=v"(w2r) : "v"(min(tid, BN - 1) * 2), "s"(w2 + n0) : "memory");
  u32x4 mrow;
  u32x2 rsp;
  unsigned msc;
  if (__builtin_expect(bld, 0)) {
    WSTAMP(11);
    BAR;
    WSTAMP(12);
    const unsigned wov = *(const unsigned*)(shm + WO_OFF + lane * 4);
    const u32x4 tb = *(const u32x4*)(shm + GT_OFF + (fi >= 24) * 1024 + lane * 16);
    bf16x8 P[2], Q[2];
    if (rblk) {
      s16x4 wl[6][2];
      u32x4 wa;
      unsigned wo2;
      asm volatile("ds_read2_b64 %0, %1 offset0:13 offset1:14" : "=v"(wa) : "v"(sb + WO_OFF + g * 16));
      asm volatile("ds_read_u16 %0, %1 offset:52" : "=v"(wo2) : "v"(sb + WO_OFF + lane * 2));
#pragma unroll
      for (int m = 0; m < 6; ++m)
#pragma unroll
        for (int p = 0; p < 2; ++p) asm volatile("ds_read_b64_tr_b16 %0, %1 offset:%2" : "=v"(wl[m][p]) : "v"(ur[p]), "i"(m * 256));
      u32x2 fl[2][2];
#pragma unroll
      for (int s = 0; s < 2; ++s)
#pragma unroll
        for (int p = 0; p < 2; ++p) asm volatile("ds_read_b64_tr_b16 %0, %1" : "=v"(fl[s][p]) : "v"(fa[s][p]));
      asm volatile("s_waitcnt lgkmcnt(0)" : "+v"(wa), "+v"(wo2), "+v"(wl[0][0]), "+v"(wl[0][1]), "+v"(wl[1][0]), "+v"(wl[1][1]), "+v"(wl[2][0]), "+v"(wl[2][1]), "+v"(wl[3][0]), "+v"(wl[3][1]),
                   "+v"(wl[4][0]), "+v"(wl[4][1]), "+v"(wl[5][0]), "+v"(wl[5][1]), "+v"(fl[0][0]), "+v"(fl[0][1]), "+v"(fl[1][0]), "+v"(fl[1][1]));
      const unsigned km = g < 3 ? ~0u : 0u;
      const u32x4 wm = {wa[0], wa[1] & km, wa[2] & km, wa[3] & km};
      f32x4 du[6];
#pragma unroll
      for (int m = 0; m < 6; ++m)
        du[m] = __builtin_amdgcn_mfma_f32_16x16x32_bf16(__builtin_bit_cast(bf16x8, wm), __builtin_shufflevector(wl[m][0], wl[m][1], 0, 1, 2, 3, 4, 5, 6, 7), (f32x4){0.f, 0.f, 0.f, 0.f}, 0, 0, 0);
      asm volatile("ds_write_b16 %0, %1" :: "v"(lane < NF ? sb + U_OFF + lane * 64 + 52 : 0x40000u), "v"(wo2) : "memory");
#pragma unroll
      for (int m = 0; m < 6; ++m) {
        asm volatile("ds_write_b16 %0, %1" :: "v"(uw[m]), "v"(pk2(du[m][0], du[m][0])) : "memory");
        asm volatile("" :: "v"(du[m]));
      }
      stageW(1); stageA(1);
      asm volatile("s_waitcnt lgkmcnt(0)\n\ts_barrier" ::: "memory");
      s16x4 ul[2][2];
#pragma unroll
      for (int s = 0; s < 2; ++s)
#pragma unroll
        for (int p = 0; p < 2; ++p)
          asm volatile("ds_read_b64_tr_b16 %0, %1 offset:%2" : "=v"(ul[s][p]) : "v"(tc + U_OFF + h0 * 64), "i"(s * 1024 + p * 256));
      const unsigned fmask = l31 < NF ? ~0u : 0u;
#pragma unroll
      for (int s = 0; s < 2; ++s) {
        u32x4 q4 = {fl[s][0][0] & fmask, fl[s][0][1] & fmask, fl[s][1][0] & fmask, fl[s][1][1] & fmask};
        Q[s] = __builtin_bit_cast(bf16x8, q4);
      }
      asm volatile("s_waitcnt lgkmcnt(0)" : "+v"(ul[0][0]), "+v"(ul[0][1]), "+v"(ul[1][0]), "+v"(ul[1][1]));
#pragma unroll
      for (int s = 0; s < 2; ++s) P[s] = __builtin_shufflevector(ul[s][0], ul[s][1], 0, 1, 2, 3, 4, 5, 6, 7);
    } else {
      stageW(1); stageA(1);
#pragma unroll
      for (int s = 0; s < 2; ++s) {
        u32x4 xf = *(const u32x4*)(shm + W3S + w * DP + s * 1024 + lane * 16);
        if (s == 1) { const unsigned m = hf ? 0u : ~0u; xf[1] &= m; xf[2] &= m; xf[3] &= m; }
        Q[s] = __builtin_bit_cast(bf16x8, xf);
        const unsigned e = (l31 >> 1) - 8 * s - 4 * hf, v = 0x3f80u << ((lane & 1) * 16);
        u32x4 q4 = {e == 0 ? v : 0u, e == 1 ? v : 0u, e == 2 ? v : 0u, e == 3 ? v : 0u};
        P[s] = __builtin_bit_cast(bf16x8, q4);
      }
    }
    f32x16 D = {};
#pragma unroll
    for (int s = 0; s < 2; ++s) {
      D = __builtin_amdgcn_mfma_f32_32x32x16_bf16(P[s], Q[s], D, 0, 0, 0);
      D = __builtin_amdgcn_mfma_f32_32x32x16_bf16(Q[s], P[s], D, 0, 0, 0);
    }
    float* dl = (float*)(shm + W3S + w * DP);
    D[14] = lane == NF ? __uint_as_float(((unsigned)__builtin_amdgcn_readlane(wov, mi_ >> 1) >> ((mi_ & 1) * 16)) << 16) : D[14];
#pragma unroll
    for (int r = 0; r < 15; ++r) dl[((r & 3) + 8 * (r >> 2) + 4 * hf) * 33 + l31] = D[r];
    __builtin_amdgcn_wave_barrier();
    float v[8], mx = 0.f;
    const unsigned zm = fi >= NF ? 0xffff : rblk ? 0x8000 : 0xc000;
#pragma unroll
    for (int e = 0; e < 8; ++e) v[e] = dl[(tb[e >> 1] >> (16 * (e & 1))) & 0x3ff];
#pragma unroll
    for (int e = 0; e < 8; ++e) {
      const unsigned c = tb[e >> 1] >> (16 * (e & 1));
      float x = __builtin_amdgcn_ldexpf(v[e], ((c >> 10) & 15) - 2);
      PIN(x);
      v[e] = c & zm ? 0.f : x;
      mx = fmaxf(mx, fabsf(v[e]));
    }
    mx = fmaxf(mx, dpp<0xB1>(mx)); mx = fmaxf(mx, dpp<0x4E>(mx)); mx = fmaxf(mx, dpp<0x141>(mx)); mx = fmaxf(mx, dpp<0x140>(mx));
    const unsigned mxu = max(max((unsigned)__builtin_amdgcn_readlane(__float_as_int(mx), 0), (unsigned)__builtin_amdgcn_readlane(__float_as_int(mx), 16)), (unsigned)__builtin_amdgcn_readlane(__float_as_int(mx), 32));
    const unsigned ex = min(max(mxu >> 23, 40u), 250u);
    const float al = __uint_as_float((268 - ex) << 23);
#pragma unroll
    for (int e = 0; e < 4; ++e) {
      const unsigned q = pk2(v[2 * e] * al, v[2 * e + 1] * al);
      mrow[e] = __builtin_bit_cast(unsigned, __builtin_amdgcn_cvt_pkrtz(__uint_as_float(q << 16), __uint_as_float(q & 0xffff0000u)));
    }
    msc = fi < NF ? (ex - 14) << 23 : 0u;
    PIN(msc);
    asm volatile("global_store_dwordx4 %0, %1, %2 sc1" :: "v"(ma_), "v"(mrow), "s"(ws + WS_M) : "memory");
    asm volatile("global_store_dword %0, %1, %2 sc1" :: "v"((ntile * 8 + w) * 4), "v"(msc), "s"(ws + WS_M + MC) : "memory");
    if (fi == 31) asm volatile("global_store_dword %0, %1, %2 sc1\n\ts_waitcnt vmcnt(0)" :: "v"((lane & 15) * 128), "v"(msc), "s"(ws + WS_POOL) : "memory");
    const unsigned mval = *(const unsigned*)(shm + NC_OFF + (tid & 255) * 4), qval = *(const unsigned*)(shm + NC_OFF);
    rsp = (u32x2){qval << 8 | chk(mval), mval ^ TX};
    PIN(rsp);
    WSTAMP(14);
    asm volatile("s_waitcnt vmcnt(8) lgkmcnt(0)\n\ts_barrier" ::: "memory");
  } else {
    stageW(1); stageA(1);
    asm volatile("s_waitcnt vmcnt(6)\n\ts_barrier" ::: "memory");
  }
  WSTAMP(3);
  u32x2 rbb[7];
#pragma unroll
  for (int c = 0; c < 7; ++c)
    asm volatile("ds_read_b64 %0, %1 offset:%2" : "=v"(rbb[c]) : "v"(sb + B1_OFF + nh * 224 + g * 8), "i"(c * 32) : "memory");

  typedef __attribute__((ext_vector_type(2))) int i32x2;
  i32x2 ai[2];
  auto read_idx = [&](int o) {
#pragma unroll
    for (int r = 0; r < 2; ++r)
      asm volatile("ds_read_b64 %0, %1 offset:%2" : "=v"(ai[r]) : "v"(aidx), "i"(o * 4 + r * 8 * IP));
  };
  const int gb = g & 1;
  const unsigned a_addr = sb + kh * 4096 + (mh * 64 + fr) * 128 + (((kh * 4 + g) ^ (fr >> 1)) << 4);
  const unsigned b_row = sb + A_BYTES + (kh * 32 + 8 * g + (fr >> 2)) * BROW + 8 * (fr & 3);
  const unsigned b_addrE = b_row + (nh ? 192 + 32 * (gb ^ 1) : 32 * gb);
  const unsigned b_addrO = b_row + (nh ? 256 + 32 * gb : 32 * (gb ^ 1));

  unsigned ra[2][2], rb[2][2];
  ra[0][0] = a_addr; ra[0][1] = a_addr - 8192 * kh; rb[0][0] = b_addrE; rb[0][1] = b_addrO;
#pragma unroll
  for (int q = 0; q < 2; ++q) {
    ra[1][q] = ra[0][q] + 2 * STAGE_BYTES; rb[1][q] = rb[0][q] + 2 * STAGE_BYTES;
    asm volatile("" : "+v"(ra[0][q]), "+v"(ra[1][q]), "+v"(rb[0][q]), "+v"(rb[1][q]));
  }
  auto rdA = [&](Frag& F, int buf, int mi) {
    asm volatile("ds_read_b128 %0, %1 offset:%2" : "=v"(F.a[mi]) : "v"(ra[buf >> 1][mi >> 1]), "i"((buf & 1) * STAGE_BYTES + mi * 2048));
  };
  auto rdBl = [&](Frag& F, int buf, int j) {
    asm volatile("ds_read_b64_tr_b16 %0, %1 offset:%2" : "=v"(F.lo[j]) : "v"(rb[buf >> 1][j & 1]), "i"((buf & 1) * STAGE_BYTES + (j >> 1) * 64));
  };
  auto rdBh = [&](Frag& F, int buf, int j) {
    asm volatile("ds_read_b64_tr_b16 %0, %1 offset:%2" : "=v"(F.hi[j]) : "v"(rb[buf >> 1][j & 1]), "i"((buf & 1) * STAGE_BYTES + (j >> 1) * 64 + 4 * BROW));
  };

  f32x4 acc[4][7];

  auto mm1 = [&](Frag& F, int j, int mi) {
    bf16x8 bfr = __builtin_shufflevector(F.lo[j], F.hi[j], 0, 1, 2, 3, 4, 5, 6, 7);
    acc[mi][j] = __builtin_amdgcn_mfma_f32_16x16x32_bf16(bfr, F.a[mi], acc[mi][j], 0, 0, 0);
  };
  auto mmt = [&](bool first, Frag& F, int j, int mi) {
    if (first) { mm1(F, j, mi); return; }
    bf16x8 bfr = __builtin_shufflevector(F.lo[j], F.hi[j], 0, 1, 2, 3, 4, 5, 6, 7);
    asm volatile("v_mfma_f32_16x16x32_bf16 %0, %1, %2, %0" : "+v"(acc[mi][j]) : "v"(bfr), "v"(F.a[mi]));
  };

  Frag F0, F1;
#pragma unroll
  for (int mi = 0; mi < 4; ++mi) rdA(F0, 0, mi);
#pragma unroll
  for (int j = 0; j < 7; ++j)
    if (j < 6 || nh == 0) { rdBl(F0, 0, j); rdBh(F0, 0, j); }
  read_idx(2);
  SB; stageW(2); stageA(2); SB;
  unsigned xa[4];
  auto xoff = [&](int v) { return 12288 * v + 2048 * ((0x32222210 >> (4 * v)) & 3); };
  const int nsb = rblk ? NSR : 4, ns = bld ? nsb : NS;
  const unsigned xb = sb + xoff(w ^ 6);
  unsigned xs[26], xr[2], xwa;
  {
    const int sl = fr >> 2, c8 = (fr & 3) * 8;
    xa[1] = ((g + sl) & 3) * XP + c8;
    xa[0] = sl < 2 ? xa[1] : (22 + sl) * XP + c8;
#pragma unroll
    for (int q = 0; q < 2; ++q) {
      int fa = 8 * g + 4 * (sl >> 1) + (sl & 1) + (((g ^ q) & 1) << 1), fb = 24 + (q ? (sl + 1) >> 1 : max(sl - 1, 0));
      PIN(fa); PIN(fb);
      xa[2 + q] = (g < 3 ? fa : fb) * XP + c8;
    }
    xwa = xb + hf * (w < ns ? XR : 0x40000) + l31 * 4;
    xr[0] = sb + (ntile * 16 + w + hf * 8) * 128 + (((l31 >> 2) ^ ((w >> 1) + hf * 4)) << 4) + (lane & 3) * 4;
    xr[1] = xr[0] + 2 * STAGE_BYTES;
    asm volatile("" : "+v"(xa[0]), "+v"(xa[1]), "+v"(xa[2]), "+v"(xa[3]), "+v"(xwa), "+v"(xr[0]), "+v"(xr[1]));
    asm volatile("ds_read_b32 %0, %1" : "=v"(xs[0]) : "v"(xr[0]) : "memory");
#pragma unroll
    for (int k = 2; k < 20; ++k) asm volatile("" : "=v"(xs[k]));
  }
  asm volatile("s_waitcnt lgkmcnt(15)" : "+v"(rbb[0]), "+v"(rbb[1]), "+v"(rbb[2]), "+v"(rbb[3]), "+v"(rbb[4]), "+v"(rbb[5]), "+v"(rbb[6]) :: "memory");
  SB;
#pragma unroll
  for (int i = 0; i < 4; ++i)
#pragma unroll
    for (int j = 0; j < 7; ++j) {
      const u32x4 bv = {rbb[j][0] << 16, rbb[j][0] & 0xffff0000u, rbb[j][1] << 16, rbb[j][1] & 0xffff0000u};
      acc[i][j] = i < 2 ? __builtin_bit_cast(f32x4, bv) : (f32x4){0.f, 0.f, 0.f, 0.f};
    }
  SB;
  WSTAMP(4);

#define WAITF(F) "+v"(F.a[0]), "+v"(F.a[1]), "+v"(F.a[2]), "+v"(F.a[3]), "+v"(F.lo[0]), "+v"(F.lo[1]), "+v"(F.lo[2]), "+v"(F.lo[3]), "+v"(F.lo[4]), "+v"(F.lo[5]), "+v"(F.lo[6]), \
                 "+v"(F.hi[0]), "+v"(F.hi[1]), "+v"(F.hi[2]), "+v"(F.hi[3]), "+v"(F.hi[4]), "+v"(F.hi[5]), "+v"(F.hi[6])
#define WAITG(F) "+v"(F.a[0]), "+v"(F.a[1]), "+v"(F.a[2]), "+v"(F.a[3]), "+v"(F.lo[0]), "+v"(F.lo[1]), "+v"(F.lo[2]), "+v"(F.lo[3]), "+v"(F.lo[4]), "+v"(F.lo[5]), \
                 "+v"(F.hi[0]), "+v"(F.hi[1]), "+v"(F.hi[2]), "+v"(F.hi[3]), "+v"(F.hi[4]), "+v"(F.hi[5])
  int tl = tid;
  unsigned wofs = (rot + 3 - (rot < NF - 3 ? NF : 2 * NF)) * WSTR;
  const unsigned long long wendm = (unsigned long long)(size_t)w1 + NF * WSTR - (1ull << 32);
  unsigned long long wb = wendm + wofs, wbn;
  unsigned long long nhm;
  asm volatile("s_cmp_lg_u32 %1, 0\n\ts_cselect_b64 %0, -1, 0" : "=s"(nhm) : "s"(nh) : "scc");
  unsigned nn, nz, tkv;
  auto claim = [&](int k) __attribute__((always_inline)) {
    asm volatile("s_cmp_lg_u32 %1, %4\n\ts_cbranch_scc1 .Ltk%=\n\ts_mov_b64 exec, 1\n\tglobal_atomic_add %0, %2, %0, %3 sc0 sc1\n\ts_mov_b64 exec, -1\n.Ltk%=:"
                 : "+v"(tkv) : "s"(w), "v"(nz), "s"(ws + WS_POOL + (blockIdx.x >> 4) * 128), "i"(k) : "memory", "scc");
  };
  unsigned long long fx;
  unsigned bx8 = WS_RESP + blockIdx.x * 8;
  asm volatile("" : "+s"(bx8));
  unsigned long long* flg = (unsigned long long*)(ws + bx8);
  unsigned mxl, mxh, em, off, s2h, qq;
  auto conv = [&](int k) __attribute__((always_inline)) {
    if (k < 6) {
      const unsigned a = xs[4 * k], b = xs[4 * k + 1], c = xs[4 * k + 2], d = xs[4 * k + 3];
      if (k == 0) asm volatile("v_max_f16_e64 %0, |%2|, |%3|\n\tv_max_f32_e64 %1, |%2|, |%3|\n\tv_max3_f16 %0, |%4|, |%5|, %0\n\tv_max3_f32 %1, |%4|, |%5|, %1" : "=&v"(mxl), "=&v"(mxh) : "v"(a), "v"(b), "v"(c), "v"(d));
      else asm volatile("v_max3_f16 %0, |%2|, |%3|, %0\n\tv_max3_f32 %1, |%2|, |%3|, %1\n\tv_max3_f16 %0, |%4|, |%5|, %0\n\tv_max3_f32 %1, |%4|, |%5|, %1" : "+v"(mxl), "+v"(mxh) : "v"(a), "v"(b), "v"(c), "v"(d));
    }
    if (k == 6) asm volatile("v_max3_f16 %0, |%2|, |%3|, %0\n\tv_max3_f32 %1, |%2|, |%3|, %1" : "+v"(mxl), "+v"(mxh) : "v"(xs[24]), "v"(xs[25]));
    if (k == 7) asm volatile("v_lshlrev_b32 %1, 16, %1\n\tv_max3_u32 %0, %1, %2, %3\n\ts_nop 1\n\tv_max_u32_dpp %0, %0, %0 quad_perm:[1,0,3,2] row_mask:0xf bank_mask:0xf" : "=&v"(em), "+v"(mxl) : "v"(mxh), "s"(123u << 23));
    if (k == 8) asm volatile("s_nop 1\n\tv_max_u32_dpp %0, %0, %0 quad_perm:[2,3,0,1] row_mask:0xf bank_mask:0xf\n\ts_nop 1\n\tv_max_u32_dpp %0, %0, %0 row_half_mirror row_mask:0xf bank_mask:0xf" : "+v"(em));
    if (k == 9) asm volatile("s_nop 1\n\tv_max_u32_dpp %0, %0, %0 row_mirror row_mask:0xf bank_mask:0xf\n\tv_mov_b32 %1, %0\n\ts_nop 1\n\tv_permlane16_swap_b32 %0, %1\n\tv_max_u32 %0, %0, %1\n\tv_lshrrev_b32 %0, 23, %0" : "+v"(em), "=&v"(mxl));
    if (k == 10) { off = __umul24(em, 0x800080u) - 22 * 0x800080u; PIN(off); }
    if (k >= 11 && k < 37) {
      unsigned t;
      asm volatile("v_and_b32 %1, %2, %0\n\tv_pk_sub_u16 %1, %1, %3 clamp\n\tv_lshlrev_b32 %1, 3, %1\n\tv_bfi_b32 %0, %2, %1, %0" : "+v"(xs[k - 11]), "=&v"(t) : "s"(0x7fff7fffu), "v"(off));
    }
    if (k == 37) { const float t = __uint_as_float((257 - em) << 23); s2h = __builtin_bit_cast(unsigned, __builtin_amdgcn_cvt_pkrtz(t, t)); PIN(s2h); }
  };
  constexpr int CVA = 4, CV2 = 18, CV3 = 34;
  constexpr int K0 = 11, KD = 12, KL = (23 - K0 + KD - 1) / KD;
  auto dmaM = [&](int k) __attribute__((always_inline)) {
    asm volatile("global_load_lds_dwordx4 %0, %1 offset:%2 sc1" :: "v"((tl & 63) * 16), "s"(ws + WS_M + w * 5376 + (k & 4) * 1024), "i"(k == 5 ? 256 : (k & 3) * 1024) : "memory");
  };
  auto hook = [&](int mode, int par, int fs, int n) __attribute__((always_inline)) {
    if (n == (mode == 4 ? 0 : 16)) {
      const int bf = (fs + 1) % 3;
      if (mode == 0 && par != 2)
        asm volatile("v_mov_b32 %0, %1\n\tv_mov_b32 %1, %2\n\tds_read_b32 %2, %3 offset:%4" : "+v"(xs[fs + 1]), "+v"(xs[fs + 7]), "+v"(xs[fs + 13]) : "v"(xr[bf >> 1]), "i"((bf & 1) * STAGE_BYTES) : "memory");
      else asm volatile("ds_read_b32 %0, %1 offset:%2" : "=v"(xs[par == 2 ? 1 : fs + 19]) : "v"(xr[bf >> 1]), "i"((bf & 1) * STAGE_BYTES) : "memory");
    }
    if (mode == 2 && n == 24) asm volatile("global_load_dwordx2 %0, %1, %2 sc1" : "=v"(fx) : "v"((tl & 7) * 2048), "s"(flg) : "memory");
    if (mode == 3 && n < 24 && n % 4 == 1) conv(n / 4);
    if (mode == 4) {
      if (n == 0) {
        const unsigned long long want = (unsigned long long)(nonce ^ TX) << 32 | chk(nonce);
        PIN(fx);
        if (__builtin_expect(__builtin_amdgcn_ballot_w64((fx & 0xffffffff000000ffull) != want) != 0, 0)) {
#pragma unroll 1
          for (int spin = 0; spin < (1 << 24); ++spin) {
            __builtin_amdgcn_s_sleep(1);
            asm volatile("global_load_dwordx2 %0, %1, %2 sc1\n\ts_waitcnt vmcnt(0)" : "=v"(fx) : "v"((tl & 7) * 2048), "s"(flg) : "memory");
            if (__builtin_amdgcn_ballot_w64((fx & 0xffffffff000000ffull) != want) == 0) break;
          }
        }
        qq = __builtin_amdgcn_readfirstlane((unsigned)fx) >> 8;
      }
      if (n == 23) {
        nn = nonce + 1;
        PIN(nn);
        asm volatile("s_cmp_lg_u32 %0, 7\n\ts_cbranch_scc1 .Lga%=\n\ts_mov_b64 exec, 1\n\tglobal_store_dword %1, %2, %3 sc1\n\ts_mov_b64 exec, -1\n.Lga%=:"
                     :: "s"(w), "v"(nz), "v"(nn), "s"(ws + blockIdx.x * 4) : "memory", "scc");
      }
      if (n == 1) { nz = 0; tkv = 1; PIN(nz); PIN(tkv); }
      if (n == 2) asm volatile("v_lshlrev_b32 %0, 16, %0\n\tds_write_b32 %1, %0" : "+v"(w2r) : "v"(sb + CS + 1536 + min(tl, BN - 1) * 4) : "memory");
      if (n >= K0 && n < 24 && (n - K0) % KD == 0) claim((n - K0) / KD);
      if (n == 16) asm volatile("ds_write_b32 %0, %1" :: "v"(sb + CS + 768), "v"(nz) : "memory");
      if (n == CVA) asm volatile("s_waitcnt lgkmcnt(%0)" :: "i"(CVA + 1) : "memory");
      if (n == 1 || n == 17) asm volatile("s_mov_b32 m0, %0" :: "s"(sb + MB + w * 5376 + (n == 17) * 4096) : "memory");
      if (n < 24 && n % 4 == 2) dmaM(n / 4);
      if (n >= CVA && n < 24) for (int k = 6 + (n - CVA) * (CV2 - 6) / (24 - CVA); k < 6 + (n + 1 - CVA) * (CV2 - 6) / (24 - CVA); ++k) conv(k);
    }
    if (mode < 3) {
      if (n == 1) set_m0(mA + (fs % 3) * STAGE_BYTES);
      if (n == 2) dmaA(ai[0][(fs + 3) & 1], 0);
      if (n == 6) dmaA(ai[1][(fs + 3) & 1], 1);
      if (n == 8) set_m0(mW + (fs % 3) * STAGE_BYTES);
      if (n == 10) dmaW(wb, 0);
      if (n == 20 && mode < 2) { wbn = wendm + wofs; asm volatile("" : "+s"(wbn)); }
      if (n == 24 && mode < 2) wb = wbn;
      if (n == 12 && mode < 2) asm volatile("s_add_u32 %0, %0, %1\n\ts_cselect_b32 %0, %2, %0" : "+s"(wofs) : "i"(WSTR), "s"(0u - NF * WSTR) : "scc");
      if (n == 14) dmaW(wb, 1);
      if (n == 18) dmaW(wb, 2);
      if (n == 22) dmaW(wb, 3);
      if (n == 7 && mode < 2 && !(fs & 1)) read_idx(fs + 4);
    }
  };
  auto tail6 = [&](Frag& P, Frag& Q, int buf) __attribute__((always_inline)) {
    bf16x8 bfr = __builtin_shufflevector(P.lo[6], P.hi[6], 0, 1, 2, 3, 4, 5, 6, 7);
    register unsigned long long nhv asm("vcc") = nhm;
    asm volatile("s_cbranch_vccnz .Lta%=\n\t"
                 "ds_read_b64_tr_b16 %4, %11 offset:%13\n\tds_read_b64_tr_b16 %5, %11 offset:%14\n\t"
                 "v_mfma_f32_16x16x32_bf16 %0, %10, %6, %0\n\tv_mfma_f32_16x16x32_bf16 %1, %10, %7, %1\n\t"
                 "v_mfma_f32_16x16x32_bf16 %2, %10, %8, %2\n\tv_mfma_f32_16x16x32_bf16 %3, %10, %9, %3\n\t"
                 ".Lta%=:"
                 : "+v"(acc[0][6]), "+v"(acc[1][6]), "+v"(acc[2][6]), "+v"(acc[3][6]), "=&v"(Q.lo[6]), "=&v"(Q.hi[6])
                 : "v"(P.a[0]), "v"(P.a[1]), "v"(P.a[2]), "v"(P.a[3]), "v"(bfr), "v"(rb[buf >> 1][0]), "s"(nhv),
                   "i"((buf & 1) * STAGE_BYTES + 192), "i"((buf & 1) * STAGE_BYTES + 192 + 4 * BROW) : "memory");
  };
#define ITER(FS, P, Q, MODE, PAR, VMW)                                                                  \
  {                                                                                                \
    if (PAR == 2) asm volatile("s_waitcnt vmcnt(" #VMW ") lgkmcnt(0)" : WAITF(P), "+v"(ai[0]), "+v"(ai[1]) :: "memory"); \
    if (MODE < 3 && PAR != 2) asm volatile("s_waitcnt vmcnt(" #VMW ") lgkmcnt(0)" : WAITG(P), "+v"(ai[0]), "+v"(ai[1]) :: "memory"); \
    if (MODE > 2) asm volatile("s_waitcnt vmcnt(" #VMW ") lgkmcnt(0)" : WAITG(P) :: "memory");     \
    __builtin_amdgcn_s_barrier();                                                                  \
    constexpr int rbuf = ((FS) + 1) % 3;                                                           \
    mmt(PAR == 2, P, OJ(0), OM(0)); SB; rdA(Q, rbuf, 0); hook(MODE, PAR, FS, 0); SB; \
    mmt(PAR == 2, P, OJ(1), OM(1)); SB; rdA(Q, rbuf, 1); hook(MODE, PAR, FS, 1); SB; \
    mmt(PAR == 2, P, OJ(2), OM(2)); SB; rdA(Q, rbuf, 2); hook(MODE, PAR, FS, 2); SB; \
    mmt(PAR == 2, P, OJ(3), OM(3)); SB; rdA(Q, rbuf, 3); hook(MODE, PAR, FS, 3); SB; \
    mmt(PAR == 2, P, OJ(4), OM(4)); SB; rdBl(Q, rbuf, 0); hook(MODE, PAR, FS, 4); SB; \
    mmt(PAR == 2, P, OJ(5), OM(5)); SB; rdBh(Q, rbuf, 0); hook(MODE, PAR, FS, 5); SB; \
    mmt(PAR == 2, P, OJ(6), OM(6)); SB; rdBl(Q, rbuf, 1); hook(MODE, PAR, FS, 6); SB; \
    mmt(PAR == 2, P, OJ(7), OM(7)); SB; rdBh(Q, rbuf, 1); hook(MODE, PAR, FS, 7); SB; \
    mmt(PAR == 2, P, OJ(8), OM(8)); SB; rdBl(Q, rbuf, 2); hook(MODE, PAR, FS, 8); SB; \
    mmt(PAR == 2, P, OJ(9), OM(9)); SB; rdBh(Q, rbuf, 2); hook(MODE, PAR, FS, 9); SB; \
    mmt(PAR == 2, P, OJ(10), OM(10)); SB; rdBl(Q, rbuf, 3); hook(MODE, PAR, FS, 10); SB; \
    mmt(PAR == 2, P, OJ(11), OM(11)); SB; rdBh(Q, rbuf, 3); hook(MODE, PAR, FS, 11); SB; \
    mmt(PAR == 2, P, OJ(12), OM(12)); SB; rdBl(Q, rbuf, 4); hook(MODE, PAR, FS, 12); SB; \
    mmt(PAR == 2, P, OJ(13), OM(13)); SB; rdBh(Q, rbuf, 4); hook(MODE, PAR, FS, 13); SB; \
    mmt(PAR == 2, P, OJ(14), OM(14)); SB; rdBl(Q, rbuf, 5); hook(MODE, PAR, FS, 14); SB; \
    mmt(PAR == 2, P, OJ(15), OM(15)); SB; rdBh(Q, rbuf, 5); hook(MODE, PAR, FS, 15); SB; \
    mmt(PAR == 2, P, OJ(16), OM(16)); SB; hook(MODE, PAR, FS, 16); SB; \
    mmt(PAR == 2, P, OJ(17), OM(17)); SB; hook(MODE, PAR, FS, 17); SB; \
    mmt(PAR == 2, P, OJ(18), OM(18)); SB; hook(MODE, PAR, FS, 18); SB; \
    mmt(PAR == 2, P, OJ(19), OM(19)); SB; hook(MODE, PAR, FS, 19); SB; \
    mmt(PAR == 2, P, OJ(20), OM(20)); SB; hook(MODE, PAR, FS, 20); SB; \
    mmt(PAR == 2, P, OJ(21), OM(21)); SB; hook(MODE, PAR, FS, 21); SB; \
    mmt(PAR == 2, P, OJ(22), OM(22)); SB; hook(MODE, PAR, FS, 22); SB; \
    mmt(PAR == 2, P, OJ(23), OM(23)); SB; hook(MODE, PAR, FS, 23); SB; \
    if (MODE < 3 && PAR != 2) tail6(P, Q, rbuf);                                                   \
    else if (nh == 0) { rdBl(Q, rbuf, 6); rdBh(Q, rbuf, 6); SB; mmt(PAR == 2, P, 6, 0); mmt(PAR == 2, P, 6, 1); mmt(PAR == 2, P, 6, 2); mmt(PAR == 2, P, 6, 3); } \
    SB;                                                                                            \
    hook(MODE, PAR, FS, 24); SB;                                                                   \
  }
  if (!bld) asm volatile("s_waitcnt vmcnt(6)" ::: "memory");
  ITER(0, F0, F1, 0, 2, 8);
  asm volatile("s_waitcnt vmcnt(12)" :: "v"(mrow), "v"(msc) : "memory");
  unsigned pub = (blockIdx.x & 31) ^ ntile, ra8 = tid * 8;
  PIN(ra8);
#pragma unroll 1
  for (int f0 = 0; f0 < 3; ++f0) {
    ITER(1, F1, F0, 0, 1, 6);
    ITER(2, F0, F1, 0, 0, 6);
    ITER(3, F1, F0, 0, 1, 6);
    ITER(4, F0, F1, 0, 0, 6);
    ITER(5, F1, F0, 0, 1, 6);
    ITER(6, F0, F1, 0, 0, 6);
    aidx += 24;
    asm volatile("s_cmp_eq_u32 %0, 0\n\ts_cbranch_scc0 .Lpb%=\n\t"
                 "s_waitcnt vmcnt(0)\n\ts_barrier\n\ts_mov_b32 %0, 1\n\ts_cmp_ge_u32 %4, 4\n\ts_cbranch_scc1 .Lpb%=\n\t"
                 "global_store_dwordx2 %1, %2, %3 sc1\n.Lpb%=:"
                 : "+s"(pub) : "v"(ra8), "v"(rsp), "s"(ws + WS_RESP + ntile * 2048), "s"(w) : "memory", "scc");
  }
  ITER(1, F1, F0, -1, 1, 6);
  ITER(2, F0, F1, -1, 0, 6);
  asm volatile("" : "+v"(tl));
  ITER(3, F1, F0, 1, 1, 6);
  asm volatile("" : "+v"(tl));
  ITER(4, F0, F1, 2, 0, 6);
  asm volatile("" : "+v"(tl));
  ITER(5, F1, F0, 3, 1, 7);
  asm volatile("" : "+v"(tl));
  ITER(6, F0, F1, 4, 0, 0);
  asm volatile("" : "+v"(tl));
  {
  const int lane = tl & 63, g = lane >> 4, fr = lane & 15, hf = lane >> 5, l31 = lane & 31;
  {
    asm volatile("s_waitcnt lgkmcnt(0)" : WAITF(F1) :: "memory");
    __builtin_amdgcn_s_barrier();
    f32x4* xw = (f32x4*)(shm + xoff(w));
    auto cv3 = [&](int n) __attribute__((always_inline)) { for (int k = CV2 + n * (CV3 - CV2) / 24; k < CV2 + (n + 1) * (CV3 - CV2) / 24; ++k) conv(k); if ((n + 24 - K0) % KD == 0 && (n + 24 - K0) / KD < NCL) claim((n + 24 - K0) / KD); };
    static_assert(K0 + (NCL - 1) * KD < 48, "");
    constexpr int LAG = 3;
    if (nh == 0) { mm1(F1, 6, 2); mm1(F1, 6, 3); mm1(F1, 6, 0); mm1(F1, 6, 1); }
    SB;
#pragma unroll
    for (int n = 0; n < 24; ++n) {
      mm1(F1, (n % 12) >> 1, (n < 12 ? 2 : 0) + (n & 1)); SB;
      if (n == 1 && nh == 0) { xw[12 * 64 + lane] = acc[2][6]; xw[13 * 64 + lane] = acc[3][6]; }
      if (n >= LAG && n < 12 + LAG) xw[(n - LAG) * 64 + lane] = acc[2 + ((n - LAG) & 1)][(n - LAG) >> 1];
      cv3(n); SB;
    }
  }
  WSTAMP(5);
  typedef __attribute__((ext_vector_type(8))) unsigned u32x8;
  const unsigned ma = sb + MB + lane * 16;
  u32x4 af[7];
  auto rdM = [&](int pi, int sl) { asm volatile("ds_read_b128 %0, %1 offset:%2" : "=v"(af[sl]) : "v"(ma), "i"((7 * pi - 1 + sl) * 1024)); };
  u32x2 X1[4][6], PQ1[4][2];
  f32x4 c4[2]; f32x2 c2[2]; float c1[2];
  u32x2 gv;
  unsigned long long x;
  __builtin_amdgcn_s_setprio(3);
  asm volatile("s_waitcnt vmcnt(1) lgkmcnt(0)\n\ts_cmp_lt_u32 %1, %2\n\ts_cbranch_scc1 .Lxw%=\n\ts_waitcnt vmcnt(0)\n.Lxw%=:" :: "v"(nn), "s"(w - KL), "i"(NCL - KL) : "memory", "scc");
  {
    __builtin_amdgcn_s_barrier();
    asm volatile("" ::: "memory");
    f32x4 o[7][2], wv[7];
#pragma unroll
    for (int c = 0; c < 7; ++c) {
#pragma unroll
      for (int q = 0; q < 2; ++q) asm volatile("ds_read_b128 %0, %1 offset:%2" : "=v"(o[c][q]) : "v"(xb + lane * 16), "i"((c * 2 + q) * 1024) : "memory");
      if (c < 3) asm volatile("ds_read_b128 %0, %1 offset:%2" : "=v"(wv[c]) : "v"(sb + CS + 1536 + nh * 448 + g * 16), "i"(c * 64) : "memory");
    }
    SB;
#pragma unroll
    for (int k = CV3; k < 38; ++k) conv(k);
    SB;
#define PRB(F, b, sl) do { if ((sl) == 1) { \
      if ((F) && (b) == 0) __builtin_amdgcn_s_setprio(2); \
      if ((F) && (b) == 16 && w < 4) __builtin_amdgcn_s_setprio(1); \
      if (!(F) && (b) == 0) __builtin_amdgcn_s_setprio(1); \
      if (!(F) && (b) == (6 * U > 16 ? 6 * U - 16 : 0) && w < 4) __builtin_amdgcn_s_setprio(0); \
      if (!(F) && (b) == 6 * U - 4) __builtin_amdgcn_s_setprio(0); } } while (0)
    f32x2 s2[4];
    constexpr float C0 = -2.f * 0.7978845608028654f * 1.4426950408889634f, C1 = C0 * 0.044715f;
    f32x2 vC0 = {C0, C0};
    asm volatile("" : "+v"(vC0));
    const f32x2 vC1 = {C1, C1}, vone = {1.f, 1.f};
#pragma unroll
    for (int c = 0; c < 7; ++c) {
      if (c == 0) asm volatile("s_waitcnt lgkmcnt(14)" : "+v"(o[0][0]), "+v"(o[0][1]), "+v"(wv[0]));
      if (c == 1) {
        SB;
        asm volatile("s_waitcnt lgkmcnt(8)" : "+v"(o[1][0]), "+v"(o[1][1]), "+v"(o[2][0]), "+v"(o[2][1]), "+v"(wv[1]), "+v"(wv[2]));
#pragma unroll
    for (int k = 0; k < NF; k += 2) {
      const unsigned f = (rot + k) * XP;
      asm volatile("ds_write2_b32 %0, %1, %2 offset1:%3" :: "v"(xwa + min(f, f - NF * XP)), "v"(xs[k]), "v"(xs[k + 1]), "i"(XP / 4) : "memory");
    }
    asm volatile("ds_write_b32 %0, %1 offset:%2" :: "v"(xwa), "v"(s2h), "i"(NF * XP) : "memory");
    asm volatile("s_mov_b64 exec, 1\n\tds_add_u32 %0, %1\n\ts_mov_b64 exec, -1" :: "v"(sb + CS + 768), "v"(1) : "memory");
        SB;
      }
      if (c == 2) {
        SB;
#pragma unroll
        for (int k = 3; k < 7; ++k) asm volatile("ds_read_b128 %0, %1 offset:%2" : "=v"(wv[k]) : "v"(sb + CS + 1536 + nh * 448 + g * 16), "i"(k * 64) : "memory");
        SB;
      }
      if (c == 3) {
        SB;
        asm volatile("s_waitcnt lgkmcnt(0)" : "+v"(o[3][0]), "+v"(o[3][1]), "+v"(o[4][0]), "+v"(o[4][1]), "+v"(o[5][0]), "+v"(o[5][1]), "+v"(o[6][0]), "+v"(o[6][1]),
                     "+v"(wv[3]), "+v"(wv[4]), "+v"(wv[5]), "+v"(wv[6]));
      }
      if (c >= 3 && c < 6) {
        SB;
        const unsigned ub = xb;
#pragma unroll
        for (int u = 0; u < 4; ++u) {
          if (c == 3) {
            asm volatile("ds_read_b64_tr_b16 %0, %1 offset:%2" : "=v"(PQ1[u][0]) : "v"(xa[2] + ub), "i"(u * 32));
            asm volatile("ds_read_b64_tr_b16 %0, %1 offset:%2" : "=v"(PQ1[u][1]) : "v"(xa[3] + ub), "i"(u * 32));
            asm volatile("ds_read_b64_tr_b16 %0, %1 offset:%2" : "=v"(X1[u][0]) : "v"(xa[0] + ub), "i"(u * 32));
          } else {
#pragma unroll
            for (int i = c == 4 ? 1 : 4; i < (c == 4 ? 4 : 6); ++i) asm volatile("ds_read_b64_tr_b16 %0, %1 offset:%2" : "=v"(X1[u][i]) : "v"(xa[1] + ub), "i"(u * 32 + i * 4 * XP));
          }
        }
        if (c == 5) {
#pragma unroll
          for (int sl = 1; sl < 8; ++sl) rdM(sl / 7, sl % 7);
        }
        SB;
      }
      if (c < 6 || nh == 0) {
#pragma unroll
        for (int q = 0; q < 2; ++q) {
#pragma unroll
          for (int e = 0; e < 2; ++e) {
            const f32x2 va = {acc[q][c][2 * e], acc[q][c][2 * e + 1]}, vo = {o[c][q][2 * e], o[c][q][2 * e + 1]};
            const f32x2 x = va + vo, vw = {wv[c][2 * e], wv[c][2 * e + 1]};
            const f32x2 y = x * __builtin_elementwise_fma(x * x, vC1, vC0);
            const f32x2 ex = {__builtin_amdgcn_exp2f(y[0]), __builtin_amdgcn_exp2f(y[1])};
            const f32x2 dn = ex + vone;
            const f32x2 rc = {__builtin_amdgcn_rcpf(dn[0]), __builtin_amdgcn_rcpf(dn[1])};
            const f32x2 gl = x * rc;
            s2[q * 2 + e] = c == 0 ? gl * vw : __builtin_elementwise_fma(gl, vw, s2[q * 2 + e]);
          }
        }
      }
    }
    const f32x2 t0 = s2[0] + s2[1], t1 = s2[2] + s2[3];
    const u32x2 u0 = __builtin_amdgcn_permlane32_swap(__float_as_uint(t0[0] + t0[1]), __float_as_uint(t1[0] + t1[1]), false, false);
    const unsigned u1 = __float_as_uint(__uint_as_float(u0[0]) + __uint_as_float(u0[1]));
    const u32x2 u2 = __builtin_amdgcn_permlane16_swap(u1, u1, false, false);
    const float dpart = __uint_as_float(u2[0]) + __uint_as_float(u2[1]);
    unsigned long long* gran = (unsigned long long*)(ws + WS_GRAN);
    {
      gv = (u32x2){((__float_as_uint(dpart) + 0x80u) & 0xffffff00u) | chk(qq), qq ^ TX};
      asm volatile("s_mov_b64 exec, %3\n\tglobal_store_dwordx2 %0, %1, %2 sc1\n\ts_mov_b64 exec, -1"
                   :: "v"(l31 * 8), "v"(gv), "s"(gran + (ntile * 2 + nh) * NB + b0 + mh * 64 + kh * 32), "s"(0xffff00000000ffffull) : "memory");
    }
    SB;
  }
  WSTAMP(7);
  int crow, cix[4];
  bool cok;
  unsigned cks;
  {
    asm volatile("s_waitcnt vmcnt(1)" : "+v"(tkv) : "v"(nz) : "memory");
    const unsigned t = __builtin_amdgcn_readfirstlane(tkv), d = t < 16 * NP ? (blockIdx.x & 16) + (t & 15) : ntile;
    asm volatile("s_cmp_lt_u32 %1, %2\n\ts_cselect_b32 %0, 1, 0" : "=s"(cks) : "s"(t), "s"(w < NCL ? 16 * NP + (blockIdx.x & 16 ? 0 : NS - nsb) : 0) : "scc");
    cok = cks;
    unsigned cr = ((d & 7) * 4 + (d >> 3)) * BM + ntile * 16 + (t < 16 * NP ? 8 + NS + (t >> 4) : 8 + nsb + (t & 15));
    asm volatile("" : "+s"(cr));
    crow = cok ? cr : b0 + ntile * 16 + w;
    asm volatile("s_cmp_eq_u32 %6, 0\n\ts_cbranch_scc1 .Lci%=\n\tv_bfe_u32 %0, %4, 3, 3\n\tv_min_u32 %3, 1, %0\n\tv_lshlrev_b32 %0, 2, %0\n\tv_lshlrev_b32 %3, 2, %3\n\t"
                 "global_load_dword %1, %0, %5 offset:32\n\tglobal_load_dword %2, %0, %5 offset:64\n\tglobal_load_dword %3, %3, %5 offset:96\n\tglobal_load_dword %0, %0, %5\n.Lci%=:"
                 : "=&v"(cix[0]), "=&v"(cix[1]), "=&v"(cix[2]), "=&v"(cix[3]) : "v"(tl), "s"(idx + crow * NF), "s"(cks) : "memory", "scc");
  }
  u32x4 xc[4];
  unsigned cem = 0, cj = 0, n2;
  float cf[7];
  asm volatile("s_waitcnt lgkmcnt(0)" ::: "memory");
  unsigned sidx = (lane & 1) ? 0xeeee : 0x4444;
  PIN(sidx);
  constexpr int UM = 5;
  u32x2 X2[UM][6], PQ2[UM][2];
  unsigned s21, s22[UM], ub2[UM], cnt;
  u32x8 z[2];
  f32x4 ac1[4][4];
  float rs1[4];
  auto pkm = [&](const u32x2* X, const u32x2* PQ, unsigned s2, int pi, int zb, int e) __attribute__((always_inline)) {
    const int n = 8 * pi - 4 + e;
    unsigned r;
    if (n < 0) asm volatile("v_pk_mul_f16 %0, %1, %2 op_sel:[0,1]" : "=v"(r) : "v"(PQ[e >> 1][e & 1]), "s"(s2));
    else if (n < 2) asm volatile("v_pk_mul_f16 %0, %1, %2" : "=v"(r) : "v"(PQ[0][n]), "v"(PQ[1][n]));
    else asm volatile("v_pk_mul_f16 %0, %1, %2 op_sel_hi:[0,1]" : "=v"(r) : "v"(X[prod(n).a >> 1][0]), "v"(X[prod(n).b >> 1][prod(n).b & 1]));
    z[zb][e] = r;
  };
  auto fin = [&](const f32x4* ac, float& rs, int k) __attribute__((always_inline)) {
    if (k < 6) {
      const float xx = k < 4 ? ac[0][k] : ac[2][k - 4], rr = k < 4 ? ac[1][k] : ac[2][k - 2], c = cf[k];
      float t;
      if (k == 0) asm volatile("v_mul_f32 %0, %1, %2\n\tv_mul_f32 %0, %0, %3" : "=&v"(rs) : "v"(xx), "v"(c), "v"(rr));
      else asm volatile("v_mul_f32 %1, %2, %3\n\tv_fmac_f32 %0, %1, %4" : "+v"(rs), "=&v"(t) : "v"(xx), "v"(c), "v"(rr));
    } else {
      const float d0 = ac[3][0], d1 = ac[3][1], d2 = ac[3][2], d3 = ac[3][3], c = cf[6];
      float t, u;
      asm volatile("v_add_f32 %1, %3, %4\n\tv_add_f32 %2, %5, %6\n\tv_mul_f32 %1, %1, %7\n\tv_fmac_f32 %0, %1, %2" : "+v"(rs), "=&v"(t), "=&v"(u) : "v"(d0), "v"(d1), "v"(d2), "v"(d3), "v"(c));
    }
  };
  auto red = [&](float* r, int k, unsigned addr) __attribute__((always_inline)) {
    if (k == 0) asm volatile("s_nop 1\n\tv_permlane32_swap_b32 %0, %1\n\tv_permlane32_swap_b32 %2, %3\n\tv_add_f32 %0, %0, %1\n\tv_add_f32 %2, %2, %3" : "+v"(r[0]), "+v"(r[1]), "+v"(r[2]), "+v"(r[3]));
    if (k == 1) asm volatile("s_nop 1\n\tv_permlane16_swap_b32 %0, %1\n\tv_add_f32 %0, %0, %1" : "+v"(r[0]), "+v"(r[2]));
    if (k == 2) asm volatile("s_nop 1\n\tv_add_f32_dpp %0, %0, %0 quad_perm:[1,0,3,2] row_mask:0xf bank_mask:0xf" : "+v"(r[0]));
    if (k == 3) asm volatile("s_nop 1\n\tv_add_f32_dpp %0, %0, %0 quad_perm:[2,3,0,1] row_mask:0xf bank_mask:0xf" : "+v"(r[0]));
    if (k == 4) asm volatile("s_nop 1\n\tv_add_f32_dpp %0, %0, %0 row_half_mirror row_mask:0xf bank_mask:0xf" : "+v"(r[0]));
    if (k == 5) asm volatile("s_nop 1\n\tv_add_f32_dpp %0, %0, %0 row_mirror row_mask:0xf bank_mask:0xf\n\tds_write_b32 %1, %0" : "+v"(r[0]) : "v"(addr) : "memory");
  };
  const unsigned gu = 2 * (g & 1) + (g >> 1);
  int orow = g == 1 ? crow : b0 + ntile * 16 + w + (g == 2 && w < ns ? 8 : 0);
  PIN(orow);
  unsigned long long omask = (unsigned long long)(w < ns) << 32 | (cok ? 0x10001u : 1u);
  asm volatile("" : "+s"(omask));
  auto pass = [&](auto Uc, auto Fc) __attribute__((always_inline)) {
    constexpr int U = decltype(Uc)::value;
    constexpr bool FIRST = decltype(Fc)::value;
    f32x4 ac2[U][4];
    float rs2[8], dsum;
    auto blk = [&](auto bc) __attribute__((always_inline)) {
      constexpr int b = decltype(bc)::value, pi = b / U, u = b % U, un = (b + 1) % U, pn = (b + 1) / U;
      f32x4* ac = FIRST ? ac1[u] : ac2[u];
#pragma unroll
      for (int sl = pi == 0; sl < 7; ++sl) {
        if (u == 0 && (pi > 0 || !FIRST) && sl == (pi == 0)) asm volatile("s_waitcnt lgkmcnt(%0)" :: "i"(U > 1 ? 4 : 0) : "memory");
        if (U > 1 && u == 0 && (pi > 0 || !FIRST) && sl == 3) asm volatile("s_waitcnt lgkmcnt(0)" ::: "memory");
        if (!FIRST && b == U - 1 && sl == 1) asm volatile("s_waitcnt lgkmcnt(0)" ::: "memory");
        const u32x8 zz = z[b & 1];
        const u32x4 a = af[sl];
        const unsigned si = sidx;
        if (sl == 6) asm volatile("v_smfmac_f32_16x16x64_f16 %0, %1, %2, %3" : "+v"(ac[3]) : "v"(a), "v"(zz), "v"(si));
        else {
          const u32x4 zh = sl >= 3 ? __builtin_shufflevector(zz, zz, 4, 5, 6, 7) : __builtin_shufflevector(zz, zz, 0, 1, 2, 3);
          if (pi == 0 && sl <= 3) asm volatile("v_mfma_f32_16x16x32_f16 %0, %1, %2, 0" : "=&v"(ac[sl % 3]) : "v"(a), "v"(zh));
          else asm volatile("v_mfma_f32_16x16x32_f16 %0, %1, %2, %0" : "+v"(ac[sl % 3]) : "v"(a), "v"(zh));
        }
        SB;
        if (u == U - 1 && pi < 5) rdM(pi + 1, sl);
        if (u == U - 1 && pi == 5 && FIRST) rdM(sl == 0, sl);
        auto pk = [&](int e) __attribute__((always_inline)) {
          if (pn == 6) { if (FIRST) pkm(X2[0], PQ2[0], s22[0], 0, 0, e); }
          else if (FIRST) pkm(X1[un], PQ1[un], s21, pn, (b + 1) & 1, e);
          else pkm(X2[un], PQ2[un], s22[un], pn, (b + 1) & 1, e);
        };
        if (pi == 0) { if (sl <= 2) { pk(2 * sl - 2); pk(2 * sl - 1); } else pk(sl + 1); }
        else { if (sl == 0) pk(0); pk(sl + 1); }
        if (pi == 0 && sl == 3) { ac[3] = (f32x4){0.f, 0.f, 0.f, 0.f}; PIN(ac[3]); }
        if (pi == 5 && u > 0) fin(FIRST ? ac1[u - 1] : ac2[u - 1], FIRST ? rs1[u - 1] : rs2[u - 1], sl);
        if (FIRST) {
          if (b == 13 && sl == 1) asm volatile("ds_read_b32 %0, %1" : "=v"(cnt) : "v"(sb + CS + 768) : "memory");
          if (b == 14 && sl < 2) {
            f32x4 r4; f32x2 r2; float r1;
            asm volatile("ds_read_b128 %0, %1 offset:%2" : "=v"(r4) : "v"(sb + MB + g * 16), "i"(MC + sl * 128));
            asm volatile("ds_read_b64 %0, %1 offset:%2" : "=v"(r2) : "v"(sb + MB + g * 8), "i"(MC + sl * 128 + 64));
            asm volatile("ds_read_b32 %0, %1 offset:%2" : "=v"(r1) : "v"(sb + MB + g * 4), "i"(MC + sl * 128 + 96));
            c4[sl] = r4; c2[sl] = r2; c1[sl] = r1;
          }
          if (b == 16) {
            const float c0 = sl < 4 ? c4[0][sl] : sl < 6 ? c2[0][sl - 4] : c1[0], cc = sl < 4 ? c4[1][sl] : sl < 6 ? c2[1][sl - 4] : c1[1];
            float t;
            asm volatile("v_mul_f32 %0, %1, %2" : "=v"(t) : "v"(c0), "v"(cc));
            cf[sl] = t;
          }
          if (b == 4 && sl == 0) {
            u32x4 r0, r1, r2, r3; unsigned t0 = cix[0], t1 = cix[1], t2 = cix[2], t3 = cix[3], t;
            asm volatile("s_waitcnt vmcnt(0)\n\ts_cmp_eq_u32 %11, 0\n\ts_cbranch_scc1 .Lcx%=\n\tv_and_b32 %8, 0x70, %9\n\t"
                         "v_lshl_or_b32 %4, %4, 7, %8\n\tv_lshl_or_b32 %5, %5, 7, %8\n\tv_lshl_or_b32 %6, %6, 7, %8\n\tv_lshl_or_b32 %7, %7, 7, %8\n\t"
                         "global_load_dwordx4 %0, %4, %10\n\tglobal_load_dwordx4 %1, %5, %10\n\tglobal_load_dwordx4 %2, %6, %10\n\tglobal_load_dwordx4 %3, %7, %10\n.Lcx%=:"
                         : "=&v"(r0), "=&v"(r1), "=&v"(r2), "=&v"(r3), "+v"(t0), "+v"(t1), "+v"(t2), "+v"(t3), "=&v"(t) : "v"(ma + 0), "s"(table), "s"(cks + 0), "v"(gv) : "memory", "scc");
            xc[0] = r0; xc[1] = r1; xc[2] = r2; xc[3] = r3;
          }
          if (b == 10 && sl == 0) {
            asm volatile("s_waitcnt vmcnt(0)" : "+v"(xc[0]), "+v"(xc[1]), "+v"(xc[2]), "+v"(xc[3]) :: "memory");
            if (cok) {
              unsigned wd, m = 123u << 23, ml = 0;
              asm volatile("s_mov_b64 exec, 1\n\tds_add_rtn_u32 %0, %1, %2\n\ts_mov_b64 exec, -1" : "=&v"(wd) : "v"(sb + CS + 768), "v"(0x100) : "memory");
#pragma unroll
              for (int k = 0; k < 4; ++k) {
                const unsigned a = xc[k][0], b = xc[k][1], c = xc[k][2], d = xc[k][3];
                asm("v_max3_f16 %0, |%2|, |%3|, %0\n\tv_max3_f32 %1, |%2|, |%3|, %1\n\tv_max3_f16 %0, |%4|, |%5|, %0\n\tv_max3_f32 %1, |%4|, |%5|, %1" : "+v"(ml), "+v"(m) : "v"(a), "v"(b), "v"(c), "v"(d));
              }
              m = max(m, ml << 16);
              m = max(m, (unsigned)__builtin_amdgcn_update_dpp(0, m, 0xB1, 0xf, 0xf, false));
              m = max(m, (unsigned)__builtin_amdgcn_update_dpp(0, m, 0x4E, 0xf, 0xf, false));
              m = max(m, (unsigned)__builtin_amdgcn_update_dpp(0, m, 0x141, 0xf, 0xf, false));
              m = max(m, (unsigned)__builtin_amdgcn_update_dpp(0, m, 0x140, 0xf, 0xf, false));
              cem = max(max((unsigned)__builtin_amdgcn_readlane(m, 0), (unsigned)__builtin_amdgcn_readlane(m, 16)), max((unsigned)__builtin_amdgcn_readlane(m, 32), (unsigned)__builtin_amdgcn_readlane(m, 48))) >> 23;
              const unsigned of = (cem - 22) * 0x800080u;
#pragma unroll
              for (int k = 0; k < 16; ++k) {
                unsigned a = xc[k >> 2][k & 3], t;
                asm("v_and_b32 %1, 0x7fff7fff, %0\n\tv_pk_sub_u16 %1, %1, %3 clamp\n\tv_lshlrev_b32 %1, 3, %1\n\tv_bfi_b32 %0, %2, %1, %0" : "+v"(a), "=&v"(t) : "s"(0x7fff7fffu), "s"(of));
                xc[k >> 2][k & 3] = a;
              }
              const float t = __uint_as_float((257 - cem) << 23);
              const unsigned sh = __builtin_bit_cast(unsigned, __builtin_amdgcn_cvt_pkrtz(t, t));
#pragma unroll
              for (int e = 0; e < 4; ++e) xc[3][e] = lane >= 16 ? sh : xc[3][e];
              asm volatile("s_waitcnt lgkmcnt(0)" : "+v"(wd) :: "memory");
              cj = ns + ((__builtin_amdgcn_readfirstlane(wd) >> 8) & 7);
#pragma unroll 1
              for (int spin = 0; spin < (1 << 24) && (__builtin_amdgcn_readfirstlane(wd) & 0xff) != 8; ++spin)
                asm volatile("s_sleep 1\n\tds_read_b32 %0, %1\n\ts_waitcnt lgkmcnt(0)" : "=v"(wd) : "v"(sb + CS + 768) : "memory");
              const unsigned ad = sb + (cj < 8 ? xoff(cj ^ 6) + XR : XE + (cj & 1) * XR) + (lane >> 3) * XP + (lane & 7) * 16;
#pragma unroll
              for (int k = 0; k < 4; ++k) asm volatile("ds_write_b128 %0, %1 offset:%2" :: "v"(k == 3 && lane >= 32 ? 0x40000 : ad), "v"(xc[k]), "i"(k * 8 * XP) : "memory");
            }
            asm volatile("s_mov_b64 exec, 1\n\tds_add_u32 %0, %1\n\ts_mov_b64 exec, -1" :: "v"(sb + CS + 768), "v"(w < NCL ? 0x10000 : 0) : "memory");
          }
          if (b == 16 && sl == 4) {
            PIN(cnt);
            if (__builtin_expect((__builtin_amdgcn_readfirstlane(cnt) & 0xff00ff) != (NCL << 16 | 8), 0)) {
#pragma unroll 1
              for (int spin = 0; spin < (1 << 24); ++spin) {
                asm volatile("s_sleep 1\n\tds_read_b32 %0, %1\n\ts_waitcnt lgkmcnt(0)" : "=v"(cnt) : "v"(sb + CS + 768) : "memory");
                if ((__builtin_amdgcn_readfirstlane(cnt) & 0xff00ff) == (NCL << 16 | 8)) break;
              }
            }
            n2 = ns + ((__builtin_amdgcn_readfirstlane(cnt) >> 8) & 7);
          }
          if (b >= 17 && b < 19)
            for (int k = ((b - 17) * 7 + sl) * 15 / 14; k < ((b - 17) * 7 + sl + 1) * 15 / 14; ++k) {
              const int v = k / 3, t = k % 3;
              if (t == 0) ub2[v] = sb + (w & 3) * 32 + (v < 4 ? xoff((2 * v + kh) ^ 6) + XR : XE + kh * XR);
              u32x2 r;
              asm volatile("ds_read_b64_tr_b16 %0, %1" : "=v"(r) : "v"(xa[(t + 2) & 3] + ub2[v]));
              if (t < 2) PQ2[v][t] = r; else X2[v][0] = r;
            }
          if (b == 21 && sl < UM) { const unsigned r = PQ2[sl][0][1]; unsigned t; asm volatile("v_readlane_b32 %0, %1, 48" : "=s"(t) : "v"(r)); s22[sl] = t; }
        } else {
          if (b == 0) { fin(ac1[3], rs1[3], sl == 1 ? 0 : sl); if (sl == 1) fin(ac1[3], rs1[3], 1); }
          if (b == 1) red(rs1, sl - 1, sb + CS + (w * 4 + gu) * 4);
          if (U > 1 && ((b == 0 && sl >= 3) || (b == 1 && sl < U - 3))) {
            const int v = b ? sl + 3 : sl - 3;
            if (v < U) {
              const unsigned ad = xa[1] + ub2[v];
#pragma unroll
              for (int i = 1; i < 6; ++i) { u32x2 r; asm volatile("ds_read_b64_tr_b16 %0, %1 offset:%2" : "=v"(r) : "v"(ad), "i"(i * 4 * XP)); X2[v][i] = r; }
            }
          }
          if (U > 2 ? b == 3 * U && sl == 0 : b == 0 && sl == 1)
            asm volatile("global_load_dwordx2 %0, %1, %2 sc1" : "=v"(x) : "v"((fr * NB + orow) * 8), "s"(ws + WS_GRAN) : "memory");
        }
        if (!FIRST && b == 5 * U - 2 && sl == 1) {
          const unsigned long long want = (unsigned long long)(qq ^ TX) << 32 | chk(qq);
          asm volatile("s_waitcnt vmcnt(0)" : "+v"(x) :: "memory");
          if (__builtin_expect(__builtin_amdgcn_ballot_w64((x & 0xffffffff000000ffull) != want) != 0, 0)) {
#pragma unroll 1
            for (int spin = 0; spin < (1 << 24); ++spin) {
              asm volatile("s_sleep 1\n\tglobal_load_dwordx2 %0, %1, %2 sc1\n\ts_waitcnt vmcnt(0)" : "=&v"(x) : "v"((fr * NB + orow) * 8), "s"(ws + WS_GRAN) : "memory");
              if (__builtin_amdgcn_ballot_w64((x & 0xffffffff000000ffull) != want) == 0) break;
            }
          }
          dsum = __uint_as_float((unsigned)x & 0xffffff00u);
          PIN(dsum);
        }
        if (!FIRST && b == 5 * U - 2 && sl >= 2 && sl < 6) { float t = dsum; t += sl == 2 ? dpp<0xB1>(t) : sl == 3 ? dpp<0x4E>(t) : sl == 4 ? dpp<0x141>(t) : dpp<0x140>(t); PIN(t); dsum = t; }
        if (!FIRST && b == 5 * U - 2 && sl == 6) {
          typedef __attribute__((ext_vector_type(3))) unsigned u32x3;
          const u32x3 t = {__float_as_uint(dsum + __uint_as_float(bo << 16)), 4 * (g == 1 ? cem : em) - 536, (unsigned)orow * 2};
          asm volatile("s_mov_b64 exec, %2\n\tds_write_b96 %0, %1\n\ts_mov_b64 exec, -1" :: "v"(sb + CS + 1024 + (g == 1 ? 8 + cj : w + hf * 8) * 16), "v"(t), "s"(omask) : "memory");
        }
        PRB(FIRST, b, sl);
        SB;
      }
    };
    if (U == 1) {
      const unsigned ad = xa[1] + ub2[0];
#pragma unroll
      for (int i = 1; i < 6; ++i) { u32x2 r; asm volatile("ds_read_b64_tr_b16 %0, %1 offset:%2" : "=v"(r) : "v"(ad), "i"(i * 4 * XP)); X2[0][i] = r; }
    }
    static_for(blk, std::make_integer_sequence<int, 6 * U>{});
    if (!FIRST) {
      asm volatile("s_nop %0" :: "i"(U == 1 ? 5 : 2));
#pragma unroll
      for (int k = 0; k < 7; ++k) fin(ac2[U - 1], rs2[U - 1], k);
#pragma unroll
      for (int u = U; u < 8; ++u) rs2[u] = 0.f;
#pragma unroll
      for (int k = 0; k < 6; ++k)
#pragma unroll
        for (int h = 0; h < (U + 3) / 4; ++h) red(rs2 + 4 * h, k, sb + CS + (32 + (4 * h + gu) * 8 + w) * 4);
      WSTAMP(9);
      asm volatile("s_waitcnt lgkmcnt(0)\n\ts_barrier" ::: "memory");
      if (w == 0) {
        const f32x4 cs = *(const f32x4*)(shm + CS + l31 * 16);
        const u32x4 fi4 = *(const u32x4*)(shm + CS + 1024 + l31 * 16);
        const float o0 = __uint_as_float(fi4[0]) + __builtin_amdgcn_ldexpf((cs[0] + cs[1]) + (cs[2] + cs[3]), fi4[1]);
        asm volatile("s_bfm_b64 exec, %3, 0\n\tglobal_store_short %0, %1, %2\n\ts_mov_b64 exec, -1" :: "v"(fi4[2]), "v"(pk2(o0, o0)), "s"(out), "s"(8 + n2) : "memory");
      }
      WSTAMP(10);
      if (U == 1) asm volatile(".Lxd_end:");
      else {
        asm volatile("s_endpgm" : "=v"(af[0]), "=v"(af[1]), "=v"(af[2]), "=v"(af[3]), "=v"(af[4]), "=v"(af[5]), "=v"(af[6]), "=v"(z[0]),
                     "=v"(ac1[3][0]), "=v"(ac1[3][1]), "=v"(ac1[3][2]), "=v"(ac1[3][3]), "=v"(rs1[0]), "=v"(rs1[1]), "=v"(rs1[2]) :: "memory");
        asm volatile("" : "=v"(PQ2[0][0]), "=v"(PQ2[0][1]), "=v"(PQ2[1][0]), "=v"(PQ2[1][1]), "=v"(PQ2[2][0]), "=v"(PQ2[2][1]), "=v"(PQ2[3][0]), "=v"(PQ2[3][1]),
                     "=v"(X2[0][0]), "=v"(X2[1][0]), "=v"(X2[2][0]), "=v"(X2[3][0]), "=v"(xa[1]), "=v"(PQ2[4][0]), "=v"(PQ2[4][1]), "=v"(X2[4][0]));
      }
    }
    SB;
  };
  { const unsigned r = PQ1[0][0][1]; asm volatile("v_readlane_b32 %0, %1, 48" : "=s"(s21) : "v"(r)); }
#pragma unroll
  for (int e = 0; e < 8; ++e) pkm(X1[0], PQ1[0], s21, 0, 0, e ^ 4);
  asm volatile("s_nop 0");
  pass(std::integral_constant<int, 4>{}, std::true_type{});
  {
    unsigned uw2 = (4 * n2 + 7 - w) >> 3;
    if (uw2 == 4) pass(std::integral_constant<int, 4>{}, std::false_type{});
    asm volatile("" : "+s"(uw2));
    if (uw2 == 5) pass(std::integral_constant<int, 5>{}, std::false_type{});
    asm volatile("" : "+s"(uw2));
    if (uw2 == 3) pass(std::integral_constant<int, 3>{}, std::false_type{});
    asm volatile("" : "+s"(uw2));
    if (uw2 == 2) pass(std::integral_constant<int, 2>{}, std::false_type{});
    pass(std::integral_constant<int, 1>{}, std::false_type{});
  }
  }
}

extern "C" void kernel_launch(void* const* d_in, const int* in_sizes, int n_in, void* d_out, int out_size,
                              void* d_ws, size_t ws_size, hipStream_t stream) {
  const int* idx = (const int*)d_in[0];
  const u16* table = (const u16*)d_in[1];
  const u16* ws_cin = (const u16*)d_in[2];
  const u16* wout = (const u16*)d_in[3];
  const u16* w1 = (const u16*)d_in[4];
  const u16* b1 = (const u16*)d_in[5];
  const u16* w2 = (const u16*)d_in[6];
  const u16* b_out = (const u16*)d_in[7];
  dnn_kernel<<<256, 512, 0, stream>>>(idx, table, ws_cin, w1, b1, w2, wout, b_out, (char*)d_ws, (u16*)d_out);
}
```

```cpp
#include <hip/hip_runtime.h>
#include <hip/hip_bf16.h>
#include <stdint.h>
#include <utility>

typedef __attribute__((ext_vector_type(8))) short bf16x8;
typedef __attribute__((ext_vector_type(4))) short s16x4;
typedef __attribute__((ext_vector_type(4))) float f32x4;
typedef __attribute__((ext_vector_type(2))) float f32x2;
typedef __attribute__((ext_vector_type(2))) __bf16 bf16x2;
typedef __attribute__((ext_vector_type(16))) float f32x16;
typedef unsigned short u16;
typedef __attribute__((ext_vector_type(2))) unsigned u32x2;
typedef __attribute__((ext_vector_type(4))) unsigned u32x4;

constexpr int NB = 4096, NF = 26, ND = 64, DIN = 1664;

#define STAMP(k,i)
#define WSTAMP(i)
template <class F, int... I>
__device__ __forceinline__ void static_for(F& f, std::integer_sequence<int, I...>) { (f(std::integral_constant<int, I>{}), ...); }
template <int CTRL, int RM = 0xf>
__device__ __forceinline__ float dpp(float v) { return __builtin_bit_cast(float, __builtin_amdgcn_update_dpp(0, __builtin_bit_cast(int, v), CTRL, RM, 0xf, false)); }
__device__ __forceinline__ unsigned pk2(float a, float b) { return __builtin_bit_cast(unsigned, __builtin_convertvector((f32x2){a, b}, bf16x2)); }

template <int V> using IC = std::integral_constant<int, V>;
typedef __attribute__((ext_vector_type(8))) unsigned u32x8;
typedef __attribute__((ext_vector_type(16))) unsigned u32x16;
constexpr int A_BYTES = 224 * 128, STAGE = A_BYTES + 64 * 256;
constexpr int G_LDS = 163840;
constexpr int IDX_OFF = 3 * STAGE, IP = 112;
constexpr int MB = STAGE, MC = 41 * 1024;
constexpr int XA = 2 * STAGE, UP = 27 * 32, NSLOT = 20, UR = NSLOT * UP;
constexpr int EXO = XA + 4 * UR, INFO = EXO + NSLOT * 16, LANEP = INFO + NSLOT * 8;
constexpr int PART = IDX_OFF + 224 * IP, SUM = PART + 2048, BW = SUM, CTR = SUM + NSLOT * 64, ZERO = CTR + 48, POOL = CTR + 64;
constexpr int W3S = STAGE, U_OFF = W3S + 35840, NC_OFF = U_OFF + 2048, GT_OFF = NC_OFF + 1024, WO_OFF = GT_OFF + 2048, DP = 4352, W2S = 0;
static_assert(LANEP + 512 <= PART && POOL + 128 <= G_LDS && WO_OFF + 256 <= 2 * STAGE, "lds");
constexpr int WS_RESP = 4096, WS_POOL = 32768, WS_GRAN = 65536, WS_M = 1 << 20;
constexpr int NCL = 4;

struct Geo { unsigned nt, s0, ns; };
constexpr __host__ __device__ Geo geo(unsigned e) {
  const unsigned i = (e * 79) >> 10, nt = e - 13 * i, c = nt < 9 ? 13108 : 13798, t = i * c + ((nt * 247) & 1023);
  return Geo{nt, t >> 10, ((t + c) >> 10) - (t >> 10)};
}
constexpr unsigned long long builders() {
  unsigned long long k = 0;
  for (unsigned x = 0; x < 8; ++x)
    for (unsigned s = 32; s-- > 0;)
      if (geo(32 * x + s).ns == 12) k = (k & ~(31ull << (5 * x))) | (unsigned long long)s << (5 * x);
  return k;
}
constexpr unsigned long long BK = builders();
#define KEEP 10, 13, 15, 13, 12
constexpr int KP[5] = {KEEP};
constexpr int WANT[5] = {133, 160, 187, 160, 150};
constexpr int kind(unsigned b) { return ((BK >> (5 * (b & 7))) & 31) == b >> 3 ? 3 + ((b & 7) >> 2) : 14 - geo((b & 7) * 32 + (b >> 3)).ns; }
constexpr bool geo_ok() {
  for (unsigned nt = 0; nt < 13; ++nt) {
    unsigned s = 0;
    for (unsigned e = nt; e < 256; e += 13) {
      const Geo g = geo(e);
      if (g.nt != nt || g.s0 != s || g.ns < 12 || g.ns > 14 || e < g.s0 || e >= g.s0 + g.ns) return false;
      s += g.ns;
    }
    if (s != 256) return false;
  }
  return true;
}
static_assert(geo_ok(), "tiles");

struct PI { int a, b; };
constexpr PI prod(int n) {
  int m = n - 2;
  for (int jj = 0; jj < 6; ++jj) {
    const int j = jj ? 6 - jj : 0;
    if (m < 2 + 2 * j) return m == 0 ? PI{2 * j, 2 * j} : m == 1 ? PI{2 * j, 1} : PI{(m - 2) & ~1, 2 * j + (m & 1)};
    m -= 2 + 2 * j;
  }
  return PI{0, 0};
}
constexpr int xfield(int k, int h, int g) { return k == 1 ? 24 + h : 4 * (k >> 1) + ((g + 2 * (k & 1) + h) & 3); }
constexpr int pqfield(int q, int sl, int g) { return g < 3 ? 8 * g + 4 * (sl >> 1) + (sl & 1) + (((g ^ q) & 1) << 1) : sl == 3 ? 26 : 24 + ((sl + q) >> 1); }
constexpr unsigned short slot_code(int f, int t, int g, int h) {
  if (f == 0) {
    const int sl = 2 * (t & 1) + h, q = t >> 1;
    return g == 3 && (q || (sl & 1)) ? 0x8000 : 0x4000 | 6 << 10 | (26 * 33 + pqfield(q, sl, g));
  }
  const int n = 4 * (f - 1) + t;
  const int p = n < 2 ? pqfield(0, 2 * n + h, g) : xfield(prod(n).a, 0, g), q = n < 2 ? pqfield(1, 2 * n + h, g) : xfield(prod(n).b, h, g);
  return (p == 26 ? 0x4000 | 10 << 10 : p == q ? 1 << 10 : 2 << 10) | (p * 33 + q);
}
struct GT { unsigned short t[2][64][8], pool[16][64]; };
constexpr GT make_gt() {
  GT r{};
  for (int l = 0; l < 64; ++l)
    for (int e = 0; e < 8; ++e) {
      r.t[0][l][e] = l < 48 ? slot_code(l >> 2, e >> 1, l & 3, e & 1) : 0x8000;
      r.t[1][l][e] = l < 48 ? slot_code(2 * (l >> 3) + ((l >> 1) & 1), 2 * ((e >> 1) & 1) + ((l >> 2) & 1), 2 * (l & 1) + (e >> 2), e & 1) : 0x8000;
    }
  int tot = 0, dem = 0, acc = 0, k = 15, b = 0;
  for (int i = 0; i < 256; ++i) { tot += 16 - KP[kind(i)]; dem += WANT[kind(i)] - 10 * KP[kind(i)]; }
  for (int q = 0, n = 0; q < 16; ++q) {
    for (int i = 0; i < 16; ++i) acc += WANT[kind(16 * q + i)] - 10 * KP[kind(16 * q + i)];
    const int len = (acc * tot + dem / 2) / dem - n;
    n += len;
    r.pool[q][0] = len;
    for (int i = 1; i <= len; ++i) {
      while (k < KP[kind(b)]) { ++b; k = 15; }
      r.pool[q][i] = ((b & 7) * 32 + (b >> 3)) * 16 + k--;
    }
  }
  return r;
}
constexpr bool pool_ok() { const GT t = make_gt(); for (int q = 0; q < 16; ++q) if (t.pool[q][0] > 62) return false; return true; }
static_assert(pool_ok(), "pools");
static __device__ const __attribute__((aligned(16))) GT g_tab = make_gt();

__global__ __launch_bounds__(512, 2) void dnn_kernel(const int* __restrict__ idx, const u16* __restrict__ table,
                                                     const u16* __restrict__ ws_cin, const u16* __restrict__ w1,
                                                     const u16* __restrict__ b1, const u16* __restrict__ w2,
                                                     const u16* __restrict__ wout, const u16* __restrict__ b_out,
                                                     char* __restrict__ ws, u16* __restrict__ out) {
  __shared__ __attribute__((aligned(1024))) char shm[G_LDS];
  u32x4 boff, ka1;
  u32x8 gs;
  u32x16 ka0;
  unsigned tch;
  asm volatile(
      "s_load_dwordx16 s[4:19], %[ka], 0\ns_load_dwordx4 s[20:23], %[ka], 64\nv_readfirstlane_b32 s24, %[tid]\ns_and_b32 s36, %[blk], 7\ns_lshr_b32 s37, %[blk], 3\ns_lshl_b32 s30, s36, 5\n"
      "s_add_u32 s30, s30, s37\ns_lshr_b32 s24, s24, 6\ns_mul_i32 s38, s30, 79\ns_lshr_b32 s38, s38, 10\ns_mul_i32 s28, s38, 13\ns_sub_u32 s28, s30, s28\nv_and_b32 v244, 63, %[tid]\n"
      "s_lshr_b32 s27, s36, 1\ns_add_u32 s27, s27, s36\ns_and_b32 s39, s28, 3\ns_mul_i32 s39, s39, 3\ns_add_u32 s27, s27, s39\ns_sub_u32 s39, s27, 12\ns_min_u32 s27, s27, s39\n"
      "s_lshl_b32 s27, s27, 1\ns_cmp_ge_u32 s24, 4\ns_cbranch_scc1 .Lf1\ns_movk_i32 s39, 13798\nv_mul_u32_u24 v245, 9363, v244\ns_cmp_lt_u32 s28, 9\ns_cselect_b32 s39, 13108, s39\n"
      "s_or_b32 s41, s27, 2\ns_mul_i32 s40, s28, 247\nv_lshrrev_b32 v245, 16, v245\ns_and_b32 s40, s40, 1023\nv_mad_i32_i24 v246, v245, -7, v244\ns_mul_i32 s38, s38, s39\n"
      "v_lshl_add_u32 v246, v246, 2, s41\ns_add_u32 s38, s38, s40\nv_subrev_u32 v247, 26, v246\ns_lshr_b32 s29, s38, 10\nv_mul_u32_u24 v245, 26, v245\ns_add_u32 s38, s38, s39\n"
      "v_min_u32 v246, v246, v247\ns_lshr_b32 s38, s38, 10\nv_add_lshl_u32 v240, v246, v245, 2\ns_sub_u32 s38, s38, s29\nv_add_u32 v243, 192, v240\ns_lshl_b32 s38, s38, 1\n"
      "v_add_u32 v242, 128, v240\ns_sub_u32 s25, s38, 24\nv_add_u32 v241, 64, v240\ns_min_u32 s26, s24, s25\ns_mul_i32 s38, s24, 6\ns_add_u32 s26, s26, s38\ns_mul_i32 s38, s26, 896\n"
      "s_add_u32 m0, s38, 135168\ns_lshl1_add_u32 s38, s29, s26\ns_mul_i32 s38, s38, 832\ns_mov_b32 exec_hi, 0xffffff\ns_sub_u32 s38, s38, 192\ns_subb_u32 s39, 0, 0\ns_waitcnt lgkmcnt(0)\n"
      "s_add_u32 s38, s4, s38\ns_addc_u32 s39, s5, s39\nglobal_load_lds_dwordx4 v243, s[38:39] offset:0\nglobal_load_lds_dwordx4 v242, s[38:39] offset:896\n"
      "global_load_lds_dwordx4 v241, s[38:39] offset:1792\nglobal_load_lds_dwordx4 v240, s[38:39] offset:2688\ns_add_u32 s38, s38, 3328\ns_addc_u32 s39, s39, 0\ns_add_u32 m0, m0, 3584\n"
      "s_cmp_ge_u32 s24, s25\nglobal_load_lds_dwordx4 v243, s[38:39]\nglobal_load_lds_dwordx4 v242, s[38:39] offset:896\ns_cbranch_scc1 .Li0\nglobal_load_lds_dwordx4 v241, s[38:39] offset:1792\n"
      ".Li0:\ns_mov_b32 exec_hi, -1\ns_and_b32 s41, s24, 3\nv_lshrrev_b32 v245, 4, v244\nv_and_b32 v246, 15, v244\ns_mul_i32 s40, s41, 53248\nv_lshlrev_b32 v238, 1, v245\n"
      "v_mul_u32_u24 v245, 3328, v245\nv_xor_b32 v247, v246, v238\nv_add_u32 v245, s40, v245\nv_lshl_add_u32 v236, v247, 4, v245\nv_xor_b32 v238, 128, v236\nv_add_u32 v237, 12288, v236\n"
      "v_add_u32 v239, 36864, v238\nv_add_u32 v238, 24576, v238\ns_lshl_b32 s40, s28, 8\ns_add_u32 s10, s10, s40\ns_addc_u32 s11, s11, 0\ns_branch .Lf2\n"
      ".Lf1:\ns_getpc_b64 s[42:43]\n"
      ".Lxd_pc0:\ns_and_b32 s41, s24, 3\nv_lshrrev_b32 v245, 4, v244\nv_and_b32 v246, 15, v244\ns_mul_i32 s40, s41, 53248\nv_lshlrev_b32 v238, 1, v245\nv_mul_u32_u24 v245, 3328, v245\n"
      "v_xor_b32 v247, v246, v238\nv_add_u32 v245, s40, v245\nv_lshl_add_u32 v236, v247, 4, v245\nv_xor_b32 v238, 128, v236\nv_add_u32 v237, 12288, v236\nv_add_u32 v239, 36864, v238\n"
      "v_add_u32 v238, 24576, v238\ns_cmp_lg_u32 s24, 7\ns_cbranch_scc1 .Lf3\nv_lshl_add_u32 v245, s37, 4, v246\nv_lshlrev_b32 v245, 7, v245\nv_min_u32 v245, .Lxd_end-.Lxd_pc0-4, v245\n"
      "global_load_dword v253, v245, s[42:43]\n"
      ".Lf3:\ns_lshl_b32 s41, s41, 12\ns_add_u32 m0, s41, 118784\ns_lshl_b32 s40, s28, 8\ns_add_u32 s39, s27, 2\ns_mul_i32 s39, s39, 212992\ns_add_u32 s39, s39, s40\nv_lshlrev_b32 v244, 4, v244\n"
      "s_waitcnt lgkmcnt(0)\ns_add_u32 s42, s10, s39\ns_addc_u32 s43, s11, 0\nglobal_load_lds_dwordx4 v236, s[42:43] offset:0\nglobal_load_lds_dwordx4 v237, s[42:43] offset:1024\n"
      "global_load_lds_dwordx4 v238, s[42:43] offset:2048\nglobal_load_lds_dwordx4 v239, s[42:43] offset:3072\ns_add_u32 s12, s12, s40\ns_addc_u32 s13, s13, 0\ns_add_u32 s14, s14, s40\n"
      "s_addc_u32 s15, s15, 0\ns_add_u32 s42, s42, 212992\ns_addc_u32 s43, s43, 0\ns_mov_b32 m0, 162304\ns_mov_b64 exec, 0xffff\nglobal_load_lds_dwordx4 v244, s[12:13]\ns_mov_b32 m0, 162560\n"
      "s_nop 0\nglobal_load_lds_dwordx4 v244, s[14:15]\ns_add_u32 m0, s41, 28672\ns_mov_b64 exec, -1\nglobal_load_lds_dwordx4 v236, s[42:43] offset:0\n"
      "global_load_lds_dwordx4 v237, s[42:43] offset:1024\nglobal_load_lds_dwordx4 v238, s[42:43] offset:2048\nglobal_load_lds_dwordx4 v239, s[42:43] offset:3072\ns_movk_i32 s39, 13798\n"
      "s_cmp_lt_u32 s28, 9\ns_cselect_b32 s39, 13108, s39\ns_mul_i32 s40, s28, 247\ns_and_b32 s40, s40, 1023\ns_mul_i32 s38, s38, s39\ns_add_u32 s38, s38, s40\ns_lshr_b32 s29, s38, 10\n"
      "s_add_u32 s38, s38, s39\ns_lshr_b32 s38, s38, 10\ns_sub_u32 s38, s38, s29\ns_lshl_b32 s38, s38, 1\ns_sub_u32 s25, s38, 24\n"
      ".Lf2:\ns_add_u32 s27, s27, 2\ns_mul_i32 s36, s36, 5\ns_mov_b32 s38, %[bkl]\ns_mov_b32 s39, %[bkh]\ns_lshr_b64 s[38:39], s[38:39], s36\ns_and_b32 s38, s38, 31\ns_xor_b32 s31, s38, s37\n"
      : "=&{v[236:239]}"(boff), "=&{v253}"(tch), "=&{s[4:19]}"(ka0), "=&{s[20:23]}"(ka1), "=&{s[24:31]}"(gs)
      : [ka] "s"(__builtin_amdgcn_kernarg_segment_ptr()), [blk] "s"(blockIdx.x), [tid] "v"(threadIdx.x), [bkl] "i"((unsigned)BK), [bkh] "i"((unsigned)(BK >> 32))
      : "memory", "scc", "s36", "s37", "s38", "s39", "s40", "s41", "s42", "s43", "v240", "v241", "v242", "v243", "v244", "v245", "v246", "v247");
#define SB __builtin_amdgcn_sched_barrier(0)
#define PIN(x) asm volatile("" : "+v"(x))
  const int tid = threadIdx.x, lane = tid & 63, w = gs[0];
  const unsigned sb = (unsigned)(size_t)(__attribute__((address_space(3))) char*)shm;
  const int g = lane >> 4, fr = lane & 15, hf = lane >> 5, l31 = lane & 31;
  constexpr unsigned TX = 0xA5C30F96u;
  auto chk = [](unsigned t) -> unsigned { return (t * 0x9E3779B1u) >> 24; };
  const int bid = blockIdx.x & 7;
  const bool bld = gs[7] == 0, rblk = bid >= 4;
  const int fi = (bid & 3) * 8 + w, mi_ = min(fi, NF - 1);
  const unsigned tc = sb + (g & 1) * 32 + (fr & 3) * 8, h0 = 8 * (g >> 1) + (fr >> 2);
  u32x4 mrow;
  u32x2 rsp;
  unsigned msc;
  if (__builtin_expect(bld, 0)) {
    unsigned ma_, ur[2], uw[6], fa[2][2];
    if (rblk) {
      const unsigned o3 = min(w * 4480, NF * NF * NF * 2 - 4480);
      asm volatile("s_mov_b32 m0, %2\n\ts_nop 0\n\tglobal_load_lds_dwordx4 %0, %1\n\tglobal_load_lds_dwordx4 %0, %1 offset:1024\n\tglobal_load_lds_dwordx4 %0, %1 offset:2048\n\t"
                   "global_load_lds_dwordx4 %0, %1 offset:3072\n\tglobal_load_lds_dwordx4 %0, %1 offset:3456"
                   :: "v"(lane * 16), "s"((const char*)(ws_cin + 2 * NF * NF * NF) + o3), "s"(sb + W3S + o3) : "memory");
      const unsigned rr = (bid & 3) * 8 + ((lane >> 2) & 7);
      const unsigned vo = (hf * NF * NF + min(rr, 25u) * NF) * 2 + (lane & 3) * 16;
#pragma unroll
      for (int m = 0; m < 2; ++m) {
        const unsigned p = min(w + 8 * m, 12);
        asm volatile("s_mov_b32 m0, %2\n\ts_nop 0\n\tglobal_load_lds_dwordx4 %0, %1" :: "v"(vo), "s"(ws_cin + (NF + 2 * p) * NF * NF), "s"(sb + W2S + p * 1024) : "memory");
      }
      asm volatile("ds_write_b32 %0, %1" :: "v"(sb + U_OFF + tid * 4), "v"(0) : "memory");
    } else {
#pragma unroll
      for (int s = 0; s < 2; ++s)
        asm volatile("s_mov_b32 m0, %2\n\ts_nop 0\n\tglobal_load_lds_dwordx4 %0, %1" :: "v"(min(l31, NF - 1) * NF * 2 + hf * 16), "s"(ws_cin + mi_ * NF * NF + s * 16), "s"(sb + W3S + w * DP + s * 1024) : "memory");
    }
    if (w < 3) asm volatile("s_mov_b32 m0, %2\n\ts_nop 0\n\tglobal_load_lds_dwordx4 %0, %1 sc1" :: "v"(lane * 16), "s"(w ? (const char*)&g_tab + (w - 1) * 1024 : ws), "s"(sb + NC_OFF + w * 1024) : "memory");
    if (w == 3) asm volatile("s_mov_b32 m0, %2\n\ts_nop 0\n\tglobal_load_lds_dword %0, %1" :: "v"(min(lane, 38) * 4), "s"(wout), "s"(sb + WO_OFF) : "memory");
    {
      const int ty = bid >> 2, f = lane >> 2;
      const bool sp = fi >= 24;
      int c0 = sp ? 5 : fi < 16 ? ty - 1 : 1, c1 = sp ? 16 : 3072, rho = sp ? 4 * (fi - 24) + 2 * ty : fi < 16 ? fi : 2 * (fi & 6) + (fi & 1) + 2 * ty;
      asm volatile("" : "+s"(c0), "+s"(c1), "+s"(rho));
      int t = (7 * (f >> 1) + c0) * 1024 + (f & 1) * c1 + ((lane & 3) * 16 + rho) * 16;
      PIN(t);
      ma_ = min((unsigned)t | (fi < 28 ? 0u : ~0u), 41u * 1024 + 1024);
      ma_ = lane < 48 ? ma_ : 43008;
    }
#pragma unroll
    for (int p = 0; p < 2; ++p) {
      ur[p] = sb + W3S + min(8 * g + 4 * p + (fr >> 2), NF - 1) * (NF * NF * 2) + w * 32 + (fr & 3) * 8;
      fa[0][p] = tc + W2S + ((h0 + 4 * p) * 8 + w) * 64; fa[1][p] = tc + W2S + (min(h0 + 16 + 4 * p, 25u) * 8 + w) * 64;
      PIN(ur[p]); PIN(fa[0][p]); PIN(fa[1][p]);
    }
#pragma unroll
    for (int m = 0; m < 6; ++m) {
      const unsigned e = (w + 8 * m) * 16 + fr;
      uw[m] = sb + U_OFF + e * 2 + ((e * 2521u) >> 16) * 12;
      PIN(uw[m]);
      if (m == 5) uw[m] = e < NF * NF ? uw[m] : 0x40000u;
    }
    PIN(uw[5]); PIN(ma_);
    asm volatile("s_waitcnt vmcnt(0) lgkmcnt(0)\n\ts_barrier" :: "{v253}"(tch) : "memory");
    const unsigned wov = *(const unsigned*)(shm + WO_OFF + lane * 4);
    const u32x4 tb = *(const u32x4*)(shm + GT_OFF + (fi >= 24) * 1024 + lane * 16);
    bf16x8 P[2], Q[2];
    if (rblk) {
      s16x4 wl[6][2];
      u32x4 wa;
      unsigned wo2;
      asm volatile("ds_read2_b64 %0, %1 offset0:13 offset1:14" : "=v"(wa) : "v"(sb + WO_OFF + g * 16));
      asm volatile("ds_read_u16 %0, %1 offset:52" : "=v"(wo2) : "v"(sb + WO_OFF + lane * 2));
#pragma unroll
      for (int m = 0; m < 6; ++m)
#pragma unroll
        for (int p = 0; p < 2; ++p) asm volatile("ds_read_b64_tr_b16 %0, %1 offset:%2" : "=v"(wl[m][p]) : "v"(ur[p]), "i"(m * 256));
      u32x2 fl[2][2];
#pragma unroll
      for (int s = 0; s < 2; ++s)
#pragma unroll
        for (int p = 0; p < 2; ++p) asm volatile("ds_read_b64_tr_b16 %0, %1" : "=v"(fl[s][p]) : "v"(fa[s][p]));
      asm volatile("s_waitcnt lgkmcnt(0)" : "+v"(wa), "+v"(wo2), "+v"(wl[0][0]), "+v"(wl[0][1]), "+v"(wl[1][0]), "+v"(wl[1][1]), "+v"(wl[2][0]), "+v"(wl[2][1]), "+v"(wl[3][0]), "+v"(wl[3][1]),
                   "+v"(wl[4][0]), "+v"(wl[4][1]), "+v"(wl[5][0]), "+v"(wl[5][1]), "+v"(fl[0][0]), "+v"(fl[0][1]), "+v"(fl[1][0]), "+v"(fl[1][1]));
      const unsigned km = g < 3 ? ~0u : 0u;
      const u32x4 wm = {wa[0], wa[1] & km, wa[2] & km, wa[3] & km};
      f32x4 du[6];
#pragma unroll
      for (int m = 0; m < 6; ++m)
        du[m] = __builtin_amdgcn_mfma_f32_16x16x32_bf16(__builtin_bit_cast(bf16x8, wm), __builtin_shufflevector(wl[m][0], wl[m][1], 0, 1, 2, 3, 4, 5, 6, 7), (f32x4){0.f, 0.f, 0.f, 0.f}, 0, 0, 0);
      asm volatile("ds_write_b16 %0, %1" :: "v"(lane < NF ? sb + U_OFF + lane * 64 + 52 : 0x40000u), "v"(wo2) : "memory");
#pragma unroll
      for (int m = 0; m < 6; ++m) {
        asm volatile("ds_write_b16 %0, %1" :: "v"(uw[m]), "v"(pk2(du[m][0], du[m][0])) : "memory");
        asm volatile("" :: "v"(du[m]));
      }
      asm volatile("s_waitcnt lgkmcnt(0)\n\ts_barrier" ::: "memory");
      s16x4 ul[2][2];
#pragma unroll
      for (int s = 0; s < 2; ++s)
#pragma unroll
        for (int p = 0; p < 2; ++p)
          asm volatile("ds_read_b64_tr_b16 %0, %1 offset:%2" : "=v"(ul[s][p]) : "v"(tc + U_OFF + h0 * 64), "i"(s * 1024 + p * 256));
      const unsigned fmask = l31 < NF ? ~0u : 0u;
#pragma unroll
      for (int s = 0; s < 2; ++s) {
        u32x4 q4 = {fl[s][0][0] & fmask, fl[s][0][1] & fmask, fl[s][1][0] & fmask, fl[s][1][1] & fmask};
        Q[s] = __builtin_bit_cast(bf16x8, q4);
      }
      asm volatile("s_waitcnt lgkmcnt(0)" : "+v"(ul[0][0]), "+v"(ul[0][1]), "+v"(ul[1][0]), "+v"(ul[1][1]));
#pragma unroll
      for (int s = 0; s < 2; ++s) P[s] = __builtin_shufflevector(ul[s][0], ul[s][1], 0, 1, 2, 3, 4, 5, 6, 7);
    } else {
#pragma unroll
      for (int s = 0; s < 2; ++s) {
        u32x4 xf = *(const u32x4*)(shm + W3S + w * DP + s * 1024 + lane * 16);
        if (s == 1) { const unsigned m = hf ? 0u : ~0u; xf[1] &= m; xf[2] &= m; xf[3] &= m; }
        Q[s] = __builtin_bit_cast(bf16x8, xf);
        const unsigned e = (l31 >> 1) - 8 * s - 4 * hf, v = 0x3f80u << ((lane & 1) * 16);
        u32x4 q4 = {e == 0 ? v : 0u, e == 1 ? v : 0u, e == 2 ? v : 0u, e == 3 ? v : 0u};
        P[s] = __builtin_bit_cast(bf16x8, q4);
      }
    }
    f32x16 D = {};
#pragma unroll
    for (int s = 0; s < 2; ++s) {
      D = __builtin_amdgcn_mfma_f32_32x32x16_bf16(P[s], Q[s], D, 0, 0, 0);
      D = __builtin_amdgcn_mfma_f32_32x32x16_bf16(Q[s], P[s], D, 0, 0, 0);
    }
    float* dl = (float*)(shm + W3S + w * DP);
    D[14] = lane == NF ? __uint_as_float(((unsigned)__builtin_amdgcn_readlane(wov, mi_ >> 1) >> ((mi_ & 1) * 16)) << 16) : D[14];
#pragma unroll
    for (int r = 0; r < 15; ++r) dl[((r & 3) + 8 * (r >> 2) + 4 * hf) * 33 + l31] = D[r];
    __builtin_amdgcn_wave_barrier();
    float v[8], mx = 0.f;
    const unsigned zm = fi >= NF ? 0xffff : rblk ? 0x8000 : 0xc000;
#pragma unroll
    for (int e = 0; e < 8; ++e) v[e] = dl[(tb[e >> 1] >> (16 * (e & 1))) & 0x3ff];
#pragma unroll
    for (int e = 0; e < 8; ++e) {
      const unsigned c = tb[e >> 1] >> (16 * (e & 1));
      float x = __builtin_amdgcn_ldexpf(v[e], ((c >> 10) & 15) - 2);
      PIN(x);
      v[e] = c & zm ? 0.f : x;
      mx = fmaxf(mx, fabsf(v[e]));
    }
    mx = fmaxf(mx, dpp<0xB1>(mx)); mx = fmaxf(mx, dpp<0x4E>(mx)); mx = fmaxf(mx, dpp<0x141>(mx)); mx = fmaxf(mx, dpp<0x140>(mx));
    const unsigned mxu = max(max((unsigned)__builtin_amdgcn_readlane(__float_as_int(mx), 0), (unsigned)__builtin_amdgcn_readlane(__float_as_int(mx), 16)), (unsigned)__builtin_amdgcn_readlane(__float_as_int(mx), 32));
    const unsigned ex = min(max(mxu >> 23, 40u), 250u);
    const float al = __uint_as_float((268 - ex) << 23);
#pragma unroll
    for (int e = 0; e < 4; ++e) {
      const unsigned q = pk2(v[2 * e] * al, v[2 * e + 1] * al);
      mrow[e] = __builtin_bit_cast(unsigned, __builtin_amdgcn_cvt_pkrtz(__uint_as_float(q << 16), __uint_as_float(q & 0xffff0000u)));
    }
    msc = fi < NF ? (ex - 14) << 23 : 0u;
    PIN(msc);
    asm volatile("global_store_dwordx4 %0, %1, %2 sc1" :: "v"(ma_), "{v[248:251]}"(mrow), "s"(ws + WS_M) : "memory");
    asm volatile("global_store_dword %0, %1, %2 sc1" :: "v"((bid * 8 + w) * 4), "{v252}"(msc), "s"(ws + WS_M + MC) : "memory");
    if (fi == 31) asm volatile("global_store_dword %0, %1, %2 sc1\n\ts_waitcnt vmcnt(0)" :: "v"((lane & 15) * 128), "{v252}"(msc), "s"(ws + WS_POOL) : "memory");
    const unsigned mval = *(const unsigned*)(shm + NC_OFF + (tid & 255) * 4), qval = *(const unsigned*)(shm + NC_OFF);
    rsp = (u32x2){qval << 8 | chk(mval), mval ^ TX};
    PIN(rsp);
    asm volatile("s_waitcnt lgkmcnt(0)\n\ts_barrier" ::: "memory");
  } else {
    asm volatile("" : "=v"(mrow), "=v"(rsp), "=v"(msc));
  }
  static_assert(A_BYTES == 28672 && IDX_OFF == 135168 && IP == 112 && STAGE == 45056 && MC == 41984 && WS_RESP == 4096 && WS_POOL == 32768 && WS_M == 1048576, "constants of the code below");
  asm volatile(
      "s_and_b32 s36, s24, 3\ns_lshl_b32 s42, %[blk], 2\ns_load_dword s37, s[20:21], s42\ns_load_dword s38, s[18:19], 0\ns_bfe_u32 s42, %[blk], 0x10002\ns_add_u32 s42, s42, 3\n"
      "s_lshr_b32 s43, s25, 1\ns_sub_u32 s43, 2, s43\ns_cmp_eq_u32 s31, 0\ns_cselect_b32 s42, s42, s43\ns_mul_i32 s42, s42, 5\ns_lshr_b32 s64, %[kp], s42\ns_and_b32 s64, s64, 31\n"
      "s_sub_u32 s33, s30, s29\ns_lshl_b32 s34, s30, 5\ns_lshl_b32 s35, s28, 8\ns_add_u32 s35, s35, s29\ns_lshl_b32 s35, s35, 7\ns_add_u32 s35, s35, 65536\ns_cmp_ge_u32 s24, 4\n"
      "s_cbranch_scc1 .Lgemm\ns_mul_i32 s42, s26, 896\ns_cmp_ge_u32 s36, s25\ns_cselect_b64 vcc, 0, -1\nv_mbcnt_lo_u32_b32 v34, -1, 0\nv_mbcnt_hi_u32_b32 v34, -1, v34\ns_cselect_b32 s43, 0, 5376\n"
      "s_and_b32 s44, s27, 2\ns_xor_b32 s44, s44, 2\ns_lshl_b32 s44, s44, 2\ns_add_u32 s42, s42, s44\ns_add_u32 s42, s42, 135168\nv_lshrrev_b32 v22, 3, v34\nv_mul_u32_u24 v6, 112, v22\n"
      "v_add_u32 v6, s42, v6\nv_add_u32 v7, s43, v6\nv_lshrrev_b32 v23, 4, v34\ns_lshl_b32 s42, s26, 2\nv_xor_b32 v23, v23, v34\nv_xor_b32 v23, s42, v23\nv_and_b32 v23, 7, v23\n"
      "v_lshlrev_b32 v4, 4, v23\nv_xor_b32 v5, 64, v4\ns_sub_u32 s44, s6, 1024\ns_subb_u32 s45, s7, 0\ns_sub_u32 s46, s6, 2048\ns_subb_u32 s47, s7, 0\ns_sub_u32 s48, s6, 3072\n"
      "s_subb_u32 s49, s7, 0\ns_lshl_b32 s53, s26, 10\ns_lshl_b32 s54, s36, 12\ns_add_u32 s54, s54, 28672\ns_add_u32 s52, s27, 1\ns_mul_i32 s52, s52, 212992\ns_add_u32 s52, s52, 212992\n"
      "s_cmp_eq_u32 s52, 5537792\ns_cselect_b32 s52, 0, s52\ns_add_u32 s50, s10, s52\ns_addc_u32 s51, s11, 0\ns_waitcnt vmcnt(0)\nds_read_b64 v[8:9], v6 offset:0\n"
      "ds_read_b64 v[10:11], v6 offset:896\nds_read_b64 v[12:13], v6 offset:1792\nds_read_b64 v[14:15], v6 offset:2688\nds_read_b64 v[16:17], v6 offset:3584\nds_read_b64 v[18:19], v6 offset:4480\n"
      "ds_read_b64 v[20:21], v7 offset:0\ns_waitcnt lgkmcnt(0)\nv_lshl_add_u32 v22, v8, 7, v4\nv_lshl_add_u32 v23, v10, 7, v5\nv_lshl_add_u32 v24, v12, 7, v4\nv_lshl_add_u32 v25, v14, 7, v5\n"
      "v_lshl_add_u32 v26, v16, 7, v4\nv_lshl_add_u32 v27, v18, 7, v5\nv_lshl_add_u32 v28, v20, 7, v4\ns_add_u32 m0, s53, 90112\ns_nop 0\nglobal_load_lds_dwordx4 v22, s[6:7] offset:0\n"
      "global_load_lds_dwordx4 v23, s[44:45] offset:1024\nglobal_load_lds_dwordx4 v24, s[46:47] offset:2048\nglobal_load_lds_dwordx4 v25, s[48:49] offset:3072\ns_add_u32 m0, s53, 94208\ns_nop 0\n"
      "global_load_lds_dwordx4 v26, s[6:7] offset:0\nglobal_load_lds_dwordx4 v27, s[44:45] offset:1024\ns_cbranch_vccz .La1001\nglobal_load_lds_dwordx4 v28, s[46:47] offset:2048\n"
      ".La1001:\nv_lshl_add_u32 v22, v9, 7, v4\nv_lshl_add_u32 v23, v11, 7, v5\nv_lshl_add_u32 v24, v13, 7, v4\nv_lshl_add_u32 v25, v15, 7, v5\nv_lshl_add_u32 v26, v17, 7, v4\n"
      "v_lshl_add_u32 v27, v19, 7, v5\nv_lshl_add_u32 v28, v21, 7, v4\ns_add_u32 m0, s53, 0\ns_nop 0\nglobal_load_lds_dwordx4 v22, s[6:7] offset:0\n"
      "global_load_lds_dwordx4 v23, s[44:45] offset:1024\nglobal_load_lds_dwordx4 v24, s[46:47] offset:2048\nglobal_load_lds_dwordx4 v25, s[48:49] offset:3072\ns_add_u32 m0, s53, 4096\ns_nop 0\n"
      "global_load_lds_dwordx4 v26, s[6:7] offset:0\nglobal_load_lds_dwordx4 v27, s[44:45] offset:1024\ns_cbranch_vccz .La1002\nglobal_load_lds_dwordx4 v28, s[46:47] offset:2048\n"
      ".La1002:\nds_read_b64 v[8:9], v6 offset:8\nds_read_b64 v[10:11], v6 offset:904\nds_read_b64 v[12:13], v6 offset:1800\nds_read_b64 v[14:15], v6 offset:2696\n"
      "ds_read_b64 v[16:17], v6 offset:3592\nds_read_b64 v[18:19], v6 offset:4488\nds_read_b64 v[20:21], v7 offset:8\nv_lshrrev_b32 v30, 2, v34\nv_bfe_u32 v31, v34, 1, 1\ns_lshl_b32 s42, s36, 1\n"
      "v_lshrrev_b32 v32, 3, v34\nv_or_b32 v31, s42, v31\ns_lshl_b32 s42, s33, 11\nv_xor_b32 v31, v31, v32\nv_lshl_add_u32 v30, v30, 7, s42\nv_and_b32 v32, 1, v34\nv_lshl_add_u32 v30, v31, 4, v30\n"
      "v_lshl_add_u32 v30, v32, 3, v30\nv_add_u32 v31, 90112, v30\nv_mov_b32 v32, 0\nv_mov_b32 v33, 0x3d800000\ns_waitcnt lgkmcnt(0)\nv_lshl_add_u32 v22, v8, 7, v4\nv_lshl_add_u32 v23, v10, 7, v5\n"
      "v_lshl_add_u32 v24, v12, 7, v4\nv_lshl_add_u32 v25, v14, 7, v5\nv_lshl_add_u32 v26, v16, 7, v4\nv_lshl_add_u32 v27, v18, 7, v5\nv_lshl_add_u32 v28, v20, 7, v4\n"
      "s_waitcnt vmcnt(7) lgkmcnt(0)\ns_cbranch_vccnz .Lw1003\ns_waitcnt vmcnt(6)\n"
      ".Lw1003:\ns_barrier\ns_add_u32 m0, s54, 45056\ns_nop 0\nglobal_load_lds_dwordx4 v236, s[50:51] offset:0\nglobal_load_lds_dwordx4 v237, s[50:51] offset:1024\n"
      "global_load_lds_dwordx4 v238, s[50:51] offset:2048\nglobal_load_lds_dwordx4 v239, s[50:51] offset:3072\ns_add_u32 m0, s53, 45056\ns_nop 0\nglobal_load_lds_dwordx4 v22, s[6:7] offset:0\n"
      "global_load_lds_dwordx4 v23, s[44:45] offset:1024\nglobal_load_lds_dwordx4 v24, s[46:47] offset:2048\nglobal_load_lds_dwordx4 v25, s[48:49] offset:3072\ns_add_u32 m0, s53, 49152\ns_nop 0\n"
      "global_load_lds_dwordx4 v26, s[6:7] offset:0\nglobal_load_lds_dwordx4 v27, s[44:45] offset:1024\ns_cbranch_vccz .La1004\nglobal_load_lds_dwordx4 v28, s[46:47] offset:2048\n"
      ".La1004:\nds_read_b64 v[44:45], v31 offset:0\nv_lshl_add_u32 v22, v9, 7, v4\nv_lshl_add_u32 v23, v11, 7, v5\nv_lshl_add_u32 v24, v13, 7, v4\nv_lshl_add_u32 v25, v15, 7, v5\n"
      "v_lshl_add_u32 v26, v17, 7, v4\nv_lshl_add_u32 v27, v19, 7, v5\nv_lshl_add_u32 v28, v21, 7, v4\nds_read_b64 v[8:9], v6 offset:16\nds_read_b64 v[10:11], v6 offset:912\n"
      "ds_read_b64 v[12:13], v6 offset:1808\nds_read_b64 v[14:15], v6 offset:2704\nds_read_b64 v[16:17], v6 offset:3600\nds_read_b64 v[18:19], v6 offset:4496\nds_read_b64 v[20:21], v7 offset:16\n"
      "s_add_u32 s52, s52, 212992\ns_cmp_eq_u32 s52, 5537792\ns_cselect_b32 s52, 0, s52\ns_add_u32 s50, s10, s52\ns_addc_u32 s51, s11, 0\ns_mov_b32 s55, 0x80008000\ns_waitcnt vmcnt(11) lgkmcnt(0)\n"
      "s_cbranch_vccnz .Lw1005\ns_waitcnt vmcnt(10)\n"
      ".Lw1005:\ns_barrier\ns_add_u32 m0, s54, 90112\ns_nop 0\nglobal_load_lds_dwordx4 v236, s[50:51] offset:0\nglobal_load_lds_dwordx4 v237, s[50:51] offset:1024\n"
      "global_load_lds_dwordx4 v238, s[50:51] offset:2048\nglobal_load_lds_dwordx4 v239, s[50:51] offset:3072\ns_add_u32 s52, s52, 212992\ns_cmp_eq_u32 s52, 5537792\ns_cselect_b32 s52, 0, s52\n"
      "s_add_u32 s50, s10, s52\ns_addc_u32 s51, s11, 0\ns_add_u32 m0, s53, 90112\ns_nop 0\nglobal_load_lds_dwordx4 v22, s[6:7] offset:0\nv_lshl_add_u32 v22, v8, 7, v4\n"
      "global_load_lds_dwordx4 v23, s[44:45] offset:1024\nv_lshl_add_u32 v23, v10, 7, v5\nglobal_load_lds_dwordx4 v24, s[46:47] offset:2048\nv_lshl_add_u32 v24, v12, 7, v4\n"
      "ds_read_b64 v[46:47], v30 offset:0\nglobal_load_lds_dwordx4 v25, s[48:49] offset:3072\nv_lshl_add_u32 v25, v14, 7, v5\nv_max3_f16 v32, |v44|, |v45|, v32\nv_max3_f32 v33, |v44|, |v45|, v33\n"
      "s_add_u32 m0, s53, 94208\ns_nop 0\nglobal_load_lds_dwordx4 v26, s[6:7] offset:0\nv_lshl_add_u32 v26, v16, 7, v4\nv_and_b32 v148, s55, v44\nv_xor_b32 v44, v44, v148\n"
      "global_load_lds_dwordx4 v27, s[44:45] offset:1024\nv_lshl_add_u32 v27, v18, 7, v5\nv_and_b32 v149, s55, v45\nv_xor_b32 v45, v45, v149\ns_cbranch_vccz .La1006\n"
      "global_load_lds_dwordx4 v28, s[46:47] offset:2048\n"
      ".La1006:\nv_lshl_add_u32 v28, v20, 7, v4\ns_lshl_b32 s42, s24, 6\nv_add_u32 v39, s42, v34\nv_lshlrev_b32 v253, 3, v39\nv_lshlrev_b32 v39, 4, v39\ns_cmp_lg_u32 s31, 0\n"
      "s_cbranch_scc1 .Lb1007\ns_and_b32 s42, %[blk], 7\ns_lshl_b32 s42, s42, 11\ns_add_u32 s42, s42, 4096\ns_add_u32 s58, s20, s42\ns_addc_u32 s59, s21, 0\n"
      "global_store_dwordx2 v253, v[254:255], s[58:59] sc1\n"
      ".Lb1007:\ns_waitcnt vmcnt(11) lgkmcnt(0)\ns_cbranch_vccnz .Lw1008\ns_waitcnt vmcnt(10)\n"
      ".Lw1008:\ns_barrier\ns_add_u32 m0, s54, 0\ns_nop 0\nglobal_load_lds_dwordx4 v236, s[50:51] offset:0\nglobal_load_lds_dwordx4 v237, s[50:51] offset:1024\n"
      "global_load_lds_dwordx4 v238, s[50:51] offset:2048\nglobal_load_lds_dwordx4 v239, s[50:51] offset:3072\ns_add_u32 s52, s52, 212992\ns_cmp_eq_u32 s52, 5537792\ns_cselect_b32 s52, 0, s52\n"
      "s_add_u32 s50, s10, s52\ns_addc_u32 s51, s11, 0\ns_add_u32 m0, s53, 0\ns_nop 0\nglobal_load_lds_dwordx4 v22, s[6:7] offset:0\nv_lshl_add_u32 v22, v9, 7, v4\n"
      "ds_read_b64 v[8:9], v6 offset:24\nglobal_load_lds_dwordx4 v23, s[44:45] offset:1024\nv_lshl_add_u32 v23, v11, 7, v5\nds_read_b64 v[10:11], v6 offset:920\n"
      "ds_read_b64 v[48:49], v30 offset:45056\nglobal_load_lds_dwordx4 v24, s[46:47] offset:2048\nv_lshl_add_u32 v24, v13, 7, v4\nds_read_b64 v[12:13], v6 offset:1816\n"
      "v_max3_f16 v32, |v46|, |v47|, v32\nv_max3_f32 v33, |v46|, |v47|, v33\nglobal_load_lds_dwordx4 v25, s[48:49] offset:3072\nv_lshl_add_u32 v25, v15, 7, v5\n"
      "ds_read_b64 v[14:15], v6 offset:2712\nv_and_b32 v150, s55, v46\nv_xor_b32 v46, v46, v150\ns_add_u32 m0, s53, 4096\ns_nop 0\nglobal_load_lds_dwordx4 v26, s[6:7] offset:0\n"
      "v_lshl_add_u32 v26, v17, 7, v4\nds_read_b64 v[16:17], v6 offset:3608\nv_and_b32 v151, s55, v47\nv_xor_b32 v47, v47, v151\nglobal_load_lds_dwordx4 v27, s[44:45] offset:1024\n"
      "v_lshl_add_u32 v27, v19, 7, v5\nds_read_b64 v[18:19], v6 offset:4504\ns_lshl_b32 s42, %[blk], 3\ns_add_u32 s42, s42, 4096\ns_add_u32 s58, s20, s42\ns_addc_u32 s59, s21, 0\n"
      "v_and_b32 v41, 7, v34\nv_lshlrev_b32 v41, 11, v41\ns_add_u32 s60, s20, 1048576\ns_addc_u32 s61, s21, 0\ns_xor_b32 s40, s37, 0xa5c30f96\ns_mul_i32 s41, s37, 0x9E3779B1\n"
      "s_lshr_b32 s41, s41, 24\ns_cbranch_vccz .La1009\nglobal_load_lds_dwordx4 v28, s[46:47] offset:2048\n"
      ".La1009:\nv_lshl_add_u32 v28, v21, 7, v4\nds_read_b64 v[20:21], v7 offset:24\ns_waitcnt vmcnt(11) lgkmcnt(0)\ns_cbranch_vccnz .Lw1010\ns_waitcnt vmcnt(10)\n"
      ".Lw1010:\ns_barrier\ns_add_u32 m0, s54, 45056\ns_nop 0\nglobal_load_lds_dwordx4 v236, s[50:51] offset:0\nglobal_load_lds_dwordx4 v237, s[50:51] offset:1024\n"
      "global_load_lds_dwordx4 v238, s[50:51] offset:2048\nglobal_load_lds_dwordx4 v239, s[50:51] offset:3072\ns_add_u32 s52, s52, 212992\ns_cmp_eq_u32 s52, 5537792\ns_cselect_b32 s52, 0, s52\n"
      "s_add_u32 s50, s10, s52\ns_addc_u32 s51, s11, 0\ns_add_u32 m0, s53, 45056\ns_nop 0\nglobal_load_lds_dwordx4 v22, s[6:7] offset:0\nv_lshl_add_u32 v22, v8, 7, v4\n"
      "ds_read_b64 v[50:51], v31 offset:0\nglobal_load_lds_dwordx4 v23, s[44:45] offset:1024\nv_lshl_add_u32 v23, v10, 7, v5\nv_max3_f16 v32, |v48|, |v49|, v32\nv_max3_f32 v33, |v48|, |v49|, v33\n"
      "global_load_lds_dwordx4 v24, s[46:47] offset:2048\nv_lshl_add_u32 v24, v12, 7, v4\nv_and_b32 v152, s55, v48\nv_xor_b32 v48, v48, v152\nglobal_load_lds_dwordx4 v25, s[48:49] offset:3072\n"
      "v_lshl_add_u32 v25, v14, 7, v5\nv_and_b32 v153, s55, v49\nv_xor_b32 v49, v49, v153\ns_add_u32 m0, s53, 49152\ns_nop 0\nglobal_load_lds_dwordx4 v26, s[6:7] offset:0\n"
      "v_lshl_add_u32 v26, v16, 7, v4\nv_cmp_eq_u32 s[62:63], 0, v34\nv_mov_b32 v38, 262144\nv_mov_b32 v40, 163584\nv_cndmask_b32 v38, v38, v40, s[62:63]\ns_add_u32 s42, s37, 1\n"
      "v_mov_b32 v40, s42\nv_mov_b32 v42, 1\nglobal_load_lds_dwordx4 v27, s[44:45] offset:1024\nv_lshl_add_u32 v27, v18, 7, v5\nv_lshrrev_b32 v224, 4, v34\nv_bfe_u32 v225, v34, 2, 2\n"
      "v_and_b32 v226, 3, v34\nv_mul_u32_u24 v226, 0x80008, v226\nv_add_u32 v208, v224, v225\nv_and_b32 v208, 3, v208\nv_add_u32 v228, 22, v225\ns_cbranch_vccz .La1011\n"
      "global_load_lds_dwordx4 v28, s[46:47] offset:2048\n"
      ".La1011:\nv_lshl_add_u32 v28, v20, 7, v4\ns_waitcnt vmcnt(11) lgkmcnt(0)\ns_cbranch_vccnz .Lw1012\ns_waitcnt vmcnt(10)\n"
      ".Lw1012:\ns_barrier\ns_add_u32 m0, s54, 90112\ns_nop 0\nglobal_load_lds_dwordx4 v236, s[50:51] offset:0\nglobal_load_lds_dwordx4 v237, s[50:51] offset:1024\n"
      "global_load_lds_dwordx4 v238, s[50:51] offset:2048\nglobal_load_lds_dwordx4 v239, s[50:51] offset:3072\ns_add_u32 s52, s52, 212992\ns_cmp_eq_u32 s52, 5537792\ns_cselect_b32 s52, 0, s52\n"
      "s_add_u32 s50, s10, s52\ns_addc_u32 s51, s11, 0\ns_add_u32 m0, s53, 90112\ns_nop 0\nglobal_load_lds_dwordx4 v22, s[6:7] offset:0\nv_lshl_add_u32 v22, v9, 7, v4\n"
      "ds_read_b64 v[8:9], v6 offset:32\nglobal_load_lds_dwordx4 v23, s[44:45] offset:1024\nv_lshl_add_u32 v23, v11, 7, v5\nds_read_b64 v[10:11], v6 offset:928\nds_read_b64 v[52:53], v30 offset:0\n"
      "global_load_lds_dwordx4 v24, s[46:47] offset:2048\nv_lshl_add_u32 v24, v13, 7, v4\nds_read_b64 v[12:13], v6 offset:1824\nv_max3_f16 v32, |v50|, |v51|, v32\n"
      "v_max3_f32 v33, |v50|, |v51|, v33\nglobal_load_lds_dwordx4 v25, s[48:49] offset:3072\nv_lshl_add_u32 v25, v15, 7, v5\nds_read_b64 v[14:15], v6 offset:2720\nv_and_b32 v154, s55, v50\n"
      "v_xor_b32 v50, v50, v154\ns_add_u32 m0, s53, 94208\ns_nop 0\nglobal_load_lds_dwordx4 v26, s[6:7] offset:0\nv_lshl_add_u32 v26, v17, 7, v4\nds_read_b64 v[16:17], v6 offset:3616\n"
      "v_and_b32 v155, s55, v51\nv_xor_b32 v51, v51, v155\nglobal_load_lds_dwordx4 v27, s[44:45] offset:1024\nv_lshl_add_u32 v27, v19, 7, v5\nds_read_b64 v[18:19], v6 offset:4512\n"
      "v_cmp_gt_u32 s[62:63], 2, v225\nv_lshrrev_b32 v225, 2, v34\nv_lshlrev_b32 v225, 2, v225\nv_cndmask_b32 v228, v228, v208, s[62:63]\nv_lshl_or_b32 v208, v208, 16, v228\n"
      "v_lshl_add_u32 v208, v208, 5, v226\ns_cbranch_vccz .La1013\nglobal_load_lds_dwordx4 v28, s[46:47] offset:2048\n"
      ".La1013:\nv_lshl_add_u32 v28, v21, 7, v4\nds_read_b64 v[20:21], v7 offset:32\ns_waitcnt vmcnt(11) lgkmcnt(0)\ns_cbranch_vccnz .Lw1014\ns_waitcnt vmcnt(10)\n"
      ".Lw1014:\ns_barrier\ns_add_u32 m0, s54, 0\ns_nop 0\nglobal_load_lds_dwordx4 v236, s[50:51] offset:0\nglobal_load_lds_dwordx4 v237, s[50:51] offset:1024\n"
      "global_load_lds_dwordx4 v238, s[50:51] offset:2048\nglobal_load_lds_dwordx4 v239, s[50:51] offset:3072\ns_add_u32 s52, s52, 212992\ns_cmp_eq_u32 s52, 5537792\ns_cselect_b32 s52, 0, s52\n"
      "s_add_u32 s50, s10, s52\ns_addc_u32 s51, s11, 0\ns_add_u32 m0, s53, 0\ns_nop 0\nglobal_load_lds_dwordx4 v22, s[6:7] offset:0\nv_lshl_add_u32 v22, v8, 7, v4\n"
      "ds_read_b64 v[54:55], v30 offset:45056\nglobal_load_lds_dwordx4 v23, s[44:45] offset:1024\nv_lshl_add_u32 v23, v10, 7, v5\nv_max3_f16 v32, |v52|, |v53|, v32\n"
      "v_max3_f32 v33, |v52|, |v53|, v33\nglobal_load_lds_dwordx4 v24, s[46:47] offset:2048\nv_lshl_add_u32 v24, v12, 7, v4\nv_and_b32 v156, s55, v52\nv_xor_b32 v52, v52, v156\n"
      "global_load_lds_dwordx4 v25, s[48:49] offset:3072\nv_lshl_add_u32 v25, v14, 7, v5\nv_and_b32 v157, s55, v53\nv_xor_b32 v53, v53, v157\ns_add_u32 m0, s53, 4096\ns_nop 0\n"
      "global_load_lds_dwordx4 v26, s[6:7] offset:0\nv_lshl_add_u32 v26, v16, 7, v4\ns_mov_b32 s42, 0x76325410\ns_mov_b32 s43, 0x21005410\nv_lshrrev_b64 v[228:229], v225, s[42:43]\n"
      "v_and_b32 v228, 7, v228\nv_lshl_add_u32 v228, v224, 3, v228\ns_mov_b32 s42, 0x54107632\ns_mov_b32 s43, 0x21107632\nv_lshrrev_b64 v[230:231], v225, s[42:43]\nv_and_b32 v230, 7, v230\n"
      "v_lshl_add_u32 v230, v224, 3, v230\nglobal_load_lds_dwordx4 v27, s[44:45] offset:1024\nv_lshl_add_u32 v27, v18, 7, v5\nv_lshl_or_b32 v209, v230, 16, v228\n"
      "v_lshl_add_u32 v209, v209, 5, v226\nv_lshlrev_b32 v206, 3, v34\nv_add_u32 v206, 159712, v206\nv_add_u32 v207, 45056, v39\ns_cbranch_vccz .La1015\n"
      "global_load_lds_dwordx4 v28, s[46:47] offset:2048\n"
      ".La1015:\nv_lshl_add_u32 v28, v20, 7, v4\ns_waitcnt vmcnt(11) lgkmcnt(0)\ns_cbranch_vccnz .Lw1016\ns_waitcnt vmcnt(10)\n"
      ".Lw1016:\ns_barrier\ns_add_u32 m0, s54, 45056\ns_nop 0\nglobal_load_lds_dwordx4 v236, s[50:51] offset:0\nglobal_load_lds_dwordx4 v237, s[50:51] offset:1024\n"
      "global_load_lds_dwordx4 v238, s[50:51] offset:2048\nglobal_load_lds_dwordx4 v239, s[50:51] offset:3072\ns_add_u32 s52, s52, 212992\ns_cmp_eq_u32 s52, 5537792\ns_cselect_b32 s52, 0, s52\n"
      "s_add_u32 s50, s10, s52\ns_addc_u32 s51, s11, 0\ns_add_u32 m0, s53, 45056\ns_nop 0\nglobal_load_lds_dwordx4 v22, s[6:7] offset:0\nv_lshl_add_u32 v22, v9, 7, v4\n"
      "ds_read_b64 v[8:9], v6 offset:40\nglobal_load_lds_dwordx4 v23, s[44:45] offset:1024\nv_lshl_add_u32 v23, v11, 7, v5\nds_read_b64 v[10:11], v6 offset:936\nds_read_b64 v[56:57], v31 offset:0\n"
      "global_load_lds_dwordx4 v24, s[46:47] offset:2048\nv_lshl_add_u32 v24, v13, 7, v4\nds_read_b64 v[12:13], v6 offset:1832\nv_max3_f16 v32, |v54|, |v55|, v32\n"
      "v_max3_f32 v33, |v54|, |v55|, v33\nglobal_load_lds_dwordx4 v25, s[48:49] offset:3072\nv_lshl_add_u32 v25, v15, 7, v5\nds_read_b64 v[14:15], v6 offset:2728\nv_and_b32 v158, s55, v54\n"
      "v_xor_b32 v54, v54, v158\ns_add_u32 m0, s53, 49152\ns_nop 0\nglobal_load_lds_dwordx4 v26, s[6:7] offset:0\nv_lshl_add_u32 v26, v17, 7, v4\nds_read_b64 v[16:17], v6 offset:3624\n"
      "v_and_b32 v159, s55, v55\nv_xor_b32 v55, v55, v159\nglobal_load_lds_dwordx4 v27, s[44:45] offset:1024\nv_lshl_add_u32 v27, v19, 7, v5\nds_read_b64 v[18:19], v6 offset:4520\n"
      "v_lshrrev_b32 v224, 2, v34\nv_cmp_gt_u32 s[62:63], s64, v224\nv_mov_b32 v201, 262144\ns_mul_i32 s42, s36, 17280\ns_add_u32 s42, s42, 90112\nv_cndmask_b32 v201, v201, 0, s[62:63]\n"
      "v_and_b32 v200, 3, v34\nv_lshl_add_u32 v200, v200, 3, s42\nv_mul_u32_u24 v225, 864, v224\nv_add3_u32 v200, v200, v225, v201\ns_cbranch_vccz .La1017\n"
      "global_load_lds_dwordx4 v28, s[46:47] offset:2048\n"
      ".La1017:\nv_lshl_add_u32 v28, v21, 7, v4\nds_read_b64 v[20:21], v7 offset:40\ns_waitcnt vmcnt(11) lgkmcnt(0)\ns_cbranch_vccnz .Lw1018\ns_waitcnt vmcnt(10)\n"
      ".Lw1018:\ns_barrier\ns_add_u32 m0, s54, 90112\ns_nop 0\nglobal_load_lds_dwordx4 v236, s[50:51] offset:0\nglobal_load_lds_dwordx4 v237, s[50:51] offset:1024\n"
      "global_load_lds_dwordx4 v238, s[50:51] offset:2048\nglobal_load_lds_dwordx4 v239, s[50:51] offset:3072\ns_add_u32 s52, s52, 212992\ns_cmp_eq_u32 s52, 5537792\ns_cselect_b32 s52, 0, s52\n"
      "s_add_u32 s50, s10, s52\ns_addc_u32 s51, s11, 0\ns_add_u32 m0, s53, 90112\ns_nop 0\nglobal_load_lds_dwordx4 v22, s[6:7] offset:0\nv_lshl_add_u32 v22, v8, 7, v4\n"
      "ds_read_b64 v[58:59], v30 offset:0\nglobal_load_lds_dwordx4 v23, s[44:45] offset:1024\nv_lshl_add_u32 v23, v10, 7, v5\nv_max3_f16 v32, |v56|, |v57|, v32\nv_max3_f32 v33, |v56|, |v57|, v33\n"
      "global_load_lds_dwordx4 v24, s[46:47] offset:2048\nv_lshl_add_u32 v24, v12, 7, v4\nv_and_b32 v160, s55, v56\nv_xor_b32 v56, v56, v160\nglobal_load_lds_dwordx4 v25, s[48:49] offset:3072\n"
      "v_lshl_add_u32 v25, v14, 7, v5\nv_and_b32 v161, s55, v57\nv_xor_b32 v57, v57, v161\ns_add_u32 m0, s53, 94208\ns_nop 0\nglobal_load_lds_dwordx4 v26, s[6:7] offset:0\n"
      "v_lshl_add_u32 v26, v16, 7, v4\ns_lshl_b32 s42, s36, 2\ns_add_u32 s43, s42, 159232\nv_lshl_add_u32 v202, v224, 4, v201\nv_add_u32 v202, s43, v202\nv_lshl_add_u32 v203, v224, 3, v201\n"
      "v_add_u32 v203, 159552, v203\nv_lshl_add_u32 v204, v224, 1, s34\nglobal_load_lds_dwordx4 v27, s[44:45] offset:1024\nv_lshl_add_u32 v27, v18, 7, v5\ns_sub_u32 s42, 20, s64\n"
      "s_min_u32 s42, s42, 6\ns_bfm_b32 s42, s42, 0\ns_lshl_b32 s43, 0x11, s36\ns_and_b32 s42, s42, s43\ns_lshr_b32 s43, s25, 1\ns_mul_i32 s43, s43, 3\ns_sub_u32 s43, 6, s43\n"
      "s_lshl_b32 s56, s42, s43\ns_mov_b64 s[68:69], 1\nv_mov_b32 v140, 63\nv_mov_b32 v43, 0\nv_mov_b32 v144, 163648\ns_cbranch_vccz .La1019\nglobal_load_lds_dwordx4 v28, s[46:47] offset:2048\n"
      ".La1019:\nv_lshl_add_u32 v28, v20, 7, v4\ns_waitcnt vmcnt(11) lgkmcnt(0)\ns_cbranch_vccnz .Lw1020\ns_waitcnt vmcnt(10)\n"
      ".Lw1020:\ns_barrier\ns_add_u32 m0, s54, 0\ns_nop 0\nglobal_load_lds_dwordx4 v236, s[50:51] offset:0\nglobal_load_lds_dwordx4 v237, s[50:51] offset:1024\n"
      "global_load_lds_dwordx4 v238, s[50:51] offset:2048\nglobal_load_lds_dwordx4 v239, s[50:51] offset:3072\ns_add_u32 s52, s52, 212992\ns_cmp_eq_u32 s52, 5537792\ns_cselect_b32 s52, 0, s52\n"
      "s_add_u32 s50, s10, s52\ns_addc_u32 s51, s11, 0\ns_add_u32 m0, s53, 0\ns_nop 0\nglobal_load_lds_dwordx4 v22, s[6:7] offset:0\nv_lshl_add_u32 v22, v9, 7, v4\n"
      "ds_read_b64 v[8:9], v6 offset:48\nglobal_load_lds_dwordx4 v23, s[44:45] offset:1024\nv_lshl_add_u32 v23, v11, 7, v5\nds_read_b64 v[10:11], v6 offset:944\n"
      "ds_read_b64 v[60:61], v30 offset:45056\nglobal_load_lds_dwordx4 v24, s[46:47] offset:2048\nv_lshl_add_u32 v24, v13, 7, v4\nds_read_b64 v[12:13], v6 offset:1840\n"
      "v_max3_f16 v32, |v58|, |v59|, v32\nv_max3_f32 v33, |v58|, |v59|, v33\nglobal_load_lds_dwordx4 v25, s[48:49] offset:3072\nv_lshl_add_u32 v25, v15, 7, v5\n"
      "ds_read_b64 v[14:15], v6 offset:2736\nv_and_b32 v162, s55, v58\nv_xor_b32 v58, v58, v162\ns_add_u32 m0, s53, 4096\ns_nop 0\nglobal_load_lds_dwordx4 v26, s[6:7] offset:0\n"
      "v_lshl_add_u32 v26, v17, 7, v4\nds_read_b64 v[16:17], v6 offset:3632\nv_and_b32 v163, s55, v59\nv_xor_b32 v59, v59, v163\nglobal_load_lds_dwordx4 v27, s[44:45] offset:1024\n"
      "v_lshl_add_u32 v27, v19, 7, v5\nds_read_b64 v[18:19], v6 offset:4528\nv_and_b32 v224, 3, v34\nv_lshrrev_b32 v225, 3, v34\nv_lshl_add_u32 v224, v224, 3, v225\nv_min_u32 v224, 25, v224\n"
      "v_lshlrev_b32 v145, 2, v224\nv_and_b32 v224, 7, v34\nv_lshlrev_b32 v146, 4, v224\ns_cbranch_vccz .La1021\nglobal_load_lds_dwordx4 v28, s[46:47] offset:2048\n"
      ".La1021:\nv_lshl_add_u32 v28, v21, 7, v4\nds_read_b64 v[20:21], v7 offset:48\ns_waitcnt vmcnt(11) lgkmcnt(0)\ns_cbranch_vccnz .Lw1022\ns_waitcnt vmcnt(10)\n"
      ".Lw1022:\ns_barrier\ns_add_u32 m0, s54, 45056\ns_nop 0\nglobal_load_lds_dwordx4 v236, s[50:51] offset:0\nglobal_load_lds_dwordx4 v237, s[50:51] offset:1024\n"
      "global_load_lds_dwordx4 v238, s[50:51] offset:2048\nglobal_load_lds_dwordx4 v239, s[50:51] offset:3072\ns_add_u32 s52, s52, 212992\ns_cmp_eq_u32 s52, 5537792\ns_cselect_b32 s52, 0, s52\n"
      "s_add_u32 s50, s10, s52\ns_addc_u32 s51, s11, 0\ns_add_u32 m0, s53, 45056\ns_nop 0\nglobal_load_lds_dwordx4 v22, s[6:7] offset:0\nv_lshl_add_u32 v22, v8, 7, v4\n"
      "ds_read_b64 v[62:63], v31 offset:0\nglobal_load_lds_dwordx4 v23, s[44:45] offset:1024\nv_lshl_add_u32 v23, v10, 7, v5\nv_max3_f16 v32, |v60|, |v61|, v32\nv_max3_f32 v33, |v60|, |v61|, v33\n"
      "global_load_lds_dwordx4 v24, s[46:47] offset:2048\nv_lshl_add_u32 v24, v12, 7, v4\nv_and_b32 v164, s55, v60\nv_xor_b32 v60, v60, v164\nglobal_load_lds_dwordx4 v25, s[48:49] offset:3072\n"
      "v_lshl_add_u32 v25, v14, 7, v5\nv_and_b32 v165, s55, v61\nv_xor_b32 v61, v61, v165\ns_add_u32 m0, s53, 49152\ns_nop 0\nglobal_load_lds_dwordx4 v26, s[6:7] offset:0\n"
      "v_lshl_add_u32 v26, v16, 7, v4\nv_bfe_u32 v224, v34, 1, 2\nv_mul_u32_u24 v226, 17280, v224\nv_lshl_add_u32 v226, v225, 5, v226\nv_lshlrev_b32 v205, 2, v224\nv_and_b32 v224, 1, v34\n"
      "v_lshl_add_u32 v147, v224, 4, v226\nglobal_load_lds_dwordx4 v27, s[44:45] offset:1024\nv_lshl_add_u32 v27, v18, 7, v5\ns_add_u32 s42, s27, 0\ns_sub_u32 s43, s42, 26\n"
      "s_min_u32 s42, s42, s43\nv_lshl_add_u32 v210, s42, 5, v200\ns_add_u32 s42, s27, 2\ns_sub_u32 s43, s42, 26\ns_min_u32 s42, s42, s43\nv_lshl_add_u32 v211, s42, 5, v200\ns_add_u32 s42, s27, 4\n"
      "s_sub_u32 s43, s42, 26\ns_min_u32 s42, s42, s43\nv_lshl_add_u32 v212, s42, 5, v200\ns_add_u32 s42, s27, 6\ns_sub_u32 s43, s42, 26\ns_min_u32 s42, s42, s43\n"
      "v_lshl_add_u32 v213, s42, 5, v200\ns_add_u32 s42, s27, 8\ns_sub_u32 s43, s42, 26\ns_min_u32 s42, s42, s43\nv_lshl_add_u32 v214, s42, 5, v200\ns_cbranch_vccz .La1023\n"
      "global_load_lds_dwordx4 v28, s[46:47] offset:2048\n"
      ".La1023:\nv_lshl_add_u32 v28, v20, 7, v4\ns_waitcnt vmcnt(11) lgkmcnt(0)\ns_cbranch_vccnz .Lw1024\ns_waitcnt vmcnt(10)\n"
      ".Lw1024:\ns_barrier\ns_add_u32 m0, s54, 90112\ns_nop 0\nglobal_load_lds_dwordx4 v236, s[50:51] offset:0\nglobal_load_lds_dwordx4 v237, s[50:51] offset:1024\n"
      "global_load_lds_dwordx4 v238, s[50:51] offset:2048\nglobal_load_lds_dwordx4 v239, s[50:51] offset:3072\ns_add_u32 s52, s52, 212992\ns_cmp_eq_u32 s52, 5537792\ns_cselect_b32 s52, 0, s52\n"
      "s_add_u32 s50, s10, s52\ns_addc_u32 s51, s11, 0\ns_add_u32 m0, s53, 90112\ns_nop 0\nglobal_load_lds_dwordx4 v22, s[6:7] offset:0\nv_lshl_add_u32 v22, v9, 7, v4\n"
      "ds_read_b64 v[8:9], v6 offset:56\nglobal_load_lds_dwordx4 v23, s[44:45] offset:1024\nv_lshl_add_u32 v23, v11, 7, v5\nds_read_b64 v[10:11], v6 offset:952\nds_read_b64 v[64:65], v30 offset:0\n"
      "global_load_lds_dwordx4 v24, s[46:47] offset:2048\nv_lshl_add_u32 v24, v13, 7, v4\nds_read_b64 v[12:13], v6 offset:1848\nv_max3_f16 v32, |v62|, |v63|, v32\n"
      "v_max3_f32 v33, |v62|, |v63|, v33\nglobal_load_lds_dwordx4 v25, s[48:49] offset:3072\nv_lshl_add_u32 v25, v15, 7, v5\nds_read_b64 v[14:15], v6 offset:2744\nv_and_b32 v166, s55, v62\n"
      "v_xor_b32 v62, v62, v166\ns_add_u32 m0, s53, 94208\ns_nop 0\nglobal_load_lds_dwordx4 v26, s[6:7] offset:0\nv_lshl_add_u32 v26, v17, 7, v4\nds_read_b64 v[16:17], v6 offset:3640\n"
      "v_and_b32 v167, s55, v63\nv_xor_b32 v63, v63, v167\nglobal_load_lds_dwordx4 v27, s[44:45] offset:1024\nv_lshl_add_u32 v27, v19, 7, v5\nds_read_b64 v[18:19], v6 offset:4536\n"
      "s_add_u32 s42, s27, 10\ns_sub_u32 s43, s42, 26\ns_min_u32 s42, s42, s43\nv_lshl_add_u32 v215, s42, 5, v200\ns_add_u32 s42, s27, 12\ns_sub_u32 s43, s42, 26\ns_min_u32 s42, s42, s43\n"
      "v_lshl_add_u32 v216, s42, 5, v200\ns_add_u32 s42, s27, 14\ns_sub_u32 s43, s42, 26\ns_min_u32 s42, s42, s43\nv_lshl_add_u32 v217, s42, 5, v200\ns_add_u32 s42, s27, 16\n"
      "s_sub_u32 s43, s42, 26\ns_min_u32 s42, s42, s43\nv_lshl_add_u32 v218, s42, 5, v200\ns_add_u32 s42, s27, 18\ns_sub_u32 s43, s42, 26\ns_min_u32 s42, s42, s43\n"
      "v_lshl_add_u32 v219, s42, 5, v200\ns_cbranch_vccz .La1025\nglobal_load_lds_dwordx4 v28, s[46:47] offset:2048\n"
      ".La1025:\nv_lshl_add_u32 v28, v21, 7, v4\nds_read_b64 v[20:21], v7 offset:56\ns_waitcnt vmcnt(11) lgkmcnt(0)\ns_cbranch_vccnz .Lw1026\ns_waitcnt vmcnt(10)\n"
      ".Lw1026:\ns_barrier\ns_add_u32 m0, s54, 0\ns_nop 0\nglobal_load_lds_dwordx4 v236, s[50:51] offset:0\nglobal_load_lds_dwordx4 v237, s[50:51] offset:1024\n"
      "global_load_lds_dwordx4 v238, s[50:51] offset:2048\nglobal_load_lds_dwordx4 v239, s[50:51] offset:3072\ns_add_u32 s52, s52, 212992\ns_cmp_eq_u32 s52, 5537792\ns_cselect_b32 s52, 0, s52\n"
      "s_add_u32 s50, s10, s52\ns_addc_u32 s51, s11, 0\ns_add_u32 m0, s53, 0\ns_nop 0\nglobal_load_lds_dwordx4 v22, s[6:7] offset:0\nv_lshl_add_u32 v22, v8, 7, v4\n"
      "global_load_lds_dwordx4 v23, s[44:45] offset:1024\nv_lshl_add_u32 v23, v10, 7, v5\nds_read_b64 v[66:67], v30 offset:45056\nglobal_load_lds_dwordx4 v24, s[46:47] offset:2048\n"
      "v_lshl_add_u32 v24, v12, 7, v4\nv_max3_f16 v32, |v64|, |v65|, v32\nv_max3_f32 v33, |v64|, |v65|, v33\nglobal_load_lds_dwordx4 v25, s[48:49] offset:3072\nv_lshl_add_u32 v25, v14, 7, v5\n"
      "v_and_b32 v168, s55, v64\nv_xor_b32 v64, v64, v168\ns_add_u32 m0, s53, 4096\ns_nop 0\nglobal_load_lds_dwordx4 v26, s[6:7] offset:0\nv_lshl_add_u32 v26, v16, 7, v4\nv_and_b32 v169, s55, v65\n"
      "v_xor_b32 v65, v65, v169\nglobal_load_lds_dwordx4 v27, s[44:45] offset:1024\nv_lshl_add_u32 v27, v18, 7, v5\ns_add_u32 s42, s27, 20\ns_sub_u32 s43, s42, 26\ns_min_u32 s42, s42, s43\n"
      "v_lshl_add_u32 v220, s42, 5, v200\ns_add_u32 s42, s27, 22\ns_sub_u32 s43, s42, 26\ns_min_u32 s42, s42, s43\nv_lshl_add_u32 v221, s42, 5, v200\ns_add_u32 s42, s27, 24\n"
      "s_sub_u32 s43, s42, 26\ns_min_u32 s42, s42, s43\nv_lshl_add_u32 v222, s42, 5, v200\ns_cbranch_vccz .La1027\nglobal_load_lds_dwordx4 v28, s[46:47] offset:2048\n"
      ".La1027:\nv_lshl_add_u32 v28, v20, 7, v4\ns_waitcnt vmcnt(11) lgkmcnt(0)\ns_cbranch_vccnz .Lw1028\ns_waitcnt vmcnt(10)\n"
      ".Lw1028:\ns_barrier\ns_add_u32 m0, s54, 45056\ns_nop 0\nglobal_load_lds_dwordx4 v236, s[50:51] offset:0\nglobal_load_lds_dwordx4 v237, s[50:51] offset:1024\n"
      "global_load_lds_dwordx4 v238, s[50:51] offset:2048\nglobal_load_lds_dwordx4 v239, s[50:51] offset:3072\ns_add_u32 s52, s52, 212992\ns_cmp_eq_u32 s52, 5537792\ns_cselect_b32 s52, 0, s52\n"
      "s_add_u32 s50, s10, s52\ns_addc_u32 s51, s11, 0\ns_add_u32 m0, s53, 45056\ns_nop 0\nglobal_load_lds_dwordx4 v22, s[6:7] offset:0\nv_lshl_add_u32 v22, v9, 7, v4\n"
      "ds_read_b64 v[8:9], v6 offset:64\nglobal_load_lds_dwordx4 v23, s[44:45] offset:1024\nv_lshl_add_u32 v23, v11, 7, v5\nds_read_b64 v[10:11], v6 offset:960\n"
      "global_load_lds_dwordx4 v24, s[46:47] offset:2048\nv_lshl_add_u32 v24, v13, 7, v4\nds_read_b64 v[12:13], v6 offset:1856\nds_read_b64 v[68:69], v31 offset:0\n"
      "global_load_lds_dwordx4 v25, s[48:49] offset:3072\nv_lshl_add_u32 v25, v15, 7, v5\nds_read_b64 v[14:15], v6 offset:2752\nv_max3_f16 v32, |v66|, |v67|, v32\n"
      "v_max3_f32 v33, |v66|, |v67|, v33\ns_add_u32 m0, s53, 49152\ns_nop 0\nglobal_load_lds_dwordx4 v26, s[6:7] offset:0\nv_lshl_add_u32 v26, v17, 7, v4\nds_read_b64 v[16:17], v6 offset:3648\n"
      "v_and_b32 v170, s55, v66\nv_xor_b32 v66, v66, v170\nglobal_load_lds_dwordx4 v27, s[44:45] offset:1024\nv_lshl_add_u32 v27, v19, 7, v5\nds_read_b64 v[18:19], v6 offset:4544\n"
      "v_and_b32 v171, s55, v67\nv_xor_b32 v67, v67, v171\ns_cbranch_vccz .La1029\nglobal_load_lds_dwordx4 v28, s[46:47] offset:2048\n"
      ".La1029:\nv_lshl_add_u32 v28, v21, 7, v4\nds_read_b64 v[20:21], v7 offset:64\nglobal_load_dwordx2 v[36:37], v41, s[58:59] sc1\ns_waitcnt vmcnt(12) lgkmcnt(0)\ns_cbranch_vccnz .Lw1030\n"
      "s_waitcnt vmcnt(11)\n"
      ".Lw1030:\ns_barrier\ns_add_u32 m0, s54, 90112\ns_nop 0\nglobal_load_lds_dwordx4 v236, s[50:51] offset:0\nglobal_load_lds_dwordx4 v237, s[50:51] offset:1024\n"
      "global_load_lds_dwordx4 v238, s[50:51] offset:2048\nglobal_load_lds_dwordx4 v239, s[50:51] offset:3072\ns_add_u32 s52, s52, 212992\ns_cmp_eq_u32 s52, 5537792\ns_cselect_b32 s52, 0, s52\n"
      "s_add_u32 s50, s10, s52\ns_addc_u32 s51, s11, 0\ns_add_u32 m0, s53, 90112\ns_nop 0\nglobal_load_lds_dwordx4 v22, s[6:7] offset:0\nv_lshl_add_u32 v22, v8, 7, v4\n"
      "global_load_lds_dwordx4 v23, s[44:45] offset:1024\nv_lshl_add_u32 v23, v10, 7, v5\nglobal_load_lds_dwordx4 v24, s[46:47] offset:2048\nv_lshl_add_u32 v24, v12, 7, v4\n"
      "ds_read_b64 v[70:71], v30 offset:0\nglobal_load_lds_dwordx4 v25, s[48:49] offset:3072\nv_lshl_add_u32 v25, v14, 7, v5\nv_max3_f16 v32, |v68|, |v69|, v32\nv_max3_f32 v33, |v68|, |v69|, v33\n"
      "s_add_u32 m0, s53, 94208\ns_nop 0\nglobal_load_lds_dwordx4 v26, s[6:7] offset:0\nv_lshl_add_u32 v26, v16, 7, v4\nv_and_b32 v172, s55, v68\nv_xor_b32 v68, v68, v172\n"
      "global_load_lds_dwordx4 v27, s[44:45] offset:1024\nv_lshl_add_u32 v27, v18, 7, v5\nv_and_b32 v173, s55, v69\nv_xor_b32 v69, v69, v173\ns_cbranch_vccz .La1031\n"
      "global_load_lds_dwordx4 v28, s[46:47] offset:2048\n"
      ".La1031:\nv_lshl_add_u32 v28, v20, 7, v4\ns_waitcnt vmcnt(12) lgkmcnt(0)\ns_cbranch_vccnz .Lw1032\ns_waitcnt vmcnt(11)\n"
      ".Lw1032:\ns_barrier\ns_add_u32 m0, s54, 0\ns_nop 0\nglobal_load_lds_dwordx4 v236, s[50:51] offset:0\nglobal_load_lds_dwordx4 v237, s[50:51] offset:1024\n"
      "global_load_lds_dwordx4 v238, s[50:51] offset:2048\nglobal_load_lds_dwordx4 v239, s[50:51] offset:3072\ns_add_u32 s52, s52, 212992\ns_cmp_eq_u32 s52, 5537792\ns_cselect_b32 s52, 0, s52\n"
      "s_add_u32 s50, s10, s52\ns_addc_u32 s51, s11, 0\ns_add_u32 m0, s53, 0\ns_nop 0\nglobal_load_lds_dwordx4 v22, s[6:7] offset:0\nv_lshl_add_u32 v22, v9, 7, v4\n"
      "ds_read_b64 v[8:9], v6 offset:72\nglobal_load_lds_dwordx4 v23, s[44:45] offset:1024\nv_lshl_add_u32 v23, v11, 7, v5\nds_read_b64 v[10:11], v6 offset:968\n"
      "global_load_lds_dwordx4 v24, s[46:47] offset:2048\nv_lshl_add_u32 v24, v13, 7, v4\nds_read_b64 v[12:13], v6 offset:1864\nds_read_b64 v[72:73], v30 offset:45056\n"
      "global_load_lds_dwordx4 v25, s[48:49] offset:3072\nv_lshl_add_u32 v25, v15, 7, v5\nds_read_b64 v[14:15], v6 offset:2760\nv_max3_f16 v32, |v70|, |v71|, v32\n"
      "v_max3_f32 v33, |v70|, |v71|, v33\ns_add_u32 m0, s53, 4096\ns_nop 0\nglobal_load_lds_dwordx4 v26, s[6:7] offset:0\nv_lshl_add_u32 v26, v17, 7, v4\nds_read_b64 v[16:17], v6 offset:3656\n"
      "v_and_b32 v174, s55, v70\nv_xor_b32 v70, v70, v174\nglobal_load_lds_dwordx4 v27, s[44:45] offset:1024\nv_lshl_add_u32 v27, v19, 7, v5\nds_read_b64 v[18:19], v6 offset:4552\n"
      "v_and_b32 v175, s55, v71\nv_xor_b32 v71, v71, v175\ns_cbranch_vccz .La1033\nglobal_load_lds_dwordx4 v28, s[46:47] offset:2048\n"
      ".La1033:\nv_lshl_add_u32 v28, v21, 7, v4\nds_read_b64 v[20:21], v7 offset:72\ns_waitcnt vmcnt(11) lgkmcnt(0)\ns_cbranch_vccnz .Lw1034\ns_waitcnt vmcnt(10)\n"
      ".Lw1034:\ns_barrier\ns_add_u32 m0, s54, 45056\ns_nop 0\nglobal_load_lds_dwordx4 v236, s[50:51] offset:0\nglobal_load_lds_dwordx4 v237, s[50:51] offset:1024\n"
      "global_load_lds_dwordx4 v238, s[50:51] offset:2048\nglobal_load_lds_dwordx4 v239, s[50:51] offset:3072\ns_add_u32 s52, s52, 212992\ns_cmp_eq_u32 s52, 5537792\ns_cselect_b32 s52, 0, s52\n"
      "s_add_u32 s50, s10, s52\ns_addc_u32 s51, s11, 0\ns_add_u32 m0, s53, 45056\ns_nop 0\nglobal_load_lds_dwordx4 v22, s[6:7] offset:0\nv_lshl_add_u32 v22, v8, 7, v4\n"
      "global_load_lds_dwordx4 v23, s[44:45] offset:1024\nv_lshl_add_u32 v23, v10, 7, v5\nglobal_load_lds_dwordx4 v24, s[46:47] offset:2048\nv_lshl_add_u32 v24, v12, 7, v4\n"
      "ds_read_b64 v[74:75], v31 offset:0\nglobal_load_lds_dwordx4 v25, s[48:49] offset:3072\nv_lshl_add_u32 v25, v14, 7, v5\nv_max3_f16 v32, |v72|, |v73|, v32\nv_max3_f32 v33, |v72|, |v73|, v33\n"
      "s_add_u32 m0, s53, 49152\ns_nop 0\nglobal_load_lds_dwordx4 v26, s[6:7] offset:0\nv_lshl_add_u32 v26, v16, 7, v4\nv_and_b32 v176, s55, v72\nv_xor_b32 v72, v72, v176\n"
      "global_load_lds_dwordx4 v27, s[44:45] offset:1024\nv_lshl_add_u32 v27, v18, 7, v5\nv_and_b32 v177, s55, v73\nv_xor_b32 v73, v73, v177\ns_cbranch_vccz .La1035\n"
      "global_load_lds_dwordx4 v28, s[46:47] offset:2048\n"
      ".La1035:\nv_lshl_add_u32 v28, v20, 7, v4\ns_mov_b32 s42, 0\n"
      ".Lq1036:\nv_and_b32 v224, 0xff, v36\nv_xor_b32 v224, s41, v224\nv_xor_b32 v225, s40, v37\nv_or_b32 v224, v224, v225\nv_cmp_ne_u32 s[62:63], 0, v224\nv_readfirstlane_b32 s39, v36\n"
      "s_cmp_eq_u64 s[62:63], 0\ns_cbranch_scc1 .Lr1037\ns_sleep 1\nglobal_load_dwordx2 v[36:37], v41, s[58:59] sc1\ns_add_u32 s42, s42, 1\ns_bitcmp0_b32 s42, 22\ns_waitcnt vmcnt(0)\n"
      "s_cbranch_scc1 .Lq1036\n"
      ".Lr1037:\ns_lshr_b32 s39, s39, 8\nv_mov_b32 v224, s39\nds_write_b32 v38, v224 offset:24\ns_lshr_b32 s42, %[blk], 4\ns_lshl_b32 s42, s42, 7\ns_add_u32 s42, s42, 32768\n"
      "s_add_u32 s58, s20, s42\ns_addc_u32 s59, s21, 0\ns_waitcnt vmcnt(11) lgkmcnt(0)\ns_cbranch_vccnz .Lw1038\ns_waitcnt vmcnt(10)\n"
      ".Lw1038:\ns_barrier\ns_bitcmp0_b32 s56, 0\ns_cbranch_scc1 .Lt1040\ns_mov_b64 exec, s[68:69]\nglobal_atomic_add v140, v43, v42, s[58:59] sc0 sc1\ns_mov_b64 exec, -1\ns_mov_b32 s68, 2\n"
      ".Lt1040:\ns_add_u32 m0, s54, 90112\ns_nop 0\nglobal_load_lds_dwordx4 v236, s[50:51] offset:0\nglobal_load_lds_dwordx4 v237, s[50:51] offset:1024\n"
      "global_load_lds_dwordx4 v238, s[50:51] offset:2048\nglobal_load_lds_dwordx4 v239, s[50:51] offset:3072\ns_add_u32 s52, s52, 212992\ns_cmp_eq_u32 s52, 5537792\ns_cselect_b32 s52, 0, s52\n"
      "s_add_u32 s50, s10, s52\ns_addc_u32 s51, s11, 0\ns_add_u32 m0, s53, 90112\ns_nop 0\nglobal_load_lds_dwordx4 v22, s[6:7] offset:0\nv_lshl_add_u32 v22, v9, 7, v4\n"
      "ds_read_b64 v[8:9], v6 offset:80\nglobal_load_lds_dwordx4 v23, s[44:45] offset:1024\nv_lshl_add_u32 v23, v11, 7, v5\nds_read_b64 v[10:11], v6 offset:976\ns_bitcmp0_b32 s56, 1\n"
      "s_cbranch_scc1 .Lt1041\ns_mov_b64 exec, s[68:69]\nglobal_atomic_add v140, v43, v42, s[58:59] sc0 sc1\ns_mov_b64 exec, -1\ns_mov_b32 s68, 2\n"
      ".Lt1041:\nglobal_load_lds_dwordx4 v24, s[46:47] offset:2048\nv_lshl_add_u32 v24, v13, 7, v4\nds_read_b64 v[12:13], v6 offset:1872\nds_read_b64 v[76:77], v30 offset:0\n"
      "global_load_lds_dwordx4 v25, s[48:49] offset:3072\nv_lshl_add_u32 v25, v15, 7, v5\nds_read_b64 v[14:15], v6 offset:2768\nv_max3_f16 v32, |v74|, |v75|, v32\n"
      "v_max3_f32 v33, |v74|, |v75|, v33\ns_add_u32 m0, s53, 94208\ns_nop 0\nglobal_load_lds_dwordx4 v26, s[6:7] offset:0\nv_lshl_add_u32 v26, v17, 7, v4\nds_read_b64 v[16:17], v6 offset:3664\n"
      "v_and_b32 v178, s55, v74\nv_xor_b32 v74, v74, v178\nglobal_load_lds_dwordx4 v27, s[44:45] offset:1024\nv_lshl_add_u32 v27, v19, 7, v5\nds_read_b64 v[18:19], v6 offset:4560\n"
      "v_and_b32 v179, s55, v75\nv_xor_b32 v75, v75, v179\ns_cbranch_vccz .La1039\nglobal_load_lds_dwordx4 v28, s[46:47] offset:2048\n"
      ".La1039:\nv_lshl_add_u32 v28, v21, 7, v4\nds_read_b64 v[20:21], v7 offset:80\ns_and_b32 s42, s56, 3\ns_cbranch_scc0 .Ly1043\ns_waitcnt vmcnt(12) lgkmcnt(0)\ns_cbranch_vccnz .Lw1042\n"
      "s_waitcnt vmcnt(11)\ns_branch .Lw1042\n"
      ".Ly1043:\ns_waitcnt vmcnt(11) lgkmcnt(0)\ns_cbranch_vccnz .Lw1042\ns_waitcnt vmcnt(10)\n"
      ".Lw1042:\ns_barrier\ns_bitcmp0_b32 s56, 2\ns_cbranch_scc1 .Lt1046\ns_mov_b64 exec, s[68:69]\nglobal_atomic_add v140, v43, v42, s[58:59] sc0 sc1\ns_mov_b64 exec, -1\ns_mov_b32 s68, 2\n"
      ".Lt1046:\ns_add_u32 m0, s54, 0\ns_nop 0\nglobal_load_lds_dwordx4 v236, s[50:51] offset:0\nglobal_load_lds_dwordx4 v237, s[50:51] offset:1024\n"
      "global_load_lds_dwordx4 v238, s[50:51] offset:2048\nglobal_load_lds_dwordx4 v239, s[50:51] offset:3072\ns_add_u32 s52, s52, 212992\ns_cmp_eq_u32 s52, 5537792\ns_cselect_b32 s52, 0, s52\n"
      "s_add_u32 s50, s10, s52\ns_addc_u32 s51, s11, 0\ns_add_u32 m0, s53, 0\ns_nop 0\nglobal_load_lds_dwordx4 v22, s[6:7] offset:0\nv_lshl_add_u32 v22, v8, 7, v4\n"
      "global_load_lds_dwordx4 v23, s[44:45] offset:1024\nv_lshl_add_u32 v23, v10, 7, v5\ns_bitcmp0_b32 s56, 3\ns_cbranch_scc1 .Lt1047\ns_mov_b64 exec, s[68:69]\n"
      "global_atomic_add v140, v43, v42, s[58:59] sc0 sc1\ns_mov_b64 exec, -1\ns_mov_b32 s68, 2\n"
      ".Lt1047:\nglobal_load_lds_dwordx4 v24, s[46:47] offset:2048\nv_lshl_add_u32 v24, v12, 7, v4\nds_read_b64 v[78:79], v30 offset:45056\nglobal_load_lds_dwordx4 v25, s[48:49] offset:3072\n"
      "v_lshl_add_u32 v25, v14, 7, v5\nv_max3_f16 v32, |v76|, |v77|, v32\nv_max3_f32 v33, |v76|, |v77|, v33\ns_add_u32 m0, s53, 4096\ns_nop 0\nglobal_load_lds_dwordx4 v26, s[6:7] offset:0\n"
      "v_lshl_add_u32 v26, v16, 7, v4\nv_and_b32 v180, s55, v76\nv_xor_b32 v76, v76, v180\nglobal_load_lds_dwordx4 v27, s[44:45] offset:1024\nv_lshl_add_u32 v27, v18, 7, v5\n"
      "v_and_b32 v181, s55, v77\nv_xor_b32 v77, v77, v181\ns_cbranch_vccz .La1045\nglobal_load_lds_dwordx4 v28, s[46:47] offset:2048\n"
      ".La1045:\nv_lshl_add_u32 v28, v20, 7, v4\ns_and_b32 s42, s56, 12\ns_cbranch_scc0 .Ly1049\ns_waitcnt vmcnt(12) lgkmcnt(0)\ns_cbranch_vccnz .Lw1048\ns_waitcnt vmcnt(11)\ns_branch .Lw1048\n"
      ".Ly1049:\ns_waitcnt vmcnt(11) lgkmcnt(0)\ns_cbranch_vccnz .Lw1048\ns_waitcnt vmcnt(10)\n"
      ".Lw1048:\ns_barrier\ns_bitcmp0_b32 s56, 4\ns_cbranch_scc1 .Lt1052\ns_mov_b64 exec, s[68:69]\nglobal_atomic_add v140, v43, v42, s[58:59] sc0 sc1\ns_mov_b64 exec, -1\ns_mov_b32 s68, 2\n"
      ".Lt1052:\ns_add_u32 m0, s54, 45056\ns_nop 0\nglobal_load_lds_dwordx4 v236, s[50:51] offset:0\nglobal_load_lds_dwordx4 v237, s[50:51] offset:1024\n"
      "global_load_lds_dwordx4 v238, s[50:51] offset:2048\nglobal_load_lds_dwordx4 v239, s[50:51] offset:3072\ns_add_u32 s52, s52, 212992\ns_cmp_eq_u32 s52, 5537792\ns_cselect_b32 s52, 0, s52\n"
      "s_add_u32 s50, s10, s52\ns_addc_u32 s51, s11, 0\ns_add_u32 m0, s53, 45056\ns_nop 0\nglobal_load_lds_dwordx4 v22, s[6:7] offset:0\nv_lshl_add_u32 v22, v9, 7, v4\n"
      "ds_read_b64 v[8:9], v6 offset:88\nglobal_load_lds_dwordx4 v23, s[44:45] offset:1024\nv_lshl_add_u32 v23, v11, 7, v5\nds_read_b64 v[10:11], v6 offset:984\ns_bitcmp0_b32 s56, 5\n"
      "s_cbranch_scc1 .Lt1053\ns_mov_b64 exec, s[68:69]\nglobal_atomic_add v140, v43, v42, s[58:59] sc0 sc1\ns_mov_b64 exec, -1\ns_mov_b32 s68, 2\n"
      ".Lt1053:\nglobal_load_lds_dwordx4 v24, s[46:47] offset:2048\nv_lshl_add_u32 v24, v13, 7, v4\nds_read_b64 v[12:13], v6 offset:1880\nds_read_b64 v[80:81], v31 offset:0\n"
      "global_load_lds_dwordx4 v25, s[48:49] offset:3072\nv_lshl_add_u32 v25, v15, 7, v5\nds_read_b64 v[14:15], v6 offset:2776\nv_max3_f16 v32, |v78|, |v79|, v32\n"
      "v_max3_f32 v33, |v78|, |v79|, v33\ns_add_u32 m0, s53, 49152\ns_nop 0\nglobal_load_lds_dwordx4 v26, s[6:7] offset:0\nv_lshl_add_u32 v26, v17, 7, v4\nds_read_b64 v[16:17], v6 offset:3672\n"
      "v_and_b32 v182, s55, v78\nv_xor_b32 v78, v78, v182\nglobal_load_lds_dwordx4 v27, s[44:45] offset:1024\nv_lshl_add_u32 v27, v19, 7, v5\nds_read_b64 v[18:19], v6 offset:4568\n"
      "v_and_b32 v183, s55, v79\nv_xor_b32 v79, v79, v183\ns_cbranch_vccz .La1051\nglobal_load_lds_dwordx4 v28, s[46:47] offset:2048\n"
      ".La1051:\nv_lshl_add_u32 v28, v21, 7, v4\nds_read_b64 v[20:21], v7 offset:88\ns_and_b32 s42, s56, 48\ns_cbranch_scc0 .Ly1055\ns_waitcnt vmcnt(12) lgkmcnt(0)\ns_cbranch_vccnz .Lw1054\n"
      "s_waitcnt vmcnt(11)\ns_branch .Lw1054\n"
      ".Ly1055:\ns_waitcnt vmcnt(11) lgkmcnt(0)\ns_cbranch_vccnz .Lw1054\ns_waitcnt vmcnt(10)\n"
      ".Lw1054:\ns_barrier\ns_bitcmp0_b32 s56, 6\ns_cbranch_scc1 .Lt1058\ns_mov_b64 exec, s[68:69]\nglobal_atomic_add v140, v43, v42, s[58:59] sc0 sc1\ns_mov_b64 exec, -1\ns_mov_b32 s68, 2\n"
      ".Lt1058:\ns_add_u32 m0, s54, 90112\ns_nop 0\nglobal_load_lds_dwordx4 v236, s[50:51] offset:0\nglobal_load_lds_dwordx4 v237, s[50:51] offset:1024\n"
      "global_load_lds_dwordx4 v238, s[50:51] offset:2048\nglobal_load_lds_dwordx4 v239, s[50:51] offset:3072\ns_add_u32 s52, s52, 212992\ns_cmp_eq_u32 s52, 5537792\ns_cselect_b32 s52, 0, s52\n"
      "s_add_u32 s50, s10, s52\ns_addc_u32 s51, s11, 0\ns_add_u32 m0, s53, 90112\ns_nop 0\nglobal_load_lds_dwordx4 v22, s[6:7] offset:0\nv_lshl_add_u32 v22, v8, 7, v4\n"
      "global_load_lds_dwordx4 v23, s[44:45] offset:1024\nv_lshl_add_u32 v23, v10, 7, v5\ns_bitcmp0_b32 s56, 7\ns_cbranch_scc1 .Lt1059\ns_mov_b64 exec, s[68:69]\n"
      "global_atomic_add v140, v43, v42, s[58:59] sc0 sc1\ns_mov_b64 exec, -1\ns_mov_b32 s68, 2\n"
      ".Lt1059:\nglobal_load_lds_dwordx4 v24, s[46:47] offset:2048\nv_lshl_add_u32 v24, v12, 7, v4\nds_read_b64 v[82:83], v30 offset:0\nglobal_load_lds_dwordx4 v25, s[48:49] offset:3072\n"
      "v_lshl_add_u32 v25, v14, 7, v5\nv_max3_f16 v32, |v80|, |v81|, v32\nv_max3_f32 v33, |v80|, |v81|, v33\ns_add_u32 m0, s53, 94208\ns_nop 0\nglobal_load_lds_dwordx4 v26, s[6:7] offset:0\n"
      "v_lshl_add_u32 v26, v16, 7, v4\nv_and_b32 v184, s55, v80\nv_xor_b32 v80, v80, v184\nglobal_load_lds_dwordx4 v27, s[44:45] offset:1024\nv_lshl_add_u32 v27, v18, 7, v5\n"
      "v_and_b32 v185, s55, v81\nv_xor_b32 v81, v81, v185\ns_cbranch_vccz .La1057\nglobal_load_lds_dwordx4 v28, s[46:47] offset:2048\n"
      ".La1057:\nv_lshl_add_u32 v28, v20, 7, v4\ns_and_b32 s42, s56, 192\ns_cbranch_scc0 .Ly1061\ns_waitcnt vmcnt(12) lgkmcnt(0)\ns_cbranch_vccnz .Lw1060\ns_waitcnt vmcnt(11)\ns_branch .Lw1060\n"
      ".Ly1061:\ns_waitcnt vmcnt(11) lgkmcnt(0)\ns_cbranch_vccnz .Lw1060\ns_waitcnt vmcnt(10)\n"
      ".Lw1060:\ns_barrier\ns_bitcmp0_b32 s56, 8\ns_cbranch_scc1 .Lt1064\ns_mov_b64 exec, s[68:69]\nglobal_atomic_add v140, v43, v42, s[58:59] sc0 sc1\ns_mov_b64 exec, -1\ns_mov_b32 s68, 2\n"
      ".Lt1064:\ns_add_u32 m0, s54, 0\ns_nop 0\nglobal_load_lds_dwordx4 v236, s[50:51] offset:0\nglobal_load_lds_dwordx4 v237, s[50:51] offset:1024\n"
      "global_load_lds_dwordx4 v238, s[50:51] offset:2048\nglobal_load_lds_dwordx4 v239, s[50:51] offset:3072\ns_add_u32 s52, s52, 212992\ns_cmp_eq_u32 s52, 5537792\ns_cselect_b32 s52, 0, s52\n"
      "s_add_u32 s50, s10, s52\ns_addc_u32 s51, s11, 0\ns_add_u32 m0, s53, 0\ns_nop 0\nglobal_load_lds_dwordx4 v22, s[6:7] offset:0\nv_lshl_add_u32 v22, v9, 7, v4\n"
      "ds_read_b64 v[8:9], v6 offset:96\nglobal_load_lds_dwordx4 v23, s[44:45] offset:1024\nv_lshl_add_u32 v23, v11, 7, v5\nds_read_b64 v[10:11], v6 offset:992\ns_bitcmp0_b32 s56, 9\n"
      "s_cbranch_scc1 .Lt1065\ns_mov_b64 exec, s[68:69]\nglobal_atomic_add v140, v43, v42, s[58:59] sc0 sc1\ns_mov_b64 exec, -1\ns_mov_b32 s68, 2\n"
      ".Lt1065:\nglobal_load_lds_dwordx4 v24, s[46:47] offset:2048\nv_lshl_add_u32 v24, v13, 7, v4\nds_read_b64 v[12:13], v6 offset:1888\nds_read_b64 v[84:85], v30 offset:45056\n"
      "global_load_lds_dwordx4 v25, s[48:49] offset:3072\nv_lshl_add_u32 v25, v15, 7, v5\nds_read_b64 v[14:15], v6 offset:2784\nv_max3_f16 v32, |v82|, |v83|, v32\n"
      "v_max3_f32 v33, |v82|, |v83|, v33\ns_add_u32 m0, s53, 4096\ns_nop 0\nglobal_load_lds_dwordx4 v26, s[6:7] offset:0\nv_lshl_add_u32 v26, v17, 7, v4\nds_read_b64 v[16:17], v6 offset:3680\n"
      "v_and_b32 v186, s55, v82\nv_xor_b32 v82, v82, v186\nglobal_load_lds_dwordx4 v27, s[44:45] offset:1024\nv_lshl_add_u32 v27, v19, 7, v5\nds_read_b64 v[18:19], v6 offset:4576\n"
      "v_and_b32 v187, s55, v83\nv_xor_b32 v83, v83, v187\ns_cbranch_vccz .La1063\nglobal_load_lds_dwordx4 v28, s[46:47] offset:2048\n"
      ".La1063:\nv_lshl_add_u32 v28, v21, 7, v4\nds_read_b64 v[20:21], v7 offset:96\ns_and_b32 s42, s56, 768\ns_cbranch_scc0 .Ly1067\ns_waitcnt vmcnt(12) lgkmcnt(0)\ns_cbranch_vccnz .Lw1066\n"
      "s_waitcnt vmcnt(11)\ns_branch .Lw1066\n"
      ".Ly1067:\ns_waitcnt vmcnt(11) lgkmcnt(0)\ns_cbranch_vccnz .Lw1066\ns_waitcnt vmcnt(10)\n"
      ".Lw1066:\ns_barrier\ns_bitcmp0_b32 s56, 10\ns_cbranch_scc1 .Lt1070\ns_mov_b64 exec, s[68:69]\nglobal_atomic_add v140, v43, v42, s[58:59] sc0 sc1\ns_mov_b64 exec, -1\ns_mov_b32 s68, 2\n"
      ".Lt1070:\ns_add_u32 m0, s54, 45056\ns_nop 0\nglobal_load_lds_dwordx4 v236, s[50:51] offset:0\nglobal_load_lds_dwordx4 v237, s[50:51] offset:1024\n"
      "global_load_lds_dwordx4 v238, s[50:51] offset:2048\nglobal_load_lds_dwordx4 v239, s[50:51] offset:3072\ns_add_u32 s52, s52, 212992\ns_cmp_eq_u32 s52, 5537792\ns_cselect_b32 s52, 0, s52\n"
      "s_add_u32 s50, s10, s52\ns_addc_u32 s51, s11, 0\ns_add_u32 m0, s53, 45056\ns_nop 0\nglobal_load_lds_dwordx4 v22, s[6:7] offset:0\nv_lshl_add_u32 v22, v8, 7, v4\n"
      "global_load_lds_dwordx4 v23, s[44:45] offset:1024\nv_lshl_add_u32 v23, v10, 7, v5\ns_bitcmp0_b32 s56, 11\ns_cbranch_scc1 .Lt1071\ns_mov_b64 exec, s[68:69]\n"
      "global_atomic_add v140, v43, v42, s[58:59] sc0 sc1\ns_mov_b64 exec, -1\ns_mov_b32 s68, 2\n"
      ".Lt1071:\nglobal_load_lds_dwordx4 v24, s[46:47] offset:2048\nv_lshl_add_u32 v24, v12, 7, v4\nds_read_b64 v[86:87], v31 offset:0\nglobal_load_lds_dwordx4 v25, s[48:49] offset:3072\n"
      "v_lshl_add_u32 v25, v14, 7, v5\nv_max3_f16 v32, |v84|, |v85|, v32\nv_max3_f32 v33, |v84|, |v85|, v33\ns_add_u32 m0, s53, 49152\ns_nop 0\nglobal_load_lds_dwordx4 v26, s[6:7] offset:0\n"
      "v_lshl_add_u32 v26, v16, 7, v4\nv_and_b32 v188, s55, v84\nv_xor_b32 v84, v84, v188\nglobal_load_lds_dwordx4 v27, s[44:45] offset:1024\nv_lshl_add_u32 v27, v18, 7, v5\n"
      "v_and_b32 v189, s55, v85\nv_xor_b32 v85, v85, v189\ns_cbranch_vccz .La1069\nglobal_load_lds_dwordx4 v28, s[46:47] offset:2048\n"
      ".La1069:\nv_lshl_add_u32 v28, v20, 7, v4\ns_and_b32 s42, s56, 3072\ns_cbranch_scc0 .Ly1073\ns_waitcnt vmcnt(12) lgkmcnt(0)\ns_cbranch_vccnz .Lw1072\ns_waitcnt vmcnt(11)\ns_branch .Lw1072\n"
      ".Ly1073:\ns_waitcnt vmcnt(11) lgkmcnt(0)\ns_cbranch_vccnz .Lw1072\ns_waitcnt vmcnt(10)\n"
      ".Lw1072:\ns_barrier\ns_add_u32 m0, s54, 90112\ns_nop 0\nglobal_load_lds_dwordx4 v236, s[50:51] offset:0\nglobal_load_lds_dwordx4 v237, s[50:51] offset:1024\n"
      "global_load_lds_dwordx4 v238, s[50:51] offset:2048\nglobal_load_lds_dwordx4 v239, s[50:51] offset:3072\ns_add_u32 s52, s52, 212992\ns_cmp_eq_u32 s52, 5537792\ns_cselect_b32 s52, 0, s52\n"
      "s_add_u32 s50, s10, s52\ns_addc_u32 s51, s11, 0\ns_add_u32 m0, s53, 90112\ns_nop 0\nglobal_load_lds_dwordx4 v22, s[6:7] offset:0\nv_lshl_add_u32 v22, v9, 7, v4\n"
      "global_load_lds_dwordx4 v23, s[44:45] offset:1024\nv_lshl_add_u32 v23, v11, 7, v5\nglobal_load_lds_dwordx4 v24, s[46:47] offset:2048\nv_lshl_add_u32 v24, v13, 7, v4\n"
      "ds_read_b64 v[88:89], v30 offset:0\nglobal_load_lds_dwordx4 v25, s[48:49] offset:3072\nv_lshl_add_u32 v25, v15, 7, v5\nv_max3_f16 v32, |v86|, |v87|, v32\nv_max3_f32 v33, |v86|, |v87|, v33\n"
      "s_add_u32 m0, s53, 94208\ns_nop 0\nglobal_load_lds_dwordx4 v26, s[6:7] offset:0\nv_lshl_add_u32 v26, v17, 7, v4\nv_and_b32 v190, s55, v86\nv_xor_b32 v86, v86, v190\n"
      "global_load_lds_dwordx4 v27, s[44:45] offset:1024\nv_lshl_add_u32 v27, v19, 7, v5\nv_and_b32 v191, s55, v87\nv_xor_b32 v87, v87, v191\ns_cbranch_vccz .La1075\n"
      "global_load_lds_dwordx4 v28, s[46:47] offset:2048\n"
      ".La1075:\nv_lshl_add_u32 v28, v21, 7, v4\ns_waitcnt vmcnt(11) lgkmcnt(0)\ns_cbranch_vccnz .Lw1076\ns_waitcnt vmcnt(10)\n"
      ".Lw1076:\ns_barrier\ns_add_u32 m0, s54, 0\ns_nop 0\nglobal_load_lds_dwordx4 v236, s[50:51] offset:0\nglobal_load_lds_dwordx4 v237, s[50:51] offset:1024\n"
      "global_load_lds_dwordx4 v238, s[50:51] offset:2048\nglobal_load_lds_dwordx4 v239, s[50:51] offset:3072\ns_add_u32 m0, s53, 0\ns_nop 0\nglobal_load_lds_dwordx4 v22, s[6:7] offset:0\n"
      "ds_read_b64 v[90:91], v30 offset:45056\nglobal_load_lds_dwordx4 v23, s[44:45] offset:1024\nv_max3_f16 v32, |v88|, |v89|, v32\nv_max3_f32 v33, |v88|, |v89|, v33\n"
      "global_load_lds_dwordx4 v24, s[46:47] offset:2048\nv_and_b32 v192, s55, v88\nv_xor_b32 v88, v88, v192\nglobal_load_lds_dwordx4 v25, s[48:49] offset:3072\nv_and_b32 v193, s55, v89\n"
      "v_xor_b32 v89, v89, v193\ns_add_u32 m0, s53, 4096\ns_nop 0\nglobal_load_lds_dwordx4 v26, s[6:7] offset:0\nds_write_b64 v206, v[208:209]\nds_write_b32 v203, v204\n"
      "global_load_lds_dwordx4 v27, s[44:45] offset:1024\nv_min_u32 v140, 62, v140\nv_lshl_add_u32 v224, v140, 1, v144\nds_read_u16 v225, v144\nds_read_u16 v224, v224 offset:2\n"
      "s_cbranch_vccz .La1077\nglobal_load_lds_dwordx4 v28, s[46:47] offset:2048\n"
      ".La1077:\ns_waitcnt vmcnt(11) lgkmcnt(0)\ns_cbranch_vccnz .Lw1078\ns_waitcnt vmcnt(10)\n"
      ".Lw1078:\ns_barrier\nv_readlane_b32 s42, v224, 0\nv_readlane_b32 s43, v224, 1\nv_cmp_lt_u32 s[62:63], v140, v225\ns_mulk_i32 s42, 104\ns_mulk_i32 s43, 104\nv_add_u32 v142, s42, v145\n"
      "v_add_u32 v143, s43, v145\nglobal_load_dword v142, v142, s[4:5]\nglobal_load_dword v143, v143, s[4:5]\nglobal_load_dwordx4 v[96:99], v39, s[60:61] sc1\ns_add_u32 s60, s60, 4096\n"
      "s_addc_u32 s61, s61, 0\nglobal_load_dwordx4 v[100:103], v39, s[60:61] sc1\ns_add_u32 s60, s60, 4096\ns_addc_u32 s61, s61, 0\nglobal_load_dwordx4 v[104:107], v39, s[60:61] sc1\n"
      "s_add_u32 s60, s60, 4096\ns_addc_u32 s61, s61, 0\nglobal_load_dwordx4 v[108:111], v39, s[60:61] sc1\ns_add_u32 s60, s60, 4096\ns_addc_u32 s61, s61, 0\n"
      "global_load_dwordx4 v[112:115], v39, s[60:61] sc1\ns_add_u32 s60, s60, 4096\ns_addc_u32 s61, s61, 0\nds_read_b64 v[92:93], v31 offset:0\nglobal_load_dwordx4 v[116:119], v39, s[60:61] sc1\n"
      "s_add_u32 s60, s60, 4096\ns_addc_u32 s61, s61, 0\nv_max3_f16 v32, |v90|, |v91|, v32\nv_max3_f32 v33, |v90|, |v91|, v33\nglobal_load_dwordx4 v[120:123], v39, s[60:61] sc1\n"
      "s_add_u32 s60, s60, 4096\ns_addc_u32 s61, s61, 0\nv_and_b32 v194, s55, v90\nv_xor_b32 v90, v90, v194\nglobal_load_dwordx4 v[124:127], v39, s[60:61] sc1\ns_add_u32 s60, s60, 4096\n"
      "s_addc_u32 s61, s61, 0\nv_and_b32 v195, s55, v91\nv_xor_b32 v91, v91, v195\nglobal_load_dwordx4 v[128:131], v39, s[60:61] sc1\ns_add_u32 s60, s60, 4096\ns_addc_u32 s61, s61, 0\n"
      "s_bcnt1_i32_b32 s57, s62\nv_mov_b32 v225, s57\nds_add_rtn_u32 v225, v38, v225 offset:16\nv_lshlrev_b32 v140, 1, v224\nglobal_load_dwordx4 v[132:135], v39, s[60:61] sc1\n"
      "s_add_u32 s60, s60, 4096\ns_addc_u32 s61, s61, 0\nglobal_load_dwordx4 v[136:139], v39, s[60:61] sc1\ns_add_u32 s60, s60, 4096\ns_addc_u32 s61, s61, 0\ns_waitcnt vmcnt(13) lgkmcnt(0)\n"
      "s_barrier\ns_setprio 3\nds_read_b64 v[94:95], v30 offset:0\ns_cmp_eq_u32 s57, 0\ns_cbranch_scc1 .Lv1080\nv_readfirstlane_b32 s63, v225\ns_add_u32 s63, s63, s64\ns_lshl_b32 s42, s63, 3\n"
      "s_add_u32 s42, s42, 159552\nv_lshl_add_u32 v141, v34, 3, s42\ns_bfm_b64 exec, s57, 0\nds_write_b32 v141, v140\ns_mov_b64 exec, -1\ns_waitcnt vmcnt(11)\nv_lshlrev_b32 v142, 7, v142\ns_nop 1\n"
      "v_add_u32_dpp v224, v142, v146 quad_perm:[0,0,0,0] row_mask:0xf bank_mask:0xf\nv_add_u32_dpp v225, v142, v146 quad_perm:[1,1,1,1] row_mask:0xf bank_mask:0xf\n"
      "v_add_u32_dpp v226, v142, v146 quad_perm:[2,2,2,2] row_mask:0xf bank_mask:0xf\nv_add_u32_dpp v227, v142, v146 quad_perm:[3,3,3,3] row_mask:0xf bank_mask:0xf\n"
      "global_load_dwordx4 v[240:243], v224, s[6:7]\nglobal_load_dwordx4 v[244:247], v225, s[6:7]\nglobal_load_dwordx4 v[248:251], v226, s[6:7]\nglobal_load_dwordx4 v[252:255], v227, s[6:7]\n"
      "s_cmp_eq_u32 s57, 1\ns_cbranch_scc1 .Lv1080\nv_lshlrev_b32 v143, 7, v143\ns_nop 1\nv_add_u32_dpp v228, v143, v146 quad_perm:[0,0,0,0] row_mask:0xf bank_mask:0xf\n"
      "v_add_u32_dpp v229, v143, v146 quad_perm:[1,1,1,1] row_mask:0xf bank_mask:0xf\nv_add_u32_dpp v230, v143, v146 quad_perm:[2,2,2,2] row_mask:0xf bank_mask:0xf\n"
      "v_add_u32_dpp v231, v143, v146 quad_perm:[3,3,3,3] row_mask:0xf bank_mask:0xf\nglobal_load_dwordx4 v[8:11], v228, s[6:7]\nglobal_load_dwordx4 v[12:15], v229, s[6:7]\n"
      "global_load_dwordx4 v[16:19], v230, s[6:7]\nglobal_load_dwordx4 v[20:23], v231, s[6:7]\n"
      ".Lv1080:\nv_max3_f16 v32, |v92|, |v93|, v32\nv_max3_f32 v33, |v92|, |v93|, v33\nv_and_b32 v196, s55, v92\nv_xor_b32 v92, v92, v196\nv_and_b32 v197, s55, v93\nv_xor_b32 v93, v93, v197\n"
      "s_waitcnt lgkmcnt(0)\nv_max3_f16 v32, |v94|, |v95|, v32\nv_max3_f32 v33, |v94|, |v95|, v33\nv_and_b32 v198, s55, v94\nv_xor_b32 v94, v94, v198\nv_and_b32 v199, s55, v95\n"
      "v_xor_b32 v95, v95, v199\nv_lshlrev_b32 v32, 16, v32\nv_max_u32 v33, v32, v33\ns_nop 1\nv_max_u32_dpp v33, v33, v33 quad_perm:[1,0,3,2] row_mask:0xf bank_mask:0xf\ns_nop 1\n"
      "v_max_u32_dpp v33, v33, v33 quad_perm:[2,3,0,1] row_mask:0xf bank_mask:0xf\nv_lshrrev_b32 v33, 23, v33\nv_subrev_u32 v32, 22, v33\nv_mul_u32_u24 v32, 0x800080, v32\n"
      "v_pk_sub_u16 v44, v44, v32 clamp\nv_pk_sub_u16 v45, v45, v32 clamp\nv_pk_sub_u16 v46, v46, v32 clamp\nv_pk_sub_u16 v47, v47, v32 clamp\nv_lshl_or_b32 v44, v44, 3, v148\n"
      "v_lshl_or_b32 v45, v45, 3, v149\nv_lshl_or_b32 v46, v46, 3, v150\nv_lshl_or_b32 v47, v47, 3, v151\nds_write2_b64 v210, v[44:45], v[46:47] offset1:4\nv_pk_sub_u16 v48, v48, v32 clamp\n"
      "v_pk_sub_u16 v49, v49, v32 clamp\nv_pk_sub_u16 v50, v50, v32 clamp\nv_pk_sub_u16 v51, v51, v32 clamp\nv_lshl_or_b32 v48, v48, 3, v152\nv_lshl_or_b32 v49, v49, 3, v153\n"
      "v_lshl_or_b32 v50, v50, 3, v154\nv_lshl_or_b32 v51, v51, 3, v155\nds_write2_b64 v211, v[48:49], v[50:51] offset1:4\nv_pk_sub_u16 v52, v52, v32 clamp\nv_pk_sub_u16 v53, v53, v32 clamp\n"
      "v_pk_sub_u16 v54, v54, v32 clamp\nv_pk_sub_u16 v55, v55, v32 clamp\nv_lshl_or_b32 v52, v52, 3, v156\nv_lshl_or_b32 v53, v53, 3, v157\nv_lshl_or_b32 v54, v54, 3, v158\n"
      "v_lshl_or_b32 v55, v55, 3, v159\nds_write2_b64 v212, v[52:53], v[54:55] offset1:4\nv_pk_sub_u16 v56, v56, v32 clamp\nv_pk_sub_u16 v57, v57, v32 clamp\nv_pk_sub_u16 v58, v58, v32 clamp\n"
      "v_pk_sub_u16 v59, v59, v32 clamp\nv_lshl_or_b32 v56, v56, 3, v160\nv_lshl_or_b32 v57, v57, 3, v161\nv_lshl_or_b32 v58, v58, 3, v162\nv_lshl_or_b32 v59, v59, 3, v163\n"
      "ds_write2_b64 v213, v[56:57], v[58:59] offset1:4\nv_pk_sub_u16 v60, v60, v32 clamp\nv_pk_sub_u16 v61, v61, v32 clamp\nv_pk_sub_u16 v62, v62, v32 clamp\nv_pk_sub_u16 v63, v63, v32 clamp\n"
      "v_lshl_or_b32 v60, v60, 3, v164\nv_lshl_or_b32 v61, v61, 3, v165\nv_lshl_or_b32 v62, v62, 3, v166\nv_lshl_or_b32 v63, v63, 3, v167\nds_write2_b64 v214, v[60:61], v[62:63] offset1:4\n"
      "v_pk_sub_u16 v64, v64, v32 clamp\nv_pk_sub_u16 v65, v65, v32 clamp\nv_pk_sub_u16 v66, v66, v32 clamp\nv_pk_sub_u16 v67, v67, v32 clamp\nv_lshl_or_b32 v64, v64, 3, v168\n"
      "v_lshl_or_b32 v65, v65, 3, v169\nv_lshl_or_b32 v66, v66, 3, v170\nv_lshl_or_b32 v67, v67, 3, v171\nds_write2_b64 v215, v[64:65], v[66:67] offset1:4\nv_pk_sub_u16 v68, v68, v32 clamp\n"
      "v_pk_sub_u16 v69, v69, v32 clamp\nv_pk_sub_u16 v70, v70, v32 clamp\nv_pk_sub_u16 v71, v71, v32 clamp\nv_lshl_or_b32 v68, v68, 3, v172\nv_lshl_or_b32 v69, v69, 3, v173\n"
      "v_lshl_or_b32 v70, v70, 3, v174\nv_lshl_or_b32 v71, v71, 3, v175\nds_write2_b64 v216, v[68:69], v[70:71] offset1:4\nv_pk_sub_u16 v72, v72, v32 clamp\nv_pk_sub_u16 v73, v73, v32 clamp\n"
      "v_pk_sub_u16 v74, v74, v32 clamp\nv_pk_sub_u16 v75, v75, v32 clamp\nv_lshl_or_b32 v72, v72, 3, v176\nv_lshl_or_b32 v73, v73, 3, v177\nv_lshl_or_b32 v74, v74, 3, v178\n"
      "v_lshl_or_b32 v75, v75, 3, v179\nds_write2_b64 v217, v[72:73], v[74:75] offset1:4\nv_pk_sub_u16 v76, v76, v32 clamp\nv_pk_sub_u16 v77, v77, v32 clamp\nv_pk_sub_u16 v78, v78, v32 clamp\n"
      "v_pk_sub_u16 v79, v79, v32 clamp\nv_lshl_or_b32 v76, v76, 3, v180\nv_lshl_or_b32 v77, v77, 3, v181\nv_lshl_or_b32 v78, v78, 3, v182\nv_lshl_or_b32 v79, v79, 3, v183\n"
      "ds_write2_b64 v218, v[76:77], v[78:79] offset1:4\nv_pk_sub_u16 v80, v80, v32 clamp\nv_pk_sub_u16 v81, v81, v32 clamp\nv_pk_sub_u16 v82, v82, v32 clamp\nv_pk_sub_u16 v83, v83, v32 clamp\n"
      "v_lshl_or_b32 v80, v80, 3, v184\nv_lshl_or_b32 v81, v81, 3, v185\nv_lshl_or_b32 v82, v82, 3, v186\nv_lshl_or_b32 v83, v83, 3, v187\nds_write2_b64 v219, v[80:81], v[82:83] offset1:4\n"
      "v_pk_sub_u16 v84, v84, v32 clamp\nv_pk_sub_u16 v85, v85, v32 clamp\nv_pk_sub_u16 v86, v86, v32 clamp\nv_pk_sub_u16 v87, v87, v32 clamp\nv_lshl_or_b32 v84, v84, 3, v188\n"
      "v_lshl_or_b32 v85, v85, 3, v189\nv_lshl_or_b32 v86, v86, 3, v190\nv_lshl_or_b32 v87, v87, 3, v191\nds_write2_b64 v220, v[84:85], v[86:87] offset1:4\nv_pk_sub_u16 v88, v88, v32 clamp\n"
      "v_pk_sub_u16 v89, v89, v32 clamp\nv_pk_sub_u16 v90, v90, v32 clamp\nv_pk_sub_u16 v91, v91, v32 clamp\nv_lshl_or_b32 v88, v88, 3, v192\nv_lshl_or_b32 v89, v89, 3, v193\n"
      "v_lshl_or_b32 v90, v90, 3, v194\nv_lshl_or_b32 v91, v91, 3, v195\nds_write2_b64 v221, v[88:89], v[90:91] offset1:4\nv_pk_sub_u16 v92, v92, v32 clamp\nv_pk_sub_u16 v93, v93, v32 clamp\n"
      "v_pk_sub_u16 v94, v94, v32 clamp\nv_pk_sub_u16 v95, v95, v32 clamp\nv_lshl_or_b32 v92, v92, 3, v196\nv_lshl_or_b32 v93, v93, 3, v197\nv_lshl_or_b32 v94, v94, 3, v198\n"
      "v_lshl_or_b32 v95, v95, 3, v199\nds_write2_b64 v222, v[92:93], v[94:95] offset1:4\nv_sub_u32 v4, 257, v33\nv_lshlrev_b32 v4, 23, v4\nv_cvt_pkrtz_f16_f32 v4, v4, v4\n"
      "v_lshlrev_b32 v33, 2, v33\nv_mov_b32 v5, v4\nv_subrev_u32 v33, 536, v33\nds_write_b64 v200, v[4:5] offset:832\nds_write_b32 v202, v33\ns_waitcnt vmcnt(0)\n"
      "ds_write_b128 v207, v[96:99] offset:0\nds_write_b128 v207, v[100:103] offset:4096\nds_write_b128 v207, v[104:107] offset:8192\nds_write_b128 v207, v[108:111] offset:12288\n"
      "ds_write_b128 v207, v[112:115] offset:16384\nds_write_b128 v207, v[116:119] offset:20480\nds_write_b128 v207, v[120:123] offset:24576\nds_write_b128 v207, v[124:127] offset:28672\n"
      "ds_write_b128 v207, v[128:131] offset:32768\nds_write_b128 v207, v[132:135] offset:36864\ns_cmp_lg_u32 s24, 1\ns_cbranch_scc1 .Lc1081\n"
      "v_mul_f32_dpp v136, v136, v136 row_ror:8 row_mask:0xf bank_mask:0xf\nv_mul_f32_dpp v137, v137, v137 row_ror:8 row_mask:0xf bank_mask:0xf\n"
      "v_mul_f32_dpp v138, v138, v138 row_ror:8 row_mask:0xf bank_mask:0xf\nv_mul_f32_dpp v139, v139, v139 row_ror:8 row_mask:0xf bank_mask:0xf\n"
      ".Lc1081:\nds_write_b128 v207, v[136:139] offset:40960\ns_cmp_eq_u32 s57, 0\ns_cbranch_scc1 .Lx1082\nv_mov_b32 v44, 0x3d800000\nv_mov_b32 v45, 0\ns_mul_i32 s42, s63, 864\n"
      "s_add_u32 s42, s42, 90112\nv_cmp_gt_u32 vcc, 24, v34\nv_add_u32 v48, s42, v147\nv_mov_b32 v47, 262144\ns_lshl_b32 s42, s63, 4\ns_add_u32 s42, s42, 159232\nv_cndmask_b32 v47, v47, v48, vcc\n"
      "v_add_u32 v49, s42, v205\nv_cmp_gt_u32 vcc, 16, v34\ns_mov_b32 s42, 0x7fff7fff\nv_max3_f16 v45, |v240|, |v241|, v45\nv_max3_f32 v44, |v240|, |v241|, v44\n"
      "v_max3_f16 v45, |v242|, |v243|, v45\nv_max3_f32 v44, |v242|, |v243|, v44\nv_max3_f16 v45, |v244|, |v245|, v45\nv_max3_f32 v44, |v244|, |v245|, v44\nv_max3_f16 v45, |v246|, |v247|, v45\n"
      "v_max3_f32 v44, |v246|, |v247|, v44\nv_max3_f16 v45, |v248|, |v249|, v45\nv_max3_f32 v44, |v248|, |v249|, v44\nv_max3_f16 v45, |v250|, |v251|, v45\nv_max3_f32 v44, |v250|, |v251|, v44\n"
      "v_max3_f16 v45, |v252|, |v253|, v45\nv_max3_f32 v44, |v252|, |v253|, v44\nv_max3_f16 v45, |v254|, |v255|, v45\nv_max3_f32 v44, |v254|, |v255|, v44\nv_lshlrev_b32 v45, 16, v45\n"
      "v_max_u32 v44, v44, v45\ns_nop 1\nv_max_u32_dpp v44, v44, v44 quad_perm:[1,0,3,2] row_mask:0xf bank_mask:0xf\ns_nop 1\nv_max_u32_dpp v44, v44, v44 row_ror:8 row_mask:0xf bank_mask:0xf\n"
      "v_mov_b32 v45, v44\ns_nop 1\nv_permlane16_swap_b32 v44, v45\nv_max_u32 v44, v44, v45\nv_mov_b32 v45, v44\ns_nop 1\nv_permlane32_swap_b32 v44, v45\nv_max_u32 v44, v44, v45\n"
      "v_lshrrev_b32 v44, 23, v44\nv_subrev_u32 v46, 22, v44\nv_mul_u32_u24 v46, 0x800080, v46\nv_sub_u32 v45, 257, v44\nv_lshlrev_b32 v45, 23, v45\nv_cvt_pkrtz_f16_f32 v45, v45, v45\n"
      "v_lshlrev_b32 v44, 2, v44\nv_subrev_u32 v44, 536, v44\nds_write_b32 v49, v44\nv_and_b32 v52, s42, v240\nv_and_b32 v53, s42, v241\nv_and_b32 v54, s42, v242\nv_and_b32 v55, s42, v243\n"
      "v_pk_sub_u16 v52, v52, v46 clamp\nv_pk_sub_u16 v53, v53, v46 clamp\nv_pk_sub_u16 v54, v54, v46 clamp\nv_pk_sub_u16 v55, v55, v46 clamp\nv_lshlrev_b32 v52, 3, v52\nv_lshlrev_b32 v53, 3, v53\n"
      "v_lshlrev_b32 v54, 3, v54\nv_lshlrev_b32 v55, 3, v55\nv_bfi_b32 v240, s42, v52, v240\nv_bfi_b32 v241, s42, v53, v241\nv_bfi_b32 v242, s42, v54, v242\nv_bfi_b32 v243, s42, v55, v243\n"
      "ds_write_b128 v48, v[240:243] offset:0\nv_and_b32 v52, s42, v244\nv_and_b32 v53, s42, v245\nv_and_b32 v54, s42, v246\nv_and_b32 v55, s42, v247\nv_pk_sub_u16 v52, v52, v46 clamp\n"
      "v_pk_sub_u16 v53, v53, v46 clamp\nv_pk_sub_u16 v54, v54, v46 clamp\nv_pk_sub_u16 v55, v55, v46 clamp\nv_lshlrev_b32 v52, 3, v52\nv_lshlrev_b32 v53, 3, v53\nv_lshlrev_b32 v54, 3, v54\n"
      "v_lshlrev_b32 v55, 3, v55\nv_bfi_b32 v244, s42, v52, v244\nv_bfi_b32 v245, s42, v53, v245\nv_bfi_b32 v246, s42, v54, v246\nv_bfi_b32 v247, s42, v55, v247\n"
      "ds_write_b128 v48, v[244:247] offset:256\nv_and_b32 v52, s42, v248\nv_and_b32 v53, s42, v249\nv_and_b32 v54, s42, v250\nv_and_b32 v55, s42, v251\nv_pk_sub_u16 v52, v52, v46 clamp\n"
      "v_pk_sub_u16 v53, v53, v46 clamp\nv_pk_sub_u16 v54, v54, v46 clamp\nv_pk_sub_u16 v55, v55, v46 clamp\nv_lshlrev_b32 v52, 3, v52\nv_lshlrev_b32 v53, 3, v53\nv_lshlrev_b32 v54, 3, v54\n"
      "v_lshlrev_b32 v55, 3, v55\nv_bfi_b32 v248, s42, v52, v248\nv_bfi_b32 v249, s42, v53, v249\nv_bfi_b32 v250, s42, v54, v250\nv_bfi_b32 v251, s42, v55, v251\n"
      "ds_write_b128 v48, v[248:251] offset:512\nv_and_b32 v52, s42, v252\nv_and_b32 v53, s42, v253\nv_and_b32 v54, s42, v254\nv_and_b32 v55, s42, v255\nv_pk_sub_u16 v52, v52, v46 clamp\n"
      "v_pk_sub_u16 v53, v53, v46 clamp\nv_pk_sub_u16 v54, v54, v46 clamp\nv_pk_sub_u16 v55, v55, v46 clamp\nv_lshlrev_b32 v52, 3, v52\nv_lshlrev_b32 v53, 3, v53\nv_lshlrev_b32 v54, 3, v54\n"
      "v_lshlrev_b32 v55, 3, v55\nv_bfi_b32 v252, s42, v52, v252\nv_bfi_b32 v253, s42, v53, v253\nv_bfi_b32 v254, s42, v54, v254\nv_bfi_b32 v255, s42, v55, v255\n"
      "v_cndmask_b32 v252, v45, v252, vcc\nv_cndmask_b32 v253, v45, v253, vcc\nv_cndmask_b32 v254, v45, v254, vcc\nv_cndmask_b32 v255, v45, v255, vcc\nds_write_b128 v47, v[252:255] offset:768\n"
      "s_add_u32 s63, s63, 1\ns_cmp_eq_u32 s57, 1\ns_cbranch_scc1 .Lx1082\nv_mov_b32 v44, 0x3d800000\nv_mov_b32 v45, 0\ns_mul_i32 s42, s63, 864\ns_add_u32 s42, s42, 90112\n"
      "v_cmp_gt_u32 vcc, 24, v34\nv_add_u32 v48, s42, v147\nv_mov_b32 v47, 262144\ns_lshl_b32 s42, s63, 4\ns_add_u32 s42, s42, 159232\nv_cndmask_b32 v47, v47, v48, vcc\nv_add_u32 v49, s42, v205\n"
      "v_cmp_gt_u32 vcc, 16, v34\ns_mov_b32 s42, 0x7fff7fff\nv_max3_f16 v45, |v8|, |v9|, v45\nv_max3_f32 v44, |v8|, |v9|, v44\nv_max3_f16 v45, |v10|, |v11|, v45\nv_max3_f32 v44, |v10|, |v11|, v44\n"
      "v_max3_f16 v45, |v12|, |v13|, v45\nv_max3_f32 v44, |v12|, |v13|, v44\nv_max3_f16 v45, |v14|, |v15|, v45\nv_max3_f32 v44, |v14|, |v15|, v44\nv_max3_f16 v45, |v16|, |v17|, v45\n"
      "v_max3_f32 v44, |v16|, |v17|, v44\nv_max3_f16 v45, |v18|, |v19|, v45\nv_max3_f32 v44, |v18|, |v19|, v44\nv_max3_f16 v45, |v20|, |v21|, v45\nv_max3_f32 v44, |v20|, |v21|, v44\n"
      "v_max3_f16 v45, |v22|, |v23|, v45\nv_max3_f32 v44, |v22|, |v23|, v44\nv_lshlrev_b32 v45, 16, v45\nv_max_u32 v44, v44, v45\ns_nop 1\n"
      "v_max_u32_dpp v44, v44, v44 quad_perm:[1,0,3,2] row_mask:0xf bank_mask:0xf\ns_nop 1\nv_max_u32_dpp v44, v44, v44 row_ror:8 row_mask:0xf bank_mask:0xf\nv_mov_b32 v45, v44\ns_nop 1\n"
      "v_permlane16_swap_b32 v44, v45\nv_max_u32 v44, v44, v45\nv_mov_b32 v45, v44\ns_nop 1\nv_permlane32_swap_b32 v44, v45\nv_max_u32 v44, v44, v45\nv_lshrrev_b32 v44, 23, v44\n"
      "v_subrev_u32 v46, 22, v44\nv_mul_u32_u24 v46, 0x800080, v46\nv_sub_u32 v45, 257, v44\nv_lshlrev_b32 v45, 23, v45\nv_cvt_pkrtz_f16_f32 v45, v45, v45\nv_lshlrev_b32 v44, 2, v44\n"
      "v_subrev_u32 v44, 536, v44\nds_write_b32 v49, v44\nv_and_b32 v52, s42, v8\nv_and_b32 v53, s42, v9\nv_and_b32 v54, s42, v10\nv_and_b32 v55, s42, v11\nv_pk_sub_u16 v52, v52, v46 clamp\n"
      "v_pk_sub_u16 v53, v53, v46 clamp\nv_pk_sub_u16 v54, v54, v46 clamp\nv_pk_sub_u16 v55, v55, v46 clamp\nv_lshlrev_b32 v52, 3, v52\nv_lshlrev_b32 v53, 3, v53\nv_lshlrev_b32 v54, 3, v54\n"
      "v_lshlrev_b32 v55, 3, v55\nv_bfi_b32 v8, s42, v52, v8\nv_bfi_b32 v9, s42, v53, v9\nv_bfi_b32 v10, s42, v54, v10\nv_bfi_b32 v11, s42, v55, v11\nds_write_b128 v48, v[8:11] offset:0\n"
      "v_and_b32 v52, s42, v12\nv_and_b32 v53, s42, v13\nv_and_b32 v54, s42, v14\nv_and_b32 v55, s42, v15\nv_pk_sub_u16 v52, v52, v46 clamp\nv_pk_sub_u16 v53, v53, v46 clamp\n"
      "v_pk_sub_u16 v54, v54, v46 clamp\nv_pk_sub_u16 v55, v55, v46 clamp\nv_lshlrev_b32 v52, 3, v52\nv_lshlrev_b32 v53, 3, v53\nv_lshlrev_b32 v54, 3, v54\nv_lshlrev_b32 v55, 3, v55\n"
      "v_bfi_b32 v12, s42, v52, v12\nv_bfi_b32 v13, s42, v53, v13\nv_bfi_b32 v14, s42, v54, v14\nv_bfi_b32 v15, s42, v55, v15\nds_write_b128 v48, v[12:15] offset:256\nv_and_b32 v52, s42, v16\n"
      "v_and_b32 v53, s42, v17\nv_and_b32 v54, s42, v18\nv_and_b32 v55, s42, v19\nv_pk_sub_u16 v52, v52, v46 clamp\nv_pk_sub_u16 v53, v53, v46 clamp\nv_pk_sub_u16 v54, v54, v46 clamp\n"
      "v_pk_sub_u16 v55, v55, v46 clamp\nv_lshlrev_b32 v52, 3, v52\nv_lshlrev_b32 v53, 3, v53\nv_lshlrev_b32 v54, 3, v54\nv_lshlrev_b32 v55, 3, v55\nv_bfi_b32 v16, s42, v52, v16\n"
      "v_bfi_b32 v17, s42, v53, v17\nv_bfi_b32 v18, s42, v54, v18\nv_bfi_b32 v19, s42, v55, v19\nds_write_b128 v48, v[16:19] offset:512\nv_and_b32 v52, s42, v20\nv_and_b32 v53, s42, v21\n"
      "v_and_b32 v54, s42, v22\nv_and_b32 v55, s42, v23\nv_pk_sub_u16 v52, v52, v46 clamp\nv_pk_sub_u16 v53, v53, v46 clamp\nv_pk_sub_u16 v54, v54, v46 clamp\nv_pk_sub_u16 v55, v55, v46 clamp\n"
      "v_lshlrev_b32 v52, 3, v52\nv_lshlrev_b32 v53, 3, v53\nv_lshlrev_b32 v54, 3, v54\nv_lshlrev_b32 v55, 3, v55\nv_bfi_b32 v20, s42, v52, v20\nv_bfi_b32 v21, s42, v53, v21\n"
      "v_bfi_b32 v22, s42, v54, v22\nv_bfi_b32 v23, s42, v55, v23\nv_cndmask_b32 v20, v45, v20, vcc\nv_cndmask_b32 v21, v45, v21, vcc\nv_cndmask_b32 v22, v45, v22, vcc\n"
      "v_cndmask_b32 v23, v45, v23, vcc\nds_write_b128 v47, v[20:23] offset:768\n"
      ".Lx1082:\nds_add_u32 v38, v42 offset:28\ns_lshl_b32 s42, %[blk], 2\nv_mov_b32 v4, s42\nv_mov_b32 v5, v40\ns_cmp_lg_u32 s24, 0\ns_cbranch_scc1 .Ln1083\ns_mov_b64 exec, 1\n"
      "global_store_dword v4, v5, s[20:21] sc1\ns_mov_b64 exec, -1\n"
      ".Ln1083:\ns_branch .Lcin\n"
      ".Lgemm:\nv_mbcnt_lo_u32_b32 v234, -1, 0\nv_mbcnt_hi_u32_b32 v234, -1, v234\ns_cmp_lg_u32 s36, 0\ns_cbranch_scc1 .Lp1084\nv_lshlrev_b32 v0, 4, v234\ns_mov_b32 m0, 163648\n"
      "s_mov_b64 exec, 0xff\nglobal_load_lds_dwordx4 v0, %[pl]\ns_mov_b64 exec, -1\n"
      ".Lp1084:\ns_setprio 2\ns_and_b32 s44, s36, 1\ns_lshr_b32 s45, s36, 1\nv_lshrrev_b32 v1, 4, v234\nv_and_b32 v2, 15, v234\nv_lshlrev_b32 v3, 2, v2\nv_add_u32 v3, 163584, v3\nv_mov_b32 v4, 0\n"
      "ds_write_b32 v3, v4\nv_cmp_eq_u32 vcc, 0, v234\nv_mov_b32 v4, 262144\nv_mov_b32 v233, 1\nv_cndmask_b32 v232, v4, v3, vcc\ns_mul_i32 s42, s44, 12288\nv_lshrrev_b32 v3, 1, v2\n"
      "v_xor_b32 v3, v3, v1\nv_lshl_add_u32 v4, v2, 7, s42\nv_lshl_add_u32 v208, v3, 4, v4\nv_xor_b32 v209, 64, v208\nv_add_u32 v210, 90112, v208\nv_add_u32 v211, 90112, v209\ns_cmp_eq_u32 s25, 4\n"
      "s_cselect_b32 s42, 5, 6\ns_mul_i32 s42, s42, s44\ns_sub_u32 s42, 12, s42\ns_lshl_b32 s42, s42, 11\nv_add_u32 v212, s42, v208\nv_add_u32 v213, s42, v209\nv_add_u32 v214, s42, v210\n"
      "v_add_u32 v215, s42, v211\nv_lshrrev_b32 v3, 2, v2\nv_lshl_add_u32 v4, v1, 3, v3\nv_and_b32 v5, 1, v1\nv_lshl_add_u32 v3, v5, 2, v3\ns_lshl_b32 s42, s36, 1\nv_xor_b32 v3, s42, v3\n"
      "v_and_b32 v5, 3, v2\nv_lshlrev_b32 v5, 3, v5\nv_lshl_add_u32 v4, v4, 8, v5\nv_lshl_add_u32 v4, v3, 5, v4\nv_add_u32 v216, 28672, v4\nv_xor_b32 v217, 32, v216\nv_xor_b32 v218, 64, v216\n"
      "v_xor_b32 v219, 96, v216\nv_add_u32 v220, 90112, v216\nv_add_u32 v221, 90112, v217\nv_add_u32 v222, 90112, v218\nv_add_u32 v223, 90112, v219\ns_lshl_b32 s42, s36, 6\n"
      "s_add_u32 s42, s42, 162304\nv_lshl_add_u32 v3, v1, 3, s42\nv_xor_b32 v4, 64, v3\ns_waitcnt vmcnt(4)\nds_read_b64 v[176:177], v3 offset:0\nds_read_b64 v[178:179], v3 offset:32\n"
      "ds_read_b64 v[180:181], v4 offset:0\nds_read_b64 v[182:183], v4 offset:32\nds_read_b64 v[224:225], v3 offset:256\nds_read_b64 v[226:227], v3 offset:288\n"
      "ds_read_b64 v[228:229], v4 offset:256\nds_read_b64 v[230:231], v4 offset:288\ns_waitcnt lgkmcnt(4)\nv_lshlrev_b32 v0, 16, v176\nv_and_b32 v1, 0xffff0000, v176\nv_lshlrev_b32 v2, 16, v177\n"
      "v_and_b32 v3, 0xffff0000, v177\nv_lshlrev_b32 v4, 16, v178\nv_and_b32 v5, 0xffff0000, v178\nv_lshlrev_b32 v6, 16, v179\nv_and_b32 v7, 0xffff0000, v179\nv_lshlrev_b32 v8, 16, v180\n"
      "v_and_b32 v9, 0xffff0000, v180\nv_lshlrev_b32 v10, 16, v181\nv_and_b32 v11, 0xffff0000, v181\nv_lshlrev_b32 v12, 16, v182\nv_and_b32 v13, 0xffff0000, v182\nv_lshlrev_b32 v14, 16, v183\n"
      "v_and_b32 v15, 0xffff0000, v183\nv_mov_b64 v[16:17], v[0:1]\nv_mov_b64 v[18:19], v[2:3]\nv_mov_b64 v[20:21], v[4:5]\nv_mov_b64 v[22:23], v[6:7]\nv_mov_b64 v[24:25], v[8:9]\n"
      "v_mov_b64 v[26:27], v[10:11]\nv_mov_b64 v[28:29], v[12:13]\nv_mov_b64 v[30:31], v[14:15]\nv_mov_b64 v[32:33], v[0:1]\nv_mov_b64 v[34:35], v[2:3]\nv_mov_b64 v[36:37], v[4:5]\n"
      "v_mov_b64 v[38:39], v[6:7]\nv_mov_b64 v[40:41], v[8:9]\nv_mov_b64 v[42:43], v[10:11]\nv_mov_b64 v[44:45], v[12:13]\nv_mov_b64 v[46:47], v[14:15]\nv_mov_b64 v[48:49], v[0:1]\n"
      "v_mov_b64 v[50:51], v[2:3]\nv_mov_b64 v[52:53], v[4:5]\nv_mov_b64 v[54:55], v[6:7]\nv_mov_b64 v[56:57], v[8:9]\nv_mov_b64 v[58:59], v[10:11]\nv_mov_b64 v[60:61], v[12:13]\n"
      "v_mov_b64 v[62:63], v[14:15]\nv_mov_b64 v[64:65], v[0:1]\nv_mov_b64 v[66:67], v[2:3]\nv_mov_b64 v[68:69], v[4:5]\nv_mov_b64 v[70:71], v[6:7]\nv_mov_b64 v[72:73], v[8:9]\n"
      "v_mov_b64 v[74:75], v[10:11]\nv_mov_b64 v[76:77], v[12:13]\nv_mov_b64 v[78:79], v[14:15]\nv_mov_b64 v[80:81], v[0:1]\nv_mov_b64 v[82:83], v[2:3]\nv_mov_b64 v[84:85], v[4:5]\n"
      "v_mov_b64 v[86:87], v[6:7]\nv_mov_b64 v[88:89], v[8:9]\nv_mov_b64 v[90:91], v[10:11]\nv_mov_b64 v[92:93], v[12:13]\nv_mov_b64 v[94:95], v[14:15]\nv_mov_b64 v[96:97], v[0:1]\n"
      "v_mov_b64 v[98:99], v[2:3]\nv_mov_b64 v[100:101], v[4:5]\nv_mov_b64 v[102:103], v[6:7]\nv_mov_b64 v[104:105], v[8:9]\nv_mov_b64 v[106:107], v[10:11]\nv_mov_b64 v[108:109], v[12:13]\n"
      "v_mov_b64 v[110:111], v[14:15]\ns_mov_b32 s46, 4\ns_waitcnt lgkmcnt(0)\ns_cmp_eq_u32 s25, 4\ns_cbranch_scc1 .Lg14\ns_cmp_eq_u32 s25, 0\ns_cbranch_scc1 .Lg12\n"
      ".Lg13:\ns_barrier\nds_read_b128 v[176:179], v214 offset:0\nds_read_b128 v[180:183], v215 offset:0\nds_read_b64_tr_b16 v[112:113], v220 offset:0\n"
      "ds_read_b64_tr_b16 v[114:115], v220 offset:1024\nds_read_b64_tr_b16 v[116:117], v220 offset:8192\nds_read_b64_tr_b16 v[118:119], v220 offset:9216\n"
      "ds_read_b64_tr_b16 v[120:121], v221 offset:0\nds_read_b64_tr_b16 v[122:123], v221 offset:1024\nds_read_b64_tr_b16 v[124:125], v221 offset:8192\n"
      "ds_read_b64_tr_b16 v[126:127], v221 offset:9216\nds_read_b64_tr_b16 v[128:129], v222 offset:0\nds_read_b64_tr_b16 v[130:131], v222 offset:1024\n"
      "ds_read_b64_tr_b16 v[132:133], v222 offset:8192\nds_read_b64_tr_b16 v[134:135], v222 offset:9216\nds_read_b64_tr_b16 v[136:137], v223 offset:0\n"
      "ds_read_b64_tr_b16 v[138:139], v223 offset:1024\nds_read_b64_tr_b16 v[140:141], v223 offset:8192\nds_read_b64_tr_b16 v[142:143], v223 offset:9216\nds_read_b128 v[184:187], v210 offset:0\n"
      "ds_read_b128 v[188:191], v211 offset:0\ns_waitcnt lgkmcnt(0)\nds_read_b128 v[192:195], v210 offset:2048\nds_read_b128 v[196:199], v211 offset:2048\n.p2align 3\n"
      ".Lgl13:\nv_mfma_f32_16x16x32_bf16 v[96:99], v[112:115], v[176:179], v[96:99]\nds_read_b128 v[200:203], v210 offset:4096\nv_mfma_f32_16x16x32_bf16 v[96:99], v[116:119], v[180:183], v[96:99]\n"
      "ds_read_b128 v[204:207], v211 offset:4096\nv_mfma_f32_16x16x32_bf16 v[100:103], v[124:127], v[180:183], v[100:103]\nv_mfma_f32_16x16x32_bf16 v[100:103], v[120:123], v[176:179], v[100:103]\n"
      "v_mfma_f32_16x16x32_bf16 v[12:15], v[136:139], v[184:187], v[12:15]\nds_read_b128 v[176:179], v210 offset:6144\nv_mfma_f32_16x16x32_bf16 v[12:15], v[140:143], v[188:191], v[12:15]\n"
      "ds_read_b128 v[180:183], v211 offset:6144\nv_mfma_f32_16x16x32_bf16 v[8:11], v[132:135], v[188:191], v[8:11]\nv_mfma_f32_16x16x32_bf16 v[8:11], v[128:131], v[184:187], v[8:11]\n"
      "v_mfma_f32_16x16x32_bf16 v[4:7], v[120:123], v[184:187], v[4:7]\nv_mfma_f32_16x16x32_bf16 v[4:7], v[124:127], v[188:191], v[4:7]\n"
      "v_mfma_f32_16x16x32_bf16 v[0:3], v[116:119], v[188:191], v[0:3]\ns_waitcnt lgkmcnt(4)\nv_mfma_f32_16x16x32_bf16 v[0:3], v[112:115], v[184:187], v[0:3]\n"
      "v_mfma_f32_16x16x32_bf16 v[16:19], v[112:115], v[192:195], v[16:19]\nds_read_b128 v[184:187], v210 offset:8192\nv_mfma_f32_16x16x32_bf16 v[16:19], v[116:119], v[196:199], v[16:19]\n"
      "ds_read_b128 v[188:191], v211 offset:8192\nv_mfma_f32_16x16x32_bf16 v[20:23], v[124:127], v[196:199], v[20:23]\nv_mfma_f32_16x16x32_bf16 v[20:23], v[120:123], v[192:195], v[20:23]\n"
      "v_mfma_f32_16x16x32_bf16 v[24:27], v[128:131], v[192:195], v[24:27]\nv_mfma_f32_16x16x32_bf16 v[24:27], v[132:135], v[196:199], v[24:27]\n"
      "v_mfma_f32_16x16x32_bf16 v[28:31], v[140:143], v[196:199], v[28:31]\ns_waitcnt lgkmcnt(2)\nv_mfma_f32_16x16x32_bf16 v[28:31], v[136:139], v[192:195], v[28:31]\n"
      "v_mfma_f32_16x16x32_bf16 v[44:47], v[136:139], v[200:203], v[44:47]\nds_read_b128 v[192:195], v210 offset:10240\nv_mfma_f32_16x16x32_bf16 v[44:47], v[140:143], v[204:207], v[44:47]\n"
      "ds_read_b128 v[196:199], v211 offset:10240\nv_mfma_f32_16x16x32_bf16 v[40:43], v[132:135], v[204:207], v[40:43]\nv_mfma_f32_16x16x32_bf16 v[40:43], v[128:131], v[200:203], v[40:43]\n"
      "v_mfma_f32_16x16x32_bf16 v[36:39], v[120:123], v[200:203], v[36:39]\nv_mfma_f32_16x16x32_bf16 v[36:39], v[124:127], v[204:207], v[36:39]\n"
      "v_mfma_f32_16x16x32_bf16 v[32:35], v[116:119], v[204:207], v[32:35]\ns_waitcnt vmcnt(0) lgkmcnt(0)\ns_barrier\nv_mfma_f32_16x16x32_bf16 v[32:35], v[112:115], v[200:203], v[32:35]\n"
      "ds_read_b64_tr_b16 v[144:145], v216 offset:0\nv_mfma_f32_16x16x32_bf16 v[48:51], v[112:115], v[176:179], v[48:51]\nds_read_b64_tr_b16 v[146:147], v216 offset:1024\n"
      "v_mfma_f32_16x16x32_bf16 v[48:51], v[116:119], v[180:183], v[48:51]\nds_read_b64_tr_b16 v[148:149], v216 offset:8192\nv_mfma_f32_16x16x32_bf16 v[52:55], v[124:127], v[180:183], v[52:55]\n"
      "ds_read_b64_tr_b16 v[150:151], v216 offset:9216\nv_mfma_f32_16x16x32_bf16 v[52:55], v[120:123], v[176:179], v[52:55]\nds_read_b64_tr_b16 v[152:153], v217 offset:0\n"
      "v_mfma_f32_16x16x32_bf16 v[56:59], v[128:131], v[176:179], v[56:59]\nds_read_b64_tr_b16 v[154:155], v217 offset:1024\nv_mfma_f32_16x16x32_bf16 v[56:59], v[132:135], v[180:183], v[56:59]\n"
      "ds_read_b64_tr_b16 v[156:157], v217 offset:8192\nv_mfma_f32_16x16x32_bf16 v[60:63], v[140:143], v[180:183], v[60:63]\nds_read_b64_tr_b16 v[158:159], v217 offset:9216\n"
      "v_mfma_f32_16x16x32_bf16 v[60:63], v[136:139], v[176:179], v[60:63]\nds_read_b64_tr_b16 v[160:161], v218 offset:0\nv_mfma_f32_16x16x32_bf16 v[76:79], v[136:139], v[184:187], v[76:79]\n"
      "ds_read_b128 v[176:179], v212 offset:0\nv_mfma_f32_16x16x32_bf16 v[76:79], v[140:143], v[188:191], v[76:79]\nds_read_b128 v[180:183], v213 offset:0\n"
      "v_mfma_f32_16x16x32_bf16 v[72:75], v[132:135], v[188:191], v[72:75]\nds_read_b64_tr_b16 v[162:163], v218 offset:1024\nv_mfma_f32_16x16x32_bf16 v[72:75], v[128:131], v[184:187], v[72:75]\n"
      "ds_read_b64_tr_b16 v[164:165], v218 offset:8192\nv_mfma_f32_16x16x32_bf16 v[68:71], v[120:123], v[184:187], v[68:71]\nds_read_b64_tr_b16 v[166:167], v218 offset:9216\n"
      "v_mfma_f32_16x16x32_bf16 v[68:71], v[124:127], v[188:191], v[68:71]\nds_read_b64_tr_b16 v[168:169], v219 offset:0\nv_mfma_f32_16x16x32_bf16 v[64:67], v[116:119], v[188:191], v[64:67]\n"
      "ds_read_b64_tr_b16 v[170:171], v219 offset:1024\nv_mfma_f32_16x16x32_bf16 v[64:67], v[112:115], v[184:187], v[64:67]\nds_read_b64_tr_b16 v[172:173], v219 offset:8192\n"
      "v_mfma_f32_16x16x32_bf16 v[80:83], v[112:115], v[192:195], v[80:83]\nds_read_b128 v[184:187], v208 offset:0\nv_mfma_f32_16x16x32_bf16 v[80:83], v[116:119], v[196:199], v[80:83]\n"
      "ds_read_b128 v[188:191], v209 offset:0\nv_mfma_f32_16x16x32_bf16 v[84:87], v[124:127], v[196:199], v[84:87]\nds_read_b64_tr_b16 v[174:175], v219 offset:9216\n"
      "v_mfma_f32_16x16x32_bf16 v[84:87], v[120:123], v[192:195], v[84:87]\nv_mfma_f32_16x16x32_bf16 v[88:91], v[128:131], v[192:195], v[88:91]\n"
      "v_mfma_f32_16x16x32_bf16 v[88:91], v[132:135], v[196:199], v[88:91]\nv_mfma_f32_16x16x32_bf16 v[92:95], v[140:143], v[196:199], v[92:95]\ns_waitcnt lgkmcnt(0)\n"
      "v_mfma_f32_16x16x32_bf16 v[92:95], v[136:139], v[192:195], v[92:95]\nds_read_b128 v[192:195], v208 offset:2048\nds_read_b128 v[196:199], v209 offset:2048\n"
      "v_mfma_f32_16x16x32_bf16 v[96:99], v[144:147], v[176:179], v[96:99]\nds_read_b128 v[200:203], v208 offset:4096\nv_mfma_f32_16x16x32_bf16 v[96:99], v[148:151], v[180:183], v[96:99]\n"
      "ds_read_b128 v[204:207], v209 offset:4096\nv_mfma_f32_16x16x32_bf16 v[100:103], v[156:159], v[180:183], v[100:103]\nv_mfma_f32_16x16x32_bf16 v[100:103], v[152:155], v[176:179], v[100:103]\n"
      "v_mfma_f32_16x16x32_bf16 v[12:15], v[168:171], v[184:187], v[12:15]\nds_read_b128 v[176:179], v208 offset:6144\nv_mfma_f32_16x16x32_bf16 v[12:15], v[172:175], v[188:191], v[12:15]\n"
      "ds_read_b128 v[180:183], v209 offset:6144\nv_mfma_f32_16x16x32_bf16 v[8:11], v[164:167], v[188:191], v[8:11]\nv_mfma_f32_16x16x32_bf16 v[8:11], v[160:163], v[184:187], v[8:11]\n"
      "v_mfma_f32_16x16x32_bf16 v[4:7], v[152:155], v[184:187], v[4:7]\nv_mfma_f32_16x16x32_bf16 v[4:7], v[156:159], v[188:191], v[4:7]\n"
      "v_mfma_f32_16x16x32_bf16 v[0:3], v[148:151], v[188:191], v[0:3]\ns_waitcnt lgkmcnt(4)\nv_mfma_f32_16x16x32_bf16 v[0:3], v[144:147], v[184:187], v[0:3]\n"
      "v_mfma_f32_16x16x32_bf16 v[16:19], v[144:147], v[192:195], v[16:19]\nds_read_b128 v[184:187], v208 offset:8192\nv_mfma_f32_16x16x32_bf16 v[16:19], v[148:151], v[196:199], v[16:19]\n"
      "ds_read_b128 v[188:191], v209 offset:8192\nv_mfma_f32_16x16x32_bf16 v[20:23], v[156:159], v[196:199], v[20:23]\nv_mfma_f32_16x16x32_bf16 v[20:23], v[152:155], v[192:195], v[20:23]\n"
      "v_mfma_f32_16x16x32_bf16 v[24:27], v[160:163], v[192:195], v[24:27]\nv_mfma_f32_16x16x32_bf16 v[24:27], v[164:167], v[196:199], v[24:27]\n"
      "v_mfma_f32_16x16x32_bf16 v[28:31], v[172:175], v[196:199], v[28:31]\ns_waitcnt lgkmcnt(2)\nv_mfma_f32_16x16x32_bf16 v[28:31], v[168:171], v[192:195], v[28:31]\n"
      "v_mfma_f32_16x16x32_bf16 v[44:47], v[168:171], v[200:203], v[44:47]\nds_read_b128 v[192:195], v208 offset:10240\nv_mfma_f32_16x16x32_bf16 v[44:47], v[172:175], v[204:207], v[44:47]\n"
      "ds_read_b128 v[196:199], v209 offset:10240\nv_mfma_f32_16x16x32_bf16 v[40:43], v[164:167], v[204:207], v[40:43]\nv_mfma_f32_16x16x32_bf16 v[40:43], v[160:163], v[200:203], v[40:43]\n"
      "v_mfma_f32_16x16x32_bf16 v[36:39], v[152:155], v[200:203], v[36:39]\nv_mfma_f32_16x16x32_bf16 v[36:39], v[156:159], v[204:207], v[36:39]\n"
      "v_mfma_f32_16x16x32_bf16 v[32:35], v[148:151], v[204:207], v[32:35]\ns_waitcnt vmcnt(0) lgkmcnt(0)\ns_barrier\nv_mfma_f32_16x16x32_bf16 v[32:35], v[144:147], v[200:203], v[32:35]\n"
      "ds_read_b64_tr_b16 v[112:113], v216 offset:45056\nv_mfma_f32_16x16x32_bf16 v[48:51], v[144:147], v[176:179], v[48:51]\nds_read_b64_tr_b16 v[114:115], v216 offset:46080\n"
      "v_mfma_f32_16x16x32_bf16 v[48:51], v[148:151], v[180:183], v[48:51]\nds_read_b64_tr_b16 v[116:117], v216 offset:53248\nv_mfma_f32_16x16x32_bf16 v[52:55], v[156:159], v[180:183], v[52:55]\n"
      "ds_read_b64_tr_b16 v[118:119], v216 offset:54272\nv_mfma_f32_16x16x32_bf16 v[52:55], v[152:155], v[176:179], v[52:55]\nds_read_b64_tr_b16 v[120:121], v217 offset:45056\n"
      "v_mfma_f32_16x16x32_bf16 v[56:59], v[160:163], v[176:179], v[56:59]\nds_read_b64_tr_b16 v[122:123], v217 offset:46080\nv_mfma_f32_16x16x32_bf16 v[56:59], v[164:167], v[180:183], v[56:59]\n"
      "ds_read_b64_tr_b16 v[124:125], v217 offset:53248\nv_mfma_f32_16x16x32_bf16 v[60:63], v[172:175], v[180:183], v[60:63]\nds_read_b64_tr_b16 v[126:127], v217 offset:54272\n"
      "v_mfma_f32_16x16x32_bf16 v[60:63], v[168:171], v[176:179], v[60:63]\nds_read_b64_tr_b16 v[128:129], v218 offset:45056\nv_mfma_f32_16x16x32_bf16 v[76:79], v[168:171], v[184:187], v[76:79]\n"
      "ds_read_b128 v[176:179], v212 offset:45056\nv_mfma_f32_16x16x32_bf16 v[76:79], v[172:175], v[188:191], v[76:79]\nds_read_b128 v[180:183], v213 offset:45056\n"
      "v_mfma_f32_16x16x32_bf16 v[72:75], v[164:167], v[188:191], v[72:75]\nds_read_b64_tr_b16 v[130:131], v218 offset:46080\nv_mfma_f32_16x16x32_bf16 v[72:75], v[160:163], v[184:187], v[72:75]\n"
      "ds_read_b64_tr_b16 v[132:133], v218 offset:53248\nv_mfma_f32_16x16x32_bf16 v[68:71], v[152:155], v[184:187], v[68:71]\nds_read_b64_tr_b16 v[134:135], v218 offset:54272\n"
      "v_mfma_f32_16x16x32_bf16 v[68:71], v[156:159], v[188:191], v[68:71]\nds_read_b64_tr_b16 v[136:137], v219 offset:45056\nv_mfma_f32_16x16x32_bf16 v[64:67], v[148:151], v[188:191], v[64:67]\n"
      "ds_read_b64_tr_b16 v[138:139], v219 offset:46080\nv_mfma_f32_16x16x32_bf16 v[64:67], v[144:147], v[184:187], v[64:67]\nds_read_b64_tr_b16 v[140:141], v219 offset:53248\n"
      "v_mfma_f32_16x16x32_bf16 v[80:83], v[144:147], v[192:195], v[80:83]\nds_read_b128 v[184:187], v208 offset:45056\nv_mfma_f32_16x16x32_bf16 v[80:83], v[148:151], v[196:199], v[80:83]\n"
      "ds_read_b128 v[188:191], v209 offset:45056\nv_mfma_f32_16x16x32_bf16 v[84:87], v[156:159], v[196:199], v[84:87]\nds_read_b64_tr_b16 v[142:143], v219 offset:54272\n"
      "v_mfma_f32_16x16x32_bf16 v[84:87], v[152:155], v[192:195], v[84:87]\nv_mfma_f32_16x16x32_bf16 v[88:91], v[160:163], v[192:195], v[88:91]\n"
      "v_mfma_f32_16x16x32_bf16 v[88:91], v[164:167], v[196:199], v[88:91]\nv_mfma_f32_16x16x32_bf16 v[92:95], v[172:175], v[196:199], v[92:95]\ns_waitcnt lgkmcnt(0)\n"
      "v_mfma_f32_16x16x32_bf16 v[92:95], v[168:171], v[192:195], v[92:95]\nds_read_b128 v[192:195], v208 offset:47104\nds_read_b128 v[196:199], v209 offset:47104\n"
      "v_mfma_f32_16x16x32_bf16 v[96:99], v[112:115], v[176:179], v[96:99]\nds_read_b128 v[200:203], v208 offset:49152\nv_mfma_f32_16x16x32_bf16 v[96:99], v[116:119], v[180:183], v[96:99]\n"
      "ds_read_b128 v[204:207], v209 offset:49152\nv_mfma_f32_16x16x32_bf16 v[100:103], v[124:127], v[180:183], v[100:103]\nv_mfma_f32_16x16x32_bf16 v[100:103], v[120:123], v[176:179], v[100:103]\n"
      "v_mfma_f32_16x16x32_bf16 v[12:15], v[136:139], v[184:187], v[12:15]\nds_read_b128 v[176:179], v208 offset:51200\nv_mfma_f32_16x16x32_bf16 v[12:15], v[140:143], v[188:191], v[12:15]\n"
      "ds_read_b128 v[180:183], v209 offset:51200\nv_mfma_f32_16x16x32_bf16 v[8:11], v[132:135], v[188:191], v[8:11]\nv_mfma_f32_16x16x32_bf16 v[8:11], v[128:131], v[184:187], v[8:11]\n"
      "v_mfma_f32_16x16x32_bf16 v[4:7], v[120:123], v[184:187], v[4:7]\nv_mfma_f32_16x16x32_bf16 v[4:7], v[124:127], v[188:191], v[4:7]\n"
      "v_mfma_f32_16x16x32_bf16 v[0:3], v[116:119], v[188:191], v[0:3]\ns_waitcnt lgkmcnt(4)\nv_mfma_f32_16x16x32_bf16 v[0:3], v[112:115], v[184:187], v[0:3]\n"
      "v_mfma_f32_16x16x32_bf16 v[16:19], v[112:115], v[192:195], v[16:19]\nds_read_b128 v[184:187], v208 offset:53248\nv_mfma_f32_16x16x32_bf16 v[16:19], v[116:119], v[196:199], v[16:19]\n"
      "ds_read_b128 v[188:191], v209 offset:53248\nv_mfma_f32_16x16x32_bf16 v[20:23], v[124:127], v[196:199], v[20:23]\nv_mfma_f32_16x16x32_bf16 v[20:23], v[120:123], v[192:195], v[20:23]\n"
      "v_mfma_f32_16x16x32_bf16 v[24:27], v[128:131], v[192:195], v[24:27]\nv_mfma_f32_16x16x32_bf16 v[24:27], v[132:135], v[196:199], v[24:27]\n"
      "v_mfma_f32_16x16x32_bf16 v[28:31], v[140:143], v[196:199], v[28:31]\ns_waitcnt lgkmcnt(2)\nv_mfma_f32_16x16x32_bf16 v[28:31], v[136:139], v[192:195], v[28:31]\n"
      "v_mfma_f32_16x16x32_bf16 v[44:47], v[136:139], v[200:203], v[44:47]\nds_read_b128 v[192:195], v208 offset:55296\nv_mfma_f32_16x16x32_bf16 v[44:47], v[140:143], v[204:207], v[44:47]\n"
      "ds_read_b128 v[196:199], v209 offset:55296\nv_mfma_f32_16x16x32_bf16 v[40:43], v[132:135], v[204:207], v[40:43]\nv_mfma_f32_16x16x32_bf16 v[40:43], v[128:131], v[200:203], v[40:43]\n"
      "v_mfma_f32_16x16x32_bf16 v[36:39], v[120:123], v[200:203], v[36:39]\nv_mfma_f32_16x16x32_bf16 v[36:39], v[124:127], v[204:207], v[36:39]\n"
      "v_mfma_f32_16x16x32_bf16 v[32:35], v[116:119], v[204:207], v[32:35]\ns_waitcnt vmcnt(0) lgkmcnt(0)\ns_barrier\nv_mfma_f32_16x16x32_bf16 v[32:35], v[112:115], v[200:203], v[32:35]\n"
      "ds_read_b64_tr_b16 v[144:145], v220 offset:0\nv_mfma_f32_16x16x32_bf16 v[48:51], v[112:115], v[176:179], v[48:51]\nds_read_b64_tr_b16 v[146:147], v220 offset:1024\n"
      "v_mfma_f32_16x16x32_bf16 v[48:51], v[116:119], v[180:183], v[48:51]\nds_read_b64_tr_b16 v[148:149], v220 offset:8192\nv_mfma_f32_16x16x32_bf16 v[52:55], v[124:127], v[180:183], v[52:55]\n"
      "ds_read_b64_tr_b16 v[150:151], v220 offset:9216\nv_mfma_f32_16x16x32_bf16 v[52:55], v[120:123], v[176:179], v[52:55]\nds_read_b64_tr_b16 v[152:153], v221 offset:0\n"
      "v_mfma_f32_16x16x32_bf16 v[56:59], v[128:131], v[176:179], v[56:59]\nds_read_b64_tr_b16 v[154:155], v221 offset:1024\nv_mfma_f32_16x16x32_bf16 v[56:59], v[132:135], v[180:183], v[56:59]\n"
      "ds_read_b64_tr_b16 v[156:157], v221 offset:8192\nv_mfma_f32_16x16x32_bf16 v[60:63], v[140:143], v[180:183], v[60:63]\nds_read_b64_tr_b16 v[158:159], v221 offset:9216\n"
      "v_mfma_f32_16x16x32_bf16 v[60:63], v[136:139], v[176:179], v[60:63]\nds_read_b64_tr_b16 v[160:161], v222 offset:0\nv_mfma_f32_16x16x32_bf16 v[76:79], v[136:139], v[184:187], v[76:79]\n"
      "ds_read_b128 v[176:179], v214 offset:0\nv_mfma_f32_16x16x32_bf16 v[76:79], v[140:143], v[188:191], v[76:79]\nds_read_b128 v[180:183], v215 offset:0\n"
      "v_mfma_f32_16x16x32_bf16 v[72:75], v[132:135], v[188:191], v[72:75]\nds_read_b64_tr_b16 v[162:163], v222 offset:1024\nv_mfma_f32_16x16x32_bf16 v[72:75], v[128:131], v[184:187], v[72:75]\n"
      "ds_read_b64_tr_b16 v[164:165], v222 offset:8192\nv_mfma_f32_16x16x32_bf16 v[68:71], v[120:123], v[184:187], v[68:71]\nds_read_b64_tr_b16 v[166:167], v222 offset:9216\n"
      "v_mfma_f32_16x16x32_bf16 v[68:71], v[124:127], v[188:191], v[68:71]\nds_read_b64_tr_b16 v[168:169], v223 offset:0\nv_mfma_f32_16x16x32_bf16 v[64:67], v[116:119], v[188:191], v[64:67]\n"
      "ds_read_b64_tr_b16 v[170:171], v223 offset:1024\nv_mfma_f32_16x16x32_bf16 v[64:67], v[112:115], v[184:187], v[64:67]\nds_read_b64_tr_b16 v[172:173], v223 offset:8192\n"
      "v_mfma_f32_16x16x32_bf16 v[80:83], v[112:115], v[192:195], v[80:83]\nds_read_b128 v[184:187], v210 offset:0\nv_mfma_f32_16x16x32_bf16 v[80:83], v[116:119], v[196:199], v[80:83]\n"
      "ds_read_b128 v[188:191], v211 offset:0\nv_mfma_f32_16x16x32_bf16 v[84:87], v[124:127], v[196:199], v[84:87]\nds_read_b64_tr_b16 v[174:175], v223 offset:9216\n"
      "v_mfma_f32_16x16x32_bf16 v[84:87], v[120:123], v[192:195], v[84:87]\nv_mfma_f32_16x16x32_bf16 v[88:91], v[128:131], v[192:195], v[88:91]\n"
      "v_mfma_f32_16x16x32_bf16 v[88:91], v[132:135], v[196:199], v[88:91]\nv_mfma_f32_16x16x32_bf16 v[92:95], v[140:143], v[196:199], v[92:95]\ns_waitcnt lgkmcnt(0)\n"
      "v_mfma_f32_16x16x32_bf16 v[92:95], v[136:139], v[192:195], v[92:95]\nds_read_b128 v[192:195], v210 offset:2048\nds_read_b128 v[196:199], v211 offset:2048\n"
      "v_mfma_f32_16x16x32_bf16 v[96:99], v[144:147], v[176:179], v[96:99]\nds_read_b128 v[200:203], v210 offset:4096\nv_mfma_f32_16x16x32_bf16 v[96:99], v[148:151], v[180:183], v[96:99]\n"
      "ds_read_b128 v[204:207], v211 offset:4096\nv_mfma_f32_16x16x32_bf16 v[100:103], v[156:159], v[180:183], v[100:103]\nv_mfma_f32_16x16x32_bf16 v[100:103], v[152:155], v[176:179], v[100:103]\n"
      "v_mfma_f32_16x16x32_bf16 v[12:15], v[168:171], v[184:187], v[12:15]\nds_read_b128 v[176:179], v210 offset:6144\nv_mfma_f32_16x16x32_bf16 v[12:15], v[172:175], v[188:191], v[12:15]\n"
      "ds_read_b128 v[180:183], v211 offset:6144\nv_mfma_f32_16x16x32_bf16 v[8:11], v[164:167], v[188:191], v[8:11]\nv_mfma_f32_16x16x32_bf16 v[8:11], v[160:163], v[184:187], v[8:11]\n"
      "v_mfma_f32_16x16x32_bf16 v[4:7], v[152:155], v[184:187], v[4:7]\nv_mfma_f32_16x16x32_bf16 v[4:7], v[156:159], v[188:191], v[4:7]\n"
      "v_mfma_f32_16x16x32_bf16 v[0:3], v[148:151], v[188:191], v[0:3]\ns_waitcnt lgkmcnt(4)\nv_mfma_f32_16x16x32_bf16 v[0:3], v[144:147], v[184:187], v[0:3]\n"
      "v_mfma_f32_16x16x32_bf16 v[16:19], v[144:147], v[192:195], v[16:19]\nds_read_b128 v[184:187], v210 offset:8192\nv_mfma_f32_16x16x32_bf16 v[16:19], v[148:151], v[196:199], v[16:19]\n"
      "ds_read_b128 v[188:191], v211 offset:8192\nv_mfma_f32_16x16x32_bf16 v[20:23], v[156:159], v[196:199], v[20:23]\nv_mfma_f32_16x16x32_bf16 v[20:23], v[152:155], v[192:195], v[20:23]\n"
      "v_mfma_f32_16x16x32_bf16 v[24:27], v[160:163], v[192:195], v[24:27]\nv_mfma_f32_16x16x32_bf16 v[24:27], v[164:167], v[196:199], v[24:27]\n"
      "v_mfma_f32_16x16x32_bf16 v[28:31], v[172:175], v[196:199], v[28:31]\ns_waitcnt lgkmcnt(2)\nv_mfma_f32_16x16x32_bf16 v[28:31], v[168:171], v[192:195], v[28:31]\n"
      "v_mfma_f32_16x16x32_bf16 v[44:47], v[168:171], v[200:203], v[44:47]\nds_read_b128 v[192:195], v210 offset:10240\nv_mfma_f32_16x16x32_bf16 v[44:47], v[172:175], v[204:207], v[44:47]\n"
      "ds_read_b128 v[196:199], v211 offset:10240\nv_mfma_f32_16x16x32_bf16 v[40:43], v[164:167], v[204:207], v[40:43]\nv_mfma_f32_16x16x32_bf16 v[40:43], v[160:163], v[200:203], v[40:43]\n"
      "v_mfma_f32_16x16x32_bf16 v[36:39], v[152:155], v[200:203], v[36:39]\nv_mfma_f32_16x16x32_bf16 v[36:39], v[156:159], v[204:207], v[36:39]\n"
      "v_mfma_f32_16x16x32_bf16 v[32:35], v[148:151], v[204:207], v[32:35]\ns_waitcnt vmcnt(0) lgkmcnt(0)\ns_barrier\nv_mfma_f32_16x16x32_bf16 v[32:35], v[144:147], v[200:203], v[32:35]\n"
      "ds_read_b64_tr_b16 v[112:113], v216 offset:0\nv_mfma_f32_16x16x32_bf16 v[48:51], v[144:147], v[176:179], v[48:51]\nds_read_b64_tr_b16 v[114:115], v216 offset:1024\n"
      "v_mfma_f32_16x16x32_bf16 v[48:51], v[148:151], v[180:183], v[48:51]\nds_read_b64_tr_b16 v[116:117], v216 offset:8192\nv_mfma_f32_16x16x32_bf16 v[52:55], v[156:159], v[180:183], v[52:55]\n"
      "ds_read_b64_tr_b16 v[118:119], v216 offset:9216\nv_mfma_f32_16x16x32_bf16 v[52:55], v[152:155], v[176:179], v[52:55]\nds_read_b64_tr_b16 v[120:121], v217 offset:0\n"
      "v_mfma_f32_16x16x32_bf16 v[56:59], v[160:163], v[176:179], v[56:59]\nds_read_b64_tr_b16 v[122:123], v217 offset:1024\nv_mfma_f32_16x16x32_bf16 v[56:59], v[164:167], v[180:183], v[56:59]\n"
      "ds_read_b64_tr_b16 v[124:125], v217 offset:8192\nv_mfma_f32_16x16x32_bf16 v[60:63], v[172:175], v[180:183], v[60:63]\nds_read_b64_tr_b16 v[126:127], v217 offset:9216\n"
      "v_mfma_f32_16x16x32_bf16 v[60:63], v[168:171], v[176:179], v[60:63]\nds_read_b64_tr_b16 v[128:129], v218 offset:0\nv_mfma_f32_16x16x32_bf16 v[76:79], v[168:171], v[184:187], v[76:79]\n"
      "ds_read_b128 v[176:179], v212 offset:0\nv_mfma_f32_16x16x32_bf16 v[76:79], v[172:175], v[188:191], v[76:79]\nds_read_b128 v[180:183], v213 offset:0\n"
      "v_mfma_f32_16x16x32_bf16 v[72:75], v[164:167], v[188:191], v[72:75]\nds_read_b64_tr_b16 v[130:131], v218 offset:1024\nv_mfma_f32_16x16x32_bf16 v[72:75], v[160:163], v[184:187], v[72:75]\n"
      "ds_read_b64_tr_b16 v[132:133], v218 offset:8192\nv_mfma_f32_16x16x32_bf16 v[68:71], v[152:155], v[184:187], v[68:71]\nds_read_b64_tr_b16 v[134:135], v218 offset:9216\n"
      "v_mfma_f32_16x16x32_bf16 v[68:71], v[156:159], v[188:191], v[68:71]\nds_read_b64_tr_b16 v[136:137], v219 offset:0\nv_mfma_f32_16x16x32_bf16 v[64:67], v[148:151], v[188:191], v[64:67]\n"
      "ds_read_b64_tr_b16 v[138:139], v219 offset:1024\nv_mfma_f32_16x16x32_bf16 v[64:67], v[144:147], v[184:187], v[64:67]\nds_read_b64_tr_b16 v[140:141], v219 offset:8192\n"
      "v_mfma_f32_16x16x32_bf16 v[80:83], v[144:147], v[192:195], v[80:83]\nds_read_b128 v[184:187], v208 offset:0\nv_mfma_f32_16x16x32_bf16 v[80:83], v[148:151], v[196:199], v[80:83]\n"
      "ds_read_b128 v[188:191], v209 offset:0\nv_mfma_f32_16x16x32_bf16 v[84:87], v[156:159], v[196:199], v[84:87]\nds_read_b64_tr_b16 v[142:143], v219 offset:9216\n"
      "v_mfma_f32_16x16x32_bf16 v[84:87], v[152:155], v[192:195], v[84:87]\nv_mfma_f32_16x16x32_bf16 v[88:91], v[160:163], v[192:195], v[88:91]\n"
      "v_mfma_f32_16x16x32_bf16 v[88:91], v[164:167], v[196:199], v[88:91]\nv_mfma_f32_16x16x32_bf16 v[92:95], v[172:175], v[196:199], v[92:95]\ns_waitcnt lgkmcnt(0)\n"
      "v_mfma_f32_16x16x32_bf16 v[92:95], v[168:171], v[192:195], v[92:95]\nds_read_b128 v[192:195], v208 offset:2048\nds_read_b128 v[196:199], v209 offset:2048\n"
      "v_mfma_f32_16x16x32_bf16 v[96:99], v[112:115], v[176:179], v[96:99]\nds_read_b128 v[200:203], v208 offset:4096\nv_mfma_f32_16x16x32_bf16 v[96:99], v[116:119], v[180:183], v[96:99]\n"
      "ds_read_b128 v[204:207], v209 offset:4096\nv_mfma_f32_16x16x32_bf16 v[100:103], v[124:127], v[180:183], v[100:103]\nv_mfma_f32_16x16x32_bf16 v[100:103], v[120:123], v[176:179], v[100:103]\n"
      "v_mfma_f32_16x16x32_bf16 v[12:15], v[136:139], v[184:187], v[12:15]\nds_read_b128 v[176:179], v208 offset:6144\nv_mfma_f32_16x16x32_bf16 v[12:15], v[140:143], v[188:191], v[12:15]\n"
      "ds_read_b128 v[180:183], v209 offset:6144\nv_mfma_f32_16x16x32_bf16 v[8:11], v[132:135], v[188:191], v[8:11]\nv_mfma_f32_16x16x32_bf16 v[8:11], v[128:131], v[184:187], v[8:11]\n"
      "v_mfma_f32_16x16x32_bf16 v[4:7], v[120:123], v[184:187], v[4:7]\nv_mfma_f32_16x16x32_bf16 v[4:7], v[124:127], v[188:191], v[4:7]\n"
      "v_mfma_f32_16x16x32_bf16 v[0:3], v[116:119], v[188:191], v[0:3]\ns_waitcnt lgkmcnt(4)\nv_mfma_f32_16x16x32_bf16 v[0:3], v[112:115], v[184:187], v[0:3]\n"
      "v_mfma_f32_16x16x32_bf16 v[16:19], v[112:115], v[192:195], v[16:19]\nds_read_b128 v[184:187], v208 offset:8192\nv_mfma_f32_16x16x32_bf16 v[16:19], v[116:119], v[196:199], v[16:19]\n"
      "ds_read_b128 v[188:191], v209 offset:8192\nv_mfma_f32_16x16x32_bf16 v[20:23], v[124:127], v[196:199], v[20:23]\nv_mfma_f32_16x16x32_bf16 v[20:23], v[120:123], v[192:195], v[20:23]\n"
      "v_mfma_f32_16x16x32_bf16 v[24:27], v[128:131], v[192:195], v[24:27]\nv_mfma_f32_16x16x32_bf16 v[24:27], v[132:135], v[196:199], v[24:27]\n"
      "v_mfma_f32_16x16x32_bf16 v[28:31], v[140:143], v[196:199], v[28:31]\ns_waitcnt lgkmcnt(2)\nv_mfma_f32_16x16x32_bf16 v[28:31], v[136:139], v[192:195], v[28:31]\n"
      "v_mfma_f32_16x16x32_bf16 v[44:47], v[136:139], v[200:203], v[44:47]\nds_read_b128 v[192:195], v208 offset:10240\nv_mfma_f32_16x16x32_bf16 v[44:47], v[140:143], v[204:207], v[44:47]\n"
      "ds_read_b128 v[196:199], v209 offset:10240\nv_mfma_f32_16x16x32_bf16 v[40:43], v[132:135], v[204:207], v[40:43]\nv_mfma_f32_16x16x32_bf16 v[40:43], v[128:131], v[200:203], v[40:43]\n"
      "v_mfma_f32_16x16x32_bf16 v[36:39], v[120:123], v[200:203], v[36:39]\nv_mfma_f32_16x16x32_bf16 v[36:39], v[124:127], v[204:207], v[36:39]\n"
      "v_mfma_f32_16x16x32_bf16 v[32:35], v[116:119], v[204:207], v[32:35]\ns_waitcnt vmcnt(0) lgkmcnt(0)\ns_barrier\nv_mfma_f32_16x16x32_bf16 v[32:35], v[112:115], v[200:203], v[32:35]\n"
      "ds_read_b64_tr_b16 v[144:145], v216 offset:45056\nv_mfma_f32_16x16x32_bf16 v[48:51], v[112:115], v[176:179], v[48:51]\nds_read_b64_tr_b16 v[146:147], v216 offset:46080\n"
      "v_mfma_f32_16x16x32_bf16 v[48:51], v[116:119], v[180:183], v[48:51]\nds_read_b64_tr_b16 v[148:149], v216 offset:53248\nv_mfma_f32_16x16x32_bf16 v[52:55], v[124:127], v[180:183], v[52:55]\n"
      "ds_read_b64_tr_b16 v[150:151], v216 offset:54272\nv_mfma_f32_16x16x32_bf16 v[52:55], v[120:123], v[176:179], v[52:55]\nds_read_b64_tr_b16 v[152:153], v217 offset:45056\n"
      "v_mfma_f32_16x16x32_bf16 v[56:59], v[128:131], v[176:179], v[56:59]\nds_read_b64_tr_b16 v[154:155], v217 offset:46080\nv_mfma_f32_16x16x32_bf16 v[56:59], v[132:135], v[180:183], v[56:59]\n"
      "ds_read_b64_tr_b16 v[156:157], v217 offset:53248\nv_mfma_f32_16x16x32_bf16 v[60:63], v[140:143], v[180:183], v[60:63]\nds_read_b64_tr_b16 v[158:159], v217 offset:54272\n"
      "v_mfma_f32_16x16x32_bf16 v[60:63], v[136:139], v[176:179], v[60:63]\nds_read_b64_tr_b16 v[160:161], v218 offset:45056\nv_mfma_f32_16x16x32_bf16 v[76:79], v[136:139], v[184:187], v[76:79]\n"
      "ds_read_b128 v[176:179], v212 offset:45056\nv_mfma_f32_16x16x32_bf16 v[76:79], v[140:143], v[188:191], v[76:79]\nds_read_b128 v[180:183], v213 offset:45056\n"
      "v_mfma_f32_16x16x32_bf16 v[72:75], v[132:135], v[188:191], v[72:75]\nds_read_b64_tr_b16 v[162:163], v218 offset:46080\nv_mfma_f32_16x16x32_bf16 v[72:75], v[128:131], v[184:187], v[72:75]\n"
      "ds_read_b64_tr_b16 v[164:165], v218 offset:53248\nv_mfma_f32_16x16x32_bf16 v[68:71], v[120:123], v[184:187], v[68:71]\nds_read_b64_tr_b16 v[166:167], v218 offset:54272\n"
      "v_mfma_f32_16x16x32_bf16 v[68:71], v[124:127], v[188:191], v[68:71]\nds_read_b64_tr_b16 v[168:169], v219 offset:45056\nv_mfma_f32_16x16x32_bf16 v[64:67], v[116:119], v[188:191], v[64:67]\n"
      "ds_read_b64_tr_b16 v[170:171], v219 offset:46080\nv_mfma_f32_16x16x32_bf16 v[64:67], v[112:115], v[184:187], v[64:67]\nds_read_b64_tr_b16 v[172:173], v219 offset:53248\n"
      "v_mfma_f32_16x16x32_bf16 v[80:83], v[112:115], v[192:195], v[80:83]\nds_read_b128 v[184:187], v208 offset:45056\nv_mfma_f32_16x16x32_bf16 v[80:83], v[116:119], v[196:199], v[80:83]\n"
      "ds_read_b128 v[188:191], v209 offset:45056\nv_mfma_f32_16x16x32_bf16 v[84:87], v[124:127], v[196:199], v[84:87]\nds_read_b64_tr_b16 v[174:175], v219 offset:54272\n"
      "v_mfma_f32_16x16x32_bf16 v[84:87], v[120:123], v[192:195], v[84:87]\nv_mfma_f32_16x16x32_bf16 v[88:91], v[128:131], v[192:195], v[88:91]\n"
      "v_mfma_f32_16x16x32_bf16 v[88:91], v[132:135], v[196:199], v[88:91]\nv_mfma_f32_16x16x32_bf16 v[92:95], v[140:143], v[196:199], v[92:95]\ns_waitcnt lgkmcnt(0)\n"
      "v_mfma_f32_16x16x32_bf16 v[92:95], v[136:139], v[192:195], v[92:95]\nds_read_b128 v[192:195], v208 offset:47104\nds_read_b128 v[196:199], v209 offset:47104\n"
      "v_mfma_f32_16x16x32_bf16 v[96:99], v[144:147], v[176:179], v[96:99]\nds_read_b128 v[200:203], v208 offset:49152\nv_mfma_f32_16x16x32_bf16 v[96:99], v[148:151], v[180:183], v[96:99]\n"
      "ds_read_b128 v[204:207], v209 offset:49152\nv_mfma_f32_16x16x32_bf16 v[100:103], v[156:159], v[180:183], v[100:103]\nv_mfma_f32_16x16x32_bf16 v[100:103], v[152:155], v[176:179], v[100:103]\n"
      "v_mfma_f32_16x16x32_bf16 v[12:15], v[168:171], v[184:187], v[12:15]\nds_read_b128 v[176:179], v208 offset:51200\nv_mfma_f32_16x16x32_bf16 v[12:15], v[172:175], v[188:191], v[12:15]\n"
      "ds_read_b128 v[180:183], v209 offset:51200\nv_mfma_f32_16x16x32_bf16 v[8:11], v[164:167], v[188:191], v[8:11]\nv_mfma_f32_16x16x32_bf16 v[8:11], v[160:163], v[184:187], v[8:11]\n"
      "v_mfma_f32_16x16x32_bf16 v[4:7], v[152:155], v[184:187], v[4:7]\nv_mfma_f32_16x16x32_bf16 v[4:7], v[156:159], v[188:191], v[4:7]\n"
      "v_mfma_f32_16x16x32_bf16 v[0:3], v[148:151], v[188:191], v[0:3]\ns_waitcnt lgkmcnt(4)\nv_mfma_f32_16x16x32_bf16 v[0:3], v[144:147], v[184:187], v[0:3]\n"
      "v_mfma_f32_16x16x32_bf16 v[16:19], v[144:147], v[192:195], v[16:19]\nds_read_b128 v[184:187], v208 offset:53248\nv_mfma_f32_16x16x32_bf16 v[16:19], v[148:151], v[196:199], v[16:19]\n"
      "ds_read_b128 v[188:191], v209 offset:53248\nv_mfma_f32_16x16x32_bf16 v[20:23], v[156:159], v[196:199], v[20:23]\nv_mfma_f32_16x16x32_bf16 v[20:23], v[152:155], v[192:195], v[20:23]\n"
      "v_mfma_f32_16x16x32_bf16 v[24:27], v[160:163], v[192:195], v[24:27]\nv_mfma_f32_16x16x32_bf16 v[24:27], v[164:167], v[196:199], v[24:27]\n"
      "v_mfma_f32_16x16x32_bf16 v[28:31], v[172:175], v[196:199], v[28:31]\ns_waitcnt lgkmcnt(2)\nv_mfma_f32_16x16x32_bf16 v[28:31], v[168:171], v[192:195], v[28:31]\n"
      "v_mfma_f32_16x16x32_bf16 v[44:47], v[168:171], v[200:203], v[44:47]\nds_read_b128 v[192:195], v208 offset:55296\nv_mfma_f32_16x16x32_bf16 v[44:47], v[172:175], v[204:207], v[44:47]\n"
      "ds_read_b128 v[196:199], v209 offset:55296\nv_mfma_f32_16x16x32_bf16 v[40:43], v[164:167], v[204:207], v[40:43]\nv_mfma_f32_16x16x32_bf16 v[40:43], v[160:163], v[200:203], v[40:43]\n"
      "v_mfma_f32_16x16x32_bf16 v[36:39], v[152:155], v[200:203], v[36:39]\nv_mfma_f32_16x16x32_bf16 v[36:39], v[156:159], v[204:207], v[36:39]\n"
      "v_mfma_f32_16x16x32_bf16 v[32:35], v[148:151], v[204:207], v[32:35]\ns_waitcnt vmcnt(0) lgkmcnt(0)\ns_barrier\nv_mfma_f32_16x16x32_bf16 v[32:35], v[144:147], v[200:203], v[32:35]\n"
      "ds_read_b64_tr_b16 v[112:113], v220 offset:0\nv_mfma_f32_16x16x32_bf16 v[48:51], v[144:147], v[176:179], v[48:51]\nds_read_b64_tr_b16 v[114:115], v220 offset:1024\n"
      "v_mfma_f32_16x16x32_bf16 v[48:51], v[148:151], v[180:183], v[48:51]\nds_read_b64_tr_b16 v[116:117], v220 offset:8192\nv_mfma_f32_16x16x32_bf16 v[52:55], v[156:159], v[180:183], v[52:55]\n"
      "ds_read_b64_tr_b16 v[118:119], v220 offset:9216\nv_mfma_f32_16x16x32_bf16 v[52:55], v[152:155], v[176:179], v[52:55]\nds_read_b64_tr_b16 v[120:121], v221 offset:0\n"
      "v_mfma_f32_16x16x32_bf16 v[56:59], v[160:163], v[176:179], v[56:59]\nds_read_b64_tr_b16 v[122:123], v221 offset:1024\nv_mfma_f32_16x16x32_bf16 v[56:59], v[164:167], v[180:183], v[56:59]\n"
      "ds_read_b64_tr_b16 v[124:125], v221 offset:8192\nv_mfma_f32_16x16x32_bf16 v[60:63], v[172:175], v[180:183], v[60:63]\nds_read_b64_tr_b16 v[126:127], v221 offset:9216\n"
      "v_mfma_f32_16x16x32_bf16 v[60:63], v[168:171], v[176:179], v[60:63]\nds_read_b64_tr_b16 v[128:129], v222 offset:0\nv_mfma_f32_16x16x32_bf16 v[76:79], v[168:171], v[184:187], v[76:79]\n"
      "ds_read_b128 v[176:179], v214 offset:0\nv_mfma_f32_16x16x32_bf16 v[76:79], v[172:175], v[188:191], v[76:79]\nds_read_b128 v[180:183], v215 offset:0\n"
      "v_mfma_f32_16x16x32_bf16 v[72:75], v[164:167], v[188:191], v[72:75]\nds_read_b64_tr_b16 v[130:131], v222 offset:1024\nv_mfma_f32_16x16x32_bf16 v[72:75], v[160:163], v[184:187], v[72:75]\n"
      "ds_read_b64_tr_b16 v[132:133], v222 offset:8192\nv_mfma_f32_16x16x32_bf16 v[68:71], v[152:155], v[184:187], v[68:71]\nds_read_b64_tr_b16 v[134:135], v222 offset:9216\n"
      "v_mfma_f32_16x16x32_bf16 v[68:71], v[156:159], v[188:191], v[68:71]\nds_read_b64_tr_b16 v[136:137], v223 offset:0\nv_mfma_f32_16x16x32_bf16 v[64:67], v[148:151], v[188:191], v[64:67]\n"
      "ds_read_b64_tr_b16 v[138:139], v223 offset:1024\nv_mfma_f32_16x16x32_bf16 v[64:67], v[144:147], v[184:187], v[64:67]\nds_read_b64_tr_b16 v[140:141], v223 offset:8192\n"
      "v_mfma_f32_16x16x32_bf16 v[80:83], v[144:147], v[192:195], v[80:83]\nds_read_b128 v[184:187], v210 offset:0\nv_mfma_f32_16x16x32_bf16 v[80:83], v[148:151], v[196:199], v[80:83]\n"
      "ds_read_b128 v[188:191], v211 offset:0\nv_mfma_f32_16x16x32_bf16 v[84:87], v[156:159], v[196:199], v[84:87]\nds_read_b64_tr_b16 v[142:143], v223 offset:9216\n"
      "v_mfma_f32_16x16x32_bf16 v[84:87], v[152:155], v[192:195], v[84:87]\nv_mfma_f32_16x16x32_bf16 v[88:91], v[160:163], v[192:195], v[88:91]\ns_sub_u32 s46, s46, 1\ns_cmp_lg_u32 s46, 0\n"
      "v_mfma_f32_16x16x32_bf16 v[88:91], v[164:167], v[196:199], v[88:91]\nv_mfma_f32_16x16x32_bf16 v[92:95], v[172:175], v[196:199], v[92:95]\ns_waitcnt lgkmcnt(0)\n"
      "v_mfma_f32_16x16x32_bf16 v[92:95], v[168:171], v[192:195], v[92:95]\nds_read_b128 v[192:195], v210 offset:2048\nds_read_b128 v[196:199], v211 offset:2048\ns_cbranch_scc1 .Lgl13\n"
      "v_mfma_f32_16x16x32_bf16 v[96:99], v[112:115], v[176:179], v[96:99]\nds_read_b128 v[200:203], v210 offset:4096\nv_mfma_f32_16x16x32_bf16 v[96:99], v[116:119], v[180:183], v[96:99]\n"
      "ds_read_b128 v[204:207], v211 offset:4096\nv_mfma_f32_16x16x32_bf16 v[100:103], v[124:127], v[180:183], v[100:103]\nv_mfma_f32_16x16x32_bf16 v[100:103], v[120:123], v[176:179], v[100:103]\n"
      "v_mfma_f32_16x16x32_bf16 v[12:15], v[136:139], v[184:187], v[12:15]\nds_read_b128 v[176:179], v210 offset:6144\nv_mfma_f32_16x16x32_bf16 v[12:15], v[140:143], v[188:191], v[12:15]\n"
      "ds_read_b128 v[180:183], v211 offset:6144\nv_mfma_f32_16x16x32_bf16 v[8:11], v[132:135], v[188:191], v[8:11]\nv_mfma_f32_16x16x32_bf16 v[8:11], v[128:131], v[184:187], v[8:11]\n"
      "v_mfma_f32_16x16x32_bf16 v[4:7], v[120:123], v[184:187], v[4:7]\nv_mfma_f32_16x16x32_bf16 v[4:7], v[124:127], v[188:191], v[4:7]\n"
      "v_mfma_f32_16x16x32_bf16 v[0:3], v[116:119], v[188:191], v[0:3]\ns_waitcnt lgkmcnt(4)\nv_mfma_f32_16x16x32_bf16 v[0:3], v[112:115], v[184:187], v[0:3]\n"
      "v_mfma_f32_16x16x32_bf16 v[16:19], v[112:115], v[192:195], v[16:19]\nds_read_b128 v[184:187], v210 offset:8192\nv_mfma_f32_16x16x32_bf16 v[16:19], v[116:119], v[196:199], v[16:19]\n"
      "ds_read_b128 v[188:191], v211 offset:8192\nv_mfma_f32_16x16x32_bf16 v[20:23], v[124:127], v[196:199], v[20:23]\nv_mfma_f32_16x16x32_bf16 v[20:23], v[120:123], v[192:195], v[20:23]\n"
      "v_mfma_f32_16x16x32_bf16 v[24:27], v[128:131], v[192:195], v[24:27]\nv_mfma_f32_16x16x32_bf16 v[24:27], v[132:135], v[196:199], v[24:27]\n"
      "v_mfma_f32_16x16x32_bf16 v[28:31], v[140:143], v[196:199], v[28:31]\ns_waitcnt lgkmcnt(2)\nv_mfma_f32_16x16x32_bf16 v[28:31], v[136:139], v[192:195], v[28:31]\n"
      "v_mfma_f32_16x16x32_bf16 v[44:47], v[136:139], v[200:203], v[44:47]\nds_read_b128 v[192:195], v210 offset:10240\nv_mfma_f32_16x16x32_bf16 v[44:47], v[140:143], v[204:207], v[44:47]\n"
      "ds_read_b128 v[196:199], v211 offset:10240\nv_mfma_f32_16x16x32_bf16 v[40:43], v[132:135], v[204:207], v[40:43]\nv_mfma_f32_16x16x32_bf16 v[40:43], v[128:131], v[200:203], v[40:43]\n"
      "v_mfma_f32_16x16x32_bf16 v[36:39], v[120:123], v[200:203], v[36:39]\nv_mfma_f32_16x16x32_bf16 v[36:39], v[124:127], v[204:207], v[36:39]\n"
      "v_mfma_f32_16x16x32_bf16 v[32:35], v[116:119], v[204:207], v[32:35]\ns_waitcnt vmcnt(0) lgkmcnt(0)\ns_barrier\nv_mfma_f32_16x16x32_bf16 v[32:35], v[112:115], v[200:203], v[32:35]\n"
      "ds_read_b64_tr_b16 v[144:145], v216 offset:0\nv_mfma_f32_16x16x32_bf16 v[48:51], v[112:115], v[176:179], v[48:51]\nds_read_b64_tr_b16 v[146:147], v216 offset:1024\n"
      "v_mfma_f32_16x16x32_bf16 v[48:51], v[116:119], v[180:183], v[48:51]\nds_read_b64_tr_b16 v[148:149], v216 offset:8192\nv_mfma_f32_16x16x32_bf16 v[52:55], v[124:127], v[180:183], v[52:55]\n"
      "ds_read_b64_tr_b16 v[150:151], v216 offset:9216\nv_mfma_f32_16x16x32_bf16 v[52:55], v[120:123], v[176:179], v[52:55]\nds_read_b64_tr_b16 v[152:153], v217 offset:0\n"
      "v_mfma_f32_16x16x32_bf16 v[56:59], v[128:131], v[176:179], v[56:59]\nds_read_b64_tr_b16 v[154:155], v217 offset:1024\nv_mfma_f32_16x16x32_bf16 v[56:59], v[132:135], v[180:183], v[56:59]\n"
      "ds_read_b64_tr_b16 v[156:157], v217 offset:8192\nv_mfma_f32_16x16x32_bf16 v[60:63], v[140:143], v[180:183], v[60:63]\nds_read_b64_tr_b16 v[158:159], v217 offset:9216\n"
      "v_mfma_f32_16x16x32_bf16 v[60:63], v[136:139], v[176:179], v[60:63]\nds_read_b64_tr_b16 v[160:161], v218 offset:0\nv_mfma_f32_16x16x32_bf16 v[76:79], v[136:139], v[184:187], v[76:79]\n"
      "ds_read_b128 v[176:179], v212 offset:0\nv_mfma_f32_16x16x32_bf16 v[76:79], v[140:143], v[188:191], v[76:79]\nds_read_b128 v[180:183], v213 offset:0\n"
      "v_mfma_f32_16x16x32_bf16 v[72:75], v[132:135], v[188:191], v[72:75]\nds_read_b64_tr_b16 v[162:163], v218 offset:1024\nv_mfma_f32_16x16x32_bf16 v[72:75], v[128:131], v[184:187], v[72:75]\n"
      "ds_read_b64_tr_b16 v[164:165], v218 offset:8192\nv_mfma_f32_16x16x32_bf16 v[68:71], v[120:123], v[184:187], v[68:71]\nds_read_b64_tr_b16 v[166:167], v218 offset:9216\n"
      "v_mfma_f32_16x16x32_bf16 v[68:71], v[124:127], v[188:191], v[68:71]\nds_read_b64_tr_b16 v[168:169], v219 offset:0\nv_mfma_f32_16x16x32_bf16 v[64:67], v[116:119], v[188:191], v[64:67]\n"
      "ds_read_b64_tr_b16 v[170:171], v219 offset:1024\nv_mfma_f32_16x16x32_bf16 v[64:67], v[112:115], v[184:187], v[64:67]\nds_read_b64_tr_b16 v[172:173], v219 offset:8192\n"
      "v_mfma_f32_16x16x32_bf16 v[80:83], v[112:115], v[192:195], v[80:83]\nds_read_b128 v[184:187], v208 offset:0\nv_mfma_f32_16x16x32_bf16 v[80:83], v[116:119], v[196:199], v[80:83]\n"
      "ds_read_b128 v[188:191], v209 offset:0\nv_mfma_f32_16x16x32_bf16 v[84:87], v[124:127], v[196:199], v[84:87]\nds_read_b64_tr_b16 v[174:175], v219 offset:9216\n"
      "v_mfma_f32_16x16x32_bf16 v[84:87], v[120:123], v[192:195], v[84:87]\nv_mfma_f32_16x16x32_bf16 v[88:91], v[128:131], v[192:195], v[88:91]\n"
      "v_mfma_f32_16x16x32_bf16 v[88:91], v[132:135], v[196:199], v[88:91]\nv_mfma_f32_16x16x32_bf16 v[92:95], v[140:143], v[196:199], v[92:95]\ns_waitcnt lgkmcnt(0)\n"
      "v_mfma_f32_16x16x32_bf16 v[92:95], v[136:139], v[192:195], v[92:95]\nds_read_b128 v[192:195], v208 offset:2048\nds_read_b128 v[196:199], v209 offset:2048\n"
      "v_mfma_f32_16x16x32_bf16 v[96:99], v[144:147], v[176:179], v[96:99]\nds_read_b128 v[200:203], v208 offset:4096\nv_mfma_f32_16x16x32_bf16 v[96:99], v[148:151], v[180:183], v[96:99]\n"
      "ds_read_b128 v[204:207], v209 offset:4096\nv_mfma_f32_16x16x32_bf16 v[100:103], v[156:159], v[180:183], v[100:103]\nv_mfma_f32_16x16x32_bf16 v[100:103], v[152:155], v[176:179], v[100:103]\n"
      "v_mfma_f32_16x16x32_bf16 v[12:15], v[168:171], v[184:187], v[12:15]\nds_read_b128 v[176:179], v208 offset:6144\nv_mfma_f32_16x16x32_bf16 v[12:15], v[172:175], v[188:191], v[12:15]\n"
      "ds_read_b128 v[180:183], v209 offset:6144\nv_mfma_f32_16x16x32_bf16 v[8:11], v[164:167], v[188:191], v[8:11]\nv_mfma_f32_16x16x32_bf16 v[8:11], v[160:163], v[184:187], v[8:11]\n"
      "v_mfma_f32_16x16x32_bf16 v[4:7], v[152:155], v[184:187], v[4:7]\nv_mfma_f32_16x16x32_bf16 v[4:7], v[156:159], v[188:191], v[4:7]\n"
      "v_mfma_f32_16x16x32_bf16 v[0:3], v[148:151], v[188:191], v[0:3]\ns_waitcnt lgkmcnt(4)\nv_mfma_f32_16x16x32_bf16 v[0:3], v[144:147], v[184:187], v[0:3]\n"
      "v_mfma_f32_16x16x32_bf16 v[16:19], v[144:147], v[192:195], v[16:19]\nds_read_b128 v[184:187], v208 offset:8192\nv_mfma_f32_16x16x32_bf16 v[16:19], v[148:151], v[196:199], v[16:19]\n"
      "ds_read_b128 v[188:191], v209 offset:8192\nv_mfma_f32_16x16x32_bf16 v[20:23], v[156:159], v[196:199], v[20:23]\nv_mfma_f32_16x16x32_bf16 v[20:23], v[152:155], v[192:195], v[20:23]\n"
      "v_mfma_f32_16x16x32_bf16 v[24:27], v[160:163], v[192:195], v[24:27]\nv_mfma_f32_16x16x32_bf16 v[24:27], v[164:167], v[196:199], v[24:27]\n"
      "v_mfma_f32_16x16x32_bf16 v[28:31], v[172:175], v[196:199], v[28:31]\ns_waitcnt lgkmcnt(2)\nv_mfma_f32_16x16x32_bf16 v[28:31], v[168:171], v[192:195], v[28:31]\n"
      "v_mfma_f32_16x16x32_bf16 v[44:47], v[168:171], v[200:203], v[44:47]\nds_read_b128 v[192:195], v208 offset:10240\nv_mfma_f32_16x16x32_bf16 v[44:47], v[172:175], v[204:207], v[44:47]\n"
      "ds_read_b128 v[196:199], v209 offset:10240\nv_mfma_f32_16x16x32_bf16 v[40:43], v[164:167], v[204:207], v[40:43]\nv_mfma_f32_16x16x32_bf16 v[40:43], v[160:163], v[200:203], v[40:43]\n"
      "v_mfma_f32_16x16x32_bf16 v[36:39], v[152:155], v[200:203], v[36:39]\nv_mfma_f32_16x16x32_bf16 v[36:39], v[156:159], v[204:207], v[36:39]\n"
      "v_mfma_f32_16x16x32_bf16 v[32:35], v[148:151], v[204:207], v[32:35]\ns_waitcnt lgkmcnt(0)\nv_mfma_f32_16x16x32_bf16 v[32:35], v[144:147], v[200:203], v[32:35]\n"
      "v_mfma_f32_16x16x32_bf16 v[48:51], v[144:147], v[176:179], v[48:51]\nv_mfma_f32_16x16x32_bf16 v[48:51], v[148:151], v[180:183], v[48:51]\n"
      "v_mfma_f32_16x16x32_bf16 v[52:55], v[156:159], v[180:183], v[52:55]\nv_mfma_f32_16x16x32_bf16 v[52:55], v[152:155], v[176:179], v[52:55]\n"
      "v_mfma_f32_16x16x32_bf16 v[56:59], v[160:163], v[176:179], v[56:59]\nv_mfma_f32_16x16x32_bf16 v[56:59], v[164:167], v[180:183], v[56:59]\n"
      "v_mfma_f32_16x16x32_bf16 v[60:63], v[172:175], v[180:183], v[60:63]\nv_mfma_f32_16x16x32_bf16 v[60:63], v[168:171], v[176:179], v[60:63]\n"
      "v_mfma_f32_16x16x32_bf16 v[76:79], v[168:171], v[184:187], v[76:79]\nv_mfma_f32_16x16x32_bf16 v[76:79], v[172:175], v[188:191], v[76:79]\n"
      "v_mfma_f32_16x16x32_bf16 v[72:75], v[164:167], v[188:191], v[72:75]\nv_mfma_f32_16x16x32_bf16 v[72:75], v[160:163], v[184:187], v[72:75]\n"
      "v_mfma_f32_16x16x32_bf16 v[68:71], v[152:155], v[184:187], v[68:71]\nv_mfma_f32_16x16x32_bf16 v[68:71], v[156:159], v[188:191], v[68:71]\n"
      "v_mfma_f32_16x16x32_bf16 v[64:67], v[148:151], v[188:191], v[64:67]\nv_mfma_f32_16x16x32_bf16 v[64:67], v[144:147], v[184:187], v[64:67]\n"
      "v_mfma_f32_16x16x32_bf16 v[80:83], v[144:147], v[192:195], v[80:83]\nv_mfma_f32_16x16x32_bf16 v[80:83], v[148:151], v[196:199], v[80:83]\n"
      "v_mfma_f32_16x16x32_bf16 v[84:87], v[156:159], v[196:199], v[84:87]\nv_mfma_f32_16x16x32_bf16 v[84:87], v[152:155], v[192:195], v[84:87]\n"
      "v_mfma_f32_16x16x32_bf16 v[88:91], v[160:163], v[192:195], v[88:91]\nv_mfma_f32_16x16x32_bf16 v[88:91], v[164:167], v[196:199], v[88:91]\n"
      "v_mfma_f32_16x16x32_bf16 v[92:95], v[172:175], v[196:199], v[92:95]\nv_mfma_f32_16x16x32_bf16 v[92:95], v[168:171], v[192:195], v[92:95]\ns_setprio 3\nv_lshlrev_b32 v112, 16, v224\n"
      "v_and_b32 v113, 0xffff0000, v224\nv_lshlrev_b32 v114, 16, v225\nv_and_b32 v115, 0xffff0000, v225\nv_lshlrev_b32 v116, 16, v226\nv_and_b32 v117, 0xffff0000, v226\n"
      "v_lshlrev_b32 v118, 16, v227\nv_and_b32 v119, 0xffff0000, v227\nv_lshlrev_b32 v120, 16, v228\nv_and_b32 v121, 0xffff0000, v228\nv_lshlrev_b32 v122, 16, v229\n"
      "v_and_b32 v123, 0xffff0000, v229\nv_lshlrev_b32 v124, 16, v230\nv_and_b32 v125, 0xffff0000, v230\nv_lshlrev_b32 v126, 16, v231\nv_and_b32 v127, 0xffff0000, v231\nv_mov_b32 v128, 0xc0135761\n"
      "s_mov_b32 s46, 0xbdd2d3e7\nv_mov_b32 v166, 0\nv_pk_mul_f32 v[132:133], v[96:97], v[96:97]\nv_pk_mul_f32 v[134:135], v[98:99], v[98:99]\nv_pk_mul_f32 v[136:137], v[100:101], v[100:101]\n"
      "v_pk_mul_f32 v[138:139], v[102:103], v[102:103]\nv_pk_fma_f32 v[132:133], v[132:133], s[46:47], v[128:129] op_sel_hi:[1,0,0]\n"
      "v_pk_fma_f32 v[134:135], v[134:135], s[46:47], v[128:129] op_sel_hi:[1,0,0]\nv_pk_fma_f32 v[136:137], v[136:137], s[46:47], v[128:129] op_sel_hi:[1,0,0]\n"
      "v_pk_fma_f32 v[138:139], v[138:139], s[46:47], v[128:129] op_sel_hi:[1,0,0]\nv_pk_mul_f32 v[132:133], v[132:133], v[96:97]\nv_pk_mul_f32 v[134:135], v[134:135], v[98:99]\n"
      "v_pk_mul_f32 v[136:137], v[136:137], v[100:101]\nv_pk_mul_f32 v[138:139], v[138:139], v[102:103]\nv_pk_mul_f32 v[96:97], v[96:97], v[112:113]\nv_pk_mul_f32 v[98:99], v[98:99], v[114:115]\n"
      "v_pk_mul_f32 v[100:101], v[100:101], v[116:117]\nv_pk_mul_f32 v[102:103], v[102:103], v[118:119]\nv_exp_f32 v132, v132\nv_exp_f32 v133, v133\nv_exp_f32 v134, v134\nv_exp_f32 v135, v135\n"
      "v_exp_f32 v136, v136\nv_exp_f32 v137, v137\nv_exp_f32 v138, v138\nv_exp_f32 v139, v139\nv_pk_add_f32 v[132:133], v[132:133], 1.0 op_sel_hi:[1,0]\n"
      "v_pk_add_f32 v[134:135], v[134:135], 1.0 op_sel_hi:[1,0]\nv_pk_add_f32 v[136:137], v[136:137], 1.0 op_sel_hi:[1,0]\nv_pk_add_f32 v[138:139], v[138:139], 1.0 op_sel_hi:[1,0]\n"
      "v_rcp_f32 v132, v132\nv_rcp_f32 v133, v133\nv_rcp_f32 v134, v134\nv_rcp_f32 v135, v135\nv_rcp_f32 v136, v136\nv_rcp_f32 v137, v137\nv_rcp_f32 v138, v138\nv_rcp_f32 v139, v139\n"
      "v_pk_mul_f32 v[148:149], v[96:97], v[132:133]\nv_pk_mul_f32 v[140:141], v[0:1], v[0:1]\nv_pk_mul_f32 v[150:151], v[98:99], v[134:135]\nv_pk_mul_f32 v[142:143], v[2:3], v[2:3]\n"
      "v_pk_fma_f32 v[148:149], v[100:101], v[136:137], v[148:149]\nv_pk_mul_f32 v[144:145], v[4:5], v[4:5]\nv_pk_fma_f32 v[150:151], v[102:103], v[138:139], v[150:151]\n"
      "v_pk_mul_f32 v[146:147], v[6:7], v[6:7]\nv_pk_add_f32 v[148:149], v[148:149], v[150:151]\nv_pk_fma_f32 v[140:141], v[140:141], s[46:47], v[128:129] op_sel_hi:[1,0,0]\n"
      "v_add_f32 v166, v148, v149\nv_pk_fma_f32 v[142:143], v[142:143], s[46:47], v[128:129] op_sel_hi:[1,0,0]\nv_pk_fma_f32 v[144:145], v[144:145], s[46:47], v[128:129] op_sel_hi:[1,0,0]\n"
      "v_pk_fma_f32 v[146:147], v[146:147], s[46:47], v[128:129] op_sel_hi:[1,0,0]\nv_pk_mul_f32 v[140:141], v[140:141], v[0:1]\nv_pk_mul_f32 v[142:143], v[142:143], v[2:3]\n"
      "v_pk_mul_f32 v[144:145], v[144:145], v[4:5]\nv_pk_mul_f32 v[146:147], v[146:147], v[6:7]\nv_pk_mul_f32 v[0:1], v[0:1], v[112:113]\nv_pk_mul_f32 v[2:3], v[2:3], v[114:115]\n"
      "v_pk_mul_f32 v[4:5], v[4:5], v[116:117]\nv_pk_mul_f32 v[6:7], v[6:7], v[118:119]\nv_exp_f32 v140, v140\nv_exp_f32 v141, v141\nv_exp_f32 v142, v142\nv_exp_f32 v143, v143\n"
      "v_exp_f32 v144, v144\nv_exp_f32 v145, v145\nv_exp_f32 v146, v146\nv_exp_f32 v147, v147\nv_pk_add_f32 v[140:141], v[140:141], 1.0 op_sel_hi:[1,0]\n"
      "v_pk_add_f32 v[142:143], v[142:143], 1.0 op_sel_hi:[1,0]\nv_pk_add_f32 v[144:145], v[144:145], 1.0 op_sel_hi:[1,0]\nv_pk_add_f32 v[146:147], v[146:147], 1.0 op_sel_hi:[1,0]\n"
      "v_rcp_f32 v140, v140\nv_rcp_f32 v141, v141\nv_rcp_f32 v142, v142\nv_rcp_f32 v143, v143\nv_rcp_f32 v144, v144\nv_rcp_f32 v145, v145\nv_rcp_f32 v146, v146\nv_rcp_f32 v147, v147\n"
      "v_pk_mul_f32 v[148:149], v[0:1], v[140:141]\nv_pk_mul_f32 v[132:133], v[8:9], v[8:9]\nv_pk_mul_f32 v[150:151], v[2:3], v[142:143]\nv_pk_mul_f32 v[134:135], v[10:11], v[10:11]\n"
      "v_pk_fma_f32 v[148:149], v[4:5], v[144:145], v[148:149]\nv_pk_mul_f32 v[136:137], v[12:13], v[12:13]\nv_pk_fma_f32 v[150:151], v[6:7], v[146:147], v[150:151]\n"
      "v_pk_mul_f32 v[138:139], v[14:15], v[14:15]\nv_pk_fma_f32 v[132:133], v[132:133], s[46:47], v[128:129] op_sel_hi:[1,0,0]\n"
      "v_pk_fma_f32 v[134:135], v[134:135], s[46:47], v[128:129] op_sel_hi:[1,0,0]\nv_pk_fma_f32 v[136:137], v[136:137], s[46:47], v[128:129] op_sel_hi:[1,0,0]\n"
      "v_pk_fma_f32 v[138:139], v[138:139], s[46:47], v[128:129] op_sel_hi:[1,0,0]\nv_pk_mul_f32 v[132:133], v[132:133], v[8:9]\nv_pk_mul_f32 v[134:135], v[134:135], v[10:11]\n"
      "v_pk_mul_f32 v[136:137], v[136:137], v[12:13]\nv_pk_mul_f32 v[138:139], v[138:139], v[14:15]\nv_pk_mul_f32 v[8:9], v[8:9], v[120:121]\nv_pk_mul_f32 v[10:11], v[10:11], v[122:123]\n"
      "v_pk_mul_f32 v[12:13], v[12:13], v[124:125]\nv_pk_mul_f32 v[14:15], v[14:15], v[126:127]\nv_exp_f32 v132, v132\nv_exp_f32 v133, v133\nv_exp_f32 v134, v134\nv_exp_f32 v135, v135\n"
      "v_exp_f32 v136, v136\nv_exp_f32 v137, v137\nv_exp_f32 v138, v138\nv_exp_f32 v139, v139\nv_pk_add_f32 v[132:133], v[132:133], 1.0 op_sel_hi:[1,0]\n"
      "v_pk_add_f32 v[134:135], v[134:135], 1.0 op_sel_hi:[1,0]\nv_pk_add_f32 v[136:137], v[136:137], 1.0 op_sel_hi:[1,0]\nv_pk_add_f32 v[138:139], v[138:139], 1.0 op_sel_hi:[1,0]\n"
      "v_rcp_f32 v132, v132\nv_rcp_f32 v133, v133\nv_rcp_f32 v134, v134\nv_rcp_f32 v135, v135\nv_rcp_f32 v136, v136\nv_rcp_f32 v137, v137\nv_rcp_f32 v138, v138\nv_rcp_f32 v139, v139\n"
      "v_pk_fma_f32 v[148:149], v[8:9], v[132:133], v[148:149]\nv_pk_mul_f32 v[140:141], v[16:17], v[16:17]\nv_pk_fma_f32 v[150:151], v[10:11], v[134:135], v[150:151]\n"
      "v_pk_mul_f32 v[142:143], v[18:19], v[18:19]\nv_pk_fma_f32 v[148:149], v[12:13], v[136:137], v[148:149]\nv_pk_mul_f32 v[144:145], v[20:21], v[20:21]\n"
      "v_pk_fma_f32 v[150:151], v[14:15], v[138:139], v[150:151]\nv_pk_mul_f32 v[146:147], v[22:23], v[22:23]\nv_pk_add_f32 v[148:149], v[148:149], v[150:151]\n"
      "v_pk_fma_f32 v[140:141], v[140:141], s[46:47], v[128:129] op_sel_hi:[1,0,0]\nv_add_f32 v160, v148, v149\nv_pk_fma_f32 v[142:143], v[142:143], s[46:47], v[128:129] op_sel_hi:[1,0,0]\n"
      "v_pk_fma_f32 v[144:145], v[144:145], s[46:47], v[128:129] op_sel_hi:[1,0,0]\nv_pk_fma_f32 v[146:147], v[146:147], s[46:47], v[128:129] op_sel_hi:[1,0,0]\n"
      "v_pk_mul_f32 v[140:141], v[140:141], v[16:17]\nv_pk_mul_f32 v[142:143], v[142:143], v[18:19]\nv_pk_mul_f32 v[144:145], v[144:145], v[20:21]\nv_pk_mul_f32 v[146:147], v[146:147], v[22:23]\n"
      "v_pk_mul_f32 v[16:17], v[16:17], v[112:113]\nv_pk_mul_f32 v[18:19], v[18:19], v[114:115]\nv_pk_mul_f32 v[20:21], v[20:21], v[116:117]\nv_pk_mul_f32 v[22:23], v[22:23], v[118:119]\n"
      "v_exp_f32 v140, v140\nv_exp_f32 v141, v141\nv_exp_f32 v142, v142\nv_exp_f32 v143, v143\nv_exp_f32 v144, v144\nv_exp_f32 v145, v145\nv_exp_f32 v146, v146\nv_exp_f32 v147, v147\n"
      "v_pk_add_f32 v[140:141], v[140:141], 1.0 op_sel_hi:[1,0]\nv_pk_add_f32 v[142:143], v[142:143], 1.0 op_sel_hi:[1,0]\nv_pk_add_f32 v[144:145], v[144:145], 1.0 op_sel_hi:[1,0]\n"
      "v_pk_add_f32 v[146:147], v[146:147], 1.0 op_sel_hi:[1,0]\nv_rcp_f32 v140, v140\nv_rcp_f32 v141, v141\nv_rcp_f32 v142, v142\nv_rcp_f32 v143, v143\nv_rcp_f32 v144, v144\nv_rcp_f32 v145, v145\n"
      "v_rcp_f32 v146, v146\nv_rcp_f32 v147, v147\nv_pk_mul_f32 v[148:149], v[16:17], v[140:141]\nv_pk_mul_f32 v[132:133], v[24:25], v[24:25]\nv_pk_mul_f32 v[150:151], v[18:19], v[142:143]\n"
      "v_pk_mul_f32 v[134:135], v[26:27], v[26:27]\nv_pk_fma_f32 v[148:149], v[20:21], v[144:145], v[148:149]\nv_pk_mul_f32 v[136:137], v[28:29], v[28:29]\n"
      "v_pk_fma_f32 v[150:151], v[22:23], v[146:147], v[150:151]\nv_pk_mul_f32 v[138:139], v[30:31], v[30:31]\nv_pk_fma_f32 v[132:133], v[132:133], s[46:47], v[128:129] op_sel_hi:[1,0,0]\n"
      "v_pk_fma_f32 v[134:135], v[134:135], s[46:47], v[128:129] op_sel_hi:[1,0,0]\nv_pk_fma_f32 v[136:137], v[136:137], s[46:47], v[128:129] op_sel_hi:[1,0,0]\n"
      "v_pk_fma_f32 v[138:139], v[138:139], s[46:47], v[128:129] op_sel_hi:[1,0,0]\nv_pk_mul_f32 v[132:133], v[132:133], v[24:25]\nv_pk_mul_f32 v[134:135], v[134:135], v[26:27]\n"
      "v_pk_mul_f32 v[136:137], v[136:137], v[28:29]\nv_pk_mul_f32 v[138:139], v[138:139], v[30:31]\nv_pk_mul_f32 v[24:25], v[24:25], v[120:121]\nv_pk_mul_f32 v[26:27], v[26:27], v[122:123]\n"
      "v_pk_mul_f32 v[28:29], v[28:29], v[124:125]\nv_pk_mul_f32 v[30:31], v[30:31], v[126:127]\nv_exp_f32 v132, v132\nv_exp_f32 v133, v133\nv_exp_f32 v134, v134\nv_exp_f32 v135, v135\n"
      "v_exp_f32 v136, v136\nv_exp_f32 v137, v137\nv_exp_f32 v138, v138\nv_exp_f32 v139, v139\nv_pk_add_f32 v[132:133], v[132:133], 1.0 op_sel_hi:[1,0]\n"
      "v_pk_add_f32 v[134:135], v[134:135], 1.0 op_sel_hi:[1,0]\nv_pk_add_f32 v[136:137], v[136:137], 1.0 op_sel_hi:[1,0]\nv_pk_add_f32 v[138:139], v[138:139], 1.0 op_sel_hi:[1,0]\n"
      "v_rcp_f32 v132, v132\nv_rcp_f32 v133, v133\nv_rcp_f32 v134, v134\nv_rcp_f32 v135, v135\nv_rcp_f32 v136, v136\nv_rcp_f32 v137, v137\nv_rcp_f32 v138, v138\nv_rcp_f32 v139, v139\n"
      "v_pk_fma_f32 v[148:149], v[24:25], v[132:133], v[148:149]\nv_pk_mul_f32 v[140:141], v[32:33], v[32:33]\nv_pk_fma_f32 v[150:151], v[26:27], v[134:135], v[150:151]\n"
      "v_pk_mul_f32 v[142:143], v[34:35], v[34:35]\nv_pk_fma_f32 v[148:149], v[28:29], v[136:137], v[148:149]\nv_pk_mul_f32 v[144:145], v[36:37], v[36:37]\n"
      "v_pk_fma_f32 v[150:151], v[30:31], v[138:139], v[150:151]\nv_pk_mul_f32 v[146:147], v[38:39], v[38:39]\nv_pk_add_f32 v[148:149], v[148:149], v[150:151]\n"
      "v_pk_fma_f32 v[140:141], v[140:141], s[46:47], v[128:129] op_sel_hi:[1,0,0]\nv_add_f32 v161, v148, v149\nv_pk_fma_f32 v[142:143], v[142:143], s[46:47], v[128:129] op_sel_hi:[1,0,0]\n"
      "v_pk_fma_f32 v[144:145], v[144:145], s[46:47], v[128:129] op_sel_hi:[1,0,0]\nv_pk_fma_f32 v[146:147], v[146:147], s[46:47], v[128:129] op_sel_hi:[1,0,0]\n"
      "v_pk_mul_f32 v[140:141], v[140:141], v[32:33]\nv_pk_mul_f32 v[142:143], v[142:143], v[34:35]\nv_pk_mul_f32 v[144:145], v[144:145], v[36:37]\nv_pk_mul_f32 v[146:147], v[146:147], v[38:39]\n"
      "v_pk_mul_f32 v[32:33], v[32:33], v[112:113]\nv_pk_mul_f32 v[34:35], v[34:35], v[114:115]\nv_pk_mul_f32 v[36:37], v[36:37], v[116:117]\nv_pk_mul_f32 v[38:39], v[38:39], v[118:119]\n"
      "v_exp_f32 v140, v140\nv_exp_f32 v141, v141\nv_exp_f32 v142, v142\nv_exp_f32 v143, v143\nv_exp_f32 v144, v144\nv_exp_f32 v145, v145\nv_exp_f32 v146, v146\nv_exp_f32 v147, v147\n"
      "v_pk_add_f32 v[140:141], v[140:141], 1.0 op_sel_hi:[1,0]\nv_pk_add_f32 v[142:143], v[142:143], 1.0 op_sel_hi:[1,0]\nv_pk_add_f32 v[144:145], v[144:145], 1.0 op_sel_hi:[1,0]\n"
      "v_pk_add_f32 v[146:147], v[146:147], 1.0 op_sel_hi:[1,0]\nv_rcp_f32 v140, v140\nv_rcp_f32 v141, v141\nv_rcp_f32 v142, v142\nv_rcp_f32 v143, v143\nv_rcp_f32 v144, v144\nv_rcp_f32 v145, v145\n"
      "v_rcp_f32 v146, v146\nv_rcp_f32 v147, v147\nv_pk_mul_f32 v[148:149], v[32:33], v[140:141]\nv_pk_mul_f32 v[132:133], v[40:41], v[40:41]\nv_pk_mul_f32 v[150:151], v[34:35], v[142:143]\n"
      "v_pk_mul_f32 v[134:135], v[42:43], v[42:43]\nv_pk_fma_f32 v[148:149], v[36:37], v[144:145], v[148:149]\nv_pk_mul_f32 v[136:137], v[44:45], v[44:45]\n"
      "v_pk_fma_f32 v[150:151], v[38:39], v[146:147], v[150:151]\nv_pk_mul_f32 v[138:139], v[46:47], v[46:47]\nv_pk_fma_f32 v[132:133], v[132:133], s[46:47], v[128:129] op_sel_hi:[1,0,0]\n"
      "v_pk_fma_f32 v[134:135], v[134:135], s[46:47], v[128:129] op_sel_hi:[1,0,0]\nv_pk_fma_f32 v[136:137], v[136:137], s[46:47], v[128:129] op_sel_hi:[1,0,0]\n"
      "v_pk_fma_f32 v[138:139], v[138:139], s[46:47], v[128:129] op_sel_hi:[1,0,0]\nv_pk_mul_f32 v[132:133], v[132:133], v[40:41]\nv_pk_mul_f32 v[134:135], v[134:135], v[42:43]\n"
      "v_pk_mul_f32 v[136:137], v[136:137], v[44:45]\nv_pk_mul_f32 v[138:139], v[138:139], v[46:47]\nv_pk_mul_f32 v[40:41], v[40:41], v[120:121]\nv_pk_mul_f32 v[42:43], v[42:43], v[122:123]\n"
      "v_pk_mul_f32 v[44:45], v[44:45], v[124:125]\nv_pk_mul_f32 v[46:47], v[46:47], v[126:127]\nv_exp_f32 v132, v132\nv_exp_f32 v133, v133\nv_exp_f32 v134, v134\nv_exp_f32 v135, v135\n"
      "v_exp_f32 v136, v136\nv_exp_f32 v137, v137\nv_exp_f32 v138, v138\nv_exp_f32 v139, v139\nv_pk_add_f32 v[132:133], v[132:133], 1.0 op_sel_hi:[1,0]\n"
      "v_pk_add_f32 v[134:135], v[134:135], 1.0 op_sel_hi:[1,0]\nv_pk_add_f32 v[136:137], v[136:137], 1.0 op_sel_hi:[1,0]\nv_pk_add_f32 v[138:139], v[138:139], 1.0 op_sel_hi:[1,0]\n"
      "v_rcp_f32 v132, v132\nv_rcp_f32 v133, v133\nv_rcp_f32 v134, v134\nv_rcp_f32 v135, v135\nv_rcp_f32 v136, v136\nv_rcp_f32 v137, v137\nv_rcp_f32 v138, v138\nv_rcp_f32 v139, v139\n"
      "v_pk_fma_f32 v[148:149], v[40:41], v[132:133], v[148:149]\nv_pk_mul_f32 v[140:141], v[48:49], v[48:49]\nv_pk_fma_f32 v[150:151], v[42:43], v[134:135], v[150:151]\n"
      "v_pk_mul_f32 v[142:143], v[50:51], v[50:51]\nv_pk_fma_f32 v[148:149], v[44:45], v[136:137], v[148:149]\nv_pk_mul_f32 v[144:145], v[52:53], v[52:53]\n"
      "v_pk_fma_f32 v[150:151], v[46:47], v[138:139], v[150:151]\nv_pk_mul_f32 v[146:147], v[54:55], v[54:55]\nv_pk_add_f32 v[148:149], v[148:149], v[150:151]\n"
      "v_pk_fma_f32 v[140:141], v[140:141], s[46:47], v[128:129] op_sel_hi:[1,0,0]\nv_add_f32 v162, v148, v149\nv_pk_fma_f32 v[142:143], v[142:143], s[46:47], v[128:129] op_sel_hi:[1,0,0]\n"
      "v_pk_fma_f32 v[144:145], v[144:145], s[46:47], v[128:129] op_sel_hi:[1,0,0]\nv_pk_fma_f32 v[146:147], v[146:147], s[46:47], v[128:129] op_sel_hi:[1,0,0]\n"
      "v_pk_mul_f32 v[140:141], v[140:141], v[48:49]\nv_pk_mul_f32 v[142:143], v[142:143], v[50:51]\nv_pk_mul_f32 v[144:145], v[144:145], v[52:53]\nv_pk_mul_f32 v[146:147], v[146:147], v[54:55]\n"
      "v_pk_mul_f32 v[48:49], v[48:49], v[112:113]\nv_pk_mul_f32 v[50:51], v[50:51], v[114:115]\nv_pk_mul_f32 v[52:53], v[52:53], v[116:117]\nv_pk_mul_f32 v[54:55], v[54:55], v[118:119]\n"
      "v_exp_f32 v140, v140\nv_exp_f32 v141, v141\nv_exp_f32 v142, v142\nv_exp_f32 v143, v143\nv_exp_f32 v144, v144\nv_exp_f32 v145, v145\nv_exp_f32 v146, v146\nv_exp_f32 v147, v147\n"
      "v_pk_add_f32 v[140:141], v[140:141], 1.0 op_sel_hi:[1,0]\nv_pk_add_f32 v[142:143], v[142:143], 1.0 op_sel_hi:[1,0]\nv_pk_add_f32 v[144:145], v[144:145], 1.0 op_sel_hi:[1,0]\n"
      "v_pk_add_f32 v[146:147], v[146:147], 1.0 op_sel_hi:[1,0]\nv_rcp_f32 v140, v140\nv_rcp_f32 v141, v141\nv_rcp_f32 v142, v142\nv_rcp_f32 v143, v143\nv_rcp_f32 v144, v144\nv_rcp_f32 v145, v145\n"
      "v_rcp_f32 v146, v146\nv_rcp_f32 v147, v147\nv_pk_mul_f32 v[148:149], v[48:49], v[140:141]\nv_pk_mul_f32 v[132:133], v[56:57], v[56:57]\nv_pk_mul_f32 v[150:151], v[50:51], v[142:143]\n"
      "v_pk_mul_f32 v[134:135], v[58:59], v[58:59]\nv_pk_fma_f32 v[148:149], v[52:53], v[144:145], v[148:149]\nv_pk_mul_f32 v[136:137], v[60:61], v[60:61]\n"
      "v_pk_fma_f32 v[150:151], v[54:55], v[146:147], v[150:151]\nv_pk_mul_f32 v[138:139], v[62:63], v[62:63]\nv_pk_fma_f32 v[132:133], v[132:133], s[46:47], v[128:129] op_sel_hi:[1,0,0]\n"
      "v_pk_fma_f32 v[134:135], v[134:135], s[46:47], v[128:129] op_sel_hi:[1,0,0]\nv_pk_fma_f32 v[136:137], v[136:137], s[46:47], v[128:129] op_sel_hi:[1,0,0]\n"
      "v_pk_fma_f32 v[138:139], v[138:139], s[46:47], v[128:129] op_sel_hi:[1,0,0]\nv_pk_mul_f32 v[132:133], v[132:133], v[56:57]\nv_pk_mul_f32 v[134:135], v[134:135], v[58:59]\n"
      "v_pk_mul_f32 v[136:137], v[136:137], v[60:61]\nv_pk_mul_f32 v[138:139], v[138:139], v[62:63]\nv_pk_mul_f32 v[56:57], v[56:57], v[120:121]\nv_pk_mul_f32 v[58:59], v[58:59], v[122:123]\n"
      "v_pk_mul_f32 v[60:61], v[60:61], v[124:125]\nv_pk_mul_f32 v[62:63], v[62:63], v[126:127]\nv_exp_f32 v132, v132\nv_exp_f32 v133, v133\nv_exp_f32 v134, v134\nv_exp_f32 v135, v135\n"
      "v_exp_f32 v136, v136\nv_exp_f32 v137, v137\nv_exp_f32 v138, v138\nv_exp_f32 v139, v139\nv_pk_add_f32 v[132:133], v[132:133], 1.0 op_sel_hi:[1,0]\n"
      "v_pk_add_f32 v[134:135], v[134:135], 1.0 op_sel_hi:[1,0]\nv_pk_add_f32 v[136:137], v[136:137], 1.0 op_sel_hi:[1,0]\nv_pk_add_f32 v[138:139], v[138:139], 1.0 op_sel_hi:[1,0]\n"
      "v_rcp_f32 v132, v132\nv_rcp_f32 v133, v133\nv_rcp_f32 v134, v134\nv_rcp_f32 v135, v135\nv_rcp_f32 v136, v136\nv_rcp_f32 v137, v137\nv_rcp_f32 v138, v138\nv_rcp_f32 v139, v139\n"
      "v_pk_fma_f32 v[148:149], v[56:57], v[132:133], v[148:149]\nv_pk_mul_f32 v[140:141], v[64:65], v[64:65]\nv_pk_fma_f32 v[150:151], v[58:59], v[134:135], v[150:151]\n"
      "v_pk_mul_f32 v[142:143], v[66:67], v[66:67]\nv_pk_fma_f32 v[148:149], v[60:61], v[136:137], v[148:149]\nv_pk_mul_f32 v[144:145], v[68:69], v[68:69]\n"
      "v_pk_fma_f32 v[150:151], v[62:63], v[138:139], v[150:151]\nv_pk_mul_f32 v[146:147], v[70:71], v[70:71]\nv_pk_add_f32 v[148:149], v[148:149], v[150:151]\n"
      "v_pk_fma_f32 v[140:141], v[140:141], s[46:47], v[128:129] op_sel_hi:[1,0,0]\nv_add_f32 v163, v148, v149\nv_pk_fma_f32 v[142:143], v[142:143], s[46:47], v[128:129] op_sel_hi:[1,0,0]\n"
      "v_pk_fma_f32 v[144:145], v[144:145], s[46:47], v[128:129] op_sel_hi:[1,0,0]\nv_pk_fma_f32 v[146:147], v[146:147], s[46:47], v[128:129] op_sel_hi:[1,0,0]\n"
      "v_pk_mul_f32 v[140:141], v[140:141], v[64:65]\nv_pk_mul_f32 v[142:143], v[142:143], v[66:67]\nv_pk_mul_f32 v[144:145], v[144:145], v[68:69]\nv_pk_mul_f32 v[146:147], v[146:147], v[70:71]\n"
      "v_pk_mul_f32 v[64:65], v[64:65], v[112:113]\nv_pk_mul_f32 v[66:67], v[66:67], v[114:115]\nv_pk_mul_f32 v[68:69], v[68:69], v[116:117]\nv_pk_mul_f32 v[70:71], v[70:71], v[118:119]\n"
      "v_exp_f32 v140, v140\nv_exp_f32 v141, v141\nv_exp_f32 v142, v142\nv_exp_f32 v143, v143\nv_exp_f32 v144, v144\nv_exp_f32 v145, v145\nv_exp_f32 v146, v146\nv_exp_f32 v147, v147\n"
      "v_pk_add_f32 v[140:141], v[140:141], 1.0 op_sel_hi:[1,0]\nv_pk_add_f32 v[142:143], v[142:143], 1.0 op_sel_hi:[1,0]\nv_pk_add_f32 v[144:145], v[144:145], 1.0 op_sel_hi:[1,0]\n"
      "v_pk_add_f32 v[146:147], v[146:147], 1.0 op_sel_hi:[1,0]\nv_rcp_f32 v140, v140\nv_rcp_f32 v141, v141\nv_rcp_f32 v142, v142\nv_rcp_f32 v143, v143\nv_rcp_f32 v144, v144\nv_rcp_f32 v145, v145\n"
      "v_rcp_f32 v146, v146\nv_rcp_f32 v147, v147\nv_pk_mul_f32 v[148:149], v[64:65], v[140:141]\nv_pk_mul_f32 v[132:133], v[72:73], v[72:73]\nv_pk_mul_f32 v[150:151], v[66:67], v[142:143]\n"
      "v_pk_mul_f32 v[134:135], v[74:75], v[74:75]\nv_pk_fma_f32 v[148:149], v[68:69], v[144:145], v[148:149]\nv_pk_mul_f32 v[136:137], v[76:77], v[76:77]\n"
      "v_pk_fma_f32 v[150:151], v[70:71], v[146:147], v[150:151]\nv_pk_mul_f32 v[138:139], v[78:79], v[78:79]\nv_pk_fma_f32 v[132:133], v[132:133], s[46:47], v[128:129] op_sel_hi:[1,0,0]\n"
      "v_pk_fma_f32 v[134:135], v[134:135], s[46:47], v[128:129] op_sel_hi:[1,0,0]\nv_pk_fma_f32 v[136:137], v[136:137], s[46:47], v[128:129] op_sel_hi:[1,0,0]\n"
      "v_pk_fma_f32 v[138:139], v[138:139], s[46:47], v[128:129] op_sel_hi:[1,0,0]\nv_pk_mul_f32 v[132:133], v[132:133], v[72:73]\nv_pk_mul_f32 v[134:135], v[134:135], v[74:75]\n"
      "v_pk_mul_f32 v[136:137], v[136:137], v[76:77]\nv_pk_mul_f32 v[138:139], v[138:139], v[78:79]\nv_pk_mul_f32 v[72:73], v[72:73], v[120:121]\nv_pk_mul_f32 v[74:75], v[74:75], v[122:123]\n"
      "v_pk_mul_f32 v[76:77], v[76:77], v[124:125]\nv_pk_mul_f32 v[78:79], v[78:79], v[126:127]\nv_exp_f32 v132, v132\nv_exp_f32 v133, v133\nv_exp_f32 v134, v134\nv_exp_f32 v135, v135\n"
      "v_exp_f32 v136, v136\nv_exp_f32 v137, v137\nv_exp_f32 v138, v138\nv_exp_f32 v139, v139\nv_pk_add_f32 v[132:133], v[132:133], 1.0 op_sel_hi:[1,0]\n"
      "v_pk_add_f32 v[134:135], v[134:135], 1.0 op_sel_hi:[1,0]\nv_pk_add_f32 v[136:137], v[136:137], 1.0 op_sel_hi:[1,0]\nv_pk_add_f32 v[138:139], v[138:139], 1.0 op_sel_hi:[1,0]\n"
      "v_rcp_f32 v132, v132\nv_rcp_f32 v133, v133\nv_rcp_f32 v134, v134\nv_rcp_f32 v135, v135\nv_rcp_f32 v136, v136\nv_rcp_f32 v137, v137\nv_rcp_f32 v138, v138\nv_rcp_f32 v139, v139\n"
      "v_pk_fma_f32 v[148:149], v[72:73], v[132:133], v[148:149]\nv_pk_mul_f32 v[140:141], v[80:81], v[80:81]\nv_pk_fma_f32 v[150:151], v[74:75], v[134:135], v[150:151]\n"
      "v_pk_mul_f32 v[142:143], v[82:83], v[82:83]\nv_pk_fma_f32 v[148:149], v[76:77], v[136:137], v[148:149]\nv_pk_mul_f32 v[144:145], v[84:85], v[84:85]\n"
      "v_pk_fma_f32 v[150:151], v[78:79], v[138:139], v[150:151]\nv_pk_mul_f32 v[146:147], v[86:87], v[86:87]\nv_pk_add_f32 v[148:149], v[148:149], v[150:151]\n"
      "v_pk_fma_f32 v[140:141], v[140:141], s[46:47], v[128:129] op_sel_hi:[1,0,0]\nv_add_f32 v164, v148, v149\nv_pk_fma_f32 v[142:143], v[142:143], s[46:47], v[128:129] op_sel_hi:[1,0,0]\n"
      "v_pk_fma_f32 v[144:145], v[144:145], s[46:47], v[128:129] op_sel_hi:[1,0,0]\nv_pk_fma_f32 v[146:147], v[146:147], s[46:47], v[128:129] op_sel_hi:[1,0,0]\n"
      "v_pk_mul_f32 v[140:141], v[140:141], v[80:81]\nv_pk_mul_f32 v[142:143], v[142:143], v[82:83]\nv_pk_mul_f32 v[144:145], v[144:145], v[84:85]\nv_pk_mul_f32 v[146:147], v[146:147], v[86:87]\n"
      "v_pk_mul_f32 v[80:81], v[80:81], v[112:113]\nv_pk_mul_f32 v[82:83], v[82:83], v[114:115]\nv_pk_mul_f32 v[84:85], v[84:85], v[116:117]\nv_pk_mul_f32 v[86:87], v[86:87], v[118:119]\n"
      "v_exp_f32 v140, v140\nv_exp_f32 v141, v141\nv_exp_f32 v142, v142\nv_exp_f32 v143, v143\nv_exp_f32 v144, v144\nv_exp_f32 v145, v145\nv_exp_f32 v146, v146\nv_exp_f32 v147, v147\n"
      "v_pk_add_f32 v[140:141], v[140:141], 1.0 op_sel_hi:[1,0]\nv_pk_add_f32 v[142:143], v[142:143], 1.0 op_sel_hi:[1,0]\nv_pk_add_f32 v[144:145], v[144:145], 1.0 op_sel_hi:[1,0]\n"
      "v_pk_add_f32 v[146:147], v[146:147], 1.0 op_sel_hi:[1,0]\nv_rcp_f32 v140, v140\nv_rcp_f32 v141, v141\nv_rcp_f32 v142, v142\nv_rcp_f32 v143, v143\nv_rcp_f32 v144, v144\nv_rcp_f32 v145, v145\n"
      "v_rcp_f32 v146, v146\nv_rcp_f32 v147, v147\nv_pk_mul_f32 v[148:149], v[80:81], v[140:141]\nv_pk_mul_f32 v[132:133], v[88:89], v[88:89]\nv_pk_mul_f32 v[150:151], v[82:83], v[142:143]\n"
      "v_pk_mul_f32 v[134:135], v[90:91], v[90:91]\nv_pk_fma_f32 v[148:149], v[84:85], v[144:145], v[148:149]\nv_pk_mul_f32 v[136:137], v[92:93], v[92:93]\n"
      "v_pk_fma_f32 v[150:151], v[86:87], v[146:147], v[150:151]\nv_pk_mul_f32 v[138:139], v[94:95], v[94:95]\nv_pk_fma_f32 v[132:133], v[132:133], s[46:47], v[128:129] op_sel_hi:[1,0,0]\n"
      "v_pk_fma_f32 v[134:135], v[134:135], s[46:47], v[128:129] op_sel_hi:[1,0,0]\nv_pk_fma_f32 v[136:137], v[136:137], s[46:47], v[128:129] op_sel_hi:[1,0,0]\n"
      "v_pk_fma_f32 v[138:139], v[138:139], s[46:47], v[128:129] op_sel_hi:[1,0,0]\nv_pk_mul_f32 v[132:133], v[132:133], v[88:89]\nv_pk_mul_f32 v[134:135], v[134:135], v[90:91]\n"
      "v_pk_mul_f32 v[136:137], v[136:137], v[92:93]\nv_pk_mul_f32 v[138:139], v[138:139], v[94:95]\nv_pk_mul_f32 v[88:89], v[88:89], v[120:121]\nv_pk_mul_f32 v[90:91], v[90:91], v[122:123]\n"
      "v_pk_mul_f32 v[92:93], v[92:93], v[124:125]\nv_pk_mul_f32 v[94:95], v[94:95], v[126:127]\nv_exp_f32 v132, v132\nv_exp_f32 v133, v133\nv_exp_f32 v134, v134\nv_exp_f32 v135, v135\n"
      "v_exp_f32 v136, v136\nv_exp_f32 v137, v137\nv_exp_f32 v138, v138\nv_exp_f32 v139, v139\nv_pk_add_f32 v[132:133], v[132:133], 1.0 op_sel_hi:[1,0]\n"
      "v_pk_add_f32 v[134:135], v[134:135], 1.0 op_sel_hi:[1,0]\nv_pk_add_f32 v[136:137], v[136:137], 1.0 op_sel_hi:[1,0]\nv_pk_add_f32 v[138:139], v[138:139], 1.0 op_sel_hi:[1,0]\n"
      "v_rcp_f32 v132, v132\nv_rcp_f32 v133, v133\nv_rcp_f32 v134, v134\nv_rcp_f32 v135, v135\nv_rcp_f32 v136, v136\nv_rcp_f32 v137, v137\nv_rcp_f32 v138, v138\nv_rcp_f32 v139, v139\n"
      "v_pk_fma_f32 v[148:149], v[88:89], v[132:133], v[148:149]\nv_pk_fma_f32 v[150:151], v[90:91], v[134:135], v[150:151]\nv_pk_fma_f32 v[148:149], v[92:93], v[136:137], v[148:149]\n"
      "v_pk_fma_f32 v[150:151], v[94:95], v[138:139], v[150:151]\nv_pk_add_f32 v[148:149], v[148:149], v[150:151]\nv_add_f32 v165, v148, v149\ns_branch .Lge\n"
      ".Lg14:\ns_barrier\nds_read_b128 v[176:179], v214 offset:0\nds_read_b128 v[180:183], v215 offset:0\nds_read_b64_tr_b16 v[112:113], v220 offset:0\n"
      "ds_read_b64_tr_b16 v[114:115], v220 offset:1024\nds_read_b64_tr_b16 v[116:117], v220 offset:8192\nds_read_b64_tr_b16 v[118:119], v220 offset:9216\n"
      "ds_read_b64_tr_b16 v[120:121], v221 offset:0\nds_read_b64_tr_b16 v[122:123], v221 offset:1024\nds_read_b64_tr_b16 v[124:125], v221 offset:8192\n"
      "ds_read_b64_tr_b16 v[126:127], v221 offset:9216\nds_read_b64_tr_b16 v[128:129], v222 offset:0\nds_read_b64_tr_b16 v[130:131], v222 offset:1024\n"
      "ds_read_b64_tr_b16 v[132:133], v222 offset:8192\nds_read_b64_tr_b16 v[134:135], v222 offset:9216\nds_read_b64_tr_b16 v[136:137], v223 offset:0\n"
      "ds_read_b64_tr_b16 v[138:139], v223 offset:1024\nds_read_b64_tr_b16 v[140:141], v223 offset:8192\nds_read_b64_tr_b16 v[142:143], v223 offset:9216\nds_read_b128 v[184:187], v210 offset:0\n"
      "ds_read_b128 v[188:191], v211 offset:0\ns_waitcnt lgkmcnt(0)\nds_read_b128 v[192:195], v210 offset:2048\nds_read_b128 v[196:199], v211 offset:2048\n.p2align 3\n"
      ".Lgl14:\nv_mfma_f32_16x16x32_bf16 v[96:99], v[112:115], v[176:179], v[96:99]\nds_read_b128 v[200:203], v210 offset:4096\nv_mfma_f32_16x16x32_bf16 v[96:99], v[116:119], v[180:183], v[96:99]\n"
      "ds_read_b128 v[204:207], v211 offset:4096\nv_mfma_f32_16x16x32_bf16 v[100:103], v[124:127], v[180:183], v[100:103]\nv_mfma_f32_16x16x32_bf16 v[100:103], v[120:123], v[176:179], v[100:103]\n"
      "v_mfma_f32_16x16x32_bf16 v[104:107], v[128:131], v[176:179], v[104:107]\nv_mfma_f32_16x16x32_bf16 v[104:107], v[132:135], v[180:183], v[104:107]\n"
      "v_mfma_f32_16x16x32_bf16 v[108:111], v[140:143], v[180:183], v[108:111]\nv_mfma_f32_16x16x32_bf16 v[108:111], v[136:139], v[176:179], v[108:111]\n"
      "v_mfma_f32_16x16x32_bf16 v[12:15], v[136:139], v[184:187], v[12:15]\nds_read_b128 v[176:179], v210 offset:6144\nv_mfma_f32_16x16x32_bf16 v[12:15], v[140:143], v[188:191], v[12:15]\n"
      "ds_read_b128 v[180:183], v211 offset:6144\nv_mfma_f32_16x16x32_bf16 v[8:11], v[132:135], v[188:191], v[8:11]\nv_mfma_f32_16x16x32_bf16 v[8:11], v[128:131], v[184:187], v[8:11]\n"
      "v_mfma_f32_16x16x32_bf16 v[4:7], v[120:123], v[184:187], v[4:7]\nv_mfma_f32_16x16x32_bf16 v[4:7], v[124:127], v[188:191], v[4:7]\n"
      "v_mfma_f32_16x16x32_bf16 v[0:3], v[116:119], v[188:191], v[0:3]\ns_waitcnt lgkmcnt(4)\nv_mfma_f32_16x16x32_bf16 v[0:3], v[112:115], v[184:187], v[0:3]\n"
      "v_mfma_f32_16x16x32_bf16 v[16:19], v[112:115], v[192:195], v[16:19]\nds_read_b128 v[184:187], v210 offset:8192\nv_mfma_f32_16x16x32_bf16 v[16:19], v[116:119], v[196:199], v[16:19]\n"
      "ds_read_b128 v[188:191], v211 offset:8192\nv_mfma_f32_16x16x32_bf16 v[20:23], v[124:127], v[196:199], v[20:23]\nv_mfma_f32_16x16x32_bf16 v[20:23], v[120:123], v[192:195], v[20:23]\n"
      "v_mfma_f32_16x16x32_bf16 v[24:27], v[128:131], v[192:195], v[24:27]\nv_mfma_f32_16x16x32_bf16 v[24:27], v[132:135], v[196:199], v[24:27]\n"
      "v_mfma_f32_16x16x32_bf16 v[28:31], v[140:143], v[196:199], v[28:31]\ns_waitcnt lgkmcnt(2)\nv_mfma_f32_16x16x32_bf16 v[28:31], v[136:139], v[192:195], v[28:31]\n"
      "v_mfma_f32_16x16x32_bf16 v[44:47], v[136:139], v[200:203], v[44:47]\nds_read_b128 v[192:195], v210 offset:10240\nv_mfma_f32_16x16x32_bf16 v[44:47], v[140:143], v[204:207], v[44:47]\n"
      "ds_read_b128 v[196:199], v211 offset:10240\nv_mfma_f32_16x16x32_bf16 v[40:43], v[132:135], v[204:207], v[40:43]\nv_mfma_f32_16x16x32_bf16 v[40:43], v[128:131], v[200:203], v[40:43]\n"
      "v_mfma_f32_16x16x32_bf16 v[36:39], v[120:123], v[200:203], v[36:39]\nv_mfma_f32_16x16x32_bf16 v[36:39], v[124:127], v[204:207], v[36:39]\n"
      "v_mfma_f32_16x16x32_bf16 v[32:35], v[116:119], v[204:207], v[32:35]\ns_waitcnt vmcnt(0) lgkmcnt(0)\ns_barrier\nv_mfma_f32_16x16x32_bf16 v[32:35], v[112:115], v[200:203], v[32:35]\n"
      "ds_read_b64_tr_b16 v[144:145], v216 offset:0\nv_mfma_f32_16x16x32_bf16 v[48:51], v[112:115], v[176:179], v[48:51]\nds_read_b64_tr_b16 v[146:147], v216 offset:1024\n"
      "v_mfma_f32_16x16x32_bf16 v[48:51], v[116:119], v[180:183], v[48:51]\nds_read_b64_tr_b16 v[148:149], v216 offset:8192\nv_mfma_f32_16x16x32_bf16 v[52:55], v[124:127], v[180:183], v[52:55]\n"
      "ds_read_b64_tr_b16 v[150:151], v216 offset:9216\nv_mfma_f32_16x16x32_bf16 v[52:55], v[120:123], v[176:179], v[52:55]\nds_read_b64_tr_b16 v[152:153], v217 offset:0\n"
      "v_mfma_f32_16x16x32_bf16 v[56:59], v[128:131], v[176:179], v[56:59]\nds_read_b64_tr_b16 v[154:155], v217 offset:1024\nv_mfma_f32_16x16x32_bf16 v[56:59], v[132:135], v[180:183], v[56:59]\n"
      "ds_read_b64_tr_b16 v[156:157], v217 offset:8192\nv_mfma_f32_16x16x32_bf16 v[60:63], v[140:143], v[180:183], v[60:63]\nds_read_b64_tr_b16 v[158:159], v217 offset:9216\n"
      "v_mfma_f32_16x16x32_bf16 v[60:63], v[136:139], v[176:179], v[60:63]\nds_read_b64_tr_b16 v[160:161], v218 offset:0\nv_mfma_f32_16x16x32_bf16 v[76:79], v[136:139], v[184:187], v[76:79]\n"
      "ds_read_b128 v[176:179], v212 offset:0\nv_mfma_f32_16x16x32_bf16 v[76:79], v[140:143], v[188:191], v[76:79]\nds_read_b128 v[180:183], v213 offset:0\n"
      "v_mfma_f32_16x16x32_bf16 v[72:75], v[132:135], v[188:191], v[72:75]\nds_read_b64_tr_b16 v[162:163], v218 offset:1024\nv_mfma_f32_16x16x32_bf16 v[72:75], v[128:131], v[184:187], v[72:75]\n"
      "ds_read_b64_tr_b16 v[164:165], v218 offset:8192\nv_mfma_f32_16x16x32_bf16 v[68:71], v[120:123], v[184:187], v[68:71]\nds_read_b64_tr_b16 v[166:167], v218 offset:9216\n"
      "v_mfma_f32_16x16x32_bf16 v[68:71], v[124:127], v[188:191], v[68:71]\nds_read_b64_tr_b16 v[168:169], v219 offset:0\nv_mfma_f32_16x16x32_bf16 v[64:67], v[116:119], v[188:191], v[64:67]\n"
      "ds_read_b64_tr_b16 v[170:171], v219 offset:1024\nv_mfma_f32_16x16x32_bf16 v[64:67], v[112:115], v[184:187], v[64:67]\nds_read_b64_tr_b16 v[172:173], v219 offset:8192\n"
      "v_mfma_f32_16x16x32_bf16 v[80:83], v[112:115], v[192:195], v[80:83]\nds_read_b128 v[184:187], v208 offset:0\nv_mfma_f32_16x16x32_bf16 v[80:83], v[116:119], v[196:199], v[80:83]\n"
      "ds_read_b128 v[188:191], v209 offset:0\nv_mfma_f32_16x16x32_bf16 v[84:87], v[124:127], v[196:199], v[84:87]\nds_read_b64_tr_b16 v[174:175], v219 offset:9216\n"
      "v_mfma_f32_16x16x32_bf16 v[84:87], v[120:123], v[192:195], v[84:87]\nv_mfma_f32_16x16x32_bf16 v[88:91], v[128:131], v[192:195], v[88:91]\n"
      "v_mfma_f32_16x16x32_bf16 v[88:91], v[132:135], v[196:199], v[88:91]\nv_mfma_f32_16x16x32_bf16 v[92:95], v[140:143], v[196:199], v[92:95]\ns_waitcnt lgkmcnt(0)\n"
      "v_mfma_f32_16x16x32_bf16 v[92:95], v[136:139], v[192:195], v[92:95]\nds_read_b128 v[192:195], v208 offset:2048\nds_read_b128 v[196:199], v209 offset:2048\n"
      "v_mfma_f32_16x16x32_bf16 v[96:99], v[144:147], v[176:179], v[96:99]\nds_read_b128 v[200:203], v208 offset:4096\nv_mfma_f32_16x16x32_bf16 v[96:99], v[148:151], v[180:183], v[96:99]\n"
      "ds_read_b128 v[204:207], v209 offset:4096\nv_mfma_f32_16x16x32_bf16 v[100:103], v[156:159], v[180:183], v[100:103]\nv_mfma_f32_16x16x32_bf16 v[100:103], v[152:155], v[176:179], v[100:103]\n"
      "v_mfma_f32_16x16x32_bf16 v[104:107], v[160:163], v[176:179], v[104:107]\nv_mfma_f32_16x16x32_bf16 v[104:107], v[164:167], v[180:183], v[104:107]\n"
      "v_mfma_f32_16x16x32_bf16 v[108:111], v[172:175], v[180:183], v[108:111]\nv_mfma_f32_16x16x32_bf16 v[108:111], v[168:171], v[176:179], v[108:111]\n"
      "v_mfma_f32_16x16x32_bf16 v[12:15], v[168:171], v[184:187], v[12:15]\nds_read_b128 v[176:179], v208 offset:6144\nv_mfma_f32_16x16x32_bf16 v[12:15], v[172:175], v[188:191], v[12:15]\n"
      "ds_read_b128 v[180:183], v209 offset:6144\nv_mfma_f32_16x16x32_bf16 v[8:11], v[164:167], v[188:191], v[8:11]\nv_mfma_f32_16x16x32_bf16 v[8:11], v[160:163], v[184:187], v[8:11]\n"
      "v_mfma_f32_16x16x32_bf16 v[4:7], v[152:155], v[184:187], v[4:7]\nv_mfma_f32_16x16x32_bf16 v[4:7], v[156:159], v[188:191], v[4:7]\n"
      "v_mfma_f32_16x16x32_bf16 v[0:3], v[148:151], v[188:191], v[0:3]\ns_waitcnt lgkmcnt(4)\nv_mfma_f32_16x16x32_bf16 v[0:3], v[144:147], v[184:187], v[0:3]\n"
      "v_mfma_f32_16x16x32_bf16 v[16:19], v[144:147], v[192:195], v[16:19]\nds_read_b128 v[184:187], v208 offset:8192\nv_mfma_f32_16x16x32_bf16 v[16:19], v[148:151], v[196:199], v[16:19]\n"
      "ds_read_b128 v[188:191], v209 offset:8192\nv_mfma_f32_16x16x32_bf16 v[20:23], v[156:159], v[196:199], v[20:23]\nv_mfma_f32_16x16x32_bf16 v[20:23], v[152:155], v[192:195], v[20:23]\n"
      "v_mfma_f32_16x16x32_bf16 v[24:27], v[160:163], v[192:195], v[24:27]\nv_mfma_f32_16x16x32_bf16 v[24:27], v[164:167], v[196:199], v[24:27]\n"
      "v_mfma_f32_16x16x32_bf16 v[28:31], v[172:175], v[196:199], v[28:31]\ns_waitcnt lgkmcnt(2)\nv_mfma_f32_16x16x32_bf16 v[28:31], v[168:171], v[192:195], v[28:31]\n"
      "v_mfma_f32_16x16x32_bf16 v[44:47], v[168:171], v[200:203], v[44:47]\nds_read_b128 v[192:195], v208 offset:10240\nv_mfma_f32_16x16x32_bf16 v[44:47], v[172:175], v[204:207], v[44:47]\n"
      "ds_read_b128 v[196:199], v209 offset:10240\nv_mfma_f32_16x16x32_bf16 v[40:43], v[164:167], v[204:207], v[40:43]\nv_mfma_f32_16x16x32_bf16 v[40:43], v[160:163], v[200:203], v[40:43]\n"
      "v_mfma_f32_16x16x32_bf16 v[36:39], v[152:155], v[200:203], v[36:39]\nv_mfma_f32_16x16x32_bf16 v[36:39], v[156:159], v[204:207], v[36:39]\n"
      "v_mfma_f32_16x16x32_bf16 v[32:35], v[148:151], v[204:207], v[32:35]\ns_waitcnt vmcnt(0) lgkmcnt(0)\ns_barrier\nv_mfma_f32_16x16x32_bf16 v[32:35], v[144:147], v[200:203], v[32:35]\n"
      "ds_read_b64_tr_b16 v[112:113], v216 offset:45056\nv_mfma_f32_16x16x32_bf16 v[48:51], v[144:147], v[176:179], v[48:51]\nds_read_b64_tr_b16 v[114:115], v216 offset:46080\n"
      "v_mfma_f32_16x16x32_bf16 v[48:51], v[148:151], v[180:183], v[48:51]\nds_read_b64_tr_b16 v[116:117], v216 offset:53248\nv_mfma_f32_16x16x32_bf16 v[52:55], v[156:159], v[180:183], v[52:55]\n"
      "ds_read_b64_tr_b16 v[118:119], v216 offset:54272\nv_mfma_f32_16x16x32_bf16 v[52:55], v[152:155], v[176:179], v[52:55]\nds_read_b64_tr_b16 v[120:121], v217 offset:45056\n"
      "v_mfma_f32_16x16x32_bf16 v[56:59], v[160:163], v[176:179], v[56:59]\nds_read_b64_tr_b16 v[122:123], v217 offset:46080\nv_mfma_f32_16x16x32_bf16 v[56:59], v[164:167], v[180:183], v[56:59]\n"
      "ds_read_b64_tr_b16 v[124:125], v217 offset:53248\nv_mfma_f32_16x16x32_bf16 v[60:63], v[172:175], v[180:183], v[60:63]\nds_read_b64_tr_b16 v[126:127], v217 offset:54272\n"
      "v_mfma_f32_16x16x32_bf16 v[60:63], v[168:171], v[176:179], v[60:63]\nds_read_b64_tr_b16 v[128:129], v218 offset:45056\nv_mfma_f32_16x16x32_bf16 v[76:79], v[168:171], v[184:187], v[76:79]\n"
      "ds_read_b128 v[176:179], v212 offset:45056\nv_mfma_f32_16x16x32_bf16 v[76:79], v[172:175], v[188:191], v[76:79]\nds_read_b128 v[180:183], v213 offset:45056\n"
      "v_mfma_f32_16x16x32_bf16 v[72:75], v[164:167], v[188:191], v[72:75]\nds_read_b64_tr_b16 v[130:131], v218 offset:46080\nv_mfma_f32_16x16x32_bf16 v[72:75], v[160:163], v[184:187], v[72:75]\n"
      "ds_read_b64_tr_b16 v[132:133], v218 offset:53248\nv_mfma_f32_16x16x32_bf16 v[68:71], v[152:155], v[184:187], v[68:71]\nds_read_b64_tr_b16 v[134:135], v218 offset:54272\n"
      "v_mfma_f32_16x16x32_bf16 v[68:71], v[156:159], v[188:191], v[68:71]\nds_read_b64_tr_b16 v[136:137], v219 offset:45056\nv_mfma_f32_16x16x32_bf16 v[64:67], v[148:151], v[188:191], v[64:67]\n"
      "ds_read_b64_tr_b16 v[138:139], v219 offset:46080\nv_mfma_f32_16x16x32_bf16 v[64:67], v[144:147], v[184:187], v[64:67]\nds_read_b64_tr_b16 v[140:141], v219 offset:53248\n"
      "v_mfma_f32_16x16x32_bf16 v[80:83], v[144:147], v[192:195], v[80:83]\nds_read_b128 v[184:187], v208 offset:45056\nv_mfma_f32_16x16x32_bf16 v[80:83], v[148:151], v[196:199], v[80:83]\n"
      "ds_read_b128 v[188:191], v209 offset:45056\nv_mfma_f32_16x16x32_bf16 v[84:87], v[156:159], v[196:199], v[84:87]\nds_read_b64_tr_b16 v[142:143], v219 offset:54272\n"
      "v_mfma_f32_16x16x32_bf16 v[84:87], v[152:155], v[192:195], v[84:87]\nv_mfma_f32_16x16x32_bf16 v[88:91], v[160:163], v[192:195], v[88:91]\n"
      "v_mfma_f32_16x16x32_bf16 v[88:91], v[164:167], v[196:199], v[88:91]\nv_mfma_f32_16x16x32_bf16 v[92:95], v[172:175], v[196:199], v[92:95]\ns_waitcnt lgkmcnt(0)\n"
      "v_mfma_f32_16x16x32_bf16 v[92:95], v[168:171], v[192:195], v[92:95]\nds_read_b128 v[192:195], v208 offset:47104\nds_read_b128 v[196:199], v209 offset:47104\n"
      "v_mfma_f32_16x16x32_bf16 v[96:99], v[112:115], v[176:179], v[96:99]\nds_read_b128 v[200:203], v208 offset:49152\nv_mfma_f32_16x16x32_bf16 v[96:99], v[116:119], v[180:183], v[96:99]\n"
      "ds_read_b128 v[204:207], v209 offset:49152\nv_mfma_f32_16x16x32_bf16 v[100:103], v[124:127], v[180:183], v[100:103]\nv_mfma_f32_16x16x32_bf16 v[100:103], v[120:123], v[176:179], v[100:103]\n"
      "v_mfma_f32_16x16x32_bf16 v[104:107], v[128:131], v[176:179], v[104:107]\nv_mfma_f32_16x16x32_bf16 v[104:107], v[132:135], v[180:183], v[104:107]\n"
      "v_mfma_f32_16x16x32_bf16 v[108:111], v[140:143], v[180:183], v[108:111]\nv_mfma_f32_16x16x32_bf16 v[108:111], v[136:139], v[176:179], v[108:111]\n"
      "v_mfma_f32_16x16x32_bf16 v[12:15], v[136:139], v[184:187], v[12:15]\nds_read_b128 v[176:179], v208 offset:51200\nv_mfma_f32_16x16x32_bf16 v[12:15], v[140:143], v[188:191], v[12:15]\n"
      "ds_read_b128 v[180:183], v209 offset:51200\nv_mfma_f32_16x16x32_bf16 v[8:11], v[132:135], v[188:191], v[8:11]\nv_mfma_f32_16x16x32_bf16 v[8:11], v[128:131], v[184:187], v[8:11]\n"
      "v_mfma_f32_16x16x32_bf16 v[4:7], v[120:123], v[184:187], v[4:7]\nv_mfma_f32_16x16x32_bf16 v[4:7], v[124:127], v[188:191], v[4:7]\n"
      "v_mfma_f32_16x16x32_bf16 v[0:3], v[116:119], v[188:191], v[0:3]\ns_waitcnt lgkmcnt(4)\nv_mfma_f32_16x16x32_bf16 v[0:3], v[112:115], v[184:187], v[0:3]\n"
      "v_mfma_f32_16x16x32_bf16 v[16:19], v[112:115], v[192:195], v[16:19]\nds_read_b128 v[184:187], v208 offset:53248\nv_mfma_f32_16x16x32_bf16 v[16:19], v[116:119], v[196:199], v[16:19]\n"
      "ds_read_b128 v[188:191], v209 offset:53248\nv_mfma_f32_16x16x32_bf16 v[20:23], v[124:127], v[196:199], v[20:23]\nv_mfma_f32_16x16x32_bf16 v[20:23], v[120:123], v[192:195], v[20:23]\n"
      "v_mfma_f32_16x16x32_bf16 v[24:27], v[128:131], v[192:195], v[24:27]\nv_mfma_f32_16x16x32_bf16 v[24:27], v[132:135], v[196:199], v[24:27]\n"
      "v_mfma_f32_16x16x32_bf16 v[28:31], v[140:143], v[196:199], v[28:31]\ns_waitcnt lgkmcnt(2)\nv_mfma_f32_16x16x32_bf16 v[28:31], v[136:139], v[192:195], v[28:31]\n"
      "v_mfma_f32_16x16x32_bf16 v[44:47], v[136:139], v[200:203], v[44:47]\nds_read_b128 v[192:195], v208 offset:55296\nv_mfma_f32_16x16x32_bf16 v[44:47], v[140:143], v[204:207], v[44:47]\n"
      "ds_read_b128 v[196:199], v209 offset:55296\nv_mfma_f32_16x16x32_bf16 v[40:43], v[132:135], v[204:207], v[40:43]\nv_mfma_f32_16x16x32_bf16 v[40:43], v[128:131], v[200:203], v[40:43]\n"
      "v_mfma_f32_16x16x32_bf16 v[36:39], v[120:123], v[200:203], v[36:39]\nv_mfma_f32_16x16x32_bf16 v[36:39], v[124:127], v[204:207], v[36:39]\n"
      "v_mfma_f32_16x16x32_bf16 v[32:35], v[116:119], v[204:207], v[32:35]\ns_waitcnt vmcnt(0) lgkmcnt(0)\ns_barrier\nv_mfma_f32_16x16x32_bf16 v[32:35], v[112:115], v[200:203], v[32:35]\n"
      "ds_read_b64_tr_b16 v[144:145], v220 offset:0\nv_mfma_f32_16x16x32_bf16 v[48:51], v[112:115], v[176:179], v[48:51]\nds_read_b64_tr_b16 v[146:147], v220 offset:1024\n"
      "v_mfma_f32_16x16x32_bf16 v[48:51], v[116:119], v[180:183], v[48:51]\nds_read_b64_tr_b16 v[148:149], v220 offset:8192\nv_mfma_f32_16x16x32_bf16 v[52:55], v[124:127], v[180:183], v[52:55]\n"
      "ds_read_b64_tr_b16 v[150:151], v220 offset:9216\nv_mfma_f32_16x16x32_bf16 v[52:55], v[120:123], v[176:179], v[52:55]\nds_read_b64_tr_b16 v[152:153], v221 offset:0\n"
      "v_mfma_f32_16x16x32_bf16 v[56:59], v[128:131], v[176:179], v[56:59]\nds_read_b64_tr_b16 v[154:155], v221 offset:1024\nv_mfma_f32_16x16x32_bf16 v[56:59], v[132:135], v[180:183], v[56:59]\n"
      "ds_read_b64_tr_b16 v[156:157], v221 offset:8192\nv_mfma_f32_16x16x32_bf16 v[60:63], v[140:143], v[180:183], v[60:63]\nds_read_b64_tr_b16 v[158:159], v221 offset:9216\n"
      "v_mfma_f32_16x16x32_bf16 v[60:63], v[136:139], v[176:179], v[60:63]\nds_read_b64_tr_b16 v[160:161], v222 offset:0\nv_mfma_f32_16x16x32_bf16 v[76:79], v[136:139], v[184:187], v[76:79]\n"
      "ds_read_b128 v[176:179], v214 offset:0\nv_mfma_f32_16x16x32_bf16 v[76:79], v[140:143], v[188:191], v[76:79]\nds_read_b128 v[180:183], v215 offset:0\n"
      "v_mfma_f32_16x16x32_bf16 v[72:75], v[132:135], v[188:191], v[72:75]\nds_read_b64_tr_b16 v[162:163], v222 offset:1024\nv_mfma_f32_16x16x32_bf16 v[72:75], v[128:131], v[184:187], v[72:75]\n"
      "ds_read_b64_tr_b16 v[164:165], v222 offset:8192\nv_mfma_f32_16x16x32_bf16 v[68:71], v[120:123], v[184:187], v[68:71]\nds_read_b64_tr_b16 v[166:167], v222 offset:9216\n"
      "v_mfma_f32_16x16x32_bf16 v[68:71], v[124:127], v[188:191], v[68:71]\nds_read_b64_tr_b16 v[168:169], v223 offset:0\nv_mfma_f32_16x16x32_bf16 v[64:67], v[116:119], v[188:191], v[64:67]\n"
      "ds_read_b64_tr_b16 v[170:171], v223 offset:1024\nv_mfma_f32_16x16x32_bf16 v[64:67], v[112:115], v[184:187], v[64:67]\nds_read_b64_tr_b16 v[172:173], v223 offset:8192\n"
      "v_mfma_f32_16x16x32_bf16 v[80:83], v[112:115], v[192:195], v[80:83]\nds_read_b128 v[184:187], v210 offset:0\nv_mfma_f32_16x16x32_bf16 v[80:83], v[116:119], v[196:199], v[80:83]\n"
      "ds_read_b128 v[188:191], v211 offset:0\nv_mfma_f32_16x16x32_bf16 v[84:87], v[124:127], v[196:199], v[84:87]\nds_read_b64_tr_b16 v[174:175], v223 offset:9216\n"
      "v_mfma_f32_16x16x32_bf16 v[84:87], v[120:123], v[192:195], v[84:87]\nv_mfma_f32_16x16x32_bf16 v[88:91], v[128:131], v[192:195], v[88:91]\n"
      "v_mfma_f32_16x16x32_bf16 v[88:91], v[132:135], v[196:199], v[88:91]\nv_mfma_f32_16x16x32_bf16 v[92:95], v[140:143], v[196:199], v[92:95]\ns_waitcnt lgkmcnt(0)\n"
      "v_mfma_f32_16x16x32_bf16 v[92:95], v[136:139], v[192:195], v[92:95]\nds_read_b128 v[192:195], v210 offset:2048\nds_read_b128 v[196:199], v211 offset:2048\n"
      "v_mfma_f32_16x16x32_bf16 v[96:99], v[144:147], v[176:179], v[96:99]\nds_read_b128 v[200:203], v210 offset:4096\nv_mfma_f32_16x16x32_bf16 v[96:99], v[148:151], v[180:183], v[96:99]\n"
      "ds_read_b128 v[204:207], v211 offset:4096\nv_mfma_f32_16x16x32_bf16 v[100:103], v[156:159], v[180:183], v[100:103]\nv_mfma_f32_16x16x32_bf16 v[100:103], v[152:155], v[176:179], v[100:103]\n"
      "v_mfma_f32_16x16x32_bf16 v[104:107], v[160:163], v[176:179], v[104:107]\nv_mfma_f32_16x16x32_bf16 v[104:107], v[164:167], v[180:183], v[104:107]\n"
      "v_mfma_f32_16x16x32_bf16 v[108:111], v[172:175], v[180:183], v[108:111]\nv_mfma_f32_16x16x32_bf16 v[108:111], v[168:171], v[176:179], v[108:111]\n"
      "v_mfma_f32_16x16x32_bf16 v[12:15], v[168:171], v[184:187], v[12:15]\nds_read_b128 v[176:179], v210 offset:6144\nv_mfma_f32_16x16x32_bf16 v[12:15], v[172:175], v[188:191], v[12:15]\n"
      "ds_read_b128 v[180:183], v211 offset:6144\nv_mfma_f32_16x16x32_bf16 v[8:11], v[164:167], v[188:191], v[8:11]\nv_mfma_f32_16x16x32_bf16 v[8:11], v[160:163], v[184:187], v[8:11]\n"
      "v_mfma_f32_16x16x32_bf16 v[4:7], v[152:155], v[184:187], v[4:7]\nv_mfma_f32_16x16x32_bf16 v[4:7], v[156:159], v[188:191], v[4:7]\n"
      "v_mfma_f32_16x16x32_bf16 v[0:3], v[148:151], v[188:191], v[0:3]\ns_waitcnt lgkmcnt(4)\nv_mfma_f32_16x16x32_bf16 v[0:3], v[144:147], v[184:187], v[0:3]\n"
      "v_mfma_f32_16x16x32_bf16 v[16:19], v[144:147], v[192:195], v[16:19]\nds_read_b128 v[184:187], v210 offset:8192\nv_mfma_f32_16x16x32_bf16 v[16:19], v[148:151], v[196:199], v[16:19]\n"
      "ds_read_b128 v[188:191], v211 offset:8192\nv_mfma_f32_16x16x32_bf16 v[20:23], v[156:159], v[196:199], v[20:23]\nv_mfma_f32_16x16x32_bf16 v[20:23], v[152:155], v[192:195], v[20:23]\n"
      "v_mfma_f32_16x16x32_bf16 v[24:27], v[160:163], v[192:195], v[24:27]\nv_mfma_f32_16x16x32_bf16 v[24:27], v[164:167], v[196:199], v[24:27]\n"
      "v_mfma_f32_16x16x32_bf16 v[28:31], v[172:175], v[196:199], v[28:31]\ns_waitcnt lgkmcnt(2)\nv_mfma_f32_16x16x32_bf16 v[28:31], v[168:171], v[192:195], v[28:31]\n"
      "v_mfma_f32_16x16x32_bf16 v[44:47], v[168:171], v[200:203], v[44:47]\nds_read_b128 v[192:195], v210 offset:10240\nv_mfma_f32_16x16x32_bf16 v[44:47], v[172:175], v[204:207], v[44:47]\n"
      "ds_read_b128 v[196:199], v211 offset:10240\nv_mfma_f32_16x16x32_bf16 v[40:43], v[164:167], v[204:207], v[40:43]\nv_mfma_f32_16x16x32_bf16 v[40:43], v[160:163], v[200:203], v[40:43]\n"
      "v_mfma_f32_16x16x32_bf16 v[36:39], v[152:155], v[200:203], v[36:39]\nv_mfma_f32_16x16x32_bf16 v[36:39], v[156:159], v[204:207], v[36:39]\n"
      "v_mfma_f32_16x16x32_bf16 v[32:35], v[148:151], v[204:207], v[32:35]\ns_waitcnt vmcnt(0) lgkmcnt(0)\ns_barrier\nv_mfma_f32_16x16x32_bf16 v[32:35], v[144:147], v[200:203], v[32:35]\n"
      "ds_read_b64_tr_b16 v[112:113], v216 offset:0\nv_mfma_f32_16x16x32_bf16 v[48:51], v[144:147], v[176:179], v[48:51]\nds_read_b64_tr_b16 v[114:115], v216 offset:1024\n"
      "v_mfma_f32_16x16x32_bf16 v[48:51], v[148:151], v[180:183], v[48:51]\nds_read_b64_tr_b16 v[116:117], v216 offset:8192\nv_mfma_f32_16x16x32_bf16 v[52:55], v[156:159], v[180:183], v[52:55]\n"
      "ds_read_b64_tr_b16 v[118:119], v216 offset:9216\nv_mfma_f32_16x16x32_bf16 v[52:55], v[152:155], v[176:179], v[52:55]\nds_read_b64_tr_b16 v[120:121], v217 offset:0\n"
      "v_mfma_f32_16x16x32_bf16 v[56:59], v[160:163], v[176:179], v[56:59]\nds_read_b64_tr_b16 v[122:123], v217 offset:1024\nv_mfma_f32_16x16x32_bf16 v[56:59], v[164:167], v[180:183], v[56:59]\n"
      "ds_read_b64_tr_b16 v[124:125], v217 offset:8192\nv_mfma_f32_16x16x32_bf16 v[60:63], v[172:175], v[180:183], v[60:63]\nds_read_b64_tr_b16 v[126:127], v217 offset:9216\n"
      "v_mfma_f32_16x16x32_bf16 v[60:63], v[168:171], v[176:179], v[60:63]\nds_read_b64_tr_b16 v[128:129], v218 offset:0\nv_mfma_f32_16x16x32_bf16 v[76:79], v[168:171], v[184:187], v[76:79]\n"
      "ds_read_b128 v[176:179], v212 offset:0\nv_mfma_f32_16x16x32_bf16 v[76:79], v[172:175], v[188:191], v[76:79]\nds_read_b128 v[180:183], v213 offset:0\n"
      "v_mfma_f32_16x16x32_bf16 v[72:75], v[164:167], v[188:191], v[72:75]\nds_read_b64_tr_b16 v[130:131], v218 offset:1024\nv_mfma_f32_16x16x32_bf16 v[72:75], v[160:163], v[184:187], v[72:75]\n"
      "ds_read_b64_tr_b16 v[132:133], v218 offset:8192\nv_mfma_f32_16x16x32_bf16 v[68:71], v[152:155], v[184:187], v[68:71]\nds_read_b64_tr_b16 v[134:135], v218 offset:9216\n"
      "v_mfma_f32_16x16x32_bf16 v[68:71], v[156:159], v[188:191], v[68:71]\nds_read_b64_tr_b16 v[136:137], v219 offset:0\nv_mfma_f32_16x16x32_bf16 v[64:67], v[148:151], v[188:191], v[64:67]\n"
      "ds_read_b64_tr_b16 v[138:139], v219 offset:1024\nv_mfma_f32_16x16x32_bf16 v[64:67], v[144:147], v[184:187], v[64:67]\nds_read_b64_tr_b16 v[140:141], v219 offset:8192\n"
      "v_mfma_f32_16x16x32_bf16 v[80:83], v[144:147], v[192:195], v[80:83]\nds_read_b128 v[184:187], v208 offset:0\nv_mfma_f32_16x16x32_bf16 v[80:83], v[148:151], v[196:199], v[80:83]\n"
      "ds_read_b128 v[188:191], v209 offset:0\nv_mfma_f32_16x16x32_bf16 v[84:87], v[156:159], v[196:199], v[84:87]\nds_read_b64_tr_b16 v[142:143], v219 offset:9216\n"
      "v_mfma_f32_16x16x32_bf16 v[84:87], v[152:155], v[192:195], v[84:87]\nv_mfma_f32_16x16x32_bf16 v[88:91], v[160:163], v[192:195], v[88:91]\n"
      "v_mfma_f32_16x16x32_bf16 v[88:91], v[164:167], v[196:199], v[88:91]\nv_mfma_f32_16x16x32_bf16 v[92:95], v[172:175], v[196:199], v[92:95]\ns_waitcnt lgkmcnt(0)\n"
      "v_mfma_f32_16x16x32_bf16 v[92:95], v[168:171], v[192:195], v[92:95]\nds_read_b128 v[192:195], v208 offset:2048\nds_read_b128 v[196:199], v209 offset:2048\n"
      "v_mfma_f32_16x16x32_bf16 v[96:99], v[112:115], v[176:179], v[96:99]\nds_read_b128 v[200:203], v208 offset:4096\nv_mfma_f32_16x16x32_bf16 v[96:99], v[116:119], v[180:183], v[96:99]\n"
      "ds_read_b128 v[204:207], v209 offset:4096\nv_mfma_f32_16x16x32_bf16 v[100:103], v[124:127], v[180:183], v[100:103]\nv_mfma_f32_16x16x32_bf16 v[100:103], v[120:123], v[176:179], v[100:103]\n"
      "v_mfma_f32_16x16x32_bf16 v[104:107], v[128:131], v[176:179], v[104:107]\nv_mfma_f32_16x16x32_bf16 v[104:107], v[132:135], v[180:183], v[104:107]\n"
      "v_mfma_f32_16x16x32_bf16 v[108:111], v[140:143], v[180:183], v[108:111]\nv_mfma_f32_16x16x32_bf16 v[108:111], v[136:139], v[176:179], v[108:111]\n"
      "v_mfma_f32_16x16x32_bf16 v[12:15], v[136:139], v[184:187], v[12:15]\nds_read_b128 v[176:179], v208 offset:6144\nv_mfma_f32_16x16x32_bf16 v[12:15], v[140:143], v[188:191], v[12:15]\n"
      "ds_read_b128 v[180:183], v209 offset:6144\nv_mfma_f32_16x16x32_bf16 v[8:11], v[132:135], v[188:191], v[8:11]\nv_mfma_f32_16x16x32_bf16 v[8:11], v[128:131], v[184:187], v[8:11]\n"
      "v_mfma_f32_16x16x32_bf16 v[4:7], v[120:123], v[184:187], v[4:7]\nv_mfma_f32_16x16x32_bf16 v[4:7], v[124:127], v[188:191], v[4:7]\n"
      "v_mfma_f32_16x16x32_bf16 v[0:3], v[116:119], v[188:191], v[0:3]\ns_waitcnt lgkmcnt(4)\nv_mfma_f32_16x16x32_bf16 v[0:3], v[112:115], v[184:187], v[0:3]\n"
      "v_mfma_f32_16x16x32_bf16 v[16:19], v[112:115], v[192:195], v[16:19]\nds_read_b128 v[184:187], v208 offset:8192\nv_mfma_f32_16x16x32_bf16 v[16:19], v[116:119], v[196:199], v[16:19]\n"
      "ds_read_b128 v[188:191], v209 offset:8192\nv_mfma_f32_16x16x32_bf16 v[20:23], v[124:127], v[196:199], v[20:23]\nv_mfma_f32_16x16x32_bf16 v[20:23], v[120:123], v[192:195], v[20:23]\n"
      "v_mfma_f32_16x16x32_bf16 v[24:27], v[128:131], v[192:195], v[24:27]\nv_mfma_f32_16x16x32_bf16 v[24:27], v[132:135], v[196:199], v[24:27]\n"
      "v_mfma_f32_16x16x32_bf16 v[28:31], v[140:143], v[196:199], v[28:31]\ns_waitcnt lgkmcnt(2)\nv_mfma_f32_16x16x32_bf16 v[28:31], v[136:139], v[192:195], v[28:31]\n"
      "v_mfma_f32_16x16x32_bf16 v[44:47], v[136:139], v[200:203], v[44:47]\nds_read_b128 v[192:195], v208 offset:10240\nv_mfma_f32_16x16x32_bf16 v[44:47], v[140:143], v[204:207], v[44:47]\n"
      "ds_read_b128 v[196:199], v209 offset:10240\nv_mfma_f32_16x16x32_bf16 v[40:43], v[132:135], v[204:207], v[40:43]\nv_mfma_f32_16x16x32_bf16 v[40:43], v[128:131], v[200:203], v[40:43]\n"
      "v_mfma_f32_16x16x32_bf16 v[36:39], v[120:123], v[200:203], v[36:39]\nv_mfma_f32_16x16x32_bf16 v[36:39], v[124:127], v[204:207], v[36:39]\n"
      "v_mfma_f32_16x16x32_bf16 v[32:35], v[116:119], v[204:207], v[32:35]\ns_waitcnt vmcnt(0) lgkmcnt(0)\ns_barrier\nv_mfma_f32_16x16x32_bf16 v[32:35], v[112:115], v[200:203], v[32:35]\n"
      "ds_read_b64_tr_b16 v[144:145], v216 offset:45056\nv_mfma_f32_16x16x32_bf16 v[48:51], v[112:115], v[176:179], v[48:51]\nds_read_b64_tr_b16 v[146:147], v216 offset:46080\n"
      "v_mfma_f32_16x16x32_bf16 v[48:51], v[116:119], v[180:183], v[48:51]\nds_read_b64_tr_b16 v[148:149], v216 offset:53248\nv_mfma_f32_16x16x32_bf16 v[52:55], v[124:127], v[180:183], v[52:55]\n"
      "ds_read_b64_tr_b16 v[150:151], v216 offset:54272\nv_mfma_f32_16x16x32_bf16 v[52:55], v[120:123], v[176:179], v[52:55]\nds_read_b64_tr_b16 v[152:153], v217 offset:45056\n"
      "v_mfma_f32_16x16x32_bf16 v[56:59], v[128:131], v[176:179], v[56:59]\nds_read_b64_tr_b16 v[154:155], v217 offset:46080\nv_mfma_f32_16x16x32_bf16 v[56:59], v[132:135], v[180:183], v[56:59]\n"
      "ds_read_b64_tr_b16 v[156:157], v217 offset:53248\nv_mfma_f32_16x16x32_bf16 v[60:63], v[140:143], v[180:183], v[60:63]\nds_read_b64_tr_b16 v[158:159], v217 offset:54272\n"
      "v_mfma_f32_16x16x32_bf16 v[60:63], v[136:139], v[176:179], v[60:63]\nds_read_b64_tr_b16 v[160:161], v218 offset:45056\nv_mfma_f32_16x16x32_bf16 v[76:79], v[136:139], v[184:187], v[76:79]\n"
      "ds_read_b128 v[176:179], v212 offset:45056\nv_mfma_f32_16x16x32_bf16 v[76:79], v[140:143], v[188:191], v[76:79]\nds_read_b128 v[180:183], v213 offset:45056\n"
      "v_mfma_f32_16x16x32_bf16 v[72:75], v[132:135], v[188:191], v[72:75]\nds_read_b64_tr_b16 v[162:163], v218 offset:46080\nv_mfma_f32_16x16x32_bf16 v[72:75], v[128:131], v[184:187], v[72:75]\n"
      "ds_read_b64_tr_b16 v[164:165], v218 offset:53248\nv_mfma_f32_16x16x32_bf16 v[68:71], v[120:123], v[184:187], v[68:71]\nds_read_b64_tr_b16 v[166:167], v218 offset:54272\n"
      "v_mfma_f32_16x16x32_bf16 v[68:71], v[124:127], v[188:191], v[68:71]\nds_read_b64_tr_b16 v[168:169], v219 offset:45056\nv_mfma_f32_16x16x32_bf16 v[64:67], v[116:119], v[188:191], v[64:67]\n"
      "ds_read_b64_tr_b16 v[170:171], v219 offset:46080\nv_mfma_f32_16x16x32_bf16 v[64:67], v[112:115], v[184:187], v[64:67]\nds_read_b64_tr_b16 v[172:173], v219 offset:53248\n"
      "v_mfma_f32_16x16x32_bf16 v[80:83], v[112:115], v[192:195], v[80:83]\nds_read_b128 v[184:187], v208 offset:45056\nv_mfma_f32_16x16x32_bf16 v[80:83], v[116:119], v[196:199], v[80:83]\n"
      "ds_read_b128 v[188:191], v209 offset:45056\nv_mfma_f32_16x16x32_bf16 v[84:87], v[124:127], v[196:199], v[84:87]\nds_read_b64_tr_b16 v[174:175], v219 offset:54272\n"
      "v_mfma_f32_16x16x32_bf16 v[84:87], v[120:123], v[192:195], v[84:87]\nv_mfma_f32_16x16x32_bf16 v[88:91], v[128:131], v[192:195], v[88:91]\n"
      "v_mfma_f32_16x16x32_bf16 v[88:91], v[132:135], v[196:199], v[88:91]\nv_mfma_f32_16x16x32_bf16 v[92:95], v[140:143], v[196:199], v[92:95]\ns_waitcnt lgkmcnt(0)\n"
      "v_mfma_f32_16x16x32_bf16 v[92:95], v[136:139], v[192:195], v[92:95]\nds_read_b128 v[192:195], v208 offset:47104\nds_read_b128 v[196:199], v209 offset:47104\n"
      "v_mfma_f32_16x16x32_bf16 v[96:99], v[144:147], v[176:179], v[96:99]\nds_read_b128 v[200:203], v208 offset:49152\nv_mfma_f32_16x16x32_bf16 v[96:99], v[148:151], v[180:183], v[96:99]\n"
      "ds_read_b128 v[204:207], v209 offset:49152\nv_mfma_f32_16x16x32_bf16 v[100:103], v[156:159], v[180:183], v[100:103]\nv_mfma_f32_16x16x32_bf16 v[100:103], v[152:155], v[176:179], v[100:103]\n"
      "v_mfma_f32_16x16x32_bf16 v[104:107], v[160:163], v[176:179], v[104:107]\nv_mfma_f32_16x16x32_bf16 v[104:107], v[164:167], v[180:183], v[104:107]\n"
      "v_mfma_f32_16x16x32_bf16 v[108:111], v[172:175], v[180:183], v[108:111]\nv_mfma_f32_16x16x32_bf16 v[108:111], v[168:171], v[176:179], v[108:111]\n"
      "v_mfma_f32_16x16x32_bf16 v[12:15], v[168:171], v[184:187], v[12:15]\nds_read_b128 v[176:179], v208 offset:51200\nv_mfma_f32_16x16x32_bf16 v[12:15], v[172:175], v[188:191], v[12:15]\n"
      "ds_read_b128 v[180:183], v209 offset:51200\nv_mfma_f32_16x16x32_bf16 v[8:11], v[164:167], v[188:191], v[8:11]\nv_mfma_f32_16x16x32_bf16 v[8:11], v[160:163], v[184:187], v[8:11]\n"
      "v_mfma_f32_16x16x32_bf16 v[4:7], v[152:155], v[184:187], v[4:7]\nv_mfma_f32_16x16x32_bf16 v[4:7], v[156:159], v[188:191], v[4:7]\n"
      "v_mfma_f32_16x16x32_bf16 v[0:3], v[148:151], v[188:191], v[0:3]\ns_waitcnt lgkmcnt(4)\nv_mfma_f32_16x16x32_bf16 v[0:3], v[144:147], v[184:187], v[0:3]\n"
      "v_mfma_f32_16x16x32_bf16 v[16:19], v[144:147], v[192:195], v[16:19]\nds_read_b128 v[184:187], v208 offset:53248\nv_mfma_f32_16x16x32_bf16 v[16:19], v[148:151], v[196:199], v[16:19]\n"
      "ds_read_b128 v[188:191], v209 offset:53248\nv_mfma_f32_16x16x32_bf16 v[20:23], v[156:159], v[196:199], v[20:23]\nv_mfma_f32_16x16x32_bf16 v[20:23], v[152:155], v[192:195], v[20:23]\n"
      "v_mfma_f32_16x16x32_bf16 v[24:27], v[160:163], v[192:195], v[24:27]\nv_mfma_f32_16x16x32_bf16 v[24:27], v[164:167], v[196:199], v[24:27]\n"
      "v_mfma_f32_16x16x32_bf16 v[28:31], v[172:175], v[196:199], v[28:31]\ns_waitcnt lgkmcnt(2)\nv_mfma_f32_16x16x32_bf16 v[28:31], v[168:171], v[192:195], v[28:31]\n"
      "v_mfma_f32_16x16x32_bf16 v[44:47], v[168:171], v[200:203], v[44:47]\nds_read_b128 v[192:195], v208 offset:55296\nv_mfma_f32_16x16x32_bf16 v[44:47], v[172:175], v[204:207], v[44:47]\n"
      "ds_read_b128 v[196:199], v209 offset:55296\nv_mfma_f32_16x16x32_bf16 v[40:43], v[164:167], v[204:207], v[40:43]\nv_mfma_f32_16x16x32_bf16 v[40:43], v[160:163], v[200:203], v[40:43]\n"
      "v_mfma_f32_16x16x32_bf16 v[36:39], v[152:155], v[200:203], v[36:39]\nv_mfma_f32_16x16x32_bf16 v[36:39], v[156:159], v[204:207], v[36:39]\n"
      "v_mfma_f32_16x16x32_bf16 v[32:35], v[148:151], v[204:207], v[32:35]\ns_waitcnt vmcnt(0) lgkmcnt(0)\ns_barrier\nv_mfma_f32_16x16x32_bf16 v[32:35], v[144:147], v[200:203], v[32:35]\n"
      "ds_read_b64_tr_b16 v[112:113], v220 offset:0\nv_mfma_f32_16x16x32_bf16 v[48:51], v[144:147], v[176:179], v[48:51]\nds_read_b64_tr_b16 v[114:115], v220 offset:1024\n"
      "v_mfma_f32_16x16x32_bf16 v[48:51], v[148:151], v[180:183], v[48:51]\nds_read_b64_tr_b16 v[116:117], v220 offset:8192\nv_mfma_f32_16x16x32_bf16 v[52:55], v[156:159], v[180:183], v[52:55]\n"
      "ds_read_b64_tr_b16 v[118:119], v220 offset:9216\nv_mfma_f32_16x16x32_bf16 v[52:55], v[152:155], v[176:179], v[52:55]\nds_read_b64_tr_b16 v[120:121], v221 offset:0\n"
      "v_mfma_f32_16x16x32_bf16 v[56:59], v[160:163], v[176:179], v[56:59]\nds_read_b64_tr_b16 v[122:123], v221 offset:1024\nv_mfma_f32_16x16x32_bf16 v[56:59], v[164:167], v[180:183], v[56:59]\n"
      "ds_read_b64_tr_b16 v[124:125], v221 offset:8192\nv_mfma_f32_16x16x32_bf16 v[60:63], v[172:175], v[180:183], v[60:63]\nds_read_b64_tr_b16 v[126:127], v221 offset:9216\n"
      "v_mfma_f32_16x16x32_bf16 v[60:63], v[168:171], v[176:179], v[60:63]\nds_read_b64_tr_b16 v[128:129], v222 offset:0\nv_mfma_f32_16x16x32_bf16 v[76:79], v[168:171], v[184:187], v[76:79]\n"
      "ds_read_b128 v[176:179], v214 offset:0\nv_mfma_f32_16x16x32_bf16 v[76:79], v[172:175], v[188:191], v[76:79]\nds_read_b128 v[180:183], v215 offset:0\n"
      "v_mfma_f32_16x16x32_bf16 v[72:75], v[164:167], v[188:191], v[72:75]\nds_read_b64_tr_b16 v[130:131], v222 offset:1024\nv_mfma_f32_16x16x32_bf16 v[72:75], v[160:163], v[184:187], v[72:75]\n"
      "ds_read_b64_tr_b16 v[132:133], v222 offset:8192\nv_mfma_f32_16x16x32_bf16 v[68:71], v[152:155], v[184:187], v[68:71]\nds_read_b64_tr_b16 v[134:135], v222 offset:9216\n"
      "v_mfma_f32_16x16x32_bf16 v[68:71], v[156:159], v[188:191], v[68:71]\nds_read_b64_tr_b16 v[136:137], v223 offset:0\nv_mfma_f32_16x16x32_bf16 v[64:67], v[148:151], v[188:191], v[64:67]\n"
      "ds_read_b64_tr_b16 v[138:139], v223 offset:1024\nv_mfma_f32_16x16x32_bf16 v[64:67], v[144:147], v[184:187], v[64:67]\nds_read_b64_tr_b16 v[140:141], v223 offset:8192\n"
      "v_mfma_f32_16x16x32_bf16 v[80:83], v[144:147], v[192:195], v[80:83]\nds_read_b128 v[184:187], v210 offset:0\nv_mfma_f32_16x16x32_bf16 v[80:83], v[148:151], v[196:199], v[80:83]\n"
      "ds_read_b128 v[188:191], v211 offset:0\nv_mfma_f32_16x16x32_bf16 v[84:87], v[156:159], v[196:199], v[84:87]\nds_read_b64_tr_b16 v[142:143], v223 offset:9216\n"
      "v_mfma_f32_16x16x32_bf16 v[84:87], v[152:155], v[192:195], v[84:87]\nv_mfma_f32_16x16x32_bf16 v[88:91], v[160:163], v[192:195], v[88:91]\ns_sub_u32 s46, s46, 1\ns_cmp_lg_u32 s46, 0\n"
      "v_mfma_f32_16x16x32_bf16 v[88:91], v[164:167], v[196:199], v[88:91]\nv_mfma_f32_16x16x32_bf16 v[92:95], v[172:175], v[196:199], v[92:95]\ns_waitcnt lgkmcnt(0)\n"
      "v_mfma_f32_16x16x32_bf16 v[92:95], v[168:171], v[192:195], v[92:95]\nds_read_b128 v[192:195], v210 offset:2048\nds_read_b128 v[196:199], v211 offset:2048\ns_cbranch_scc1 .Lgl14\n"
      "v_mfma_f32_16x16x32_bf16 v[96:99], v[112:115], v[176:179], v[96:99]\nds_read_b128 v[200:203], v210 offset:4096\nv_mfma_f32_16x16x32_bf16 v[96:99], v[116:119], v[180:183], v[96:99]\n"
      "ds_read_b128 v[204:207], v211 offset:4096\nv_mfma_f32_16x16x32_bf16 v[100:103], v[124:127], v[180:183], v[100:103]\nv_mfma_f32_16x16x32_bf16 v[100:103], v[120:123], v[176:179], v[100:103]\n"
      "v_mfma_f32_16x16x32_bf16 v[104:107], v[128:131], v[176:179], v[104:107]\nv_mfma_f32_16x16x32_bf16 v[104:107], v[132:135], v[180:183], v[104:107]\n"
      "v_mfma_f32_16x16x32_bf16 v[108:111], v[140:143], v[180:183], v[108:111]\nv_mfma_f32_16x16x32_bf16 v[108:111], v[136:139], v[176:179], v[108:111]\n"
      "v_mfma_f32_16x16x32_bf16 v[12:15], v[136:139], v[184:187], v[12:15]\nds_read_b128 v[176:179], v210 offset:6144\nv_mfma_f32_16x16x32_bf16 v[12:15], v[140:143], v[188:191], v[12:15]\n"
      "ds_read_b128 v[180:183], v211 offset:6144\nv_mfma_f32_16x16x32_bf16 v[8:11], v[132:135], v[188:191], v[8:11]\nv_mfma_f32_16x16x32_bf16 v[8:11], v[128:131], v[184:187], v[8:11]\n"
      "v_mfma_f32_16x16x32_bf16 v[4:7], v[120:123], v[184:187], v[4:7]\nv_mfma_f32_16x16x32_bf16 v[4:7], v[124:127], v[188:191], v[4:7]\n"
      "v_mfma_f32_16x16x32_bf16 v[0:3], v[116:119], v[188:191], v[0:3]\ns_waitcnt lgkmcnt(4)\nv_mfma_f32_16x16x32_bf16 v[0:3], v[112:115], v[184:187], v[0:3]\n"
      "v_mfma_f32_16x16x32_bf16 v[16:19], v[112:115], v[192:195], v[16:19]\nds_read_b128 v[184:187], v210 offset:8192\nv_mfma_f32_16x16x32_bf16 v[16:19], v[116:119], v[196:199], v[16:19]\n"
      "ds_read_b128 v[188:191], v211 offset:8192\nv_mfma_f32_16x16x32_bf16 v[20:23], v[124:127], v[196:199], v[20:23]\nv_mfma_f32_16x16x32_bf16 v[20:23], v[120:123], v[192:195], v[20:23]\n"
      "v_mfma_f32_16x16x32_bf16 v[24:27], v[128:131], v[192:195], v[24:27]\nv_mfma_f32_16x16x32_bf16 v[24:27], v[132:135], v[196:199], v[24:27]\n"
      "v_mfma_f32_16x16x32_bf16 v[28:31], v[140:143], v[196:199], v[28:31]\ns_waitcnt lgkmcnt(2)\nv_mfma_f32_16x16x32_bf16 v[28:31], v[136:139], v[192:195], v[28:31]\n"
      "v_mfma_f32_16x16x32_bf16 v[44:47], v[136:139], v[200:203], v[44:47]\nds_read_b128 v[192:195], v210 offset:10240\nv_mfma_f32_16x16x32_bf16 v[44:47], v[140:143], v[204:207], v[44:47]\n"
      "ds_read_b128 v[196:199], v211 offset:10240\nv_mfma_f32_16x16x32_bf16 v[40:43], v[132:135], v[204:207], v[40:43]\nv_mfma_f32_16x16x32_bf16 v[40:43], v[128:131], v[200:203], v[40:43]\n"
      "v_mfma_f32_16x16x32_bf16 v[36:39], v[120:123], v[200:203], v[36:39]\nv_mfma_f32_16x16x32_bf16 v[36:39], v[124:127], v[204:207], v[36:39]\n"
      "v_mfma_f32_16x16x32_bf16 v[32:35], v[116:119], v[204:207], v[32:35]\ns_waitcnt vmcnt(0) lgkmcnt(0)\ns_barrier\nv_mfma_f32_16x16x32_bf16 v[32:35], v[112:115], v[200:203], v[32:35]\n"
      "ds_read_b64_tr_b16 v[144:145], v216 offset:0\nv_mfma_f32_16x16x32_bf16 v[48:51], v[112:115], v[176:179], v[48:51]\nds_read_b64_tr_b16 v[146:147], v216 offset:1024\n"
      "v_mfma_f32_16x16x32_bf16 v[48:51], v[116:119], v[180:183], v[48:51]\nds_read_b64_tr_b16 v[148:149], v216 offset:8192\nv_mfma_f32_16x16x32_bf16 v[52:55], v[124:127], v[180:183], v[52:55]\n"
      "ds_read_b64_tr_b16 v[150:151], v216 offset:9216\nv_mfma_f32_16x16x32_bf16 v[52:55], v[120:123], v[176:179], v[52:55]\nds_read_b64_tr_b16 v[152:153], v217 offset:0\n"
      "v_mfma_f32_16x16x32_bf16 v[56:59], v[128:131], v[176:179], v[56:59]\nds_read_b64_tr_b16 v[154:155], v217 offset:1024\nv_mfma_f32_16x16x32_bf16 v[56:59], v[132:135], v[180:183], v[56:59]\n"
      "ds_read_b64_tr_b16 v[156:157], v217 offset:8192\nv_mfma_f32_16x16x32_bf16 v[60:63], v[140:143], v[180:183], v[60:63]\nds_read_b64_tr_b16 v[158:159], v217 offset:9216\n"
      "v_mfma_f32_16x16x32_bf16 v[60:63], v[136:139], v[176:179], v[60:63]\nds_read_b64_tr_b16 v[160:161], v218 offset:0\nv_mfma_f32_16x16x32_bf16 v[76:79], v[136:139], v[184:187], v[76:79]\n"
      "ds_read_b128 v[176:179], v212 offset:0\nv_mfma_f32_16x16x32_bf16 v[76:79], v[140:143], v[188:191], v[76:79]\nds_read_b128 v[180:183], v213 offset:0\n"
      "v_mfma_f32_16x16x32_bf16 v[72:75], v[132:135], v[188:191], v[72:75]\nds_read_b64_tr_b16 v[162:163], v218 offset:1024\nv_mfma_f32_16x16x32_bf16 v[72:75], v[128:131], v[184:187], v[72:75]\n"
      "ds_read_b64_tr_b16 v[164:165], v218 offset:8192\nv_mfma_f32_16x16x32_bf16 v[68:71], v[120:123], v[184:187], v[68:71]\nds_read_b64_tr_b16 v[166:167], v218 offset:9216\n"
      "v_mfma_f32_16x16x32_bf16 v[68:71], v[124:127], v[188:191], v[68:71]\nds_read_b64_tr_b16 v[168:169], v219 offset:0\nv_mfma_f32_16x16x32_bf16 v[64:67], v[116:119], v[188:191], v[64:67]\n"
      "ds_read_b64_tr_b16 v[170:171], v219 offset:1024\nv_mfma_f32_16x16x32_bf16 v[64:67], v[112:115], v[184:187], v[64:67]\nds_read_b64_tr_b16 v[172:173], v219 offset:8192\n"
      "v_mfma_f32_16x16x32_bf16 v[80:83], v[112:115], v[192:195], v[80:83]\nds_read_b128 v[184:187], v208 offset:0\nv_mfma_f32_16x16x32_bf16 v[80:83], v[116:119], v[196:199], v[80:83]\n"
      "ds_read_b128 v[188:191], v209 offset:0\nv_mfma_f32_16x16x32_bf16 v[84:87], v[124:127], v[196:199], v[84:87]\nds_read_b64_tr_b16 v[174:175], v219 offset:9216\n"
      "v_mfma_f32_16x16x32_bf16 v[84:87], v[120:123], v[192:195], v[84:87]\nv_mfma_f32_16x16x32_bf16 v[88:91], v[128:131], v[192:195], v[88:91]\n"
      "v_mfma_f32_16x16x32_bf16 v[88:91], v[132:135], v[196:199], v[88:91]\nv_mfma_f32_16x16x32_bf16 v[92:95], v[140:143], v[196:199], v[92:95]\ns_waitcnt lgkmcnt(0)\n"
      "v_mfma_f32_16x16x32_bf16 v[92:95], v[136:139], v[192:195], v[92:95]\nds_read_b128 v[192:195], v208 offset:2048\nds_read_b128 v[196:199], v209 offset:2048\n"
      "v_mfma_f32_16x16x32_bf16 v[96:99], v[144:147], v[176:179], v[96:99]\nds_read_b128 v[200:203], v208 offset:4096\nv_mfma_f32_16x16x32_bf16 v[96:99], v[148:151], v[180:183], v[96:99]\n"
      "ds_read_b128 v[204:207], v209 offset:4096\nv_mfma_f32_16x16x32_bf16 v[100:103], v[156:159], v[180:183], v[100:103]\nv_mfma_f32_16x16x32_bf16 v[100:103], v[152:155], v[176:179], v[100:103]\n"
      "v_mfma_f32_16x16x32_bf16 v[104:107], v[160:163], v[176:179], v[104:107]\nv_mfma_f32_16x16x32_bf16 v[104:107], v[164:167], v[180:183], v[104:107]\n"
      "v_mfma_f32_16x16x32_bf16 v[108:111], v[172:175], v[180:183], v[108:111]\nv_mfma_f32_16x16x32_bf16 v[108:111], v[168:171], v[176:179], v[108:111]\n"
      "v_mfma_f32_16x16x32_bf16 v[12:15], v[168:171], v[184:187], v[12:15]\nds_read_b128 v[176:179], v208 offset:6144\nv_mfma_f32_16x16x32_bf16 v[12:15], v[172:175], v[188:191], v[12:15]\n"
      "ds_read_b128 v[180:183], v209 offset:6144\nv_mfma_f32_16x16x32_bf16 v[8:11], v[164:167], v[188:191], v[8:11]\nv_mfma_f32_16x16x32_bf16 v[8:11], v[160:163], v[184:187], v[8:11]\n"
      "v_mfma_f32_16x16x32_bf16 v[4:7], v[152:155], v[184:187], v[4:7]\nv_mfma_f32_16x16x32_bf16 v[4:7], v[156:159], v[188:191], v[4:7]\n"
      "v_mfma_f32_16x16x32_bf16 v[0:3], v[148:151], v[188:191], v[0:3]\ns_waitcnt lgkmcnt(4)\nv_mfma_f32_16x16x32_bf16 v[0:3], v[144:147], v[184:187], v[0:3]\n"
      "v_mfma_f32_16x16x32_bf16 v[16:19], v[144:147], v[192:195], v[16:19]\nds_read_b128 v[184:187], v208 offset:8192\nv_mfma_f32_16x16x32_bf16 v[16:19], v[148:151], v[196:199], v[16:19]\n"
      "ds_read_b128 v[188:191], v209 offset:8192\nv_mfma_f32_16x16x32_bf16 v[20:23], v[156:159], v[196:199], v[20:23]\nv_mfma_f32_16x16x32_bf16 v[20:23], v[152:155], v[192:195], v[20:23]\n"
      "v_mfma_f32_16x16x32_bf16 v[24:27], v[160:163], v[192:195], v[24:27]\nv_mfma_f32_16x16x32_bf16 v[24:27], v[164:167], v[196:199], v[24:27]\n"
      "v_mfma_f32_16x16x32_bf16 v[28:31], v[172:175], v[196:199], v[28:31]\ns_waitcnt lgkmcnt(2)\nv_mfma_f32_16x16x32_bf16 v[28:31], v[168:171], v[192:195], v[28:31]\n"
      "v_mfma_f32_16x16x32_bf16 v[44:47], v[168:171], v[200:203], v[44:47]\nds_read_b128 v[192:195], v208 offset:10240\nv_mfma_f32_16x16x32_bf16 v[44:47], v[172:175], v[204:207], v[44:47]\n"
      "ds_read_b128 v[196:199], v209 offset:10240\nv_mfma_f32_16x16x32_bf16 v[40:43], v[164:167], v[204:207], v[40:43]\nv_mfma_f32_16x16x32_bf16 v[40:43], v[160:163], v[200:203], v[40:43]\n"
      "v_mfma_f32_16x16x32_bf16 v[36:39], v[152:155], v[200:203], v[36:39]\nv_mfma_f32_16x16x32_bf16 v[36:39], v[156:159], v[204:207], v[36:39]\n"
      "v_mfma_f32_16x16x32_bf16 v[32:35], v[148:151], v[204:207], v[32:35]\ns_waitcnt lgkmcnt(0)\nv_mfma_f32_16x16x32_bf16 v[32:35], v[144:147], v[200:203], v[32:35]\n"
      "v_mfma_f32_16x16x32_bf16 v[48:51], v[144:147], v[176:179], v[48:51]\nv_mfma_f32_16x16x32_bf16 v[48:51], v[148:151], v[180:183], v[48:51]\n"
      "v_mfma_f32_16x16x32_bf16 v[52:55], v[156:159], v[180:183], v[52:55]\nv_mfma_f32_16x16x32_bf16 v[52:55], v[152:155], v[176:179], v[52:55]\n"
      "v_mfma_f32_16x16x32_bf16 v[56:59], v[160:163], v[176:179], v[56:59]\nv_mfma_f32_16x16x32_bf16 v[56:59], v[164:167], v[180:183], v[56:59]\n"
      "v_mfma_f32_16x16x32_bf16 v[60:63], v[172:175], v[180:183], v[60:63]\nv_mfma_f32_16x16x32_bf16 v[60:63], v[168:171], v[176:179], v[60:63]\n"
      "v_mfma_f32_16x16x32_bf16 v[76:79], v[168:171], v[184:187], v[76:79]\nv_mfma_f32_16x16x32_bf16 v[76:79], v[172:175], v[188:191], v[76:79]\n"
      "v_mfma_f32_16x16x32_bf16 v[72:75], v[164:167], v[188:191], v[72:75]\nv_mfma_f32_16x16x32_bf16 v[72:75], v[160:163], v[184:187], v[72:75]\n"
      "v_mfma_f32_16x16x32_bf16 v[68:71], v[152:155], v[184:187], v[68:71]\nv_mfma_f32_16x16x32_bf16 v[68:71], v[156:159], v[188:191], v[68:71]\n"
      "v_mfma_f32_16x16x32_bf16 v[64:67], v[148:151], v[188:191], v[64:67]\nv_mfma_f32_16x16x32_bf16 v[64:67], v[144:147], v[184:187], v[64:67]\n"
      "v_mfma_f32_16x16x32_bf16 v[80:83], v[144:147], v[192:195], v[80:83]\nv_mfma_f32_16x16x32_bf16 v[80:83], v[148:151], v[196:199], v[80:83]\n"
      "v_mfma_f32_16x16x32_bf16 v[84:87], v[156:159], v[196:199], v[84:87]\nv_mfma_f32_16x16x32_bf16 v[84:87], v[152:155], v[192:195], v[84:87]\n"
      "v_mfma_f32_16x16x32_bf16 v[88:91], v[160:163], v[192:195], v[88:91]\nv_mfma_f32_16x16x32_bf16 v[88:91], v[164:167], v[196:199], v[88:91]\n"
      "v_mfma_f32_16x16x32_bf16 v[92:95], v[172:175], v[196:199], v[92:95]\nv_mfma_f32_16x16x32_bf16 v[92:95], v[168:171], v[192:195], v[92:95]\ns_setprio 3\nv_lshlrev_b32 v112, 16, v224\n"
      "v_and_b32 v113, 0xffff0000, v224\nv_lshlrev_b32 v114, 16, v225\nv_and_b32 v115, 0xffff0000, v225\nv_lshlrev_b32 v116, 16, v226\nv_and_b32 v117, 0xffff0000, v226\n"
      "v_lshlrev_b32 v118, 16, v227\nv_and_b32 v119, 0xffff0000, v227\nv_lshlrev_b32 v120, 16, v228\nv_and_b32 v121, 0xffff0000, v228\nv_lshlrev_b32 v122, 16, v229\n"
      "v_and_b32 v123, 0xffff0000, v229\nv_lshlrev_b32 v124, 16, v230\nv_and_b32 v125, 0xffff0000, v230\nv_lshlrev_b32 v126, 16, v231\nv_and_b32 v127, 0xffff0000, v231\nv_mov_b32 v128, 0xc0135761\n"
      "s_mov_b32 s46, 0xbdd2d3e7\nv_mov_b32 v166, 0\nv_pk_mul_f32 v[132:133], v[96:97], v[96:97]\nv_pk_mul_f32 v[134:135], v[98:99], v[98:99]\nv_pk_mul_f32 v[136:137], v[100:101], v[100:101]\n"
      "v_pk_mul_f32 v[138:139], v[102:103], v[102:103]\nv_pk_fma_f32 v[132:133], v[132:133], s[46:47], v[128:129] op_sel_hi:[1,0,0]\n"
      "v_pk_fma_f32 v[134:135], v[134:135], s[46:47], v[128:129] op_sel_hi:[1,0,0]\nv_pk_fma_f32 v[136:137], v[136:137], s[46:47], v[128:129] op_sel_hi:[1,0,0]\n"
      "v_pk_fma_f32 v[138:139], v[138:139], s[46:47], v[128:129] op_sel_hi:[1,0,0]\nv_pk_mul_f32 v[132:133], v[132:133], v[96:97]\nv_pk_mul_f32 v[134:135], v[134:135], v[98:99]\n"
      "v_pk_mul_f32 v[136:137], v[136:137], v[100:101]\nv_pk_mul_f32 v[138:139], v[138:139], v[102:103]\nv_pk_mul_f32 v[96:97], v[96:97], v[112:113]\nv_pk_mul_f32 v[98:99], v[98:99], v[114:115]\n"
      "v_pk_mul_f32 v[100:101], v[100:101], v[116:117]\nv_pk_mul_f32 v[102:103], v[102:103], v[118:119]\nv_exp_f32 v132, v132\nv_exp_f32 v133, v133\nv_exp_f32 v134, v134\nv_exp_f32 v135, v135\n"
      "v_exp_f32 v136, v136\nv_exp_f32 v137, v137\nv_exp_f32 v138, v138\nv_exp_f32 v139, v139\nv_pk_add_f32 v[132:133], v[132:133], 1.0 op_sel_hi:[1,0]\n"
      "v_pk_add_f32 v[134:135], v[134:135], 1.0 op_sel_hi:[1,0]\nv_pk_add_f32 v[136:137], v[136:137], 1.0 op_sel_hi:[1,0]\nv_pk_add_f32 v[138:139], v[138:139], 1.0 op_sel_hi:[1,0]\n"
      "v_rcp_f32 v132, v132\nv_rcp_f32 v133, v133\nv_rcp_f32 v134, v134\nv_rcp_f32 v135, v135\nv_rcp_f32 v136, v136\nv_rcp_f32 v137, v137\nv_rcp_f32 v138, v138\nv_rcp_f32 v139, v139\n"
      "v_pk_mul_f32 v[148:149], v[96:97], v[132:133]\nv_pk_mul_f32 v[140:141], v[104:105], v[104:105]\nv_pk_mul_f32 v[150:151], v[98:99], v[134:135]\n"
      "v_pk_mul_f32 v[142:143], v[106:107], v[106:107]\nv_pk_fma_f32 v[148:149], v[100:101], v[136:137], v[148:149]\nv_pk_mul_f32 v[144:145], v[108:109], v[108:109]\n"
      "v_pk_fma_f32 v[150:151], v[102:103], v[138:139], v[150:151]\nv_pk_mul_f32 v[146:147], v[110:111], v[110:111]\nv_pk_fma_f32 v[140:141], v[140:141], s[46:47], v[128:129] op_sel_hi:[1,0,0]\n"
      "v_pk_fma_f32 v[142:143], v[142:143], s[46:47], v[128:129] op_sel_hi:[1,0,0]\nv_pk_fma_f32 v[144:145], v[144:145], s[46:47], v[128:129] op_sel_hi:[1,0,0]\n"
      "v_pk_fma_f32 v[146:147], v[146:147], s[46:47], v[128:129] op_sel_hi:[1,0,0]\nv_pk_mul_f32 v[140:141], v[140:141], v[104:105]\nv_pk_mul_f32 v[142:143], v[142:143], v[106:107]\n"
      "v_pk_mul_f32 v[144:145], v[144:145], v[108:109]\nv_pk_mul_f32 v[146:147], v[146:147], v[110:111]\nv_pk_mul_f32 v[104:105], v[104:105], v[120:121]\n"
      "v_pk_mul_f32 v[106:107], v[106:107], v[122:123]\nv_pk_mul_f32 v[108:109], v[108:109], v[124:125]\nv_pk_mul_f32 v[110:111], v[110:111], v[126:127]\nv_exp_f32 v140, v140\n"
      "v_exp_f32 v141, v141\nv_exp_f32 v142, v142\nv_exp_f32 v143, v143\nv_exp_f32 v144, v144\nv_exp_f32 v145, v145\nv_exp_f32 v146, v146\nv_exp_f32 v147, v147\n"
      "v_pk_add_f32 v[140:141], v[140:141], 1.0 op_sel_hi:[1,0]\nv_pk_add_f32 v[142:143], v[142:143], 1.0 op_sel_hi:[1,0]\nv_pk_add_f32 v[144:145], v[144:145], 1.0 op_sel_hi:[1,0]\n"
      "v_pk_add_f32 v[146:147], v[146:147], 1.0 op_sel_hi:[1,0]\nv_rcp_f32 v140, v140\nv_rcp_f32 v141, v141\nv_rcp_f32 v142, v142\nv_rcp_f32 v143, v143\nv_rcp_f32 v144, v144\nv_rcp_f32 v145, v145\n"
      "v_rcp_f32 v146, v146\nv_rcp_f32 v147, v147\nv_pk_fma_f32 v[148:149], v[104:105], v[140:141], v[148:149]\nv_pk_mul_f32 v[132:133], v[0:1], v[0:1]\n"
      "v_pk_fma_f32 v[150:151], v[106:107], v[142:143], v[150:151]\nv_pk_mul_f32 v[134:135], v[2:3], v[2:3]\nv_pk_fma_f32 v[148:149], v[108:109], v[144:145], v[148:149]\n"
      "v_pk_mul_f32 v[136:137], v[4:5], v[4:5]\nv_pk_fma_f32 v[150:151], v[110:111], v[146:147], v[150:151]\nv_pk_mul_f32 v[138:139], v[6:7], v[6:7]\n"
      "v_pk_add_f32 v[148:149], v[148:149], v[150:151]\nv_pk_fma_f32 v[132:133], v[132:133], s[46:47], v[128:129] op_sel_hi:[1,0,0]\nv_add_f32 v166, v148, v149\n"
      "v_pk_fma_f32 v[134:135], v[134:135], s[46:47], v[128:129] op_sel_hi:[1,0,0]\nv_pk_fma_f32 v[136:137], v[136:137], s[46:47], v[128:129] op_sel_hi:[1,0,0]\n"
      "v_pk_fma_f32 v[138:139], v[138:139], s[46:47], v[128:129] op_sel_hi:[1,0,0]\nv_pk_mul_f32 v[132:133], v[132:133], v[0:1]\nv_pk_mul_f32 v[134:135], v[134:135], v[2:3]\n"
      "v_pk_mul_f32 v[136:137], v[136:137], v[4:5]\nv_pk_mul_f32 v[138:139], v[138:139], v[6:7]\nv_pk_mul_f32 v[0:1], v[0:1], v[112:113]\nv_pk_mul_f32 v[2:3], v[2:3], v[114:115]\n"
      "v_pk_mul_f32 v[4:5], v[4:5], v[116:117]\nv_pk_mul_f32 v[6:7], v[6:7], v[118:119]\nv_exp_f32 v132, v132\nv_exp_f32 v133, v133\nv_exp_f32 v134, v134\nv_exp_f32 v135, v135\n"
      "v_exp_f32 v136, v136\nv_exp_f32 v137, v137\nv_exp_f32 v138, v138\nv_exp_f32 v139, v139\nv_pk_add_f32 v[132:133], v[132:133], 1.0 op_sel_hi:[1,0]\n"
      "v_pk_add_f32 v[134:135], v[134:135], 1.0 op_sel_hi:[1,0]\nv_pk_add_f32 v[136:137], v[136:137], 1.0 op_sel_hi:[1,0]\nv_pk_add_f32 v[138:139], v[138:139], 1.0 op_sel_hi:[1,0]\n"
      "v_rcp_f32 v132, v132\nv_rcp_f32 v133, v133\nv_rcp_f32 v134, v134\nv_rcp_f32 v135, v135\nv_rcp_f32 v136, v136\nv_rcp_f32 v137, v137\nv_rcp_f32 v138, v138\nv_rcp_f32 v139, v139\n"
      "v_pk_mul_f32 v[148:149], v[0:1], v[132:133]\nv_pk_mul_f32 v[140:141], v[8:9], v[8:9]\nv_pk_mul_f32 v[150:151], v[2:3], v[134:135]\nv_pk_mul_f32 v[142:143], v[10:11], v[10:11]\n"
      "v_pk_fma_f32 v[148:149], v[4:5], v[136:137], v[148:149]\nv_pk_mul_f32 v[144:145], v[12:13], v[12:13]\nv_pk_fma_f32 v[150:151], v[6:7], v[138:139], v[150:151]\n"
      "v_pk_mul_f32 v[146:147], v[14:15], v[14:15]\nv_pk_fma_f32 v[140:141], v[140:141], s[46:47], v[128:129] op_sel_hi:[1,0,0]\n"
      "v_pk_fma_f32 v[142:143], v[142:143], s[46:47], v[128:129] op_sel_hi:[1,0,0]\nv_pk_fma_f32 v[144:145], v[144:145], s[46:47], v[128:129] op_sel_hi:[1,0,0]\n"
      "v_pk_fma_f32 v[146:147], v[146:147], s[46:47], v[128:129] op_sel_hi:[1,0,0]\nv_pk_mul_f32 v[140:141], v[140:141], v[8:9]\nv_pk_mul_f32 v[142:143], v[142:143], v[10:11]\n"
      "v_pk_mul_f32 v[144:145], v[144:145], v[12:13]\nv_pk_mul_f32 v[146:147], v[146:147], v[14:15]\nv_pk_mul_f32 v[8:9], v[8:9], v[120:121]\nv_pk_mul_f32 v[10:11], v[10:11], v[122:123]\n"
      "v_pk_mul_f32 v[12:13], v[12:13], v[124:125]\nv_pk_mul_f32 v[14:15], v[14:15], v[126:127]\nv_exp_f32 v140, v140\nv_exp_f32 v141, v141\nv_exp_f32 v142, v142\nv_exp_f32 v143, v143\n"
      "v_exp_f32 v144, v144\nv_exp_f32 v145, v145\nv_exp_f32 v146, v146\nv_exp_f32 v147, v147\nv_pk_add_f32 v[140:141], v[140:141], 1.0 op_sel_hi:[1,0]\n"
      "v_pk_add_f32 v[142:143], v[142:143], 1.0 op_sel_hi:[1,0]\nv_pk_add_f32 v[144:145], v[144:145], 1.0 op_sel_hi:[1,0]\nv_pk_add_f32 v[146:147], v[146:147], 1.0 op_sel_hi:[1,0]\n"
      "v_rcp_f32 v140, v140\nv_rcp_f32 v141, v141\nv_rcp_f32 v142, v142\nv_rcp_f32 v143, v143\nv_rcp_f32 v144, v144\nv_rcp_f32 v145, v145\nv_rcp_f32 v146, v146\nv_rcp_f32 v147, v147\n"
      "v_pk_fma_f32 v[148:149], v[8:9], v[140:141], v[148:149]\nv_pk_mul_f32 v[132:133], v[16:17], v[16:17]\nv_pk_fma_f32 v[150:151], v[10:11], v[142:143], v[150:151]\n"
      "v_pk_mul_f32 v[134:135], v[18:19], v[18:19]\nv_pk_fma_f32 v[148:149], v[12:13], v[144:145], v[148:149]\nv_pk_mul_f32 v[136:137], v[20:21], v[20:21]\n"
      "v_pk_fma_f32 v[150:151], v[14:15], v[146:147], v[150:151]\nv_pk_mul_f32 v[138:139], v[22:23], v[22:23]\nv_pk_add_f32 v[148:149], v[148:149], v[150:151]\n"
      "v_pk_fma_f32 v[132:133], v[132:133], s[46:47], v[128:129] op_sel_hi:[1,0,0]\nv_add_f32 v160, v148, v149\nv_pk_fma_f32 v[134:135], v[134:135], s[46:47], v[128:129] op_sel_hi:[1,0,0]\n"
      "v_pk_fma_f32 v[136:137], v[136:137], s[46:47], v[128:129] op_sel_hi:[1,0,0]\nv_pk_fma_f32 v[138:139], v[138:139], s[46:47], v[128:129] op_sel_hi:[1,0,0]\n"
      "v_pk_mul_f32 v[132:133], v[132:133], v[16:17]\nv_pk_mul_f32 v[134:135], v[134:135], v[18:19]\nv_pk_mul_f32 v[136:137], v[136:137], v[20:21]\nv_pk_mul_f32 v[138:139], v[138:139], v[22:23]\n"
      "v_pk_mul_f32 v[16:17], v[16:17], v[112:113]\nv_pk_mul_f32 v[18:19], v[18:19], v[114:115]\nv_pk_mul_f32 v[20:21], v[20:21], v[116:117]\nv_pk_mul_f32 v[22:23], v[22:23], v[118:119]\n"
      "v_exp_f32 v132, v132\nv_exp_f32 v133, v133\nv_exp_f32 v134, v134\nv_exp_f32 v135, v135\nv_exp_f32 v136, v136\nv_exp_f32 v137, v137\nv_exp_f32 v138, v138\nv_exp_f32 v139, v139\n"
      "v_pk_add_f32 v[132:133], v[132:133], 1.0 op_sel_hi:[1,0]\nv_pk_add_f32 v[134:135], v[134:135], 1.0 op_sel_hi:[1,0]\nv_pk_add_f32 v[136:137], v[136:137], 1.0 op_sel_hi:[1,0]\n"
      "v_pk_add_f32 v[138:139], v[138:139], 1.0 op_sel_hi:[1,0]\nv_rcp_f32 v132, v132\nv_rcp_f32 v133, v133\nv_rcp_f32 v134, v134\nv_rcp_f32 v135, v135\nv_rcp_f32 v136, v136\nv_rcp_f32 v137, v137\n"
      "v_rcp_f32 v138, v138\nv_rcp_f32 v139, v139\nv_pk_mul_f32 v[148:149], v[16:17], v[132:133]\nv_pk_mul_f32 v[140:141], v[24:25], v[24:25]\nv_pk_mul_f32 v[150:151], v[18:19], v[134:135]\n"
      "v_pk_mul_f32 v[142:143], v[26:27], v[26:27]\nv_pk_fma_f32 v[148:149], v[20:21], v[136:137], v[148:149]\nv_pk_mul_f32 v[144:145], v[28:29], v[28:29]\n"
      "v_pk_fma_f32 v[150:151], v[22:23], v[138:139], v[150:151]\nv_pk_mul_f32 v[146:147], v[30:31], v[30:31]\nv_pk_fma_f32 v[140:141], v[140:141], s[46:47], v[128:129] op_sel_hi:[1,0,0]\n"
      "v_pk_fma_f32 v[142:143], v[142:143], s[46:47], v[128:129] op_sel_hi:[1,0,0]\nv_pk_fma_f32 v[144:145], v[144:145], s[46:47], v[128:129] op_sel_hi:[1,0,0]\n"
      "v_pk_fma_f32 v[146:147], v[146:147], s[46:47], v[128:129] op_sel_hi:[1,0,0]\nv_pk_mul_f32 v[140:141], v[140:141], v[24:25]\nv_pk_mul_f32 v[142:143], v[142:143], v[26:27]\n"
      "v_pk_mul_f32 v[144:145], v[144:145], v[28:29]\nv_pk_mul_f32 v[146:147], v[146:147], v[30:31]\nv_pk_mul_f32 v[24:25], v[24:25], v[120:121]\nv_pk_mul_f32 v[26:27], v[26:27], v[122:123]\n"
      "v_pk_mul_f32 v[28:29], v[28:29], v[124:125]\nv_pk_mul_f32 v[30:31], v[30:31], v[126:127]\nv_exp_f32 v140, v140\nv_exp_f32 v141, v141\nv_exp_f32 v142, v142\nv_exp_f32 v143, v143\n"
      "v_exp_f32 v144, v144\nv_exp_f32 v145, v145\nv_exp_f32 v146, v146\nv_exp_f32 v147, v147\nv_pk_add_f32 v[140:141], v[140:141], 1.0 op_sel_hi:[1,0]\n"
      "v_pk_add_f32 v[142:143], v[142:143], 1.0 op_sel_hi:[1,0]\nv_pk_add_f32 v[144:145], v[144:145], 1.0 op_sel_hi:[1,0]\nv_pk_add_f32 v[146:147], v[146:147], 1.0 op_sel_hi:[1,0]\n"
      "v_rcp_f32 v140, v140\nv_rcp_f32 v141, v141\nv_rcp_f32 v142, v142\nv_rcp_f32 v143, v143\nv_rcp_f32 v144, v144\nv_rcp_f32 v145, v145\nv_rcp_f32 v146, v146\nv_rcp_f32 v147, v147\n"
      "v_pk_fma_f32 v[148:149], v[24:25], v[140:141], v[148:149]\nv_pk_mul_f32 v[132:133], v[32:33], v[32:33]\nv_pk_fma_f32 v[150:151], v[26:27], v[142:143], v[150:151]\n"
      "v_pk_mul_f32 v[134:135], v[34:35], v[34:35]\nv_pk_fma_f32 v[148:149], v[28:29], v[144:145], v[148:149]\nv_pk_mul_f32 v[136:137], v[36:37], v[36:37]\n"
      "v_pk_fma_f32 v[150:151], v[30:31], v[146:147], v[150:151]\nv_pk_mul_f32 v[138:139], v[38:39], v[38:39]\nv_pk_add_f32 v[148:149], v[148:149], v[150:151]\n"
      "v_pk_fma_f32 v[132:133], v[132:133], s[46:47], v[128:129] op_sel_hi:[1,0,0]\nv_add_f32 v161, v148, v149\nv_pk_fma_f32 v[134:135], v[134:135], s[46:47], v[128:129] op_sel_hi:[1,0,0]\n"
      "v_pk_fma_f32 v[136:137], v[136:137], s[46:47], v[128:129] op_sel_hi:[1,0,0]\nv_pk_fma_f32 v[138:139], v[138:139], s[46:47], v[128:129] op_sel_hi:[1,0,0]\n"
      "v_pk_mul_f32 v[132:133], v[132:133], v[32:33]\nv_pk_mul_f32 v[134:135], v[134:135], v[34:35]\nv_pk_mul_f32 v[136:137], v[136:137], v[36:37]\nv_pk_mul_f32 v[138:139], v[138:139], v[38:39]\n"
      "v_pk_mul_f32 v[32:33], v[32:33], v[112:113]\nv_pk_mul_f32 v[34:35], v[34:35], v[114:115]\nv_pk_mul_f32 v[36:37], v[36:37], v[116:117]\nv_pk_mul_f32 v[38:39], v[38:39], v[118:119]\n"
      "v_exp_f32 v132, v132\nv_exp_f32 v133, v133\nv_exp_f32 v134, v134\nv_exp_f32 v135, v135\nv_exp_f32 v136, v136\nv_exp_f32 v137, v137\nv_exp_f32 v138, v138\nv_exp_f32 v139, v139\n"
      "v_pk_add_f32 v[132:133], v[132:133], 1.0 op_sel_hi:[1,0]\nv_pk_add_f32 v[134:135], v[134:135], 1.0 op_sel_hi:[1,0]\nv_pk_add_f32 v[136:137], v[136:137], 1.0 op_sel_hi:[1,0]\n"
      "v_pk_add_f32 v[138:139], v[138:139], 1.0 op_sel_hi:[1,0]\nv_rcp_f32 v132, v132\nv_rcp_f32 v133, v133\nv_rcp_f32 v134, v134\nv_rcp_f32 v135, v135\nv_rcp_f32 v136, v136\nv_rcp_f32 v137, v137\n"
      "v_rcp_f32 v138, v138\nv_rcp_f32 v139, v139\nv_pk_mul_f32 v[148:149], v[32:33], v[132:133]\nv_pk_mul_f32 v[140:141], v[40:41], v[40:41]\nv_pk_mul_f32 v[150:151], v[34:35], v[134:135]\n"
      "v_pk_mul_f32 v[142:143], v[42:43], v[42:43]\nv_pk_fma_f32 v[148:149], v[36:37], v[136:137], v[148:149]\nv_pk_mul_f32 v[144:145], v[44:45], v[44:45]\n"
      "v_pk_fma_f32 v[150:151], v[38:39], v[138:139], v[150:151]\nv_pk_mul_f32 v[146:147], v[46:47], v[46:47]\nv_pk_fma_f32 v[140:141], v[140:141], s[46:47], v[128:129] op_sel_hi:[1,0,0]\n"
      "v_pk_fma_f32 v[142:143], v[142:143], s[46:47], v[128:129] op_sel_hi:[1,0,0]\nv_pk_fma_f32 v[144:145], v[144:145], s[46:47], v[128:129] op_sel_hi:[1,0,0]\n"
      "v_pk_fma_f32 v[146:147], v[146:147], s[46:47], v[128:129] op_sel_hi:[1,0,0]\nv_pk_mul_f32 v[140:141], v[140:141], v[40:41]\nv_pk_mul_f32 v[142:143], v[142:143], v[42:43]\n"
      "v_pk_mul_f32 v[144:145], v[144:145], v[44:45]\nv_pk_mul_f32 v[146:147], v[146:147], v[46:47]\nv_pk_mul_f32 v[40:41], v[40:41], v[120:121]\nv_pk_mul_f32 v[42:43], v[42:43], v[122:123]\n"
      "v_pk_mul_f32 v[44:45], v[44:45], v[124:125]\nv_pk_mul_f32 v[46:47], v[46:47], v[126:127]\nv_exp_f32 v140, v140\nv_exp_f32 v141, v141\nv_exp_f32 v142, v142\nv_exp_f32 v143, v143\n"
      "v_exp_f32 v144, v144\nv_exp_f32 v145, v145\nv_exp_f32 v146, v146\nv_exp_f32 v147, v147\nv_pk_add_f32 v[140:141], v[140:141], 1.0 op_sel_hi:[1,0]\n"
      "v_pk_add_f32 v[142:143], v[142:143], 1.0 op_sel_hi:[1,0]\nv_pk_add_f32 v[144:145], v[144:145], 1.0 op_sel_hi:[1,0]\nv_pk_add_f32 v[146:147], v[146:147], 1.0 op_sel_hi:[1,0]\n"
      "v_rcp_f32 v140, v140\nv_rcp_f32 v141, v141\nv_rcp_f32 v142, v142\nv_rcp_f32 v143, v143\nv_rcp_f32 v144, v144\nv_rcp_f32 v145, v145\nv_rcp_f32 v146, v146\nv_rcp_f32 v147, v147\n"
      "v_pk_fma_f32 v[148:149], v[40:41], v[140:141], v[148:149]\nv_pk_mul_f32 v[132:133], v[48:49], v[48:49]\nv_pk_fma_f32 v[150:151], v[42:43], v[142:143], v[150:151]\n"
      "v_pk_mul_f32 v[134:135], v[50:51], v[50:51]\nv_pk_fma_f32 v[148:149], v[44:45], v[144:145], v[148:149]\nv_pk_mul_f32 v[136:137], v[52:53], v[52:53]\n"
      "v_pk_fma_f32 v[150:151], v[46:47], v[146:147], v[150:151]\nv_pk_mul_f32 v[138:139], v[54:55], v[54:55]\nv_pk_add_f32 v[148:149], v[148:149], v[150:151]\n"
      "v_pk_fma_f32 v[132:133], v[132:133], s[46:47], v[128:129] op_sel_hi:[1,0,0]\nv_add_f32 v162, v148, v149\nv_pk_fma_f32 v[134:135], v[134:135], s[46:47], v[128:129] op_sel_hi:[1,0,0]\n"
      "v_pk_fma_f32 v[136:137], v[136:137], s[46:47], v[128:129] op_sel_hi:[1,0,0]\nv_pk_fma_f32 v[138:139], v[138:139], s[46:47], v[128:129] op_sel_hi:[1,0,0]\n"
      "v_pk_mul_f32 v[132:133], v[132:133], v[48:49]\nv_pk_mul_f32 v[134:135], v[134:135], v[50:51]\nv_pk_mul_f32 v[136:137], v[136:137], v[52:53]\nv_pk_mul_f32 v[138:139], v[138:139], v[54:55]\n"
      "v_pk_mul_f32 v[48:49], v[48:49], v[112:113]\nv_pk_mul_f32 v[50:51], v[50:51], v[114:115]\nv_pk_mul_f32 v[52:53], v[52:53], v[116:117]\nv_pk_mul_f32 v[54:55], v[54:55], v[118:119]\n"
      "v_exp_f32 v132, v132\nv_exp_f32 v133, v133\nv_exp_f32 v134, v134\nv_exp_f32 v135, v135\nv_exp_f32 v136, v136\nv_exp_f32 v137, v137\nv_exp_f32 v138, v138\nv_exp_f32 v139, v139\n"
      "v_pk_add_f32 v[132:133], v[132:133], 1.0 op_sel_hi:[1,0]\nv_pk_add_f32 v[134:135], v[134:135], 1.0 op_sel_hi:[1,0]\nv_pk_add_f32 v[136:137], v[136:137], 1.0 op_sel_hi:[1,0]\n"
      "v_pk_add_f32 v[138:139], v[138:139], 1.0 op_sel_hi:[1,0]\nv_rcp_f32 v132, v132\nv_rcp_f32 v133, v133\nv_rcp_f32 v134, v134\nv_rcp_f32 v135, v135\nv_rcp_f32 v136, v136\nv_rcp_f32 v137, v137\n"
      "v_rcp_f32 v138, v138\nv_rcp_f32 v139, v139\nv_pk_mul_f32 v[148:149], v[48:49], v[132:133]\nv_pk_mul_f32 v[140:141], v[56:57], v[56:57]\nv_pk_mul_f32 v[150:151], v[50:51], v[134:135]\n"
      "v_pk_mul_f32 v[142:143], v[58:59], v[58:59]\nv_pk_fma_f32 v[148:149], v[52:53], v[136:137], v[148:149]\nv_pk_mul_f32 v[144:145], v[60:61], v[60:61]\n"
      "v_pk_fma_f32 v[150:151], v[54:55], v[138:139], v[150:151]\nv_pk_mul_f32 v[146:147], v[62:63], v[62:63]\nv_pk_fma_f32 v[140:141], v[140:141], s[46:47], v[128:129] op_sel_hi:[1,0,0]\n"
      "v_pk_fma_f32 v[142:143], v[142:143], s[46:47], v[128:129] op_sel_hi:[1,0,0]\nv_pk_fma_f32 v[144:145], v[144:145], s[46:47], v[128:129] op_sel_hi:[1,0,0]\n"
      "v_pk_fma_f32 v[146:147], v[146:147], s[46:47], v[128:129] op_sel_hi:[1,0,0]\nv_pk_mul_f32 v[140:141], v[140:141], v[56:57]\nv_pk_mul_f32 v[142:143], v[142:143], v[58:59]\n"
      "v_pk_mul_f32 v[144:145], v[144:145], v[60:61]\nv_pk_mul_f32 v[146:147], v[146:147], v[62:63]\nv_pk_mul_f32 v[56:57], v[56:57], v[120:121]\nv_pk_mul_f32 v[58:59], v[58:59], v[122:123]\n"
      "v_pk_mul_f32 v[60:61], v[60:61], v[124:125]\nv_pk_mul_f32 v[62:63], v[62:63], v[126:127]\nv_exp_f32 v140, v140\nv_exp_f32 v141, v141\nv_exp_f32 v142, v142\nv_exp_f32 v143, v143\n"
      "v_exp_f32 v144, v144\nv_exp_f32 v145, v145\nv_exp_f32 v146, v146\nv_exp_f32 v147, v147\nv_pk_add_f32 v[140:141], v[140:141], 1.0 op_sel_hi:[1,0]\n"
      "v_pk_add_f32 v[142:143], v[142:143], 1.0 op_sel_hi:[1,0]\nv_pk_add_f32 v[144:145], v[144:145], 1.0 op_sel_hi:[1,0]\nv_pk_add_f32 v[146:147], v[146:147], 1.0 op_sel_hi:[1,0]\n"
      "v_rcp_f32 v140, v140\nv_rcp_f32 v141, v141\nv_rcp_f32 v142, v142\nv_rcp_f32 v143, v143\nv_rcp_f32 v144, v144\nv_rcp_f32 v145, v145\nv_rcp_f32 v146, v146\nv_rcp_f32 v147, v147\n"
      "v_pk_fma_f32 v[148:149], v[56:57], v[140:141], v[148:149]\nv_pk_mul_f32 v[132:133], v[64:65], v[64:65]\nv_pk_fma_f32 v[150:151], v[58:59], v[142:143], v[150:151]\n"
      "v_pk_mul_f32 v[134:135], v[66:67], v[66:67]\nv_pk_fma_f32 v[148:149], v[60:61], v[144:145], v[148:149]\nv_pk_mul_f32 v[136:137], v[68:69], v[68:69]\n"
      "v_pk_fma_f32 v[150:151], v[62:63], v[146:147], v[150:151]\nv_pk_mul_f32 v[138:139], v[70:71], v[70:71]\nv_pk_add_f32 v[148:149], v[148:149], v[150:151]\n"
      "v_pk_fma_f32 v[132:133], v[132:133], s[46:47], v[128:129] op_sel_hi:[1,0,0]\nv_add_f32 v163, v148, v149\nv_pk_fma_f32 v[134:135], v[134:135], s[46:47], v[128:129] op_sel_hi:[1,0,0]\n"
      "v_pk_fma_f32 v[136:137], v[136:137], s[46:47], v[128:129] op_sel_hi:[1,0,0]\nv_pk_fma_f32 v[138:139], v[138:139], s[46:47], v[128:129] op_sel_hi:[1,0,0]\n"
      "v_pk_mul_f32 v[132:133], v[132:133], v[64:65]\nv_pk_mul_f32 v[134:135], v[134:135], v[66:67]\nv_pk_mul_f32 v[136:137], v[136:137], v[68:69]\nv_pk_mul_f32 v[138:139], v[138:139], v[70:71]\n"
      "v_pk_mul_f32 v[64:65], v[64:65], v[112:113]\nv_pk_mul_f32 v[66:67], v[66:67], v[114:115]\nv_pk_mul_f32 v[68:69], v[68:69], v[116:117]\nv_pk_mul_f32 v[70:71], v[70:71], v[118:119]\n"
      "v_exp_f32 v132, v132\nv_exp_f32 v133, v133\nv_exp_f32 v134, v134\nv_exp_f32 v135, v135\nv_exp_f32 v136, v136\nv_exp_f32 v137, v137\nv_exp_f32 v138, v138\nv_exp_f32 v139, v139\n"
      "v_pk_add_f32 v[132:133], v[132:133], 1.0 op_sel_hi:[1,0]\nv_pk_add_f32 v[134:135], v[134:135], 1.0 op_sel_hi:[1,0]\nv_pk_add_f32 v[136:137], v[136:137], 1.0 op_sel_hi:[1,0]\n"
      "v_pk_add_f32 v[138:139], v[138:139], 1.0 op_sel_hi:[1,0]\nv_rcp_f32 v132, v132\nv_rcp_f32 v133, v133\nv_rcp_f32 v134, v134\nv_rcp_f32 v135, v135\nv_rcp_f32 v136, v136\nv_rcp_f32 v137, v137\n"
      "v_rcp_f32 v138, v138\nv_rcp_f32 v139, v139\nv_pk_mul_f32 v[148:149], v[64:65], v[132:133]\nv_pk_mul_f32 v[140:141], v[72:73], v[72:73]\nv_pk_mul_f32 v[150:151], v[66:67], v[134:135]\n"
      "v_pk_mul_f32 v[142:143], v[74:75], v[74:75]\nv_pk_fma_f32 v[148:149], v[68:69], v[136:137], v[148:149]\nv_pk_mul_f32 v[144:145], v[76:77], v[76:77]\n"
      "v_pk_fma_f32 v[150:151], v[70:71], v[138:139], v[150:151]\nv_pk_mul_f32 v[146:147], v[78:79], v[78:79]\nv_pk_fma_f32 v[140:141], v[140:141], s[46:47], v[128:129] op_sel_hi:[1,0,0]\n"
      "v_pk_fma_f32 v[142:143], v[142:143], s[46:47], v[128:129] op_sel_hi:[1,0,0]\nv_pk_fma_f32 v[144:145], v[144:145], s[46:47], v[128:129] op_sel_hi:[1,0,0]\n"
      "v_pk_fma_f32 v[146:147], v[146:147], s[46:47], v[128:129] op_sel_hi:[1,0,0]\nv_pk_mul_f32 v[140:141], v[140:141], v[72:73]\nv_pk_mul_f32 v[142:143], v[142:143], v[74:75]\n"
      "v_pk_mul_f32 v[144:145], v[144:145], v[76:77]\nv_pk_mul_f32 v[146:147], v[146:147], v[78:79]\nv_pk_mul_f32 v[72:73], v[72:73], v[120:121]\nv_pk_mul_f32 v[74:75], v[74:75], v[122:123]\n"
      "v_pk_mul_f32 v[76:77], v[76:77], v[124:125]\nv_pk_mul_f32 v[78:79], v[78:79], v[126:127]\nv_exp_f32 v140, v140\nv_exp_f32 v141, v141\nv_exp_f32 v142, v142\nv_exp_f32 v143, v143\n"
      "v_exp_f32 v144, v144\nv_exp_f32 v145, v145\nv_exp_f32 v146, v146\nv_exp_f32 v147, v147\nv_pk_add_f32 v[140:141], v[140:141], 1.0 op_sel_hi:[1,0]\n"
      "v_pk_add_f32 v[142:143], v[142:143], 1.0 op_sel_hi:[1,0]\nv_pk_add_f32 v[144:145], v[144:145], 1.0 op_sel_hi:[1,0]\nv_pk_add_f32 v[146:147], v[146:147], 1.0 op_sel_hi:[1,0]\n"
      "v_rcp_f32 v140, v140\nv_rcp_f32 v141, v141\nv_rcp_f32 v142, v142\nv_rcp_f32 v143, v143\nv_rcp_f32 v144, v144\nv_rcp_f32 v145, v145\nv_rcp_f32 v146, v146\nv_rcp_f32 v147, v147\n"
      "v_pk_fma_f32 v[148:149], v[72:73], v[140:141], v[148:149]\nv_pk_mul_f32 v[132:133], v[80:81], v[80:81]\nv_pk_fma_f32 v[150:151], v[74:75], v[142:143], v[150:151]\n"
      "v_pk_mul_f32 v[134:135], v[82:83], v[82:83]\nv_pk_fma_f32 v[148:149], v[76:77], v[144:145], v[148:149]\nv_pk_mul_f32 v[136:137], v[84:85], v[84:85]\n"
      "v_pk_fma_f32 v[150:151], v[78:79], v[146:147], v[150:151]\nv_pk_mul_f32 v[138:139], v[86:87], v[86:87]\nv_pk_add_f32 v[148:149], v[148:149], v[150:151]\n"
      "v_pk_fma_f32 v[132:133], v[132:133], s[46:47], v[128:129] op_sel_hi:[1,0,0]\nv_add_f32 v164, v148, v149\nv_pk_fma_f32 v[134:135], v[134:135], s[46:47], v[128:129] op_sel_hi:[1,0,0]\n"
      "v_pk_fma_f32 v[136:137], v[136:137], s[46:47], v[128:129] op_sel_hi:[1,0,0]\nv_pk_fma_f32 v[138:139], v[138:139], s[46:47], v[128:129] op_sel_hi:[1,0,0]\n"
      "v_pk_mul_f32 v[132:133], v[132:133], v[80:81]\nv_pk_mul_f32 v[134:135], v[134:135], v[82:83]\nv_pk_mul_f32 v[136:137], v[136:137], v[84:85]\nv_pk_mul_f32 v[138:139], v[138:139], v[86:87]\n"
      "v_pk_mul_f32 v[80:81], v[80:81], v[112:113]\nv_pk_mul_f32 v[82:83], v[82:83], v[114:115]\nv_pk_mul_f32 v[84:85], v[84:85], v[116:117]\nv_pk_mul_f32 v[86:87], v[86:87], v[118:119]\n"
      "v_exp_f32 v132, v132\nv_exp_f32 v133, v133\nv_exp_f32 v134, v134\nv_exp_f32 v135, v135\nv_exp_f32 v136, v136\nv_exp_f32 v137, v137\nv_exp_f32 v138, v138\nv_exp_f32 v139, v139\n"
      "v_pk_add_f32 v[132:133], v[132:133], 1.0 op_sel_hi:[1,0]\nv_pk_add_f32 v[134:135], v[134:135], 1.0 op_sel_hi:[1,0]\nv_pk_add_f32 v[136:137], v[136:137], 1.0 op_sel_hi:[1,0]\n"
      "v_pk_add_f32 v[138:139], v[138:139], 1.0 op_sel_hi:[1,0]\nv_rcp_f32 v132, v132\nv_rcp_f32 v133, v133\nv_rcp_f32 v134, v134\nv_rcp_f32 v135, v135\nv_rcp_f32 v136, v136\nv_rcp_f32 v137, v137\n"
      "v_rcp_f32 v138, v138\nv_rcp_f32 v139, v139\nv_pk_mul_f32 v[148:149], v[80:81], v[132:133]\nv_pk_mul_f32 v[140:141], v[88:89], v[88:89]\nv_pk_mul_f32 v[150:151], v[82:83], v[134:135]\n"
      "v_pk_mul_f32 v[142:143], v[90:91], v[90:91]\nv_pk_fma_f32 v[148:149], v[84:85], v[136:137], v[148:149]\nv_pk_mul_f32 v[144:145], v[92:93], v[92:93]\n"
      "v_pk_fma_f32 v[150:151], v[86:87], v[138:139], v[150:151]\nv_pk_mul_f32 v[146:147], v[94:95], v[94:95]\nv_pk_fma_f32 v[140:141], v[140:141], s[46:47], v[128:129] op_sel_hi:[1,0,0]\n"
      "v_pk_fma_f32 v[142:143], v[142:143], s[46:47], v[128:129] op_sel_hi:[1,0,0]\nv_pk_fma_f32 v[144:145], v[144:145], s[46:47], v[128:129] op_sel_hi:[1,0,0]\n"
      "v_pk_fma_f32 v[146:147], v[146:147], s[46:47], v[128:129] op_sel_hi:[1,0,0]\nv_pk_mul_f32 v[140:141], v[140:141], v[88:89]\nv_pk_mul_f32 v[142:143], v[142:143], v[90:91]\n"
      "v_pk_mul_f32 v[144:145], v[144:145], v[92:93]\nv_pk_mul_f32 v[146:147], v[146:147], v[94:95]\nv_pk_mul_f32 v[88:89], v[88:89], v[120:121]\nv_pk_mul_f32 v[90:91], v[90:91], v[122:123]\n"
      "v_pk_mul_f32 v[92:93], v[92:93], v[124:125]\nv_pk_mul_f32 v[94:95], v[94:95], v[126:127]\nv_exp_f32 v140, v140\nv_exp_f32 v141, v141\nv_exp_f32 v142, v142\nv_exp_f32 v143, v143\n"
      "v_exp_f32 v144, v144\nv_exp_f32 v145, v145\nv_exp_f32 v146, v146\nv_exp_f32 v147, v147\nv_pk_add_f32 v[140:141], v[140:141], 1.0 op_sel_hi:[1,0]\n"
      "v_pk_add_f32 v[142:143], v[142:143], 1.0 op_sel_hi:[1,0]\nv_pk_add_f32 v[144:145], v[144:145], 1.0 op_sel_hi:[1,0]\nv_pk_add_f32 v[146:147], v[146:147], 1.0 op_sel_hi:[1,0]\n"
      "v_rcp_f32 v140, v140\nv_rcp_f32 v141, v141\nv_rcp_f32 v142, v142\nv_rcp_f32 v143, v143\nv_rcp_f32 v144, v144\nv_rcp_f32 v145, v145\nv_rcp_f32 v146, v146\nv_rcp_f32 v147, v147\n"
      "v_pk_fma_f32 v[148:149], v[88:89], v[140:141], v[148:149]\nv_pk_fma_f32 v[150:151], v[90:91], v[142:143], v[150:151]\nv_pk_fma_f32 v[148:149], v[92:93], v[144:145], v[148:149]\n"
      "v_pk_fma_f32 v[150:151], v[94:95], v[146:147], v[150:151]\nv_pk_add_f32 v[148:149], v[148:149], v[150:151]\nv_add_f32 v165, v148, v149\ns_branch .Lge\n"
      ".Lg12:\ns_barrier\nds_read_b128 v[176:179], v210 offset:0\nds_read_b128 v[180:183], v211 offset:0\nds_read_b64_tr_b16 v[112:113], v220 offset:0\n"
      "ds_read_b64_tr_b16 v[114:115], v220 offset:1024\nds_read_b64_tr_b16 v[116:117], v220 offset:8192\nds_read_b64_tr_b16 v[118:119], v220 offset:9216\n"
      "ds_read_b64_tr_b16 v[120:121], v221 offset:0\nds_read_b64_tr_b16 v[122:123], v221 offset:1024\nds_read_b64_tr_b16 v[124:125], v221 offset:8192\n"
      "ds_read_b64_tr_b16 v[126:127], v221 offset:9216\nds_read_b64_tr_b16 v[128:129], v222 offset:0\nds_read_b64_tr_b16 v[130:131], v222 offset:1024\n"
      "ds_read_b64_tr_b16 v[132:133], v222 offset:8192\nds_read_b64_tr_b16 v[134:135], v222 offset:9216\nds_read_b64_tr_b16 v[136:137], v223 offset:0\n"
      "ds_read_b64_tr_b16 v[138:139], v223 offset:1024\nds_read_b64_tr_b16 v[140:141], v223 offset:8192\nds_read_b64_tr_b16 v[142:143], v223 offset:9216\nds_read_b128 v[184:187], v210 offset:2048\n"
      "ds_read_b128 v[188:191], v211 offset:2048\nds_read_b128 v[192:195], v210 offset:4096\nds_read_b128 v[196:199], v211 offset:4096\ns_waitcnt lgkmcnt(0)\n"
      "ds_read_b128 v[200:203], v210 offset:6144\nds_read_b128 v[204:207], v211 offset:6144\n.p2align 3\n"
      ".Lgl12:\nv_mfma_f32_16x16x32_bf16 v[0:3], v[112:115], v[176:179], v[0:3]\nv_mfma_f32_16x16x32_bf16 v[0:3], v[116:119], v[180:183], v[0:3]\n"
      "v_mfma_f32_16x16x32_bf16 v[4:7], v[124:127], v[180:183], v[4:7]\nv_mfma_f32_16x16x32_bf16 v[4:7], v[120:123], v[176:179], v[4:7]\n"
      "v_mfma_f32_16x16x32_bf16 v[8:11], v[128:131], v[176:179], v[8:11]\nv_mfma_f32_16x16x32_bf16 v[8:11], v[132:135], v[180:183], v[8:11]\n"
      "v_mfma_f32_16x16x32_bf16 v[12:15], v[140:143], v[180:183], v[12:15]\nv_mfma_f32_16x16x32_bf16 v[12:15], v[136:139], v[176:179], v[12:15]\n"
      "v_mfma_f32_16x16x32_bf16 v[28:31], v[136:139], v[184:187], v[28:31]\nds_read_b128 v[176:179], v210 offset:8192\nv_mfma_f32_16x16x32_bf16 v[28:31], v[140:143], v[188:191], v[28:31]\n"
      "ds_read_b128 v[180:183], v211 offset:8192\nv_mfma_f32_16x16x32_bf16 v[24:27], v[132:135], v[188:191], v[24:27]\nv_mfma_f32_16x16x32_bf16 v[24:27], v[128:131], v[184:187], v[24:27]\n"
      "v_mfma_f32_16x16x32_bf16 v[20:23], v[120:123], v[184:187], v[20:23]\nv_mfma_f32_16x16x32_bf16 v[20:23], v[124:127], v[188:191], v[20:23]\n"
      "v_mfma_f32_16x16x32_bf16 v[16:19], v[116:119], v[188:191], v[16:19]\nv_mfma_f32_16x16x32_bf16 v[16:19], v[112:115], v[184:187], v[16:19]\n"
      "v_mfma_f32_16x16x32_bf16 v[32:35], v[112:115], v[192:195], v[32:35]\nds_read_b128 v[184:187], v210 offset:10240\nv_mfma_f32_16x16x32_bf16 v[32:35], v[116:119], v[196:199], v[32:35]\n"
      "ds_read_b128 v[188:191], v211 offset:10240\nv_mfma_f32_16x16x32_bf16 v[36:39], v[124:127], v[196:199], v[36:39]\nv_mfma_f32_16x16x32_bf16 v[36:39], v[120:123], v[192:195], v[36:39]\n"
      "v_mfma_f32_16x16x32_bf16 v[40:43], v[128:131], v[192:195], v[40:43]\nv_mfma_f32_16x16x32_bf16 v[40:43], v[132:135], v[196:199], v[40:43]\n"
      "v_mfma_f32_16x16x32_bf16 v[44:47], v[140:143], v[196:199], v[44:47]\ns_waitcnt vmcnt(0) lgkmcnt(0)\ns_barrier\nv_mfma_f32_16x16x32_bf16 v[44:47], v[136:139], v[192:195], v[44:47]\n"
      "ds_read_b64_tr_b16 v[144:145], v216 offset:0\nv_mfma_f32_16x16x32_bf16 v[60:63], v[136:139], v[200:203], v[60:63]\nds_read_b128 v[192:195], v208 offset:0\n"
      "v_mfma_f32_16x16x32_bf16 v[60:63], v[140:143], v[204:207], v[60:63]\nds_read_b128 v[196:199], v209 offset:0\nv_mfma_f32_16x16x32_bf16 v[56:59], v[132:135], v[204:207], v[56:59]\n"
      "ds_read_b64_tr_b16 v[146:147], v216 offset:1024\nds_read_b64_tr_b16 v[148:149], v216 offset:8192\nv_mfma_f32_16x16x32_bf16 v[56:59], v[128:131], v[200:203], v[56:59]\n"
      "ds_read_b64_tr_b16 v[150:151], v216 offset:9216\nds_read_b64_tr_b16 v[152:153], v217 offset:0\nv_mfma_f32_16x16x32_bf16 v[52:55], v[120:123], v[200:203], v[52:55]\n"
      "ds_read_b64_tr_b16 v[154:155], v217 offset:1024\nv_mfma_f32_16x16x32_bf16 v[52:55], v[124:127], v[204:207], v[52:55]\nds_read_b64_tr_b16 v[156:157], v217 offset:8192\n"
      "v_mfma_f32_16x16x32_bf16 v[48:51], v[116:119], v[204:207], v[48:51]\nds_read_b64_tr_b16 v[158:159], v217 offset:9216\nv_mfma_f32_16x16x32_bf16 v[48:51], v[112:115], v[200:203], v[48:51]\n"
      "ds_read_b64_tr_b16 v[160:161], v218 offset:0\nv_mfma_f32_16x16x32_bf16 v[64:67], v[112:115], v[176:179], v[64:67]\nds_read_b128 v[200:203], v208 offset:2048\n"
      "v_mfma_f32_16x16x32_bf16 v[64:67], v[116:119], v[180:183], v[64:67]\nds_read_b128 v[204:207], v209 offset:2048\nv_mfma_f32_16x16x32_bf16 v[68:71], v[124:127], v[180:183], v[68:71]\n"
      "ds_read_b64_tr_b16 v[162:163], v218 offset:1024\nv_mfma_f32_16x16x32_bf16 v[68:71], v[120:123], v[176:179], v[68:71]\nds_read_b64_tr_b16 v[164:165], v218 offset:8192\n"
      "v_mfma_f32_16x16x32_bf16 v[72:75], v[128:131], v[176:179], v[72:75]\nds_read_b64_tr_b16 v[166:167], v218 offset:9216\nv_mfma_f32_16x16x32_bf16 v[72:75], v[132:135], v[180:183], v[72:75]\n"
      "ds_read_b64_tr_b16 v[168:169], v219 offset:0\nv_mfma_f32_16x16x32_bf16 v[76:79], v[140:143], v[180:183], v[76:79]\nds_read_b64_tr_b16 v[170:171], v219 offset:1024\n"
      "v_mfma_f32_16x16x32_bf16 v[76:79], v[136:139], v[176:179], v[76:79]\nds_read_b64_tr_b16 v[172:173], v219 offset:8192\nv_mfma_f32_16x16x32_bf16 v[92:95], v[136:139], v[184:187], v[92:95]\n"
      "ds_read_b128 v[176:179], v208 offset:4096\nv_mfma_f32_16x16x32_bf16 v[92:95], v[140:143], v[188:191], v[92:95]\nds_read_b128 v[180:183], v209 offset:4096\n"
      "v_mfma_f32_16x16x32_bf16 v[88:91], v[132:135], v[188:191], v[88:91]\nds_read_b64_tr_b16 v[174:175], v219 offset:9216\nv_mfma_f32_16x16x32_bf16 v[88:91], v[128:131], v[184:187], v[88:91]\n"
      "v_mfma_f32_16x16x32_bf16 v[84:87], v[120:123], v[184:187], v[84:87]\nv_mfma_f32_16x16x32_bf16 v[84:87], v[124:127], v[188:191], v[84:87]\n"
      "v_mfma_f32_16x16x32_bf16 v[80:83], v[116:119], v[188:191], v[80:83]\ns_waitcnt lgkmcnt(0)\nv_mfma_f32_16x16x32_bf16 v[80:83], v[112:115], v[184:187], v[80:83]\n"
      "ds_read_b128 v[184:187], v208 offset:6144\nds_read_b128 v[188:191], v209 offset:6144\nv_mfma_f32_16x16x32_bf16 v[0:3], v[144:147], v[192:195], v[0:3]\n"
      "v_mfma_f32_16x16x32_bf16 v[0:3], v[148:151], v[196:199], v[0:3]\nv_mfma_f32_16x16x32_bf16 v[4:7], v[156:159], v[196:199], v[4:7]\n"
      "v_mfma_f32_16x16x32_bf16 v[4:7], v[152:155], v[192:195], v[4:7]\nv_mfma_f32_16x16x32_bf16 v[8:11], v[160:163], v[192:195], v[8:11]\n"
      "v_mfma_f32_16x16x32_bf16 v[8:11], v[164:167], v[196:199], v[8:11]\nv_mfma_f32_16x16x32_bf16 v[12:15], v[172:175], v[196:199], v[12:15]\n"
      "v_mfma_f32_16x16x32_bf16 v[12:15], v[168:171], v[192:195], v[12:15]\nv_mfma_f32_16x16x32_bf16 v[28:31], v[168:171], v[200:203], v[28:31]\nds_read_b128 v[192:195], v208 offset:8192\n"
      "v_mfma_f32_16x16x32_bf16 v[28:31], v[172:175], v[204:207], v[28:31]\nds_read_b128 v[196:199], v209 offset:8192\nv_mfma_f32_16x16x32_bf16 v[24:27], v[164:167], v[204:207], v[24:27]\n"
      "v_mfma_f32_16x16x32_bf16 v[24:27], v[160:163], v[200:203], v[24:27]\nv_mfma_f32_16x16x32_bf16 v[20:23], v[152:155], v[200:203], v[20:23]\n"
      "v_mfma_f32_16x16x32_bf16 v[20:23], v[156:159], v[204:207], v[20:23]\nv_mfma_f32_16x16x32_bf16 v[16:19], v[148:151], v[204:207], v[16:19]\n"
      "v_mfma_f32_16x16x32_bf16 v[16:19], v[144:147], v[200:203], v[16:19]\nv_mfma_f32_16x16x32_bf16 v[32:35], v[144:147], v[176:179], v[32:35]\nds_read_b128 v[200:203], v208 offset:10240\n"
      "v_mfma_f32_16x16x32_bf16 v[32:35], v[148:151], v[180:183], v[32:35]\nds_read_b128 v[204:207], v209 offset:10240\nv_mfma_f32_16x16x32_bf16 v[36:39], v[156:159], v[180:183], v[36:39]\n"
      "v_mfma_f32_16x16x32_bf16 v[36:39], v[152:155], v[176:179], v[36:39]\nv_mfma_f32_16x16x32_bf16 v[40:43], v[160:163], v[176:179], v[40:43]\n"
      "v_mfma_f32_16x16x32_bf16 v[40:43], v[164:167], v[180:183], v[40:43]\nv_mfma_f32_16x16x32_bf16 v[44:47], v[172:175], v[180:183], v[44:47]\ns_waitcnt vmcnt(0) lgkmcnt(0)\ns_barrier\n"
      "v_mfma_f32_16x16x32_bf16 v[44:47], v[168:171], v[176:179], v[44:47]\nds_read_b64_tr_b16 v[112:113], v216 offset:45056\nv_mfma_f32_16x16x32_bf16 v[60:63], v[168:171], v[184:187], v[60:63]\n"
      "ds_read_b128 v[176:179], v208 offset:45056\nv_mfma_f32_16x16x32_bf16 v[60:63], v[172:175], v[188:191], v[60:63]\nds_read_b128 v[180:183], v209 offset:45056\n"
      "v_mfma_f32_16x16x32_bf16 v[56:59], v[164:167], v[188:191], v[56:59]\nds_read_b64_tr_b16 v[114:115], v216 offset:46080\nds_read_b64_tr_b16 v[116:117], v216 offset:53248\n"
      "v_mfma_f32_16x16x32_bf16 v[56:59], v[160:163], v[184:187], v[56:59]\nds_read_b64_tr_b16 v[118:119], v216 offset:54272\nds_read_b64_tr_b16 v[120:121], v217 offset:45056\n"
      "v_mfma_f32_16x16x32_bf16 v[52:55], v[152:155], v[184:187], v[52:55]\nds_read_b64_tr_b16 v[122:123], v217 offset:46080\nv_mfma_f32_16x16x32_bf16 v[52:55], v[156:159], v[188:191], v[52:55]\n"
      "ds_read_b64_tr_b16 v[124:125], v217 offset:53248\nv_mfma_f32_16x16x32_bf16 v[48:51], v[148:151], v[188:191], v[48:51]\nds_read_b64_tr_b16 v[126:127], v217 offset:54272\n"
      "v_mfma_f32_16x16x32_bf16 v[48:51], v[144:147], v[184:187], v[48:51]\nds_read_b64_tr_b16 v[128:129], v218 offset:45056\nv_mfma_f32_16x16x32_bf16 v[64:67], v[144:147], v[192:195], v[64:67]\n"
      "ds_read_b128 v[184:187], v208 offset:47104\nv_mfma_f32_16x16x32_bf16 v[64:67], v[148:151], v[196:199], v[64:67]\nds_read_b128 v[188:191], v209 offset:47104\n"
      "v_mfma_f32_16x16x32_bf16 v[68:71], v[156:159], v[196:199], v[68:71]\nds_read_b64_tr_b16 v[130:131], v218 offset:46080\nv_mfma_f32_16x16x32_bf16 v[68:71], v[152:155], v[192:195], v[68:71]\n"
      "ds_read_b64_tr_b16 v[132:133], v218 offset:53248\nv_mfma_f32_16x16x32_bf16 v[72:75], v[160:163], v[192:195], v[72:75]\nds_read_b64_tr_b16 v[134:135], v218 offset:54272\n"
      "v_mfma_f32_16x16x32_bf16 v[72:75], v[164:167], v[196:199], v[72:75]\nds_read_b64_tr_b16 v[136:137], v219 offset:45056\nv_mfma_f32_16x16x32_bf16 v[76:79], v[172:175], v[196:199], v[76:79]\n"
      "ds_read_b64_tr_b16 v[138:139], v219 offset:46080\nv_mfma_f32_16x16x32_bf16 v[76:79], v[168:171], v[192:195], v[76:79]\nds_read_b64_tr_b16 v[140:141], v219 offset:53248\n"
      "v_mfma_f32_16x16x32_bf16 v[92:95], v[168:171], v[200:203], v[92:95]\nds_read_b128 v[192:195], v208 offset:49152\nv_mfma_f32_16x16x32_bf16 v[92:95], v[172:175], v[204:207], v[92:95]\n"
      "ds_read_b128 v[196:199], v209 offset:49152\nv_mfma_f32_16x16x32_bf16 v[88:91], v[164:167], v[204:207], v[88:91]\nds_read_b64_tr_b16 v[142:143], v219 offset:54272\n"
      "v_mfma_f32_16x16x32_bf16 v[88:91], v[160:163], v[200:203], v[88:91]\nv_mfma_f32_16x16x32_bf16 v[84:87], v[152:155], v[200:203], v[84:87]\n"
      "v_mfma_f32_16x16x32_bf16 v[84:87], v[156:159], v[204:207], v[84:87]\nv_mfma_f32_16x16x32_bf16 v[80:83], v[148:151], v[204:207], v[80:83]\ns_waitcnt lgkmcnt(0)\n"
      "v_mfma_f32_16x16x32_bf16 v[80:83], v[144:147], v[200:203], v[80:83]\nds_read_b128 v[200:203], v208 offset:51200\nds_read_b128 v[204:207], v209 offset:51200\n"
      "v_mfma_f32_16x16x32_bf16 v[0:3], v[112:115], v[176:179], v[0:3]\nv_mfma_f32_16x16x32_bf16 v[0:3], v[116:119], v[180:183], v[0:3]\n"
      "v_mfma_f32_16x16x32_bf16 v[4:7], v[124:127], v[180:183], v[4:7]\nv_mfma_f32_16x16x32_bf16 v[4:7], v[120:123], v[176:179], v[4:7]\n"
      "v_mfma_f32_16x16x32_bf16 v[8:11], v[128:131], v[176:179], v[8:11]\nv_mfma_f32_16x16x32_bf16 v[8:11], v[132:135], v[180:183], v[8:11]\n"
      "v_mfma_f32_16x16x32_bf16 v[12:15], v[140:143], v[180:183], v[12:15]\nv_mfma_f32_16x16x32_bf16 v[12:15], v[136:139], v[176:179], v[12:15]\n"
      "v_mfma_f32_16x16x32_bf16 v[28:31], v[136:139], v[184:187], v[28:31]\nds_read_b128 v[176:179], v208 offset:53248\nv_mfma_f32_16x16x32_bf16 v[28:31], v[140:143], v[188:191], v[28:31]\n"
      "ds_read_b128 v[180:183], v209 offset:53248\nv_mfma_f32_16x16x32_bf16 v[24:27], v[132:135], v[188:191], v[24:27]\nv_mfma_f32_16x16x32_bf16 v[24:27], v[128:131], v[184:187], v[24:27]\n"
      "v_mfma_f32_16x16x32_bf16 v[20:23], v[120:123], v[184:187], v[20:23]\nv_mfma_f32_16x16x32_bf16 v[20:23], v[124:127], v[188:191], v[20:23]\n"
      "v_mfma_f32_16x16x32_bf16 v[16:19], v[116:119], v[188:191], v[16:19]\nv_mfma_f32_16x16x32_bf16 v[16:19], v[112:115], v[184:187], v[16:19]\n"
      "v_mfma_f32_16x16x32_bf16 v[32:35], v[112:115], v[192:195], v[32:35]\nds_read_b128 v[184:187], v208 offset:55296\nv_mfma_f32_16x16x32_bf16 v[32:35], v[116:119], v[196:199], v[32:35]\n"
      "ds_read_b128 v[188:191], v209 offset:55296\nv_mfma_f32_16x16x32_bf16 v[36:39], v[124:127], v[196:199], v[36:39]\nv_mfma_f32_16x16x32_bf16 v[36:39], v[120:123], v[192:195], v[36:39]\n"
      "v_mfma_f32_16x16x32_bf16 v[40:43], v[128:131], v[192:195], v[40:43]\nv_mfma_f32_16x16x32_bf16 v[40:43], v[132:135], v[196:199], v[40:43]\n"
      "v_mfma_f32_16x16x32_bf16 v[44:47], v[140:143], v[196:199], v[44:47]\ns_waitcnt vmcnt(0) lgkmcnt(0)\ns_barrier\nv_mfma_f32_16x16x32_bf16 v[44:47], v[136:139], v[192:195], v[44:47]\n"
      "ds_read_b64_tr_b16 v[144:145], v220 offset:0\nv_mfma_f32_16x16x32_bf16 v[60:63], v[136:139], v[200:203], v[60:63]\nds_read_b128 v[192:195], v210 offset:0\n"
      "v_mfma_f32_16x16x32_bf16 v[60:63], v[140:143], v[204:207], v[60:63]\nds_read_b128 v[196:199], v211 offset:0\nv_mfma_f32_16x16x32_bf16 v[56:59], v[132:135], v[204:207], v[56:59]\n"
      "ds_read_b64_tr_b16 v[146:147], v220 offset:1024\nds_read_b64_tr_b16 v[148:149], v220 offset:8192\nv_mfma_f32_16x16x32_bf16 v[56:59], v[128:131], v[200:203], v[56:59]\n"
      "ds_read_b64_tr_b16 v[150:151], v220 offset:9216\nds_read_b64_tr_b16 v[152:153], v221 offset:0\nv_mfma_f32_16x16x32_bf16 v[52:55], v[120:123], v[200:203], v[52:55]\n"
      "ds_read_b64_tr_b16 v[154:155], v221 offset:1024\nv_mfma_f32_16x16x32_bf16 v[52:55], v[124:127], v[204:207], v[52:55]\nds_read_b64_tr_b16 v[156:157], v221 offset:8192\n"
      "v_mfma_f32_16x16x32_bf16 v[48:51], v[116:119], v[204:207], v[48:51]\nds_read_b64_tr_b16 v[158:159], v221 offset:9216\nv_mfma_f32_16x16x32_bf16 v[48:51], v[112:115], v[200:203], v[48:51]\n"
      "ds_read_b64_tr_b16 v[160:161], v222 offset:0\nv_mfma_f32_16x16x32_bf16 v[64:67], v[112:115], v[176:179], v[64:67]\nds_read_b128 v[200:203], v210 offset:2048\n"
      "v_mfma_f32_16x16x32_bf16 v[64:67], v[116:119], v[180:183], v[64:67]\nds_read_b128 v[204:207], v211 offset:2048\nv_mfma_f32_16x16x32_bf16 v[68:71], v[124:127], v[180:183], v[68:71]\n"
      "ds_read_b64_tr_b16 v[162:163], v222 offset:1024\nv_mfma_f32_16x16x32_bf16 v[68:71], v[120:123], v[176:179], v[68:71]\nds_read_b64_tr_b16 v[164:165], v222 offset:8192\n"
      "v_mfma_f32_16x16x32_bf16 v[72:75], v[128:131], v[176:179], v[72:75]\nds_read_b64_tr_b16 v[166:167], v222 offset:9216\nv_mfma_f32_16x16x32_bf16 v[72:75], v[132:135], v[180:183], v[72:75]\n"
      "ds_read_b64_tr_b16 v[168:169], v223 offset:0\nv_mfma_f32_16x16x32_bf16 v[76:79], v[140:143], v[180:183], v[76:79]\nds_read_b64_tr_b16 v[170:171], v223 offset:1024\n"
      "v_mfma_f32_16x16x32_bf16 v[76:79], v[136:139], v[176:179], v[76:79]\nds_read_b64_tr_b16 v[172:173], v223 offset:8192\nv_mfma_f32_16x16x32_bf16 v[92:95], v[136:139], v[184:187], v[92:95]\n"
      "ds_read_b128 v[176:179], v210 offset:4096\nv_mfma_f32_16x16x32_bf16 v[92:95], v[140:143], v[188:191], v[92:95]\nds_read_b128 v[180:183], v211 offset:4096\n"
      "v_mfma_f32_16x16x32_bf16 v[88:91], v[132:135], v[188:191], v[88:91]\nds_read_b64_tr_b16 v[174:175], v223 offset:9216\nv_mfma_f32_16x16x32_bf16 v[88:91], v[128:131], v[184:187], v[88:91]\n"
      "v_mfma_f32_16x16x32_bf16 v[84:87], v[120:123], v[184:187], v[84:87]\nv_mfma_f32_16x16x32_bf16 v[84:87], v[124:127], v[188:191], v[84:87]\n"
      "v_mfma_f32_16x16x32_bf16 v[80:83], v[116:119], v[188:191], v[80:83]\ns_waitcnt lgkmcnt(0)\nv_mfma_f32_16x16x32_bf16 v[80:83], v[112:115], v[184:187], v[80:83]\n"
      "ds_read_b128 v[184:187], v210 offset:6144\nds_read_b128 v[188:191], v211 offset:6144\nv_mfma_f32_16x16x32_bf16 v[0:3], v[144:147], v[192:195], v[0:3]\n"
      "v_mfma_f32_16x16x32_bf16 v[0:3], v[148:151], v[196:199], v[0:3]\nv_mfma_f32_16x16x32_bf16 v[4:7], v[156:159], v[196:199], v[4:7]\n"
      "v_mfma_f32_16x16x32_bf16 v[4:7], v[152:155], v[192:195], v[4:7]\nv_mfma_f32_16x16x32_bf16 v[8:11], v[160:163], v[192:195], v[8:11]\n"
      "v_mfma_f32_16x16x32_bf16 v[8:11], v[164:167], v[196:199], v[8:11]\nv_mfma_f32_16x16x32_bf16 v[12:15], v[172:175], v[196:199], v[12:15]\n"
      "v_mfma_f32_16x16x32_bf16 v[12:15], v[168:171], v[192:195], v[12:15]\nv_mfma_f32_16x16x32_bf16 v[28:31], v[168:171], v[200:203], v[28:31]\nds_read_b128 v[192:195], v210 offset:8192\n"
      "v_mfma_f32_16x16x32_bf16 v[28:31], v[172:175], v[204:207], v[28:31]\nds_read_b128 v[196:199], v211 offset:8192\nv_mfma_f32_16x16x32_bf16 v[24:27], v[164:167], v[204:207], v[24:27]\n"
      "v_mfma_f32_16x16x32_bf16 v[24:27], v[160:163], v[200:203], v[24:27]\nv_mfma_f32_16x16x32_bf16 v[20:23], v[152:155], v[200:203], v[20:23]\n"
      "v_mfma_f32_16x16x32_bf16 v[20:23], v[156:159], v[204:207], v[20:23]\nv_mfma_f32_16x16x32_bf16 v[16:19], v[148:151], v[204:207], v[16:19]\n"
      "v_mfma_f32_16x16x32_bf16 v[16:19], v[144:147], v[200:203], v[16:19]\nv_mfma_f32_16x16x32_bf16 v[32:35], v[144:147], v[176:179], v[32:35]\nds_read_b128 v[200:203], v210 offset:10240\n"
      "v_mfma_f32_16x16x32_bf16 v[32:35], v[148:151], v[180:183], v[32:35]\nds_read_b128 v[204:207], v211 offset:10240\nv_mfma_f32_16x16x32_bf16 v[36:39], v[156:159], v[180:183], v[36:39]\n"
      "v_mfma_f32_16x16x32_bf16 v[36:39], v[152:155], v[176:179], v[36:39]\nv_mfma_f32_16x16x32_bf16 v[40:43], v[160:163], v[176:179], v[40:43]\n"
      "v_mfma_f32_16x16x32_bf16 v[40:43], v[164:167], v[180:183], v[40:43]\nv_mfma_f32_16x16x32_bf16 v[44:47], v[172:175], v[180:183], v[44:47]\ns_waitcnt vmcnt(0) lgkmcnt(0)\ns_barrier\n"
      "v_mfma_f32_16x16x32_bf16 v[44:47], v[168:171], v[176:179], v[44:47]\nds_read_b64_tr_b16 v[112:113], v216 offset:0\nv_mfma_f32_16x16x32_bf16 v[60:63], v[168:171], v[184:187], v[60:63]\n"
      "ds_read_b128 v[176:179], v208 offset:0\nv_mfma_f32_16x16x32_bf16 v[60:63], v[172:175], v[188:191], v[60:63]\nds_read_b128 v[180:183], v209 offset:0\n"
      "v_mfma_f32_16x16x32_bf16 v[56:59], v[164:167], v[188:191], v[56:59]\nds_read_b64_tr_b16 v[114:115], v216 offset:1024\nds_read_b64_tr_b16 v[116:117], v216 offset:8192\n"
      "v_mfma_f32_16x16x32_bf16 v[56:59], v[160:163], v[184:187], v[56:59]\nds_read_b64_tr_b16 v[118:119], v216 offset:9216\nds_read_b64_tr_b16 v[120:121], v217 offset:0\n"
      "v_mfma_f32_16x16x32_bf16 v[52:55], v[152:155], v[184:187], v[52:55]\nds_read_b64_tr_b16 v[122:123], v217 offset:1024\nv_mfma_f32_16x16x32_bf16 v[52:55], v[156:159], v[188:191], v[52:55]\n"
      "ds_read_b64_tr_b16 v[124:125], v217 offset:8192\nv_mfma_f32_16x16x32_bf16 v[48:51], v[148:151], v[188:191], v[48:51]\nds_read_b64_tr_b16 v[126:127], v217 offset:9216\n"
      "v_mfma_f32_16x16x32_bf16 v[48:51], v[144:147], v[184:187], v[48:51]\nds_read_b64_tr_b16 v[128:129], v218 offset:0\nv_mfma_f32_16x16x32_bf16 v[64:67], v[144:147], v[192:195], v[64:67]\n"
      "ds_read_b128 v[184:187], v208 offset:2048\nv_mfma_f32_16x16x32_bf16 v[64:67], v[148:151], v[196:199], v[64:67]\nds_read_b128 v[188:191], v209 offset:2048\n"
      "v_mfma_f32_16x16x32_bf16 v[68:71], v[156:159], v[196:199], v[68:71]\nds_read_b64_tr_b16 v[130:131], v218 offset:1024\nv_mfma_f32_16x16x32_bf16 v[68:71], v[152:155], v[192:195], v[68:71]\n"
      "ds_read_b64_tr_b16 v[132:133], v218 offset:8192\nv_mfma_f32_16x16x32_bf16 v[72:75], v[160:163], v[192:195], v[72:75]\nds_read_b64_tr_b16 v[134:135], v218 offset:9216\n"
      "v_mfma_f32_16x16x32_bf16 v[72:75], v[164:167], v[196:199], v[72:75]\nds_read_b64_tr_b16 v[136:137], v219 offset:0\nv_mfma_f32_16x16x32_bf16 v[76:79], v[172:175], v[196:199], v[76:79]\n"
      "ds_read_b64_tr_b16 v[138:139], v219 offset:1024\nv_mfma_f32_16x16x32_bf16 v[76:79], v[168:171], v[192:195], v[76:79]\nds_read_b64_tr_b16 v[140:141], v219 offset:8192\n"
      "v_mfma_f32_16x16x32_bf16 v[92:95], v[168:171], v[200:203], v[92:95]\nds_read_b128 v[192:195], v208 offset:4096\nv_mfma_f32_16x16x32_bf16 v[92:95], v[172:175], v[204:207], v[92:95]\n"
      "ds_read_b128 v[196:199], v209 offset:4096\nv_mfma_f32_16x16x32_bf16 v[88:91], v[164:167], v[204:207], v[88:91]\nds_read_b64_tr_b16 v[142:143], v219 offset:9216\n"
      "v_mfma_f32_16x16x32_bf16 v[88:91], v[160:163], v[200:203], v[88:91]\nv_mfma_f32_16x16x32_bf16 v[84:87], v[152:155], v[200:203], v[84:87]\n"
      "v_mfma_f32_16x16x32_bf16 v[84:87], v[156:159], v[204:207], v[84:87]\nv_mfma_f32_16x16x32_bf16 v[80:83], v[148:151], v[204:207], v[80:83]\ns_waitcnt lgkmcnt(0)\n"
      "v_mfma_f32_16x16x32_bf16 v[80:83], v[144:147], v[200:203], v[80:83]\nds_read_b128 v[200:203], v208 offset:6144\nds_read_b128 v[204:207], v209 offset:6144\n"
      "v_mfma_f32_16x16x32_bf16 v[0:3], v[112:115], v[176:179], v[0:3]\nv_mfma_f32_16x16x32_bf16 v[0:3], v[116:119], v[180:183], v[0:3]\n"
      "v_mfma_f32_16x16x32_bf16 v[4:7], v[124:127], v[180:183], v[4:7]\nv_mfma_f32_16x16x32_bf16 v[4:7], v[120:123], v[176:179], v[4:7]\n"
      "v_mfma_f32_16x16x32_bf16 v[8:11], v[128:131], v[176:179], v[8:11]\nv_mfma_f32_16x16x32_bf16 v[8:11], v[132:135], v[180:183], v[8:11]\n"
      "v_mfma_f32_16x16x32_bf16 v[12:15], v[140:143], v[180:183], v[12:15]\nv_mfma_f32_16x16x32_bf16 v[12:15], v[136:139], v[176:179], v[12:15]\n"
      "v_mfma_f32_16x16x32_bf16 v[28:31], v[136:139], v[184:187], v[28:31]\nds_read_b128 v[176:179], v208 offset:8192\nv_mfma_f32_16x16x32_bf16 v[28:31], v[140:143], v[188:191], v[28:31]\n"
      "ds_read_b128 v[180:183], v209 offset:8192\nv_mfma_f32_16x16x32_bf16 v[24:27], v[132:135], v[188:191], v[24:27]\nv_mfma_f32_16x16x32_bf16 v[24:27], v[128:131], v[184:187], v[24:27]\n"
      "v_mfma_f32_16x16x32_bf16 v[20:23], v[120:123], v[184:187], v[20:23]\nv_mfma_f32_16x16x32_bf16 v[20:23], v[124:127], v[188:191], v[20:23]\n"
      "v_mfma_f32_16x16x32_bf16 v[16:19], v[116:119], v[188:191], v[16:19]\nv_mfma_f32_16x16x32_bf16 v[16:19], v[112:115], v[184:187], v[16:19]\n"
      "v_mfma_f32_16x16x32_bf16 v[32:35], v[112:115], v[192:195], v[32:35]\nds_read_b128 v[184:187], v208 offset:10240\nv_mfma_f32_16x16x32_bf16 v[32:35], v[116:119], v[196:199], v[32:35]\n"
      "ds_read_b128 v[188:191], v209 offset:10240\nv_mfma_f32_16x16x32_bf16 v[36:39], v[124:127], v[196:199], v[36:39]\nv_mfma_f32_16x16x32_bf16 v[36:39], v[120:123], v[192:195], v[36:39]\n"
      "v_mfma_f32_16x16x32_bf16 v[40:43], v[128:131], v[192:195], v[40:43]\nv_mfma_f32_16x16x32_bf16 v[40:43], v[132:135], v[196:199], v[40:43]\n"
      "v_mfma_f32_16x16x32_bf16 v[44:47], v[140:143], v[196:199], v[44:47]\ns_waitcnt vmcnt(0) lgkmcnt(0)\ns_barrier\nv_mfma_f32_16x16x32_bf16 v[44:47], v[136:139], v[192:195], v[44:47]\n"
      "ds_read_b64_tr_b16 v[144:145], v216 offset:45056\nv_mfma_f32_16x16x32_bf16 v[60:63], v[136:139], v[200:203], v[60:63]\nds_read_b128 v[192:195], v208 offset:45056\n"
      "v_mfma_f32_16x16x32_bf16 v[60:63], v[140:143], v[204:207], v[60:63]\nds_read_b128 v[196:199], v209 offset:45056\nv_mfma_f32_16x16x32_bf16 v[56:59], v[132:135], v[204:207], v[56:59]\n"
      "ds_read_b64_tr_b16 v[146:147], v216 offset:46080\nds_read_b64_tr_b16 v[148:149], v216 offset:53248\nv_mfma_f32_16x16x32_bf16 v[56:59], v[128:131], v[200:203], v[56:59]\n"
      "ds_read_b64_tr_b16 v[150:151], v216 offset:54272\nds_read_b64_tr_b16 v[152:153], v217 offset:45056\nv_mfma_f32_16x16x32_bf16 v[52:55], v[120:123], v[200:203], v[52:55]\n"
      "ds_read_b64_tr_b16 v[154:155], v217 offset:46080\nv_mfma_f32_16x16x32_bf16 v[52:55], v[124:127], v[204:207], v[52:55]\nds_read_b64_tr_b16 v[156:157], v217 offset:53248\n"
      "v_mfma_f32_16x16x32_bf16 v[48:51], v[116:119], v[204:207], v[48:51]\nds_read_b64_tr_b16 v[158:159], v217 offset:54272\nv_mfma_f32_16x16x32_bf16 v[48:51], v[112:115], v[200:203], v[48:51]\n"
      "ds_read_b64_tr_b16 v[160:161], v218 offset:45056\nv_mfma_f32_16x16x32_bf16 v[64:67], v[112:115], v[176:179], v[64:67]\nds_read_b128 v[200:203], v208 offset:47104\n"
      "v_mfma_f32_16x16x32_bf16 v[64:67], v[116:119], v[180:183], v[64:67]\nds_read_b128 v[204:207], v209 offset:47104\nv_mfma_f32_16x16x32_bf16 v[68:71], v[124:127], v[180:183], v[68:71]\n"
      "ds_read_b64_tr_b16 v[162:163], v218 offset:46080\nv_mfma_f32_16x16x32_bf16 v[68:71], v[120:123], v[176:179], v[68:71]\nds_read_b64_tr_b16 v[164:165], v218 offset:53248\n"
      "v_mfma_f32_16x16x32_bf16 v[72:75], v[128:131], v[176:179], v[72:75]\nds_read_b64_tr_b16 v[166:167], v218 offset:54272\nv_mfma_f32_16x16x32_bf16 v[72:75], v[132:135], v[180:183], v[72:75]\n"
      "ds_read_b64_tr_b16 v[168:169], v219 offset:45056\nv_mfma_f32_16x16x32_bf16 v[76:79], v[140:143], v[180:183], v[76:79]\nds_read_b64_tr_b16 v[170:171], v219 offset:46080\n"
      "v_mfma_f32_16x16x32_bf16 v[76:79], v[136:139], v[176:179], v[76:79]\nds_read_b64_tr_b16 v[172:173], v219 offset:53248\nv_mfma_f32_16x16x32_bf16 v[92:95], v[136:139], v[184:187], v[92:95]\n"
      "ds_read_b128 v[176:179], v208 offset:49152\nv_mfma_f32_16x16x32_bf16 v[92:95], v[140:143], v[188:191], v[92:95]\nds_read_b128 v[180:183], v209 offset:49152\n"
      "v_mfma_f32_16x16x32_bf16 v[88:91], v[132:135], v[188:191], v[88:91]\nds_read_b64_tr_b16 v[174:175], v219 offset:54272\nv_mfma_f32_16x16x32_bf16 v[88:91], v[128:131], v[184:187], v[88:91]\n"
      "v_mfma_f32_16x16x32_bf16 v[84:87], v[120:123], v[184:187], v[84:87]\nv_mfma_f32_16x16x32_bf16 v[84:87], v[124:127], v[188:191], v[84:87]\n"
      "v_mfma_f32_16x16x32_bf16 v[80:83], v[116:119], v[188:191], v[80:83]\ns_waitcnt lgkmcnt(0)\nv_mfma_f32_16x16x32_bf16 v[80:83], v[112:115], v[184:187], v[80:83]\n"
      "ds_read_b128 v[184:187], v208 offset:51200\nds_read_b128 v[188:191], v209 offset:51200\nv_mfma_f32_16x16x32_bf16 v[0:3], v[144:147], v[192:195], v[0:3]\n"
      "v_mfma_f32_16x16x32_bf16 v[0:3], v[148:151], v[196:199], v[0:3]\nv_mfma_f32_16x16x32_bf16 v[4:7], v[156:159], v[196:199], v[4:7]\n"
      "v_mfma_f32_16x16x32_bf16 v[4:7], v[152:155], v[192:195], v[4:7]\nv_mfma_f32_16x16x32_bf16 v[8:11], v[160:163], v[192:195], v[8:11]\n"
      "v_mfma_f32_16x16x32_bf16 v[8:11], v[164:167], v[196:199], v[8:11]\nv_mfma_f32_16x16x32_bf16 v[12:15], v[172:175], v[196:199], v[12:15]\n"
      "v_mfma_f32_16x16x32_bf16 v[12:15], v[168:171], v[192:195], v[12:15]\nv_mfma_f32_16x16x32_bf16 v[28:31], v[168:171], v[200:203], v[28:31]\nds_read_b128 v[192:195], v208 offset:53248\n"
      "v_mfma_f32_16x16x32_bf16 v[28:31], v[172:175], v[204:207], v[28:31]\nds_read_b128 v[196:199], v209 offset:53248\nv_mfma_f32_16x16x32_bf16 v[24:27], v[164:167], v[204:207], v[24:27]\n"
      "v_mfma_f32_16x16x32_bf16 v[24:27], v[160:163], v[200:203], v[24:27]\nv_mfma_f32_16x16x32_bf16 v[20:23], v[152:155], v[200:203], v[20:23]\n"
      "v_mfma_f32_16x16x32_bf16 v[20:23], v[156:159], v[204:207], v[20:23]\nv_mfma_f32_16x16x32_bf16 v[16:19], v[148:151], v[204:207], v[16:19]\n"
      "v_mfma_f32_16x16x32_bf16 v[16:19], v[144:147], v[200:203], v[16:19]\nv_mfma_f32_16x16x32_bf16 v[32:35], v[144:147], v[176:179], v[32:35]\nds_read_b128 v[200:203], v208 offset:55296\n"
      "v_mfma_f32_16x16x32_bf16 v[32:35], v[148:151], v[180:183], v[32:35]\nds_read_b128 v[204:207], v209 offset:55296\nv_mfma_f32_16x16x32_bf16 v[36:39], v[156:159], v[180:183], v[36:39]\n"
      "v_mfma_f32_16x16x32_bf16 v[36:39], v[152:155], v[176:179], v[36:39]\nv_mfma_f32_16x16x32_bf16 v[40:43], v[160:163], v[176:179], v[40:43]\n"
      "v_mfma_f32_16x16x32_bf16 v[40:43], v[164:167], v[180:183], v[40:43]\nv_mfma_f32_16x16x32_bf16 v[44:47], v[172:175], v[180:183], v[44:47]\ns_waitcnt vmcnt(0) lgkmcnt(0)\ns_barrier\n"
      "v_mfma_f32_16x16x32_bf16 v[44:47], v[168:171], v[176:179], v[44:47]\nds_read_b64_tr_b16 v[112:113], v220 offset:0\nv_mfma_f32_16x16x32_bf16 v[60:63], v[168:171], v[184:187], v[60:63]\n"
      "ds_read_b128 v[176:179], v210 offset:0\nv_mfma_f32_16x16x32_bf16 v[60:63], v[172:175], v[188:191], v[60:63]\nds_read_b128 v[180:183], v211 offset:0\n"
      "v_mfma_f32_16x16x32_bf16 v[56:59], v[164:167], v[188:191], v[56:59]\nds_read_b64_tr_b16 v[114:115], v220 offset:1024\nds_read_b64_tr_b16 v[116:117], v220 offset:8192\n"
      "v_mfma_f32_16x16x32_bf16 v[56:59], v[160:163], v[184:187], v[56:59]\nds_read_b64_tr_b16 v[118:119], v220 offset:9216\nds_read_b64_tr_b16 v[120:121], v221 offset:0\n"
      "v_mfma_f32_16x16x32_bf16 v[52:55], v[152:155], v[184:187], v[52:55]\nds_read_b64_tr_b16 v[122:123], v221 offset:1024\nv_mfma_f32_16x16x32_bf16 v[52:55], v[156:159], v[188:191], v[52:55]\n"
      "ds_read_b64_tr_b16 v[124:125], v221 offset:8192\nv_mfma_f32_16x16x32_bf16 v[48:51], v[148:151], v[188:191], v[48:51]\nds_read_b64_tr_b16 v[126:127], v221 offset:9216\n"
      "v_mfma_f32_16x16x32_bf16 v[48:51], v[144:147], v[184:187], v[48:51]\nds_read_b64_tr_b16 v[128:129], v222 offset:0\nv_mfma_f32_16x16x32_bf16 v[64:67], v[144:147], v[192:195], v[64:67]\n"
      "ds_read_b128 v[184:187], v210 offset:2048\nv_mfma_f32_16x16x32_bf16 v[64:67], v[148:151], v[196:199], v[64:67]\nds_read_b128 v[188:191], v211 offset:2048\n"
      "v_mfma_f32_16x16x32_bf16 v[68:71], v[156:159], v[196:199], v[68:71]\nds_read_b64_tr_b16 v[130:131], v222 offset:1024\nv_mfma_f32_16x16x32_bf16 v[68:71], v[152:155], v[192:195], v[68:71]\n"
      "ds_read_b64_tr_b16 v[132:133], v222 offset:8192\nv_mfma_f32_16x16x32_bf16 v[72:75], v[160:163], v[192:195], v[72:75]\nds_read_b64_tr_b16 v[134:135], v222 offset:9216\n"
      "v_mfma_f32_16x16x32_bf16 v[72:75], v[164:167], v[196:199], v[72:75]\nds_read_b64_tr_b16 v[136:137], v223 offset:0\nv_mfma_f32_16x16x32_bf16 v[76:79], v[172:175], v[196:199], v[76:79]\n"
      "ds_read_b64_tr_b16 v[138:139], v223 offset:1024\nv_mfma_f32_16x16x32_bf16 v[76:79], v[168:171], v[192:195], v[76:79]\nds_read_b64_tr_b16 v[140:141], v223 offset:8192\n"
      "v_mfma_f32_16x16x32_bf16 v[92:95], v[168:171], v[200:203], v[92:95]\nds_read_b128 v[192:195], v210 offset:4096\nv_mfma_f32_16x16x32_bf16 v[92:95], v[172:175], v[204:207], v[92:95]\n"
      "ds_read_b128 v[196:199], v211 offset:4096\nv_mfma_f32_16x16x32_bf16 v[88:91], v[164:167], v[204:207], v[88:91]\nds_read_b64_tr_b16 v[142:143], v223 offset:9216\n"
      "v_mfma_f32_16x16x32_bf16 v[88:91], v[160:163], v[200:203], v[88:91]\nv_mfma_f32_16x16x32_bf16 v[84:87], v[152:155], v[200:203], v[84:87]\ns_sub_u32 s46, s46, 1\ns_cmp_lg_u32 s46, 0\n"
      "v_mfma_f32_16x16x32_bf16 v[84:87], v[156:159], v[204:207], v[84:87]\nv_mfma_f32_16x16x32_bf16 v[80:83], v[148:151], v[204:207], v[80:83]\ns_waitcnt lgkmcnt(0)\n"
      "v_mfma_f32_16x16x32_bf16 v[80:83], v[144:147], v[200:203], v[80:83]\nds_read_b128 v[200:203], v210 offset:6144\nds_read_b128 v[204:207], v211 offset:6144\ns_cbranch_scc1 .Lgl12\n"
      "v_mfma_f32_16x16x32_bf16 v[0:3], v[112:115], v[176:179], v[0:3]\nv_mfma_f32_16x16x32_bf16 v[0:3], v[116:119], v[180:183], v[0:3]\n"
      "v_mfma_f32_16x16x32_bf16 v[4:7], v[124:127], v[180:183], v[4:7]\nv_mfma_f32_16x16x32_bf16 v[4:7], v[120:123], v[176:179], v[4:7]\n"
      "v_mfma_f32_16x16x32_bf16 v[8:11], v[128:131], v[176:179], v[8:11]\nv_mfma_f32_16x16x32_bf16 v[8:11], v[132:135], v[180:183], v[8:11]\n"
      "v_mfma_f32_16x16x32_bf16 v[12:15], v[140:143], v[180:183], v[12:15]\nv_mfma_f32_16x16x32_bf16 v[12:15], v[136:139], v[176:179], v[12:15]\n"
      "v_mfma_f32_16x16x32_bf16 v[28:31], v[136:139], v[184:187], v[28:31]\nds_read_b128 v[176:179], v210 offset:8192\nv_mfma_f32_16x16x32_bf16 v[28:31], v[140:143], v[188:191], v[28:31]\n"
      "ds_read_b128 v[180:183], v211 offset:8192\nv_mfma_f32_16x16x32_bf16 v[24:27], v[132:135], v[188:191], v[24:27]\nv_mfma_f32_16x16x32_bf16 v[24:27], v[128:131], v[184:187], v[24:27]\n"
      "v_mfma_f32_16x16x32_bf16 v[20:23], v[120:123], v[184:187], v[20:23]\nv_mfma_f32_16x16x32_bf16 v[20:23], v[124:127], v[188:191], v[20:23]\n"
      "v_mfma_f32_16x16x32_bf16 v[16:19], v[116:119], v[188:191], v[16:19]\nv_mfma_f32_16x16x32_bf16 v[16:19], v[112:115], v[184:187], v[16:19]\n"
      "v_mfma_f32_16x16x32_bf16 v[32:35], v[112:115], v[192:195], v[32:35]\nds_read_b128 v[184:187], v210 offset:10240\nv_mfma_f32_16x16x32_bf16 v[32:35], v[116:119], v[196:199], v[32:35]\n"
      "ds_read_b128 v[188:191], v211 offset:10240\nv_mfma_f32_16x16x32_bf16 v[36:39], v[124:127], v[196:199], v[36:39]\nv_mfma_f32_16x16x32_bf16 v[36:39], v[120:123], v[192:195], v[36:39]\n"
      "v_mfma_f32_16x16x32_bf16 v[40:43], v[128:131], v[192:195], v[40:43]\nv_mfma_f32_16x16x32_bf16 v[40:43], v[132:135], v[196:199], v[40:43]\n"
      "v_mfma_f32_16x16x32_bf16 v[44:47], v[140:143], v[196:199], v[44:47]\ns_waitcnt vmcnt(0) lgkmcnt(0)\ns_barrier\nv_mfma_f32_16x16x32_bf16 v[44:47], v[136:139], v[192:195], v[44:47]\n"
      "ds_read_b64_tr_b16 v[144:145], v216 offset:0\nv_mfma_f32_16x16x32_bf16 v[60:63], v[136:139], v[200:203], v[60:63]\nds_read_b128 v[192:195], v208 offset:0\n"
      "v_mfma_f32_16x16x32_bf16 v[60:63], v[140:143], v[204:207], v[60:63]\nds_read_b128 v[196:199], v209 offset:0\nv_mfma_f32_16x16x32_bf16 v[56:59], v[132:135], v[204:207], v[56:59]\n"
      "ds_read_b64_tr_b16 v[146:147], v216 offset:1024\nds_read_b64_tr_b16 v[148:149], v216 offset:8192\nv_mfma_f32_16x16x32_bf16 v[56:59], v[128:131], v[200:203], v[56:59]\n"
      "ds_read_b64_tr_b16 v[150:151], v216 offset:9216\nds_read_b64_tr_b16 v[152:153], v217 offset:0\nv_mfma_f32_16x16x32_bf16 v[52:55], v[120:123], v[200:203], v[52:55]\n"
      "ds_read_b64_tr_b16 v[154:155], v217 offset:1024\nv_mfma_f32_16x16x32_bf16 v[52:55], v[124:127], v[204:207], v[52:55]\nds_read_b64_tr_b16 v[156:157], v217 offset:8192\n"
      "v_mfma_f32_16x16x32_bf16 v[48:51], v[116:119], v[204:207], v[48:51]\nds_read_b64_tr_b16 v[158:159], v217 offset:9216\nv_mfma_f32_16x16x32_bf16 v[48:51], v[112:115], v[200:203], v[48:51]\n"
      "ds_read_b64_tr_b16 v[160:161], v218 offset:0\nv_mfma_f32_16x16x32_bf16 v[64:67], v[112:115], v[176:179], v[64:67]\nds_read_b128 v[200:203], v208 offset:2048\n"
      "v_mfma_f32_16x16x32_bf16 v[64:67], v[116:119], v[180:183], v[64:67]\nds_read_b128 v[204:207], v209 offset:2048\nv_mfma_f32_16x16x32_bf16 v[68:71], v[124:127], v[180:183], v[68:71]\n"
      "ds_read_b64_tr_b16 v[162:163], v218 offset:1024\nv_mfma_f32_16x16x32_bf16 v[68:71], v[120:123], v[176:179], v[68:71]\nds_read_b64_tr_b16 v[164:165], v218 offset:8192\n"
      "v_mfma_f32_16x16x32_bf16 v[72:75], v[128:131], v[176:179], v[72:75]\nds_read_b64_tr_b16 v[166:167], v218 offset:9216\nv_mfma_f32_16x16x32_bf16 v[72:75], v[132:135], v[180:183], v[72:75]\n"
      "ds_read_b64_tr_b16 v[168:169], v219 offset:0\nv_mfma_f32_16x16x32_bf16 v[76:79], v[140:143], v[180:183], v[76:79]\nds_read_b64_tr_b16 v[170:171], v219 offset:1024\n"
      "v_mfma_f32_16x16x32_bf16 v[76:79], v[136:139], v[176:179], v[76:79]\nds_read_b64_tr_b16 v[172:173], v219 offset:8192\nv_mfma_f32_16x16x32_bf16 v[92:95], v[136:139], v[184:187], v[92:95]\n"
      "ds_read_b128 v[176:179], v208 offset:4096\nv_mfma_f32_16x16x32_bf16 v[92:95], v[140:143], v[188:191], v[92:95]\nds_read_b128 v[180:183], v209 offset:4096\n"
      "v_mfma_f32_16x16x32_bf16 v[88:91], v[132:135], v[188:191], v[88:91]\nds_read_b64_tr_b16 v[174:175], v219 offset:9216\nv_mfma_f32_16x16x32_bf16 v[88:91], v[128:131], v[184:187], v[88:91]\n"
      "v_mfma_f32_16x16x32_bf16 v[84:87], v[120:123], v[184:187], v[84:87]\nv_mfma_f32_16x16x32_bf16 v[84:87], v[124:127], v[188:191], v[84:87]\n"
      "v_mfma_f32_16x16x32_bf16 v[80:83], v[116:119], v[188:191], v[80:83]\ns_waitcnt lgkmcnt(0)\nv_mfma_f32_16x16x32_bf16 v[80:83], v[112:115], v[184:187], v[80:83]\n"
      "ds_read_b128 v[184:187], v208 offset:6144\nds_read_b128 v[188:191], v209 offset:6144\nv_mfma_f32_16x16x32_bf16 v[0:3], v[144:147], v[192:195], v[0:3]\n"
      "v_mfma_f32_16x16x32_bf16 v[0:3], v[148:151], v[196:199], v[0:3]\nv_mfma_f32_16x16x32_bf16 v[4:7], v[156:159], v[196:199], v[4:7]\n"
      "v_mfma_f32_16x16x32_bf16 v[4:7], v[152:155], v[192:195], v[4:7]\nv_mfma_f32_16x16x32_bf16 v[8:11], v[160:163], v[192:195], v[8:11]\n"
      "v_mfma_f32_16x16x32_bf16 v[8:11], v[164:167], v[196:199], v[8:11]\nv_mfma_f32_16x16x32_bf16 v[12:15], v[172:175], v[196:199], v[12:15]\n"
      "v_mfma_f32_16x16x32_bf16 v[12:15], v[168:171], v[192:195], v[12:15]\nv_mfma_f32_16x16x32_bf16 v[28:31], v[168:171], v[200:203], v[28:31]\nds_read_b128 v[192:195], v208 offset:8192\n"
      "v_mfma_f32_16x16x32_bf16 v[28:31], v[172:175], v[204:207], v[28:31]\nds_read_b128 v[196:199], v209 offset:8192\nv_mfma_f32_16x16x32_bf16 v[24:27], v[164:167], v[204:207], v[24:27]\n"
      "v_mfma_f32_16x16x32_bf16 v[24:27], v[160:163], v[200:203], v[24:27]\nv_mfma_f32_16x16x32_bf16 v[20:23], v[152:155], v[200:203], v[20:23]\n"
      "v_mfma_f32_16x16x32_bf16 v[20:23], v[156:159], v[204:207], v[20:23]\nv_mfma_f32_16x16x32_bf16 v[16:19], v[148:151], v[204:207], v[16:19]\n"
      "v_mfma_f32_16x16x32_bf16 v[16:19], v[144:147], v[200:203], v[16:19]\nv_mfma_f32_16x16x32_bf16 v[32:35], v[144:147], v[176:179], v[32:35]\nds_read_b128 v[200:203], v208 offset:10240\n"
      "v_mfma_f32_16x16x32_bf16 v[32:35], v[148:151], v[180:183], v[32:35]\nds_read_b128 v[204:207], v209 offset:10240\nv_mfma_f32_16x16x32_bf16 v[36:39], v[156:159], v[180:183], v[36:39]\n"
      "v_mfma_f32_16x16x32_bf16 v[36:39], v[152:155], v[176:179], v[36:39]\nv_mfma_f32_16x16x32_bf16 v[40:43], v[160:163], v[176:179], v[40:43]\n"
      "v_mfma_f32_16x16x32_bf16 v[40:43], v[164:167], v[180:183], v[40:43]\nv_mfma_f32_16x16x32_bf16 v[44:47], v[172:175], v[180:183], v[44:47]\ns_waitcnt lgkmcnt(0)\n"
      "v_mfma_f32_16x16x32_bf16 v[44:47], v[168:171], v[176:179], v[44:47]\nv_mfma_f32_16x16x32_bf16 v[60:63], v[168:171], v[184:187], v[60:63]\n"
      "v_mfma_f32_16x16x32_bf16 v[60:63], v[172:175], v[188:191], v[60:63]\nv_mfma_f32_16x16x32_bf16 v[56:59], v[164:167], v[188:191], v[56:59]\n"
      "v_mfma_f32_16x16x32_bf16 v[56:59], v[160:163], v[184:187], v[56:59]\nv_mfma_f32_16x16x32_bf16 v[52:55], v[152:155], v[184:187], v[52:55]\n"
      "v_mfma_f32_16x16x32_bf16 v[52:55], v[156:159], v[188:191], v[52:55]\nv_mfma_f32_16x16x32_bf16 v[48:51], v[148:151], v[188:191], v[48:51]\n"
      "v_mfma_f32_16x16x32_bf16 v[48:51], v[144:147], v[184:187], v[48:51]\nv_mfma_f32_16x16x32_bf16 v[64:67], v[144:147], v[192:195], v[64:67]\n"
      "v_mfma_f32_16x16x32_bf16 v[64:67], v[148:151], v[196:199], v[64:67]\nv_mfma_f32_16x16x32_bf16 v[68:71], v[156:159], v[196:199], v[68:71]\n"
      "v_mfma_f32_16x16x32_bf16 v[68:71], v[152:155], v[192:195], v[68:71]\nv_mfma_f32_16x16x32_bf16 v[72:75], v[160:163], v[192:195], v[72:75]\n"
      "v_mfma_f32_16x16x32_bf16 v[72:75], v[164:167], v[196:199], v[72:75]\nv_mfma_f32_16x16x32_bf16 v[76:79], v[172:175], v[196:199], v[76:79]\n"
      "v_mfma_f32_16x16x32_bf16 v[76:79], v[168:171], v[192:195], v[76:79]\nv_mfma_f32_16x16x32_bf16 v[92:95], v[168:171], v[200:203], v[92:95]\n"
      "v_mfma_f32_16x16x32_bf16 v[92:95], v[172:175], v[204:207], v[92:95]\nv_mfma_f32_16x16x32_bf16 v[88:91], v[164:167], v[204:207], v[88:91]\n"
      "v_mfma_f32_16x16x32_bf16 v[88:91], v[160:163], v[200:203], v[88:91]\nv_mfma_f32_16x16x32_bf16 v[84:87], v[152:155], v[200:203], v[84:87]\n"
      "v_mfma_f32_16x16x32_bf16 v[84:87], v[156:159], v[204:207], v[84:87]\nv_mfma_f32_16x16x32_bf16 v[80:83], v[148:151], v[204:207], v[80:83]\n"
      "v_mfma_f32_16x16x32_bf16 v[80:83], v[144:147], v[200:203], v[80:83]\ns_setprio 3\nv_lshlrev_b32 v112, 16, v224\nv_and_b32 v113, 0xffff0000, v224\nv_lshlrev_b32 v114, 16, v225\n"
      "v_and_b32 v115, 0xffff0000, v225\nv_lshlrev_b32 v116, 16, v226\nv_and_b32 v117, 0xffff0000, v226\nv_lshlrev_b32 v118, 16, v227\nv_and_b32 v119, 0xffff0000, v227\n"
      "v_lshlrev_b32 v120, 16, v228\nv_and_b32 v121, 0xffff0000, v228\nv_lshlrev_b32 v122, 16, v229\nv_and_b32 v123, 0xffff0000, v229\nv_lshlrev_b32 v124, 16, v230\n"
      "v_and_b32 v125, 0xffff0000, v230\nv_lshlrev_b32 v126, 16, v231\nv_and_b32 v127, 0xffff0000, v231\nv_mov_b32 v128, 0xc0135761\ns_mov_b32 s46, 0xbdd2d3e7\nv_mov_b32 v166, 0\n"
      "v_pk_mul_f32 v[132:133], v[0:1], v[0:1]\nv_pk_mul_f32 v[134:135], v[2:3], v[2:3]\nv_pk_mul_f32 v[136:137], v[4:5], v[4:5]\nv_pk_mul_f32 v[138:139], v[6:7], v[6:7]\n"
      "v_pk_fma_f32 v[132:133], v[132:133], s[46:47], v[128:129] op_sel_hi:[1,0,0]\nv_pk_fma_f32 v[134:135], v[134:135], s[46:47], v[128:129] op_sel_hi:[1,0,0]\n"
      "v_pk_fma_f32 v[136:137], v[136:137], s[46:47], v[128:129] op_sel_hi:[1,0,0]\nv_pk_fma_f32 v[138:139], v[138:139], s[46:47], v[128:129] op_sel_hi:[1,0,0]\n"
      "v_pk_mul_f32 v[132:133], v[132:133], v[0:1]\nv_pk_mul_f32 v[134:135], v[134:135], v[2:3]\nv_pk_mul_f32 v[136:137], v[136:137], v[4:5]\nv_pk_mul_f32 v[138:139], v[138:139], v[6:7]\n"
      "v_pk_mul_f32 v[0:1], v[0:1], v[112:113]\nv_pk_mul_f32 v[2:3], v[2:3], v[114:115]\nv_pk_mul_f32 v[4:5], v[4:5], v[116:117]\nv_pk_mul_f32 v[6:7], v[6:7], v[118:119]\nv_exp_f32 v132, v132\n"
      "v_exp_f32 v133, v133\nv_exp_f32 v134, v134\nv_exp_f32 v135, v135\nv_exp_f32 v136, v136\nv_exp_f32 v137, v137\nv_exp_f32 v138, v138\nv_exp_f32 v139, v139\n"
      "v_pk_add_f32 v[132:133], v[132:133], 1.0 op_sel_hi:[1,0]\nv_pk_add_f32 v[134:135], v[134:135], 1.0 op_sel_hi:[1,0]\nv_pk_add_f32 v[136:137], v[136:137], 1.0 op_sel_hi:[1,0]\n"
      "v_pk_add_f32 v[138:139], v[138:139], 1.0 op_sel_hi:[1,0]\nv_rcp_f32 v132, v132\nv_rcp_f32 v133, v133\nv_rcp_f32 v134, v134\nv_rcp_f32 v135, v135\nv_rcp_f32 v136, v136\nv_rcp_f32 v137, v137\n"
      "v_rcp_f32 v138, v138\nv_rcp_f32 v139, v139\nv_pk_mul_f32 v[148:149], v[0:1], v[132:133]\nv_pk_mul_f32 v[140:141], v[8:9], v[8:9]\nv_pk_mul_f32 v[150:151], v[2:3], v[134:135]\n"
      "v_pk_mul_f32 v[142:143], v[10:11], v[10:11]\nv_pk_fma_f32 v[148:149], v[4:5], v[136:137], v[148:149]\nv_pk_mul_f32 v[144:145], v[12:13], v[12:13]\n"
      "v_pk_fma_f32 v[150:151], v[6:7], v[138:139], v[150:151]\nv_pk_mul_f32 v[146:147], v[14:15], v[14:15]\nv_pk_fma_f32 v[140:141], v[140:141], s[46:47], v[128:129] op_sel_hi:[1,0,0]\n"
      "v_pk_fma_f32 v[142:143], v[142:143], s[46:47], v[128:129] op_sel_hi:[1,0,0]\nv_pk_fma_f32 v[144:145], v[144:145], s[46:47], v[128:129] op_sel_hi:[1,0,0]\n"
      "v_pk_fma_f32 v[146:147], v[146:147], s[46:47], v[128:129] op_sel_hi:[1,0,0]\nv_pk_mul_f32 v[140:141], v[140:141], v[8:9]\nv_pk_mul_f32 v[142:143], v[142:143], v[10:11]\n"
      "v_pk_mul_f32 v[144:145], v[144:145], v[12:13]\nv_pk_mul_f32 v[146:147], v[146:147], v[14:15]\nv_pk_mul_f32 v[8:9], v[8:9], v[120:121]\nv_pk_mul_f32 v[10:11], v[10:11], v[122:123]\n"
      "v_pk_mul_f32 v[12:13], v[12:13], v[124:125]\nv_pk_mul_f32 v[14:15], v[14:15], v[126:127]\nv_exp_f32 v140, v140\nv_exp_f32 v141, v141\nv_exp_f32 v142, v142\nv_exp_f32 v143, v143\n"
      "v_exp_f32 v144, v144\nv_exp_f32 v145, v145\nv_exp_f32 v146, v146\nv_exp_f32 v147, v147\nv_pk_add_f32 v[140:141], v[140:141], 1.0 op_sel_hi:[1,0]\n"
      "v_pk_add_f32 v[142:143], v[142:143], 1.0 op_sel_hi:[1,0]\nv_pk_add_f32 v[144:145], v[144:145], 1.0 op_sel_hi:[1,0]\nv_pk_add_f32 v[146:147], v[146:147], 1.0 op_sel_hi:[1,0]\n"
      "v_rcp_f32 v140, v140\nv_rcp_f32 v141, v141\nv_rcp_f32 v142, v142\nv_rcp_f32 v143, v143\nv_rcp_f32 v144, v144\nv_rcp_f32 v145, v145\nv_rcp_f32 v146, v146\nv_rcp_f32 v147, v147\n"
      "v_pk_fma_f32 v[148:149], v[8:9], v[140:141], v[148:149]\nv_pk_mul_f32 v[132:133], v[16:17], v[16:17]\nv_pk_fma_f32 v[150:151], v[10:11], v[142:143], v[150:151]\n"
      "v_pk_mul_f32 v[134:135], v[18:19], v[18:19]\nv_pk_fma_f32 v[148:149], v[12:13], v[144:145], v[148:149]\nv_pk_mul_f32 v[136:137], v[20:21], v[20:21]\n"
      "v_pk_fma_f32 v[150:151], v[14:15], v[146:147], v[150:151]\nv_pk_mul_f32 v[138:139], v[22:23], v[22:23]\nv_pk_add_f32 v[148:149], v[148:149], v[150:151]\n"
      "v_pk_fma_f32 v[132:133], v[132:133], s[46:47], v[128:129] op_sel_hi:[1,0,0]\nv_add_f32 v160, v148, v149\nv_pk_fma_f32 v[134:135], v[134:135], s[46:47], v[128:129] op_sel_hi:[1,0,0]\n"
      "v_pk_fma_f32 v[136:137], v[136:137], s[46:47], v[128:129] op_sel_hi:[1,0,0]\nv_pk_fma_f32 v[138:139], v[138:139], s[46:47], v[128:129] op_sel_hi:[1,0,0]\n"
      "v_pk_mul_f32 v[132:133], v[132:133], v[16:17]\nv_pk_mul_f32 v[134:135], v[134:135], v[18:19]\nv_pk_mul_f32 v[136:137], v[136:137], v[20:21]\nv_pk_mul_f32 v[138:139], v[138:139], v[22:23]\n"
      "v_pk_mul_f32 v[16:17], v[16:17], v[112:113]\nv_pk_mul_f32 v[18:19], v[18:19], v[114:115]\nv_pk_mul_f32 v[20:21], v[20:21], v[116:117]\nv_pk_mul_f32 v[22:23], v[22:23], v[118:119]\n"
      "v_exp_f32 v132, v132\nv_exp_f32 v133, v133\nv_exp_f32 v134, v134\nv_exp_f32 v135, v135\nv_exp_f32 v136, v136\nv_exp_f32 v137, v137\nv_exp_f32 v138, v138\nv_exp_f32 v139, v139\n"
      "v_pk_add_f32 v[132:133], v[132:133], 1.0 op_sel_hi:[1,0]\nv_pk_add_f32 v[134:135], v[134:135], 1.0 op_sel_hi:[1,0]\nv_pk_add_f32 v[136:137], v[136:137], 1.0 op_sel_hi:[1,0]\n"
      "v_pk_add_f32 v[138:139], v[138:139], 1.0 op_sel_hi:[1,0]\nv_rcp_f32 v132, v132\nv_rcp_f32 v133, v133\nv_rcp_f32 v134, v134\nv_rcp_f32 v135, v135\nv_rcp_f32 v136, v136\nv_rcp_f32 v137, v137\n"
      "v_rcp_f32 v138, v138\nv_rcp_f32 v139, v139\nv_pk_mul_f32 v[148:149], v[16:17], v[132:133]\nv_pk_mul_f32 v[140:141], v[24:25], v[24:25]\nv_pk_mul_f32 v[150:151], v[18:19], v[134:135]\n"
      "v_pk_mul_f32 v[142:143], v[26:27], v[26:27]\nv_pk_fma_f32 v[148:149], v[20:21], v[136:137], v[148:149]\nv_pk_mul_f32 v[144:145], v[28:29], v[28:29]\n"
      "v_pk_fma_f32 v[150:151], v[22:23], v[138:139], v[150:151]\nv_pk_mul_f32 v[146:147], v[30:31], v[30:31]\nv_pk_fma_f32 v[140:141], v[140:141], s[46:47], v[128:129] op_sel_hi:[1,0,0]\n"
      "v_pk_fma_f32 v[142:143], v[142:143], s[46:47], v[128:129] op_sel_hi:[1,0,0]\nv_pk_fma_f32 v[144:145], v[144:145], s[46:47], v[128:129] op_sel_hi:[1,0,0]\n"
      "v_pk_fma_f32 v[146:147], v[146:147], s[46:47], v[128:129] op_sel_hi:[1,0,0]\nv_pk_mul_f32 v[140:141], v[140:141], v[24:25]\nv_pk_mul_f32 v[142:143], v[142:143], v[26:27]\n"
      "v_pk_mul_f32 v[144:145], v[144:145], v[28:29]\nv_pk_mul_f32 v[146:147], v[146:147], v[30:31]\nv_pk_mul_f32 v[24:25], v[24:25], v[120:121]\nv_pk_mul_f32 v[26:27], v[26:27], v[122:123]\n"
      "v_pk_mul_f32 v[28:29], v[28:29], v[124:125]\nv_pk_mul_f32 v[30:31], v[30:31], v[126:127]\nv_exp_f32 v140, v140\nv_exp_f32 v141, v141\nv_exp_f32 v142, v142\nv_exp_f32 v143, v143\n"
      "v_exp_f32 v144, v144\nv_exp_f32 v145, v145\nv_exp_f32 v146, v146\nv_exp_f32 v147, v147\nv_pk_add_f32 v[140:141], v[140:141], 1.0 op_sel_hi:[1,0]\n"
      "v_pk_add_f32 v[142:143], v[142:143], 1.0 op_sel_hi:[1,0]\nv_pk_add_f32 v[144:145], v[144:145], 1.0 op_sel_hi:[1,0]\nv_pk_add_f32 v[146:147], v[146:147], 1.0 op_sel_hi:[1,0]\n"
      "v_rcp_f32 v140, v140\nv_rcp_f32 v141, v141\nv_rcp_f32 v142, v142\nv_rcp_f32 v143, v143\nv_rcp_f32 v144, v144\nv_rcp_f32 v145, v145\nv_rcp_f32 v146, v146\nv_rcp_f32 v147, v147\n"
      "v_pk_fma_f32 v[148:149], v[24:25], v[140:141], v[148:149]\nv_pk_mul_f32 v[132:133], v[32:33], v[32:33]\nv_pk_fma_f32 v[150:151], v[26:27], v[142:143], v[150:151]\n"
      "v_pk_mul_f32 v[134:135], v[34:35], v[34:35]\nv_pk_fma_f32 v[148:149], v[28:29], v[144:145], v[148:149]\nv_pk_mul_f32 v[136:137], v[36:37], v[36:37]\n"
      "v_pk_fma_f32 v[150:151], v[30:31], v[146:147], v[150:151]\nv_pk_mul_f32 v[138:139], v[38:39], v[38:39]\nv_pk_add_f32 v[148:149], v[148:149], v[150:151]\n"
      "v_pk_fma_f32 v[132:133], v[132:133], s[46:47], v[128:129] op_sel_hi:[1,0,0]\nv_add_f32 v161, v148, v149\nv_pk_fma_f32 v[134:135], v[134:135], s[46:47], v[128:129] op_sel_hi:[1,0,0]\n"
      "v_pk_fma_f32 v[136:137], v[136:137], s[46:47], v[128:129] op_sel_hi:[1,0,0]\nv_pk_fma_f32 v[138:139], v[138:139], s[46:47], v[128:129] op_sel_hi:[1,0,0]\n"
      "v_pk_mul_f32 v[132:133], v[132:133], v[32:33]\nv_pk_mul_f32 v[134:135], v[134:135], v[34:35]\nv_pk_mul_f32 v[136:137], v[136:137], v[36:37]\nv_pk_mul_f32 v[138:139], v[138:139], v[38:39]\n"
      "v_pk_mul_f32 v[32:33], v[32:33], v[112:113]\nv_pk_mul_f32 v[34:35], v[34:35], v[114:115]\nv_pk_mul_f32 v[36:37], v[36:37], v[116:117]\nv_pk_mul_f32 v[38:39], v[38:39], v[118:119]\n"
      "v_exp_f32 v132, v132\nv_exp_f32 v133, v133\nv_exp_f32 v134, v134\nv_exp_f32 v135, v135\nv_exp_f32 v136, v136\nv_exp_f32 v137, v137\nv_exp_f32 v138, v138\nv_exp_f32 v139, v139\n"
      "v_pk_add_f32 v[132:133], v[132:133], 1.0 op_sel_hi:[1,0]\nv_pk_add_f32 v[134:135], v[134:135], 1.0 op_sel_hi:[1,0]\nv_pk_add_f32 v[136:137], v[136:137], 1.0 op_sel_hi:[1,0]\n"
      "v_pk_add_f32 v[138:139], v[138:139], 1.0 op_sel_hi:[1,0]\nv_rcp_f32 v132, v132\nv_rcp_f32 v133, v133\nv_rcp_f32 v134, v134\nv_rcp_f32 v135, v135\nv_rcp_f32 v136, v136\nv_rcp_f32 v137, v137\n"
      "v_rcp_f32 v138, v138\nv_rcp_f32 v139, v139\nv_pk_mul_f32 v[148:149], v[32:33], v[132:133]\nv_pk_mul_f32 v[140:141], v[40:41], v[40:41]\nv_pk_mul_f32 v[150:151], v[34:35], v[134:135]\n"
      "v_pk_mul_f32 v[142:143], v[42:43], v[42:43]\nv_pk_fma_f32 v[148:149], v[36:37], v[136:137], v[148:149]\nv_pk_mul_f32 v[144:145], v[44:45], v[44:45]\n"
      "v_pk_fma_f32 v[150:151], v[38:39], v[138:139], v[150:151]\nv_pk_mul_f32 v[146:147], v[46:47], v[46:47]\nv_pk_fma_f32 v[140:141], v[140:141], s[46:47], v[128:129] op_sel_hi:[1,0,0]\n"
      "v_pk_fma_f32 v[142:143], v[142:143], s[46:47], v[128:129] op_sel_hi:[1,0,0]\nv_pk_fma_f32 v[144:145], v[144:145], s[46:47], v[128:129] op_sel_hi:[1,0,0]\n"
      "v_pk_fma_f32 v[146:147], v[146:147], s[46:47], v[128:129] op_sel_hi:[1,0,0]\nv_pk_mul_f32 v[140:141], v[140:141], v[40:41]\nv_pk_mul_f32 v[142:143], v[142:143], v[42:43]\n"
      "v_pk_mul_f32 v[144:145], v[144:145], v[44:45]\nv_pk_mul_f32 v[146:147], v[146:147], v[46:47]\nv_pk_mul_f32 v[40:41], v[40:41], v[120:121]\nv_pk_mul_f32 v[42:43], v[42:43], v[122:123]\n"
      "v_pk_mul_f32 v[44:45], v[44:45], v[124:125]\nv_pk_mul_f32 v[46:47], v[46:47], v[126:127]\nv_exp_f32 v140, v140\nv_exp_f32 v141, v141\nv_exp_f32 v142, v142\nv_exp_f32 v143, v143\n"
      "v_exp_f32 v144, v144\nv_exp_f32 v145, v145\nv_exp_f32 v146, v146\nv_exp_f32 v147, v147\nv_pk_add_f32 v[140:141], v[140:141], 1.0 op_sel_hi:[1,0]\n"
      "v_pk_add_f32 v[142:143], v[142:143], 1.0 op_sel_hi:[1,0]\nv_pk_add_f32 v[144:145], v[144:145], 1.0 op_sel_hi:[1,0]\nv_pk_add_f32 v[146:147], v[146:147], 1.0 op_sel_hi:[1,0]\n"
      "v_rcp_f32 v140, v140\nv_rcp_f32 v141, v141\nv_rcp_f32 v142, v142\nv_rcp_f32 v143, v143\nv_rcp_f32 v144, v144\nv_rcp_f32 v145, v145\nv_rcp_f32 v146, v146\nv_rcp_f32 v147, v147\n"
      "v_pk_fma_f32 v[148:149], v[40:41], v[140:141], v[148:149]\nv_pk_mul_f32 v[132:133], v[48:49], v[48:49]\nv_pk_fma_f32 v[150:151], v[42:43], v[142:143], v[150:151]\n"
      "v_pk_mul_f32 v[134:135], v[50:51], v[50:51]\nv_pk_fma_f32 v[148:149], v[44:45], v[144:145], v[148:149]\nv_pk_mul_f32 v[136:137], v[52:53], v[52:53]\n"
      "v_pk_fma_f32 v[150:151], v[46:47], v[146:147], v[150:151]\nv_pk_mul_f32 v[138:139], v[54:55], v[54:55]\nv_pk_add_f32 v[148:149], v[148:149], v[150:151]\n"
      "v_pk_fma_f32 v[132:133], v[132:133], s[46:47], v[128:129] op_sel_hi:[1,0,0]\nv_add_f32 v162, v148, v149\nv_pk_fma_f32 v[134:135], v[134:135], s[46:47], v[128:129] op_sel_hi:[1,0,0]\n"
      "v_pk_fma_f32 v[136:137], v[136:137], s[46:47], v[128:129] op_sel_hi:[1,0,0]\nv_pk_fma_f32 v[138:139], v[138:139], s[46:47], v[128:129] op_sel_hi:[1,0,0]\n"
      "v_pk_mul_f32 v[132:133], v[132:133], v[48:49]\nv_pk_mul_f32 v[134:135], v[134:135], v[50:51]\nv_pk_mul_f32 v[136:137], v[136:137], v[52:53]\nv_pk_mul_f32 v[138:139], v[138:139], v[54:55]\n"
      "v_pk_mul_f32 v[48:49], v[48:49], v[112:113]\nv_pk_mul_f32 v[50:51], v[50:51], v[114:115]\nv_pk_mul_f32 v[52:53], v[52:53], v[116:117]\nv_pk_mul_f32 v[54:55], v[54:55], v[118:119]\n"
      "v_exp_f32 v132, v132\nv_exp_f32 v133, v133\nv_exp_f32 v134, v134\nv_exp_f32 v135, v135\nv_exp_f32 v136, v136\nv_exp_f32 v137, v137\nv_exp_f32 v138, v138\nv_exp_f32 v139, v139\n"
      "v_pk_add_f32 v[132:133], v[132:133], 1.0 op_sel_hi:[1,0]\nv_pk_add_f32 v[134:135], v[134:135], 1.0 op_sel_hi:[1,0]\nv_pk_add_f32 v[136:137], v[136:137], 1.0 op_sel_hi:[1,0]\n"
      "v_pk_add_f32 v[138:139], v[138:139], 1.0 op_sel_hi:[1,0]\nv_rcp_f32 v132, v132\nv_rcp_f32 v133, v133\nv_rcp_f32 v134, v134\nv_rcp_f32 v135, v135\nv_rcp_f32 v136, v136\nv_rcp_f32 v137, v137\n"
      "v_rcp_f32 v138, v138\nv_rcp_f32 v139, v139\nv_pk_mul_f32 v[148:149], v[48:49], v[132:133]\nv_pk_mul_f32 v[140:141], v[56:57], v[56:57]\nv_pk_mul_f32 v[150:151], v[50:51], v[134:135]\n"
      "v_pk_mul_f32 v[142:143], v[58:59], v[58:59]\nv_pk_fma_f32 v[148:149], v[52:53], v[136:137], v[148:149]\nv_pk_mul_f32 v[144:145], v[60:61], v[60:61]\n"
      "v_pk_fma_f32 v[150:151], v[54:55], v[138:139], v[150:151]\nv_pk_mul_f32 v[146:147], v[62:63], v[62:63]\nv_pk_fma_f32 v[140:141], v[140:141], s[46:47], v[128:129] op_sel_hi:[1,0,0]\n"
      "v_pk_fma_f32 v[142:143], v[142:143], s[46:47], v[128:129] op_sel_hi:[1,0,0]\nv_pk_fma_f32 v[144:145], v[144:145], s[46:47], v[128:129] op_sel_hi:[1,0,0]\n"
      "v_pk_fma_f32 v[146:147], v[146:147], s[46:47], v[128:129] op_sel_hi:[1,0,0]\nv_pk_mul_f32 v[140:141], v[140:141], v[56:57]\nv_pk_mul_f32 v[142:143], v[142:143], v[58:59]\n"
      "v_pk_mul_f32 v[144:145], v[144:145], v[60:61]\nv_pk_mul_f32 v[146:147], v[146:147], v[62:63]\nv_pk_mul_f32 v[56:57], v[56:57], v[120:121]\nv_pk_mul_f32 v[58:59], v[58:59], v[122:123]\n"
      "v_pk_mul_f32 v[60:61], v[60:61], v[124:125]\nv_pk_mul_f32 v[62:63], v[62:63], v[126:127]\nv_exp_f32 v140, v140\nv_exp_f32 v141, v141\nv_exp_f32 v142, v142\nv_exp_f32 v143, v143\n"
      "v_exp_f32 v144, v144\nv_exp_f32 v145, v145\nv_exp_f32 v146, v146\nv_exp_f32 v147, v147\nv_pk_add_f32 v[140:141], v[140:141], 1.0 op_sel_hi:[1,0]\n"
      "v_pk_add_f32 v[142:143], v[142:143], 1.0 op_sel_hi:[1,0]\nv_pk_add_f32 v[144:145], v[144:145], 1.0 op_sel_hi:[1,0]\nv_pk_add_f32 v[146:147], v[146:147], 1.0 op_sel_hi:[1,0]\n"
      "v_rcp_f32 v140, v140\nv_rcp_f32 v141, v141\nv_rcp_f32 v142, v142\nv_rcp_f32 v143, v143\nv_rcp_f32 v144, v144\nv_rcp_f32 v145, v145\nv_rcp_f32 v146, v146\nv_rcp_f32 v147, v147\n"
      "v_pk_fma_f32 v[148:149], v[56:57], v[140:141], v[148:149]\nv_pk_mul_f32 v[132:133], v[64:65], v[64:65]\nv_pk_fma_f32 v[150:151], v[58:59], v[142:143], v[150:151]\n"
      "v_pk_mul_f32 v[134:135], v[66:67], v[66:67]\nv_pk_fma_f32 v[148:149], v[60:61], v[144:145], v[148:149]\nv_pk_mul_f32 v[136:137], v[68:69], v[68:69]\n"
      "v_pk_fma_f32 v[150:151], v[62:63], v[146:147], v[150:151]\nv_pk_mul_f32 v[138:139], v[70:71], v[70:71]\nv_pk_add_f32 v[148:149], v[148:149], v[150:151]\n"
      "v_pk_fma_f32 v[132:133], v[132:133], s[46:47], v[128:129] op_sel_hi:[1,0,0]\nv_add_f32 v163, v148, v149\nv_pk_fma_f32 v[134:135], v[134:135], s[46:47], v[128:129] op_sel_hi:[1,0,0]\n"
      "v_pk_fma_f32 v[136:137], v[136:137], s[46:47], v[128:129] op_sel_hi:[1,0,0]\nv_pk_fma_f32 v[138:139], v[138:139], s[46:47], v[128:129] op_sel_hi:[1,0,0]\n"
      "v_pk_mul_f32 v[132:133], v[132:133], v[64:65]\nv_pk_mul_f32 v[134:135], v[134:135], v[66:67]\nv_pk_mul_f32 v[136:137], v[136:137], v[68:69]\nv_pk_mul_f32 v[138:139], v[138:139], v[70:71]\n"
      "v_pk_mul_f32 v[64:65], v[64:65], v[112:113]\nv_pk_mul_f32 v[66:67], v[66:67], v[114:115]\nv_pk_mul_f32 v[68:69], v[68:69], v[116:117]\nv_pk_mul_f32 v[70:71], v[70:71], v[118:119]\n"
      "v_exp_f32 v132, v132\nv_exp_f32 v133, v133\nv_exp_f32 v134, v134\nv_exp_f32 v135, v135\nv_exp_f32 v136, v136\nv_exp_f32 v137, v137\nv_exp_f32 v138, v138\nv_exp_f32 v139, v139\n"
      "v_pk_add_f32 v[132:133], v[132:133], 1.0 op_sel_hi:[1,0]\nv_pk_add_f32 v[134:135], v[134:135], 1.0 op_sel_hi:[1,0]\nv_pk_add_f32 v[136:137], v[136:137], 1.0 op_sel_hi:[1,0]\n"
      "v_pk_add_f32 v[138:139], v[138:139], 1.0 op_sel_hi:[1,0]\nv_rcp_f32 v132, v132\nv_rcp_f32 v133, v133\nv_rcp_f32 v134, v134\nv_rcp_f32 v135, v135\nv_rcp_f32 v136, v136\nv_rcp_f32 v137, v137\n"
      "v_rcp_f32 v138, v138\nv_rcp_f32 v139, v139\nv_pk_mul_f32 v[148:149], v[64:65], v[132:133]\nv_pk_mul_f32 v[140:141], v[72:73], v[72:73]\nv_pk_mul_f32 v[150:151], v[66:67], v[134:135]\n"
      "v_pk_mul_f32 v[142:143], v[74:75], v[74:75]\nv_pk_fma_f32 v[148:149], v[68:69], v[136:137], v[148:149]\nv_pk_mul_f32 v[144:145], v[76:77], v[76:77]\n"
      "v_pk_fma_f32 v[150:151], v[70:71], v[138:139], v[150:151]\nv_pk_mul_f32 v[146:147], v[78:79], v[78:79]\nv_pk_fma_f32 v[140:141], v[140:141], s[46:47], v[128:129] op_sel_hi:[1,0,0]\n"
      "v_pk_fma_f32 v[142:143], v[142:143], s[46:47], v[128:129] op_sel_hi:[1,0,0]\nv_pk_fma_f32 v[144:145], v[144:145], s[46:47], v[128:129] op_sel_hi:[1,0,0]\n"
      "v_pk_fma_f32 v[146:147], v[146:147], s[46:47], v[128:129] op_sel_hi:[1,0,0]\nv_pk_mul_f32 v[140:141], v[140:141], v[72:73]\nv_pk_mul_f32 v[142:143], v[142:143], v[74:75]\n"
      "v_pk_mul_f32 v[144:145], v[144:145], v[76:77]\nv_pk_mul_f32 v[146:147], v[146:147], v[78:79]\nv_pk_mul_f32 v[72:73], v[72:73], v[120:121]\nv_pk_mul_f32 v[74:75], v[74:75], v[122:123]\n"
      "v_pk_mul_f32 v[76:77], v[76:77], v[124:125]\nv_pk_mul_f32 v[78:79], v[78:79], v[126:127]\nv_exp_f32 v140, v140\nv_exp_f32 v141, v141\nv_exp_f32 v142, v142\nv_exp_f32 v143, v143\n"
      "v_exp_f32 v144, v144\nv_exp_f32 v145, v145\nv_exp_f32 v146, v146\nv_exp_f32 v147, v147\nv_pk_add_f32 v[140:141], v[140:141], 1.0 op_sel_hi:[1,0]\n"
      "v_pk_add_f32 v[142:143], v[142:143], 1.0 op_sel_hi:[1,0]\nv_pk_add_f32 v[144:145], v[144:145], 1.0 op_sel_hi:[1,0]\nv_pk_add_f32 v[146:147], v[146:147], 1.0 op_sel_hi:[1,0]\n"
      "v_rcp_f32 v140, v140\nv_rcp_f32 v141, v141\nv_rcp_f32 v142, v142\nv_rcp_f32 v143, v143\nv_rcp_f32 v144, v144\nv_rcp_f32 v145, v145\nv_rcp_f32 v146, v146\nv_rcp_f32 v147, v147\n"
      "v_pk_fma_f32 v[148:149], v[72:73], v[140:141], v[148:149]\nv_pk_mul_f32 v[132:133], v[80:81], v[80:81]\nv_pk_fma_f32 v[150:151], v[74:75], v[142:143], v[150:151]\n"
      "v_pk_mul_f32 v[134:135], v[82:83], v[82:83]\nv_pk_fma_f32 v[148:149], v[76:77], v[144:145], v[148:149]\nv_pk_mul_f32 v[136:137], v[84:85], v[84:85]\n"
      "v_pk_fma_f32 v[150:151], v[78:79], v[146:147], v[150:151]\nv_pk_mul_f32 v[138:139], v[86:87], v[86:87]\nv_pk_add_f32 v[148:149], v[148:149], v[150:151]\n"
      "v_pk_fma_f32 v[132:133], v[132:133], s[46:47], v[128:129] op_sel_hi:[1,0,0]\nv_add_f32 v164, v148, v149\nv_pk_fma_f32 v[134:135], v[134:135], s[46:47], v[128:129] op_sel_hi:[1,0,0]\n"
      "v_pk_fma_f32 v[136:137], v[136:137], s[46:47], v[128:129] op_sel_hi:[1,0,0]\nv_pk_fma_f32 v[138:139], v[138:139], s[46:47], v[128:129] op_sel_hi:[1,0,0]\n"
      "v_pk_mul_f32 v[132:133], v[132:133], v[80:81]\nv_pk_mul_f32 v[134:135], v[134:135], v[82:83]\nv_pk_mul_f32 v[136:137], v[136:137], v[84:85]\nv_pk_mul_f32 v[138:139], v[138:139], v[86:87]\n"
      "v_pk_mul_f32 v[80:81], v[80:81], v[112:113]\nv_pk_mul_f32 v[82:83], v[82:83], v[114:115]\nv_pk_mul_f32 v[84:85], v[84:85], v[116:117]\nv_pk_mul_f32 v[86:87], v[86:87], v[118:119]\n"
      "v_exp_f32 v132, v132\nv_exp_f32 v133, v133\nv_exp_f32 v134, v134\nv_exp_f32 v135, v135\nv_exp_f32 v136, v136\nv_exp_f32 v137, v137\nv_exp_f32 v138, v138\nv_exp_f32 v139, v139\n"
      "v_pk_add_f32 v[132:133], v[132:133], 1.0 op_sel_hi:[1,0]\nv_pk_add_f32 v[134:135], v[134:135], 1.0 op_sel_hi:[1,0]\nv_pk_add_f32 v[136:137], v[136:137], 1.0 op_sel_hi:[1,0]\n"
      "v_pk_add_f32 v[138:139], v[138:139], 1.0 op_sel_hi:[1,0]\nv_rcp_f32 v132, v132\nv_rcp_f32 v133, v133\nv_rcp_f32 v134, v134\nv_rcp_f32 v135, v135\nv_rcp_f32 v136, v136\nv_rcp_f32 v137, v137\n"
      "v_rcp_f32 v138, v138\nv_rcp_f32 v139, v139\nv_pk_mul_f32 v[148:149], v[80:81], v[132:133]\nv_pk_mul_f32 v[140:141], v[88:89], v[88:89]\nv_pk_mul_f32 v[150:151], v[82:83], v[134:135]\n"
      "v_pk_mul_f32 v[142:143], v[90:91], v[90:91]\nv_pk_fma_f32 v[148:149], v[84:85], v[136:137], v[148:149]\nv_pk_mul_f32 v[144:145], v[92:93], v[92:93]\n"
      "v_pk_fma_f32 v[150:151], v[86:87], v[138:139], v[150:151]\nv_pk_mul_f32 v[146:147], v[94:95], v[94:95]\nv_pk_fma_f32 v[140:141], v[140:141], s[46:47], v[128:129] op_sel_hi:[1,0,0]\n"
      "v_pk_fma_f32 v[142:143], v[142:143], s[46:47], v[128:129] op_sel_hi:[1,0,0]\nv_pk_fma_f32 v[144:145], v[144:145], s[46:47], v[128:129] op_sel_hi:[1,0,0]\n"
      "v_pk_fma_f32 v[146:147], v[146:147], s[46:47], v[128:129] op_sel_hi:[1,0,0]\nv_pk_mul_f32 v[140:141], v[140:141], v[88:89]\nv_pk_mul_f32 v[142:143], v[142:143], v[90:91]\n"
      "v_pk_mul_f32 v[144:145], v[144:145], v[92:93]\nv_pk_mul_f32 v[146:147], v[146:147], v[94:95]\nv_pk_mul_f32 v[88:89], v[88:89], v[120:121]\nv_pk_mul_f32 v[90:91], v[90:91], v[122:123]\n"
      "v_pk_mul_f32 v[92:93], v[92:93], v[124:125]\nv_pk_mul_f32 v[94:95], v[94:95], v[126:127]\nv_exp_f32 v140, v140\nv_exp_f32 v141, v141\nv_exp_f32 v142, v142\nv_exp_f32 v143, v143\n"
      "v_exp_f32 v144, v144\nv_exp_f32 v145, v145\nv_exp_f32 v146, v146\nv_exp_f32 v147, v147\nv_pk_add_f32 v[140:141], v[140:141], 1.0 op_sel_hi:[1,0]\n"
      "v_pk_add_f32 v[142:143], v[142:143], 1.0 op_sel_hi:[1,0]\nv_pk_add_f32 v[144:145], v[144:145], 1.0 op_sel_hi:[1,0]\nv_pk_add_f32 v[146:147], v[146:147], 1.0 op_sel_hi:[1,0]\n"
      "v_rcp_f32 v140, v140\nv_rcp_f32 v141, v141\nv_rcp_f32 v142, v142\nv_rcp_f32 v143, v143\nv_rcp_f32 v144, v144\nv_rcp_f32 v145, v145\nv_rcp_f32 v146, v146\nv_rcp_f32 v147, v147\n"
      "v_pk_fma_f32 v[148:149], v[88:89], v[140:141], v[148:149]\nv_pk_fma_f32 v[150:151], v[90:91], v[142:143], v[150:151]\nv_pk_fma_f32 v[148:149], v[92:93], v[144:145], v[148:149]\n"
      "v_pk_fma_f32 v[150:151], v[94:95], v[146:147], v[150:151]\nv_pk_add_f32 v[148:149], v[148:149], v[150:151]\nv_add_f32 v165, v148, v149\n"
      ".Lge:\nv_mov_b32 v167, v166\ns_lshl_b32 s42, s45, 10\ns_add_u32 s42, s42, 160256\ns_mul_i32 s43, s44, 384\ns_add_u32 s43, s43, s42\nv_lshl_add_u32 v0, v234, 2, s43\n"
      "v_permlane32_swap_b32 v160, v162\nv_permlane32_swap_b32 v161, v163\nv_permlane32_swap_b32 v164, v166\nv_permlane32_swap_b32 v165, v167\nv_add_f32 v160, v160, v162\n"
      "v_add_f32 v161, v161, v163\nv_add_f32 v164, v164, v166\nv_add_f32 v165, v165, v167\ns_lshl_b32 s43, s44, 6\ns_add_u32 s42, s42, s43\ns_add_u32 s42, s42, 512\nv_and_b32 v1, 15, v234\n"
      "v_cmp_gt_u32 vcc, 32, v234\nv_permlane16_swap_b32 v160, v161\nv_permlane16_swap_b32 v164, v165\nv_lshl_add_u32 v1, v1, 2, s42\nv_add_f32 v160, v160, v161\nv_add_f32 v164, v164, v165\n"
      "v_cndmask_b32 v1, v1, v0, vcc\nds_write_b32 v0, v160\nds_write_b32 v1, v164 offset:256\nds_add_rtn_u32 v2, v232, v233 offset:20\nds_read_b32 v3, v232 offset:24\nv_lshlrev_b32 v0, 2, v234\n"
      "v_add_u32 v1, 160256, v0\ns_waitcnt lgkmcnt(0)\nv_readfirstlane_b32 s42, v2\nv_readfirstlane_b32 s39, v3\ns_cmp_lg_u32 s42, 3\ns_cbranch_scc1 .Lgd\n"
      "ds_read2st64_b32 v[4:5], v1 offset0:0 offset1:4\nds_read2st64_b32 v[6:7], v1 offset0:1 offset1:5\nds_read2st64_b32 v[8:9], v1 offset0:2 offset1:6\n"
      "ds_read2st64_b32 v[10:11], v1 offset0:3 offset1:7\ns_mul_i32 s41, s39, 0x9E3779B1\ns_lshr_b32 s41, s41, 24\ns_xor_b32 s40, s39, 0xa5c30f96\ns_add_u32 s46, s20, s35\ns_addc_u32 s47, s21, 0\n"
      "v_lshlrev_b32 v12, 1, v0\ns_cmp_eq_u32 s25, 2\ns_cselect_b64 vcc, -1, 0\ns_movk_i32 s42, 0xff00\nv_mov_b32 v14, s41\ns_lshl_b32 s43, s25, 3\ns_waitcnt lgkmcnt(0)\nv_add_f32 v10, v10, v11\n"
      "v_add_f32 v4, v4, v5\nv_add_f32 v6, v6, v7\nv_add_f32 v8, v8, v9\nv_mov_b32 v11, v10\nv_mov_b32 v13, v10\ns_nop 1\nv_permlane16_swap_b32 v11, v13\nv_add_f32 v11, v11, v13\n"
      "v_cndmask_b32 v10, v10, v11, vcc\nv_add_u32 v4, 0x80, v4\nv_and_or_b32 v4, v4, s42, v14\nv_mov_b32 v5, s40\nv_add_u32 v6, 0x80, v6\nv_and_or_b32 v6, v6, s42, v14\nv_mov_b32 v7, s40\n"
      "v_add_u32 v8, 0x80, v8\nv_and_or_b32 v8, v8, s42, v14\nv_mov_b32 v9, s40\nv_add_u32 v10, 0x80, v10\nv_and_or_b32 v10, v10, s42, v14\nv_mov_b32 v11, s40\n"
      "global_store_dwordx2 v12, v[4:5], s[46:47] offset:0 sc1\nglobal_store_dwordx2 v12, v[6:7], s[46:47] offset:512 sc1\nglobal_store_dwordx2 v12, v[8:9], s[46:47] offset:1024 sc1\n"
      "s_cmp_eq_u32 s43, 0\ns_cbranch_scc1 .Lgd\ns_bfm_b64 exec, s43, 0\nglobal_store_dwordx2 v12, v[10:11], s[46:47] offset:1536 sc1\ns_mov_b64 exec, -1\n"
      ".Lgd:\ns_setprio 0\n"
      ".Lcin:\nv_mbcnt_lo_u32_b32 v17, -1, 0\nv_mbcnt_hi_u32_b32 v17, -1, v17\nv_mov_b32 v25, 1\nv_cmp_eq_u32 vcc, 0, v17\ns_lshl_b32 s49, s36, 2\ns_add_u32 s49, s49, 163584\n"
      "v_mov_b32 v23, 262144\nv_mov_b32 v1, s49\nv_cndmask_b32 v26, v23, v1, vcc\nv_lshlrev_b32 v0, 3, v17\nds_add_rtn_u32 v21, v26, v25\nv_add_u32 v1, 159712, v0\nv_lshlrev_b32 v0, 1, v0\n"
      "ds_read_b64 v[34:35], v1\nv_add_u32 v0, 45056, v0\nv_and_b32 v1, 48, v17\ns_mul_i32 s53, s36, 17280\ns_add_u32 s53, s53, 90112\nv_mov_b32 v22, 163584\ns_mov_b32 s51, 0\n"
      "v_lshrrev_b32 v2, 1, v1\nv_lshrrev_b32 v3, 2, v1\n"
      ".Lc_m:\nds_read_b128 v[60:63], v22 offset:16\ns_add_u32 s51, s51, 1\ns_waitcnt lgkmcnt(0)\nv_readfirstlane_b32 s50, v63\nv_readfirstlane_b32 s56, v60\ns_lshr_b32 s49, s51, 20\n"
      "s_or_b32 s50, s50, s49\ns_bitcmp0_b32 s50, 2\ns_cbranch_scc1 .Lc_m\nv_readfirstlane_b32 s43, v21\ns_add_u32 s56, s56, s64\ns_min_u32 s56, s56, 20\ns_sub_u32 s50, s56, 1\n"
      "v_add_u32 v32, s24, v2\nv_min_u32 v32, s50, v32\nv_lshlrev_b32 v32, 3, v32\nv_add_u32 v32, 159552, v32\nds_read_b32 v33, v32\ns_min_u32 s49, s43, 19\ns_mulk_i32 s49, 864\n"
      "s_add_u32 s49, s49, s53\nv_add_u32_sdwa v30, s49, v35 dst_sel:DWORD dst_unused:UNUSED_PAD src0_sel:DWORD src1_sel:WORD_0\n"
      "v_add_u32_sdwa v31, s49, v35 dst_sel:DWORD dst_unused:UNUSED_PAD src0_sel:DWORD src1_sel:WORD_1\n"
      "v_add_u32_sdwa v28, s49, v34 dst_sel:DWORD dst_unused:UNUSED_PAD src0_sel:DWORD src1_sel:WORD_0\n"
      "v_add_u32_sdwa v29, s49, v34 dst_sel:DWORD dst_unused:UNUSED_PAD src0_sel:DWORD src1_sel:WORD_1\nds_read_b64_tr_b16 v[56:57], v30\nds_read_b64_tr_b16 v[58:59], v31\n"
      "ds_read_b64_tr_b16 v[44:45], v28 offset:0\nds_read_b64_tr_b16 v[46:47], v29 offset:128\nds_read_b64_tr_b16 v[48:49], v29 offset:256\nds_read_b64_tr_b16 v[50:51], v29 offset:384\n"
      "ds_read_b64_tr_b16 v[52:53], v29 offset:512\nds_read_b64_tr_b16 v[54:55], v29 offset:640\nds_read_b128 v[92:95], v0 offset:0\nds_read_b128 v[96:99], v0 offset:1024\n"
      "ds_read_b128 v[100:103], v0 offset:2048\nds_read_b128 v[104:107], v0 offset:3072\nds_read_b128 v[108:111], v0 offset:4096\nds_read_b128 v[112:115], v0 offset:5120\ns_lshl_b32 s49, s36, 4\n"
      "s_add_u32 s49, s49, 162304\nv_add_u32 v24, s49, v3\nv_add_u32 v1, 87040, v1\nv_add_u32 v2, 87104, v2\nv_add_u32 v3, 87136, v3\nv_and_b32 v16, 15, v17\nv_min_u32 v16, 12, v16\n"
      "v_lshlrev_b32 v16, 15, v16\ns_sub_u32 s55, s56, 2\ns_waitcnt lgkmcnt(6)\nv_readlane_b32 s48, v57, 48\nv_and_b32 v33, 0x1ffe, v33\nv_lshl_add_u32 v33, v33, 2, v16\n"
      "v_pk_mul_f16 v80, v56, v58\nv_pk_mul_f16 v81, v57, v59\nv_pk_mul_f16 v82, v44, v44 op_sel_hi:[0,1]\nv_pk_mul_f16 v83, v44, v45 op_sel_hi:[0,1]\nv_pk_mul_f16 v76, v56, s48 op_sel:[0,1]\n"
      "v_pk_mul_f16 v77, v57, s48 op_sel:[0,1]\nv_pk_mul_f16 v78, v58, s48 op_sel:[0,1]\nv_pk_mul_f16 v79, v59, s48 op_sel:[0,1]\nds_read_b128 v[116:119], v0 offset:6144\n"
      "ds_read_b128 v[120:123], v0 offset:7168\nds_read_b128 v[124:127], v0 offset:8192\nds_read_b128 v[128:131], v0 offset:9216\nds_read_b128 v[132:135], v0 offset:10240\n"
      "ds_read_b128 v[136:139], v0 offset:11264\nds_read_b128 v[140:143], v0 offset:12288\nds_read_b128 v[144:147], v0 offset:13312\nds_read_b128 v[148:151], v0 offset:14336\n"
      "ds_read_b128 v[152:155], v0 offset:15360\nds_read_b128 v[156:159], v0 offset:16384\nds_read_b128 v[160:163], v0 offset:17408\nds_read_b128 v[164:167], v0 offset:18432\n"
      "ds_read_b128 v[168:171], v0 offset:19456\nds_read_b128 v[172:175], v0 offset:20480\nds_read_b128 v[176:179], v0 offset:21504\nds_read_b128 v[180:183], v0 offset:22528\n"
      "ds_read_b128 v[184:187], v0 offset:23552\nds_read_b128 v[188:191], v0 offset:24576\nds_read_b128 v[192:195], v0 offset:25600\nds_read_b128 v[196:199], v0 offset:26624\n"
      "ds_read_b128 v[200:203], v0 offset:27648\nds_read_b128 v[204:207], v0 offset:28672\nds_read_b128 v[208:211], v0 offset:29696\nds_read_b128 v[212:215], v0 offset:30720\n"
      "ds_read_b128 v[216:219], v0 offset:31744\nds_read_b128 v[220:223], v0 offset:32768\nds_read_b128 v[224:227], v0 offset:33792\nds_read_b128 v[228:231], v0 offset:34816\n"
      "ds_read_b128 v[232:235], v0 offset:35840\nds_read_b128 v[236:239], v0 offset:36864\nds_read_b128 v[240:243], v0 offset:37888\nds_read_b128 v[244:247], v0 offset:38912\n"
      "ds_read_b128 v[248:251], v0 offset:39936\nds_read_b128 v[252:255], v0 offset:40960\nds_read_b128 v[36:39], v1\nds_read_b64 v[40:41], v2\nds_read_b32 v42, v3\n"
      "ds_read_b128 v[72:75], v22 offset:48\nv_and_b32 v1, 1, v17\nv_mul_u32_u24 v1, 0xaaaa, v1\nv_add_u32 v27, 0x4444, v1\ns_add_u32 s60, s20, 65536\ns_addc_u32 s61, s21, 0\n"
      "s_xor_b32 s40, s39, 0xa5c30f96\ns_lshl_b32 s38, s38, 16\ns_not_b32 s50, s40\nv_mov_b32 v19, s50\ns_mul_i32 s41, s39, 0x9E3779B1\ns_lshr_b32 s41, s41, 24\ns_cmp_ge_u32 s43, s56\n"
      "s_waitcnt lgkmcnt(15)\ns_cbranch_scc1 .Lc_fin\n.p2align 6\n"
      ".Lc_top:\nv_mfma_f32_16x16x32_f16 v[64:67], v[92:95], v[76:79], 0\nds_add_rtn_u32 v21, v26, v25\nv_pk_mul_f16 v84, v54, v54 op_sel_hi:[0,1]\nv_pk_mul_f16 v85, v54, v45 op_sel_hi:[0,1]\n"
      "v_mfma_f32_16x16x32_f16 v[64:67], v[104:107], v[80:83], v[64:67]\nv_pk_mul_f16 v86, v44, v54 op_sel_hi:[0,1]\nv_add_f32_dpp v43, v43, v43 quad_perm:[1,0,3,2] row_mask:0xf bank_mask:0xf\n"
      "v_mfma_f32_16x16x32_f16 v[68:71], v[108:111], v[80:83], 0\nv_pk_mul_f16 v87, v44, v55 op_sel_hi:[0,1]\nv_add_f32_dpp v43, v43, v43 quad_perm:[2,3,0,1] row_mask:0xf bank_mask:0xf\n"
      "s_setprio 0\nv_mfma_f32_16x16x32_f16 v[68:71], v[96:99], v[76:79], v[68:71]\nv_pk_mul_f16 v88, v46, v54 op_sel_hi:[0,1]\n"
      "v_add_f32_dpp v43, v43, v43 row_half_mirror row_mask:0xf bank_mask:0xf\nv_mfma_f32_16x16x32_f16 v[60:63], v[100:103], v[80:83], 0\nv_pk_mul_f16 v89, v46, v55 op_sel_hi:[0,1]\n"
      "v_add_f32_dpp v43, v43, v43 row_mirror row_mask:0xf bank_mask:0xf\nds_write_b32 v23, v43\ns_waitcnt lgkmcnt(1)\nv_smfmac_f32_16x16x64_f16 v[72:75], v[112:115], v[76:83], v27\n"
      "v_pk_mul_f16 v90, v48, v54 op_sel_hi:[0,1]\nv_pk_mul_f16 v91, v48, v55 op_sel_hi:[0,1]\ns_lshl_b32 s50, s43, 6\nv_mfma_f32_16x16x32_f16 v[60:63], v[116:119], v[84:87], v[60:63]\n"
      "v_pk_mul_f16 v76, v50, v54 op_sel_hi:[0,1]\nv_pk_mul_f16 v77, v50, v55 op_sel_hi:[0,1]\nv_mfma_f32_16x16x32_f16 v[60:63], v[128:131], v[88:91], v[60:63]\n"
      "v_pk_mul_f16 v78, v52, v54 op_sel_hi:[0,1]\nv_readfirstlane_b32 s43, v21\nv_mfma_f32_16x16x32_f16 v[64:67], v[132:135], v[88:91], v[64:67]\nv_pk_mul_f16 v79, v52, v55 op_sel_hi:[0,1]\n"
      "v_add_u32 v23, s50, v24\ns_min_u32 s49, s43, 19\ns_mulk_i32 s49, 864\ns_add_u32 s49, s49, s53\nv_mfma_f32_16x16x32_f16 v[64:67], v[120:123], v[84:87], v[64:67]\n"
      "v_pk_mul_f16 v80, v52, v52 op_sel_hi:[0,1]\nv_add_u32_sdwa v30, s49, v35 dst_sel:DWORD dst_unused:UNUSED_PAD src0_sel:DWORD src1_sel:WORD_0\n"
      "v_mfma_f32_16x16x32_f16 v[68:71], v[124:127], v[84:87], v[68:71]\nv_pk_mul_f16 v81, v52, v45 op_sel_hi:[0,1]\n"
      "v_add_u32_sdwa v31, s49, v35 dst_sel:DWORD dst_unused:UNUSED_PAD src0_sel:DWORD src1_sel:WORD_1\nv_mfma_f32_16x16x32_f16 v[68:71], v[136:139], v[88:91], v[68:71]\n"
      "v_pk_mul_f16 v82, v44, v52 op_sel_hi:[0,1]\nv_add_u32_sdwa v28, s49, v34 dst_sel:DWORD dst_unused:UNUSED_PAD src0_sel:DWORD src1_sel:WORD_0\n"
      "v_smfmac_f32_16x16x64_f16 v[72:75], v[140:143], v[84:91], v27\nv_pk_mul_f16 v83, v44, v53 op_sel_hi:[0,1]\n"
      "v_add_u32_sdwa v29, s49, v34 dst_sel:DWORD dst_unused:UNUSED_PAD src0_sel:DWORD src1_sel:WORD_1\nv_mfma_f32_16x16x32_f16 v[60:63], v[144:147], v[76:79], v[60:63]\n"
      "v_pk_mul_f16 v84, v46, v52 op_sel_hi:[0,1]\nv_pk_mul_f16 v85, v46, v53 op_sel_hi:[0,1]\nds_read_b64_tr_b16 v[58:59], v31\nv_mfma_f32_16x16x32_f16 v[60:63], v[156:159], v[80:83], v[60:63]\n"
      "v_pk_mul_f16 v86, v48, v52 op_sel_hi:[0,1]\ns_setprio 1\nds_read_b64_tr_b16 v[56:57], v30\nv_mfma_f32_16x16x32_f16 v[64:67], v[160:163], v[80:83], v[64:67]\n"
      "v_pk_mul_f16 v87, v48, v53 op_sel_hi:[0,1]\nds_read_b64_tr_b16 v[54:55], v29 offset:640\nv_mfma_f32_16x16x32_f16 v[64:67], v[148:151], v[76:79], v[64:67]\n"
      "v_pk_mul_f16 v88, v50, v52 op_sel_hi:[0,1]\nv_mfma_f32_16x16x32_f16 v[68:71], v[152:155], v[76:79], v[68:71]\nv_pk_mul_f16 v89, v50, v53 op_sel_hi:[0,1]\ns_cmp_lt_u32 s43, s55\n"
      "s_cbranch_scc1 .Ll1085\nglobal_load_dwordx2 v[18:19], v33, s[60:61] sc1\ns_movk_i32 s55, 99\n"
      ".Ll1085:\nv_mfma_f32_16x16x32_f16 v[68:71], v[164:167], v[80:83], v[68:71]\nv_pk_mul_f16 v90, v50, v50 op_sel_hi:[0,1]\nv_smfmac_f32_16x16x64_f16 v[72:75], v[168:171], v[76:83], v27\n"
      "v_pk_mul_f16 v91, v50, v45 op_sel_hi:[0,1]\nv_mfma_f32_16x16x32_f16 v[60:63], v[172:175], v[84:87], v[60:63]\nv_pk_mul_f16 v76, v44, v50 op_sel_hi:[0,1]\n"
      "v_pk_mul_f16 v77, v44, v51 op_sel_hi:[0,1]\nds_read_b64_tr_b16 v[52:53], v29 offset:512\nv_mfma_f32_16x16x32_f16 v[60:63], v[184:187], v[88:91], v[60:63]\n"
      "v_pk_mul_f16 v78, v46, v50 op_sel_hi:[0,1]\nv_mfma_f32_16x16x32_f16 v[64:67], v[188:191], v[88:91], v[64:67]\nv_pk_mul_f16 v79, v46, v51 op_sel_hi:[0,1]\n"
      "v_mfma_f32_16x16x32_f16 v[64:67], v[176:179], v[84:87], v[64:67]\nv_pk_mul_f16 v80, v48, v50 op_sel_hi:[0,1]\ns_waitcnt lgkmcnt(1)\nv_readlane_b32 s48, v57, 48\n"
      "v_mfma_f32_16x16x32_f16 v[68:71], v[180:183], v[84:87], v[68:71]\nv_pk_mul_f16 v81, v48, v51 op_sel_hi:[0,1]\nv_mfma_f32_16x16x32_f16 v[68:71], v[192:195], v[88:91], v[68:71]\n"
      "v_pk_mul_f16 v82, v48, v48 op_sel_hi:[0,1]\nv_smfmac_f32_16x16x64_f16 v[72:75], v[196:199], v[84:91], v27\nv_pk_mul_f16 v83, v48, v45 op_sel_hi:[0,1]\n"
      "v_mfma_f32_16x16x32_f16 v[60:63], v[200:203], v[76:79], v[60:63]\nv_pk_mul_f16 v84, v44, v48 op_sel_hi:[0,1]\nv_pk_mul_f16 v85, v44, v49 op_sel_hi:[0,1]\ns_setprio 2\n"
      "ds_read_b64_tr_b16 v[50:51], v29 offset:384\nv_mfma_f32_16x16x32_f16 v[60:63], v[212:215], v[80:83], v[60:63]\nv_pk_mul_f16 v86, v46, v48 op_sel_hi:[0,1]\n"
      "v_pk_mul_f16 v87, v46, v49 op_sel_hi:[0,1]\nv_mfma_f32_16x16x32_f16 v[64:67], v[216:219], v[80:83], v[64:67]\nv_pk_mul_f16 v88, v46, v46 op_sel_hi:[0,1]\n"
      "v_mfma_f32_16x16x32_f16 v[64:67], v[204:207], v[76:79], v[64:67]\nv_pk_mul_f16 v89, v46, v45 op_sel_hi:[0,1]\nds_read_b64_tr_b16 v[48:49], v29 offset:256\n"
      "v_mfma_f32_16x16x32_f16 v[68:71], v[208:211], v[76:79], v[68:71]\nv_pk_mul_f16 v90, v44, v46 op_sel_hi:[0,1]\nv_mfma_f32_16x16x32_f16 v[68:71], v[220:223], v[80:83], v[68:71]\n"
      "v_pk_mul_f16 v91, v44, v47 op_sel_hi:[0,1]\nv_smfmac_f32_16x16x64_f16 v[72:75], v[224:227], v[76:83], v27\nds_read_b64_tr_b16 v[44:45], v28 offset:0\n"
      "ds_read_b64_tr_b16 v[46:47], v29 offset:128\nv_smfmac_f32_16x16x64_f16 v[72:75], v[252:255], v[84:91], v27\nv_pk_mul_f16 v76, v56, s48 op_sel:[0,1]\nv_pk_mul_f16 v77, v57, s48 op_sel:[0,1]\n"
      "v_mfma_f32_16x16x32_f16 v[68:71], v[236:239], v[84:87], v[68:71]\nv_pk_mul_f16 v78, v58, s48 op_sel:[0,1]\nv_pk_mul_f16 v79, v59, s48 op_sel:[0,1]\n"
      "v_mfma_f32_16x16x32_f16 v[68:71], v[248:251], v[88:91], v[68:71]\nv_pk_mul_f16 v80, v56, v58\nv_pk_mul_f16 v81, v57, v59\nv_mfma_f32_16x16x32_f16 v[60:63], v[240:243], v[88:91], v[60:63]\n"
      "v_add_f32 v72, v72, v73\nv_add_f32 v74, v74, v75\nv_mfma_f32_16x16x32_f16 v[60:63], v[228:231], v[84:87], v[60:63]\nv_mul_f32 v43, v72, v42\nv_mul_f32 v43, v43, v74\n"
      "ds_read_b128 v[72:75], v22 offset:48\nv_mfma_f32_16x16x32_f16 v[64:67], v[232:235], v[84:87], v[64:67]\nv_mul_f32 v16, v68, v40\nv_fmac_f32 v43, v16, v70\n"
      "v_mfma_f32_16x16x32_f16 v[64:67], v[244:247], v[88:91], v[64:67]\nv_mul_f32 v16, v69, v41\nv_fmac_f32 v43, v16, v71\nv_mul_f32 v60, v60, v36\nv_mul_f32 v61, v61, v37\n"
      "v_mul_f32 v62, v62, v38\nv_mul_f32 v63, v63, v39\ns_waitcnt lgkmcnt(1)\nv_pk_mul_f16 v82, v44, v44 op_sel_hi:[0,1]\nv_pk_mul_f16 v83, v44, v45 op_sel_hi:[0,1]\ns_cmp_lt_u32 s43, s56\n"
      "v_fmac_f32 v43, v60, v64\nv_fmac_f32 v43, v61, v65\nv_fmac_f32 v43, v62, v66\nv_fmac_f32 v43, v63, v67\ns_cbranch_scc1 .Lc_top\n"
      ".Lc_fin:\ns_mov_b32 s51, 0\nv_and_b32 v1, 15, v17\ns_nop 1\nv_add_f32_dpp v43, v43, v43 quad_perm:[1,0,3,2] row_mask:0xf bank_mask:0xf\ns_nop 1\n"
      "v_add_f32_dpp v43, v43, v43 quad_perm:[2,3,0,1] row_mask:0xf bank_mask:0xf\ns_nop 1\nv_add_f32_dpp v43, v43, v43 row_half_mirror row_mask:0xf bank_mask:0xf\ns_nop 1\n"
      "v_add_f32_dpp v43, v43, v43 row_mirror row_mask:0xf bank_mask:0xf\nds_write_b32 v23, v43\n"
      ".Lc_g:\ns_waitcnt vmcnt(0)\nv_and_b32 v2, 0xff, v18\nv_xor_b32 v2, s41, v2\nv_xor_b32 v3, s40, v19\nv_or_b32 v2, v2, v3\nv_cmp_ne_u32 vcc, 0, v2\ns_cbranch_vccz .Lc_g1\n"
      "global_load_dwordx2 v[18:19], v33, s[60:61] sc1\ns_add_u32 s51, s51, 1\ns_bitcmp0_b32 s51, 22\ns_cbranch_scc1 .Lc_g\ns_waitcnt vmcnt(0)\n"
      ".Lc_g1:\nv_cmp_gt_u32 vcc, 13, v1\nv_and_b32 v2, 0xffffff00, v18\ns_nop 0\nv_cndmask_b32 v2, 0, v2, vcc\ns_nop 1\nv_add_f32_dpp v2, v2, v2 quad_perm:[1,0,3,2] row_mask:0xf bank_mask:0xf\n"
      "s_nop 1\nv_add_f32_dpp v2, v2, v2 quad_perm:[2,3,0,1] row_mask:0xf bank_mask:0xf\ns_nop 1\nv_add_f32_dpp v2, v2, v2 row_half_mirror row_mask:0xf bank_mask:0xf\ns_nop 1\n"
      "v_add_f32_dpp v2, v2, v2 row_mirror row_mask:0xf bank_mask:0xf\nv_add_f32 v2, s38, v2\nds_write_b32 v32, v2 offset:4\ns_lshl_b32 s50, s24, 6\nv_add_u32 v0, s50, v17\nv_min_u32 v1, 79, v0\n"
      "v_lshlrev_b32 v2, 4, v1\nv_lshlrev_b32 v3, 2, v1\nv_and_b32 v1, 0x7c, v1\nv_lshlrev_b32 v1, 1, v1\nv_add_u32 v2, 162304, v2\nv_add_u32 v3, 159232, v3\nv_add_u32 v1, 159552, v1\n"
      "v_alignbit_b32 v0, v0, v0, 2\nv_cmp_gt_u32 vcc, s56, v0\ns_waitcnt lgkmcnt(0)\ns_barrier\ns_cbranch_vccz .Lc_end\nds_read_b128 v[4:7], v2\nds_read_b32 v3, v3\nds_read_b64 v[8:9], v1\n"
      "s_waitcnt lgkmcnt(1)\nv_add_f32 v4, v4, v5\nv_add_f32 v6, v6, v7\nv_add_f32 v4, v4, v6\nv_ldexp_f32 v4, v4, v3\ns_nop 1\n"
      "v_add_f32_dpp v4, v4, v4 quad_perm:[1,0,3,2] row_mask:0xf bank_mask:0xf\ns_nop 1\nv_add_f32_dpp v4, v4, v4 quad_perm:[2,3,0,1] row_mask:0xf bank_mask:0xf\ns_waitcnt lgkmcnt(0)\n"
      "v_add_f32 v4, v4, v9\nv_and_b32 v8, 0x1ffe, v8\nv_cvt_pk_bf16_f32 v4, v4, v4\ns_mov_b64 exec, vcc\nglobal_store_short v8, v4, s[22:23]\ns_mov_b64 exec, -1\n"
      ".Lc_end:\n"
      ".Lxd_end:"
      :
      : "{v[236:239]}"(boff), "{v[248:251]}"(mrow), "{v252}"(msc), "{v[254:255]}"(rsp), "{s[4:19]}"(ka0), "{s[20:23]}"(ka1), "{s[24:31]}"(gs), [blk] "s"(blockIdx.x), [pl] "s"(g_tab.pool[blockIdx.x
        >> 4]), [kp] "i"(KP[0] | KP[1] << 5 | KP[2] << 10 | KP[3] << 15 | KP[4] << 20)
      : "memory", "vcc", "scc", "s33", "s34", "s35", "s36", "s37", "s38", "s39", "s40", "s41", "s42", "s43", "s44", "s45", "s46", "s47", "s48", "s49", "s50", "s51", "s52", "s53", "s54", "s55", "s56", "s57", "s58", "s59", "s60", "s61", "s62", "s63", "s64", "s65", "s66", "s67", "s68", "s69",
        "v0", "v1", "v2", "v3", "v4", "v5", "v6", "v7", "v8", "v9", "v10", "v11", "v12", "v13", "v14", "v15", "v16", "v17", "v18", "v19", "v20", "v21", "v22", "v23", "v24", "v25", "v26", "v27",
        "v28", "v29", "v30", "v31", "v32", "v33", "v34", "v35", "v36", "v37", "v38", "v39", "v40", "v41", "v42", "v43", "v44", "v45", "v46", "v47", "v48", "v49", "v50", "v51", "v52", "v53", "v54",
        "v55", "v56", "v57", "v58", "v59", "v60", "v61", "v62", "v63", "v64", "v65", "v66", "v67", "v68", "v69", "v70", "v71", "v72", "v73", "v74", "v75", "v76", "v77", "v78", "v79", "v80", "v81",
        "v82", "v83", "v84", "v85", "v86", "v87", "v88", "v89", "v90", "v91", "v92", "v93", "v94", "v95", "v96", "v97", "v98", "v99", "v100", "v101", "v102", "v103", "v104", "v105", "v106", "v107",
        "v108", "v109", "v110", "v111", "v112", "v113", "v114", "v115", "v116", "v117", "v118", "v119", "v120", "v121", "v122", "v123", "v124", "v125", "v126", "v127", "v128", "v129", "v130",
        "v131", "v132", "v133", "v134", "v135", "v136", "v137", "v138", "v139", "v140", "v141", "v142", "v143", "v144", "v145", "v146", "v147", "v148", "v149", "v150", "v151", "v152", "v153",
        "v154", "v155", "v156", "v157", "v158", "v159", "v160", "v161", "v162", "v163", "v164", "v165", "v166", "v167", "v168", "v169", "v170", "v171", "v172", "v173", "v174", "v175", "v176",
        "v177", "v178", "v179", "v180", "v181", "v182", "v183", "v184", "v185", "v186", "v187", "v188", "v189", "v190", "v191", "v192", "v193", "v194", "v195", "v196", "v197", "v198", "v199",
        "v200", "v201", "v202", "v203", "v204", "v205", "v206", "v207", "v208", "v209", "v210", "v211", "v212", "v213", "v214", "v215", "v216", "v217", "v218", "v219", "v220", "v221", "v222",
        "v223", "v224", "v225", "v226", "v227", "v228", "v229", "v230", "v231", "v232", "v233", "v234", "v235", "v240", "v241", "v242", "v243", "v244", "v245", "v246", "v247", "v253");
}

extern "C" void kernel_launch(void* const* d_in, const int* in_sizes, int n_in, void* d_out, int out_size,
                              void* d_ws, size_t ws_size, hipStream_t stream) {
  const int* idx = (const int*)d_in[0];
  const u16* table = (const u16*)d_in[1];
  const u16* ws_cin = (const u16*)d_in[2];
  const u16* wout = (const u16*)d_in[3];
  const u16* w1 = (const u16*)d_in[4];
  const u16* b1 = (const u16*)d_in[5];
  const u16* w2 = (const u16*)d_in[6];
  const u16* b_out = (const u16*)d_in[7];
  dnn_kernel<<<256, 512, 0, stream>>>(idx, table, ws_cin, w1, b1, w2, wout, b_out, (char*)d_ws, (u16*)d_out);
}
```

```cpp
#include <hip/hip_runtime.h>
#include <hip/hip_bf16.h>
#include <stdint.h>
#include <utility>

typedef __attribute__((ext_vector_type(8))) short bf16x8;
typedef __attribute__((ext_vector_type(4))) short s16x4;
typedef __attribute__((ext_vector_type(4))) float f32x4;
typedef __attribute__((ext_vector_type(2))) float f32x2;
typedef __attribute__((ext_vector_type(2))) __bf16 bf16x2;
typedef __attribute__((ext_vector_type(16))) float f32x16;
typedef unsigned short u16;
typedef __attribute__((ext_vector_type(2))) unsigned u32x2;
typedef __attribute__((ext_vector_type(4))) unsigned u32x4;

constexpr int NB = 4096, NF = 26, ND = 64, DIN = 1664;

#define STAMP(k,i)
#define WSTAMP(i)
template <class F, int... I>
__device__ __forceinline__ void static_for(F& f, std::integer_sequence<int, I...>) { (f(std::integral_constant<int, I>{}), ...); }
template <int CTRL, int RM = 0xf>
__device__ __forceinline__ float dpp(float v) { return __builtin_bit_cast(float, __builtin_amdgcn_update_dpp(0, __builtin_bit_cast(int, v), CTRL, RM, 0xf, false)); }
__device__ __forceinline__ unsigned pk2(float a, float b) { return __builtin_bit_cast(unsigned, __builtin_convertvector((f32x2){a, b}, bf16x2)); }

template <int V> using IC = std::integral_constant<int, V>;
typedef __attribute__((ext_vector_type(8))) unsigned u32x8;
typedef __attribute__((ext_vector_type(16))) unsigned u32x16;
constexpr int STAGE = 224 * 128 + 64 * 256, G_LDS = 163840;
constexpr int MC = 41 * 1024;
constexpr int W3S = STAGE, U_OFF = W3S + 35840, NC_OFF = U_OFF + 2048, GT_OFF = NC_OFF + 1024, WO_OFF = GT_OFF + 2048, DP = 4352, W2S = 0;
static_assert(WO_OFF + 256 <= 2 * STAGE, "lds");
constexpr int WS_RESP = 4096, WS_POOL = 32768, WS_M = 1 << 20;
#define NCLV 6
constexpr int NSLOT = 19, NCL = NCLV;

struct Geo { unsigned nt, s0, ns; };
constexpr __host__ __device__ Geo geo(unsigned e) {
  const unsigned i = (e * 79) >> 10, nt = e - 13 * i, c = nt < 9 ? 13108 : 13798, t = i * c + ((nt * 247) & 1023);
  return Geo{nt, t >> 10, ((t + c) >> 10) - (t >> 10)};
}
constexpr unsigned long long builders() {
  unsigned long long k = 0;
  for (unsigned x = 0; x < 8; ++x)
    for (unsigned s = 32; s-- > 0;)
      if (geo(32 * x + s).ns == 12) k = (k & ~(31ull << (5 * x))) | (unsigned long long)s << (5 * x);
  return k;
}
constexpr unsigned long long BK = builders();
#define KEEP 10, 13, 15, 12, 10
constexpr int KP[5] = {KEEP};
constexpr int WANT[5] = {138, 163, 177, 135, 113};
constexpr int kind(unsigned b) { return ((BK >> (5 * (b & 7))) & 31) == b >> 3 ? 3 + ((b & 7) >> 2) : 14 - geo((b & 7) * 32 + (b >> 3)).ns; }
constexpr __host__ __device__ unsigned group(unsigned b) { return ((b >> 3) + 2 * b) & 15; }
constexpr bool geo_ok() {
  for (unsigned nt = 0; nt < 13; ++nt) {
    unsigned s = 0;
    for (unsigned e = nt; e < 256; e += 13) {
      const Geo g = geo(e);
      if (g.nt != nt || g.s0 != s || g.ns < 12 || g.ns > 14 || e < g.s0 || e >= g.s0 + g.ns) return false;
      s += g.ns;
    }
    if (s != 256) return false;
  }
  return true;
}
static_assert(geo_ok(), "tiles");

struct PI { int a, b; };
constexpr PI prod(int n) {
  int m = n - 2;
  for (int jj = 0; jj < 6; ++jj) {
    const int j = jj ? 6 - jj : 0;
    if (m < 2 + 2 * j) return m == 0 ? PI{2 * j, 2 * j} : m == 1 ? PI{2 * j, 1} : PI{(m - 2) & ~1, 2 * j + (m & 1)};
    m -= 2 + 2 * j;
  }
  return PI{0, 0};
}
constexpr int xfield(int k, int h, int g) { return k == 1 ? 24 + h : 4 * (k >> 1) + ((g + 2 * (k & 1) + h) & 3); }
constexpr int pqfield(int q, int sl, int g) { return g < 3 ? 8 * g + 4 * (sl >> 1) + (sl & 1) + (((g ^ q) & 1) << 1) : sl == 3 ? 26 : 24 + ((sl + q) >> 1); }
constexpr unsigned short slot_code(int f, int t, int g, int h) {
  if (f == 0) {
    const int sl = 2 * (t & 1) + h, q = t >> 1;
    return g == 3 && (q || (sl & 1)) ? 0x8000 : 0x4000 | 6 << 10 | (26 * 33 + pqfield(q, sl, g));
  }
  const int n = 4 * (f - 1) + t;
  const int p = n < 2 ? pqfield(0, 2 * n + h, g) : xfield(prod(n).a, 0, g), q = n < 2 ? pqfield(1, 2 * n + h, g) : xfield(prod(n).b, h, g);
  return (p == 26 ? 0x4000 | 10 << 10 : p == q ? 1 << 10 : 2 << 10) | (p * 33 + q);
}
struct GT { unsigned short t[2][64][8], pool[16][64]; };
constexpr GT make_gt() {
  GT r{};
  for (int l = 0; l < 64; ++l)
    for (int e = 0; e < 8; ++e) {
      r.t[0][l][e] = l < 48 ? slot_code(l >> 2, e >> 1, l & 3, e & 1) : 0x8000;
      r.t[1][l][e] = l < 48 ? slot_code(2 * (l >> 3) + ((l >> 1) & 1), 2 * ((e >> 1) & 1) + ((l >> 2) & 1), 2 * (l & 1) + (e >> 2), e & 1) : 0x8000;
    }
  int tot = 0, dem = 0, acc = 0, k = 15, b = 0;
  for (int i = 0; i < 256; ++i) { tot += 16 - KP[kind(i)]; dem += WANT[kind(i)] - 10 * KP[kind(i)]; }
  for (int q = 0, n = 0; q < 16; ++q) {
    for (int i = 0; i < 256; ++i) acc += group(i) == q ? WANT[kind(i)] - 10 * KP[kind(i)] : 0;
    const int len = (acc * tot + dem / 2) / dem - n;
    n += len;
    r.pool[q][0] = len;
    for (int i = 1; i <= len; ++i) {
      while (k < KP[kind(b)]) { ++b; k = 15; }
      r.pool[q][i] = ((b & 7) * 32 + (b >> 3)) * 16 + k--;
    }
  }
  return r;
}
constexpr bool pool_ok() {
  const GT t = make_gt();
  for (int q = 0; q < 16; ++q) {
    int n = 0;
    for (int i = 0; i < 256; ++i) n += group(i) != q ? 0 : NSLOT - KP[kind(i)] < NCL ? NSLOT - KP[kind(i)] : NCL;
    if (t.pool[q][0] > 62 || t.pool[q][0] > n) return false;
    const int fb = 16 * q < 24 ? 0 : 16 * q - 24 > 192 ? 192 : 16 * q - 24;
    for (int i = 1; i <= t.pool[q][0]; ++i) {
      const int b = ((t.pool[q][i] >> 4) & 31) * 8 + (t.pool[q][i] >> 9);
      if (b < fb || b >= fb + 64) return false;
    }
  }
  for (int k = 0; k < 5; ++k) if (KP[k] < 10 || KP[k] > 16) return false;
  return true;
}
static_assert(pool_ok(), "pools");
static __device__ const __attribute__((aligned(16))) GT g_tab = make_gt();

__global__ __launch_bounds__(512, 2) void dnn_kernel(const int* __restrict__ idx, const u16* __restrict__ table,
                                                     const u16* __restrict__ ws_cin, const u16* __restrict__ w1,
                                                     const u16* __restrict__ b1, const u16* __restrict__ w2,
                                                     const u16* __restrict__ wout, const u16* __restrict__ b_out,
                                                     char* __restrict__ ws, u16* __restrict__ out) {
  __shared__ __attribute__((aligned(1024))) char shm[G_LDS];
  u32x4 boff, ka1;
  u32x8 gs;
  u32x16 ka0;
  unsigned tch;
  int lane;
  asm volatile(
      "s_load_dwordx16 s[4:19], %[ka], 0\ns_load_dwordx4 s[20:23], %[ka], 64\nv_readfirstlane_b32 s24, %[tid]\ns_and_b32 s36, %[blk], 7\ns_lshr_b32 s37, %[blk], 3\ns_lshl_b32 s30, s36, 5\n"
      "s_add_u32 s30, s30, s37\ns_lshr_b32 s24, s24, 6\ns_mul_i32 s38, s30, 79\ns_lshr_b32 s38, s38, 10\ns_mul_i32 s28, s38, 13\ns_sub_u32 s28, s30, s28\nv_and_b32 v240, 63, %[tid]\n"
      "s_lshr_b32 s27, s36, 1\ns_add_u32 s27, s27, s36\ns_and_b32 s39, s28, 3\ns_mul_i32 s39, s39, 3\ns_add_u32 s27, s27, s39\ns_sub_u32 s39, s27, 12\ns_min_u32 s27, s27, s39\n"
      "s_lshl_b32 s27, s27, 1\ns_cmp_ge_u32 s24, 4\ns_cbranch_scc1 .Lf1\ns_movk_i32 s39, 13798\nv_mul_u32_u24 v245, 9363, v240\ns_cmp_lt_u32 s28, 9\ns_cselect_b32 s39, 13108, s39\n"
      "s_or_b32 s41, s27, 2\ns_mul_i32 s40, s28, 247\nv_lshrrev_b32 v245, 16, v245\ns_and_b32 s40, s40, 1023\nv_mad_i32_i24 v246, v245, -7, v240\ns_mul_i32 s38, s38, s39\n"
      "v_lshl_add_u32 v246, v246, 2, s41\ns_add_u32 s38, s38, s40\nv_subrev_u32 v247, 26, v246\ns_lshr_b32 s29, s38, 10\nv_mul_u32_u24 v245, 26, v245\ns_add_u32 s38, s38, s39\n"
      "v_min_u32 v246, v246, v247\ns_lshr_b32 s38, s38, 10\nv_add_lshl_u32 v241, v246, v245, 2\ns_sub_u32 s38, s38, s29\nv_add_u32 v244, 192, v241\ns_lshl_b32 s38, s38, 1\n"
      "v_add_u32 v243, 128, v241\ns_sub_u32 s25, s38, 24\nv_add_u32 v242, 64, v241\ns_min_u32 s26, s24, s25\ns_mul_i32 s38, s24, 6\ns_add_u32 s26, s26, s38\ns_mul_i32 s38, s26, 896\n"
      "s_add_u32 m0, s38, 135168\ns_lshl1_add_u32 s38, s29, s26\ns_mul_i32 s38, s38, 832\ns_mov_b32 exec_hi, 0xffffff\ns_sub_u32 s38, s38, 192\ns_subb_u32 s39, 0, 0\ns_waitcnt lgkmcnt(0)\n"
      "s_add_u32 s38, s4, s38\ns_addc_u32 s39, s5, s39\nglobal_load_lds_dwordx4 v244, s[38:39] offset:0\nglobal_load_lds_dwordx4 v243, s[38:39] offset:896\n"
      "global_load_lds_dwordx4 v242, s[38:39] offset:1792\nglobal_load_lds_dwordx4 v241, s[38:39] offset:2688\ns_add_u32 s38, s38, 3328\ns_addc_u32 s39, s39, 0\ns_add_u32 m0, m0, 3584\n"
      "s_cmp_ge_u32 s24, s25\nglobal_load_lds_dwordx4 v244, s[38:39]\nglobal_load_lds_dwordx4 v243, s[38:39] offset:896\ns_cbranch_scc1 .Li0\nglobal_load_lds_dwordx4 v242, s[38:39] offset:1792\n"
      ".Li0:\ns_mov_b32 exec_hi, -1\ns_and_b32 s41, s24, 3\nv_lshrrev_b32 v245, 4, v240\nv_and_b32 v246, 15, v240\ns_mul_i32 s40, s41, 53248\nv_lshlrev_b32 v238, 1, v245\n"
      "v_mul_u32_u24 v245, 3328, v245\nv_xor_b32 v247, v246, v238\nv_add_u32 v245, s40, v245\nv_lshl_add_u32 v236, v247, 4, v245\nv_xor_b32 v238, 128, v236\nv_add_u32 v237, 12288, v236\n"
      "v_add_u32 v239, 36864, v238\nv_add_u32 v238, 24576, v238\ns_lshl_b32 s40, s28, 8\ns_add_u32 s10, s10, s40\ns_addc_u32 s11, s11, 0\ns_branch .Lf2\n"
      ".Lf1:\ns_getpc_b64 s[42:43]\n"
      ".Lxd_pc0:\ns_and_b32 s41, s24, 3\nv_lshrrev_b32 v245, 4, v240\nv_and_b32 v246, 15, v240\ns_mul_i32 s40, s41, 53248\nv_lshlrev_b32 v238, 1, v245\nv_mul_u32_u24 v245, 3328, v245\n"
      "v_xor_b32 v247, v246, v238\nv_add_u32 v245, s40, v245\nv_lshl_add_u32 v236, v247, 4, v245\nv_xor_b32 v238, 128, v236\nv_add_u32 v237, 12288, v236\nv_add_u32 v239, 36864, v238\n"
      "v_add_u32 v238, 24576, v238\ns_cmp_lg_u32 s24, 7\ns_cbranch_scc1 .Lf3\nv_lshl_add_u32 v245, s37, 4, v246\nv_lshlrev_b32 v245, 7, v245\nv_min_u32 v245, .Lxd_end-.Lxd_pc0-4, v245\n"
      "global_load_dword v253, v245, s[42:43]\n"
      ".Lf3:\ns_lshl_b32 s41, s41, 12\ns_add_u32 m0, s41, 118784\ns_lshl_b32 s40, s28, 8\ns_add_u32 s39, s27, 2\ns_mul_i32 s39, s39, 212992\ns_add_u32 s39, s39, s40\nv_lshlrev_b32 v244, 4, v240\n"
      "s_waitcnt lgkmcnt(0)\ns_add_u32 s42, s10, s39\ns_addc_u32 s43, s11, 0\nglobal_load_lds_dwordx4 v236, s[42:43] offset:0\nglobal_load_lds_dwordx4 v237, s[42:43] offset:1024\n"
      "global_load_lds_dwordx4 v238, s[42:43] offset:2048\nglobal_load_lds_dwordx4 v239, s[42:43] offset:3072\ns_add_u32 s12, s12, s40\ns_addc_u32 s13, s13, 0\ns_add_u32 s14, s14, s40\n"
      "s_addc_u32 s15, s15, 0\ns_add_u32 s42, s42, 212992\ns_addc_u32 s43, s43, 0\ns_mov_b32 m0, 160896\ns_mov_b64 exec, 0xffff\nglobal_load_lds_dwordx4 v244, s[12:13]\ns_mov_b32 m0, 161152\n"
      "s_nop 0\nglobal_load_lds_dwordx4 v244, s[14:15]\ns_add_u32 m0, s41, 28672\ns_mov_b64 exec, -1\nglobal_load_lds_dwordx4 v236, s[42:43] offset:0\n"
      "global_load_lds_dwordx4 v237, s[42:43] offset:1024\nglobal_load_lds_dwordx4 v238, s[42:43] offset:2048\nglobal_load_lds_dwordx4 v239, s[42:43] offset:3072\ns_movk_i32 s39, 13798\n"
      "s_cmp_lt_u32 s28, 9\ns_cselect_b32 s39, 13108, s39\ns_mul_i32 s40, s28, 247\ns_and_b32 s40, s40, 1023\ns_mul_i32 s38, s38, s39\ns_add_u32 s38, s38, s40\ns_lshr_b32 s29, s38, 10\n"
      "s_add_u32 s38, s38, s39\ns_lshr_b32 s38, s38, 10\ns_sub_u32 s38, s38, s29\ns_lshl_b32 s38, s38, 1\ns_sub_u32 s25, s38, 24\n"
      ".Lf2:\ns_add_u32 s27, s27, 2\ns_mul_i32 s36, s36, 5\ns_mov_b32 s38, %[bkl]\ns_mov_b32 s39, %[bkh]\ns_lshr_b64 s[38:39], s[38:39], s36\ns_and_b32 s38, s38, 31\ns_xor_b32 s31, s38, s37\n"
      : "=&{v[236:239]}"(boff), "=&{v240}"(lane), "=&{v253}"(tch), "=&{s[4:19]}"(ka0), "=&{s[20:23]}"(ka1), "=&{s[24:31]}"(gs)
      : [ka] "s"(__builtin_amdgcn_kernarg_segment_ptr()), [blk] "s"(blockIdx.x), [tid] "v"(threadIdx.x), [bkl] "i"((unsigned)BK), [bkh] "i"((unsigned)(BK >> 32))
      : "memory", "scc", "s36", "s37", "s38", "s39", "s40", "s41", "s42", "s43", "v241", "v242", "v243", "v244", "v245", "v246", "v247");
#define SB __builtin_amdgcn_sched_barrier(0)
#define PIN(x) asm volatile("" : "+v"(x))
  const int tid = threadIdx.x, w = gs[0];
  const unsigned sb = (unsigned)(size_t)(__attribute__((address_space(3))) char*)shm;
  const int g = lane >> 4, fr = lane & 15, hf = lane >> 5, l31 = lane & 31;
  constexpr unsigned TX = 0xA5C30F96u;
  auto chk = [](unsigned t) -> unsigned { return (t * 0x9E3779B1u) >> 24; };
  const int bid = blockIdx.x & 7;
  const bool bld = gs[7] == 0, rblk = bid >= 4;
  const int fi = (bid & 3) * 8 + w, mi_ = min(fi, NF - 1);
  const unsigned tc = sb + (g & 1) * 32 + (fr & 3) * 8, h0 = 8 * (g >> 1) + (fr >> 2);
  u32x4 mrow;
  u32x2 rsp;
  unsigned msc;
  if (__builtin_expect(bld, 0)) {
    unsigned ma_, ur[2], uw[6], fa[2][2];
    if (rblk) {
      const unsigned o3 = min(w * 4480, NF * NF * NF * 2 - 4480);
      asm volatile("s_mov_b32 m0, %2\n\ts_nop 0\n\tglobal_load_lds_dwordx4 %0, %1\n\tglobal_load_lds_dwordx4 %0, %1 offset:1024\n\tglobal_load_lds_dwordx4 %0, %1 offset:2048\n\t"
                   "global_load_lds_dwordx4 %0, %1 offset:3072\n\tglobal_load_lds_dwordx4 %0, %1 offset:3456"
                   :: "v"(lane * 16), "s"((const char*)(ws_cin + 2 * NF * NF * NF) + o3), "s"(sb + W3S + o3) : "memory");
      const unsigned rr = (bid & 3) * 8 + ((lane >> 2) & 7);
      const unsigned vo = (hf * NF * NF + min(rr, 25u) * NF) * 2 + (lane & 3) * 16;
#pragma unroll
      for (int m = 0; m < 2; ++m) {
        const unsigned p = min(w + 8 * m, 12);
        asm volatile("s_mov_b32 m0, %2\n\ts_nop 0\n\tglobal_load_lds_dwordx4 %0, %1" :: "v"(vo), "s"(ws_cin + (NF + 2 * p) * NF * NF), "s"(sb + W2S + p * 1024) : "memory");
      }
      asm volatile("ds_write_b32 %0, %1" :: "v"(sb + U_OFF + tid * 4), "v"(0) : "memory");
    } else {
#pragma unroll
      for (int s = 0; s < 2; ++s)
        asm volatile("s_mov_b32 m0, %2\n\ts_nop 0\n\tglobal_load_lds_dwordx4 %0, %1" :: "v"(min(l31, NF - 1) * NF * 2 + hf * 16), "s"(ws_cin + mi_ * NF * NF + s * 16), "s"(sb + W3S + w * DP + s * 1024) : "memory");
    }
    if (w < 3) asm volatile("s_mov_b32 m0, %2\n\ts_nop 0\n\tglobal_load_lds_dwordx4 %0, %1 sc1" :: "v"(lane * 16), "s"(w ? (const char*)&g_tab + (w - 1) * 1024 : ws), "s"(sb + NC_OFF + w * 1024) : "memory");
    if (w == 3) asm volatile("s_mov_b32 m0, %2\n\ts_nop 0\n\tglobal_load_lds_dword %0, %1" :: "v"(min(lane, 38) * 4), "s"(wout), "s"(sb + WO_OFF) : "memory");
    {
      const int ty = bid >> 2, f = lane >> 2;
      const bool sp = fi >= 24;
      int c0 = sp ? 5 : fi < 16 ? ty - 1 : 1, c1 = sp ? 16 : 3072, rho = sp ? 4 * (fi - 24) + 2 * ty : fi < 16 ? fi : 2 * (fi & 6) + (fi & 1) + 2 * ty;
      asm volatile("" : "+s"(c0), "+s"(c1), "+s"(rho));
      int t = (7 * (f >> 1) + c0) * 1024 + (f & 1) * c1 + ((lane & 3) * 16 + rho) * 16;
      PIN(t);
      ma_ = min((unsigned)t | (fi < 28 ? 0u : ~0u), 41u * 1024 + 1024);
      ma_ = lane < 48 ? ma_ : 43008;
    }
#pragma unroll
    for (int p = 0; p < 2; ++p) {
      ur[p] = sb + W3S + min(8 * g + 4 * p + (fr >> 2), NF - 1) * (NF * NF * 2) + w * 32 + (fr & 3) * 8;
      fa[0][p] = tc + W2S + ((h0 + 4 * p) * 8 + w) * 64; fa[1][p] = tc + W2S + (min(h0 + 16 + 4 * p, 25u) * 8 + w) * 64;
      PIN(ur[p]); PIN(fa[0][p]); PIN(fa[1][p]);
    }
#pragma unroll
    for (int m = 0; m < 6; ++m) {
      const unsigned e = (w + 8 * m) * 16 + fr;
      uw[m] = sb + U_OFF + e * 2 + ((e * 2521u) >> 16) * 12;
      PIN(uw[m]);
      if (m == 5) uw[m] = e < NF * NF ? uw[m] : 0x40000u;
    }
    PIN(uw[5]); PIN(ma_);
    asm volatile("s_waitcnt vmcnt(0) lgkmcnt(0)\n\ts_barrier" :: "{v253}"(tch) : "memory");
    const unsigned wov = *(const unsigned*)(shm + WO_OFF + lane * 4);
    const u32x4 tb = *(const u32x4*)(shm + GT_OFF + (fi >= 24) * 1024 + lane * 16);
    bf16x8 P[2], Q[2];
    if (rblk) {
      s16x4 wl[6][2];
      u32x4 wa;
      unsigned wo2;
      asm volatile("ds_read2_b64 %0, %1 offset0:13 offset1:14" : "=v"(wa) : "v"(sb + WO_OFF + g * 16));
      asm volatile("ds_read_u16 %0, %1 offset:52" : "=v"(wo2) : "v"(sb + WO_OFF + lane * 2));
#pragma unroll
      for (int m = 0; m < 6; ++m)
#pragma unroll
        for (int p = 0; p < 2; ++p) asm volatile("ds_read_b64_tr_b16 %0, %1 offset:%2" : "=v"(wl[m][p]) : "v"(ur[p]), "i"(m * 256));
      u32x2 fl[2][2];
#pragma unroll
      for (int s = 0; s < 2; ++s)
#pragma unroll
        for (int p = 0; p < 2; ++p) asm volatile("ds_read_b64_tr_b16 %0, %1" : "=v"(fl[s][p]) : "v"(fa[s][p]));
      asm volatile("s_waitcnt lgkmcnt(0)" : "+v"(wa), "+v"(wo2), "+v"(wl[0][0]), "+v"(wl[0][1]), "+v"(wl[1][0]), "+v"(wl[1][1]), "+v"(wl[2][0]), "+v"(wl[2][1]), "+v"(wl[3][0]), "+v"(wl[3][1]),
                   "+v"(wl[4][0]), "+v"(wl[4][1]), "+v"(wl[5][0]), "+v"(wl[5][1]), "+v"(fl[0][0]), "+v"(fl[0][1]), "+v"(fl[1][0]), "+v"(fl[1][1]));
      const unsigned km = g < 3 ? ~0u : 0u;
      const u32x4 wm = {wa[0], wa[1] & km, wa[2] & km, wa[3] & km};
      f32x4 du[6];
#pragma unroll
      for (int m = 0; m < 6; ++m)
        du[m] = __builtin_amdgcn_mfma_f32_16x16x32_bf16(__builtin_bit_cast(bf16x8, wm), __builtin_shufflevector(wl[m][0], wl[m][1], 0, 1, 2, 3, 4, 5, 6, 7), (f32x4){0.f, 0.f, 0.f, 0.f}, 0, 0, 0);
      asm volatile("ds_write_b16 %0, %1" :: "v"(lane < NF ? sb + U_OFF + lane * 64 + 52 : 0x40000u), "v"(wo2) : "memory");
#pragma unroll
      for (int m = 0; m < 6; ++m) {
        asm volatile("ds_write_b16 %0, %1" :: "v"(uw[m]), "v"(pk2(du[m][0], du[m][0])) : "memory");
        asm volatile("" :: "v"(du[m]));
      }
      asm volatile("s_waitcnt lgkmcnt(0)\n\ts_barrier" ::: "memory");
      s16x4 ul[2][2];
#pragma unroll
      for (int s = 0; s < 2; ++s)
#pragma unroll
        for (int p = 0; p < 2; ++p)
          asm volatile("ds_read_b64_tr_b16 %0, %1 offset:%2" : "=v"(ul[s][p]) : "v"(tc + U_OFF + h0 * 64), "i"(s * 1024 + p * 256));
      const unsigned fmask = l31 < NF ? ~0u : 0u;
#pragma unroll
      for (int s = 0; s < 2; ++s) {
        u32x4 q4 = {fl[s][0][0] & fmask, fl[s][0][1] & fmask, fl[s][1][0] & fmask, fl[s][1][1] & fmask};
        Q[s] = __builtin_bit_cast(bf16x8, q4);
      }
      asm volatile("s_waitcnt lgkmcnt(0)" : "+v"(ul[0][0]), "+v"(ul[0][1]), "+v"(ul[1][0]), "+v"(ul[1][1]));
#pragma unroll
      for (int s = 0; s < 2; ++s) P[s] = __builtin_shufflevector(ul[s][0], ul[s][1], 0, 1, 2, 3, 4, 5, 6, 7);
    } else {
#pragma unroll
      for (int s = 0; s < 2; ++s) {
        u32x4 xf = *(const u32x4*)(shm + W3S + w * DP + s * 1024 + lane * 16);
        if (s == 1) { const unsigned m = hf ? 0u : ~0u; xf[1] &= m; xf[2] &= m; xf[3] &= m; }
        Q[s] = __builtin_bit_cast(bf16x8, xf);
        const unsigned e = (l31 >> 1) - 8 * s - 4 * hf, v = 0x3f80u << ((lane & 1) * 16);
        u32x4 q4 = {e == 0 ? v : 0u, e == 1 ? v : 0u, e == 2 ? v : 0u, e == 3 ? v : 0u};
        P[s] = __builtin_bit_cast(bf16x8, q4);
      }
    }
    f32x16 D = {};
#pragma unroll
    for (int s = 0; s < 2; ++s) {
      D = __builtin_amdgcn_mfma_f32_32x32x16_bf16(P[s], Q[s], D, 0, 0, 0);
      D = __builtin_amdgcn_mfma_f32_32x32x16_bf16(Q[s], P[s], D, 0, 0, 0);
    }
    float* dl = (float*)(shm + W3S + w * DP);
    D[14] = lane == NF ? __uint_as_float(((unsigned)__builtin_amdgcn_readlane(wov, mi_ >> 1) >> ((mi_ & 1) * 16)) << 16) : D[14];
#pragma unroll
    for (int r = 0; r < 15; ++r) dl[((r & 3) + 8 * (r >> 2) + 4 * hf) * 33 + l31] = D[r];
    __builtin_amdgcn_wave_barrier();
    float v[8], mx = 0.f;
    const unsigned zm = fi >= NF ? 0xffff : rblk ? 0x8000 : 0xc000;
#pragma unroll
    for (int e = 0; e < 8; ++e) v[e] = dl[(tb[e >> 1] >> (16 * (e & 1))) & 0x3ff];
#pragma unroll
    for (int e = 0; e < 8; ++e) {
      const unsigned c = tb[e >> 1] >> (16 * (e & 1));
      float x = __builtin_amdgcn_ldexpf(v[e], ((c >> 10) & 15) - 2);
      PIN(x);
      v[e] = c & zm ? 0.f : x;
      mx = fmaxf(mx, fabsf(v[e]));
    }
    mx = fmaxf(mx, dpp<0xB1>(mx)); mx = fmaxf(mx, dpp<0x4E>(mx)); mx = fmaxf(mx, dpp<0x141>(mx)); mx = fmaxf(mx, dpp<0x140>(mx));
    const unsigned mxu = max(max((unsigned)__builtin_amdgcn_readlane(__float_as_int(mx), 0), (unsigned)__builtin_amdgcn_readlane(__float_as_int(mx), 16)), (unsigned)__builtin_amdgcn_readlane(__float_as_int(mx), 32));
    const unsigned ex = min(max(mxu >> 23, 40u), 250u);
    const float al = __uint_as_float((268 - ex) << 23);
#pragma unroll
    for (int e = 0; e < 4; ++e) {
      const unsigned q = pk2(v[2 * e] * al, v[2 * e + 1] * al);
      mrow[e] = __builtin_bit_cast(unsigned, __builtin_amdgcn_cvt_pkrtz(__uint_as_float(q << 16), __uint_as_float(q & 0xffff0000u)));
    }
    msc = fi < NF ? (ex - 14) << 23 : 0u;
    PIN(msc);
    asm volatile("global_store_dwordx4 %0, %1, %2 sc1" :: "v"(ma_), "{v[248:251]}"(mrow), "s"(ws + WS_M) : "memory");
    asm volatile("global_store_dword %0, %1, %2 sc1" :: "v"((fi < 16 ? (fi >> 2) * 64 + (fi & 3) * 4 : fi < 24 ? ((fi & 6) << 5) + (fi & 1) * 4 + 16 : (fi & 3) * 64 + (fi & 4) + 24) + (bid & 4) * 8), "{v252}"(msc), "s"(ws + WS_M + MC) : "memory");
    if (fi == 31) asm volatile("global_store_dword %0, %1, %2 sc1\n\ts_waitcnt vmcnt(0)" :: "v"((lane & 15) * 128), "{v252}"(msc), "s"(ws + WS_POOL) : "memory");
    const unsigned mval = *(const unsigned*)(shm + NC_OFF + (tid & 255) * 4), qval = *(const unsigned*)(shm + NC_OFF);
    rsp = (u32x2){qval << 8 | chk(mval), mval ^ TX};
    PIN(rsp);
    asm volatile("s_waitcnt lgkmcnt(0)\n\ts_barrier" ::: "memory");
  } else {
    asm volatile("" : "=v"(mrow), "=v"(rsp), "=v"(msc));
  }
  static_assert(STAGE == 45056 && MC == 41984 && NSLOT == 19 && WS_RESP == 4096 && WS_POOL == 32768 && WS_M == 1048576 && NCL == 6, "constants of the code below");
  asm volatile(
      "s_and_b32 s36, s24, 3\ns_lshl_b32 s42, %[blk], 2\ns_load_dword s37, s[20:21], s42\ns_load_dword s38, s[18:19], 0\ns_bfe_u32 s42, %[blk], 0x10002\ns_add_u32 s42, s42, 3\n"
      "s_lshr_b32 s43, s25, 1\ns_sub_u32 s43, 2, s43\ns_cmp_eq_u32 s31, 0\ns_cselect_b32 s42, s42, s43\ns_mul_i32 s42, s42, 5\ns_lshr_b32 s64, %[kp], s42\ns_and_b32 s64, s64, 31\n"
      "s_cmp_ge_u32 s24, 4\ns_cbranch_scc1 .Lgemm\ns_mul_i32 s42, s26, 896\ns_cmp_ge_u32 s36, s25\ns_cselect_b64 vcc, 0, -1\ns_cselect_b32 s43, 0, 5376\ns_and_b32 s44, s27, 2\n"
      "s_xor_b32 s44, s44, 2\ns_lshl_b32 s44, s44, 2\ns_add_u32 s42, s42, s44\ns_add_u32 s42, s42, 135168\nv_lshrrev_b32 v22, 3, v240\nv_mul_u32_u24 v6, 112, v22\nv_add_u32 v6, s42, v6\n"
      "v_add_u32 v7, s43, v6\nv_lshrrev_b32 v22, 4, v240\ns_lshl_b32 s42, s26, 2\nv_xor_b32 v22, v22, v240\nv_xor_b32 v22, s42, v22\nv_and_b32 v22, 7, v22\nv_lshlrev_b32 v4, 4, v22\n"
      "v_xor_b32 v5, 64, v4\ns_sub_u32 s44, s6, 1024\ns_subb_u32 s45, s7, 0\ns_sub_u32 s46, s6, 2048\ns_subb_u32 s47, s7, 0\ns_sub_u32 s48, s6, 3072\ns_subb_u32 s49, s7, 0\n"
      "s_lshl_b32 s53, s26, 10\ns_lshl_b32 s54, s36, 12\ns_add_u32 s54, s54, 28672\ns_add_u32 s52, s27, 1\ns_mul_i32 s52, s52, 212992\ns_add_u32 s52, s52, 212992\ns_cmp_eq_u32 s52, 5537792\n"
      "s_cselect_b32 s52, 0, s52\ns_add_u32 s50, s10, s52\ns_addc_u32 s51, s11, 0\ns_waitcnt vmcnt(0)\nds_read_b64 v[8:9], v6 offset:0\nds_read_b64 v[10:11], v6 offset:896\n"
      "ds_read_b64 v[12:13], v6 offset:1792\nds_read_b64 v[14:15], v6 offset:2688\nds_read_b64 v[16:17], v6 offset:3584\nds_read_b64 v[18:19], v6 offset:4480\nds_read_b64 v[20:21], v7 offset:0\n"
      "s_waitcnt lgkmcnt(0)\nv_lshl_add_u32 v22, v8, 7, v4\nv_lshl_add_u32 v23, v10, 7, v5\nv_lshl_add_u32 v24, v12, 7, v4\nv_lshl_add_u32 v25, v14, 7, v5\nv_lshl_add_u32 v26, v16, 7, v4\n"
      "v_lshl_add_u32 v27, v18, 7, v5\nv_lshl_add_u32 v28, v20, 7, v4\ns_add_u32 m0, s53, 90112\ns_nop 0\nglobal_load_lds_dwordx4 v22, s[6:7] offset:0\n"
      "global_load_lds_dwordx4 v23, s[44:45] offset:1024\nglobal_load_lds_dwordx4 v24, s[46:47] offset:2048\nglobal_load_lds_dwordx4 v25, s[48:49] offset:3072\ns_add_u32 m0, s53, 94208\ns_nop 0\n"
      "global_load_lds_dwordx4 v26, s[6:7] offset:0\nglobal_load_lds_dwordx4 v27, s[44:45] offset:1024\ns_cbranch_vccz .La1001\nglobal_load_lds_dwordx4 v28, s[46:47] offset:2048\n"
      ".La1001:\nv_lshl_add_u32 v22, v9, 7, v4\nv_lshl_add_u32 v23, v11, 7, v5\nv_lshl_add_u32 v24, v13, 7, v4\nv_lshl_add_u32 v25, v15, 7, v5\nv_lshl_add_u32 v26, v17, 7, v4\n"
      "v_lshl_add_u32 v27, v19, 7, v5\nv_lshl_add_u32 v28, v21, 7, v4\ns_barrier\ns_add_u32 m0, s53, 0\ns_nop 0\nglobal_load_lds_dwordx4 v22, s[6:7] offset:0\n"
      "global_load_lds_dwordx4 v23, s[44:45] offset:1024\nglobal_load_lds_dwordx4 v24, s[46:47] offset:2048\nglobal_load_lds_dwordx4 v25, s[48:49] offset:3072\ns_add_u32 m0, s53, 4096\ns_nop 0\n"
      "global_load_lds_dwordx4 v26, s[6:7] offset:0\nglobal_load_lds_dwordx4 v27, s[44:45] offset:1024\ns_cbranch_vccz .La1002\nglobal_load_lds_dwordx4 v28, s[46:47] offset:2048\n"
      ".La1002:\nds_read_b64 v[8:9], v6 offset:8\nds_read_b64 v[10:11], v6 offset:904\nds_read_b64 v[12:13], v6 offset:1800\nds_read_b64 v[14:15], v6 offset:2696\n"
      "ds_read_b64 v[16:17], v6 offset:3592\nds_read_b64 v[18:19], v6 offset:4488\nds_read_b64 v[20:21], v7 offset:8\nv_lshrrev_b32 v30, 2, v240\nv_bfe_u32 v31, v240, 1, 1\ns_lshl_b32 s42, s36, 1\n"
      "v_lshrrev_b32 v32, 3, v240\nv_or_b32 v31, s42, v31\ns_sub_u32 s42, s30, s29\ns_lshl_b32 s42, s42, 11\nv_xor_b32 v31, v31, v32\nv_lshl_add_u32 v30, v30, 7, s42\nv_and_b32 v32, 1, v240\n"
      "v_lshl_add_u32 v30, v31, 4, v30\nv_lshl_add_u32 v30, v32, 3, v30\nv_add_u32 v31, 90112, v30\nv_mov_b32 v32, 0\nv_mov_b32 v33, 0x3d800000\ns_mov_b32 s55, 0x80008000\ns_waitcnt lgkmcnt(0)\n"
      "v_lshl_add_u32 v22, v8, 7, v4\nv_lshl_add_u32 v23, v10, 7, v5\nv_lshl_add_u32 v24, v12, 7, v4\nv_lshl_add_u32 v25, v14, 7, v5\nv_lshl_add_u32 v26, v16, 7, v4\n"
      "v_lshl_add_u32 v27, v18, 7, v5\nv_lshl_add_u32 v28, v20, 7, v4\ns_waitcnt vmcnt(7) lgkmcnt(0)\ns_cbranch_vccnz .Lw1003\ns_waitcnt vmcnt(6)\n"
      ".Lw1003:\ns_barrier\ns_add_u32 m0, s54, 45056\ns_nop 0\nglobal_load_lds_dwordx4 v236, s[50:51] offset:0\nglobal_load_lds_dwordx4 v237, s[50:51] offset:1024\n"
      "global_load_lds_dwordx4 v238, s[50:51] offset:2048\nglobal_load_lds_dwordx4 v239, s[50:51] offset:3072\ns_add_u32 m0, s53, 45056\ns_nop 0\nglobal_load_lds_dwordx4 v22, s[6:7] offset:0\n"
      "global_load_lds_dwordx4 v23, s[44:45] offset:1024\nglobal_load_lds_dwordx4 v24, s[46:47] offset:2048\nglobal_load_lds_dwordx4 v25, s[48:49] offset:3072\ns_add_u32 m0, s53, 49152\ns_nop 0\n"
      "global_load_lds_dwordx4 v26, s[6:7] offset:0\nglobal_load_lds_dwordx4 v27, s[44:45] offset:1024\ns_cbranch_vccz .La1004\nglobal_load_lds_dwordx4 v28, s[46:47] offset:2048\n"
      ".La1004:\nds_read_b64 v[44:45], v31 offset:0\nv_lshl_add_u32 v22, v9, 7, v4\nv_lshl_add_u32 v23, v11, 7, v5\nv_lshl_add_u32 v24, v13, 7, v4\nv_lshl_add_u32 v25, v15, 7, v5\n"
      "v_lshl_add_u32 v26, v17, 7, v4\nv_lshl_add_u32 v27, v19, 7, v5\nv_lshl_add_u32 v28, v21, 7, v4\nds_read_b64 v[8:9], v6 offset:16\nds_read_b64 v[10:11], v6 offset:912\n"
      "ds_read_b64 v[12:13], v6 offset:1808\nds_read_b64 v[14:15], v6 offset:2704\nds_read_b64 v[16:17], v6 offset:3600\nds_read_b64 v[18:19], v6 offset:4496\nds_read_b64 v[20:21], v7 offset:16\n"
      "s_add_u32 s52, s52, 212992\ns_cmp_eq_u32 s52, 5537792\ns_cselect_b32 s52, 0, s52\ns_add_u32 s50, s10, s52\ns_addc_u32 s51, s11, 0\ns_waitcnt vmcnt(11) lgkmcnt(0)\ns_cbranch_vccnz .Lw1006\n"
      "s_waitcnt vmcnt(10)\n"
      ".Lw1006:\ns_barrier\ns_add_u32 m0, s54, 90112\ns_nop 0\nglobal_load_lds_dwordx4 v236, s[50:51] offset:0\nglobal_load_lds_dwordx4 v237, s[50:51] offset:1024\n"
      "global_load_lds_dwordx4 v238, s[50:51] offset:2048\nglobal_load_lds_dwordx4 v239, s[50:51] offset:3072\ns_add_u32 s52, s52, 212992\ns_cmp_eq_u32 s52, 5537792\ns_cselect_b32 s52, 0, s52\n"
      "s_add_u32 s50, s10, s52\ns_addc_u32 s51, s11, 0\ns_add_u32 m0, s53, 90112\ns_nop 0\nglobal_load_lds_dwordx4 v22, s[6:7] offset:0\nv_lshl_add_u32 v22, v8, 7, v4\n"
      "global_load_lds_dwordx4 v23, s[44:45] offset:1024\nv_lshl_add_u32 v23, v10, 7, v5\nglobal_load_lds_dwordx4 v24, s[46:47] offset:2048\nv_lshl_add_u32 v24, v12, 7, v4\n"
      "ds_read_b64 v[46:47], v30 offset:0\nglobal_load_lds_dwordx4 v25, s[48:49] offset:3072\nv_lshl_add_u32 v25, v14, 7, v5\nv_max3_f16 v32, |v44|, |v45|, v32\nv_max3_f32 v33, |v44|, |v45|, v33\n"
      "s_add_u32 m0, s53, 94208\ns_nop 0\nglobal_load_lds_dwordx4 v26, s[6:7] offset:0\nv_lshl_add_u32 v26, v16, 7, v4\nv_and_b32 v96, s55, v44\nv_xor_b32 v44, v44, v96\n"
      "global_load_lds_dwordx4 v27, s[44:45] offset:1024\nv_lshl_add_u32 v27, v18, 7, v5\nv_and_b32 v97, s55, v45\nv_xor_b32 v45, v45, v97\ns_cbranch_vccz .La1007\n"
      "global_load_lds_dwordx4 v28, s[46:47] offset:2048\n"
      ".La1007:\nv_lshl_add_u32 v28, v20, 7, v4\ns_cmp_lg_u32 s31, 0\ns_cbranch_scc1 .Lb1008\ns_lshl_b32 s42, s24, 9\nv_lshl_add_u32 v39, v240, 3, s42\ns_and_b32 s42, %[blk], 7\n"
      "s_lshl_b32 s42, s42, 11\ns_add_u32 s42, s42, 4096\ns_add_u32 s58, s20, s42\ns_addc_u32 s59, s21, 0\nglobal_store_dwordx2 v39, v[254:255], s[58:59] sc1\n"
      ".Lb1008:\ns_waitcnt vmcnt(11) lgkmcnt(0)\ns_cbranch_vccnz .Lw1009\ns_waitcnt vmcnt(10)\n"
      ".Lw1009:\ns_barrier\ns_add_u32 m0, s54, 0\ns_nop 0\nglobal_load_lds_dwordx4 v236, s[50:51] offset:0\nglobal_load_lds_dwordx4 v237, s[50:51] offset:1024\n"
      "global_load_lds_dwordx4 v238, s[50:51] offset:2048\nglobal_load_lds_dwordx4 v239, s[50:51] offset:3072\ns_add_u32 s52, s52, 212992\ns_cmp_eq_u32 s52, 5537792\ns_cselect_b32 s52, 0, s52\n"
      "s_add_u32 s50, s10, s52\ns_addc_u32 s51, s11, 0\ns_add_u32 m0, s53, 0\ns_nop 0\nglobal_load_lds_dwordx4 v22, s[6:7] offset:0\nv_lshl_add_u32 v22, v9, 7, v4\n"
      "ds_read_b64 v[8:9], v6 offset:24\nglobal_load_lds_dwordx4 v23, s[44:45] offset:1024\nv_lshl_add_u32 v23, v11, 7, v5\nds_read_b64 v[10:11], v6 offset:920\n"
      "ds_read_b64 v[48:49], v30 offset:45056\nglobal_load_lds_dwordx4 v24, s[46:47] offset:2048\nv_lshl_add_u32 v24, v13, 7, v4\nds_read_b64 v[12:13], v6 offset:1816\n"
      "v_max3_f16 v32, |v46|, |v47|, v32\nv_max3_f32 v33, |v46|, |v47|, v33\nglobal_load_lds_dwordx4 v25, s[48:49] offset:3072\nv_lshl_add_u32 v25, v15, 7, v5\n"
      "ds_read_b64 v[14:15], v6 offset:2712\nv_and_b32 v98, s55, v46\nv_xor_b32 v46, v46, v98\ns_add_u32 m0, s53, 4096\ns_nop 0\nglobal_load_lds_dwordx4 v26, s[6:7] offset:0\n"
      "v_lshl_add_u32 v26, v17, 7, v4\nds_read_b64 v[16:17], v6 offset:3608\nv_and_b32 v99, s55, v47\nv_xor_b32 v47, v47, v99\nglobal_load_lds_dwordx4 v27, s[44:45] offset:1024\n"
      "v_lshl_add_u32 v27, v19, 7, v5\nds_read_b64 v[18:19], v6 offset:4504\nv_cmp_eq_u32 s[62:63], 0, v240\nv_mov_b32 v248, 262144\nv_mov_b32 v247, 162176\nv_mov_b32 v241, 1\n"
      "v_cndmask_b32 v253, v248, v247, s[62:63]\ns_lshl_b32 s42, s36, 2\nv_add_u32 v245, s42, v253\ns_cbranch_vccz .La1010\nglobal_load_lds_dwordx4 v28, s[46:47] offset:2048\n"
      ".La1010:\nv_lshl_add_u32 v28, v21, 7, v4\nds_read_b64 v[20:21], v7 offset:24\ns_waitcnt vmcnt(11) lgkmcnt(0)\ns_cbranch_vccnz .Lw1011\ns_waitcnt vmcnt(10)\n"
      ".Lw1011:\ns_barrier\ns_add_u32 m0, s54, 45056\ns_nop 0\nglobal_load_lds_dwordx4 v236, s[50:51] offset:0\nglobal_load_lds_dwordx4 v237, s[50:51] offset:1024\n"
      "global_load_lds_dwordx4 v238, s[50:51] offset:2048\nglobal_load_lds_dwordx4 v239, s[50:51] offset:3072\ns_add_u32 s52, s52, 212992\ns_cmp_eq_u32 s52, 5537792\ns_cselect_b32 s52, 0, s52\n"
      "s_add_u32 s50, s10, s52\ns_addc_u32 s51, s11, 0\ns_add_u32 m0, s53, 45056\ns_nop 0\nglobal_load_lds_dwordx4 v22, s[6:7] offset:0\nv_lshl_add_u32 v22, v8, 7, v4\n"
      "ds_read_b64 v[50:51], v31 offset:0\nglobal_load_lds_dwordx4 v23, s[44:45] offset:1024\nv_lshl_add_u32 v23, v10, 7, v5\nv_max3_f16 v32, |v48|, |v49|, v32\nv_max3_f32 v33, |v48|, |v49|, v33\n"
      "global_load_lds_dwordx4 v24, s[46:47] offset:2048\nv_lshl_add_u32 v24, v12, 7, v4\nv_and_b32 v100, s55, v48\nv_xor_b32 v48, v48, v100\nglobal_load_lds_dwordx4 v25, s[48:49] offset:3072\n"
      "v_lshl_add_u32 v25, v14, 7, v5\nv_and_b32 v101, s55, v49\nv_xor_b32 v49, v49, v101\ns_add_u32 m0, s53, 49152\ns_nop 0\nglobal_load_lds_dwordx4 v26, s[6:7] offset:0\n"
      "v_lshl_add_u32 v26, v16, 7, v4\ns_mov_b32 s42, 161152\nv_lshl_add_u32 v40, v240, 2, s42\nds_read_b32 v41, v40\ns_mov_b32 s42, 163328\nv_lshl_add_u32 v42, v240, 3, s42\n"
      "global_load_lds_dwordx4 v27, s[44:45] offset:1024\nv_lshl_add_u32 v27, v18, 7, v5\nv_lshrrev_b32 v164, 4, v240\nv_bfe_u32 v165, v240, 2, 2\nv_and_b32 v166, 3, v240\n"
      "v_mul_u32_u24 v166, 0x80008, v166\nv_add_u32 v242, v164, v165\nv_and_b32 v242, 3, v242\nv_add_u32 v168, 22, v165\ns_cbranch_vccz .La1012\nglobal_load_lds_dwordx4 v28, s[46:47] offset:2048\n"
      ".La1012:\nv_lshl_add_u32 v28, v20, 7, v4\ns_waitcnt vmcnt(11) lgkmcnt(0)\ns_cbranch_vccnz .Lw1013\ns_waitcnt vmcnt(10)\n"
      ".Lw1013:\ns_barrier\ns_add_u32 m0, s54, 90112\ns_nop 0\nglobal_load_lds_dwordx4 v236, s[50:51] offset:0\nglobal_load_lds_dwordx4 v237, s[50:51] offset:1024\n"
      "global_load_lds_dwordx4 v238, s[50:51] offset:2048\nglobal_load_lds_dwordx4 v239, s[50:51] offset:3072\ns_add_u32 s52, s52, 212992\ns_cmp_eq_u32 s52, 5537792\ns_cselect_b32 s52, 0, s52\n"
      "s_add_u32 s50, s10, s52\ns_addc_u32 s51, s11, 0\ns_add_u32 m0, s53, 90112\ns_nop 0\nglobal_load_lds_dwordx4 v22, s[6:7] offset:0\nv_lshl_add_u32 v22, v9, 7, v4\n"
      "ds_read_b64 v[8:9], v6 offset:32\nglobal_load_lds_dwordx4 v23, s[44:45] offset:1024\nv_lshl_add_u32 v23, v11, 7, v5\nds_read_b64 v[10:11], v6 offset:928\nds_read_b64 v[52:53], v30 offset:0\n"
      "global_load_lds_dwordx4 v24, s[46:47] offset:2048\nv_lshl_add_u32 v24, v13, 7, v4\nds_read_b64 v[12:13], v6 offset:1824\nv_max3_f16 v32, |v50|, |v51|, v32\n"
      "v_max3_f32 v33, |v50|, |v51|, v33\nglobal_load_lds_dwordx4 v25, s[48:49] offset:3072\nv_lshl_add_u32 v25, v15, 7, v5\nds_read_b64 v[14:15], v6 offset:2720\nv_and_b32 v102, s55, v50\n"
      "v_xor_b32 v50, v50, v102\ns_add_u32 m0, s53, 94208\ns_nop 0\nglobal_load_lds_dwordx4 v26, s[6:7] offset:0\nv_lshl_add_u32 v26, v17, 7, v4\nds_read_b64 v[16:17], v6 offset:3616\n"
      "v_and_b32 v103, s55, v51\nv_xor_b32 v51, v51, v103\nglobal_load_lds_dwordx4 v27, s[44:45] offset:1024\nv_lshl_add_u32 v27, v19, 7, v5\nds_read_b64 v[18:19], v6 offset:4512\n"
      "v_lshlrev_b32 v40, 16, v41\nv_and_b32 v41, 0xffff0000, v41\nv_mul_f32 v40, 0x56000000, v40\nv_mul_f32 v41, 0x56000000, v41\nds_write_b64 v42, v[40:41]\ns_cbranch_vccz .La1014\n"
      "global_load_lds_dwordx4 v28, s[46:47] offset:2048\n"
      ".La1014:\nv_lshl_add_u32 v28, v21, 7, v4\nds_read_b64 v[20:21], v7 offset:32\ns_waitcnt vmcnt(11) lgkmcnt(0)\ns_cbranch_vccnz .Lw1015\ns_waitcnt vmcnt(10)\n"
      ".Lw1015:\ns_barrier\ns_add_u32 m0, s54, 0\ns_nop 0\nglobal_load_lds_dwordx4 v236, s[50:51] offset:0\nglobal_load_lds_dwordx4 v237, s[50:51] offset:1024\n"
      "global_load_lds_dwordx4 v238, s[50:51] offset:2048\nglobal_load_lds_dwordx4 v239, s[50:51] offset:3072\ns_add_u32 s52, s52, 212992\ns_cmp_eq_u32 s52, 5537792\ns_cselect_b32 s52, 0, s52\n"
      "s_add_u32 s50, s10, s52\ns_addc_u32 s51, s11, 0\ns_add_u32 m0, s53, 0\ns_nop 0\nglobal_load_lds_dwordx4 v22, s[6:7] offset:0\nv_lshl_add_u32 v22, v8, 7, v4\n"
      "ds_read_b64 v[54:55], v30 offset:45056\nglobal_load_lds_dwordx4 v23, s[44:45] offset:1024\nv_lshl_add_u32 v23, v10, 7, v5\nv_max3_f16 v32, |v52|, |v53|, v32\n"
      "v_max3_f32 v33, |v52|, |v53|, v33\nglobal_load_lds_dwordx4 v24, s[46:47] offset:2048\nv_lshl_add_u32 v24, v12, 7, v4\nv_and_b32 v104, s55, v52\nv_xor_b32 v52, v52, v104\n"
      "global_load_lds_dwordx4 v25, s[48:49] offset:3072\nv_lshl_add_u32 v25, v14, 7, v5\nv_and_b32 v105, s55, v53\nv_xor_b32 v53, v53, v105\ns_add_u32 m0, s53, 4096\ns_nop 0\n"
      "global_load_lds_dwordx4 v26, s[6:7] offset:0\nv_lshl_add_u32 v26, v16, 7, v4\nv_cmp_gt_u32 s[62:63], 2, v165\nv_and_b32 v165, 60, v240\ns_mov_b32 s42, 45056\n"
      "v_cndmask_b32 v168, v168, v242, s[62:63]\nv_lshl_or_b32 v242, v242, 16, v168\nv_lshl_add_u32 v242, v242, 5, v166\nv_lshl_add_u32 v252, v240, 4, s42\n"
      "global_load_lds_dwordx4 v27, s[44:45] offset:1024\nv_lshl_add_u32 v27, v18, 7, v5\ns_mov_b32 s42, 0x76325410\ns_mov_b32 s43, 0x21005410\nv_lshrrev_b64 v[168:169], v165, s[42:43]\n"
      "v_and_b32 v168, 7, v168\nv_lshl_add_u32 v168, v164, 3, v168\ns_mov_b32 s42, 0x54107632\ns_mov_b32 s43, 0x21107632\nv_lshrrev_b64 v[170:171], v165, s[42:43]\nv_and_b32 v170, 7, v170\n"
      "v_lshl_add_u32 v170, v164, 3, v170\ns_cbranch_vccz .La1016\nglobal_load_lds_dwordx4 v28, s[46:47] offset:2048\n"
      ".La1016:\nv_lshl_add_u32 v28, v20, 7, v4\ns_waitcnt vmcnt(11) lgkmcnt(0)\ns_cbranch_vccnz .Lw1017\ns_waitcnt vmcnt(10)\n"
      ".Lw1017:\ns_barrier\ns_add_u32 m0, s54, 45056\ns_nop 0\nglobal_load_lds_dwordx4 v236, s[50:51] offset:0\nglobal_load_lds_dwordx4 v237, s[50:51] offset:1024\n"
      "global_load_lds_dwordx4 v238, s[50:51] offset:2048\nglobal_load_lds_dwordx4 v239, s[50:51] offset:3072\ns_add_u32 s52, s52, 212992\ns_cmp_eq_u32 s52, 5537792\ns_cselect_b32 s52, 0, s52\n"
      "s_add_u32 s50, s10, s52\ns_addc_u32 s51, s11, 0\ns_add_u32 m0, s53, 45056\ns_nop 0\nglobal_load_lds_dwordx4 v22, s[6:7] offset:0\nv_lshl_add_u32 v22, v9, 7, v4\n"
      "ds_read_b64 v[8:9], v6 offset:40\nglobal_load_lds_dwordx4 v23, s[44:45] offset:1024\nv_lshl_add_u32 v23, v11, 7, v5\nds_read_b64 v[10:11], v6 offset:936\nds_read_b64 v[56:57], v31 offset:0\n"
      "global_load_lds_dwordx4 v24, s[46:47] offset:2048\nv_lshl_add_u32 v24, v13, 7, v4\nds_read_b64 v[12:13], v6 offset:1832\nv_max3_f16 v32, |v54|, |v55|, v32\n"
      "v_max3_f32 v33, |v54|, |v55|, v33\nglobal_load_lds_dwordx4 v25, s[48:49] offset:3072\nv_lshl_add_u32 v25, v15, 7, v5\nds_read_b64 v[14:15], v6 offset:2728\nv_and_b32 v106, s55, v54\n"
      "v_xor_b32 v54, v54, v106\ns_add_u32 m0, s53, 49152\ns_nop 0\nglobal_load_lds_dwordx4 v26, s[6:7] offset:0\nv_lshl_add_u32 v26, v17, 7, v4\nds_read_b64 v[16:17], v6 offset:3624\n"
      "v_and_b32 v107, s55, v55\nv_xor_b32 v55, v55, v107\nglobal_load_lds_dwordx4 v27, s[44:45] offset:1024\nv_lshl_add_u32 v27, v19, 7, v5\nds_read_b64 v[18:19], v6 offset:4520\n"
      "v_lshl_or_b32 v243, v170, 16, v168\nv_lshl_add_u32 v243, v243, 5, v166\nv_and_b32 v165, 1, v240\nv_mul_u32_u24 v165, 0xaaaa, v165\nv_add_u32 v244, 0x4444, v165\ns_lshl_b32 s42, s36, 4\n"
      "s_add_u32 s42, s42, 160896\nv_lshl_add_u32 v246, v164, 2, s42\ns_cbranch_vccz .La1018\nglobal_load_lds_dwordx4 v28, s[46:47] offset:2048\n"
      ".La1018:\nv_lshl_add_u32 v28, v21, 7, v4\nds_read_b64 v[20:21], v7 offset:40\ns_waitcnt vmcnt(11) lgkmcnt(0)\ns_cbranch_vccnz .Lw1019\ns_waitcnt vmcnt(10)\n"
      ".Lw1019:\ns_barrier\ns_add_u32 m0, s54, 90112\ns_nop 0\nglobal_load_lds_dwordx4 v236, s[50:51] offset:0\nglobal_load_lds_dwordx4 v237, s[50:51] offset:1024\n"
      "global_load_lds_dwordx4 v238, s[50:51] offset:2048\nglobal_load_lds_dwordx4 v239, s[50:51] offset:3072\ns_add_u32 s52, s52, 212992\ns_cmp_eq_u32 s52, 5537792\ns_cselect_b32 s52, 0, s52\n"
      "s_add_u32 s50, s10, s52\ns_addc_u32 s51, s11, 0\ns_add_u32 m0, s53, 90112\ns_nop 0\nglobal_load_lds_dwordx4 v22, s[6:7] offset:0\nv_lshl_add_u32 v22, v8, 7, v4\n"
      "ds_read_b64 v[58:59], v30 offset:0\nglobal_load_lds_dwordx4 v23, s[44:45] offset:1024\nv_lshl_add_u32 v23, v10, 7, v5\nv_max3_f16 v32, |v56|, |v57|, v32\nv_max3_f32 v33, |v56|, |v57|, v33\n"
      "global_load_lds_dwordx4 v24, s[46:47] offset:2048\nv_lshl_add_u32 v24, v12, 7, v4\nv_and_b32 v108, s55, v56\nv_xor_b32 v56, v56, v108\nglobal_load_lds_dwordx4 v25, s[48:49] offset:3072\n"
      "v_lshl_add_u32 v25, v14, 7, v5\nv_and_b32 v109, s55, v57\nv_xor_b32 v57, v57, v109\ns_add_u32 m0, s53, 94208\ns_nop 0\nglobal_load_lds_dwordx4 v26, s[6:7] offset:0\n"
      "v_lshl_add_u32 v26, v16, 7, v4\nv_and_b32 v164, 1, v164\nv_lshl_add_u32 v164, v164, 3, s24\ns_sub_u32 s42, s64, 1\nv_min_u32 v164, s42, v164\nv_lshlrev_b32 v164, 3, v164\n"
      "v_add_u32 v250, 160640, v164\nv_and_b32 v165, 15, v240\nv_min_u32 v165, 12, v165\ns_lshl_b32 s42, s30, 7\nv_lshl_add_u32 v165, v165, 15, s42\nv_add_u32 v249, v165, v164\n"
      "global_load_lds_dwordx4 v27, s[44:45] offset:1024\nv_lshl_add_u32 v27, v18, 7, v5\ns_mul_i32 s65, s36, 17632\ns_add_u32 s65, s65, 90112\ns_add_u32 s68, s64, 6\ns_min_u32 s68, s68, 19\n"
      "s_lshl_b32 s38, s38, 16\ns_cbranch_vccz .La1020\nglobal_load_lds_dwordx4 v28, s[46:47] offset:2048\n"
      ".La1020:\nv_lshl_add_u32 v28, v20, 7, v4\ns_waitcnt vmcnt(11) lgkmcnt(0)\ns_cbranch_vccnz .Lw1021\ns_waitcnt vmcnt(10)\n"
      ".Lw1021:\ns_barrier\ns_add_u32 m0, s54, 0\ns_nop 0\nglobal_load_lds_dwordx4 v236, s[50:51] offset:0\nglobal_load_lds_dwordx4 v237, s[50:51] offset:1024\n"
      "global_load_lds_dwordx4 v238, s[50:51] offset:2048\nglobal_load_lds_dwordx4 v239, s[50:51] offset:3072\ns_add_u32 s52, s52, 212992\ns_cmp_eq_u32 s52, 5537792\ns_cselect_b32 s52, 0, s52\n"
      "s_add_u32 s50, s10, s52\ns_addc_u32 s51, s11, 0\ns_add_u32 m0, s53, 0\ns_nop 0\nglobal_load_lds_dwordx4 v22, s[6:7] offset:0\nv_lshl_add_u32 v22, v9, 7, v4\n"
      "ds_read_b64 v[8:9], v6 offset:48\nglobal_load_lds_dwordx4 v23, s[44:45] offset:1024\nv_lshl_add_u32 v23, v11, 7, v5\nds_read_b64 v[10:11], v6 offset:944\n"
      "ds_read_b64 v[60:61], v30 offset:45056\nglobal_load_lds_dwordx4 v24, s[46:47] offset:2048\nv_lshl_add_u32 v24, v13, 7, v4\nds_read_b64 v[12:13], v6 offset:1840\n"
      "v_max3_f16 v32, |v58|, |v59|, v32\nv_max3_f32 v33, |v58|, |v59|, v33\nglobal_load_lds_dwordx4 v25, s[48:49] offset:3072\nv_lshl_add_u32 v25, v15, 7, v5\n"
      "ds_read_b64 v[14:15], v6 offset:2736\nv_and_b32 v110, s55, v58\nv_xor_b32 v58, v58, v110\ns_add_u32 m0, s53, 4096\ns_nop 0\nglobal_load_lds_dwordx4 v26, s[6:7] offset:0\n"
      "v_lshl_add_u32 v26, v17, 7, v4\nds_read_b64 v[16:17], v6 offset:3632\nv_and_b32 v111, s55, v59\nv_xor_b32 v59, v59, v111\nglobal_load_lds_dwordx4 v27, s[44:45] offset:1024\n"
      "v_lshl_add_u32 v27, v19, 7, v5\nds_read_b64 v[18:19], v6 offset:4528\ns_lshr_b32 s42, s24, 2\ns_mul_i32 s43, s42, 3\ns_xor_b32 s43, s43, s36\ns_add_u32 s42, s42, s64\ns_add_u32 s42, s42, 7\n"
      "s_and_b32 s42, s42, 1\ns_lshl1_add_u32 s67, s43, s42\ns_add_u32 s66, s64, s67\ns_cmp_lt_u32 s66, s68\ns_cselect_b32 s66, s66, 127\ns_sub_u32 s66, s66, 7\ns_cbranch_vccz .La1022\n"
      "global_load_lds_dwordx4 v28, s[46:47] offset:2048\n"
      ".La1022:\nv_lshl_add_u32 v28, v21, 7, v4\nds_read_b64 v[20:21], v7 offset:48\ns_waitcnt vmcnt(11) lgkmcnt(0)\ns_cbranch_vccnz .Lw1023\ns_waitcnt vmcnt(10)\n"
      ".Lw1023:\ns_barrier\ns_add_u32 m0, s54, 45056\ns_nop 0\nglobal_load_lds_dwordx4 v236, s[50:51] offset:0\nglobal_load_lds_dwordx4 v237, s[50:51] offset:1024\n"
      "global_load_lds_dwordx4 v238, s[50:51] offset:2048\nglobal_load_lds_dwordx4 v239, s[50:51] offset:3072\ns_add_u32 s52, s52, 212992\ns_cmp_eq_u32 s52, 5537792\ns_cselect_b32 s52, 0, s52\n"
      "s_add_u32 s50, s10, s52\ns_addc_u32 s51, s11, 0\ns_add_u32 m0, s53, 45056\ns_nop 0\nglobal_load_lds_dwordx4 v22, s[6:7] offset:0\nv_lshl_add_u32 v22, v8, 7, v4\n"
      "ds_read_b64 v[62:63], v31 offset:0\nglobal_load_lds_dwordx4 v23, s[44:45] offset:1024\nv_lshl_add_u32 v23, v10, 7, v5\nv_max3_f16 v32, |v60|, |v61|, v32\nv_max3_f32 v33, |v60|, |v61|, v33\n"
      "global_load_lds_dwordx4 v24, s[46:47] offset:2048\nv_lshl_add_u32 v24, v12, 7, v4\nv_and_b32 v112, s55, v60\nv_xor_b32 v60, v60, v112\nglobal_load_lds_dwordx4 v25, s[48:49] offset:3072\n"
      "v_lshl_add_u32 v25, v14, 7, v5\nv_and_b32 v113, s55, v61\nv_xor_b32 v61, v61, v113\ns_add_u32 m0, s53, 49152\ns_nop 0\nglobal_load_lds_dwordx4 v26, s[6:7] offset:0\n"
      "v_lshl_add_u32 v26, v16, 7, v4\ns_getpc_b64 s[4:5]\n"
      ".Lk1005:\ns_add_u32 s34, s4, .Lt_p-.Lk1005\ns_addc_u32 s35, s5, 0\ns_add_u32 s4, s4, .Lc_back-.Lk1005\ns_addc_u32 s5, s5, 0\nglobal_load_lds_dwordx4 v27, s[44:45] offset:1024\n"
      "v_lshl_add_u32 v27, v18, 7, v5\ns_lshr_b32 s42, %[blk], 3\ns_lshl1_add_u32 s42, %[blk], s42\ns_and_b32 s69, s42, 15\ns_lshl_b32 s43, s69, 7\ns_add_u32 s43, s43, 32768\n"
      "s_add_u32 s8, s20, s43\ns_addc_u32 s9, s21, 0\ns_add_u32 s12, s20, 65536\ns_addc_u32 s13, s21, 0\ns_add_u32 s16, s20, 2052096\ns_addc_u32 s17, s21, 0\ns_cbranch_vccz .La1024\n"
      "global_load_lds_dwordx4 v28, s[46:47] offset:2048\n"
      ".La1024:\nv_lshl_add_u32 v28, v20, 7, v4\ns_waitcnt vmcnt(11) lgkmcnt(0)\ns_cbranch_vccnz .Lw1025\ns_waitcnt vmcnt(10)\n"
      ".Lw1025:\ns_barrier\ns_add_u32 m0, s54, 90112\ns_nop 0\nglobal_load_lds_dwordx4 v236, s[50:51] offset:0\nglobal_load_lds_dwordx4 v237, s[50:51] offset:1024\n"
      "global_load_lds_dwordx4 v238, s[50:51] offset:2048\nglobal_load_lds_dwordx4 v239, s[50:51] offset:3072\ns_add_u32 s52, s52, 212992\ns_cmp_eq_u32 s52, 5537792\ns_cselect_b32 s52, 0, s52\n"
      "s_add_u32 s50, s10, s52\ns_addc_u32 s51, s11, 0\ns_add_u32 m0, s53, 90112\ns_nop 0\nglobal_load_lds_dwordx4 v22, s[6:7] offset:0\nv_lshl_add_u32 v22, v9, 7, v4\n"
      "ds_read_b64 v[8:9], v6 offset:56\nglobal_load_lds_dwordx4 v23, s[44:45] offset:1024\nv_lshl_add_u32 v23, v11, 7, v5\nds_read_b64 v[10:11], v6 offset:952\nds_read_b64 v[64:65], v30 offset:0\n"
      "global_load_lds_dwordx4 v24, s[46:47] offset:2048\nv_lshl_add_u32 v24, v13, 7, v4\nds_read_b64 v[12:13], v6 offset:1848\nv_max3_f16 v32, |v62|, |v63|, v32\n"
      "v_max3_f32 v33, |v62|, |v63|, v33\nglobal_load_lds_dwordx4 v25, s[48:49] offset:3072\nv_lshl_add_u32 v25, v15, 7, v5\nds_read_b64 v[14:15], v6 offset:2744\nv_and_b32 v114, s55, v62\n"
      "v_xor_b32 v62, v62, v114\ns_add_u32 m0, s53, 94208\ns_nop 0\nglobal_load_lds_dwordx4 v26, s[6:7] offset:0\nv_lshl_add_u32 v26, v17, 7, v4\nds_read_b64 v[16:17], v6 offset:3640\n"
      "v_and_b32 v115, s55, v63\nv_xor_b32 v63, v63, v115\nglobal_load_lds_dwordx4 v27, s[44:45] offset:1024\nv_lshl_add_u32 v27, v19, 7, v5\nds_read_b64 v[18:19], v6 offset:4536\n"
      "s_lshl_b32 s42, s69, 4\ns_max_u32 s42, s42, 24\ns_sub_u32 s42, s42, 24\ns_min_u32 s69, s42, 192\ns_lshl_b32 s42, s69, 3\ns_add_u32 s42, s42, 24576\ns_add_u32 s14, s20, s42\n"
      "s_addc_u32 s15, s21, 0\ns_cbranch_vccz .La1026\nglobal_load_lds_dwordx4 v28, s[46:47] offset:2048\n"
      ".La1026:\nv_lshl_add_u32 v28, v21, 7, v4\nds_read_b64 v[20:21], v7 offset:56\ns_waitcnt vmcnt(11) lgkmcnt(0)\ns_cbranch_vccnz .Lw1027\ns_waitcnt vmcnt(10)\n"
      ".Lw1027:\ns_barrier\ns_add_u32 m0, s54, 0\ns_nop 0\nglobal_load_lds_dwordx4 v236, s[50:51] offset:0\nglobal_load_lds_dwordx4 v237, s[50:51] offset:1024\n"
      "global_load_lds_dwordx4 v238, s[50:51] offset:2048\nglobal_load_lds_dwordx4 v239, s[50:51] offset:3072\ns_add_u32 s52, s52, 212992\ns_cmp_eq_u32 s52, 5537792\ns_cselect_b32 s52, 0, s52\n"
      "s_add_u32 s50, s10, s52\ns_addc_u32 s51, s11, 0\ns_add_u32 m0, s53, 0\ns_nop 0\nglobal_load_lds_dwordx4 v22, s[6:7] offset:0\nv_lshl_add_u32 v22, v8, 7, v4\n"
      "ds_read_b64 v[66:67], v30 offset:45056\nglobal_load_lds_dwordx4 v23, s[44:45] offset:1024\nv_lshl_add_u32 v23, v10, 7, v5\nv_max3_f16 v32, |v64|, |v65|, v32\n"
      "v_max3_f32 v33, |v64|, |v65|, v33\nglobal_load_lds_dwordx4 v24, s[46:47] offset:2048\nv_lshl_add_u32 v24, v12, 7, v4\nv_and_b32 v116, s55, v64\nv_xor_b32 v64, v64, v116\n"
      "global_load_lds_dwordx4 v25, s[48:49] offset:3072\nv_lshl_add_u32 v25, v14, 7, v5\nv_and_b32 v117, s55, v65\nv_xor_b32 v65, v65, v117\ns_add_u32 m0, s53, 4096\ns_nop 0\n"
      "global_load_lds_dwordx4 v26, s[6:7] offset:0\nv_lshl_add_u32 v26, v16, 7, v4\ns_lshl_b32 s42, %[blk], 3\ns_add_u32 s42, s42, 4096\ns_add_u32 s58, s20, s42\ns_addc_u32 s59, s21, 0\n"
      "v_and_b32 v38, 7, v240\nv_lshlrev_b32 v38, 11, v38\ns_xor_b32 s40, s37, 0xa5c30f96\ns_mul_i32 s41, s37, 0x9E3779B1\ns_lshr_b32 s41, s41, 24\n"
      "global_load_lds_dwordx4 v27, s[44:45] offset:1024\nv_lshl_add_u32 v27, v18, 7, v5\nv_lshrrev_b32 v164, 2, v240\nv_and_b32 v148, 3, v240\nv_lshl_add_u32 v148, v148, 3, s65\n"
      "v_mul_u32_u24 v164, 928, v164\nv_add_u32 v149, s65, v164\nv_add_u32 v148, v148, v164\ns_add_u32 s42, s65, -35776\nv_add_u32 v163, s42, v252\ns_cbranch_vccz .La1028\n"
      "global_load_lds_dwordx4 v28, s[46:47] offset:2048\n"
      ".La1028:\nv_lshl_add_u32 v28, v20, 7, v4\ns_waitcnt vmcnt(11) lgkmcnt(0)\ns_cbranch_vccnz .Lw1029\ns_waitcnt vmcnt(10)\n"
      ".Lw1029:\ns_barrier\ns_add_u32 m0, s54, 45056\ns_nop 0\nglobal_load_lds_dwordx4 v236, s[50:51] offset:0\nglobal_load_lds_dwordx4 v237, s[50:51] offset:1024\n"
      "global_load_lds_dwordx4 v238, s[50:51] offset:2048\nglobal_load_lds_dwordx4 v239, s[50:51] offset:3072\ns_add_u32 s52, s52, 212992\ns_cmp_eq_u32 s52, 5537792\ns_cselect_b32 s52, 0, s52\n"
      "s_add_u32 s50, s10, s52\ns_addc_u32 s51, s11, 0\ns_add_u32 m0, s53, 45056\ns_nop 0\nglobal_load_lds_dwordx4 v22, s[6:7] offset:0\nv_lshl_add_u32 v22, v9, 7, v4\n"
      "ds_read_b64 v[8:9], v6 offset:64\nglobal_load_lds_dwordx4 v23, s[44:45] offset:1024\nv_lshl_add_u32 v23, v11, 7, v5\nds_read_b64 v[10:11], v6 offset:960\nds_read_b64 v[68:69], v31 offset:0\n"
      "global_load_lds_dwordx4 v24, s[46:47] offset:2048\nv_lshl_add_u32 v24, v13, 7, v4\nds_read_b64 v[12:13], v6 offset:1856\nv_max3_f16 v32, |v66|, |v67|, v32\n"
      "v_max3_f32 v33, |v66|, |v67|, v33\nglobal_load_lds_dwordx4 v25, s[48:49] offset:3072\nv_lshl_add_u32 v25, v15, 7, v5\nds_read_b64 v[14:15], v6 offset:2752\nv_and_b32 v118, s55, v66\n"
      "v_xor_b32 v66, v66, v118\ns_add_u32 m0, s53, 49152\ns_nop 0\nglobal_load_lds_dwordx4 v26, s[6:7] offset:0\nv_lshl_add_u32 v26, v17, 7, v4\nds_read_b64 v[16:17], v6 offset:3648\n"
      "v_and_b32 v119, s55, v67\nv_xor_b32 v67, v67, v119\nglobal_load_lds_dwordx4 v27, s[44:45] offset:1024\nv_lshl_add_u32 v27, v19, 7, v5\nds_read_b64 v[18:19], v6 offset:4544\n"
      "s_mul_i32 s42, s36, 10752\ns_add_u32 s56, s42, 45056\ns_add_u32 s42, s42, 1003520\ns_add_u32 s60, s20, s42\ns_addc_u32 s61, s21, 0\ns_cbranch_vccz .La1030\n"
      "global_load_lds_dwordx4 v28, s[46:47] offset:2048\n"
      ".La1030:\nv_lshl_add_u32 v28, v21, 7, v4\nds_read_b64 v[20:21], v7 offset:64\ns_waitcnt vmcnt(11) lgkmcnt(0)\ns_cbranch_vccnz .Lw1031\ns_waitcnt vmcnt(10)\n"
      ".Lw1031:\ns_barrier\ns_add_u32 m0, s54, 90112\ns_nop 0\nglobal_load_lds_dwordx4 v236, s[50:51] offset:0\nglobal_load_lds_dwordx4 v237, s[50:51] offset:1024\n"
      "global_load_lds_dwordx4 v238, s[50:51] offset:2048\nglobal_load_lds_dwordx4 v239, s[50:51] offset:3072\ns_add_u32 s52, s52, 212992\ns_cmp_eq_u32 s52, 5537792\ns_cselect_b32 s52, 0, s52\n"
      "s_add_u32 s50, s10, s52\ns_addc_u32 s51, s11, 0\ns_add_u32 m0, s53, 90112\ns_nop 0\nglobal_load_lds_dwordx4 v22, s[6:7] offset:0\nv_lshl_add_u32 v22, v8, 7, v4\n"
      "ds_read_b64 v[70:71], v30 offset:0\nglobal_load_lds_dwordx4 v23, s[44:45] offset:1024\nv_lshl_add_u32 v23, v10, 7, v5\nv_max3_f16 v32, |v68|, |v69|, v32\nv_max3_f32 v33, |v68|, |v69|, v33\n"
      "global_load_lds_dwordx4 v24, s[46:47] offset:2048\nv_lshl_add_u32 v24, v12, 7, v4\nv_and_b32 v120, s55, v68\nv_xor_b32 v68, v68, v120\nglobal_load_lds_dwordx4 v25, s[48:49] offset:3072\n"
      "v_lshl_add_u32 v25, v14, 7, v5\nv_and_b32 v121, s55, v69\nv_xor_b32 v69, v69, v121\ns_add_u32 m0, s53, 94208\ns_nop 0\nglobal_load_lds_dwordx4 v26, s[6:7] offset:0\n"
      "v_lshl_add_u32 v26, v16, 7, v4\ns_add_u32 s42, s27, 0\ns_sub_u32 s43, s42, 26\ns_min_u32 s42, s42, s43\nv_lshl_add_u32 v150, s42, 5, v148\ns_add_u32 s42, s27, 2\ns_sub_u32 s43, s42, 26\n"
      "s_min_u32 s42, s42, s43\nv_lshl_add_u32 v151, s42, 5, v148\ns_add_u32 s42, s27, 4\ns_sub_u32 s43, s42, 26\ns_min_u32 s42, s42, s43\nv_lshl_add_u32 v152, s42, 5, v148\n"
      "global_load_lds_dwordx4 v27, s[44:45] offset:1024\nv_lshl_add_u32 v27, v18, 7, v5\ns_add_u32 s42, s27, 6\ns_sub_u32 s43, s42, 26\ns_min_u32 s42, s42, s43\nv_lshl_add_u32 v153, s42, 5, v148\n"
      "s_add_u32 s42, s27, 8\ns_sub_u32 s43, s42, 26\ns_min_u32 s42, s42, s43\nv_lshl_add_u32 v154, s42, 5, v148\ns_add_u32 s42, s27, 10\ns_sub_u32 s43, s42, 26\ns_min_u32 s42, s42, s43\n"
      "v_lshl_add_u32 v155, s42, 5, v148\ns_cbranch_vccz .La1032\nglobal_load_lds_dwordx4 v28, s[46:47] offset:2048\n"
      ".La1032:\nv_lshl_add_u32 v28, v20, 7, v4\ns_waitcnt vmcnt(11) lgkmcnt(0)\ns_cbranch_vccnz .Lw1033\ns_waitcnt vmcnt(10)\n"
      ".Lw1033:\ns_barrier\ns_add_u32 m0, s54, 0\ns_nop 0\nglobal_load_lds_dwordx4 v236, s[50:51] offset:0\nglobal_load_lds_dwordx4 v237, s[50:51] offset:1024\n"
      "global_load_lds_dwordx4 v238, s[50:51] offset:2048\nglobal_load_lds_dwordx4 v239, s[50:51] offset:3072\ns_add_u32 s52, s52, 212992\ns_cmp_eq_u32 s52, 5537792\ns_cselect_b32 s52, 0, s52\n"
      "s_add_u32 s50, s10, s52\ns_addc_u32 s51, s11, 0\ns_add_u32 m0, s53, 0\ns_nop 0\nglobal_load_lds_dwordx4 v22, s[6:7] offset:0\nv_lshl_add_u32 v22, v9, 7, v4\n"
      "ds_read_b64 v[8:9], v6 offset:72\nglobal_load_lds_dwordx4 v23, s[44:45] offset:1024\nv_lshl_add_u32 v23, v11, 7, v5\nds_read_b64 v[10:11], v6 offset:968\n"
      "ds_read_b64 v[72:73], v30 offset:45056\nglobal_load_lds_dwordx4 v24, s[46:47] offset:2048\nv_lshl_add_u32 v24, v13, 7, v4\nds_read_b64 v[12:13], v6 offset:1864\n"
      "v_max3_f16 v32, |v70|, |v71|, v32\nv_max3_f32 v33, |v70|, |v71|, v33\nglobal_load_lds_dwordx4 v25, s[48:49] offset:3072\nv_lshl_add_u32 v25, v15, 7, v5\n"
      "ds_read_b64 v[14:15], v6 offset:2760\nv_and_b32 v122, s55, v70\nv_xor_b32 v70, v70, v122\ns_add_u32 m0, s53, 4096\ns_nop 0\nglobal_load_lds_dwordx4 v26, s[6:7] offset:0\n"
      "v_lshl_add_u32 v26, v17, 7, v4\nds_read_b64 v[16:17], v6 offset:3656\nv_and_b32 v123, s55, v71\nv_xor_b32 v71, v71, v123\nglobal_load_lds_dwordx4 v27, s[44:45] offset:1024\n"
      "v_lshl_add_u32 v27, v19, 7, v5\nds_read_b64 v[18:19], v6 offset:4552\ns_add_u32 s42, s27, 12\ns_sub_u32 s43, s42, 26\ns_min_u32 s42, s42, s43\nv_lshl_add_u32 v156, s42, 5, v148\n"
      "s_add_u32 s42, s27, 14\ns_sub_u32 s43, s42, 26\ns_min_u32 s42, s42, s43\nv_lshl_add_u32 v157, s42, 5, v148\ns_add_u32 s42, s27, 16\ns_sub_u32 s43, s42, 26\ns_min_u32 s42, s42, s43\n"
      "v_lshl_add_u32 v158, s42, 5, v148\ns_cbranch_vccz .La1034\nglobal_load_lds_dwordx4 v28, s[46:47] offset:2048\n"
      ".La1034:\nv_lshl_add_u32 v28, v21, 7, v4\nds_read_b64 v[20:21], v7 offset:72\ns_waitcnt vmcnt(11) lgkmcnt(0)\ns_cbranch_vccnz .Lw1035\ns_waitcnt vmcnt(10)\n"
      ".Lw1035:\ns_barrier\ns_add_u32 m0, s54, 45056\ns_nop 0\nglobal_load_lds_dwordx4 v236, s[50:51] offset:0\nglobal_load_lds_dwordx4 v237, s[50:51] offset:1024\n"
      "global_load_lds_dwordx4 v238, s[50:51] offset:2048\nglobal_load_lds_dwordx4 v239, s[50:51] offset:3072\ns_add_u32 s52, s52, 212992\ns_cmp_eq_u32 s52, 5537792\ns_cselect_b32 s52, 0, s52\n"
      "s_add_u32 s50, s10, s52\ns_addc_u32 s51, s11, 0\ns_add_u32 m0, s53, 45056\ns_nop 0\nglobal_load_lds_dwordx4 v22, s[6:7] offset:0\nv_lshl_add_u32 v22, v8, 7, v4\n"
      "ds_read_b64 v[74:75], v31 offset:0\nglobal_load_lds_dwordx4 v23, s[44:45] offset:1024\nv_lshl_add_u32 v23, v10, 7, v5\nv_max3_f16 v32, |v72|, |v73|, v32\nv_max3_f32 v33, |v72|, |v73|, v33\n"
      "global_load_lds_dwordx4 v24, s[46:47] offset:2048\nv_lshl_add_u32 v24, v12, 7, v4\nv_and_b32 v124, s55, v72\nv_xor_b32 v72, v72, v124\nglobal_load_lds_dwordx4 v25, s[48:49] offset:3072\n"
      "v_lshl_add_u32 v25, v14, 7, v5\nv_and_b32 v125, s55, v73\nv_xor_b32 v73, v73, v125\ns_add_u32 m0, s53, 49152\ns_nop 0\nglobal_load_lds_dwordx4 v26, s[6:7] offset:0\n"
      "v_lshl_add_u32 v26, v16, 7, v4\ns_add_u32 s42, s27, 18\ns_sub_u32 s43, s42, 26\ns_min_u32 s42, s42, s43\nv_lshl_add_u32 v159, s42, 5, v148\ns_add_u32 s42, s27, 20\ns_sub_u32 s43, s42, 26\n"
      "s_min_u32 s42, s42, s43\nv_lshl_add_u32 v160, s42, 5, v148\ns_add_u32 s42, s27, 22\ns_sub_u32 s43, s42, 26\ns_min_u32 s42, s42, s43\nv_lshl_add_u32 v161, s42, 5, v148\n"
      "global_load_lds_dwordx4 v27, s[44:45] offset:1024\nv_lshl_add_u32 v27, v18, 7, v5\ns_add_u32 s42, s27, 24\ns_sub_u32 s43, s42, 26\ns_min_u32 s42, s42, s43\n"
      "v_lshl_add_u32 v162, s42, 5, v148\ns_cbranch_vccz .La1036\nglobal_load_lds_dwordx4 v28, s[46:47] offset:2048\n"
      ".La1036:\nv_lshl_add_u32 v28, v20, 7, v4\ns_waitcnt vmcnt(11) lgkmcnt(0)\ns_cbranch_vccnz .Lw1037\ns_waitcnt vmcnt(10)\n"
      ".Lw1037:\ns_barrier\ns_add_u32 m0, s54, 90112\ns_nop 0\nglobal_load_lds_dwordx4 v236, s[50:51] offset:0\nglobal_load_lds_dwordx4 v237, s[50:51] offset:1024\n"
      "global_load_lds_dwordx4 v238, s[50:51] offset:2048\nglobal_load_lds_dwordx4 v239, s[50:51] offset:3072\ns_add_u32 s52, s52, 212992\ns_cmp_eq_u32 s52, 5537792\ns_cselect_b32 s52, 0, s52\n"
      "s_add_u32 s50, s10, s52\ns_addc_u32 s51, s11, 0\ns_add_u32 m0, s53, 90112\ns_nop 0\nglobal_load_lds_dwordx4 v22, s[6:7] offset:0\nv_lshl_add_u32 v22, v9, 7, v4\n"
      "ds_read_b64 v[8:9], v6 offset:80\nglobal_load_lds_dwordx4 v23, s[44:45] offset:1024\nv_lshl_add_u32 v23, v11, 7, v5\nds_read_b64 v[10:11], v6 offset:976\n"
      "global_load_lds_dwordx4 v24, s[46:47] offset:2048\nv_lshl_add_u32 v24, v13, 7, v4\nds_read_b64 v[12:13], v6 offset:1872\nds_read_b64 v[76:77], v30 offset:0\n"
      "global_load_lds_dwordx4 v25, s[48:49] offset:3072\nv_lshl_add_u32 v25, v15, 7, v5\nds_read_b64 v[14:15], v6 offset:2768\nv_max3_f16 v32, |v74|, |v75|, v32\n"
      "v_max3_f32 v33, |v74|, |v75|, v33\ns_add_u32 m0, s53, 94208\ns_nop 0\nglobal_load_lds_dwordx4 v26, s[6:7] offset:0\nv_lshl_add_u32 v26, v17, 7, v4\nds_read_b64 v[16:17], v6 offset:3664\n"
      "v_and_b32 v126, s55, v74\nv_xor_b32 v74, v74, v126\nglobal_load_lds_dwordx4 v27, s[44:45] offset:1024\nv_lshl_add_u32 v27, v19, 7, v5\nds_read_b64 v[18:19], v6 offset:4560\n"
      "v_and_b32 v127, s55, v75\nv_xor_b32 v75, v75, v127\ns_cbranch_vccz .La1038\nglobal_load_lds_dwordx4 v28, s[46:47] offset:2048\n"
      ".La1038:\nv_lshl_add_u32 v28, v21, 7, v4\nds_read_b64 v[20:21], v7 offset:80\ns_waitcnt vmcnt(11) lgkmcnt(0)\ns_cbranch_vccnz .Lw1039\ns_waitcnt vmcnt(10)\n"
      ".Lw1039:\ns_barrier\ns_add_u32 m0, s54, 0\ns_nop 0\nglobal_load_lds_dwordx4 v236, s[50:51] offset:0\nglobal_load_lds_dwordx4 v237, s[50:51] offset:1024\n"
      "global_load_lds_dwordx4 v238, s[50:51] offset:2048\nglobal_load_lds_dwordx4 v239, s[50:51] offset:3072\ns_add_u32 s52, s52, 212992\ns_cmp_eq_u32 s52, 5537792\ns_cselect_b32 s52, 0, s52\n"
      "s_add_u32 s50, s10, s52\ns_addc_u32 s51, s11, 0\ns_add_u32 m0, s53, 0\ns_nop 0\nglobal_load_lds_dwordx4 v22, s[6:7] offset:0\nv_lshl_add_u32 v22, v8, 7, v4\n"
      "global_load_lds_dwordx4 v23, s[44:45] offset:1024\nv_lshl_add_u32 v23, v10, 7, v5\nglobal_load_lds_dwordx4 v24, s[46:47] offset:2048\nv_lshl_add_u32 v24, v12, 7, v4\n"
      "ds_read_b64 v[78:79], v30 offset:45056\nglobal_load_lds_dwordx4 v25, s[48:49] offset:3072\nv_lshl_add_u32 v25, v14, 7, v5\nv_max3_f16 v32, |v76|, |v77|, v32\n"
      "v_max3_f32 v33, |v76|, |v77|, v33\ns_add_u32 m0, s53, 4096\ns_nop 0\nglobal_load_lds_dwordx4 v26, s[6:7] offset:0\nv_lshl_add_u32 v26, v16, 7, v4\nv_and_b32 v128, s55, v76\n"
      "v_xor_b32 v76, v76, v128\nglobal_load_lds_dwordx4 v27, s[44:45] offset:1024\nv_lshl_add_u32 v27, v18, 7, v5\nv_and_b32 v129, s55, v77\nv_xor_b32 v77, v77, v129\ns_cbranch_vccz .La1040\n"
      "global_load_lds_dwordx4 v28, s[46:47] offset:2048\n"
      ".La1040:\nv_lshl_add_u32 v28, v20, 7, v4\ns_waitcnt vmcnt(11) lgkmcnt(0)\ns_cbranch_vccnz .Lw1041\ns_waitcnt vmcnt(10)\n"
      ".Lw1041:\ns_barrier\ns_add_u32 m0, s54, 45056\ns_nop 0\nglobal_load_lds_dwordx4 v236, s[50:51] offset:0\nglobal_load_lds_dwordx4 v237, s[50:51] offset:1024\n"
      "global_load_lds_dwordx4 v238, s[50:51] offset:2048\nglobal_load_lds_dwordx4 v239, s[50:51] offset:3072\ns_add_u32 s52, s52, 212992\ns_cmp_eq_u32 s52, 5537792\ns_cselect_b32 s52, 0, s52\n"
      "s_add_u32 s50, s10, s52\ns_addc_u32 s51, s11, 0\ns_add_u32 m0, s53, 45056\ns_nop 0\nglobal_load_lds_dwordx4 v22, s[6:7] offset:0\nv_lshl_add_u32 v22, v9, 7, v4\n"
      "ds_read_b64 v[8:9], v6 offset:88\nglobal_load_lds_dwordx4 v23, s[44:45] offset:1024\nv_lshl_add_u32 v23, v11, 7, v5\nds_read_b64 v[10:11], v6 offset:984\n"
      "global_load_lds_dwordx4 v24, s[46:47] offset:2048\nv_lshl_add_u32 v24, v13, 7, v4\nds_read_b64 v[12:13], v6 offset:1880\nds_read_b64 v[80:81], v31 offset:0\n"
      "global_load_lds_dwordx4 v25, s[48:49] offset:3072\nv_lshl_add_u32 v25, v15, 7, v5\nds_read_b64 v[14:15], v6 offset:2776\nv_max3_f16 v32, |v78|, |v79|, v32\n"
      "v_max3_f32 v33, |v78|, |v79|, v33\ns_add_u32 m0, s53, 49152\ns_nop 0\nglobal_load_lds_dwordx4 v26, s[6:7] offset:0\nv_lshl_add_u32 v26, v17, 7, v4\nds_read_b64 v[16:17], v6 offset:3672\n"
      "v_and_b32 v130, s55, v78\nv_xor_b32 v78, v78, v130\nglobal_load_lds_dwordx4 v27, s[44:45] offset:1024\nv_lshl_add_u32 v27, v19, 7, v5\nds_read_b64 v[18:19], v6 offset:4568\n"
      "v_and_b32 v131, s55, v79\nv_xor_b32 v79, v79, v131\ns_cbranch_vccz .La1042\nglobal_load_lds_dwordx4 v28, s[46:47] offset:2048\n"
      ".La1042:\nv_lshl_add_u32 v28, v21, 7, v4\nds_read_b64 v[20:21], v7 offset:88\ns_cmp_lg_u32 s24, 3\ns_cbranch_scc1 .Lo1043\nglobal_load_dwordx2 v[36:37], v38, s[58:59] sc1\n"
      ".Lo1043:\ns_waitcnt vmcnt(11) lgkmcnt(0)\ns_cbranch_vccnz .Lw1044\ns_waitcnt vmcnt(10)\n"
      ".Lw1044:\ns_barrier\ns_add_u32 m0, s54, 90112\ns_nop 0\nglobal_load_lds_dwordx4 v236, s[50:51] offset:0\nglobal_load_lds_dwordx4 v237, s[50:51] offset:1024\n"
      "global_load_lds_dwordx4 v238, s[50:51] offset:2048\nglobal_load_lds_dwordx4 v239, s[50:51] offset:3072\ns_add_u32 s52, s52, 212992\ns_cmp_eq_u32 s52, 5537792\ns_cselect_b32 s52, 0, s52\n"
      "s_add_u32 s50, s10, s52\ns_addc_u32 s51, s11, 0\ns_add_u32 m0, s53, 90112\ns_nop 0\nglobal_load_lds_dwordx4 v22, s[6:7] offset:0\nv_lshl_add_u32 v22, v8, 7, v4\n"
      "global_load_lds_dwordx4 v23, s[44:45] offset:1024\nv_lshl_add_u32 v23, v10, 7, v5\nglobal_load_lds_dwordx4 v24, s[46:47] offset:2048\nv_lshl_add_u32 v24, v12, 7, v4\n"
      "ds_read_b64 v[82:83], v30 offset:0\nglobal_load_lds_dwordx4 v25, s[48:49] offset:3072\nv_lshl_add_u32 v25, v14, 7, v5\nv_max3_f16 v32, |v80|, |v81|, v32\nv_max3_f32 v33, |v80|, |v81|, v33\n"
      "s_add_u32 m0, s53, 94208\ns_nop 0\nglobal_load_lds_dwordx4 v26, s[6:7] offset:0\nv_lshl_add_u32 v26, v16, 7, v4\nv_and_b32 v132, s55, v80\nv_xor_b32 v80, v80, v132\n"
      "global_load_lds_dwordx4 v27, s[44:45] offset:1024\nv_lshl_add_u32 v27, v18, 7, v5\nv_and_b32 v133, s55, v81\nv_xor_b32 v81, v81, v133\ns_cbranch_vccz .La1045\n"
      "global_load_lds_dwordx4 v28, s[46:47] offset:2048\n"
      ".La1045:\nv_lshl_add_u32 v28, v20, 7, v4\ns_waitcnt vmcnt(11) lgkmcnt(0)\ns_cbranch_vccnz .Lw1046\ns_waitcnt vmcnt(10)\n"
      ".Lw1046:\ns_barrier\ns_add_u32 m0, s54, 0\ns_nop 0\nglobal_load_lds_dwordx4 v236, s[50:51] offset:0\nglobal_load_lds_dwordx4 v237, s[50:51] offset:1024\n"
      "global_load_lds_dwordx4 v238, s[50:51] offset:2048\nglobal_load_lds_dwordx4 v239, s[50:51] offset:3072\ns_add_u32 s52, s52, 212992\ns_cmp_eq_u32 s52, 5537792\ns_cselect_b32 s52, 0, s52\n"
      "s_add_u32 s50, s10, s52\ns_addc_u32 s51, s11, 0\ns_add_u32 m0, s53, 0\ns_nop 0\nglobal_load_lds_dwordx4 v22, s[6:7] offset:0\nv_lshl_add_u32 v22, v9, 7, v4\n"
      "ds_read_b64 v[8:9], v6 offset:96\nglobal_load_lds_dwordx4 v23, s[44:45] offset:1024\nv_lshl_add_u32 v23, v11, 7, v5\nds_read_b64 v[10:11], v6 offset:992\n"
      "global_load_lds_dwordx4 v24, s[46:47] offset:2048\nv_lshl_add_u32 v24, v13, 7, v4\nds_read_b64 v[12:13], v6 offset:1888\nds_read_b64 v[84:85], v30 offset:45056\n"
      "global_load_lds_dwordx4 v25, s[48:49] offset:3072\nv_lshl_add_u32 v25, v15, 7, v5\nds_read_b64 v[14:15], v6 offset:2784\nv_max3_f16 v32, |v82|, |v83|, v32\n"
      "v_max3_f32 v33, |v82|, |v83|, v33\ns_add_u32 m0, s53, 4096\ns_nop 0\nglobal_load_lds_dwordx4 v26, s[6:7] offset:0\nv_lshl_add_u32 v26, v17, 7, v4\nds_read_b64 v[16:17], v6 offset:3680\n"
      "v_and_b32 v134, s55, v82\nv_xor_b32 v82, v82, v134\nglobal_load_lds_dwordx4 v27, s[44:45] offset:1024\nv_lshl_add_u32 v27, v19, 7, v5\nds_read_b64 v[18:19], v6 offset:4576\n"
      "v_and_b32 v135, s55, v83\nv_xor_b32 v83, v83, v135\ns_cbranch_vccz .La1047\nglobal_load_lds_dwordx4 v28, s[46:47] offset:2048\n"
      ".La1047:\nv_lshl_add_u32 v28, v21, 7, v4\nds_read_b64 v[20:21], v7 offset:96\ns_waitcnt vmcnt(11) lgkmcnt(0)\ns_cbranch_vccnz .Lw1048\ns_waitcnt vmcnt(10)\n"
      ".Lw1048:\ns_barrier\ns_add_u32 m0, s54, 45056\ns_nop 0\nglobal_load_lds_dwordx4 v236, s[50:51] offset:0\nglobal_load_lds_dwordx4 v237, s[50:51] offset:1024\n"
      "global_load_lds_dwordx4 v238, s[50:51] offset:2048\nglobal_load_lds_dwordx4 v239, s[50:51] offset:3072\ns_add_u32 s52, s52, 212992\ns_cmp_eq_u32 s52, 5537792\ns_cselect_b32 s52, 0, s52\n"
      "s_add_u32 s50, s10, s52\ns_addc_u32 s51, s11, 0\ns_add_u32 m0, s53, 45056\ns_nop 0\nglobal_load_lds_dwordx4 v22, s[6:7] offset:0\nv_lshl_add_u32 v22, v8, 7, v4\n"
      "global_load_lds_dwordx4 v23, s[44:45] offset:1024\nv_lshl_add_u32 v23, v10, 7, v5\nglobal_load_lds_dwordx4 v24, s[46:47] offset:2048\nv_lshl_add_u32 v24, v12, 7, v4\n"
      "ds_read_b64 v[86:87], v31 offset:0\nglobal_load_lds_dwordx4 v25, s[48:49] offset:3072\nv_lshl_add_u32 v25, v14, 7, v5\nv_max3_f16 v32, |v84|, |v85|, v32\nv_max3_f32 v33, |v84|, |v85|, v33\n"
      "s_add_u32 m0, s53, 49152\ns_nop 0\nglobal_load_lds_dwordx4 v26, s[6:7] offset:0\nv_lshl_add_u32 v26, v16, 7, v4\nv_and_b32 v136, s55, v84\nv_xor_b32 v84, v84, v136\n"
      "global_load_lds_dwordx4 v27, s[44:45] offset:1024\nv_lshl_add_u32 v27, v18, 7, v5\nv_and_b32 v137, s55, v85\nv_xor_b32 v85, v85, v137\ns_cbranch_vccz .La1049\n"
      "global_load_lds_dwordx4 v28, s[46:47] offset:2048\n"
      ".La1049:\nv_lshl_add_u32 v28, v20, 7, v4\ns_cmp_lg_u32 s24, 3\ns_cbranch_scc1 .Lo1052\ns_mov_b32 s42, 0\n"
      ".Lq1050:\nv_and_b32 v164, 0xff, v36\nv_xor_b32 v164, s41, v164\nv_xor_b32 v165, s40, v37\nv_or_b32 v164, v164, v165\nv_cmp_ne_u32 s[62:63], 0, v164\nv_lshrrev_b32 v164, 8, v36\n"
      "s_cmp_eq_u64 s[62:63], 0\ns_cbranch_scc1 .Lr1051\ns_sleep 1\nglobal_load_dwordx2 v[36:37], v38, s[58:59] sc1\ns_add_u32 s42, s42, 1\ns_bitcmp0_b32 s42, 22\ns_waitcnt vmcnt(0)\n"
      "s_cbranch_scc1 .Lq1050\n"
      ".Lr1051:\nds_write_b32 v253, v164 offset:24\n"
      ".Lo1052:\ns_waitcnt vmcnt(11) lgkmcnt(0)\ns_cbranch_vccnz .Lw1053\ns_waitcnt vmcnt(10)\n"
      ".Lw1053:\ns_barrier\ns_add_u32 m0, s54, 90112\ns_nop 0\nglobal_load_lds_dwordx4 v236, s[50:51] offset:0\nglobal_load_lds_dwordx4 v237, s[50:51] offset:1024\n"
      "global_load_lds_dwordx4 v238, s[50:51] offset:2048\nglobal_load_lds_dwordx4 v239, s[50:51] offset:3072\ns_add_u32 s52, s52, 212992\ns_cmp_eq_u32 s52, 5537792\ns_cselect_b32 s52, 0, s52\n"
      "s_add_u32 s50, s10, s52\ns_addc_u32 s51, s11, 0\ns_add_u32 m0, s53, 90112\ns_nop 0\nglobal_load_lds_dwordx4 v22, s[6:7] offset:0\nv_lshl_add_u32 v22, v9, 7, v4\n"
      "global_load_lds_dwordx4 v23, s[44:45] offset:1024\nv_lshl_add_u32 v23, v11, 7, v5\nglobal_load_lds_dwordx4 v24, s[46:47] offset:2048\nv_lshl_add_u32 v24, v13, 7, v4\n"
      "ds_read_b64 v[88:89], v30 offset:0\nglobal_load_lds_dwordx4 v25, s[48:49] offset:3072\nv_lshl_add_u32 v25, v15, 7, v5\nv_max3_f16 v32, |v86|, |v87|, v32\nv_max3_f32 v33, |v86|, |v87|, v33\n"
      "s_add_u32 m0, s53, 94208\ns_nop 0\nglobal_load_lds_dwordx4 v26, s[6:7] offset:0\nv_lshl_add_u32 v26, v17, 7, v4\nv_and_b32 v138, s55, v86\nv_xor_b32 v86, v86, v138\n"
      "global_load_lds_dwordx4 v27, s[44:45] offset:1024\nv_lshl_add_u32 v27, v19, 7, v5\nv_and_b32 v139, s55, v87\nv_xor_b32 v87, v87, v139\ns_cbranch_vccz .La1054\n"
      "global_load_lds_dwordx4 v28, s[46:47] offset:2048\n"
      ".La1054:\nv_lshl_add_u32 v28, v21, 7, v4\ns_waitcnt vmcnt(11) lgkmcnt(0)\ns_cbranch_vccnz .Lw1055\ns_waitcnt vmcnt(10)\n"
      ".Lw1055:\ns_barrier\ns_add_u32 m0, s54, 0\ns_nop 0\nglobal_load_lds_dwordx4 v236, s[50:51] offset:0\nglobal_load_lds_dwordx4 v237, s[50:51] offset:1024\n"
      "ds_read_b64 v[90:91], v30 offset:45056\nglobal_load_lds_dwordx4 v238, s[50:51] offset:2048\nv_max3_f16 v32, |v88|, |v89|, v32\nv_max3_f32 v33, |v88|, |v89|, v33\n"
      "global_load_lds_dwordx4 v239, s[50:51] offset:3072\nv_and_b32 v140, s55, v88\nv_xor_b32 v88, v88, v140\ns_add_u32 m0, s53, 0\ns_nop 0\nglobal_load_lds_dwordx4 v22, s[6:7] offset:0\n"
      "v_and_b32 v141, s55, v89\nv_xor_b32 v89, v89, v141\nglobal_load_lds_dwordx4 v23, s[44:45] offset:1024\ns_lshl_b32 s42, s24, 6\nv_add_u32 v10, s42, v240\nv_min_u32 v10, 75, v10\n"
      "v_and_b32 v11, 3, v10\nglobal_load_lds_dwordx4 v24, s[46:47] offset:2048\nv_lshrrev_b32 v12, 2, v10\nv_mul_u32_u24 v11, 17632, v11\nv_mul_u32_u24 v13, 928, v12\n"
      "global_load_lds_dwordx4 v25, s[48:49] offset:3072\ns_mov_b32 s42, 90976\nv_add3_u32 v11, v11, v13, s42\ns_mov_b32 s42, 160640\nv_lshl_add_u32 v13, v12, 3, s42\ns_add_u32 m0, s53, 4096\n"
      "s_nop 0\nglobal_load_lds_dwordx4 v26, s[6:7] offset:0\ns_mov_b32 s42, 160896\nv_lshl_add_u32 v10, v10, 4, s42\ns_bfm_b32 s42, s64, 0\nv_lshrrev_b32 v15, v12, s42\n"
      "global_load_lds_dwordx4 v27, s[44:45] offset:1024\ns_sub_u32 s42, s64, 8\nv_subrev_u32 v14, s42, v12\nv_lshl_add_u32 v12, v240, 1, v247\nds_read_u16 v251, v12 offset:64\n"
      "ds_read_b32 v9, v247 offset:24\ns_cbranch_vccz .La1056\nglobal_load_lds_dwordx4 v28, s[46:47] offset:2048\n"
      ".La1056:\ns_waitcnt vmcnt(11) lgkmcnt(0)\ns_cbranch_vccnz .Lw1057\ns_waitcnt vmcnt(10)\n"
      ".Lw1057:\ns_barrier\ns_add_u32 m0, s56, 0\ns_nop 0\nglobal_load_lds_dwordx4 v252, s[60:61] offset:0 sc1\nds_read_b64 v[92:93], v31 offset:0\n"
      "global_load_lds_dwordx4 v252, s[60:61] offset:1024 sc1\nv_max3_f16 v32, |v90|, |v91|, v32\nv_max3_f32 v33, |v90|, |v91|, v33\nglobal_load_lds_dwordx4 v252, s[60:61] offset:2048 sc1\n"
      "v_and_b32 v142, s55, v90\nv_xor_b32 v90, v90, v142\nglobal_load_lds_dwordx4 v252, s[60:61] offset:3072 sc1\ns_add_u32 s60, s60, 4096\ns_addc_u32 s61, s61, 0\nv_and_b32 v143, s55, v91\n"
      "v_xor_b32 v91, v91, v143\ns_add_u32 m0, s56, 4096\ns_nop 0\nglobal_load_lds_dwordx4 v252, s[60:61] offset:0 sc1\nv_readfirstlane_b32 s39, v9\nv_readlane_b32 s57, v251, 0\n"
      "s_xor_b32 s40, s39, 0xa5c30f96\ns_mul_i32 s41, s39, 0x9E3779B1\ns_lshr_b32 s41, s41, 24\nglobal_load_lds_dwordx4 v252, s[60:61] offset:1024 sc1\ns_lshl_b32 s42, %[blk], 3\n"
      "s_add_u32 s42, s42, 24576\nv_mov_b32 v7, s42\nv_mov_b32 v8, s41\nv_mov_b32 v9, s40\nglobal_load_lds_dwordx4 v252, s[60:61] offset:2048 sc1\ns_lshl_b32 s42, %[blk], 2\nv_mov_b32 v2, s42\n"
      "s_add_u32 s42, s37, 1\nv_mov_b32 v3, s42\nglobal_load_lds_dwordx4 v252, s[60:61] offset:3072 sc1\ns_add_u32 s60, s60, 4096\ns_addc_u32 s61, s61, 0\nv_and_b32 v1, 48, v240\n"
      "s_mov_b32 s42, 87040\nv_lshl_add_u32 v1, v1, 2, s42\ns_mov_b32 s49, s65\ns_add_u32 m0, s56, 8192\ns_nop 0\nglobal_load_lds_dwordx4 v252, s[60:61] offset:0 sc1\n"
      "v_add_u32_sdwa v236, s49, v242 dst_sel:DWORD dst_unused:UNUSED_PAD src0_sel:DWORD src1_sel:WORD_0\nglobal_load_lds_dwordx4 v252, s[60:61] offset:1024 sc1\n"
      "v_add_u32_sdwa v237, s49, v242 dst_sel:DWORD dst_unused:UNUSED_PAD src0_sel:DWORD src1_sel:WORD_1\nglobal_load_lds_dwordx4 v252, s[60:61] offset:1536 sc1\n"
      "v_add_u32_sdwa v238, s49, v243 dst_sel:DWORD dst_unused:UNUSED_PAD src0_sel:DWORD src1_sel:WORD_0\n"
      "v_add_u32_sdwa v239, s49, v243 dst_sel:DWORD dst_unused:UNUSED_PAD src0_sel:DWORD src1_sel:WORD_1\ns_waitcnt vmcnt(11) lgkmcnt(0)\ns_barrier\ns_setprio 3\n"
      "ds_read_b64 v[94:95], v30 offset:0\nv_max3_f16 v32, |v92|, |v93|, v32\nv_max3_f32 v33, |v92|, |v93|, v33\nv_and_b32 v144, s55, v92\nv_xor_b32 v92, v92, v144\nv_and_b32 v145, s55, v93\n"
      "v_xor_b32 v93, v93, v145\ns_lshl_b32 s42, s30, 6\ns_add_u32 s42, s42, s36\ns_add_u32 s42, s42, 60\ns_mulk_i32 s42, 896\ns_add_u32 s44, s16, s42\ns_addc_u32 s45, s17, 0\n"
      "s_waitcnt lgkmcnt(0)\nv_max3_f16 v32, |v94|, |v95|, v32\nv_max3_f32 v33, |v94|, |v95|, v33\nv_and_b32 v146, s55, v94\nv_xor_b32 v94, v94, v146\nv_and_b32 v147, s55, v95\n"
      "v_xor_b32 v95, v95, v147\nv_lshlrev_b32 v32, 16, v32\nv_max_u32 v33, v32, v33\ns_nop 1\nv_max_u32_dpp v33, v33, v33 quad_perm:[1,0,3,2] row_mask:0xf bank_mask:0xf\ns_nop 1\n"
      "v_max_u32_dpp v33, v33, v33 quad_perm:[2,3,0,1] row_mask:0xf bank_mask:0xf\nv_lshrrev_b32 v33, 23, v33\nv_subrev_u32 v32, 22, v33\nv_mul_u32_u24 v32, 0x800080, v32\n"
      "v_pk_sub_u16 v44, v44, v32 clamp\nv_pk_sub_u16 v45, v45, v32 clamp\nv_pk_sub_u16 v46, v46, v32 clamp\nv_pk_sub_u16 v47, v47, v32 clamp\nv_lshl_or_b32 v44, v44, 3, v96\n"
      "v_lshl_or_b32 v45, v45, 3, v97\nv_lshl_or_b32 v46, v46, 3, v98\nv_lshl_or_b32 v47, v47, 3, v99\nv_pk_sub_u16 v48, v48, v32 clamp\nv_pk_sub_u16 v49, v49, v32 clamp\n"
      "v_pk_sub_u16 v50, v50, v32 clamp\nv_pk_sub_u16 v51, v51, v32 clamp\nv_lshl_or_b32 v48, v48, 3, v100\nv_lshl_or_b32 v49, v49, 3, v101\nv_lshl_or_b32 v50, v50, 3, v102\n"
      "v_lshl_or_b32 v51, v51, 3, v103\nv_pk_sub_u16 v52, v52, v32 clamp\nv_pk_sub_u16 v53, v53, v32 clamp\nv_pk_sub_u16 v54, v54, v32 clamp\nv_pk_sub_u16 v55, v55, v32 clamp\n"
      "v_lshl_or_b32 v52, v52, 3, v104\nv_lshl_or_b32 v53, v53, 3, v105\nv_lshl_or_b32 v54, v54, 3, v106\nv_lshl_or_b32 v55, v55, 3, v107\nv_pk_sub_u16 v56, v56, v32 clamp\n"
      "v_pk_sub_u16 v57, v57, v32 clamp\nv_pk_sub_u16 v58, v58, v32 clamp\nv_pk_sub_u16 v59, v59, v32 clamp\nv_lshl_or_b32 v56, v56, 3, v108\nv_lshl_or_b32 v57, v57, 3, v109\n"
      "v_lshl_or_b32 v58, v58, 3, v110\nv_lshl_or_b32 v59, v59, 3, v111\nv_pk_sub_u16 v60, v60, v32 clamp\nv_pk_sub_u16 v61, v61, v32 clamp\nv_pk_sub_u16 v62, v62, v32 clamp\n"
      "v_pk_sub_u16 v63, v63, v32 clamp\nv_lshl_or_b32 v60, v60, 3, v112\nv_lshl_or_b32 v61, v61, 3, v113\nv_lshl_or_b32 v62, v62, 3, v114\nv_lshl_or_b32 v63, v63, 3, v115\n"
      "v_pk_sub_u16 v64, v64, v32 clamp\nv_pk_sub_u16 v65, v65, v32 clamp\nv_pk_sub_u16 v66, v66, v32 clamp\nv_pk_sub_u16 v67, v67, v32 clamp\nv_lshl_or_b32 v64, v64, 3, v116\n"
      "v_lshl_or_b32 v65, v65, 3, v117\nv_lshl_or_b32 v66, v66, 3, v118\nv_lshl_or_b32 v67, v67, 3, v119\ns_waitcnt vmcnt(0)\ns_barrier\nv_pk_sub_u16 v68, v68, v32 clamp\n"
      "v_pk_sub_u16 v69, v69, v32 clamp\nv_pk_sub_u16 v70, v70, v32 clamp\nv_pk_sub_u16 v71, v71, v32 clamp\nv_lshl_or_b32 v68, v68, 3, v120\nv_lshl_or_b32 v69, v69, 3, v121\n"
      "v_lshl_or_b32 v70, v70, 3, v122\nv_lshl_or_b32 v71, v71, 3, v123\nv_pk_sub_u16 v72, v72, v32 clamp\nv_pk_sub_u16 v73, v73, v32 clamp\nv_pk_sub_u16 v74, v74, v32 clamp\n"
      "v_pk_sub_u16 v75, v75, v32 clamp\nv_lshl_or_b32 v72, v72, 3, v124\nv_lshl_or_b32 v73, v73, 3, v125\nv_lshl_or_b32 v74, v74, 3, v126\nv_lshl_or_b32 v75, v75, 3, v127\n"
      "v_pk_sub_u16 v76, v76, v32 clamp\nv_pk_sub_u16 v77, v77, v32 clamp\nv_pk_sub_u16 v78, v78, v32 clamp\nv_pk_sub_u16 v79, v79, v32 clamp\nv_lshl_or_b32 v76, v76, 3, v128\n"
      "v_lshl_or_b32 v77, v77, 3, v129\nv_lshl_or_b32 v78, v78, 3, v130\nv_lshl_or_b32 v79, v79, 3, v131\nv_pk_sub_u16 v80, v80, v32 clamp\nv_pk_sub_u16 v81, v81, v32 clamp\n"
      "v_pk_sub_u16 v82, v82, v32 clamp\nv_pk_sub_u16 v83, v83, v32 clamp\nv_lshl_or_b32 v80, v80, 3, v132\nv_lshl_or_b32 v81, v81, 3, v133\nv_lshl_or_b32 v82, v82, 3, v134\n"
      "v_lshl_or_b32 v83, v83, 3, v135\nv_pk_sub_u16 v84, v84, v32 clamp\nv_pk_sub_u16 v85, v85, v32 clamp\nv_pk_sub_u16 v86, v86, v32 clamp\nv_pk_sub_u16 v87, v87, v32 clamp\n"
      "v_lshl_or_b32 v84, v84, 3, v136\nv_lshl_or_b32 v85, v85, 3, v137\nv_lshl_or_b32 v86, v86, 3, v138\nv_lshl_or_b32 v87, v87, 3, v139\nv_pk_sub_u16 v88, v88, v32 clamp\n"
      "v_pk_sub_u16 v89, v89, v32 clamp\nv_pk_sub_u16 v90, v90, v32 clamp\nv_pk_sub_u16 v91, v91, v32 clamp\nv_lshl_or_b32 v88, v88, 3, v140\nv_lshl_or_b32 v89, v89, 3, v141\n"
      "v_lshl_or_b32 v90, v90, 3, v142\nv_lshl_or_b32 v91, v91, 3, v143\nv_pk_sub_u16 v92, v92, v32 clamp\nv_pk_sub_u16 v93, v93, v32 clamp\nv_pk_sub_u16 v94, v94, v32 clamp\n"
      "v_pk_sub_u16 v95, v95, v32 clamp\nv_lshl_or_b32 v92, v92, 3, v144\nv_lshl_or_b32 v93, v93, 3, v145\nv_lshl_or_b32 v94, v94, 3, v146\nv_lshl_or_b32 v95, v95, 3, v147\nv_sub_u32 v4, 257, v33\n"
      "v_lshlrev_b32 v4, 23, v4\nv_cvt_pkrtz_f16_f32 v4, v4, v4\nv_lshlrev_b32 v33, 2, v33\nv_mov_b32 v5, v4\nv_subrev_u32 v33, 536, v33\ns_setprio 0\nds_write_b64 v148, v[4:5] offset:832\n"
      "ds_write_b32 v149, v33 offset:864\nds_write2_b64 v150, v[44:45], v[46:47] offset1:4\nds_write2_b64 v151, v[48:49], v[50:51] offset1:4\nds_write2_b64 v152, v[52:53], v[54:55] offset1:4\n"
      "ds_write2_b64 v153, v[56:57], v[58:59] offset1:4\nds_write2_b64 v154, v[60:61], v[62:63] offset1:4\nds_write2_b64 v155, v[64:65], v[66:67] offset1:4\n"
      "ds_write2_b64 v156, v[68:69], v[70:71] offset1:4\nds_write2_b64 v157, v[72:73], v[74:75] offset1:4\nds_write2_b64 v158, v[76:77], v[78:79] offset1:4\n"
      "ds_write2_b64 v159, v[80:81], v[82:83] offset1:4\nds_write2_b64 v160, v[84:85], v[86:87] offset1:4\nds_write2_b64 v161, v[88:89], v[90:91] offset1:4\n"
      "ds_write2_b64 v162, v[92:93], v[94:95] offset1:4\nds_read_b128 v[16:19], v163 offset:4640\nds_read_b128 v[20:23], v163 offset:3712\nds_read_b128 v[24:27], v163 offset:2784\n"
      "ds_read_b128 v[28:31], v163 offset:1856\nds_read_b128 v[32:35], v163 offset:928\nds_read_b128 v[36:39], v163 offset:0\ns_mov_b32 exec_hi, 0x7fffff\ns_waitcnt lgkmcnt(0)\n"
      "s_cmp_gt_u32 s64, 15\ns_cbranch_scc1 .Ld1059\nglobal_store_dwordx4 v252, v[16:19], s[44:45] sc1\ns_sub_u32 s44, s44, 3584\ns_subb_u32 s45, s45, 0\ns_cmp_gt_u32 s64, 14\n"
      "s_cbranch_scc1 .Ld1059\nglobal_store_dwordx4 v252, v[20:23], s[44:45] sc1\ns_sub_u32 s44, s44, 3584\ns_subb_u32 s45, s45, 0\ns_cmp_gt_u32 s64, 13\ns_cbranch_scc1 .Ld1059\n"
      "global_store_dwordx4 v252, v[24:27], s[44:45] sc1\ns_sub_u32 s44, s44, 3584\ns_subb_u32 s45, s45, 0\ns_cmp_gt_u32 s64, 12\ns_cbranch_scc1 .Ld1059\n"
      "global_store_dwordx4 v252, v[28:31], s[44:45] sc1\ns_sub_u32 s44, s44, 3584\ns_subb_u32 s45, s45, 0\ns_cmp_gt_u32 s64, 11\ns_cbranch_scc1 .Ld1059\n"
      "global_store_dwordx4 v252, v[32:35], s[44:45] sc1\ns_sub_u32 s44, s44, 3584\ns_subb_u32 s45, s45, 0\ns_cmp_gt_u32 s64, 10\ns_cbranch_scc1 .Ld1059\n"
      "global_store_dwordx4 v252, v[36:39], s[44:45] sc1\ns_sub_u32 s44, s44, 3584\ns_subb_u32 s45, s45, 0\n"
      ".Ld1059:\ns_mov_b32 exec_hi, -1\nds_read_b128 v[48:51], v1 offset:0\nds_read_b128 v[52:55], v1 offset:16\nds_read_b128 v[56:59], v1 offset:32\nds_read_b128 v[60:63], v1 offset:48\n"
      "ds_read_b128 v[72:75], v252 offset:0\nds_read_b128 v[76:79], v252 offset:1024\nds_read_b128 v[80:83], v252 offset:2048\nds_read_b128 v[84:87], v252 offset:3072\n"
      "ds_read_b128 v[88:91], v252 offset:4096\nds_read_b128 v[92:95], v252 offset:5120\nds_read_b128 v[96:99], v252 offset:6144\nds_read_b128 v[100:103], v252 offset:7168\n"
      "ds_read_b128 v[104:107], v252 offset:8192\nds_read_b128 v[108:111], v252 offset:9216\nds_read_b128 v[112:115], v252 offset:10240\nds_read_b128 v[116:119], v252 offset:11264\n"
      "ds_read_b128 v[120:123], v252 offset:12288\nds_read_b128 v[124:127], v252 offset:13312\nds_read_b128 v[128:131], v252 offset:14336\nds_read_b128 v[132:135], v252 offset:15360\n"
      "ds_read_b128 v[136:139], v252 offset:16384\nds_read_b128 v[140:143], v252 offset:17408\nds_read_b128 v[144:147], v252 offset:18432\nds_read_b128 v[148:151], v252 offset:19456\n"
      "ds_read_b128 v[152:155], v252 offset:20480\nds_read_b128 v[156:159], v252 offset:21504\nds_read_b128 v[160:163], v252 offset:22528\nds_read_b128 v[164:167], v252 offset:23552\n"
      "ds_read_b128 v[168:171], v252 offset:24576\nds_read_b128 v[172:175], v252 offset:25600\nds_read_b128 v[176:179], v252 offset:26624\nds_read_b128 v[180:183], v252 offset:27648\n"
      "ds_read_b128 v[184:187], v252 offset:28672\nds_read_b128 v[188:191], v252 offset:29696\nds_read_b128 v[192:195], v252 offset:30720\nds_read_b128 v[196:199], v252 offset:31744\n"
      "ds_read_b128 v[200:203], v252 offset:32768\nds_read_b128 v[204:207], v252 offset:33792\nds_read_b128 v[208:211], v252 offset:34816\nds_read_b128 v[212:215], v252 offset:35840\n"
      "ds_read_b128 v[216:219], v252 offset:36864\nds_read_b128 v[220:223], v252 offset:37888\nds_read_b128 v[224:227], v252 offset:38912\nds_read_b128 v[228:231], v252 offset:39936\n"
      "ds_read_b128 v[232:235], v252 offset:40960\ns_mov_b32 s55, s57\ns_mov_b32 s45, 0\ns_mov_b32 s46, 0\ns_waitcnt lgkmcnt(15)\nv_mul_f32 v48, v48, v56\nv_mul_f32 v49, v49, v57\n"
      "v_mul_f32 v50, v50, v58\nv_mul_f32 v51, v51, v59\nv_mul_f32 v52, v52, v60\nv_mul_f32 v53, v53, v61\nv_mul_f32 v54, v54, v62\nds_write_b128 v1, v[48:51] offset:256\n"
      "ds_write_b128 v1, v[52:55] offset:272\nds_read_b64_tr_b16 v[68:69], v238\nds_read_b64_tr_b16 v[70:71], v239\nds_read_b64_tr_b16 v[56:57], v236 offset:0\n"
      "ds_read_b64_tr_b16 v[58:59], v237 offset:128\nds_read_b64_tr_b16 v[60:61], v237 offset:256\nds_read_b64_tr_b16 v[62:63], v237 offset:384\nds_read_b64_tr_b16 v[64:65], v237 offset:512\n"
      "ds_read_b64_tr_b16 v[66:67], v237 offset:640\nds_read_b128 v[44:47], v247 offset:48\ns_waitcnt vmcnt(0)\nds_add_rtn_u32 v6, v253, v241 offset:32\ns_waitcnt lgkmcnt(0)\n"
      "v_readfirstlane_b32 s42, v6\ns_cmp_lg_u32 s42, 3\ns_cbranch_scc1 .Lf1060\ns_mov_b64 exec, 1\nglobal_store_dwordx2 v7, v[8:9], s[20:21] sc1\nglobal_store_dword v2, v3, s[20:21] sc1\n"
      "s_mov_b64 exec, -1\n"
      ".Lf1060:\ns_branch .Lcin\n"
      ".Lgemm:\ns_cmp_lg_u32 s36, 0\ns_cbranch_scc1 .Lp1061\nv_lshlrev_b32 v0, 4, v240\ns_mov_b32 m0, 162240\ns_mov_b64 exec, 0xff\nglobal_load_lds_dwordx4 v0, %[pl]\ns_mov_b64 exec, -1\n"
      ".Lp1061:\ns_setprio 2\ns_and_b32 s56, s36, 1\ns_lshr_b32 s57, s36, 1\nv_lshrrev_b32 v1, 4, v240\nv_and_b32 v2, 15, v240\nv_lshlrev_b32 v3, 2, v2\nv_add_u32 v3, 162176, v3\n"
      "v_cmp_gt_u32 vcc, 4, v2\ns_nop 1\nv_cndmask_b32 v4, 0, 2, vcc\nds_write_b32 v3, v4\ns_mul_i32 s42, s56, 12288\nv_lshrrev_b32 v3, 1, v2\nv_xor_b32 v3, v3, v1\nv_lshl_add_u32 v4, v2, 7, s42\n"
      "v_lshl_add_u32 v96, v3, 4, v4\nv_xor_b32 v97, 64, v96\nv_add_u32 v98, 90112, v96\nv_add_u32 v99, 90112, v97\ns_cmp_eq_u32 s25, 4\ns_cselect_b32 s42, 5, 6\ns_mul_i32 s42, s42, s56\n"
      "s_sub_u32 s42, 12, s42\ns_lshl_b32 s42, s42, 11\nv_add_u32 v100, s42, v96\nv_add_u32 v101, s42, v97\nv_add_u32 v102, s42, v98\nv_add_u32 v103, s42, v99\nv_lshrrev_b32 v3, 2, v2\n"
      "v_lshl_add_u32 v4, v1, 3, v3\nv_and_b32 v5, 1, v1\nv_lshl_add_u32 v3, v5, 2, v3\ns_lshl_b32 s42, s36, 1\nv_xor_b32 v3, s42, v3\nv_and_b32 v5, 3, v2\nv_lshlrev_b32 v5, 3, v5\n"
      "v_lshl_add_u32 v4, v4, 8, v5\nv_lshl_add_u32 v4, v3, 5, v4\nv_add_u32 v104, 28672, v4\nv_xor_b32 v105, 32, v104\nv_xor_b32 v106, 64, v104\nv_xor_b32 v107, 96, v104\n"
      "v_add_u32 v108, 90112, v104\nv_add_u32 v109, 90112, v105\nv_add_u32 v110, 90112, v106\nv_add_u32 v111, 90112, v107\ns_lshl_b32 s42, s57, 10\ns_add_u32 s42, s42, 88064\n"
      "s_mul_i32 s43, s56, 384\ns_add_u32 s43, s43, s42\nv_lshl_add_u32 v254, v240, 2, s43\nv_cmp_gt_u32 vcc, 32, v240\ns_lshl_b32 s43, s56, 6\ns_add_u32 s42, s42, s43\ns_add_u32 s42, s42, 512\n"
      "v_lshl_add_u32 v255, v2, 2, s42\nv_cndmask_b32 v255, v255, v254, vcc\ns_lshl_b32 s42, s36, 6\ns_add_u32 s42, s42, 160896\nv_lshl_add_u32 v3, v1, 3, s42\nv_xor_b32 v4, 64, v3\n"
      "s_lshl_b32 s42, s36, 7\ns_add_u32 s42, s42, 163328\nv_lshl_add_u32 v236, v1, 4, s42\nv_xor_b32 v237, 128, v236\ns_barrier\ns_waitcnt vmcnt(4)\nds_read_b64 v[8:9], v3 offset:0\n"
      "ds_read_b64 v[10:11], v3 offset:32\nds_read_b64 v[12:13], v4 offset:0\nds_read_b64 v[14:15], v4 offset:32\ns_waitcnt lgkmcnt(0)\nv_lshlrev_b32 v124, 16, v8\nv_and_b32 v125, 0xffff0000, v8\n"
      "v_lshlrev_b32 v126, 16, v9\nv_and_b32 v127, 0xffff0000, v9\nv_lshlrev_b32 v128, 16, v10\nv_and_b32 v129, 0xffff0000, v10\nv_lshlrev_b32 v130, 16, v11\nv_and_b32 v131, 0xffff0000, v11\n"
      "v_lshlrev_b32 v132, 16, v12\nv_and_b32 v133, 0xffff0000, v12\nv_lshlrev_b32 v134, 16, v13\nv_and_b32 v135, 0xffff0000, v13\nv_lshlrev_b32 v136, 16, v14\nv_and_b32 v137, 0xffff0000, v14\n"
      "v_lshlrev_b32 v138, 16, v15\nv_and_b32 v139, 0xffff0000, v15\nv_mov_b64 v[140:141], v[124:125]\nv_mov_b64 v[142:143], v[126:127]\nv_mov_b64 v[144:145], v[128:129]\n"
      "v_mov_b64 v[146:147], v[130:131]\nv_mov_b64 v[148:149], v[132:133]\nv_mov_b64 v[150:151], v[134:135]\nv_mov_b64 v[152:153], v[136:137]\nv_mov_b64 v[154:155], v[138:139]\n"
      "v_mov_b64 v[156:157], v[124:125]\nv_mov_b64 v[158:159], v[126:127]\nv_mov_b64 v[160:161], v[128:129]\nv_mov_b64 v[162:163], v[130:131]\nv_mov_b64 v[164:165], v[132:133]\n"
      "v_mov_b64 v[166:167], v[134:135]\nv_mov_b64 v[168:169], v[136:137]\nv_mov_b64 v[170:171], v[138:139]\nv_mov_b64 v[172:173], v[124:125]\nv_mov_b64 v[174:175], v[126:127]\n"
      "v_mov_b64 v[176:177], v[128:129]\nv_mov_b64 v[178:179], v[130:131]\nv_mov_b64 v[180:181], v[132:133]\nv_mov_b64 v[182:183], v[134:135]\nv_mov_b64 v[184:185], v[136:137]\n"
      "v_mov_b64 v[186:187], v[138:139]\nv_mov_b64 v[188:189], v[124:125]\nv_mov_b64 v[190:191], v[126:127]\nv_mov_b64 v[192:193], v[128:129]\nv_mov_b64 v[194:195], v[130:131]\n"
      "v_mov_b64 v[196:197], v[132:133]\nv_mov_b64 v[198:199], v[134:135]\nv_mov_b64 v[200:201], v[136:137]\nv_mov_b64 v[202:203], v[138:139]\nv_mov_b64 v[204:205], v[124:125]\n"
      "v_mov_b64 v[206:207], v[126:127]\nv_mov_b64 v[208:209], v[128:129]\nv_mov_b64 v[210:211], v[130:131]\nv_mov_b64 v[212:213], v[132:133]\nv_mov_b64 v[214:215], v[134:135]\n"
      "v_mov_b64 v[216:217], v[136:137]\nv_mov_b64 v[218:219], v[138:139]\nv_mov_b64 v[220:221], v[124:125]\nv_mov_b64 v[222:223], v[126:127]\nv_mov_b64 v[224:225], v[128:129]\n"
      "v_mov_b64 v[226:227], v[130:131]\nv_mov_b64 v[228:229], v[132:133]\nv_mov_b64 v[230:231], v[134:135]\nv_mov_b64 v[232:233], v[136:137]\nv_mov_b64 v[234:235], v[138:139]\ns_mov_b32 s58, 4\n"
      "s_cmp_eq_u32 s25, 2\ns_cbranch_scc1 .Lg13\ns_cmp_eq_u32 s25, 0\ns_cbranch_scc1 .Lg12\n"
      ".Lg14:\ns_barrier\nds_read_b128 v[0:3], v102 offset:0\nds_read_b128 v[4:7], v103 offset:0\nds_read_b64_tr_b16 v[64:65], v108 offset:0\nds_read_b64_tr_b16 v[66:67], v108 offset:1024\n"
      "ds_read_b64_tr_b16 v[68:69], v108 offset:8192\nds_read_b64_tr_b16 v[70:71], v108 offset:9216\nds_read_b64_tr_b16 v[72:73], v109 offset:0\nds_read_b64_tr_b16 v[74:75], v109 offset:1024\n"
      "ds_read_b64_tr_b16 v[76:77], v109 offset:8192\nds_read_b64_tr_b16 v[78:79], v109 offset:9216\nds_read_b64_tr_b16 v[80:81], v110 offset:0\nds_read_b64_tr_b16 v[82:83], v110 offset:1024\n"
      "ds_read_b64_tr_b16 v[84:85], v110 offset:8192\nds_read_b64_tr_b16 v[86:87], v110 offset:9216\nds_read_b64_tr_b16 v[88:89], v111 offset:0\nds_read_b64_tr_b16 v[90:91], v111 offset:1024\n"
      "ds_read_b64_tr_b16 v[92:93], v111 offset:8192\nds_read_b64_tr_b16 v[94:95], v111 offset:9216\nds_read_b128 v[8:11], v98 offset:0\nds_read_b128 v[12:15], v99 offset:0\ns_waitcnt lgkmcnt(0)\n"
      "ds_read_b128 v[16:19], v98 offset:2048\nds_read_b128 v[20:23], v99 offset:2048\n.p2align 3\n"
      ".Lgl14:\nv_mfma_f32_16x16x32_bf16 v[124:127], v[64:67], v[0:3], v[124:127]\nds_read_b128 v[24:27], v98 offset:4096\nv_mfma_f32_16x16x32_bf16 v[124:127], v[68:71], v[4:7], v[124:127]\n"
      "ds_read_b128 v[28:31], v99 offset:4096\nv_mfma_f32_16x16x32_bf16 v[128:131], v[76:79], v[4:7], v[128:131]\nv_mfma_f32_16x16x32_bf16 v[128:131], v[72:75], v[0:3], v[128:131]\n"
      "v_mfma_f32_16x16x32_bf16 v[132:135], v[80:83], v[0:3], v[132:135]\nv_mfma_f32_16x16x32_bf16 v[132:135], v[84:87], v[4:7], v[132:135]\n"
      "v_mfma_f32_16x16x32_bf16 v[136:139], v[92:95], v[4:7], v[136:139]\nv_mfma_f32_16x16x32_bf16 v[136:139], v[88:91], v[0:3], v[136:139]\n"
      "v_mfma_f32_16x16x32_bf16 v[152:155], v[88:91], v[8:11], v[152:155]\nds_read_b128 v[0:3], v98 offset:6144\nv_mfma_f32_16x16x32_bf16 v[152:155], v[92:95], v[12:15], v[152:155]\n"
      "ds_read_b128 v[4:7], v99 offset:6144\nv_mfma_f32_16x16x32_bf16 v[148:151], v[84:87], v[12:15], v[148:151]\nv_mfma_f32_16x16x32_bf16 v[148:151], v[80:83], v[8:11], v[148:151]\n"
      "v_mfma_f32_16x16x32_bf16 v[144:147], v[72:75], v[8:11], v[144:147]\nv_mfma_f32_16x16x32_bf16 v[144:147], v[76:79], v[12:15], v[144:147]\n"
      "v_mfma_f32_16x16x32_bf16 v[140:143], v[68:71], v[12:15], v[140:143]\ns_waitcnt lgkmcnt(4)\nv_mfma_f32_16x16x32_bf16 v[140:143], v[64:67], v[8:11], v[140:143]\n"
      "v_mfma_f32_16x16x32_bf16 v[156:159], v[64:67], v[16:19], v[156:159]\nds_read_b128 v[8:11], v98 offset:8192\nv_mfma_f32_16x16x32_bf16 v[156:159], v[68:71], v[20:23], v[156:159]\n"
      "ds_read_b128 v[12:15], v99 offset:8192\nv_mfma_f32_16x16x32_bf16 v[160:163], v[76:79], v[20:23], v[160:163]\nv_mfma_f32_16x16x32_bf16 v[160:163], v[72:75], v[16:19], v[160:163]\n"
      "v_mfma_f32_16x16x32_bf16 v[164:167], v[80:83], v[16:19], v[164:167]\nv_mfma_f32_16x16x32_bf16 v[164:167], v[84:87], v[20:23], v[164:167]\n"
      "v_mfma_f32_16x16x32_bf16 v[168:171], v[92:95], v[20:23], v[168:171]\ns_waitcnt lgkmcnt(2)\nv_mfma_f32_16x16x32_bf16 v[168:171], v[88:91], v[16:19], v[168:171]\n"
      "v_mfma_f32_16x16x32_bf16 v[184:187], v[88:91], v[24:27], v[184:187]\nds_read_b128 v[16:19], v98 offset:10240\nv_mfma_f32_16x16x32_bf16 v[184:187], v[92:95], v[28:31], v[184:187]\n"
      "ds_read_b128 v[20:23], v99 offset:10240\nv_mfma_f32_16x16x32_bf16 v[180:183], v[84:87], v[28:31], v[180:183]\nv_mfma_f32_16x16x32_bf16 v[180:183], v[80:83], v[24:27], v[180:183]\n"
      "v_mfma_f32_16x16x32_bf16 v[176:179], v[72:75], v[24:27], v[176:179]\nv_mfma_f32_16x16x32_bf16 v[176:179], v[76:79], v[28:31], v[176:179]\n"
      "v_mfma_f32_16x16x32_bf16 v[172:175], v[68:71], v[28:31], v[172:175]\ns_waitcnt vmcnt(0) lgkmcnt(0)\ns_barrier\nv_mfma_f32_16x16x32_bf16 v[172:175], v[64:67], v[24:27], v[172:175]\n"
      "ds_read_b64_tr_b16 v[32:33], v104 offset:0\nv_mfma_f32_16x16x32_bf16 v[188:191], v[64:67], v[0:3], v[188:191]\nds_read_b64_tr_b16 v[34:35], v104 offset:1024\n"
      "v_mfma_f32_16x16x32_bf16 v[188:191], v[68:71], v[4:7], v[188:191]\nds_read_b64_tr_b16 v[36:37], v104 offset:8192\nv_mfma_f32_16x16x32_bf16 v[192:195], v[76:79], v[4:7], v[192:195]\n"
      "ds_read_b64_tr_b16 v[38:39], v104 offset:9216\nv_mfma_f32_16x16x32_bf16 v[192:195], v[72:75], v[0:3], v[192:195]\nds_read_b64_tr_b16 v[40:41], v105 offset:0\n"
      "v_mfma_f32_16x16x32_bf16 v[196:199], v[80:83], v[0:3], v[196:199]\nds_read_b64_tr_b16 v[42:43], v105 offset:1024\nv_mfma_f32_16x16x32_bf16 v[196:199], v[84:87], v[4:7], v[196:199]\n"
      "ds_read_b64_tr_b16 v[44:45], v105 offset:8192\nv_mfma_f32_16x16x32_bf16 v[200:203], v[92:95], v[4:7], v[200:203]\nds_read_b64_tr_b16 v[46:47], v105 offset:9216\n"
      "v_mfma_f32_16x16x32_bf16 v[200:203], v[88:91], v[0:3], v[200:203]\nds_read_b64_tr_b16 v[48:49], v106 offset:0\nv_mfma_f32_16x16x32_bf16 v[216:219], v[88:91], v[8:11], v[216:219]\n"
      "ds_read_b128 v[0:3], v100 offset:0\nv_mfma_f32_16x16x32_bf16 v[216:219], v[92:95], v[12:15], v[216:219]\nds_read_b128 v[4:7], v101 offset:0\n"
      "v_mfma_f32_16x16x32_bf16 v[212:215], v[84:87], v[12:15], v[212:215]\nds_read_b64_tr_b16 v[50:51], v106 offset:1024\nv_mfma_f32_16x16x32_bf16 v[212:215], v[80:83], v[8:11], v[212:215]\n"
      "ds_read_b64_tr_b16 v[52:53], v106 offset:8192\nv_mfma_f32_16x16x32_bf16 v[208:211], v[72:75], v[8:11], v[208:211]\nds_read_b64_tr_b16 v[54:55], v106 offset:9216\n"
      "v_mfma_f32_16x16x32_bf16 v[208:211], v[76:79], v[12:15], v[208:211]\nds_read_b64_tr_b16 v[56:57], v107 offset:0\nv_mfma_f32_16x16x32_bf16 v[204:207], v[68:71], v[12:15], v[204:207]\n"
      "ds_read_b64_tr_b16 v[58:59], v107 offset:1024\nv_mfma_f32_16x16x32_bf16 v[204:207], v[64:67], v[8:11], v[204:207]\nds_read_b64_tr_b16 v[60:61], v107 offset:8192\n"
      "v_mfma_f32_16x16x32_bf16 v[220:223], v[64:67], v[16:19], v[220:223]\nds_read_b128 v[8:11], v96 offset:0\nv_mfma_f32_16x16x32_bf16 v[220:223], v[68:71], v[20:23], v[220:223]\n"
      "ds_read_b128 v[12:15], v97 offset:0\nv_mfma_f32_16x16x32_bf16 v[224:227], v[76:79], v[20:23], v[224:227]\nds_read_b64_tr_b16 v[62:63], v107 offset:9216\n"
      "v_mfma_f32_16x16x32_bf16 v[224:227], v[72:75], v[16:19], v[224:227]\nv_mfma_f32_16x16x32_bf16 v[228:231], v[80:83], v[16:19], v[228:231]\n"
      "v_mfma_f32_16x16x32_bf16 v[228:231], v[84:87], v[20:23], v[228:231]\nv_mfma_f32_16x16x32_bf16 v[232:235], v[92:95], v[20:23], v[232:235]\ns_waitcnt lgkmcnt(0)\n"
      "v_mfma_f32_16x16x32_bf16 v[232:235], v[88:91], v[16:19], v[232:235]\nds_read_b128 v[16:19], v96 offset:2048\nds_read_b128 v[20:23], v97 offset:2048\n"
      "v_mfma_f32_16x16x32_bf16 v[124:127], v[32:35], v[0:3], v[124:127]\nds_read_b128 v[24:27], v96 offset:4096\nv_mfma_f32_16x16x32_bf16 v[124:127], v[36:39], v[4:7], v[124:127]\n"
      "ds_read_b128 v[28:31], v97 offset:4096\nv_mfma_f32_16x16x32_bf16 v[128:131], v[44:47], v[4:7], v[128:131]\nv_mfma_f32_16x16x32_bf16 v[128:131], v[40:43], v[0:3], v[128:131]\n"
      "v_mfma_f32_16x16x32_bf16 v[132:135], v[48:51], v[0:3], v[132:135]\nv_mfma_f32_16x16x32_bf16 v[132:135], v[52:55], v[4:7], v[132:135]\n"
      "v_mfma_f32_16x16x32_bf16 v[136:139], v[60:63], v[4:7], v[136:139]\nv_mfma_f32_16x16x32_bf16 v[136:139], v[56:59], v[0:3], v[136:139]\n"
      "v_mfma_f32_16x16x32_bf16 v[152:155], v[56:59], v[8:11], v[152:155]\nds_read_b128 v[0:3], v96 offset:6144\nv_mfma_f32_16x16x32_bf16 v[152:155], v[60:63], v[12:15], v[152:155]\n"
      "ds_read_b128 v[4:7], v97 offset:6144\nv_mfma_f32_16x16x32_bf16 v[148:151], v[52:55], v[12:15], v[148:151]\nv_mfma_f32_16x16x32_bf16 v[148:151], v[48:51], v[8:11], v[148:151]\n"
      "v_mfma_f32_16x16x32_bf16 v[144:147], v[40:43], v[8:11], v[144:147]\nv_mfma_f32_16x16x32_bf16 v[144:147], v[44:47], v[12:15], v[144:147]\n"
      "v_mfma_f32_16x16x32_bf16 v[140:143], v[36:39], v[12:15], v[140:143]\ns_waitcnt lgkmcnt(4)\nv_mfma_f32_16x16x32_bf16 v[140:143], v[32:35], v[8:11], v[140:143]\n"
      "v_mfma_f32_16x16x32_bf16 v[156:159], v[32:35], v[16:19], v[156:159]\nds_read_b128 v[8:11], v96 offset:8192\nv_mfma_f32_16x16x32_bf16 v[156:159], v[36:39], v[20:23], v[156:159]\n"
      "ds_read_b128 v[12:15], v97 offset:8192\nv_mfma_f32_16x16x32_bf16 v[160:163], v[44:47], v[20:23], v[160:163]\nv_mfma_f32_16x16x32_bf16 v[160:163], v[40:43], v[16:19], v[160:163]\n"
      "v_mfma_f32_16x16x32_bf16 v[164:167], v[48:51], v[16:19], v[164:167]\nv_mfma_f32_16x16x32_bf16 v[164:167], v[52:55], v[20:23], v[164:167]\n"
      "v_mfma_f32_16x16x32_bf16 v[168:171], v[60:63], v[20:23], v[168:171]\ns_waitcnt lgkmcnt(2)\nv_mfma_f32_16x16x32_bf16 v[168:171], v[56:59], v[16:19], v[168:171]\n"
      "v_mfma_f32_16x16x32_bf16 v[184:187], v[56:59], v[24:27], v[184:187]\nds_read_b128 v[16:19], v96 offset:10240\nv_mfma_f32_16x16x32_bf16 v[184:187], v[60:63], v[28:31], v[184:187]\n"
      "ds_read_b128 v[20:23], v97 offset:10240\nv_mfma_f32_16x16x32_bf16 v[180:183], v[52:55], v[28:31], v[180:183]\nv_mfma_f32_16x16x32_bf16 v[180:183], v[48:51], v[24:27], v[180:183]\n"
      "v_mfma_f32_16x16x32_bf16 v[176:179], v[40:43], v[24:27], v[176:179]\nv_mfma_f32_16x16x32_bf16 v[176:179], v[44:47], v[28:31], v[176:179]\n"
      "v_mfma_f32_16x16x32_bf16 v[172:175], v[36:39], v[28:31], v[172:175]\ns_waitcnt vmcnt(0) lgkmcnt(0)\ns_barrier\nv_mfma_f32_16x16x32_bf16 v[172:175], v[32:35], v[24:27], v[172:175]\n"
      "ds_read_b64_tr_b16 v[64:65], v104 offset:45056\nv_mfma_f32_16x16x32_bf16 v[188:191], v[32:35], v[0:3], v[188:191]\nds_read_b64_tr_b16 v[66:67], v104 offset:46080\n"
      "v_mfma_f32_16x16x32_bf16 v[188:191], v[36:39], v[4:7], v[188:191]\nds_read_b64_tr_b16 v[68:69], v104 offset:53248\nv_mfma_f32_16x16x32_bf16 v[192:195], v[44:47], v[4:7], v[192:195]\n"
      "ds_read_b64_tr_b16 v[70:71], v104 offset:54272\nv_mfma_f32_16x16x32_bf16 v[192:195], v[40:43], v[0:3], v[192:195]\nds_read_b64_tr_b16 v[72:73], v105 offset:45056\n"
      "v_mfma_f32_16x16x32_bf16 v[196:199], v[48:51], v[0:3], v[196:199]\nds_read_b64_tr_b16 v[74:75], v105 offset:46080\nv_mfma_f32_16x16x32_bf16 v[196:199], v[52:55], v[4:7], v[196:199]\n"
      "ds_read_b64_tr_b16 v[76:77], v105 offset:53248\nv_mfma_f32_16x16x32_bf16 v[200:203], v[60:63], v[4:7], v[200:203]\nds_read_b64_tr_b16 v[78:79], v105 offset:54272\n"
      "v_mfma_f32_16x16x32_bf16 v[200:203], v[56:59], v[0:3], v[200:203]\nds_read_b64_tr_b16 v[80:81], v106 offset:45056\nv_mfma_f32_16x16x32_bf16 v[216:219], v[56:59], v[8:11], v[216:219]\n"
      "ds_read_b128 v[0:3], v100 offset:45056\nv_mfma_f32_16x16x32_bf16 v[216:219], v[60:63], v[12:15], v[216:219]\nds_read_b128 v[4:7], v101 offset:45056\n"
      "v_mfma_f32_16x16x32_bf16 v[212:215], v[52:55], v[12:15], v[212:215]\nds_read_b64_tr_b16 v[82:83], v106 offset:46080\nv_mfma_f32_16x16x32_bf16 v[212:215], v[48:51], v[8:11], v[212:215]\n"
      "ds_read_b64_tr_b16 v[84:85], v106 offset:53248\nv_mfma_f32_16x16x32_bf16 v[208:211], v[40:43], v[8:11], v[208:211]\nds_read_b64_tr_b16 v[86:87], v106 offset:54272\n"
      "v_mfma_f32_16x16x32_bf16 v[208:211], v[44:47], v[12:15], v[208:211]\nds_read_b64_tr_b16 v[88:89], v107 offset:45056\nv_mfma_f32_16x16x32_bf16 v[204:207], v[36:39], v[12:15], v[204:207]\n"
      "ds_read_b64_tr_b16 v[90:91], v107 offset:46080\nv_mfma_f32_16x16x32_bf16 v[204:207], v[32:35], v[8:11], v[204:207]\nds_read_b64_tr_b16 v[92:93], v107 offset:53248\n"
      "v_mfma_f32_16x16x32_bf16 v[220:223], v[32:35], v[16:19], v[220:223]\nds_read_b128 v[8:11], v96 offset:45056\nv_mfma_f32_16x16x32_bf16 v[220:223], v[36:39], v[20:23], v[220:223]\n"
      "ds_read_b128 v[12:15], v97 offset:45056\nv_mfma_f32_16x16x32_bf16 v[224:227], v[44:47], v[20:23], v[224:227]\nds_read_b64_tr_b16 v[94:95], v107 offset:54272\n"
      "v_mfma_f32_16x16x32_bf16 v[224:227], v[40:43], v[16:19], v[224:227]\nv_mfma_f32_16x16x32_bf16 v[228:231], v[48:51], v[16:19], v[228:231]\n"
      "v_mfma_f32_16x16x32_bf16 v[228:231], v[52:55], v[20:23], v[228:231]\nv_mfma_f32_16x16x32_bf16 v[232:235], v[60:63], v[20:23], v[232:235]\ns_waitcnt lgkmcnt(0)\n"
      "v_mfma_f32_16x16x32_bf16 v[232:235], v[56:59], v[16:19], v[232:235]\nds_read_b128 v[16:19], v96 offset:47104\nds_read_b128 v[20:23], v97 offset:47104\n"
      "v_mfma_f32_16x16x32_bf16 v[124:127], v[64:67], v[0:3], v[124:127]\nds_read_b128 v[24:27], v96 offset:49152\nv_mfma_f32_16x16x32_bf16 v[124:127], v[68:71], v[4:7], v[124:127]\n"
      "ds_read_b128 v[28:31], v97 offset:49152\nv_mfma_f32_16x16x32_bf16 v[128:131], v[76:79], v[4:7], v[128:131]\nv_mfma_f32_16x16x32_bf16 v[128:131], v[72:75], v[0:3], v[128:131]\n"
      "v_mfma_f32_16x16x32_bf16 v[132:135], v[80:83], v[0:3], v[132:135]\nv_mfma_f32_16x16x32_bf16 v[132:135], v[84:87], v[4:7], v[132:135]\n"
      "v_mfma_f32_16x16x32_bf16 v[136:139], v[92:95], v[4:7], v[136:139]\nv_mfma_f32_16x16x32_bf16 v[136:139], v[88:91], v[0:3], v[136:139]\n"
      "v_mfma_f32_16x16x32_bf16 v[152:155], v[88:91], v[8:11], v[152:155]\nds_read_b128 v[0:3], v96 offset:51200\nv_mfma_f32_16x16x32_bf16 v[152:155], v[92:95], v[12:15], v[152:155]\n"
      "ds_read_b128 v[4:7], v97 offset:51200\nv_mfma_f32_16x16x32_bf16 v[148:151], v[84:87], v[12:15], v[148:151]\nv_mfma_f32_16x16x32_bf16 v[148:151], v[80:83], v[8:11], v[148:151]\n"
      "v_mfma_f32_16x16x32_bf16 v[144:147], v[72:75], v[8:11], v[144:147]\nv_mfma_f32_16x16x32_bf16 v[144:147], v[76:79], v[12:15], v[144:147]\n"
      "v_mfma_f32_16x16x32_bf16 v[140:143], v[68:71], v[12:15], v[140:143]\ns_waitcnt lgkmcnt(4)\nv_mfma_f32_16x16x32_bf16 v[140:143], v[64:67], v[8:11], v[140:143]\n"
      "v_mfma_f32_16x16x32_bf16 v[156:159], v[64:67], v[16:19], v[156:159]\nds_read_b128 v[8:11], v96 offset:53248\nv_mfma_f32_16x16x32_bf16 v[156:159], v[68:71], v[20:23], v[156:159]\n"
      "ds_read_b128 v[12:15], v97 offset:53248\nv_mfma_f32_16x16x32_bf16 v[160:163], v[76:79], v[20:23], v[160:163]\nv_mfma_f32_16x16x32_bf16 v[160:163], v[72:75], v[16:19], v[160:163]\n"
      "v_mfma_f32_16x16x32_bf16 v[164:167], v[80:83], v[16:19], v[164:167]\nv_mfma_f32_16x16x32_bf16 v[164:167], v[84:87], v[20:23], v[164:167]\n"
      "v_mfma_f32_16x16x32_bf16 v[168:171], v[92:95], v[20:23], v[168:171]\ns_waitcnt lgkmcnt(2)\nv_mfma_f32_16x16x32_bf16 v[168:171], v[88:91], v[16:19], v[168:171]\n"
      "v_mfma_f32_16x16x32_bf16 v[184:187], v[88:91], v[24:27], v[184:187]\nds_read_b128 v[16:19], v96 offset:55296\nv_mfma_f32_16x16x32_bf16 v[184:187], v[92:95], v[28:31], v[184:187]\n"
      "ds_read_b128 v[20:23], v97 offset:55296\nv_mfma_f32_16x16x32_bf16 v[180:183], v[84:87], v[28:31], v[180:183]\nv_mfma_f32_16x16x32_bf16 v[180:183], v[80:83], v[24:27], v[180:183]\n"
      "v_mfma_f32_16x16x32_bf16 v[176:179], v[72:75], v[24:27], v[176:179]\nv_mfma_f32_16x16x32_bf16 v[176:179], v[76:79], v[28:31], v[176:179]\n"
      "v_mfma_f32_16x16x32_bf16 v[172:175], v[68:71], v[28:31], v[172:175]\ns_waitcnt vmcnt(0) lgkmcnt(0)\ns_barrier\nv_mfma_f32_16x16x32_bf16 v[172:175], v[64:67], v[24:27], v[172:175]\n"
      "ds_read_b64_tr_b16 v[32:33], v108 offset:0\nv_mfma_f32_16x16x32_bf16 v[188:191], v[64:67], v[0:3], v[188:191]\nds_read_b64_tr_b16 v[34:35], v108 offset:1024\n"
      "v_mfma_f32_16x16x32_bf16 v[188:191], v[68:71], v[4:7], v[188:191]\nds_read_b64_tr_b16 v[36:37], v108 offset:8192\nv_mfma_f32_16x16x32_bf16 v[192:195], v[76:79], v[4:7], v[192:195]\n"
      "ds_read_b64_tr_b16 v[38:39], v108 offset:9216\nv_mfma_f32_16x16x32_bf16 v[192:195], v[72:75], v[0:3], v[192:195]\nds_read_b64_tr_b16 v[40:41], v109 offset:0\n"
      "v_mfma_f32_16x16x32_bf16 v[196:199], v[80:83], v[0:3], v[196:199]\nds_read_b64_tr_b16 v[42:43], v109 offset:1024\nv_mfma_f32_16x16x32_bf16 v[196:199], v[84:87], v[4:7], v[196:199]\n"
      "ds_read_b64_tr_b16 v[44:45], v109 offset:8192\nv_mfma_f32_16x16x32_bf16 v[200:203], v[92:95], v[4:7], v[200:203]\nds_read_b64_tr_b16 v[46:47], v109 offset:9216\n"
      "v_mfma_f32_16x16x32_bf16 v[200:203], v[88:91], v[0:3], v[200:203]\nds_read_b64_tr_b16 v[48:49], v110 offset:0\nv_mfma_f32_16x16x32_bf16 v[216:219], v[88:91], v[8:11], v[216:219]\n"
      "ds_read_b128 v[0:3], v102 offset:0\nv_mfma_f32_16x16x32_bf16 v[216:219], v[92:95], v[12:15], v[216:219]\nds_read_b128 v[4:7], v103 offset:0\n"
      "v_mfma_f32_16x16x32_bf16 v[212:215], v[84:87], v[12:15], v[212:215]\nds_read_b64_tr_b16 v[50:51], v110 offset:1024\nv_mfma_f32_16x16x32_bf16 v[212:215], v[80:83], v[8:11], v[212:215]\n"
      "ds_read_b64_tr_b16 v[52:53], v110 offset:8192\nv_mfma_f32_16x16x32_bf16 v[208:211], v[72:75], v[8:11], v[208:211]\nds_read_b64_tr_b16 v[54:55], v110 offset:9216\n"
      "v_mfma_f32_16x16x32_bf16 v[208:211], v[76:79], v[12:15], v[208:211]\nds_read_b64_tr_b16 v[56:57], v111 offset:0\nv_mfma_f32_16x16x32_bf16 v[204:207], v[68:71], v[12:15], v[204:207]\n"
      "ds_read_b64_tr_b16 v[58:59], v111 offset:1024\nv_mfma_f32_16x16x32_bf16 v[204:207], v[64:67], v[8:11], v[204:207]\nds_read_b64_tr_b16 v[60:61], v111 offset:8192\n"
      "v_mfma_f32_16x16x32_bf16 v[220:223], v[64:67], v[16:19], v[220:223]\nds_read_b128 v[8:11], v98 offset:0\nv_mfma_f32_16x16x32_bf16 v[220:223], v[68:71], v[20:23], v[220:223]\n"
      "ds_read_b128 v[12:15], v99 offset:0\nv_mfma_f32_16x16x32_bf16 v[224:227], v[76:79], v[20:23], v[224:227]\nds_read_b64_tr_b16 v[62:63], v111 offset:9216\n"
      "v_mfma_f32_16x16x32_bf16 v[224:227], v[72:75], v[16:19], v[224:227]\nv_mfma_f32_16x16x32_bf16 v[228:231], v[80:83], v[16:19], v[228:231]\n"
      "v_mfma_f32_16x16x32_bf16 v[228:231], v[84:87], v[20:23], v[228:231]\nv_mfma_f32_16x16x32_bf16 v[232:235], v[92:95], v[20:23], v[232:235]\ns_waitcnt lgkmcnt(0)\n"
      "v_mfma_f32_16x16x32_bf16 v[232:235], v[88:91], v[16:19], v[232:235]\nds_read_b128 v[16:19], v98 offset:2048\nds_read_b128 v[20:23], v99 offset:2048\n"
      "v_mfma_f32_16x16x32_bf16 v[124:127], v[32:35], v[0:3], v[124:127]\nds_read_b128 v[24:27], v98 offset:4096\nv_mfma_f32_16x16x32_bf16 v[124:127], v[36:39], v[4:7], v[124:127]\n"
      "ds_read_b128 v[28:31], v99 offset:4096\nv_mfma_f32_16x16x32_bf16 v[128:131], v[44:47], v[4:7], v[128:131]\nv_mfma_f32_16x16x32_bf16 v[128:131], v[40:43], v[0:3], v[128:131]\n"
      "v_mfma_f32_16x16x32_bf16 v[132:135], v[48:51], v[0:3], v[132:135]\nv_mfma_f32_16x16x32_bf16 v[132:135], v[52:55], v[4:7], v[132:135]\n"
      "v_mfma_f32_16x16x32_bf16 v[136:139], v[60:63], v[4:7], v[136:139]\nv_mfma_f32_16x16x32_bf16 v[136:139], v[56:59], v[0:3], v[136:139]\n"
      "v_mfma_f32_16x16x32_bf16 v[152:155], v[56:59], v[8:11], v[152:155]\nds_read_b128 v[0:3], v98 offset:6144\nv_mfma_f32_16x16x32_bf16 v[152:155], v[60:63], v[12:15], v[152:155]\n"
      "ds_read_b128 v[4:7], v99 offset:6144\nv_mfma_f32_16x16x32_bf16 v[148:151], v[52:55], v[12:15], v[148:151]\nv_mfma_f32_16x16x32_bf16 v[148:151], v[48:51], v[8:11], v[148:151]\n"
      "v_mfma_f32_16x16x32_bf16 v[144:147], v[40:43], v[8:11], v[144:147]\nv_mfma_f32_16x16x32_bf16 v[144:147], v[44:47], v[12:15], v[144:147]\n"
      "v_mfma_f32_16x16x32_bf16 v[140:143], v[36:39], v[12:15], v[140:143]\ns_waitcnt lgkmcnt(4)\nv_mfma_f32_16x16x32_bf16 v[140:143], v[32:35], v[8:11], v[140:143]\n"
      "v_mfma_f32_16x16x32_bf16 v[156:159], v[32:35], v[16:19], v[156:159]\nds_read_b128 v[8:11], v98 offset:8192\nv_mfma_f32_16x16x32_bf16 v[156:159], v[36:39], v[20:23], v[156:159]\n"
      "ds_read_b128 v[12:15], v99 offset:8192\nv_mfma_f32_16x16x32_bf16 v[160:163], v[44:47], v[20:23], v[160:163]\nv_mfma_f32_16x16x32_bf16 v[160:163], v[40:43], v[16:19], v[160:163]\n"
      "v_mfma_f32_16x16x32_bf16 v[164:167], v[48:51], v[16:19], v[164:167]\nv_mfma_f32_16x16x32_bf16 v[164:167], v[52:55], v[20:23], v[164:167]\n"
      "v_mfma_f32_16x16x32_bf16 v[168:171], v[60:63], v[20:23], v[168:171]\ns_waitcnt lgkmcnt(2)\nv_mfma_f32_16x16x32_bf16 v[168:171], v[56:59], v[16:19], v[168:171]\n"
      "v_mfma_f32_16x16x32_bf16 v[184:187], v[56:59], v[24:27], v[184:187]\nds_read_b128 v[16:19], v98 offset:10240\nv_mfma_f32_16x16x32_bf16 v[184:187], v[60:63], v[28:31], v[184:187]\n"
      "ds_read_b128 v[20:23], v99 offset:10240\nv_mfma_f32_16x16x32_bf16 v[180:183], v[52:55], v[28:31], v[180:183]\nv_mfma_f32_16x16x32_bf16 v[180:183], v[48:51], v[24:27], v[180:183]\n"
      "v_mfma_f32_16x16x32_bf16 v[176:179], v[40:43], v[24:27], v[176:179]\nv_mfma_f32_16x16x32_bf16 v[176:179], v[44:47], v[28:31], v[176:179]\n"
      "v_mfma_f32_16x16x32_bf16 v[172:175], v[36:39], v[28:31], v[172:175]\ns_waitcnt vmcnt(0) lgkmcnt(0)\ns_barrier\nv_mfma_f32_16x16x32_bf16 v[172:175], v[32:35], v[24:27], v[172:175]\n"
      "ds_read_b64_tr_b16 v[64:65], v104 offset:0\nv_mfma_f32_16x16x32_bf16 v[188:191], v[32:35], v[0:3], v[188:191]\nds_read_b64_tr_b16 v[66:67], v104 offset:1024\n"
      "v_mfma_f32_16x16x32_bf16 v[188:191], v[36:39], v[4:7], v[188:191]\nds_read_b64_tr_b16 v[68:69], v104 offset:8192\nv_mfma_f32_16x16x32_bf16 v[192:195], v[44:47], v[4:7], v[192:195]\n"
      "ds_read_b64_tr_b16 v[70:71], v104 offset:9216\nv_mfma_f32_16x16x32_bf16 v[192:195], v[40:43], v[0:3], v[192:195]\nds_read_b64_tr_b16 v[72:73], v105 offset:0\n"
      "v_mfma_f32_16x16x32_bf16 v[196:199], v[48:51], v[0:3], v[196:199]\nds_read_b64_tr_b16 v[74:75], v105 offset:1024\nv_mfma_f32_16x16x32_bf16 v[196:199], v[52:55], v[4:7], v[196:199]\n"
      "ds_read_b64_tr_b16 v[76:77], v105 offset:8192\nv_mfma_f32_16x16x32_bf16 v[200:203], v[60:63], v[4:7], v[200:203]\nds_read_b64_tr_b16 v[78:79], v105 offset:9216\n"
      "v_mfma_f32_16x16x32_bf16 v[200:203], v[56:59], v[0:3], v[200:203]\nds_read_b64_tr_b16 v[80:81], v106 offset:0\nv_mfma_f32_16x16x32_bf16 v[216:219], v[56:59], v[8:11], v[216:219]\n"
      "ds_read_b128 v[0:3], v100 offset:0\nv_mfma_f32_16x16x32_bf16 v[216:219], v[60:63], v[12:15], v[216:219]\nds_read_b128 v[4:7], v101 offset:0\n"
      "v_mfma_f32_16x16x32_bf16 v[212:215], v[52:55], v[12:15], v[212:215]\nds_read_b64_tr_b16 v[82:83], v106 offset:1024\nv_mfma_f32_16x16x32_bf16 v[212:215], v[48:51], v[8:11], v[212:215]\n"
      "ds_read_b64_tr_b16 v[84:85], v106 offset:8192\nv_mfma_f32_16x16x32_bf16 v[208:211], v[40:43], v[8:11], v[208:211]\nds_read_b64_tr_b16 v[86:87], v106 offset:9216\n"
      "v_mfma_f32_16x16x32_bf16 v[208:211], v[44:47], v[12:15], v[208:211]\nds_read_b64_tr_b16 v[88:89], v107 offset:0\nv_mfma_f32_16x16x32_bf16 v[204:207], v[36:39], v[12:15], v[204:207]\n"
      "ds_read_b64_tr_b16 v[90:91], v107 offset:1024\nv_mfma_f32_16x16x32_bf16 v[204:207], v[32:35], v[8:11], v[204:207]\nds_read_b64_tr_b16 v[92:93], v107 offset:8192\n"
      "v_mfma_f32_16x16x32_bf16 v[220:223], v[32:35], v[16:19], v[220:223]\nds_read_b128 v[8:11], v96 offset:0\nv_mfma_f32_16x16x32_bf16 v[220:223], v[36:39], v[20:23], v[220:223]\n"
      "ds_read_b128 v[12:15], v97 offset:0\nv_mfma_f32_16x16x32_bf16 v[224:227], v[44:47], v[20:23], v[224:227]\nds_read_b64_tr_b16 v[94:95], v107 offset:9216\n"
      "v_mfma_f32_16x16x32_bf16 v[224:227], v[40:43], v[16:19], v[224:227]\nv_mfma_f32_16x16x32_bf16 v[228:231], v[48:51], v[16:19], v[228:231]\n"
      "v_mfma_f32_16x16x32_bf16 v[228:231], v[52:55], v[20:23], v[228:231]\nv_mfma_f32_16x16x32_bf16 v[232:235], v[60:63], v[20:23], v[232:235]\ns_waitcnt lgkmcnt(0)\n"
      "v_mfma_f32_16x16x32_bf16 v[232:235], v[56:59], v[16:19], v[232:235]\nds_read_b128 v[16:19], v96 offset:2048\nds_read_b128 v[20:23], v97 offset:2048\n"
      "v_mfma_f32_16x16x32_bf16 v[124:127], v[64:67], v[0:3], v[124:127]\nds_read_b128 v[24:27], v96 offset:4096\nv_mfma_f32_16x16x32_bf16 v[124:127], v[68:71], v[4:7], v[124:127]\n"
      "ds_read_b128 v[28:31], v97 offset:4096\nv_mfma_f32_16x16x32_bf16 v[128:131], v[76:79], v[4:7], v[128:131]\nv_mfma_f32_16x16x32_bf16 v[128:131], v[72:75], v[0:3], v[128:131]\n"
      "v_mfma_f32_16x16x32_bf16 v[132:135], v[80:83], v[0:3], v[132:135]\nv_mfma_f32_16x16x32_bf16 v[132:135], v[84:87], v[4:7], v[132:135]\n"
      "v_mfma_f32_16x16x32_bf16 v[136:139], v[92:95], v[4:7], v[136:139]\nv_mfma_f32_16x16x32_bf16 v[136:139], v[88:91], v[0:3], v[136:139]\n"
      "v_mfma_f32_16x16x32_bf16 v[152:155], v[88:91], v[8:11], v[152:155]\nds_read_b128 v[0:3], v96 offset:6144\nv_mfma_f32_16x16x32_bf16 v[152:155], v[92:95], v[12:15], v[152:155]\n"
      "ds_read_b128 v[4:7], v97 offset:6144\nv_mfma_f32_16x16x32_bf16 v[148:151], v[84:87], v[12:15], v[148:151]\nv_mfma_f32_16x16x32_bf16 v[148:151], v[80:83], v[8:11], v[148:151]\n"
      "v_mfma_f32_16x16x32_bf16 v[144:147], v[72:75], v[8:11], v[144:147]\nv_mfma_f32_16x16x32_bf16 v[144:147], v[76:79], v[12:15], v[144:147]\n"
      "v_mfma_f32_16x16x32_bf16 v[140:143], v[68:71], v[12:15], v[140:143]\ns_waitcnt lgkmcnt(4)\nv_mfma_f32_16x16x32_bf16 v[140:143], v[64:67], v[8:11], v[140:143]\n"
      "v_mfma_f32_16x16x32_bf16 v[156:159], v[64:67], v[16:19], v[156:159]\nds_read_b128 v[8:11], v96 offset:8192\nv_mfma_f32_16x16x32_bf16 v[156:159], v[68:71], v[20:23], v[156:159]\n"
      "ds_read_b128 v[12:15], v97 offset:8192\nv_mfma_f32_16x16x32_bf16 v[160:163], v[76:79], v[20:23], v[160:163]\nv_mfma_f32_16x16x32_bf16 v[160:163], v[72:75], v[16:19], v[160:163]\n"
      "v_mfma_f32_16x16x32_bf16 v[164:167], v[80:83], v[16:19], v[164:167]\nv_mfma_f32_16x16x32_bf16 v[164:167], v[84:87], v[20:23], v[164:167]\n"
      "v_mfma_f32_16x16x32_bf16 v[168:171], v[92:95], v[20:23], v[168:171]\ns_waitcnt lgkmcnt(2)\nv_mfma_f32_16x16x32_bf16 v[168:171], v[88:91], v[16:19], v[168:171]\n"
      "v_mfma_f32_16x16x32_bf16 v[184:187], v[88:91], v[24:27], v[184:187]\nds_read_b128 v[16:19], v96 offset:10240\nv_mfma_f32_16x16x32_bf16 v[184:187], v[92:95], v[28:31], v[184:187]\n"
      "ds_read_b128 v[20:23], v97 offset:10240\nv_mfma_f32_16x16x32_bf16 v[180:183], v[84:87], v[28:31], v[180:183]\nv_mfma_f32_16x16x32_bf16 v[180:183], v[80:83], v[24:27], v[180:183]\n"
      "v_mfma_f32_16x16x32_bf16 v[176:179], v[72:75], v[24:27], v[176:179]\nv_mfma_f32_16x16x32_bf16 v[176:179], v[76:79], v[28:31], v[176:179]\n"
      "v_mfma_f32_16x16x32_bf16 v[172:175], v[68:71], v[28:31], v[172:175]\ns_waitcnt vmcnt(0) lgkmcnt(0)\ns_barrier\nv_mfma_f32_16x16x32_bf16 v[172:175], v[64:67], v[24:27], v[172:175]\n"
      "ds_read_b64_tr_b16 v[32:33], v104 offset:45056\nv_mfma_f32_16x16x32_bf16 v[188:191], v[64:67], v[0:3], v[188:191]\nds_read_b64_tr_b16 v[34:35], v104 offset:46080\n"
      "v_mfma_f32_16x16x32_bf16 v[188:191], v[68:71], v[4:7], v[188:191]\nds_read_b64_tr_b16 v[36:37], v104 offset:53248\nv_mfma_f32_16x16x32_bf16 v[192:195], v[76:79], v[4:7], v[192:195]\n"
      "ds_read_b64_tr_b16 v[38:39], v104 offset:54272\nv_mfma_f32_16x16x32_bf16 v[192:195], v[72:75], v[0:3], v[192:195]\nds_read_b64_tr_b16 v[40:41], v105 offset:45056\n"
      "v_mfma_f32_16x16x32_bf16 v[196:199], v[80:83], v[0:3], v[196:199]\nds_read_b64_tr_b16 v[42:43], v105 offset:46080\nv_mfma_f32_16x16x32_bf16 v[196:199], v[84:87], v[4:7], v[196:199]\n"
      "ds_read_b64_tr_b16 v[44:45], v105 offset:53248\nv_mfma_f32_16x16x32_bf16 v[200:203], v[92:95], v[4:7], v[200:203]\nds_read_b64_tr_b16 v[46:47], v105 offset:54272\n"
      "v_mfma_f32_16x16x32_bf16 v[200:203], v[88:91], v[0:3], v[200:203]\nds_read_b64_tr_b16 v[48:49], v106 offset:45056\nv_mfma_f32_16x16x32_bf16 v[216:219], v[88:91], v[8:11], v[216:219]\n"
      "ds_read_b128 v[0:3], v100 offset:45056\nv_mfma_f32_16x16x32_bf16 v[216:219], v[92:95], v[12:15], v[216:219]\nds_read_b128 v[4:7], v101 offset:45056\n"
      "v_mfma_f32_16x16x32_bf16 v[212:215], v[84:87], v[12:15], v[212:215]\nds_read_b64_tr_b16 v[50:51], v106 offset:46080\nv_mfma_f32_16x16x32_bf16 v[212:215], v[80:83], v[8:11], v[212:215]\n"
      "ds_read_b64_tr_b16 v[52:53], v106 offset:53248\nv_mfma_f32_16x16x32_bf16 v[208:211], v[72:75], v[8:11], v[208:211]\nds_read_b64_tr_b16 v[54:55], v106 offset:54272\n"
      "v_mfma_f32_16x16x32_bf16 v[208:211], v[76:79], v[12:15], v[208:211]\nds_read_b64_tr_b16 v[56:57], v107 offset:45056\nv_mfma_f32_16x16x32_bf16 v[204:207], v[68:71], v[12:15], v[204:207]\n"
      "ds_read_b64_tr_b16 v[58:59], v107 offset:46080\nv_mfma_f32_16x16x32_bf16 v[204:207], v[64:67], v[8:11], v[204:207]\nds_read_b64_tr_b16 v[60:61], v107 offset:53248\n"
      "v_mfma_f32_16x16x32_bf16 v[220:223], v[64:67], v[16:19], v[220:223]\nds_read_b128 v[8:11], v96 offset:45056\nv_mfma_f32_16x16x32_bf16 v[220:223], v[68:71], v[20:23], v[220:223]\n"
      "ds_read_b128 v[12:15], v97 offset:45056\nv_mfma_f32_16x16x32_bf16 v[224:227], v[76:79], v[20:23], v[224:227]\nds_read_b64_tr_b16 v[62:63], v107 offset:54272\n"
      "v_mfma_f32_16x16x32_bf16 v[224:227], v[72:75], v[16:19], v[224:227]\nv_mfma_f32_16x16x32_bf16 v[228:231], v[80:83], v[16:19], v[228:231]\n"
      "v_mfma_f32_16x16x32_bf16 v[228:231], v[84:87], v[20:23], v[228:231]\nv_mfma_f32_16x16x32_bf16 v[232:235], v[92:95], v[20:23], v[232:235]\ns_waitcnt lgkmcnt(0)\n"
      "v_mfma_f32_16x16x32_bf16 v[232:235], v[88:91], v[16:19], v[232:235]\nds_read_b128 v[16:19], v96 offset:47104\nds_read_b128 v[20:23], v97 offset:47104\n"
      "v_mfma_f32_16x16x32_bf16 v[124:127], v[32:35], v[0:3], v[124:127]\nds_read_b128 v[24:27], v96 offset:49152\nv_mfma_f32_16x16x32_bf16 v[124:127], v[36:39], v[4:7], v[124:127]\n"
      "ds_read_b128 v[28:31], v97 offset:49152\nv_mfma_f32_16x16x32_bf16 v[128:131], v[44:47], v[4:7], v[128:131]\nv_mfma_f32_16x16x32_bf16 v[128:131], v[40:43], v[0:3], v[128:131]\n"
      "v_mfma_f32_16x16x32_bf16 v[132:135], v[48:51], v[0:3], v[132:135]\nv_mfma_f32_16x16x32_bf16 v[132:135], v[52:55], v[4:7], v[132:135]\n"
      "v_mfma_f32_16x16x32_bf16 v[136:139], v[60:63], v[4:7], v[136:139]\nv_mfma_f32_16x16x32_bf16 v[136:139], v[56:59], v[0:3], v[136:139]\n"
      "v_mfma_f32_16x16x32_bf16 v[152:155], v[56:59], v[8:11], v[152:155]\nds_read_b128 v[0:3], v96 offset:51200\nv_mfma_f32_16x16x32_bf16 v[152:155], v[60:63], v[12:15], v[152:155]\n"
      "ds_read_b128 v[4:7], v97 offset:51200\nv_mfma_f32_16x16x32_bf16 v[148:151], v[52:55], v[12:15], v[148:151]\nv_mfma_f32_16x16x32_bf16 v[148:151], v[48:51], v[8:11], v[148:151]\n"
      "v_mfma_f32_16x16x32_bf16 v[144:147], v[40:43], v[8:11], v[144:147]\nv_mfma_f32_16x16x32_bf16 v[144:147], v[44:47], v[12:15], v[144:147]\n"
      "v_mfma_f32_16x16x32_bf16 v[140:143], v[36:39], v[12:15], v[140:143]\ns_waitcnt lgkmcnt(4)\nv_mfma_f32_16x16x32_bf16 v[140:143], v[32:35], v[8:11], v[140:143]\n"
      "v_mfma_f32_16x16x32_bf16 v[156:159], v[32:35], v[16:19], v[156:159]\nds_read_b128 v[8:11], v96 offset:53248\nv_mfma_f32_16x16x32_bf16 v[156:159], v[36:39], v[20:23], v[156:159]\n"
      "ds_read_b128 v[12:15], v97 offset:53248\nv_mfma_f32_16x16x32_bf16 v[160:163], v[44:47], v[20:23], v[160:163]\nv_mfma_f32_16x16x32_bf16 v[160:163], v[40:43], v[16:19], v[160:163]\n"
      "v_mfma_f32_16x16x32_bf16 v[164:167], v[48:51], v[16:19], v[164:167]\nv_mfma_f32_16x16x32_bf16 v[164:167], v[52:55], v[20:23], v[164:167]\n"
      "v_mfma_f32_16x16x32_bf16 v[168:171], v[60:63], v[20:23], v[168:171]\ns_waitcnt lgkmcnt(2)\nv_mfma_f32_16x16x32_bf16 v[168:171], v[56:59], v[16:19], v[168:171]\n"
      "v_mfma_f32_16x16x32_bf16 v[184:187], v[56:59], v[24:27], v[184:187]\nds_read_b128 v[16:19], v96 offset:55296\nv_mfma_f32_16x16x32_bf16 v[184:187], v[60:63], v[28:31], v[184:187]\n"
      "ds_read_b128 v[20:23], v97 offset:55296\nv_mfma_f32_16x16x32_bf16 v[180:183], v[52:55], v[28:31], v[180:183]\nv_mfma_f32_16x16x32_bf16 v[180:183], v[48:51], v[24:27], v[180:183]\n"
      "v_mfma_f32_16x16x32_bf16 v[176:179], v[40:43], v[24:27], v[176:179]\nv_mfma_f32_16x16x32_bf16 v[176:179], v[44:47], v[28:31], v[176:179]\n"
      "v_mfma_f32_16x16x32_bf16 v[172:175], v[36:39], v[28:31], v[172:175]\ns_waitcnt vmcnt(0) lgkmcnt(0)\ns_barrier\nv_mfma_f32_16x16x32_bf16 v[172:175], v[32:35], v[24:27], v[172:175]\n"
      "ds_read_b64_tr_b16 v[64:65], v108 offset:0\nv_mfma_f32_16x16x32_bf16 v[188:191], v[32:35], v[0:3], v[188:191]\nds_read_b64_tr_b16 v[66:67], v108 offset:1024\n"
      "v_mfma_f32_16x16x32_bf16 v[188:191], v[36:39], v[4:7], v[188:191]\nds_read_b64_tr_b16 v[68:69], v108 offset:8192\nv_mfma_f32_16x16x32_bf16 v[192:195], v[44:47], v[4:7], v[192:195]\n"
      "ds_read_b64_tr_b16 v[70:71], v108 offset:9216\nv_mfma_f32_16x16x32_bf16 v[192:195], v[40:43], v[0:3], v[192:195]\nds_read_b64_tr_b16 v[72:73], v109 offset:0\n"
      "v_mfma_f32_16x16x32_bf16 v[196:199], v[48:51], v[0:3], v[196:199]\nds_read_b64_tr_b16 v[74:75], v109 offset:1024\nv_mfma_f32_16x16x32_bf16 v[196:199], v[52:55], v[4:7], v[196:199]\n"
      "ds_read_b64_tr_b16 v[76:77], v109 offset:8192\nv_mfma_f32_16x16x32_bf16 v[200:203], v[60:63], v[4:7], v[200:203]\nds_read_b64_tr_b16 v[78:79], v109 offset:9216\n"
      "v_mfma_f32_16x16x32_bf16 v[200:203], v[56:59], v[0:3], v[200:203]\nds_read_b64_tr_b16 v[80:81], v110 offset:0\nv_mfma_f32_16x16x32_bf16 v[216:219], v[56:59], v[8:11], v[216:219]\n"
      "ds_read_b128 v[0:3], v102 offset:0\nv_mfma_f32_16x16x32_bf16 v[216:219], v[60:63], v[12:15], v[216:219]\nds_read_b128 v[4:7], v103 offset:0\n"
      "v_mfma_f32_16x16x32_bf16 v[212:215], v[52:55], v[12:15], v[212:215]\nds_read_b64_tr_b16 v[82:83], v110 offset:1024\nv_mfma_f32_16x16x32_bf16 v[212:215], v[48:51], v[8:11], v[212:215]\n"
      "ds_read_b64_tr_b16 v[84:85], v110 offset:8192\nv_mfma_f32_16x16x32_bf16 v[208:211], v[40:43], v[8:11], v[208:211]\nds_read_b64_tr_b16 v[86:87], v110 offset:9216\n"
      "v_mfma_f32_16x16x32_bf16 v[208:211], v[44:47], v[12:15], v[208:211]\nds_read_b64_tr_b16 v[88:89], v111 offset:0\nv_mfma_f32_16x16x32_bf16 v[204:207], v[36:39], v[12:15], v[204:207]\n"
      "ds_read_b64_tr_b16 v[90:91], v111 offset:1024\nv_mfma_f32_16x16x32_bf16 v[204:207], v[32:35], v[8:11], v[204:207]\nds_read_b64_tr_b16 v[92:93], v111 offset:8192\n"
      "v_mfma_f32_16x16x32_bf16 v[220:223], v[32:35], v[16:19], v[220:223]\nds_read_b128 v[8:11], v98 offset:0\nv_mfma_f32_16x16x32_bf16 v[220:223], v[36:39], v[20:23], v[220:223]\n"
      "ds_read_b128 v[12:15], v99 offset:0\nv_mfma_f32_16x16x32_bf16 v[224:227], v[44:47], v[20:23], v[224:227]\nds_read_b64_tr_b16 v[94:95], v111 offset:9216\n"
      "v_mfma_f32_16x16x32_bf16 v[224:227], v[40:43], v[16:19], v[224:227]\nv_mfma_f32_16x16x32_bf16 v[228:231], v[48:51], v[16:19], v[228:231]\ns_sub_u32 s58, s58, 1\ns_cmp_lg_u32 s58, 0\n"
      "v_mfma_f32_16x16x32_bf16 v[228:231], v[52:55], v[20:23], v[228:231]\nv_mfma_f32_16x16x32_bf16 v[232:235], v[60:63], v[20:23], v[232:235]\ns_waitcnt lgkmcnt(0)\n"
      "v_mfma_f32_16x16x32_bf16 v[232:235], v[56:59], v[16:19], v[232:235]\nds_read_b128 v[16:19], v98 offset:2048\nds_read_b128 v[20:23], v99 offset:2048\ns_cbranch_scc1 .Lgl14\n"
      "v_mfma_f32_16x16x32_bf16 v[124:127], v[64:67], v[0:3], v[124:127]\nds_read_b128 v[24:27], v98 offset:4096\nv_cmp_eq_u32 s[62:63], 0, v240\n"
      "v_mfma_f32_16x16x32_bf16 v[124:127], v[68:71], v[4:7], v[124:127]\nds_read_b128 v[28:31], v99 offset:4096\nv_mov_b32 v248, 262144\n"
      "v_mfma_f32_16x16x32_bf16 v[128:131], v[76:79], v[4:7], v[128:131]\nv_mov_b32 v247, 162176\nv_mov_b32 v241, 1\nv_mfma_f32_16x16x32_bf16 v[128:131], v[72:75], v[0:3], v[128:131]\n"
      "v_cndmask_b32 v253, v248, v247, s[62:63]\ns_lshl_b32 s42, s36, 2\nv_mfma_f32_16x16x32_bf16 v[132:135], v[80:83], v[0:3], v[132:135]\nv_add_u32 v245, s42, v253\nv_lshrrev_b32 v112, 4, v240\n"
      "v_mfma_f32_16x16x32_bf16 v[132:135], v[84:87], v[4:7], v[132:135]\nv_bfe_u32 v113, v240, 2, 2\nv_and_b32 v114, 3, v240\nv_mfma_f32_16x16x32_bf16 v[136:139], v[92:95], v[4:7], v[136:139]\n"
      "v_mul_u32_u24 v114, 0x80008, v114\nv_add_u32 v242, v112, v113\nv_mfma_f32_16x16x32_bf16 v[136:139], v[88:91], v[0:3], v[136:139]\nv_and_b32 v242, 3, v242\nv_add_u32 v116, 22, v113\n"
      "v_mfma_f32_16x16x32_bf16 v[152:155], v[88:91], v[8:11], v[152:155]\nds_read_b128 v[0:3], v98 offset:6144\nv_cmp_gt_u32 s[62:63], 2, v113\n"
      "v_mfma_f32_16x16x32_bf16 v[152:155], v[92:95], v[12:15], v[152:155]\nds_read_b128 v[4:7], v99 offset:6144\nv_and_b32 v113, 60, v240\n"
      "v_mfma_f32_16x16x32_bf16 v[148:151], v[84:87], v[12:15], v[148:151]\ns_mov_b32 s42, 45056\nv_cndmask_b32 v116, v116, v242, s[62:63]\n"
      "v_mfma_f32_16x16x32_bf16 v[148:151], v[80:83], v[8:11], v[148:151]\nv_lshl_or_b32 v242, v242, 16, v116\nv_lshl_add_u32 v242, v242, 5, v114\n"
      "v_mfma_f32_16x16x32_bf16 v[144:147], v[72:75], v[8:11], v[144:147]\nv_lshl_add_u32 v252, v240, 4, s42\ns_mov_b32 s42, 0x76325410\n"
      "v_mfma_f32_16x16x32_bf16 v[144:147], v[76:79], v[12:15], v[144:147]\ns_mov_b32 s43, 0x21005410\nv_lshrrev_b64 v[116:117], v113, s[42:43]\n"
      "v_mfma_f32_16x16x32_bf16 v[140:143], v[68:71], v[12:15], v[140:143]\ns_waitcnt lgkmcnt(4)\nv_and_b32 v116, 7, v116\nv_mfma_f32_16x16x32_bf16 v[140:143], v[64:67], v[8:11], v[140:143]\n"
      "v_lshl_add_u32 v116, v112, 3, v116\ns_mov_b32 s42, 0x54107632\nv_mfma_f32_16x16x32_bf16 v[156:159], v[64:67], v[16:19], v[156:159]\nds_read_b128 v[8:11], v98 offset:8192\n"
      "s_mov_b32 s43, 0x21107632\nv_mfma_f32_16x16x32_bf16 v[156:159], v[68:71], v[20:23], v[156:159]\nds_read_b128 v[12:15], v99 offset:8192\nv_lshrrev_b64 v[118:119], v113, s[42:43]\n"
      "v_mfma_f32_16x16x32_bf16 v[160:163], v[76:79], v[20:23], v[160:163]\nv_and_b32 v118, 7, v118\nv_lshl_add_u32 v118, v112, 3, v118\n"
      "v_mfma_f32_16x16x32_bf16 v[160:163], v[72:75], v[16:19], v[160:163]\nv_lshl_or_b32 v243, v118, 16, v116\nv_lshl_add_u32 v243, v243, 5, v114\n"
      "v_mfma_f32_16x16x32_bf16 v[164:167], v[80:83], v[16:19], v[164:167]\nv_and_b32 v113, 1, v240\nv_mul_u32_u24 v113, 0xaaaa, v113\n"
      "v_mfma_f32_16x16x32_bf16 v[164:167], v[84:87], v[20:23], v[164:167]\nv_add_u32 v244, 0x4444, v113\ns_lshl_b32 s42, s36, 4\n"
      "v_mfma_f32_16x16x32_bf16 v[168:171], v[92:95], v[20:23], v[168:171]\ns_waitcnt lgkmcnt(2)\ns_add_u32 s42, s42, 160896\nv_mfma_f32_16x16x32_bf16 v[168:171], v[88:91], v[16:19], v[168:171]\n"
      "v_lshl_add_u32 v246, v112, 2, s42\nv_and_b32 v112, 1, v112\nv_mfma_f32_16x16x32_bf16 v[184:187], v[88:91], v[24:27], v[184:187]\nds_read_b128 v[16:19], v98 offset:10240\n"
      "v_lshl_add_u32 v112, v112, 3, s24\nv_mfma_f32_16x16x32_bf16 v[184:187], v[92:95], v[28:31], v[184:187]\nds_read_b128 v[20:23], v99 offset:10240\ns_sub_u32 s42, s64, 1\n"
      "v_mfma_f32_16x16x32_bf16 v[180:183], v[84:87], v[28:31], v[180:183]\nv_min_u32 v112, s42, v112\nv_lshlrev_b32 v112, 3, v112\n"
      "v_mfma_f32_16x16x32_bf16 v[180:183], v[80:83], v[24:27], v[180:183]\nv_add_u32 v250, 160640, v112\nv_and_b32 v113, 15, v240\n"
      "v_mfma_f32_16x16x32_bf16 v[176:179], v[72:75], v[24:27], v[176:179]\nv_min_u32 v113, 12, v113\ns_lshl_b32 s42, s30, 7\nv_mfma_f32_16x16x32_bf16 v[176:179], v[76:79], v[28:31], v[176:179]\n"
      "v_lshl_add_u32 v113, v113, 15, s42\nv_add_u32 v249, v113, v112\nv_mfma_f32_16x16x32_bf16 v[172:175], v[68:71], v[28:31], v[172:175]\ns_waitcnt vmcnt(0) lgkmcnt(0)\ns_barrier\n"
      "v_mfma_f32_16x16x32_bf16 v[172:175], v[64:67], v[24:27], v[172:175]\nds_read_b64_tr_b16 v[32:33], v104 offset:0\ns_mul_i32 s65, s36, 17632\n"
      "v_mfma_f32_16x16x32_bf16 v[188:191], v[64:67], v[0:3], v[188:191]\nds_read_b64_tr_b16 v[34:35], v104 offset:1024\ns_add_u32 s65, s65, 90112\n"
      "v_mfma_f32_16x16x32_bf16 v[188:191], v[68:71], v[4:7], v[188:191]\nds_read_b64_tr_b16 v[36:37], v104 offset:8192\ns_add_u32 s68, s64, 6\n"
      "v_mfma_f32_16x16x32_bf16 v[192:195], v[76:79], v[4:7], v[192:195]\nds_read_b64_tr_b16 v[38:39], v104 offset:9216\ns_min_u32 s68, s68, 19\n"
      "v_mfma_f32_16x16x32_bf16 v[192:195], v[72:75], v[0:3], v[192:195]\nds_read_b64_tr_b16 v[40:41], v105 offset:0\ns_lshl_b32 s38, s38, 16\n"
      "v_mfma_f32_16x16x32_bf16 v[196:199], v[80:83], v[0:3], v[196:199]\nds_read_b64_tr_b16 v[42:43], v105 offset:1024\ns_lshr_b32 s42, s24, 2\n"
      "v_mfma_f32_16x16x32_bf16 v[196:199], v[84:87], v[4:7], v[196:199]\nds_read_b64_tr_b16 v[44:45], v105 offset:8192\ns_mul_i32 s43, s42, 3\n"
      "v_mfma_f32_16x16x32_bf16 v[200:203], v[92:95], v[4:7], v[200:203]\nds_read_b64_tr_b16 v[46:47], v105 offset:9216\ns_xor_b32 s43, s43, s36\n"
      "v_mfma_f32_16x16x32_bf16 v[200:203], v[88:91], v[0:3], v[200:203]\nds_read_b64_tr_b16 v[48:49], v106 offset:0\ns_add_u32 s42, s42, s64\n"
      "v_mfma_f32_16x16x32_bf16 v[216:219], v[88:91], v[8:11], v[216:219]\nds_read_b128 v[0:3], v100 offset:0\ns_add_u32 s42, s42, 7\n"
      "v_mfma_f32_16x16x32_bf16 v[216:219], v[92:95], v[12:15], v[216:219]\nds_read_b128 v[4:7], v101 offset:0\ns_and_b32 s42, s42, 1\n"
      "v_mfma_f32_16x16x32_bf16 v[212:215], v[84:87], v[12:15], v[212:215]\nds_read_b64_tr_b16 v[50:51], v106 offset:1024\ns_lshl1_add_u32 s67, s43, s42\n"
      "v_mfma_f32_16x16x32_bf16 v[212:215], v[80:83], v[8:11], v[212:215]\nds_read_b64_tr_b16 v[52:53], v106 offset:8192\ns_add_u32 s66, s64, s67\n"
      "v_mfma_f32_16x16x32_bf16 v[208:211], v[72:75], v[8:11], v[208:211]\nds_read_b64_tr_b16 v[54:55], v106 offset:9216\ns_cmp_lt_u32 s66, s68\n"
      "v_mfma_f32_16x16x32_bf16 v[208:211], v[76:79], v[12:15], v[208:211]\nds_read_b64_tr_b16 v[56:57], v107 offset:0\ns_cselect_b32 s66, s66, 127\n"
      "v_mfma_f32_16x16x32_bf16 v[204:207], v[68:71], v[12:15], v[204:207]\nds_read_b64_tr_b16 v[58:59], v107 offset:1024\ns_sub_u32 s66, s66, 7\n"
      "v_mfma_f32_16x16x32_bf16 v[204:207], v[64:67], v[8:11], v[204:207]\nds_read_b64_tr_b16 v[60:61], v107 offset:8192\ns_getpc_b64 s[4:5]\n"
      ".Lk1062:\nv_mfma_f32_16x16x32_bf16 v[220:223], v[64:67], v[16:19], v[220:223]\nds_read_b128 v[8:11], v96 offset:0\ns_add_u32 s34, s4, .Lt_g-.Lk1062\n"
      "v_mfma_f32_16x16x32_bf16 v[220:223], v[68:71], v[20:23], v[220:223]\nds_read_b128 v[12:15], v97 offset:0\ns_addc_u32 s35, s5, 0\n"
      "v_mfma_f32_16x16x32_bf16 v[224:227], v[76:79], v[20:23], v[224:227]\nds_read_b64_tr_b16 v[62:63], v107 offset:9216\ns_add_u32 s4, s4, .Lc_back-.Lk1062\n"
      "v_mfma_f32_16x16x32_bf16 v[224:227], v[72:75], v[16:19], v[224:227]\ns_addc_u32 s5, s5, 0\ns_lshr_b32 s42, %[blk], 3\nv_mfma_f32_16x16x32_bf16 v[228:231], v[80:83], v[16:19], v[228:231]\n"
      "s_lshl1_add_u32 s42, %[blk], s42\ns_and_b32 s69, s42, 15\nv_mfma_f32_16x16x32_bf16 v[228:231], v[84:87], v[20:23], v[228:231]\ns_lshl_b32 s43, s69, 7\ns_add_u32 s43, s43, 32768\n"
      "v_mfma_f32_16x16x32_bf16 v[232:235], v[92:95], v[20:23], v[232:235]\ns_waitcnt lgkmcnt(0)\ns_add_u32 s8, s20, s43\nv_mfma_f32_16x16x32_bf16 v[232:235], v[88:91], v[16:19], v[232:235]\n"
      "ds_read_b128 v[16:19], v96 offset:2048\nds_read_b128 v[20:23], v97 offset:2048\nv_mfma_f32_16x16x32_bf16 v[124:127], v[32:35], v[0:3], v[124:127]\nds_read_b128 v[24:27], v96 offset:4096\n"
      "s_addc_u32 s9, s21, 0\nv_mfma_f32_16x16x32_bf16 v[124:127], v[36:39], v[4:7], v[124:127]\nds_read_b128 v[28:31], v97 offset:4096\ns_add_u32 s12, s20, 65536\n"
      "v_mfma_f32_16x16x32_bf16 v[128:131], v[44:47], v[4:7], v[128:131]\ns_addc_u32 s13, s21, 0\ns_add_u32 s16, s20, 2052096\nv_mfma_f32_16x16x32_bf16 v[128:131], v[40:43], v[0:3], v[128:131]\n"
      "s_addc_u32 s17, s21, 0\ns_lshl_b32 s42, s69, 4\nv_mfma_f32_16x16x32_bf16 v[132:135], v[48:51], v[0:3], v[132:135]\ns_max_u32 s42, s42, 24\ns_sub_u32 s42, s42, 24\n"
      "v_mfma_f32_16x16x32_bf16 v[132:135], v[52:55], v[4:7], v[132:135]\ns_min_u32 s69, s42, 192\ns_lshl_b32 s42, s69, 3\nv_mfma_f32_16x16x32_bf16 v[136:139], v[60:63], v[4:7], v[136:139]\n"
      "s_add_u32 s42, s42, 24576\ns_add_u32 s14, s20, s42\nv_mfma_f32_16x16x32_bf16 v[136:139], v[56:59], v[0:3], v[136:139]\ns_addc_u32 s15, s21, 0\n"
      "v_mfma_f32_16x16x32_bf16 v[152:155], v[56:59], v[8:11], v[152:155]\nds_read_b128 v[0:3], v96 offset:6144\nv_mfma_f32_16x16x32_bf16 v[152:155], v[60:63], v[12:15], v[152:155]\n"
      "ds_read_b128 v[4:7], v97 offset:6144\nv_mfma_f32_16x16x32_bf16 v[148:151], v[52:55], v[12:15], v[148:151]\nv_mfma_f32_16x16x32_bf16 v[148:151], v[48:51], v[8:11], v[148:151]\n"
      "v_mfma_f32_16x16x32_bf16 v[144:147], v[40:43], v[8:11], v[144:147]\nv_mfma_f32_16x16x32_bf16 v[144:147], v[44:47], v[12:15], v[144:147]\n"
      "v_mfma_f32_16x16x32_bf16 v[140:143], v[36:39], v[12:15], v[140:143]\ns_waitcnt lgkmcnt(4)\nv_mfma_f32_16x16x32_bf16 v[140:143], v[32:35], v[8:11], v[140:143]\n"
      "v_mfma_f32_16x16x32_bf16 v[156:159], v[32:35], v[16:19], v[156:159]\nds_read_b128 v[8:11], v96 offset:8192\nv_mfma_f32_16x16x32_bf16 v[156:159], v[36:39], v[20:23], v[156:159]\n"
      "ds_read_b128 v[12:15], v97 offset:8192\nv_mfma_f32_16x16x32_bf16 v[160:163], v[44:47], v[20:23], v[160:163]\nv_mfma_f32_16x16x32_bf16 v[160:163], v[40:43], v[16:19], v[160:163]\n"
      "v_mfma_f32_16x16x32_bf16 v[164:167], v[48:51], v[16:19], v[164:167]\nv_mfma_f32_16x16x32_bf16 v[164:167], v[52:55], v[20:23], v[164:167]\n"
      "v_mfma_f32_16x16x32_bf16 v[168:171], v[60:63], v[20:23], v[168:171]\ns_waitcnt lgkmcnt(2)\nv_mfma_f32_16x16x32_bf16 v[168:171], v[56:59], v[16:19], v[168:171]\n"
      "v_mfma_f32_16x16x32_bf16 v[184:187], v[56:59], v[24:27], v[184:187]\nds_read_b128 v[16:19], v96 offset:10240\nv_mfma_f32_16x16x32_bf16 v[184:187], v[60:63], v[28:31], v[184:187]\n"
      "ds_read_b128 v[20:23], v97 offset:10240\nv_mfma_f32_16x16x32_bf16 v[180:183], v[52:55], v[28:31], v[180:183]\nv_mfma_f32_16x16x32_bf16 v[180:183], v[48:51], v[24:27], v[180:183]\n"
      "v_mfma_f32_16x16x32_bf16 v[176:179], v[40:43], v[24:27], v[176:179]\nv_mfma_f32_16x16x32_bf16 v[176:179], v[44:47], v[28:31], v[176:179]\n"
      "v_mfma_f32_16x16x32_bf16 v[172:175], v[36:39], v[28:31], v[172:175]\ns_waitcnt vmcnt(0) lgkmcnt(0)\ns_barrier\nv_mfma_f32_16x16x32_bf16 v[172:175], v[32:35], v[24:27], v[172:175]\n"
      "ds_read_b128 v[72:75], v252 offset:0\nv_lshl_add_u32 v65, v240, 1, v247\nv_mfma_f32_16x16x32_bf16 v[188:191], v[32:35], v[0:3], v[188:191]\nds_read_b128 v[76:79], v252 offset:1024\n"
      "ds_read_u16 v251, v65 offset:64\nv_mfma_f32_16x16x32_bf16 v[188:191], v[36:39], v[4:7], v[188:191]\nds_read_b128 v[80:83], v252 offset:2048\nds_read_b32 v66, v247 offset:24\n"
      "v_mfma_f32_16x16x32_bf16 v[192:195], v[44:47], v[4:7], v[192:195]\nds_read_b128 v[84:87], v252 offset:3072\nv_and_b32 v64, 48, v240\n"
      "v_mfma_f32_16x16x32_bf16 v[192:195], v[40:43], v[0:3], v[192:195]\nds_read_b128 v[88:91], v252 offset:4096\ns_mov_b32 s42, 87296\n"
      "v_mfma_f32_16x16x32_bf16 v[196:199], v[48:51], v[0:3], v[196:199]\nds_read_b128 v[92:95], v252 offset:5120\nv_lshl_add_u32 v64, v64, 2, s42\n"
      "v_mfma_f32_16x16x32_bf16 v[196:199], v[52:55], v[4:7], v[196:199]\nds_read_b128 v[96:99], v252 offset:6144\ns_mov_b32 s58, 0xbdd2d3e7\n"
      "v_mfma_f32_16x16x32_bf16 v[200:203], v[60:63], v[4:7], v[200:203]\nds_read_b128 v[100:103], v252 offset:7168\ns_mov_b32 s59, 0xc0135761\n"
      "v_mfma_f32_16x16x32_bf16 v[200:203], v[56:59], v[0:3], v[200:203]\nds_read_b128 v[104:107], v252 offset:8192\ns_mov_b32 s60, 0xc1f00000\n"
      "v_mfma_f32_16x16x32_bf16 v[216:219], v[56:59], v[8:11], v[216:219]\nds_read_b128 v[0:3], v236 offset:0\ns_mov_b32 s61, 0x30800000\n"
      "v_mfma_f32_16x16x32_bf16 v[216:219], v[60:63], v[12:15], v[216:219]\nds_read_b128 v[4:7], v236 offset:64\ns_add_u32 s49, s65, 928\n"
      "v_mfma_f32_16x16x32_bf16 v[212:215], v[52:55], v[12:15], v[212:215]\nds_read_b128 v[108:111], v252 offset:9216\ns_mov_b32 s45, 1\n"
      "v_mfma_f32_16x16x32_bf16 v[212:215], v[48:51], v[8:11], v[212:215]\nds_read_b128 v[112:115], v252 offset:10240\ns_mov_b32 s46, 0\n"
      "v_mfma_f32_16x16x32_bf16 v[208:211], v[40:43], v[8:11], v[208:211]\nds_read_b128 v[116:119], v252 offset:11264\ns_waitcnt lgkmcnt(8)\n"
      "v_mfma_f32_16x16x32_bf16 v[208:211], v[44:47], v[12:15], v[208:211]\nds_read_b128 v[120:123], v252 offset:12288\nv_readfirstlane_b32 s39, v66\n"
      "v_mfma_f32_16x16x32_bf16 v[204:207], v[36:39], v[12:15], v[204:207]\nv_readlane_b32 s55, v251, 0\nv_mfma_f32_16x16x32_bf16 v[204:207], v[32:35], v[8:11], v[204:207]\ns_setprio 3\n"
      "v_mfma_f32_16x16x32_bf16 v[220:223], v[32:35], v[16:19], v[220:223]\nds_read_b128 v[8:11], v237 offset:0\ns_xor_b32 s40, s39, 0xa5c30f96\n"
      "v_mfma_f32_16x16x32_bf16 v[220:223], v[36:39], v[20:23], v[220:223]\nds_read_b128 v[12:15], v237 offset:64\ns_mul_i32 s41, s39, 0x9E3779B1\n"
      "v_mfma_f32_16x16x32_bf16 v[224:227], v[44:47], v[20:23], v[224:227]\ns_lshr_b32 s41, s41, 24\nv_mfma_f32_16x16x32_bf16 v[224:227], v[40:43], v[16:19], v[224:227]\n"
      "v_add_u32_sdwa v239, s49, v243 dst_sel:DWORD dst_unused:UNUSED_PAD src0_sel:DWORD src1_sel:WORD_1\nv_mfma_f32_16x16x32_bf16 v[228:231], v[48:51], v[16:19], v[228:231]\n"
      "v_add_u32_sdwa v236, s49, v242 dst_sel:DWORD dst_unused:UNUSED_PAD src0_sel:DWORD src1_sel:WORD_0\nv_mfma_f32_16x16x32_bf16 v[228:231], v[52:55], v[20:23], v[228:231]\n"
      "v_add_u32_sdwa v237, s49, v242 dst_sel:DWORD dst_unused:UNUSED_PAD src0_sel:DWORD src1_sel:WORD_1\nv_mfma_f32_16x16x32_bf16 v[232:235], v[60:63], v[20:23], v[232:235]\n"
      "v_add_u32_sdwa v238, s49, v243 dst_sel:DWORD dst_unused:UNUSED_PAD src0_sel:DWORD src1_sel:WORD_0\ns_waitcnt lgkmcnt(0)\nv_mfma_f32_16x16x32_bf16 v[232:235], v[56:59], v[16:19], v[232:235]\n"
      "v_pk_mul_f32 v[16:17], v[124:125], v[124:125]\nv_pk_mul_f32 v[18:19], v[126:127], v[126:127]\nv_pk_mul_f32 v[20:21], v[128:129], v[128:129]\nv_pk_mul_f32 v[22:23], v[130:131], v[130:131]\n"
      "v_pk_fma_f32 v[16:17], v[16:17], s[58:59], s[58:59] op_sel:[0,0,1] op_sel_hi:[1,0,1]\nv_pk_fma_f32 v[18:19], v[18:19], s[58:59], s[58:59] op_sel:[0,0,1] op_sel_hi:[1,0,1]\n"
      "v_pk_fma_f32 v[20:21], v[20:21], s[58:59], s[58:59] op_sel:[0,0,1] op_sel_hi:[1,0,1]\nv_pk_fma_f32 v[22:23], v[22:23], s[58:59], s[58:59] op_sel:[0,0,1] op_sel_hi:[1,0,1]\n"
      "v_pk_fma_f32 v[16:17], v[16:17], v[124:125], s[60:61] op_sel_hi:[1,1,0]\nv_pk_fma_f32 v[18:19], v[18:19], v[126:127], s[60:61] op_sel_hi:[1,1,0]\n"
      "v_pk_fma_f32 v[20:21], v[20:21], v[128:129], s[60:61] op_sel_hi:[1,1,0]\nv_pk_fma_f32 v[22:23], v[22:23], v[130:131], s[60:61] op_sel_hi:[1,1,0]\n"
      "v_pk_mul_f32 v[124:125], v[124:125], v[0:1]\nv_pk_mul_f32 v[126:127], v[126:127], v[2:3]\nv_pk_mul_f32 v[128:129], v[128:129], v[4:5]\nv_pk_mul_f32 v[130:131], v[130:131], v[6:7]\n"
      "v_exp_f32 v16, v16 clamp\nv_exp_f32 v17, v17 clamp\nv_exp_f32 v18, v18 clamp\nv_exp_f32 v19, v19 clamp\nv_exp_f32 v20, v20 clamp\nv_exp_f32 v21, v21 clamp\nv_exp_f32 v22, v22 clamp\n"
      "v_exp_f32 v23, v23 clamp\nv_pk_add_f32 v[16:17], v[16:17], s[60:61] op_sel:[0,1] op_sel_hi:[1,1]\nv_pk_add_f32 v[18:19], v[18:19], s[60:61] op_sel:[0,1] op_sel_hi:[1,1]\n"
      "v_pk_add_f32 v[20:21], v[20:21], s[60:61] op_sel:[0,1] op_sel_hi:[1,1]\nv_pk_add_f32 v[22:23], v[22:23], s[60:61] op_sel:[0,1] op_sel_hi:[1,1]\n"
      "v_pk_mul_f32 v[124:125], v[124:125], v[18:19]\nv_pk_mul_f32 v[128:129], v[128:129], v[22:23]\nv_pk_fma_f32 v[124:125], v[126:127], v[16:17], v[124:125]\n"
      "v_pk_fma_f32 v[128:129], v[130:131], v[20:21], v[128:129]\nv_pk_mul_f32 v[16:17], v[16:17], v[18:19]\nv_pk_mul_f32 v[20:21], v[20:21], v[22:23]\nv_pk_mul_f32 v[18:19], v[16:17], v[20:21]\n"
      "v_pk_mul_f32 v[24:25], v[132:133], v[132:133]\nv_pk_mul_f32 v[124:125], v[124:125], v[20:21]\nv_pk_mul_f32 v[26:27], v[134:135], v[134:135]\nv_rcp_f32 v18, v18\n"
      "v_pk_mul_f32 v[28:29], v[136:137], v[136:137]\nv_rcp_f32 v19, v19\nv_pk_mul_f32 v[30:31], v[138:139], v[138:139]\nv_pk_fma_f32 v[124:125], v[128:129], v[16:17], v[124:125]\n"
      "v_pk_fma_f32 v[24:25], v[24:25], s[58:59], s[58:59] op_sel:[0,0,1] op_sel_hi:[1,0,1]\nv_pk_mul_f32 v[32:33], v[124:125], v[18:19]\n"
      "v_pk_fma_f32 v[26:27], v[26:27], s[58:59], s[58:59] op_sel:[0,0,1] op_sel_hi:[1,0,1]\nv_pk_fma_f32 v[28:29], v[28:29], s[58:59], s[58:59] op_sel:[0,0,1] op_sel_hi:[1,0,1]\n"
      "v_pk_fma_f32 v[30:31], v[30:31], s[58:59], s[58:59] op_sel:[0,0,1] op_sel_hi:[1,0,1]\nv_pk_fma_f32 v[24:25], v[24:25], v[132:133], s[60:61] op_sel_hi:[1,1,0]\n"
      "ds_read_b128 v[124:127], v252 offset:13312\nv_pk_fma_f32 v[26:27], v[26:27], v[134:135], s[60:61] op_sel_hi:[1,1,0]\nds_read_b128 v[128:131], v252 offset:14336\n"
      "v_pk_fma_f32 v[28:29], v[28:29], v[136:137], s[60:61] op_sel_hi:[1,1,0]\nv_pk_fma_f32 v[30:31], v[30:31], v[138:139], s[60:61] op_sel_hi:[1,1,0]\n"
      "v_pk_mul_f32 v[132:133], v[132:133], v[8:9]\nv_pk_mul_f32 v[134:135], v[134:135], v[10:11]\nv_pk_mul_f32 v[136:137], v[136:137], v[12:13]\nv_pk_mul_f32 v[138:139], v[138:139], v[14:15]\n"
      "v_exp_f32 v24, v24 clamp\nv_exp_f32 v25, v25 clamp\nv_exp_f32 v26, v26 clamp\nv_exp_f32 v27, v27 clamp\nv_exp_f32 v28, v28 clamp\nv_exp_f32 v29, v29 clamp\nv_exp_f32 v30, v30 clamp\n"
      "v_exp_f32 v31, v31 clamp\nv_pk_add_f32 v[24:25], v[24:25], s[60:61] op_sel:[0,1] op_sel_hi:[1,1]\nv_pk_add_f32 v[26:27], v[26:27], s[60:61] op_sel:[0,1] op_sel_hi:[1,1]\n"
      "v_pk_add_f32 v[28:29], v[28:29], s[60:61] op_sel:[0,1] op_sel_hi:[1,1]\nv_pk_add_f32 v[30:31], v[30:31], s[60:61] op_sel:[0,1] op_sel_hi:[1,1]\n"
      "v_pk_mul_f32 v[132:133], v[132:133], v[26:27]\nv_pk_mul_f32 v[136:137], v[136:137], v[30:31]\nv_pk_fma_f32 v[132:133], v[134:135], v[24:25], v[132:133]\n"
      "v_pk_fma_f32 v[136:137], v[138:139], v[28:29], v[136:137]\nv_pk_mul_f32 v[24:25], v[24:25], v[26:27]\nv_pk_mul_f32 v[28:29], v[28:29], v[30:31]\nv_pk_mul_f32 v[26:27], v[24:25], v[28:29]\n"
      "v_pk_mul_f32 v[16:17], v[140:141], v[140:141]\nv_pk_mul_f32 v[132:133], v[132:133], v[28:29]\nv_pk_mul_f32 v[18:19], v[142:143], v[142:143]\nv_rcp_f32 v26, v26\n"
      "v_pk_mul_f32 v[20:21], v[144:145], v[144:145]\nv_rcp_f32 v27, v27\nv_pk_mul_f32 v[22:23], v[146:147], v[146:147]\nv_pk_fma_f32 v[132:133], v[136:137], v[24:25], v[132:133]\n"
      "v_pk_fma_f32 v[16:17], v[16:17], s[58:59], s[58:59] op_sel:[0,0,1] op_sel_hi:[1,0,1]\nv_pk_fma_f32 v[32:33], v[132:133], v[26:27], v[32:33]\n"
      "v_pk_fma_f32 v[18:19], v[18:19], s[58:59], s[58:59] op_sel:[0,0,1] op_sel_hi:[1,0,1]\nv_add_f32 v42, v32, v33\n"
      "v_pk_fma_f32 v[20:21], v[20:21], s[58:59], s[58:59] op_sel:[0,0,1] op_sel_hi:[1,0,1]\nv_pk_fma_f32 v[22:23], v[22:23], s[58:59], s[58:59] op_sel:[0,0,1] op_sel_hi:[1,0,1]\n"
      "v_pk_fma_f32 v[16:17], v[16:17], v[140:141], s[60:61] op_sel_hi:[1,1,0]\nds_read_b128 v[132:135], v252 offset:15360\nv_pk_fma_f32 v[18:19], v[18:19], v[142:143], s[60:61] op_sel_hi:[1,1,0]\n"
      "ds_read_b128 v[136:139], v252 offset:16384\nv_pk_fma_f32 v[20:21], v[20:21], v[144:145], s[60:61] op_sel_hi:[1,1,0]\nv_pk_fma_f32 v[22:23], v[22:23], v[146:147], s[60:61] op_sel_hi:[1,1,0]\n"
      "v_pk_mul_f32 v[140:141], v[140:141], v[0:1]\nv_pk_mul_f32 v[142:143], v[142:143], v[2:3]\nv_pk_mul_f32 v[144:145], v[144:145], v[4:5]\nv_pk_mul_f32 v[146:147], v[146:147], v[6:7]\n"
      "v_exp_f32 v16, v16 clamp\nv_exp_f32 v17, v17 clamp\nv_exp_f32 v18, v18 clamp\nv_exp_f32 v19, v19 clamp\nv_exp_f32 v20, v20 clamp\nv_exp_f32 v21, v21 clamp\nv_exp_f32 v22, v22 clamp\n"
      "v_exp_f32 v23, v23 clamp\nv_pk_add_f32 v[16:17], v[16:17], s[60:61] op_sel:[0,1] op_sel_hi:[1,1]\nv_pk_add_f32 v[18:19], v[18:19], s[60:61] op_sel:[0,1] op_sel_hi:[1,1]\n"
      "v_pk_add_f32 v[20:21], v[20:21], s[60:61] op_sel:[0,1] op_sel_hi:[1,1]\nv_pk_add_f32 v[22:23], v[22:23], s[60:61] op_sel:[0,1] op_sel_hi:[1,1]\n"
      "v_pk_mul_f32 v[140:141], v[140:141], v[18:19]\nv_pk_mul_f32 v[144:145], v[144:145], v[22:23]\nv_pk_fma_f32 v[140:141], v[142:143], v[16:17], v[140:141]\n"
      "v_pk_fma_f32 v[144:145], v[146:147], v[20:21], v[144:145]\nv_pk_mul_f32 v[16:17], v[16:17], v[18:19]\nv_pk_mul_f32 v[20:21], v[20:21], v[22:23]\nv_pk_mul_f32 v[18:19], v[16:17], v[20:21]\n"
      "v_pk_mul_f32 v[24:25], v[148:149], v[148:149]\nv_pk_mul_f32 v[140:141], v[140:141], v[20:21]\nv_pk_mul_f32 v[26:27], v[150:151], v[150:151]\nv_rcp_f32 v18, v18\n"
      "v_pk_mul_f32 v[28:29], v[152:153], v[152:153]\nv_rcp_f32 v19, v19\nv_pk_mul_f32 v[30:31], v[154:155], v[154:155]\nv_pk_fma_f32 v[140:141], v[144:145], v[16:17], v[140:141]\n"
      "v_pk_fma_f32 v[24:25], v[24:25], s[58:59], s[58:59] op_sel:[0,0,1] op_sel_hi:[1,0,1]\nv_pk_mul_f32 v[32:33], v[140:141], v[18:19]\n"
      "v_pk_fma_f32 v[26:27], v[26:27], s[58:59], s[58:59] op_sel:[0,0,1] op_sel_hi:[1,0,1]\nv_pk_fma_f32 v[28:29], v[28:29], s[58:59], s[58:59] op_sel:[0,0,1] op_sel_hi:[1,0,1]\n"
      "v_pk_fma_f32 v[30:31], v[30:31], s[58:59], s[58:59] op_sel:[0,0,1] op_sel_hi:[1,0,1]\nv_pk_fma_f32 v[24:25], v[24:25], v[148:149], s[60:61] op_sel_hi:[1,1,0]\n"
      "ds_read_b128 v[140:143], v252 offset:17408\nv_pk_fma_f32 v[26:27], v[26:27], v[150:151], s[60:61] op_sel_hi:[1,1,0]\nds_read_b128 v[144:147], v252 offset:18432\n"
      "v_pk_fma_f32 v[28:29], v[28:29], v[152:153], s[60:61] op_sel_hi:[1,1,0]\nv_pk_fma_f32 v[30:31], v[30:31], v[154:155], s[60:61] op_sel_hi:[1,1,0]\n"
      "v_pk_mul_f32 v[148:149], v[148:149], v[8:9]\nv_pk_mul_f32 v[150:151], v[150:151], v[10:11]\nv_pk_mul_f32 v[152:153], v[152:153], v[12:13]\nv_pk_mul_f32 v[154:155], v[154:155], v[14:15]\n"
      "v_exp_f32 v24, v24 clamp\nv_exp_f32 v25, v25 clamp\nv_exp_f32 v26, v26 clamp\nv_exp_f32 v27, v27 clamp\nv_exp_f32 v28, v28 clamp\nv_exp_f32 v29, v29 clamp\nv_exp_f32 v30, v30 clamp\n"
      "v_exp_f32 v31, v31 clamp\nv_pk_add_f32 v[24:25], v[24:25], s[60:61] op_sel:[0,1] op_sel_hi:[1,1]\nv_pk_add_f32 v[26:27], v[26:27], s[60:61] op_sel:[0,1] op_sel_hi:[1,1]\n"
      "v_pk_add_f32 v[28:29], v[28:29], s[60:61] op_sel:[0,1] op_sel_hi:[1,1]\nv_pk_add_f32 v[30:31], v[30:31], s[60:61] op_sel:[0,1] op_sel_hi:[1,1]\n"
      "v_pk_mul_f32 v[148:149], v[148:149], v[26:27]\nv_pk_mul_f32 v[152:153], v[152:153], v[30:31]\nv_pk_fma_f32 v[148:149], v[150:151], v[24:25], v[148:149]\n"
      "v_pk_fma_f32 v[152:153], v[154:155], v[28:29], v[152:153]\nv_pk_mul_f32 v[24:25], v[24:25], v[26:27]\nv_pk_mul_f32 v[28:29], v[28:29], v[30:31]\nv_pk_mul_f32 v[26:27], v[24:25], v[28:29]\n"
      "v_pk_mul_f32 v[16:17], v[156:157], v[156:157]\nv_pk_mul_f32 v[148:149], v[148:149], v[28:29]\nv_pk_mul_f32 v[18:19], v[158:159], v[158:159]\nv_rcp_f32 v26, v26\n"
      "v_pk_mul_f32 v[20:21], v[160:161], v[160:161]\nv_rcp_f32 v27, v27\nv_pk_mul_f32 v[22:23], v[162:163], v[162:163]\nv_pk_fma_f32 v[148:149], v[152:153], v[24:25], v[148:149]\n"
      "v_pk_fma_f32 v[16:17], v[16:17], s[58:59], s[58:59] op_sel:[0,0,1] op_sel_hi:[1,0,1]\nv_pk_fma_f32 v[32:33], v[148:149], v[26:27], v[32:33]\n"
      "v_pk_fma_f32 v[18:19], v[18:19], s[58:59], s[58:59] op_sel:[0,0,1] op_sel_hi:[1,0,1]\nv_add_f32 v36, v32, v33\n"
      "v_pk_fma_f32 v[20:21], v[20:21], s[58:59], s[58:59] op_sel:[0,0,1] op_sel_hi:[1,0,1]\nv_pk_fma_f32 v[22:23], v[22:23], s[58:59], s[58:59] op_sel:[0,0,1] op_sel_hi:[1,0,1]\n"
      "v_pk_fma_f32 v[16:17], v[16:17], v[156:157], s[60:61] op_sel_hi:[1,1,0]\nds_read_b128 v[148:151], v252 offset:19456\nv_pk_fma_f32 v[18:19], v[18:19], v[158:159], s[60:61] op_sel_hi:[1,1,0]\n"
      "ds_read_b128 v[152:155], v252 offset:20480\nv_pk_fma_f32 v[20:21], v[20:21], v[160:161], s[60:61] op_sel_hi:[1,1,0]\nv_pk_fma_f32 v[22:23], v[22:23], v[162:163], s[60:61] op_sel_hi:[1,1,0]\n"
      "v_pk_mul_f32 v[156:157], v[156:157], v[0:1]\nv_pk_mul_f32 v[158:159], v[158:159], v[2:3]\nv_pk_mul_f32 v[160:161], v[160:161], v[4:5]\nv_pk_mul_f32 v[162:163], v[162:163], v[6:7]\n"
      "v_exp_f32 v16, v16 clamp\nv_exp_f32 v17, v17 clamp\nv_exp_f32 v18, v18 clamp\nv_exp_f32 v19, v19 clamp\nv_exp_f32 v20, v20 clamp\nv_exp_f32 v21, v21 clamp\nv_exp_f32 v22, v22 clamp\n"
      "v_exp_f32 v23, v23 clamp\nv_pk_add_f32 v[16:17], v[16:17], s[60:61] op_sel:[0,1] op_sel_hi:[1,1]\nv_pk_add_f32 v[18:19], v[18:19], s[60:61] op_sel:[0,1] op_sel_hi:[1,1]\n"
      "v_pk_add_f32 v[20:21], v[20:21], s[60:61] op_sel:[0,1] op_sel_hi:[1,1]\nv_pk_add_f32 v[22:23], v[22:23], s[60:61] op_sel:[0,1] op_sel_hi:[1,1]\n"
      "v_pk_mul_f32 v[156:157], v[156:157], v[18:19]\nv_pk_mul_f32 v[160:161], v[160:161], v[22:23]\nv_pk_fma_f32 v[156:157], v[158:159], v[16:17], v[156:157]\n"
      "v_pk_fma_f32 v[160:161], v[162:163], v[20:21], v[160:161]\nv_pk_mul_f32 v[16:17], v[16:17], v[18:19]\nv_pk_mul_f32 v[20:21], v[20:21], v[22:23]\nv_pk_mul_f32 v[18:19], v[16:17], v[20:21]\n"
      "v_pk_mul_f32 v[24:25], v[164:165], v[164:165]\nv_pk_mul_f32 v[156:157], v[156:157], v[20:21]\nv_pk_mul_f32 v[26:27], v[166:167], v[166:167]\nv_rcp_f32 v18, v18\n"
      "v_pk_mul_f32 v[28:29], v[168:169], v[168:169]\nv_rcp_f32 v19, v19\nv_pk_mul_f32 v[30:31], v[170:171], v[170:171]\nv_pk_fma_f32 v[156:157], v[160:161], v[16:17], v[156:157]\n"
      "v_pk_fma_f32 v[24:25], v[24:25], s[58:59], s[58:59] op_sel:[0,0,1] op_sel_hi:[1,0,1]\nv_pk_mul_f32 v[32:33], v[156:157], v[18:19]\n"
      "v_pk_fma_f32 v[26:27], v[26:27], s[58:59], s[58:59] op_sel:[0,0,1] op_sel_hi:[1,0,1]\nv_pk_fma_f32 v[28:29], v[28:29], s[58:59], s[58:59] op_sel:[0,0,1] op_sel_hi:[1,0,1]\n"
      "v_pk_fma_f32 v[30:31], v[30:31], s[58:59], s[58:59] op_sel:[0,0,1] op_sel_hi:[1,0,1]\nv_pk_fma_f32 v[24:25], v[24:25], v[164:165], s[60:61] op_sel_hi:[1,1,0]\n"
      "ds_read_b128 v[156:159], v252 offset:21504\nv_pk_fma_f32 v[26:27], v[26:27], v[166:167], s[60:61] op_sel_hi:[1,1,0]\nds_read_b128 v[160:163], v252 offset:22528\n"
      "v_pk_fma_f32 v[28:29], v[28:29], v[168:169], s[60:61] op_sel_hi:[1,1,0]\nv_pk_fma_f32 v[30:31], v[30:31], v[170:171], s[60:61] op_sel_hi:[1,1,0]\n"
      "v_pk_mul_f32 v[164:165], v[164:165], v[8:9]\nv_pk_mul_f32 v[166:167], v[166:167], v[10:11]\nv_pk_mul_f32 v[168:169], v[168:169], v[12:13]\nv_pk_mul_f32 v[170:171], v[170:171], v[14:15]\n"
      "v_exp_f32 v24, v24 clamp\nv_exp_f32 v25, v25 clamp\nv_exp_f32 v26, v26 clamp\nv_exp_f32 v27, v27 clamp\nv_exp_f32 v28, v28 clamp\nv_exp_f32 v29, v29 clamp\nv_exp_f32 v30, v30 clamp\n"
      "v_exp_f32 v31, v31 clamp\nv_pk_add_f32 v[24:25], v[24:25], s[60:61] op_sel:[0,1] op_sel_hi:[1,1]\nv_pk_add_f32 v[26:27], v[26:27], s[60:61] op_sel:[0,1] op_sel_hi:[1,1]\n"
      "v_pk_add_f32 v[28:29], v[28:29], s[60:61] op_sel:[0,1] op_sel_hi:[1,1]\nv_pk_add_f32 v[30:31], v[30:31], s[60:61] op_sel:[0,1] op_sel_hi:[1,1]\n"
      "v_pk_mul_f32 v[164:165], v[164:165], v[26:27]\nv_pk_mul_f32 v[168:169], v[168:169], v[30:31]\nv_pk_fma_f32 v[164:165], v[166:167], v[24:25], v[164:165]\n"
      "v_pk_fma_f32 v[168:169], v[170:171], v[28:29], v[168:169]\nv_pk_mul_f32 v[24:25], v[24:25], v[26:27]\nv_pk_mul_f32 v[28:29], v[28:29], v[30:31]\nv_pk_mul_f32 v[26:27], v[24:25], v[28:29]\n"
      "v_pk_mul_f32 v[16:17], v[172:173], v[172:173]\nv_pk_mul_f32 v[164:165], v[164:165], v[28:29]\nv_pk_mul_f32 v[18:19], v[174:175], v[174:175]\nv_rcp_f32 v26, v26\n"
      "v_pk_mul_f32 v[20:21], v[176:177], v[176:177]\nv_rcp_f32 v27, v27\nv_pk_mul_f32 v[22:23], v[178:179], v[178:179]\nv_pk_fma_f32 v[164:165], v[168:169], v[24:25], v[164:165]\n"
      "v_pk_fma_f32 v[16:17], v[16:17], s[58:59], s[58:59] op_sel:[0,0,1] op_sel_hi:[1,0,1]\nv_pk_fma_f32 v[32:33], v[164:165], v[26:27], v[32:33]\n"
      "v_pk_fma_f32 v[18:19], v[18:19], s[58:59], s[58:59] op_sel:[0,0,1] op_sel_hi:[1,0,1]\nv_add_f32 v37, v32, v33\n"
      "v_pk_fma_f32 v[20:21], v[20:21], s[58:59], s[58:59] op_sel:[0,0,1] op_sel_hi:[1,0,1]\nv_pk_fma_f32 v[22:23], v[22:23], s[58:59], s[58:59] op_sel:[0,0,1] op_sel_hi:[1,0,1]\n"
      "v_pk_fma_f32 v[16:17], v[16:17], v[172:173], s[60:61] op_sel_hi:[1,1,0]\nds_read_b128 v[164:167], v252 offset:23552\nv_pk_fma_f32 v[18:19], v[18:19], v[174:175], s[60:61] op_sel_hi:[1,1,0]\n"
      "ds_read_b128 v[168:171], v252 offset:24576\nv_pk_fma_f32 v[20:21], v[20:21], v[176:177], s[60:61] op_sel_hi:[1,1,0]\nv_pk_fma_f32 v[22:23], v[22:23], v[178:179], s[60:61] op_sel_hi:[1,1,0]\n"
      "v_pk_mul_f32 v[172:173], v[172:173], v[0:1]\nv_pk_mul_f32 v[174:175], v[174:175], v[2:3]\nv_pk_mul_f32 v[176:177], v[176:177], v[4:5]\nv_pk_mul_f32 v[178:179], v[178:179], v[6:7]\n"
      "v_exp_f32 v16, v16 clamp\nv_exp_f32 v17, v17 clamp\nv_exp_f32 v18, v18 clamp\nv_exp_f32 v19, v19 clamp\nv_exp_f32 v20, v20 clamp\nv_exp_f32 v21, v21 clamp\nv_exp_f32 v22, v22 clamp\n"
      "v_exp_f32 v23, v23 clamp\nv_pk_add_f32 v[16:17], v[16:17], s[60:61] op_sel:[0,1] op_sel_hi:[1,1]\nv_pk_add_f32 v[18:19], v[18:19], s[60:61] op_sel:[0,1] op_sel_hi:[1,1]\n"
      "v_pk_add_f32 v[20:21], v[20:21], s[60:61] op_sel:[0,1] op_sel_hi:[1,1]\nv_pk_add_f32 v[22:23], v[22:23], s[60:61] op_sel:[0,1] op_sel_hi:[1,1]\n"
      "v_pk_mul_f32 v[172:173], v[172:173], v[18:19]\nv_pk_mul_f32 v[176:177], v[176:177], v[22:23]\nv_pk_fma_f32 v[172:173], v[174:175], v[16:17], v[172:173]\n"
      "v_pk_fma_f32 v[176:177], v[178:179], v[20:21], v[176:177]\nv_pk_mul_f32 v[16:17], v[16:17], v[18:19]\nv_pk_mul_f32 v[20:21], v[20:21], v[22:23]\nv_pk_mul_f32 v[18:19], v[16:17], v[20:21]\n"
      "v_pk_mul_f32 v[24:25], v[180:181], v[180:181]\nv_pk_mul_f32 v[172:173], v[172:173], v[20:21]\nv_pk_mul_f32 v[26:27], v[182:183], v[182:183]\nv_rcp_f32 v18, v18\n"
      "v_pk_mul_f32 v[28:29], v[184:185], v[184:185]\nv_rcp_f32 v19, v19\nv_pk_mul_f32 v[30:31], v[186:187], v[186:187]\nv_pk_fma_f32 v[172:173], v[176:177], v[16:17], v[172:173]\n"
      "v_pk_fma_f32 v[24:25], v[24:25], s[58:59], s[58:59] op_sel:[0,0,1] op_sel_hi:[1,0,1]\nv_pk_mul_f32 v[32:33], v[172:173], v[18:19]\n"
      "v_pk_fma_f32 v[26:27], v[26:27], s[58:59], s[58:59] op_sel:[0,0,1] op_sel_hi:[1,0,1]\nv_pk_fma_f32 v[28:29], v[28:29], s[58:59], s[58:59] op_sel:[0,0,1] op_sel_hi:[1,0,1]\n"
      "v_pk_fma_f32 v[30:31], v[30:31], s[58:59], s[58:59] op_sel:[0,0,1] op_sel_hi:[1,0,1]\nv_pk_fma_f32 v[24:25], v[24:25], v[180:181], s[60:61] op_sel_hi:[1,1,0]\n"
      "ds_read_b128 v[172:175], v252 offset:25600\nv_pk_fma_f32 v[26:27], v[26:27], v[182:183], s[60:61] op_sel_hi:[1,1,0]\nds_read_b128 v[176:179], v252 offset:26624\n"
      "v_pk_fma_f32 v[28:29], v[28:29], v[184:185], s[60:61] op_sel_hi:[1,1,0]\nv_pk_fma_f32 v[30:31], v[30:31], v[186:187], s[60:61] op_sel_hi:[1,1,0]\n"
      "v_pk_mul_f32 v[180:181], v[180:181], v[8:9]\nv_pk_mul_f32 v[182:183], v[182:183], v[10:11]\nv_pk_mul_f32 v[184:185], v[184:185], v[12:13]\nv_pk_mul_f32 v[186:187], v[186:187], v[14:15]\n"
      "v_exp_f32 v24, v24 clamp\nv_exp_f32 v25, v25 clamp\nv_exp_f32 v26, v26 clamp\nv_exp_f32 v27, v27 clamp\nv_exp_f32 v28, v28 clamp\nv_exp_f32 v29, v29 clamp\nv_exp_f32 v30, v30 clamp\n"
      "v_exp_f32 v31, v31 clamp\nv_pk_add_f32 v[24:25], v[24:25], s[60:61] op_sel:[0,1] op_sel_hi:[1,1]\nv_pk_add_f32 v[26:27], v[26:27], s[60:61] op_sel:[0,1] op_sel_hi:[1,1]\n"
      "v_pk_add_f32 v[28:29], v[28:29], s[60:61] op_sel:[0,1] op_sel_hi:[1,1]\nv_pk_add_f32 v[30:31], v[30:31], s[60:61] op_sel:[0,1] op_sel_hi:[1,1]\n"
      "v_pk_mul_f32 v[180:181], v[180:181], v[26:27]\nv_pk_mul_f32 v[184:185], v[184:185], v[30:31]\nv_pk_fma_f32 v[180:181], v[182:183], v[24:25], v[180:181]\n"
      "v_pk_fma_f32 v[184:185], v[186:187], v[28:29], v[184:185]\nv_pk_mul_f32 v[24:25], v[24:25], v[26:27]\nv_pk_mul_f32 v[28:29], v[28:29], v[30:31]\nv_pk_mul_f32 v[26:27], v[24:25], v[28:29]\n"
      "v_pk_mul_f32 v[16:17], v[188:189], v[188:189]\nv_pk_mul_f32 v[180:181], v[180:181], v[28:29]\nv_pk_mul_f32 v[18:19], v[190:191], v[190:191]\nv_rcp_f32 v26, v26\n"
      "v_pk_mul_f32 v[20:21], v[192:193], v[192:193]\nv_rcp_f32 v27, v27\nv_pk_mul_f32 v[22:23], v[194:195], v[194:195]\nv_pk_fma_f32 v[180:181], v[184:185], v[24:25], v[180:181]\n"
      "v_pk_fma_f32 v[16:17], v[16:17], s[58:59], s[58:59] op_sel:[0,0,1] op_sel_hi:[1,0,1]\nv_pk_fma_f32 v[32:33], v[180:181], v[26:27], v[32:33]\n"
      "v_pk_fma_f32 v[18:19], v[18:19], s[58:59], s[58:59] op_sel:[0,0,1] op_sel_hi:[1,0,1]\nv_add_f32 v38, v32, v33\n"
      "v_pk_fma_f32 v[20:21], v[20:21], s[58:59], s[58:59] op_sel:[0,0,1] op_sel_hi:[1,0,1]\nv_pk_fma_f32 v[22:23], v[22:23], s[58:59], s[58:59] op_sel:[0,0,1] op_sel_hi:[1,0,1]\n"
      "v_pk_fma_f32 v[16:17], v[16:17], v[188:189], s[60:61] op_sel_hi:[1,1,0]\nds_read_b128 v[180:183], v252 offset:27648\nv_pk_fma_f32 v[18:19], v[18:19], v[190:191], s[60:61] op_sel_hi:[1,1,0]\n"
      "ds_read_b128 v[184:187], v252 offset:28672\nv_pk_fma_f32 v[20:21], v[20:21], v[192:193], s[60:61] op_sel_hi:[1,1,0]\nv_pk_fma_f32 v[22:23], v[22:23], v[194:195], s[60:61] op_sel_hi:[1,1,0]\n"
      "v_pk_mul_f32 v[188:189], v[188:189], v[0:1]\nv_pk_mul_f32 v[190:191], v[190:191], v[2:3]\nv_pk_mul_f32 v[192:193], v[192:193], v[4:5]\nv_pk_mul_f32 v[194:195], v[194:195], v[6:7]\n"
      "v_exp_f32 v16, v16 clamp\nv_exp_f32 v17, v17 clamp\nv_exp_f32 v18, v18 clamp\nv_exp_f32 v19, v19 clamp\nv_exp_f32 v20, v20 clamp\nv_exp_f32 v21, v21 clamp\nv_exp_f32 v22, v22 clamp\n"
      "v_exp_f32 v23, v23 clamp\nv_pk_add_f32 v[16:17], v[16:17], s[60:61] op_sel:[0,1] op_sel_hi:[1,1]\nv_pk_add_f32 v[18:19], v[18:19], s[60:61] op_sel:[0,1] op_sel_hi:[1,1]\n"
      "v_pk_add_f32 v[20:21], v[20:21], s[60:61] op_sel:[0,1] op_sel_hi:[1,1]\nv_pk_add_f32 v[22:23], v[22:23], s[60:61] op_sel:[0,1] op_sel_hi:[1,1]\n"
      "v_pk_mul_f32 v[188:189], v[188:189], v[18:19]\nv_pk_mul_f32 v[192:193], v[192:193], v[22:23]\nv_pk_fma_f32 v[188:189], v[190:191], v[16:17], v[188:189]\n"
      "v_pk_fma_f32 v[192:193], v[194:195], v[20:21], v[192:193]\nv_pk_mul_f32 v[16:17], v[16:17], v[18:19]\nv_pk_mul_f32 v[20:21], v[20:21], v[22:23]\nv_pk_mul_f32 v[18:19], v[16:17], v[20:21]\n"
      "v_pk_mul_f32 v[24:25], v[196:197], v[196:197]\nv_pk_mul_f32 v[188:189], v[188:189], v[20:21]\nv_pk_mul_f32 v[26:27], v[198:199], v[198:199]\nv_rcp_f32 v18, v18\n"
      "v_pk_mul_f32 v[28:29], v[200:201], v[200:201]\nv_rcp_f32 v19, v19\nv_pk_mul_f32 v[30:31], v[202:203], v[202:203]\nv_pk_fma_f32 v[188:189], v[192:193], v[16:17], v[188:189]\n"
      "v_pk_fma_f32 v[24:25], v[24:25], s[58:59], s[58:59] op_sel:[0,0,1] op_sel_hi:[1,0,1]\nv_pk_mul_f32 v[32:33], v[188:189], v[18:19]\n"
      "v_pk_fma_f32 v[26:27], v[26:27], s[58:59], s[58:59] op_sel:[0,0,1] op_sel_hi:[1,0,1]\nv_pk_fma_f32 v[28:29], v[28:29], s[58:59], s[58:59] op_sel:[0,0,1] op_sel_hi:[1,0,1]\n"
      "v_pk_fma_f32 v[30:31], v[30:31], s[58:59], s[58:59] op_sel:[0,0,1] op_sel_hi:[1,0,1]\nv_pk_fma_f32 v[24:25], v[24:25], v[196:197], s[60:61] op_sel_hi:[1,1,0]\n"
      "ds_read_b128 v[188:191], v252 offset:29696\nv_pk_fma_f32 v[26:27], v[26:27], v[198:199], s[60:61] op_sel_hi:[1,1,0]\nds_read_b128 v[192:195], v252 offset:30720\n"
      "v_pk_fma_f32 v[28:29], v[28:29], v[200:201], s[60:61] op_sel_hi:[1,1,0]\nv_pk_fma_f32 v[30:31], v[30:31], v[202:203], s[60:61] op_sel_hi:[1,1,0]\n"
      "v_pk_mul_f32 v[196:197], v[196:197], v[8:9]\nv_pk_mul_f32 v[198:199], v[198:199], v[10:11]\nv_pk_mul_f32 v[200:201], v[200:201], v[12:13]\nv_pk_mul_f32 v[202:203], v[202:203], v[14:15]\n"
      "v_exp_f32 v24, v24 clamp\nv_exp_f32 v25, v25 clamp\nv_exp_f32 v26, v26 clamp\nv_exp_f32 v27, v27 clamp\nv_exp_f32 v28, v28 clamp\nv_exp_f32 v29, v29 clamp\nv_exp_f32 v30, v30 clamp\n"
      "v_exp_f32 v31, v31 clamp\nv_pk_add_f32 v[24:25], v[24:25], s[60:61] op_sel:[0,1] op_sel_hi:[1,1]\nv_pk_add_f32 v[26:27], v[26:27], s[60:61] op_sel:[0,1] op_sel_hi:[1,1]\n"
      "v_pk_add_f32 v[28:29], v[28:29], s[60:61] op_sel:[0,1] op_sel_hi:[1,1]\nv_pk_add_f32 v[30:31], v[30:31], s[60:61] op_sel:[0,1] op_sel_hi:[1,1]\n"
      "v_pk_mul_f32 v[196:197], v[196:197], v[26:27]\nv_pk_mul_f32 v[200:201], v[200:201], v[30:31]\nv_pk_fma_f32 v[196:197], v[198:199], v[24:25], v[196:197]\n"
      "v_pk_fma_f32 v[200:201], v[202:203], v[28:29], v[200:201]\nv_pk_mul_f32 v[24:25], v[24:25], v[26:27]\nv_pk_mul_f32 v[28:29], v[28:29], v[30:31]\nv_pk_mul_f32 v[26:27], v[24:25], v[28:29]\n"
      "v_pk_mul_f32 v[16:17], v[204:205], v[204:205]\nv_pk_mul_f32 v[196:197], v[196:197], v[28:29]\nv_pk_mul_f32 v[18:19], v[206:207], v[206:207]\nv_rcp_f32 v26, v26\n"
      "v_pk_mul_f32 v[20:21], v[208:209], v[208:209]\nv_rcp_f32 v27, v27\nv_pk_mul_f32 v[22:23], v[210:211], v[210:211]\nv_pk_fma_f32 v[196:197], v[200:201], v[24:25], v[196:197]\n"
      "v_pk_fma_f32 v[16:17], v[16:17], s[58:59], s[58:59] op_sel:[0,0,1] op_sel_hi:[1,0,1]\nv_pk_fma_f32 v[32:33], v[196:197], v[26:27], v[32:33]\n"
      "v_pk_fma_f32 v[18:19], v[18:19], s[58:59], s[58:59] op_sel:[0,0,1] op_sel_hi:[1,0,1]\nv_add_f32 v39, v32, v33\n"
      "v_pk_fma_f32 v[20:21], v[20:21], s[58:59], s[58:59] op_sel:[0,0,1] op_sel_hi:[1,0,1]\nv_pk_fma_f32 v[22:23], v[22:23], s[58:59], s[58:59] op_sel:[0,0,1] op_sel_hi:[1,0,1]\n"
      "v_pk_fma_f32 v[16:17], v[16:17], v[204:205], s[60:61] op_sel_hi:[1,1,0]\nds_read_b128 v[196:199], v252 offset:31744\nv_pk_fma_f32 v[18:19], v[18:19], v[206:207], s[60:61] op_sel_hi:[1,1,0]\n"
      "ds_read_b128 v[200:203], v252 offset:32768\nv_pk_fma_f32 v[20:21], v[20:21], v[208:209], s[60:61] op_sel_hi:[1,1,0]\nv_pk_fma_f32 v[22:23], v[22:23], v[210:211], s[60:61] op_sel_hi:[1,1,0]\n"
      "v_pk_mul_f32 v[204:205], v[204:205], v[0:1]\nv_pk_mul_f32 v[206:207], v[206:207], v[2:3]\nv_pk_mul_f32 v[208:209], v[208:209], v[4:5]\nv_pk_mul_f32 v[210:211], v[210:211], v[6:7]\n"
      "v_exp_f32 v16, v16 clamp\nv_exp_f32 v17, v17 clamp\nv_exp_f32 v18, v18 clamp\nv_exp_f32 v19, v19 clamp\nv_exp_f32 v20, v20 clamp\nv_exp_f32 v21, v21 clamp\nv_exp_f32 v22, v22 clamp\n"
      "v_exp_f32 v23, v23 clamp\nv_pk_add_f32 v[16:17], v[16:17], s[60:61] op_sel:[0,1] op_sel_hi:[1,1]\nv_pk_add_f32 v[18:19], v[18:19], s[60:61] op_sel:[0,1] op_sel_hi:[1,1]\n"
      "v_pk_add_f32 v[20:21], v[20:21], s[60:61] op_sel:[0,1] op_sel_hi:[1,1]\nv_pk_add_f32 v[22:23], v[22:23], s[60:61] op_sel:[0,1] op_sel_hi:[1,1]\n"
      "v_pk_mul_f32 v[204:205], v[204:205], v[18:19]\nv_pk_mul_f32 v[208:209], v[208:209], v[22:23]\nv_pk_fma_f32 v[204:205], v[206:207], v[16:17], v[204:205]\n"
      "v_pk_fma_f32 v[208:209], v[210:211], v[20:21], v[208:209]\nv_pk_mul_f32 v[16:17], v[16:17], v[18:19]\nv_pk_mul_f32 v[20:21], v[20:21], v[22:23]\nv_pk_mul_f32 v[18:19], v[16:17], v[20:21]\n"
      "v_pk_mul_f32 v[24:25], v[212:213], v[212:213]\nv_pk_mul_f32 v[204:205], v[204:205], v[20:21]\nv_pk_mul_f32 v[26:27], v[214:215], v[214:215]\nv_rcp_f32 v18, v18\n"
      "v_pk_mul_f32 v[28:29], v[216:217], v[216:217]\nv_rcp_f32 v19, v19\nv_pk_mul_f32 v[30:31], v[218:219], v[218:219]\nv_pk_fma_f32 v[204:205], v[208:209], v[16:17], v[204:205]\n"
      "v_pk_fma_f32 v[24:25], v[24:25], s[58:59], s[58:59] op_sel:[0,0,1] op_sel_hi:[1,0,1]\nv_pk_mul_f32 v[32:33], v[204:205], v[18:19]\n"
      "v_pk_fma_f32 v[26:27], v[26:27], s[58:59], s[58:59] op_sel:[0,0,1] op_sel_hi:[1,0,1]\nv_pk_fma_f32 v[28:29], v[28:29], s[58:59], s[58:59] op_sel:[0,0,1] op_sel_hi:[1,0,1]\n"
      "v_pk_fma_f32 v[30:31], v[30:31], s[58:59], s[58:59] op_sel:[0,0,1] op_sel_hi:[1,0,1]\nv_pk_fma_f32 v[24:25], v[24:25], v[212:213], s[60:61] op_sel_hi:[1,1,0]\n"
      "ds_read_b128 v[204:207], v252 offset:33792\nv_pk_fma_f32 v[26:27], v[26:27], v[214:215], s[60:61] op_sel_hi:[1,1,0]\nds_read_b128 v[208:211], v252 offset:34816\n"
      "v_pk_fma_f32 v[28:29], v[28:29], v[216:217], s[60:61] op_sel_hi:[1,1,0]\nv_pk_fma_f32 v[30:31], v[30:31], v[218:219], s[60:61] op_sel_hi:[1,1,0]\n"
      "v_pk_mul_f32 v[212:213], v[212:213], v[8:9]\nv_pk_mul_f32 v[214:215], v[214:215], v[10:11]\nv_pk_mul_f32 v[216:217], v[216:217], v[12:13]\nv_pk_mul_f32 v[218:219], v[218:219], v[14:15]\n"
      "v_exp_f32 v24, v24 clamp\nv_exp_f32 v25, v25 clamp\nv_exp_f32 v26, v26 clamp\nv_exp_f32 v27, v27 clamp\nv_exp_f32 v28, v28 clamp\nv_exp_f32 v29, v29 clamp\nv_exp_f32 v30, v30 clamp\n"
      "v_exp_f32 v31, v31 clamp\nv_pk_add_f32 v[24:25], v[24:25], s[60:61] op_sel:[0,1] op_sel_hi:[1,1]\nv_pk_add_f32 v[26:27], v[26:27], s[60:61] op_sel:[0,1] op_sel_hi:[1,1]\n"
      "v_pk_add_f32 v[28:29], v[28:29], s[60:61] op_sel:[0,1] op_sel_hi:[1,1]\nv_pk_add_f32 v[30:31], v[30:31], s[60:61] op_sel:[0,1] op_sel_hi:[1,1]\n"
      "v_pk_mul_f32 v[212:213], v[212:213], v[26:27]\nv_pk_mul_f32 v[216:217], v[216:217], v[30:31]\nv_pk_fma_f32 v[212:213], v[214:215], v[24:25], v[212:213]\n"
      "v_pk_fma_f32 v[216:217], v[218:219], v[28:29], v[216:217]\nv_pk_mul_f32 v[24:25], v[24:25], v[26:27]\nv_pk_mul_f32 v[28:29], v[28:29], v[30:31]\nv_pk_mul_f32 v[26:27], v[24:25], v[28:29]\n"
      "v_pk_mul_f32 v[16:17], v[220:221], v[220:221]\nv_pk_mul_f32 v[212:213], v[212:213], v[28:29]\nv_pk_mul_f32 v[18:19], v[222:223], v[222:223]\nv_rcp_f32 v26, v26\n"
      "v_pk_mul_f32 v[20:21], v[224:225], v[224:225]\nv_rcp_f32 v27, v27\nv_pk_mul_f32 v[22:23], v[226:227], v[226:227]\nv_pk_fma_f32 v[212:213], v[216:217], v[24:25], v[212:213]\n"
      "v_pk_fma_f32 v[16:17], v[16:17], s[58:59], s[58:59] op_sel:[0,0,1] op_sel_hi:[1,0,1]\nv_pk_fma_f32 v[32:33], v[212:213], v[26:27], v[32:33]\n"
      "v_pk_fma_f32 v[18:19], v[18:19], s[58:59], s[58:59] op_sel:[0,0,1] op_sel_hi:[1,0,1]\nv_add_f32 v40, v32, v33\n"
      "v_pk_fma_f32 v[20:21], v[20:21], s[58:59], s[58:59] op_sel:[0,0,1] op_sel_hi:[1,0,1]\nv_pk_fma_f32 v[22:23], v[22:23], s[58:59], s[58:59] op_sel:[0,0,1] op_sel_hi:[1,0,1]\n"
      "v_pk_fma_f32 v[16:17], v[16:17], v[220:221], s[60:61] op_sel_hi:[1,1,0]\nds_read_b128 v[212:215], v252 offset:35840\nv_pk_fma_f32 v[18:19], v[18:19], v[222:223], s[60:61] op_sel_hi:[1,1,0]\n"
      "ds_read_b128 v[216:219], v252 offset:36864\nv_pk_fma_f32 v[20:21], v[20:21], v[224:225], s[60:61] op_sel_hi:[1,1,0]\nv_pk_fma_f32 v[22:23], v[22:23], v[226:227], s[60:61] op_sel_hi:[1,1,0]\n"
      "v_pk_mul_f32 v[220:221], v[220:221], v[0:1]\nv_pk_mul_f32 v[222:223], v[222:223], v[2:3]\nv_pk_mul_f32 v[224:225], v[224:225], v[4:5]\nv_pk_mul_f32 v[226:227], v[226:227], v[6:7]\n"
      "v_exp_f32 v16, v16 clamp\nv_exp_f32 v17, v17 clamp\nv_exp_f32 v18, v18 clamp\nv_exp_f32 v19, v19 clamp\nv_exp_f32 v20, v20 clamp\nv_exp_f32 v21, v21 clamp\nv_exp_f32 v22, v22 clamp\n"
      "v_exp_f32 v23, v23 clamp\nv_pk_add_f32 v[16:17], v[16:17], s[60:61] op_sel:[0,1] op_sel_hi:[1,1]\nv_pk_add_f32 v[18:19], v[18:19], s[60:61] op_sel:[0,1] op_sel_hi:[1,1]\n"
      "v_pk_add_f32 v[20:21], v[20:21], s[60:61] op_sel:[0,1] op_sel_hi:[1,1]\nv_pk_add_f32 v[22:23], v[22:23], s[60:61] op_sel:[0,1] op_sel_hi:[1,1]\n"
      "v_pk_mul_f32 v[220:221], v[220:221], v[18:19]\nv_pk_mul_f32 v[224:225], v[224:225], v[22:23]\nv_pk_fma_f32 v[220:221], v[222:223], v[16:17], v[220:221]\n"
      "v_pk_fma_f32 v[224:225], v[226:227], v[20:21], v[224:225]\nv_pk_mul_f32 v[16:17], v[16:17], v[18:19]\nv_pk_mul_f32 v[20:21], v[20:21], v[22:23]\nv_pk_mul_f32 v[18:19], v[16:17], v[20:21]\n"
      "v_pk_mul_f32 v[24:25], v[228:229], v[228:229]\nv_pk_mul_f32 v[220:221], v[220:221], v[20:21]\nv_pk_mul_f32 v[26:27], v[230:231], v[230:231]\nv_rcp_f32 v18, v18\n"
      "v_pk_mul_f32 v[28:29], v[232:233], v[232:233]\nv_rcp_f32 v19, v19\nv_pk_mul_f32 v[30:31], v[234:235], v[234:235]\nv_pk_fma_f32 v[220:221], v[224:225], v[16:17], v[220:221]\n"
      "v_pk_fma_f32 v[24:25], v[24:25], s[58:59], s[58:59] op_sel:[0,0,1] op_sel_hi:[1,0,1]\nv_pk_mul_f32 v[32:33], v[220:221], v[18:19]\n"
      "v_pk_fma_f32 v[26:27], v[26:27], s[58:59], s[58:59] op_sel:[0,0,1] op_sel_hi:[1,0,1]\nv_pk_fma_f32 v[28:29], v[28:29], s[58:59], s[58:59] op_sel:[0,0,1] op_sel_hi:[1,0,1]\n"
      "v_pk_fma_f32 v[30:31], v[30:31], s[58:59], s[58:59] op_sel:[0,0,1] op_sel_hi:[1,0,1]\nv_pk_fma_f32 v[24:25], v[24:25], v[228:229], s[60:61] op_sel_hi:[1,1,0]\n"
      "ds_read_b128 v[220:223], v252 offset:37888\nv_pk_fma_f32 v[26:27], v[26:27], v[230:231], s[60:61] op_sel_hi:[1,1,0]\nds_read_b128 v[224:227], v252 offset:38912\n"
      "v_pk_fma_f32 v[28:29], v[28:29], v[232:233], s[60:61] op_sel_hi:[1,1,0]\nv_pk_fma_f32 v[30:31], v[30:31], v[234:235], s[60:61] op_sel_hi:[1,1,0]\n"
      "v_pk_mul_f32 v[228:229], v[228:229], v[8:9]\nv_pk_mul_f32 v[230:231], v[230:231], v[10:11]\nv_pk_mul_f32 v[232:233], v[232:233], v[12:13]\nv_pk_mul_f32 v[234:235], v[234:235], v[14:15]\n"
      "v_exp_f32 v24, v24 clamp\nv_exp_f32 v25, v25 clamp\nv_exp_f32 v26, v26 clamp\nv_exp_f32 v27, v27 clamp\nv_exp_f32 v28, v28 clamp\nv_exp_f32 v29, v29 clamp\nv_exp_f32 v30, v30 clamp\n"
      "v_exp_f32 v31, v31 clamp\nv_pk_add_f32 v[24:25], v[24:25], s[60:61] op_sel:[0,1] op_sel_hi:[1,1]\nv_pk_add_f32 v[26:27], v[26:27], s[60:61] op_sel:[0,1] op_sel_hi:[1,1]\n"
      "v_pk_add_f32 v[28:29], v[28:29], s[60:61] op_sel:[0,1] op_sel_hi:[1,1]\nv_pk_add_f32 v[30:31], v[30:31], s[60:61] op_sel:[0,1] op_sel_hi:[1,1]\n"
      "v_pk_mul_f32 v[228:229], v[228:229], v[26:27]\nv_pk_mul_f32 v[232:233], v[232:233], v[30:31]\nv_pk_fma_f32 v[228:229], v[230:231], v[24:25], v[228:229]\n"
      "v_pk_fma_f32 v[232:233], v[234:235], v[28:29], v[232:233]\nv_pk_mul_f32 v[24:25], v[24:25], v[26:27]\nv_pk_mul_f32 v[28:29], v[28:29], v[30:31]\nv_pk_mul_f32 v[26:27], v[24:25], v[28:29]\n"
      "v_pk_mul_f32 v[228:229], v[228:229], v[28:29]\nv_rcp_f32 v26, v26\nv_rcp_f32 v27, v27\nv_pk_fma_f32 v[228:229], v[232:233], v[24:25], v[228:229]\n"
      "v_pk_fma_f32 v[32:33], v[228:229], v[26:27], v[32:33]\nv_add_f32 v41, v32, v33\ns_barrier\nds_read_b128 v[228:231], v252 offset:39936\nds_read_b128 v[232:235], v252 offset:40960\n"
      "s_branch .Lge\n"
      ".Lg12:\ns_barrier\nds_read_b128 v[0:3], v98 offset:0\nds_read_b128 v[4:7], v99 offset:0\nds_read_b64_tr_b16 v[64:65], v108 offset:0\nds_read_b64_tr_b16 v[66:67], v108 offset:1024\n"
      "ds_read_b64_tr_b16 v[68:69], v108 offset:8192\nds_read_b64_tr_b16 v[70:71], v108 offset:9216\nds_read_b64_tr_b16 v[72:73], v109 offset:0\nds_read_b64_tr_b16 v[74:75], v109 offset:1024\n"
      "ds_read_b64_tr_b16 v[76:77], v109 offset:8192\nds_read_b64_tr_b16 v[78:79], v109 offset:9216\nds_read_b64_tr_b16 v[80:81], v110 offset:0\nds_read_b64_tr_b16 v[82:83], v110 offset:1024\n"
      "ds_read_b64_tr_b16 v[84:85], v110 offset:8192\nds_read_b64_tr_b16 v[86:87], v110 offset:9216\nds_read_b64_tr_b16 v[88:89], v111 offset:0\nds_read_b64_tr_b16 v[90:91], v111 offset:1024\n"
      "ds_read_b64_tr_b16 v[92:93], v111 offset:8192\nds_read_b64_tr_b16 v[94:95], v111 offset:9216\nds_read_b128 v[8:11], v98 offset:2048\nds_read_b128 v[12:15], v99 offset:2048\n"
      "ds_read_b128 v[16:19], v98 offset:4096\nds_read_b128 v[20:23], v99 offset:4096\ns_waitcnt lgkmcnt(0)\nds_read_b128 v[24:27], v98 offset:6144\nds_read_b128 v[28:31], v99 offset:6144\n"
      ".p2align 3\n"
      ".Lgl12:\nv_mfma_f32_16x16x32_bf16 v[124:127], v[64:67], v[0:3], v[124:127]\nv_mfma_f32_16x16x32_bf16 v[124:127], v[68:71], v[4:7], v[124:127]\n"
      "v_mfma_f32_16x16x32_bf16 v[128:131], v[76:79], v[4:7], v[128:131]\nv_mfma_f32_16x16x32_bf16 v[128:131], v[72:75], v[0:3], v[128:131]\n"
      "v_mfma_f32_16x16x32_bf16 v[132:135], v[80:83], v[0:3], v[132:135]\nv_mfma_f32_16x16x32_bf16 v[132:135], v[84:87], v[4:7], v[132:135]\n"
      "v_mfma_f32_16x16x32_bf16 v[136:139], v[92:95], v[4:7], v[136:139]\nv_mfma_f32_16x16x32_bf16 v[136:139], v[88:91], v[0:3], v[136:139]\n"
      "v_mfma_f32_16x16x32_bf16 v[152:155], v[88:91], v[8:11], v[152:155]\nds_read_b128 v[0:3], v98 offset:8192\nv_mfma_f32_16x16x32_bf16 v[152:155], v[92:95], v[12:15], v[152:155]\n"
      "ds_read_b128 v[4:7], v99 offset:8192\nv_mfma_f32_16x16x32_bf16 v[148:151], v[84:87], v[12:15], v[148:151]\nv_mfma_f32_16x16x32_bf16 v[148:151], v[80:83], v[8:11], v[148:151]\n"
      "v_mfma_f32_16x16x32_bf16 v[144:147], v[72:75], v[8:11], v[144:147]\nv_mfma_f32_16x16x32_bf16 v[144:147], v[76:79], v[12:15], v[144:147]\n"
      "v_mfma_f32_16x16x32_bf16 v[140:143], v[68:71], v[12:15], v[140:143]\nv_mfma_f32_16x16x32_bf16 v[140:143], v[64:67], v[8:11], v[140:143]\n"
      "v_mfma_f32_16x16x32_bf16 v[156:159], v[64:67], v[16:19], v[156:159]\nds_read_b128 v[8:11], v98 offset:10240\nv_mfma_f32_16x16x32_bf16 v[156:159], v[68:71], v[20:23], v[156:159]\n"
      "ds_read_b128 v[12:15], v99 offset:10240\nv_mfma_f32_16x16x32_bf16 v[160:163], v[76:79], v[20:23], v[160:163]\nv_mfma_f32_16x16x32_bf16 v[160:163], v[72:75], v[16:19], v[160:163]\n"
      "v_mfma_f32_16x16x32_bf16 v[164:167], v[80:83], v[16:19], v[164:167]\nv_mfma_f32_16x16x32_bf16 v[164:167], v[84:87], v[20:23], v[164:167]\n"
      "v_mfma_f32_16x16x32_bf16 v[168:171], v[92:95], v[20:23], v[168:171]\ns_waitcnt vmcnt(0) lgkmcnt(0)\ns_barrier\nv_mfma_f32_16x16x32_bf16 v[168:171], v[88:91], v[16:19], v[168:171]\n"
      "ds_read_b64_tr_b16 v[32:33], v104 offset:0\nv_mfma_f32_16x16x32_bf16 v[184:187], v[88:91], v[24:27], v[184:187]\nds_read_b128 v[16:19], v96 offset:0\n"
      "v_mfma_f32_16x16x32_bf16 v[184:187], v[92:95], v[28:31], v[184:187]\nds_read_b128 v[20:23], v97 offset:0\nv_mfma_f32_16x16x32_bf16 v[180:183], v[84:87], v[28:31], v[180:183]\n"
      "ds_read_b64_tr_b16 v[34:35], v104 offset:1024\nds_read_b64_tr_b16 v[36:37], v104 offset:8192\nv_mfma_f32_16x16x32_bf16 v[180:183], v[80:83], v[24:27], v[180:183]\n"
      "ds_read_b64_tr_b16 v[38:39], v104 offset:9216\nds_read_b64_tr_b16 v[40:41], v105 offset:0\nv_mfma_f32_16x16x32_bf16 v[176:179], v[72:75], v[24:27], v[176:179]\n"
      "ds_read_b64_tr_b16 v[42:43], v105 offset:1024\nv_mfma_f32_16x16x32_bf16 v[176:179], v[76:79], v[28:31], v[176:179]\nds_read_b64_tr_b16 v[44:45], v105 offset:8192\n"
      "v_mfma_f32_16x16x32_bf16 v[172:175], v[68:71], v[28:31], v[172:175]\nds_read_b64_tr_b16 v[46:47], v105 offset:9216\nv_mfma_f32_16x16x32_bf16 v[172:175], v[64:67], v[24:27], v[172:175]\n"
      "ds_read_b64_tr_b16 v[48:49], v106 offset:0\nv_mfma_f32_16x16x32_bf16 v[188:191], v[64:67], v[0:3], v[188:191]\nds_read_b128 v[24:27], v96 offset:2048\n"
      "v_mfma_f32_16x16x32_bf16 v[188:191], v[68:71], v[4:7], v[188:191]\nds_read_b128 v[28:31], v97 offset:2048\nv_mfma_f32_16x16x32_bf16 v[192:195], v[76:79], v[4:7], v[192:195]\n"
      "ds_read_b64_tr_b16 v[50:51], v106 offset:1024\nv_mfma_f32_16x16x32_bf16 v[192:195], v[72:75], v[0:3], v[192:195]\nds_read_b64_tr_b16 v[52:53], v106 offset:8192\n"
      "v_mfma_f32_16x16x32_bf16 v[196:199], v[80:83], v[0:3], v[196:199]\nds_read_b64_tr_b16 v[54:55], v106 offset:9216\nv_mfma_f32_16x16x32_bf16 v[196:199], v[84:87], v[4:7], v[196:199]\n"
      "ds_read_b64_tr_b16 v[56:57], v107 offset:0\nv_mfma_f32_16x16x32_bf16 v[200:203], v[92:95], v[4:7], v[200:203]\nds_read_b64_tr_b16 v[58:59], v107 offset:1024\n"
      "v_mfma_f32_16x16x32_bf16 v[200:203], v[88:91], v[0:3], v[200:203]\nds_read_b64_tr_b16 v[60:61], v107 offset:8192\nv_mfma_f32_16x16x32_bf16 v[216:219], v[88:91], v[8:11], v[216:219]\n"
      "ds_read_b128 v[0:3], v96 offset:4096\nv_mfma_f32_16x16x32_bf16 v[216:219], v[92:95], v[12:15], v[216:219]\nds_read_b128 v[4:7], v97 offset:4096\n"
      "v_mfma_f32_16x16x32_bf16 v[212:215], v[84:87], v[12:15], v[212:215]\nds_read_b64_tr_b16 v[62:63], v107 offset:9216\nv_mfma_f32_16x16x32_bf16 v[212:215], v[80:83], v[8:11], v[212:215]\n"
      "v_mfma_f32_16x16x32_bf16 v[208:211], v[72:75], v[8:11], v[208:211]\nv_mfma_f32_16x16x32_bf16 v[208:211], v[76:79], v[12:15], v[208:211]\n"
      "v_mfma_f32_16x16x32_bf16 v[204:207], v[68:71], v[12:15], v[204:207]\ns_waitcnt lgkmcnt(0)\nv_mfma_f32_16x16x32_bf16 v[204:207], v[64:67], v[8:11], v[204:207]\n"
      "ds_read_b128 v[8:11], v96 offset:6144\nds_read_b128 v[12:15], v97 offset:6144\nv_mfma_f32_16x16x32_bf16 v[124:127], v[32:35], v[16:19], v[124:127]\n"
      "v_mfma_f32_16x16x32_bf16 v[124:127], v[36:39], v[20:23], v[124:127]\nv_mfma_f32_16x16x32_bf16 v[128:131], v[44:47], v[20:23], v[128:131]\n"
      "v_mfma_f32_16x16x32_bf16 v[128:131], v[40:43], v[16:19], v[128:131]\nv_mfma_f32_16x16x32_bf16 v[132:135], v[48:51], v[16:19], v[132:135]\n"
      "v_mfma_f32_16x16x32_bf16 v[132:135], v[52:55], v[20:23], v[132:135]\nv_mfma_f32_16x16x32_bf16 v[136:139], v[60:63], v[20:23], v[136:139]\n"
      "v_mfma_f32_16x16x32_bf16 v[136:139], v[56:59], v[16:19], v[136:139]\nv_mfma_f32_16x16x32_bf16 v[152:155], v[56:59], v[24:27], v[152:155]\nds_read_b128 v[16:19], v96 offset:8192\n"
      "v_mfma_f32_16x16x32_bf16 v[152:155], v[60:63], v[28:31], v[152:155]\nds_read_b128 v[20:23], v97 offset:8192\nv_mfma_f32_16x16x32_bf16 v[148:151], v[52:55], v[28:31], v[148:151]\n"
      "v_mfma_f32_16x16x32_bf16 v[148:151], v[48:51], v[24:27], v[148:151]\nv_mfma_f32_16x16x32_bf16 v[144:147], v[40:43], v[24:27], v[144:147]\n"
      "v_mfma_f32_16x16x32_bf16 v[144:147], v[44:47], v[28:31], v[144:147]\nv_mfma_f32_16x16x32_bf16 v[140:143], v[36:39], v[28:31], v[140:143]\n"
      "v_mfma_f32_16x16x32_bf16 v[140:143], v[32:35], v[24:27], v[140:143]\nv_mfma_f32_16x16x32_bf16 v[156:159], v[32:35], v[0:3], v[156:159]\nds_read_b128 v[24:27], v96 offset:10240\n"
      "v_mfma_f32_16x16x32_bf16 v[156:159], v[36:39], v[4:7], v[156:159]\nds_read_b128 v[28:31], v97 offset:10240\nv_mfma_f32_16x16x32_bf16 v[160:163], v[44:47], v[4:7], v[160:163]\n"
      "v_mfma_f32_16x16x32_bf16 v[160:163], v[40:43], v[0:3], v[160:163]\nv_mfma_f32_16x16x32_bf16 v[164:167], v[48:51], v[0:3], v[164:167]\n"
      "v_mfma_f32_16x16x32_bf16 v[164:167], v[52:55], v[4:7], v[164:167]\nv_mfma_f32_16x16x32_bf16 v[168:171], v[60:63], v[4:7], v[168:171]\ns_waitcnt vmcnt(0) lgkmcnt(0)\ns_barrier\n"
      "v_mfma_f32_16x16x32_bf16 v[168:171], v[56:59], v[0:3], v[168:171]\nds_read_b64_tr_b16 v[64:65], v104 offset:45056\nv_mfma_f32_16x16x32_bf16 v[184:187], v[56:59], v[8:11], v[184:187]\n"
      "ds_read_b128 v[0:3], v96 offset:45056\nv_mfma_f32_16x16x32_bf16 v[184:187], v[60:63], v[12:15], v[184:187]\nds_read_b128 v[4:7], v97 offset:45056\n"
      "v_mfma_f32_16x16x32_bf16 v[180:183], v[52:55], v[12:15], v[180:183]\nds_read_b64_tr_b16 v[66:67], v104 offset:46080\nds_read_b64_tr_b16 v[68:69], v104 offset:53248\n"
      "v_mfma_f32_16x16x32_bf16 v[180:183], v[48:51], v[8:11], v[180:183]\nds_read_b64_tr_b16 v[70:71], v104 offset:54272\nds_read_b64_tr_b16 v[72:73], v105 offset:45056\n"
      "v_mfma_f32_16x16x32_bf16 v[176:179], v[40:43], v[8:11], v[176:179]\nds_read_b64_tr_b16 v[74:75], v105 offset:46080\nv_mfma_f32_16x16x32_bf16 v[176:179], v[44:47], v[12:15], v[176:179]\n"
      "ds_read_b64_tr_b16 v[76:77], v105 offset:53248\nv_mfma_f32_16x16x32_bf16 v[172:175], v[36:39], v[12:15], v[172:175]\nds_read_b64_tr_b16 v[78:79], v105 offset:54272\n"
      "v_mfma_f32_16x16x32_bf16 v[172:175], v[32:35], v[8:11], v[172:175]\nds_read_b64_tr_b16 v[80:81], v106 offset:45056\nv_mfma_f32_16x16x32_bf16 v[188:191], v[32:35], v[16:19], v[188:191]\n"
      "ds_read_b128 v[8:11], v96 offset:47104\nv_mfma_f32_16x16x32_bf16 v[188:191], v[36:39], v[20:23], v[188:191]\nds_read_b128 v[12:15], v97 offset:47104\n"
      "v_mfma_f32_16x16x32_bf16 v[192:195], v[44:47], v[20:23], v[192:195]\nds_read_b64_tr_b16 v[82:83], v106 offset:46080\nv_mfma_f32_16x16x32_bf16 v[192:195], v[40:43], v[16:19], v[192:195]\n"
      "ds_read_b64_tr_b16 v[84:85], v106 offset:53248\nv_mfma_f32_16x16x32_bf16 v[196:199], v[48:51], v[16:19], v[196:199]\nds_read_b64_tr_b16 v[86:87], v106 offset:54272\n"
      "v_mfma_f32_16x16x32_bf16 v[196:199], v[52:55], v[20:23], v[196:199]\nds_read_b64_tr_b16 v[88:89], v107 offset:45056\nv_mfma_f32_16x16x32_bf16 v[200:203], v[60:63], v[20:23], v[200:203]\n"
      "ds_read_b64_tr_b16 v[90:91], v107 offset:46080\nv_mfma_f32_16x16x32_bf16 v[200:203], v[56:59], v[16:19], v[200:203]\nds_read_b64_tr_b16 v[92:93], v107 offset:53248\n"
      "v_mfma_f32_16x16x32_bf16 v[216:219], v[56:59], v[24:27], v[216:219]\nds_read_b128 v[16:19], v96 offset:49152\nv_mfma_f32_16x16x32_bf16 v[216:219], v[60:63], v[28:31], v[216:219]\n"
      "ds_read_b128 v[20:23], v97 offset:49152\nv_mfma_f32_16x16x32_bf16 v[212:215], v[52:55], v[28:31], v[212:215]\nds_read_b64_tr_b16 v[94:95], v107 offset:54272\n"
      "v_mfma_f32_16x16x32_bf16 v[212:215], v[48:51], v[24:27], v[212:215]\nv_mfma_f32_16x16x32_bf16 v[208:211], v[40:43], v[24:27], v[208:211]\n"
      "v_mfma_f32_16x16x32_bf16 v[208:211], v[44:47], v[28:31], v[208:211]\nv_mfma_f32_16x16x32_bf16 v[204:207], v[36:39], v[28:31], v[204:207]\ns_waitcnt lgkmcnt(0)\n"
      "v_mfma_f32_16x16x32_bf16 v[204:207], v[32:35], v[24:27], v[204:207]\nds_read_b128 v[24:27], v96 offset:51200\nds_read_b128 v[28:31], v97 offset:51200\n"
      "v_mfma_f32_16x16x32_bf16 v[124:127], v[64:67], v[0:3], v[124:127]\nv_mfma_f32_16x16x32_bf16 v[124:127], v[68:71], v[4:7], v[124:127]\n"
      "v_mfma_f32_16x16x32_bf16 v[128:131], v[76:79], v[4:7], v[128:131]\nv_mfma_f32_16x16x32_bf16 v[128:131], v[72:75], v[0:3], v[128:131]\n"
      "v_mfma_f32_16x16x32_bf16 v[132:135], v[80:83], v[0:3], v[132:135]\nv_mfma_f32_16x16x32_bf16 v[132:135], v[84:87], v[4:7], v[132:135]\n"
      "v_mfma_f32_16x16x32_bf16 v[136:139], v[92:95], v[4:7], v[136:139]\nv_mfma_f32_16x16x32_bf16 v[136:139], v[88:91], v[0:3], v[136:139]\n"
      "v_mfma_f32_16x16x32_bf16 v[152:155], v[88:91], v[8:11], v[152:155]\nds_read_b128 v[0:3], v96 offset:53248\nv_mfma_f32_16x16x32_bf16 v[152:155], v[92:95], v[12:15], v[152:155]\n"
      "ds_read_b128 v[4:7], v97 offset:53248\nv_mfma_f32_16x16x32_bf16 v[148:151], v[84:87], v[12:15], v[148:151]\nv_mfma_f32_16x16x32_bf16 v[148:151], v[80:83], v[8:11], v[148:151]\n"
      "v_mfma_f32_16x16x32_bf16 v[144:147], v[72:75], v[8:11], v[144:147]\nv_mfma_f32_16x16x32_bf16 v[144:147], v[76:79], v[12:15], v[144:147]\n"
      "v_mfma_f32_16x16x32_bf16 v[140:143], v[68:71], v[12:15], v[140:143]\nv_mfma_f32_16x16x32_bf16 v[140:143], v[64:67], v[8:11], v[140:143]\n"
      "v_mfma_f32_16x16x32_bf16 v[156:159], v[64:67], v[16:19], v[156:159]\nds_read_b128 v[8:11], v96 offset:55296\nv_mfma_f32_16x16x32_bf16 v[156:159], v[68:71], v[20:23], v[156:159]\n"
      "ds_read_b128 v[12:15], v97 offset:55296\nv_mfma_f32_16x16x32_bf16 v[160:163], v[76:79], v[20:23], v[160:163]\nv_mfma_f32_16x16x32_bf16 v[160:163], v[72:75], v[16:19], v[160:163]\n"
      "v_mfma_f32_16x16x32_bf16 v[164:167], v[80:83], v[16:19], v[164:167]\nv_mfma_f32_16x16x32_bf16 v[164:167], v[84:87], v[20:23], v[164:167]\n"
      "v_mfma_f32_16x16x32_bf16 v[168:171], v[92:95], v[20:23], v[168:171]\ns_waitcnt vmcnt(0) lgkmcnt(0)\ns_barrier\nv_mfma_f32_16x16x32_bf16 v[168:171], v[88:91], v[16:19], v[168:171]\n"
      "ds_read_b64_tr_b16 v[32:33], v108 offset:0\nv_mfma_f32_16x16x32_bf16 v[184:187], v[88:91], v[24:27], v[184:187]\nds_read_b128 v[16:19], v98 offset:0\n"
      "v_mfma_f32_16x16x32_bf16 v[184:187], v[92:95], v[28:31], v[184:187]\nds_read_b128 v[20:23], v99 offset:0\nv_mfma_f32_16x16x32_bf16 v[180:183], v[84:87], v[28:31], v[180:183]\n"
      "ds_read_b64_tr_b16 v[34:35], v108 offset:1024\nds_read_b64_tr_b16 v[36:37], v108 offset:8192\nv_mfma_f32_16x16x32_bf16 v[180:183], v[80:83], v[24:27], v[180:183]\n"
      "ds_read_b64_tr_b16 v[38:39], v108 offset:9216\nds_read_b64_tr_b16 v[40:41], v109 offset:0\nv_mfma_f32_16x16x32_bf16 v[176:179], v[72:75], v[24:27], v[176:179]\n"
      "ds_read_b64_tr_b16 v[42:43], v109 offset:1024\nv_mfma_f32_16x16x32_bf16 v[176:179], v[76:79], v[28:31], v[176:179]\nds_read_b64_tr_b16 v[44:45], v109 offset:8192\n"
      "v_mfma_f32_16x16x32_bf16 v[172:175], v[68:71], v[28:31], v[172:175]\nds_read_b64_tr_b16 v[46:47], v109 offset:9216\nv_mfma_f32_16x16x32_bf16 v[172:175], v[64:67], v[24:27], v[172:175]\n"
      "ds_read_b64_tr_b16 v[48:49], v110 offset:0\nv_mfma_f32_16x16x32_bf16 v[188:191], v[64:67], v[0:3], v[188:191]\nds_read_b128 v[24:27], v98 offset:2048\n"
      "v_mfma_f32_16x16x32_bf16 v[188:191], v[68:71], v[4:7], v[188:191]\nds_read_b128 v[28:31], v99 offset:2048\nv_mfma_f32_16x16x32_bf16 v[192:195], v[76:79], v[4:7], v[192:195]\n"
      "ds_read_b64_tr_b16 v[50:51], v110 offset:1024\nv_mfma_f32_16x16x32_bf16 v[192:195], v[72:75], v[0:3], v[192:195]\nds_read_b64_tr_b16 v[52:53], v110 offset:8192\n"
      "v_mfma_f32_16x16x32_bf16 v[196:199], v[80:83], v[0:3], v[196:199]\nds_read_b64_tr_b16 v[54:55], v110 offset:9216\nv_mfma_f32_16x16x32_bf16 v[196:199], v[84:87], v[4:7], v[196:199]\n"
      "ds_read_b64_tr_b16 v[56:57], v111 offset:0\nv_mfma_f32_16x16x32_bf16 v[200:203], v[92:95], v[4:7], v[200:203]\nds_read_b64_tr_b16 v[58:59], v111 offset:1024\n"
      "v_mfma_f32_16x16x32_bf16 v[200:203], v[88:91], v[0:3], v[200:203]\nds_read_b64_tr_b16 v[60:61], v111 offset:8192\nv_mfma_f32_16x16x32_bf16 v[216:219], v[88:91], v[8:11], v[216:219]\n"
      "ds_read_b128 v[0:3], v98 offset:4096\nv_mfma_f32_16x16x32_bf16 v[216:219], v[92:95], v[12:15], v[216:219]\nds_read_b128 v[4:7], v99 offset:4096\n"
      "v_mfma_f32_16x16x32_bf16 v[212:215], v[84:87], v[12:15], v[212:215]\nds_read_b64_tr_b16 v[62:63], v111 offset:9216\nv_mfma_f32_16x16x32_bf16 v[212:215], v[80:83], v[8:11], v[212:215]\n"
      "v_mfma_f32_16x16x32_bf16 v[208:211], v[72:75], v[8:11], v[208:211]\nv_mfma_f32_16x16x32_bf16 v[208:211], v[76:79], v[12:15], v[208:211]\n"
      "v_mfma_f32_16x16x32_bf16 v[204:207], v[68:71], v[12:15], v[204:207]\ns_waitcnt lgkmcnt(0)\nv_mfma_f32_16x16x32_bf16 v[204:207], v[64:67], v[8:11], v[204:207]\n"
      "ds_read_b128 v[8:11], v98 offset:6144\nds_read_b128 v[12:15], v99 offset:6144\nv_mfma_f32_16x16x32_bf16 v[124:127], v[32:35], v[16:19], v[124:127]\n"
      "v_mfma_f32_16x16x32_bf16 v[124:127], v[36:39], v[20:23], v[124:127]\nv_mfma_f32_16x16x32_bf16 v[128:131], v[44:47], v[20:23], v[128:131]\n"
      "v_mfma_f32_16x16x32_bf16 v[128:131], v[40:43], v[16:19], v[128:131]\nv_mfma_f32_16x16x32_bf16 v[132:135], v[48:51], v[16:19], v[132:135]\n"
      "v_mfma_f32_16x16x32_bf16 v[132:135], v[52:55], v[20:23], v[132:135]\nv_mfma_f32_16x16x32_bf16 v[136:139], v[60:63], v[20:23], v[136:139]\n"
      "v_mfma_f32_16x16x32_bf16 v[136:139], v[56:59], v[16:19], v[136:139]\nv_mfma_f32_16x16x32_bf16 v[152:155], v[56:59], v[24:27], v[152:155]\nds_read_b128 v[16:19], v98 offset:8192\n"
      "v_mfma_f32_16x16x32_bf16 v[152:155], v[60:63], v[28:31], v[152:155]\nds_read_b128 v[20:23], v99 offset:8192\nv_mfma_f32_16x16x32_bf16 v[148:151], v[52:55], v[28:31], v[148:151]\n"
      "v_mfma_f32_16x16x32_bf16 v[148:151], v[48:51], v[24:27], v[148:151]\nv_mfma_f32_16x16x32_bf16 v[144:147], v[40:43], v[24:27], v[144:147]\n"
      "v_mfma_f32_16x16x32_bf16 v[144:147], v[44:47], v[28:31], v[144:147]\nv_mfma_f32_16x16x32_bf16 v[140:143], v[36:39], v[28:31], v[140:143]\n"
      "v_mfma_f32_16x16x32_bf16 v[140:143], v[32:35], v[24:27], v[140:143]\nv_mfma_f32_16x16x32_bf16 v[156:159], v[32:35], v[0:3], v[156:159]\nds_read_b128 v[24:27], v98 offset:10240\n"
      "v_mfma_f32_16x16x32_bf16 v[156:159], v[36:39], v[4:7], v[156:159]\nds_read_b128 v[28:31], v99 offset:10240\nv_mfma_f32_16x16x32_bf16 v[160:163], v[44:47], v[4:7], v[160:163]\n"
      "v_mfma_f32_16x16x32_bf16 v[160:163], v[40:43], v[0:3], v[160:163]\nv_mfma_f32_16x16x32_bf16 v[164:167], v[48:51], v[0:3], v[164:167]\n"
      "v_mfma_f32_16x16x32_bf16 v[164:167], v[52:55], v[4:7], v[164:167]\nv_mfma_f32_16x16x32_bf16 v[168:171], v[60:63], v[4:7], v[168:171]\ns_waitcnt vmcnt(0) lgkmcnt(0)\ns_barrier\n"
      "v_mfma_f32_16x16x32_bf16 v[168:171], v[56:59], v[0:3], v[168:171]\nds_read_b64_tr_b16 v[64:65], v104 offset:0\nv_mfma_f32_16x16x32_bf16 v[184:187], v[56:59], v[8:11], v[184:187]\n"
      "ds_read_b128 v[0:3], v96 offset:0\nv_mfma_f32_16x16x32_bf16 v[184:187], v[60:63], v[12:15], v[184:187]\nds_read_b128 v[4:7], v97 offset:0\n"
      "v_mfma_f32_16x16x32_bf16 v[180:183], v[52:55], v[12:15], v[180:183]\nds_read_b64_tr_b16 v[66:67], v104 offset:1024\nds_read_b64_tr_b16 v[68:69], v104 offset:8192\n"
      "v_mfma_f32_16x16x32_bf16 v[180:183], v[48:51], v[8:11], v[180:183]\nds_read_b64_tr_b16 v[70:71], v104 offset:9216\nds_read_b64_tr_b16 v[72:73], v105 offset:0\n"
      "v_mfma_f32_16x16x32_bf16 v[176:179], v[40:43], v[8:11], v[176:179]\nds_read_b64_tr_b16 v[74:75], v105 offset:1024\nv_mfma_f32_16x16x32_bf16 v[176:179], v[44:47], v[12:15], v[176:179]\n"
      "ds_read_b64_tr_b16 v[76:77], v105 offset:8192\nv_mfma_f32_16x16x32_bf16 v[172:175], v[36:39], v[12:15], v[172:175]\nds_read_b64_tr_b16 v[78:79], v105 offset:9216\n"
      "v_mfma_f32_16x16x32_bf16 v[172:175], v[32:35], v[8:11], v[172:175]\nds_read_b64_tr_b16 v[80:81], v106 offset:0\nv_mfma_f32_16x16x32_bf16 v[188:191], v[32:35], v[16:19], v[188:191]\n"
      "ds_read_b128 v[8:11], v96 offset:2048\nv_mfma_f32_16x16x32_bf16 v[188:191], v[36:39], v[20:23], v[188:191]\nds_read_b128 v[12:15], v97 offset:2048\n"
      "v_mfma_f32_16x16x32_bf16 v[192:195], v[44:47], v[20:23], v[192:195]\nds_read_b64_tr_b16 v[82:83], v106 offset:1024\nv_mfma_f32_16x16x32_bf16 v[192:195], v[40:43], v[16:19], v[192:195]\n"
      "ds_read_b64_tr_b16 v[84:85], v106 offset:8192\nv_mfma_f32_16x16x32_bf16 v[196:199], v[48:51], v[16:19], v[196:199]\nds_read_b64_tr_b16 v[86:87], v106 offset:9216\n"
      "v_mfma_f32_16x16x32_bf16 v[196:199], v[52:55], v[20:23], v[196:199]\nds_read_b64_tr_b16 v[88:89], v107 offset:0\nv_mfma_f32_16x16x32_bf16 v[200:203], v[60:63], v[20:23], v[200:203]\n"
      "ds_read_b64_tr_b16 v[90:91], v107 offset:1024\nv_mfma_f32_16x16x32_bf16 v[200:203], v[56:59], v[16:19], v[200:203]\nds_read_b64_tr_b16 v[92:93], v107 offset:8192\n"
      "v_mfma_f32_16x16x32_bf16 v[216:219], v[56:59], v[24:27], v[216:219]\nds_read_b128 v[16:19], v96 offset:4096\nv_mfma_f32_16x16x32_bf16 v[216:219], v[60:63], v[28:31], v[216:219]\n"
      "ds_read_b128 v[20:23], v97 offset:4096\nv_mfma_f32_16x16x32_bf16 v[212:215], v[52:55], v[28:31], v[212:215]\nds_read_b64_tr_b16 v[94:95], v107 offset:9216\n"
      "v_mfma_f32_16x16x32_bf16 v[212:215], v[48:51], v[24:27], v[212:215]\nv_mfma_f32_16x16x32_bf16 v[208:211], v[40:43], v[24:27], v[208:211]\n"
      "v_mfma_f32_16x16x32_bf16 v[208:211], v[44:47], v[28:31], v[208:211]\nv_mfma_f32_16x16x32_bf16 v[204:207], v[36:39], v[28:31], v[204:207]\ns_waitcnt lgkmcnt(0)\n"
      "v_mfma_f32_16x16x32_bf16 v[204:207], v[32:35], v[24:27], v[204:207]\nds_read_b128 v[24:27], v96 offset:6144\nds_read_b128 v[28:31], v97 offset:6144\n"
      "v_mfma_f32_16x16x32_bf16 v[124:127], v[64:67], v[0:3], v[124:127]\nv_mfma_f32_16x16x32_bf16 v[124:127], v[68:71], v[4:7], v[124:127]\n"
      "v_mfma_f32_16x16x32_bf16 v[128:131], v[76:79], v[4:7], v[128:131]\nv_mfma_f32_16x16x32_bf16 v[128:131], v[72:75], v[0:3], v[128:131]\n"
      "v_mfma_f32_16x16x32_bf16 v[132:135], v[80:83], v[0:3], v[132:135]\nv_mfma_f32_16x16x32_bf16 v[132:135], v[84:87], v[4:7], v[132:135]\n"
      "v_mfma_f32_16x16x32_bf16 v[136:139], v[92:95], v[4:7], v[136:139]\nv_mfma_f32_16x16x32_bf16 v[136:139], v[88:91], v[0:3], v[136:139]\n"
      "v_mfma_f32_16x16x32_bf16 v[152:155], v[88:91], v[8:11], v[152:155]\nds_read_b128 v[0:3], v96 offset:8192\nv_mfma_f32_16x16x32_bf16 v[152:155], v[92:95], v[12:15], v[152:155]\n"
      "ds_read_b128 v[4:7], v97 offset:8192\nv_mfma_f32_16x16x32_bf16 v[148:151], v[84:87], v[12:15], v[148:151]\nv_mfma_f32_16x16x32_bf16 v[148:151], v[80:83], v[8:11], v[148:151]\n"
      "v_mfma_f32_16x16x32_bf16 v[144:147], v[72:75], v[8:11], v[144:147]\nv_mfma_f32_16x16x32_bf16 v[144:147], v[76:79], v[12:15], v[144:147]\n"
      "v_mfma_f32_16x16x32_bf16 v[140:143], v[68:71], v[12:15], v[140:143]\nv_mfma_f32_16x16x32_bf16 v[140:143], v[64:67], v[8:11], v[140:143]\n"
      "v_mfma_f32_16x16x32_bf16 v[156:159], v[64:67], v[16:19], v[156:159]\nds_read_b128 v[8:11], v96 offset:10240\nv_mfma_f32_16x16x32_bf16 v[156:159], v[68:71], v[20:23], v[156:159]\n"
      "ds_read_b128 v[12:15], v97 offset:10240\nv_mfma_f32_16x16x32_bf16 v[160:163], v[76:79], v[20:23], v[160:163]\nv_mfma_f32_16x16x32_bf16 v[160:163], v[72:75], v[16:19], v[160:163]\n"
      "v_mfma_f32_16x16x32_bf16 v[164:167], v[80:83], v[16:19], v[164:167]\nv_mfma_f32_16x16x32_bf16 v[164:167], v[84:87], v[20:23], v[164:167]\n"
      "v_mfma_f32_16x16x32_bf16 v[168:171], v[92:95], v[20:23], v[168:171]\ns_waitcnt vmcnt(0) lgkmcnt(0)\ns_barrier\nv_mfma_f32_16x16x32_bf16 v[168:171], v[88:91], v[16:19], v[168:171]\n"
      "ds_read_b64_tr_b16 v[32:33], v104 offset:45056\nv_mfma_f32_16x16x32_bf16 v[184:187], v[88:91], v[24:27], v[184:187]\nds_read_b128 v[16:19], v96 offset:45056\n"
      "v_mfma_f32_16x16x32_bf16 v[184:187], v[92:95], v[28:31], v[184:187]\nds_read_b128 v[20:23], v97 offset:45056\nv_mfma_f32_16x16x32_bf16 v[180:183], v[84:87], v[28:31], v[180:183]\n"
      "ds_read_b64_tr_b16 v[34:35], v104 offset:46080\nds_read_b64_tr_b16 v[36:37], v104 offset:53248\nv_mfma_f32_16x16x32_bf16 v[180:183], v[80:83], v[24:27], v[180:183]\n"
      "ds_read_b64_tr_b16 v[38:39], v104 offset:54272\nds_read_b64_tr_b16 v[40:41], v105 offset:45056\nv_mfma_f32_16x16x32_bf16 v[176:179], v[72:75], v[24:27], v[176:179]\n"
      "ds_read_b64_tr_b16 v[42:43], v105 offset:46080\nv_mfma_f32_16x16x32_bf16 v[176:179], v[76:79], v[28:31], v[176:179]\nds_read_b64_tr_b16 v[44:45], v105 offset:53248\n"
      "v_mfma_f32_16x16x32_bf16 v[172:175], v[68:71], v[28:31], v[172:175]\nds_read_b64_tr_b16 v[46:47], v105 offset:54272\nv_mfma_f32_16x16x32_bf16 v[172:175], v[64:67], v[24:27], v[172:175]\n"
      "ds_read_b64_tr_b16 v[48:49], v106 offset:45056\nv_mfma_f32_16x16x32_bf16 v[188:191], v[64:67], v[0:3], v[188:191]\nds_read_b128 v[24:27], v96 offset:47104\n"
      "v_mfma_f32_16x16x32_bf16 v[188:191], v[68:71], v[4:7], v[188:191]\nds_read_b128 v[28:31], v97 offset:47104\nv_mfma_f32_16x16x32_bf16 v[192:195], v[76:79], v[4:7], v[192:195]\n"
      "ds_read_b64_tr_b16 v[50:51], v106 offset:46080\nv_mfma_f32_16x16x32_bf16 v[192:195], v[72:75], v[0:3], v[192:195]\nds_read_b64_tr_b16 v[52:53], v106 offset:53248\n"
      "v_mfma_f32_16x16x32_bf16 v[196:199], v[80:83], v[0:3], v[196:199]\nds_read_b64_tr_b16 v[54:55], v106 offset:54272\nv_mfma_f32_16x16x32_bf16 v[196:199], v[84:87], v[4:7], v[196:199]\n"
      "ds_read_b64_tr_b16 v[56:57], v107 offset:45056\nv_mfma_f32_16x16x32_bf16 v[200:203], v[92:95], v[4:7], v[200:203]\nds_read_b64_tr_b16 v[58:59], v107 offset:46080\n"
      "v_mfma_f32_16x16x32_bf16 v[200:203], v[88:91], v[0:3], v[200:203]\nds_read_b64_tr_b16 v[60:61], v107 offset:53248\nv_mfma_f32_16x16x32_bf16 v[216:219], v[88:91], v[8:11], v[216:219]\n"
      "ds_read_b128 v[0:3], v96 offset:49152\nv_mfma_f32_16x16x32_bf16 v[216:219], v[92:95], v[12:15], v[216:219]\nds_read_b128 v[4:7], v97 offset:49152\n"
      "v_mfma_f32_16x16x32_bf16 v[212:215], v[84:87], v[12:15], v[212:215]\nds_read_b64_tr_b16 v[62:63], v107 offset:54272\nv_mfma_f32_16x16x32_bf16 v[212:215], v[80:83], v[8:11], v[212:215]\n"
      "v_mfma_f32_16x16x32_bf16 v[208:211], v[72:75], v[8:11], v[208:211]\nv_mfma_f32_16x16x32_bf16 v[208:211], v[76:79], v[12:15], v[208:211]\n"
      "v_mfma_f32_16x16x32_bf16 v[204:207], v[68:71], v[12:15], v[204:207]\ns_waitcnt lgkmcnt(0)\nv_mfma_f32_16x16x32_bf16 v[204:207], v[64:67], v[8:11], v[204:207]\n"
      "ds_read_b128 v[8:11], v96 offset:51200\nds_read_b128 v[12:15], v97 offset:51200\nv_mfma_f32_16x16x32_bf16 v[124:127], v[32:35], v[16:19], v[124:127]\n"
      "v_mfma_f32_16x16x32_bf16 v[124:127], v[36:39], v[20:23], v[124:127]\nv_mfma_f32_16x16x32_bf16 v[128:131], v[44:47], v[20:23], v[128:131]\n"
      "v_mfma_f32_16x16x32_bf16 v[128:131], v[40:43], v[16:19], v[128:131]\nv_mfma_f32_16x16x32_bf16 v[132:135], v[48:51], v[16:19], v[132:135]\n"
      "v_mfma_f32_16x16x32_bf16 v[132:135], v[52:55], v[20:23], v[132:135]\nv_mfma_f32_16x16x32_bf16 v[136:139], v[60:63], v[20:23], v[136:139]\n"
      "v_mfma_f32_16x16x32_bf16 v[136:139], v[56:59], v[16:19], v[136:139]\nv_mfma_f32_16x16x32_bf16 v[152:155], v[56:59], v[24:27], v[152:155]\nds_read_b128 v[16:19], v96 offset:53248\n"
      "v_mfma_f32_16x16x32_bf16 v[152:155], v[60:63], v[28:31], v[152:155]\nds_read_b128 v[20:23], v97 offset:53248\nv_mfma_f32_16x16x32_bf16 v[148:151], v[52:55], v[28:31], v[148:151]\n"
      "v_mfma_f32_16x16x32_bf16 v[148:151], v[48:51], v[24:27], v[148:151]\nv_mfma_f32_16x16x32_bf16 v[144:147], v[40:43], v[24:27], v[144:147]\n"
      "v_mfma_f32_16x16x32_bf16 v[144:147], v[44:47], v[28:31], v[144:147]\nv_mfma_f32_16x16x32_bf16 v[140:143], v[36:39], v[28:31], v[140:143]\n"
      "v_mfma_f32_16x16x32_bf16 v[140:143], v[32:35], v[24:27], v[140:143]\nv_mfma_f32_16x16x32_bf16 v[156:159], v[32:35], v[0:3], v[156:159]\nds_read_b128 v[24:27], v96 offset:55296\n"
      "v_mfma_f32_16x16x32_bf16 v[156:159], v[36:39], v[4:7], v[156:159]\nds_read_b128 v[28:31], v97 offset:55296\nv_mfma_f32_16x16x32_bf16 v[160:163], v[44:47], v[4:7], v[160:163]\n"
      "v_mfma_f32_16x16x32_bf16 v[160:163], v[40:43], v[0:3], v[160:163]\nv_mfma_f32_16x16x32_bf16 v[164:167], v[48:51], v[0:3], v[164:167]\n"
      "v_mfma_f32_16x16x32_bf16 v[164:167], v[52:55], v[4:7], v[164:167]\nv_mfma_f32_16x16x32_bf16 v[168:171], v[60:63], v[4:7], v[168:171]\ns_waitcnt vmcnt(0) lgkmcnt(0)\ns_barrier\n"
      "v_mfma_f32_16x16x32_bf16 v[168:171], v[56:59], v[0:3], v[168:171]\nds_read_b64_tr_b16 v[64:65], v108 offset:0\nv_mfma_f32_16x16x32_bf16 v[184:187], v[56:59], v[8:11], v[184:187]\n"
      "ds_read_b128 v[0:3], v98 offset:0\nv_mfma_f32_16x16x32_bf16 v[184:187], v[60:63], v[12:15], v[184:187]\nds_read_b128 v[4:7], v99 offset:0\n"
      "v_mfma_f32_16x16x32_bf16 v[180:183], v[52:55], v[12:15], v[180:183]\nds_read_b64_tr_b16 v[66:67], v108 offset:1024\nds_read_b64_tr_b16 v[68:69], v108 offset:8192\n"
      "v_mfma_f32_16x16x32_bf16 v[180:183], v[48:51], v[8:11], v[180:183]\nds_read_b64_tr_b16 v[70:71], v108 offset:9216\nds_read_b64_tr_b16 v[72:73], v109 offset:0\n"
      "v_mfma_f32_16x16x32_bf16 v[176:179], v[40:43], v[8:11], v[176:179]\nds_read_b64_tr_b16 v[74:75], v109 offset:1024\nv_mfma_f32_16x16x32_bf16 v[176:179], v[44:47], v[12:15], v[176:179]\n"
      "ds_read_b64_tr_b16 v[76:77], v109 offset:8192\nv_mfma_f32_16x16x32_bf16 v[172:175], v[36:39], v[12:15], v[172:175]\nds_read_b64_tr_b16 v[78:79], v109 offset:9216\n"
      "v_mfma_f32_16x16x32_bf16 v[172:175], v[32:35], v[8:11], v[172:175]\nds_read_b64_tr_b16 v[80:81], v110 offset:0\nv_mfma_f32_16x16x32_bf16 v[188:191], v[32:35], v[16:19], v[188:191]\n"
      "ds_read_b128 v[8:11], v98 offset:2048\nv_mfma_f32_16x16x32_bf16 v[188:191], v[36:39], v[20:23], v[188:191]\nds_read_b128 v[12:15], v99 offset:2048\n"
      "v_mfma_f32_16x16x32_bf16 v[192:195], v[44:47], v[20:23], v[192:195]\nds_read_b64_tr_b16 v[82:83], v110 offset:1024\nv_mfma_f32_16x16x32_bf16 v[192:195], v[40:43], v[16:19], v[192:195]\n"
      "ds_read_b64_tr_b16 v[84:85], v110 offset:8192\nv_mfma_f32_16x16x32_bf16 v[196:199], v[48:51], v[16:19], v[196:199]\nds_read_b64_tr_b16 v[86:87], v110 offset:9216\n"
      "v_mfma_f32_16x16x32_bf16 v[196:199], v[52:55], v[20:23], v[196:199]\nds_read_b64_tr_b16 v[88:89], v111 offset:0\nv_mfma_f32_16x16x32_bf16 v[200:203], v[60:63], v[20:23], v[200:203]\n"
      "ds_read_b64_tr_b16 v[90:91], v111 offset:1024\nv_mfma_f32_16x16x32_bf16 v[200:203], v[56:59], v[16:19], v[200:203]\nds_read_b64_tr_b16 v[92:93], v111 offset:8192\n"
      "v_mfma_f32_16x16x32_bf16 v[216:219], v[56:59], v[24:27], v[216:219]\nds_read_b128 v[16:19], v98 offset:4096\nv_mfma_f32_16x16x32_bf16 v[216:219], v[60:63], v[28:31], v[216:219]\n"
      "ds_read_b128 v[20:23], v99 offset:4096\nv_mfma_f32_16x16x32_bf16 v[212:215], v[52:55], v[28:31], v[212:215]\nds_read_b64_tr_b16 v[94:95], v111 offset:9216\n"
      "v_mfma_f32_16x16x32_bf16 v[212:215], v[48:51], v[24:27], v[212:215]\nv_mfma_f32_16x16x32_bf16 v[208:211], v[40:43], v[24:27], v[208:211]\ns_sub_u32 s58, s58, 1\ns_cmp_lg_u32 s58, 0\n"
      "v_mfma_f32_16x16x32_bf16 v[208:211], v[44:47], v[28:31], v[208:211]\nv_mfma_f32_16x16x32_bf16 v[204:207], v[36:39], v[28:31], v[204:207]\ns_waitcnt lgkmcnt(0)\n"
      "v_mfma_f32_16x16x32_bf16 v[204:207], v[32:35], v[24:27], v[204:207]\nds_read_b128 v[24:27], v98 offset:6144\nds_read_b128 v[28:31], v99 offset:6144\ns_cbranch_scc1 .Lgl12\n"
      "v_mfma_f32_16x16x32_bf16 v[124:127], v[64:67], v[0:3], v[124:127]\nv_cmp_eq_u32 s[62:63], 0, v240\nv_mov_b32 v248, 262144\nv_mfma_f32_16x16x32_bf16 v[124:127], v[68:71], v[4:7], v[124:127]\n"
      "v_mov_b32 v247, 162176\nv_mov_b32 v241, 1\nv_mfma_f32_16x16x32_bf16 v[128:131], v[76:79], v[4:7], v[128:131]\nv_cndmask_b32 v253, v248, v247, s[62:63]\ns_lshl_b32 s42, s36, 2\n"
      "v_mfma_f32_16x16x32_bf16 v[128:131], v[72:75], v[0:3], v[128:131]\nv_add_u32 v245, s42, v253\nv_lshrrev_b32 v112, 4, v240\nv_mfma_f32_16x16x32_bf16 v[132:135], v[80:83], v[0:3], v[132:135]\n"
      "v_bfe_u32 v113, v240, 2, 2\nv_and_b32 v114, 3, v240\nv_mfma_f32_16x16x32_bf16 v[132:135], v[84:87], v[4:7], v[132:135]\nv_mul_u32_u24 v114, 0x80008, v114\nv_add_u32 v242, v112, v113\n"
      "v_mfma_f32_16x16x32_bf16 v[136:139], v[92:95], v[4:7], v[136:139]\nv_and_b32 v242, 3, v242\nv_add_u32 v116, 22, v113\nv_mfma_f32_16x16x32_bf16 v[136:139], v[88:91], v[0:3], v[136:139]\n"
      "v_cmp_gt_u32 s[62:63], 2, v113\nv_and_b32 v113, 60, v240\nv_mfma_f32_16x16x32_bf16 v[152:155], v[88:91], v[8:11], v[152:155]\nds_read_b128 v[0:3], v98 offset:8192\ns_mov_b32 s42, 45056\n"
      "v_mfma_f32_16x16x32_bf16 v[152:155], v[92:95], v[12:15], v[152:155]\nds_read_b128 v[4:7], v99 offset:8192\nv_cndmask_b32 v116, v116, v242, s[62:63]\n"
      "v_mfma_f32_16x16x32_bf16 v[148:151], v[84:87], v[12:15], v[148:151]\nv_lshl_or_b32 v242, v242, 16, v116\nv_lshl_add_u32 v242, v242, 5, v114\n"
      "v_mfma_f32_16x16x32_bf16 v[148:151], v[80:83], v[8:11], v[148:151]\nv_lshl_add_u32 v252, v240, 4, s42\ns_mov_b32 s42, 0x76325410\n"
      "v_mfma_f32_16x16x32_bf16 v[144:147], v[72:75], v[8:11], v[144:147]\ns_mov_b32 s43, 0x21005410\nv_lshrrev_b64 v[116:117], v113, s[42:43]\n"
      "v_mfma_f32_16x16x32_bf16 v[144:147], v[76:79], v[12:15], v[144:147]\nv_and_b32 v116, 7, v116\nv_lshl_add_u32 v116, v112, 3, v116\n"
      "v_mfma_f32_16x16x32_bf16 v[140:143], v[68:71], v[12:15], v[140:143]\ns_mov_b32 s42, 0x54107632\ns_mov_b32 s43, 0x21107632\n"
      "v_mfma_f32_16x16x32_bf16 v[140:143], v[64:67], v[8:11], v[140:143]\nv_lshrrev_b64 v[118:119], v113, s[42:43]\nv_and_b32 v118, 7, v118\n"
      "v_mfma_f32_16x16x32_bf16 v[156:159], v[64:67], v[16:19], v[156:159]\nds_read_b128 v[8:11], v98 offset:10240\nv_lshl_add_u32 v118, v112, 3, v118\n"
      "v_mfma_f32_16x16x32_bf16 v[156:159], v[68:71], v[20:23], v[156:159]\nds_read_b128 v[12:15], v99 offset:10240\nv_lshl_or_b32 v243, v118, 16, v116\n"
      "v_mfma_f32_16x16x32_bf16 v[160:163], v[76:79], v[20:23], v[160:163]\nv_lshl_add_u32 v243, v243, 5, v114\nv_and_b32 v113, 1, v240\n"
      "v_mfma_f32_16x16x32_bf16 v[160:163], v[72:75], v[16:19], v[160:163]\nv_mul_u32_u24 v113, 0xaaaa, v113\nv_add_u32 v244, 0x4444, v113\n"
      "v_mfma_f32_16x16x32_bf16 v[164:167], v[80:83], v[16:19], v[164:167]\ns_lshl_b32 s42, s36, 4\ns_add_u32 s42, s42, 160896\nv_mfma_f32_16x16x32_bf16 v[164:167], v[84:87], v[20:23], v[164:167]\n"
      "v_lshl_add_u32 v246, v112, 2, s42\nv_and_b32 v112, 1, v112\nv_mfma_f32_16x16x32_bf16 v[168:171], v[92:95], v[20:23], v[168:171]\ns_waitcnt vmcnt(0) lgkmcnt(0)\ns_barrier\n"
      "v_mfma_f32_16x16x32_bf16 v[168:171], v[88:91], v[16:19], v[168:171]\nds_read_b64_tr_b16 v[32:33], v104 offset:0\nv_lshl_add_u32 v112, v112, 3, s24\n"
      "v_mfma_f32_16x16x32_bf16 v[184:187], v[88:91], v[24:27], v[184:187]\nds_read_b128 v[16:19], v96 offset:0\ns_sub_u32 s42, s64, 1\n"
      "v_mfma_f32_16x16x32_bf16 v[184:187], v[92:95], v[28:31], v[184:187]\nds_read_b128 v[20:23], v97 offset:0\nv_min_u32 v112, s42, v112\n"
      "v_mfma_f32_16x16x32_bf16 v[180:183], v[84:87], v[28:31], v[180:183]\nds_read_b64_tr_b16 v[34:35], v104 offset:1024\nds_read_b64_tr_b16 v[36:37], v104 offset:8192\n"
      "v_mfma_f32_16x16x32_bf16 v[180:183], v[80:83], v[24:27], v[180:183]\nds_read_b64_tr_b16 v[38:39], v104 offset:9216\nds_read_b64_tr_b16 v[40:41], v105 offset:0\n"
      "v_mfma_f32_16x16x32_bf16 v[176:179], v[72:75], v[24:27], v[176:179]\nds_read_b64_tr_b16 v[42:43], v105 offset:1024\nv_lshlrev_b32 v112, 3, v112\n"
      "v_mfma_f32_16x16x32_bf16 v[176:179], v[76:79], v[28:31], v[176:179]\nds_read_b64_tr_b16 v[44:45], v105 offset:8192\nv_add_u32 v250, 160640, v112\n"
      "v_mfma_f32_16x16x32_bf16 v[172:175], v[68:71], v[28:31], v[172:175]\nds_read_b64_tr_b16 v[46:47], v105 offset:9216\nv_and_b32 v113, 15, v240\n"
      "v_mfma_f32_16x16x32_bf16 v[172:175], v[64:67], v[24:27], v[172:175]\nds_read_b64_tr_b16 v[48:49], v106 offset:0\nv_min_u32 v113, 12, v113\n"
      "v_mfma_f32_16x16x32_bf16 v[188:191], v[64:67], v[0:3], v[188:191]\nds_read_b128 v[24:27], v96 offset:2048\ns_lshl_b32 s42, s30, 7\n"
      "v_mfma_f32_16x16x32_bf16 v[188:191], v[68:71], v[4:7], v[188:191]\nds_read_b128 v[28:31], v97 offset:2048\nv_lshl_add_u32 v113, v113, 15, s42\n"
      "v_mfma_f32_16x16x32_bf16 v[192:195], v[76:79], v[4:7], v[192:195]\nds_read_b64_tr_b16 v[50:51], v106 offset:1024\nv_add_u32 v249, v113, v112\n"
      "v_mfma_f32_16x16x32_bf16 v[192:195], v[72:75], v[0:3], v[192:195]\nds_read_b64_tr_b16 v[52:53], v106 offset:8192\ns_mul_i32 s65, s36, 17632\n"
      "v_mfma_f32_16x16x32_bf16 v[196:199], v[80:83], v[0:3], v[196:199]\nds_read_b64_tr_b16 v[54:55], v106 offset:9216\ns_add_u32 s65, s65, 90112\n"
      "v_mfma_f32_16x16x32_bf16 v[196:199], v[84:87], v[4:7], v[196:199]\nds_read_b64_tr_b16 v[56:57], v107 offset:0\ns_add_u32 s68, s64, 6\n"
      "v_mfma_f32_16x16x32_bf16 v[200:203], v[92:95], v[4:7], v[200:203]\nds_read_b64_tr_b16 v[58:59], v107 offset:1024\ns_min_u32 s68, s68, 19\n"
      "v_mfma_f32_16x16x32_bf16 v[200:203], v[88:91], v[0:3], v[200:203]\nds_read_b64_tr_b16 v[60:61], v107 offset:8192\ns_lshl_b32 s38, s38, 16\n"
      "v_mfma_f32_16x16x32_bf16 v[216:219], v[88:91], v[8:11], v[216:219]\nds_read_b128 v[0:3], v96 offset:4096\ns_lshr_b32 s42, s24, 2\n"
      "v_mfma_f32_16x16x32_bf16 v[216:219], v[92:95], v[12:15], v[216:219]\nds_read_b128 v[4:7], v97 offset:4096\ns_mul_i32 s43, s42, 3\n"
      "v_mfma_f32_16x16x32_bf16 v[212:215], v[84:87], v[12:15], v[212:215]\nds_read_b64_tr_b16 v[62:63], v107 offset:9216\ns_xor_b32 s43, s43, s36\n"
      "v_mfma_f32_16x16x32_bf16 v[212:215], v[80:83], v[8:11], v[212:215]\ns_add_u32 s42, s42, s64\ns_add_u32 s42, s42, 7\nv_mfma_f32_16x16x32_bf16 v[208:211], v[72:75], v[8:11], v[208:211]\n"
      "s_and_b32 s42, s42, 1\ns_lshl1_add_u32 s67, s43, s42\nv_mfma_f32_16x16x32_bf16 v[208:211], v[76:79], v[12:15], v[208:211]\ns_add_u32 s66, s64, s67\ns_cmp_lt_u32 s66, s68\n"
      "v_mfma_f32_16x16x32_bf16 v[204:207], v[68:71], v[12:15], v[204:207]\ns_waitcnt lgkmcnt(0)\ns_cselect_b32 s66, s66, 127\nv_mfma_f32_16x16x32_bf16 v[204:207], v[64:67], v[8:11], v[204:207]\n"
      "ds_read_b128 v[8:11], v96 offset:6144\nds_read_b128 v[12:15], v97 offset:6144\nv_mfma_f32_16x16x32_bf16 v[124:127], v[32:35], v[16:19], v[124:127]\ns_sub_u32 s66, s66, 7\n"
      "s_getpc_b64 s[4:5]\n"
      ".Lk1063:\nv_mfma_f32_16x16x32_bf16 v[124:127], v[36:39], v[20:23], v[124:127]\ns_add_u32 s34, s4, .Lt_g-.Lk1063\ns_addc_u32 s35, s5, 0\n"
      "v_mfma_f32_16x16x32_bf16 v[128:131], v[44:47], v[20:23], v[128:131]\ns_add_u32 s4, s4, .Lc_back-.Lk1063\ns_addc_u32 s5, s5, 0\n"
      "v_mfma_f32_16x16x32_bf16 v[128:131], v[40:43], v[16:19], v[128:131]\ns_lshr_b32 s42, %[blk], 3\ns_lshl1_add_u32 s42, %[blk], s42\n"
      "v_mfma_f32_16x16x32_bf16 v[132:135], v[48:51], v[16:19], v[132:135]\ns_and_b32 s69, s42, 15\ns_lshl_b32 s43, s69, 7\nv_mfma_f32_16x16x32_bf16 v[132:135], v[52:55], v[20:23], v[132:135]\n"
      "s_add_u32 s43, s43, 32768\ns_add_u32 s8, s20, s43\nv_mfma_f32_16x16x32_bf16 v[136:139], v[60:63], v[20:23], v[136:139]\ns_addc_u32 s9, s21, 0\ns_add_u32 s12, s20, 65536\n"
      "v_mfma_f32_16x16x32_bf16 v[136:139], v[56:59], v[16:19], v[136:139]\ns_addc_u32 s13, s21, 0\ns_add_u32 s16, s20, 2052096\n"
      "v_mfma_f32_16x16x32_bf16 v[152:155], v[56:59], v[24:27], v[152:155]\nds_read_b128 v[16:19], v96 offset:8192\ns_addc_u32 s17, s21, 0\n"
      "v_mfma_f32_16x16x32_bf16 v[152:155], v[60:63], v[28:31], v[152:155]\nds_read_b128 v[20:23], v97 offset:8192\ns_lshl_b32 s42, s69, 4\n"
      "v_mfma_f32_16x16x32_bf16 v[148:151], v[52:55], v[28:31], v[148:151]\ns_max_u32 s42, s42, 24\ns_sub_u32 s42, s42, 24\nv_mfma_f32_16x16x32_bf16 v[148:151], v[48:51], v[24:27], v[148:151]\n"
      "s_min_u32 s69, s42, 192\ns_lshl_b32 s42, s69, 3\nv_mfma_f32_16x16x32_bf16 v[144:147], v[40:43], v[24:27], v[144:147]\ns_add_u32 s42, s42, 24576\ns_add_u32 s14, s20, s42\n"
      "v_mfma_f32_16x16x32_bf16 v[144:147], v[44:47], v[28:31], v[144:147]\ns_addc_u32 s15, s21, 0\nv_mfma_f32_16x16x32_bf16 v[140:143], v[36:39], v[28:31], v[140:143]\n"
      "v_mfma_f32_16x16x32_bf16 v[140:143], v[32:35], v[24:27], v[140:143]\nv_mfma_f32_16x16x32_bf16 v[156:159], v[32:35], v[0:3], v[156:159]\nds_read_b128 v[24:27], v96 offset:10240\n"
      "v_mfma_f32_16x16x32_bf16 v[156:159], v[36:39], v[4:7], v[156:159]\nds_read_b128 v[28:31], v97 offset:10240\nv_mfma_f32_16x16x32_bf16 v[160:163], v[44:47], v[4:7], v[160:163]\n"
      "v_mfma_f32_16x16x32_bf16 v[160:163], v[40:43], v[0:3], v[160:163]\nv_mfma_f32_16x16x32_bf16 v[164:167], v[48:51], v[0:3], v[164:167]\n"
      "v_mfma_f32_16x16x32_bf16 v[164:167], v[52:55], v[4:7], v[164:167]\nv_mfma_f32_16x16x32_bf16 v[168:171], v[60:63], v[4:7], v[168:171]\ns_waitcnt vmcnt(0) lgkmcnt(0)\ns_barrier\n"
      "v_mfma_f32_16x16x32_bf16 v[168:171], v[56:59], v[0:3], v[168:171]\nds_read_b128 v[72:75], v252 offset:0\nv_lshl_add_u32 v65, v240, 1, v247\n"
      "v_mfma_f32_16x16x32_bf16 v[184:187], v[56:59], v[8:11], v[184:187]\nds_read_b128 v[0:3], v236 offset:0\nds_read_u16 v251, v65 offset:64\n"
      "v_mfma_f32_16x16x32_bf16 v[184:187], v[60:63], v[12:15], v[184:187]\nds_read_b128 v[4:7], v236 offset:64\nds_read_b32 v66, v247 offset:24\n"
      "v_mfma_f32_16x16x32_bf16 v[180:183], v[52:55], v[12:15], v[180:183]\nds_read_b128 v[76:79], v252 offset:1024\nv_and_b32 v64, 48, v240\n"
      "v_mfma_f32_16x16x32_bf16 v[180:183], v[48:51], v[8:11], v[180:183]\nds_read_b128 v[80:83], v252 offset:2048\ns_mov_b32 s42, 87296\n"
      "v_mfma_f32_16x16x32_bf16 v[176:179], v[40:43], v[8:11], v[176:179]\nds_read_b128 v[84:87], v252 offset:3072\nv_lshl_add_u32 v64, v64, 2, s42\n"
      "v_mfma_f32_16x16x32_bf16 v[176:179], v[44:47], v[12:15], v[176:179]\nds_read_b128 v[88:91], v252 offset:4096\ns_mov_b32 s58, 0xbdd2d3e7\n"
      "v_mfma_f32_16x16x32_bf16 v[172:175], v[36:39], v[12:15], v[172:175]\nds_read_b128 v[92:95], v252 offset:5120\ns_mov_b32 s59, 0xc0135761\n"
      "v_mfma_f32_16x16x32_bf16 v[172:175], v[32:35], v[8:11], v[172:175]\nds_read_b128 v[96:99], v252 offset:6144\ns_mov_b32 s60, 0xc1f00000\n"
      "v_mfma_f32_16x16x32_bf16 v[188:191], v[32:35], v[16:19], v[188:191]\nds_read_b128 v[8:11], v237 offset:0\ns_mov_b32 s61, 0x30800000\n"
      "v_mfma_f32_16x16x32_bf16 v[188:191], v[36:39], v[20:23], v[188:191]\nds_read_b128 v[12:15], v237 offset:64\ns_add_u32 s49, s65, 928\n"
      "v_mfma_f32_16x16x32_bf16 v[192:195], v[44:47], v[20:23], v[192:195]\nds_read_b128 v[100:103], v252 offset:7168\ns_mov_b32 s45, 1\n"
      "v_mfma_f32_16x16x32_bf16 v[192:195], v[40:43], v[16:19], v[192:195]\nds_read_b128 v[104:107], v252 offset:8192\ns_mov_b32 s46, 0\n"
      "v_mfma_f32_16x16x32_bf16 v[196:199], v[48:51], v[16:19], v[196:199]\nds_read_b128 v[108:111], v252 offset:9216\ns_waitcnt lgkmcnt(8)\n"
      "v_mfma_f32_16x16x32_bf16 v[196:199], v[52:55], v[20:23], v[196:199]\nds_read_b128 v[112:115], v252 offset:10240\nv_readfirstlane_b32 s39, v66\n"
      "v_mfma_f32_16x16x32_bf16 v[200:203], v[60:63], v[20:23], v[200:203]\nds_read_b128 v[116:119], v252 offset:11264\nv_readlane_b32 s55, v251, 0\n"
      "v_mfma_f32_16x16x32_bf16 v[200:203], v[56:59], v[16:19], v[200:203]\nds_read_b128 v[120:123], v252 offset:12288\ns_setprio 3\n"
      "v_mfma_f32_16x16x32_bf16 v[216:219], v[56:59], v[24:27], v[216:219]\nds_read_b128 v[220:223], v252 offset:37888\ns_xor_b32 s40, s39, 0xa5c30f96\n"
      "v_mfma_f32_16x16x32_bf16 v[216:219], v[60:63], v[28:31], v[216:219]\nds_read_b128 v[224:227], v252 offset:38912\ns_mul_i32 s41, s39, 0x9E3779B1\n"
      "v_mfma_f32_16x16x32_bf16 v[212:215], v[52:55], v[28:31], v[212:215]\nds_read_b128 v[228:231], v252 offset:39936\ns_lshr_b32 s41, s41, 24\n"
      "v_mfma_f32_16x16x32_bf16 v[212:215], v[48:51], v[24:27], v[212:215]\nds_read_b128 v[232:235], v252 offset:40960\n"
      "v_add_u32_sdwa v239, s49, v243 dst_sel:DWORD dst_unused:UNUSED_PAD src0_sel:DWORD src1_sel:WORD_1\nv_mfma_f32_16x16x32_bf16 v[208:211], v[40:43], v[24:27], v[208:211]\n"
      "v_add_u32_sdwa v236, s49, v242 dst_sel:DWORD dst_unused:UNUSED_PAD src0_sel:DWORD src1_sel:WORD_0\nv_mfma_f32_16x16x32_bf16 v[208:211], v[44:47], v[28:31], v[208:211]\n"
      "v_add_u32_sdwa v237, s49, v242 dst_sel:DWORD dst_unused:UNUSED_PAD src0_sel:DWORD src1_sel:WORD_1\nv_mfma_f32_16x16x32_bf16 v[204:207], v[36:39], v[28:31], v[204:207]\n"
      "v_add_u32_sdwa v238, s49, v243 dst_sel:DWORD dst_unused:UNUSED_PAD src0_sel:DWORD src1_sel:WORD_0\ns_waitcnt lgkmcnt(0)\nv_mfma_f32_16x16x32_bf16 v[204:207], v[32:35], v[24:27], v[204:207]\n"
      "v_mov_b32 v42, 0\nv_pk_mul_f32 v[16:17], v[124:125], v[124:125]\nv_pk_mul_f32 v[18:19], v[126:127], v[126:127]\nv_pk_mul_f32 v[20:21], v[128:129], v[128:129]\n"
      "v_pk_mul_f32 v[22:23], v[130:131], v[130:131]\nv_pk_fma_f32 v[16:17], v[16:17], s[58:59], s[58:59] op_sel:[0,0,1] op_sel_hi:[1,0,1]\n"
      "v_pk_fma_f32 v[18:19], v[18:19], s[58:59], s[58:59] op_sel:[0,0,1] op_sel_hi:[1,0,1]\nv_pk_fma_f32 v[20:21], v[20:21], s[58:59], s[58:59] op_sel:[0,0,1] op_sel_hi:[1,0,1]\n"
      "v_pk_fma_f32 v[22:23], v[22:23], s[58:59], s[58:59] op_sel:[0,0,1] op_sel_hi:[1,0,1]\nv_pk_fma_f32 v[16:17], v[16:17], v[124:125], s[60:61] op_sel_hi:[1,1,0]\n"
      "v_pk_fma_f32 v[18:19], v[18:19], v[126:127], s[60:61] op_sel_hi:[1,1,0]\nv_pk_fma_f32 v[20:21], v[20:21], v[128:129], s[60:61] op_sel_hi:[1,1,0]\n"
      "v_pk_fma_f32 v[22:23], v[22:23], v[130:131], s[60:61] op_sel_hi:[1,1,0]\nv_pk_mul_f32 v[124:125], v[124:125], v[0:1]\nv_pk_mul_f32 v[126:127], v[126:127], v[2:3]\n"
      "v_pk_mul_f32 v[128:129], v[128:129], v[4:5]\nv_pk_mul_f32 v[130:131], v[130:131], v[6:7]\nv_exp_f32 v16, v16 clamp\nv_exp_f32 v17, v17 clamp\nv_exp_f32 v18, v18 clamp\n"
      "v_exp_f32 v19, v19 clamp\nv_exp_f32 v20, v20 clamp\nv_exp_f32 v21, v21 clamp\nv_exp_f32 v22, v22 clamp\nv_exp_f32 v23, v23 clamp\n"
      "v_pk_add_f32 v[16:17], v[16:17], s[60:61] op_sel:[0,1] op_sel_hi:[1,1]\nv_pk_add_f32 v[18:19], v[18:19], s[60:61] op_sel:[0,1] op_sel_hi:[1,1]\n"
      "v_pk_add_f32 v[20:21], v[20:21], s[60:61] op_sel:[0,1] op_sel_hi:[1,1]\nv_pk_add_f32 v[22:23], v[22:23], s[60:61] op_sel:[0,1] op_sel_hi:[1,1]\n"
      "v_pk_mul_f32 v[124:125], v[124:125], v[18:19]\nv_pk_mul_f32 v[128:129], v[128:129], v[22:23]\nv_pk_fma_f32 v[124:125], v[126:127], v[16:17], v[124:125]\n"
      "v_pk_fma_f32 v[128:129], v[130:131], v[20:21], v[128:129]\nv_pk_mul_f32 v[16:17], v[16:17], v[18:19]\nv_pk_mul_f32 v[20:21], v[20:21], v[22:23]\nv_pk_mul_f32 v[18:19], v[16:17], v[20:21]\n"
      "v_pk_mul_f32 v[24:25], v[132:133], v[132:133]\nv_pk_mul_f32 v[124:125], v[124:125], v[20:21]\nv_pk_mul_f32 v[26:27], v[134:135], v[134:135]\nv_rcp_f32 v18, v18\n"
      "v_pk_mul_f32 v[28:29], v[136:137], v[136:137]\nv_rcp_f32 v19, v19\nv_pk_mul_f32 v[30:31], v[138:139], v[138:139]\nv_pk_fma_f32 v[124:125], v[128:129], v[16:17], v[124:125]\n"
      "v_pk_fma_f32 v[24:25], v[24:25], s[58:59], s[58:59] op_sel:[0,0,1] op_sel_hi:[1,0,1]\nv_pk_mul_f32 v[32:33], v[124:125], v[18:19]\n"
      "v_pk_fma_f32 v[26:27], v[26:27], s[58:59], s[58:59] op_sel:[0,0,1] op_sel_hi:[1,0,1]\nv_pk_fma_f32 v[28:29], v[28:29], s[58:59], s[58:59] op_sel:[0,0,1] op_sel_hi:[1,0,1]\n"
      "v_pk_fma_f32 v[30:31], v[30:31], s[58:59], s[58:59] op_sel:[0,0,1] op_sel_hi:[1,0,1]\nv_pk_fma_f32 v[24:25], v[24:25], v[132:133], s[60:61] op_sel_hi:[1,1,0]\n"
      "ds_read_b128 v[124:127], v252 offset:13312\nv_pk_fma_f32 v[26:27], v[26:27], v[134:135], s[60:61] op_sel_hi:[1,1,0]\nds_read_b128 v[128:131], v252 offset:14336\n"
      "v_pk_fma_f32 v[28:29], v[28:29], v[136:137], s[60:61] op_sel_hi:[1,1,0]\nv_pk_fma_f32 v[30:31], v[30:31], v[138:139], s[60:61] op_sel_hi:[1,1,0]\n"
      "v_pk_mul_f32 v[132:133], v[132:133], v[8:9]\nv_pk_mul_f32 v[134:135], v[134:135], v[10:11]\nv_pk_mul_f32 v[136:137], v[136:137], v[12:13]\nv_pk_mul_f32 v[138:139], v[138:139], v[14:15]\n"
      "v_exp_f32 v24, v24 clamp\nv_exp_f32 v25, v25 clamp\nv_exp_f32 v26, v26 clamp\nv_exp_f32 v27, v27 clamp\nv_exp_f32 v28, v28 clamp\nv_exp_f32 v29, v29 clamp\nv_exp_f32 v30, v30 clamp\n"
      "v_exp_f32 v31, v31 clamp\nv_pk_add_f32 v[24:25], v[24:25], s[60:61] op_sel:[0,1] op_sel_hi:[1,1]\nv_pk_add_f32 v[26:27], v[26:27], s[60:61] op_sel:[0,1] op_sel_hi:[1,1]\n"
      "v_pk_add_f32 v[28:29], v[28:29], s[60:61] op_sel:[0,1] op_sel_hi:[1,1]\nv_pk_add_f32 v[30:31], v[30:31], s[60:61] op_sel:[0,1] op_sel_hi:[1,1]\n"
      "v_pk_mul_f32 v[132:133], v[132:133], v[26:27]\nv_pk_mul_f32 v[136:137], v[136:137], v[30:31]\nv_pk_fma_f32 v[132:133], v[134:135], v[24:25], v[132:133]\n"
      "v_pk_fma_f32 v[136:137], v[138:139], v[28:29], v[136:137]\nv_pk_mul_f32 v[24:25], v[24:25], v[26:27]\nv_pk_mul_f32 v[28:29], v[28:29], v[30:31]\nv_pk_mul_f32 v[26:27], v[24:25], v[28:29]\n"
      "v_pk_mul_f32 v[16:17], v[140:141], v[140:141]\nv_pk_mul_f32 v[132:133], v[132:133], v[28:29]\nv_pk_mul_f32 v[18:19], v[142:143], v[142:143]\nv_rcp_f32 v26, v26\n"
      "v_pk_mul_f32 v[20:21], v[144:145], v[144:145]\nv_rcp_f32 v27, v27\nv_pk_mul_f32 v[22:23], v[146:147], v[146:147]\nv_pk_fma_f32 v[132:133], v[136:137], v[24:25], v[132:133]\n"
      "v_pk_fma_f32 v[16:17], v[16:17], s[58:59], s[58:59] op_sel:[0,0,1] op_sel_hi:[1,0,1]\nv_pk_fma_f32 v[32:33], v[132:133], v[26:27], v[32:33]\n"
      "v_pk_fma_f32 v[18:19], v[18:19], s[58:59], s[58:59] op_sel:[0,0,1] op_sel_hi:[1,0,1]\nv_add_f32 v36, v32, v33\n"
      "v_pk_fma_f32 v[20:21], v[20:21], s[58:59], s[58:59] op_sel:[0,0,1] op_sel_hi:[1,0,1]\nv_pk_fma_f32 v[22:23], v[22:23], s[58:59], s[58:59] op_sel:[0,0,1] op_sel_hi:[1,0,1]\n"
      "v_pk_fma_f32 v[16:17], v[16:17], v[140:141], s[60:61] op_sel_hi:[1,1,0]\nds_read_b128 v[132:135], v252 offset:15360\nv_pk_fma_f32 v[18:19], v[18:19], v[142:143], s[60:61] op_sel_hi:[1,1,0]\n"
      "ds_read_b128 v[136:139], v252 offset:16384\nv_pk_fma_f32 v[20:21], v[20:21], v[144:145], s[60:61] op_sel_hi:[1,1,0]\nv_pk_fma_f32 v[22:23], v[22:23], v[146:147], s[60:61] op_sel_hi:[1,1,0]\n"
      "v_pk_mul_f32 v[140:141], v[140:141], v[0:1]\nv_pk_mul_f32 v[142:143], v[142:143], v[2:3]\nv_pk_mul_f32 v[144:145], v[144:145], v[4:5]\nv_pk_mul_f32 v[146:147], v[146:147], v[6:7]\n"
      "v_exp_f32 v16, v16 clamp\nv_exp_f32 v17, v17 clamp\nv_exp_f32 v18, v18 clamp\nv_exp_f32 v19, v19 clamp\nv_exp_f32 v20, v20 clamp\nv_exp_f32 v21, v21 clamp\nv_exp_f32 v22, v22 clamp\n"
      "v_exp_f32 v23, v23 clamp\nv_pk_add_f32 v[16:17], v[16:17], s[60:61] op_sel:[0,1] op_sel_hi:[1,1]\nv_pk_add_f32 v[18:19], v[18:19], s[60:61] op_sel:[0,1] op_sel_hi:[1,1]\n"
      "v_pk_add_f32 v[20:21], v[20:21], s[60:61] op_sel:[0,1] op_sel_hi:[1,1]\nv_pk_add_f32 v[22:23], v[22:23], s[60:61] op_sel:[0,1] op_sel_hi:[1,1]\n"
      "v_pk_mul_f32 v[140:141], v[140:141], v[18:19]\nv_pk_mul_f32 v[144:145], v[144:145], v[22:23]\nv_pk_fma_f32 v[140:141], v[142:143], v[16:17], v[140:141]\n"
      "v_pk_fma_f32 v[144:145], v[146:147], v[20:21], v[144:145]\nv_pk_mul_f32 v[16:17], v[16:17], v[18:19]\nv_pk_mul_f32 v[20:21], v[20:21], v[22:23]\nv_pk_mul_f32 v[18:19], v[16:17], v[20:21]\n"
      "v_pk_mul_f32 v[24:25], v[148:149], v[148:149]\nv_pk_mul_f32 v[140:141], v[140:141], v[20:21]\nv_pk_mul_f32 v[26:27], v[150:151], v[150:151]\nv_rcp_f32 v18, v18\n"
      "v_pk_mul_f32 v[28:29], v[152:153], v[152:153]\nv_rcp_f32 v19, v19\nv_pk_mul_f32 v[30:31], v[154:155], v[154:155]\nv_pk_fma_f32 v[140:141], v[144:145], v[16:17], v[140:141]\n"
      "v_pk_fma_f32 v[24:25], v[24:25], s[58:59], s[58:59] op_sel:[0,0,1] op_sel_hi:[1,0,1]\nv_pk_mul_f32 v[32:33], v[140:141], v[18:19]\n"
      "v_pk_fma_f32 v[26:27], v[26:27], s[58:59], s[58:59] op_sel:[0,0,1] op_sel_hi:[1,0,1]\nv_pk_fma_f32 v[28:29], v[28:29], s[58:59], s[58:59] op_sel:[0,0,1] op_sel_hi:[1,0,1]\n"
      "v_pk_fma_f32 v[30:31], v[30:31], s[58:59], s[58:59] op_sel:[0,0,1] op_sel_hi:[1,0,1]\nv_pk_fma_f32 v[24:25], v[24:25], v[148:149], s[60:61] op_sel_hi:[1,1,0]\n"
      "ds_read_b128 v[140:143], v252 offset:17408\nv_pk_fma_f32 v[26:27], v[26:27], v[150:151], s[60:61] op_sel_hi:[1,1,0]\nds_read_b128 v[144:147], v252 offset:18432\n"
      "v_pk_fma_f32 v[28:29], v[28:29], v[152:153], s[60:61] op_sel_hi:[1,1,0]\nv_pk_fma_f32 v[30:31], v[30:31], v[154:155], s[60:61] op_sel_hi:[1,1,0]\n"
      "v_pk_mul_f32 v[148:149], v[148:149], v[8:9]\nv_pk_mul_f32 v[150:151], v[150:151], v[10:11]\nv_pk_mul_f32 v[152:153], v[152:153], v[12:13]\nv_pk_mul_f32 v[154:155], v[154:155], v[14:15]\n"
      "v_exp_f32 v24, v24 clamp\nv_exp_f32 v25, v25 clamp\nv_exp_f32 v26, v26 clamp\nv_exp_f32 v27, v27 clamp\nv_exp_f32 v28, v28 clamp\nv_exp_f32 v29, v29 clamp\nv_exp_f32 v30, v30 clamp\n"
      "v_exp_f32 v31, v31 clamp\nv_pk_add_f32 v[24:25], v[24:25], s[60:61] op_sel:[0,1] op_sel_hi:[1,1]\nv_pk_add_f32 v[26:27], v[26:27], s[60:61] op_sel:[0,1] op_sel_hi:[1,1]\n"
      "v_pk_add_f32 v[28:29], v[28:29], s[60:61] op_sel:[0,1] op_sel_hi:[1,1]\nv_pk_add_f32 v[30:31], v[30:31], s[60:61] op_sel:[0,1] op_sel_hi:[1,1]\n"
      "v_pk_mul_f32 v[148:149], v[148:149], v[26:27]\nv_pk_mul_f32 v[152:153], v[152:153], v[30:31]\nv_pk_fma_f32 v[148:149], v[150:151], v[24:25], v[148:149]\n"
      "v_pk_fma_f32 v[152:153], v[154:155], v[28:29], v[152:153]\nv_pk_mul_f32 v[24:25], v[24:25], v[26:27]\nv_pk_mul_f32 v[28:29], v[28:29], v[30:31]\nv_pk_mul_f32 v[26:27], v[24:25], v[28:29]\n"
      "v_pk_mul_f32 v[16:17], v[156:157], v[156:157]\nv_pk_mul_f32 v[148:149], v[148:149], v[28:29]\nv_pk_mul_f32 v[18:19], v[158:159], v[158:159]\nv_rcp_f32 v26, v26\n"
      "v_pk_mul_f32 v[20:21], v[160:161], v[160:161]\nv_rcp_f32 v27, v27\nv_pk_mul_f32 v[22:23], v[162:163], v[162:163]\nv_pk_fma_f32 v[148:149], v[152:153], v[24:25], v[148:149]\n"
      "v_pk_fma_f32 v[16:17], v[16:17], s[58:59], s[58:59] op_sel:[0,0,1] op_sel_hi:[1,0,1]\nv_pk_fma_f32 v[32:33], v[148:149], v[26:27], v[32:33]\n"
      "v_pk_fma_f32 v[18:19], v[18:19], s[58:59], s[58:59] op_sel:[0,0,1] op_sel_hi:[1,0,1]\nv_add_f32 v37, v32, v33\n"
      "v_pk_fma_f32 v[20:21], v[20:21], s[58:59], s[58:59] op_sel:[0,0,1] op_sel_hi:[1,0,1]\nv_pk_fma_f32 v[22:23], v[22:23], s[58:59], s[58:59] op_sel:[0,0,1] op_sel_hi:[1,0,1]\n"
      "v_pk_fma_f32 v[16:17], v[16:17], v[156:157], s[60:61] op_sel_hi:[1,1,0]\nds_read_b128 v[148:151], v252 offset:19456\nv_pk_fma_f32 v[18:19], v[18:19], v[158:159], s[60:61] op_sel_hi:[1,1,0]\n"
      "ds_read_b128 v[152:155], v252 offset:20480\nv_pk_fma_f32 v[20:21], v[20:21], v[160:161], s[60:61] op_sel_hi:[1,1,0]\nv_pk_fma_f32 v[22:23], v[22:23], v[162:163], s[60:61] op_sel_hi:[1,1,0]\n"
      "v_pk_mul_f32 v[156:157], v[156:157], v[0:1]\nv_pk_mul_f32 v[158:159], v[158:159], v[2:3]\nv_pk_mul_f32 v[160:161], v[160:161], v[4:5]\nv_pk_mul_f32 v[162:163], v[162:163], v[6:7]\n"
      "v_exp_f32 v16, v16 clamp\nv_exp_f32 v17, v17 clamp\nv_exp_f32 v18, v18 clamp\nv_exp_f32 v19, v19 clamp\nv_exp_f32 v20, v20 clamp\nv_exp_f32 v21, v21 clamp\nv_exp_f32 v22, v22 clamp\n"
      "v_exp_f32 v23, v23 clamp\nv_pk_add_f32 v[16:17], v[16:17], s[60:61] op_sel:[0,1] op_sel_hi:[1,1]\nv_pk_add_f32 v[18:19], v[18:19], s[60:61] op_sel:[0,1] op_sel_hi:[1,1]\n"
      "v_pk_add_f32 v[20:21], v[20:21], s[60:61] op_sel:[0,1] op_sel_hi:[1,1]\nv_pk_add_f32 v[22:23], v[22:23], s[60:61] op_sel:[0,1] op_sel_hi:[1,1]\n"
      "v_pk_mul_f32 v[156:157], v[156:157], v[18:19]\nv_pk_mul_f32 v[160:161], v[160:161], v[22:23]\nv_pk_fma_f32 v[156:157], v[158:159], v[16:17], v[156:157]\n"
      "v_pk_fma_f32 v[160:161], v[162:163], v[20:21], v[160:161]\nv_pk_mul_f32 v[16:17], v[16:17], v[18:19]\nv_pk_mul_f32 v[20:21], v[20:21], v[22:23]\nv_pk_mul_f32 v[18:19], v[16:17], v[20:21]\n"
      "v_pk_mul_f32 v[24:25], v[164:165], v[164:165]\nv_pk_mul_f32 v[156:157], v[156:157], v[20:21]\nv_pk_mul_f32 v[26:27], v[166:167], v[166:167]\nv_rcp_f32 v18, v18\n"
      "v_pk_mul_f32 v[28:29], v[168:169], v[168:169]\nv_rcp_f32 v19, v19\nv_pk_mul_f32 v[30:31], v[170:171], v[170:171]\nv_pk_fma_f32 v[156:157], v[160:161], v[16:17], v[156:157]\n"
      "v_pk_fma_f32 v[24:25], v[24:25], s[58:59], s[58:59] op_sel:[0,0,1] op_sel_hi:[1,0,1]\nv_pk_mul_f32 v[32:33], v[156:157], v[18:19]\n"
      "v_pk_fma_f32 v[26:27], v[26:27], s[58:59], s[58:59] op_sel:[0,0,1] op_sel_hi:[1,0,1]\nv_pk_fma_f32 v[28:29], v[28:29], s[58:59], s[58:59] op_sel:[0,0,1] op_sel_hi:[1,0,1]\n"
      "v_pk_fma_f32 v[30:31], v[30:31], s[58:59], s[58:59] op_sel:[0,0,1] op_sel_hi:[1,0,1]\nv_pk_fma_f32 v[24:25], v[24:25], v[164:165], s[60:61] op_sel_hi:[1,1,0]\n"
      "ds_read_b128 v[156:159], v252 offset:21504\nv_pk_fma_f32 v[26:27], v[26:27], v[166:167], s[60:61] op_sel_hi:[1,1,0]\nds_read_b128 v[160:163], v252 offset:22528\n"
      "v_pk_fma_f32 v[28:29], v[28:29], v[168:169], s[60:61] op_sel_hi:[1,1,0]\nv_pk_fma_f32 v[30:31], v[30:31], v[170:171], s[60:61] op_sel_hi:[1,1,0]\n"
      "v_pk_mul_f32 v[164:165], v[164:165], v[8:9]\nv_pk_mul_f32 v[166:167], v[166:167], v[10:11]\nv_pk_mul_f32 v[168:169], v[168:169], v[12:13]\nv_pk_mul_f32 v[170:171], v[170:171], v[14:15]\n"
      "v_exp_f32 v24, v24 clamp\nv_exp_f32 v25, v25 clamp\nv_exp_f32 v26, v26 clamp\nv_exp_f32 v27, v27 clamp\nv_exp_f32 v28, v28 clamp\nv_exp_f32 v29, v29 clamp\nv_exp_f32 v30, v30 clamp\n"
      "v_exp_f32 v31, v31 clamp\nv_pk_add_f32 v[24:25], v[24:25], s[60:61] op_sel:[0,1] op_sel_hi:[1,1]\nv_pk_add_f32 v[26:27], v[26:27], s[60:61] op_sel:[0,1] op_sel_hi:[1,1]\n"
      "v_pk_add_f32 v[28:29], v[28:29], s[60:61] op_sel:[0,1] op_sel_hi:[1,1]\nv_pk_add_f32 v[30:31], v[30:31], s[60:61] op_sel:[0,1] op_sel_hi:[1,1]\n"
      "v_pk_mul_f32 v[164:165], v[164:165], v[26:27]\nv_pk_mul_f32 v[168:169], v[168:169], v[30:31]\nv_pk_fma_f32 v[164:165], v[166:167], v[24:25], v[164:165]\n"
      "v_pk_fma_f32 v[168:169], v[170:171], v[28:29], v[168:169]\nv_pk_mul_f32 v[24:25], v[24:25], v[26:27]\nv_pk_mul_f32 v[28:29], v[28:29], v[30:31]\nv_pk_mul_f32 v[26:27], v[24:25], v[28:29]\n"
      "v_pk_mul_f32 v[16:17], v[172:173], v[172:173]\nv_pk_mul_f32 v[164:165], v[164:165], v[28:29]\nv_pk_mul_f32 v[18:19], v[174:175], v[174:175]\nv_rcp_f32 v26, v26\n"
      "v_pk_mul_f32 v[20:21], v[176:177], v[176:177]\nv_rcp_f32 v27, v27\nv_pk_mul_f32 v[22:23], v[178:179], v[178:179]\nv_pk_fma_f32 v[164:165], v[168:169], v[24:25], v[164:165]\n"
      "v_pk_fma_f32 v[16:17], v[16:17], s[58:59], s[58:59] op_sel:[0,0,1] op_sel_hi:[1,0,1]\nv_pk_fma_f32 v[32:33], v[164:165], v[26:27], v[32:33]\n"
      "v_pk_fma_f32 v[18:19], v[18:19], s[58:59], s[58:59] op_sel:[0,0,1] op_sel_hi:[1,0,1]\nv_add_f32 v38, v32, v33\n"
      "v_pk_fma_f32 v[20:21], v[20:21], s[58:59], s[58:59] op_sel:[0,0,1] op_sel_hi:[1,0,1]\nv_pk_fma_f32 v[22:23], v[22:23], s[58:59], s[58:59] op_sel:[0,0,1] op_sel_hi:[1,0,1]\n"
      "v_pk_fma_f32 v[16:17], v[16:17], v[172:173], s[60:61] op_sel_hi:[1,1,0]\nds_read_b128 v[164:167], v252 offset:23552\nv_pk_fma_f32 v[18:19], v[18:19], v[174:175], s[60:61] op_sel_hi:[1,1,0]\n"
      "ds_read_b128 v[168:171], v252 offset:24576\nv_pk_fma_f32 v[20:21], v[20:21], v[176:177], s[60:61] op_sel_hi:[1,1,0]\nv_pk_fma_f32 v[22:23], v[22:23], v[178:179], s[60:61] op_sel_hi:[1,1,0]\n"
      "v_pk_mul_f32 v[172:173], v[172:173], v[0:1]\nv_pk_mul_f32 v[174:175], v[174:175], v[2:3]\nv_pk_mul_f32 v[176:177], v[176:177], v[4:5]\nv_pk_mul_f32 v[178:179], v[178:179], v[6:7]\n"
      "v_exp_f32 v16, v16 clamp\nv_exp_f32 v17, v17 clamp\nv_exp_f32 v18, v18 clamp\nv_exp_f32 v19, v19 clamp\nv_exp_f32 v20, v20 clamp\nv_exp_f32 v21, v21 clamp\nv_exp_f32 v22, v22 clamp\n"
      "v_exp_f32 v23, v23 clamp\nv_pk_add_f32 v[16:17], v[16:17], s[60:61] op_sel:[0,1] op_sel_hi:[1,1]\nv_pk_add_f32 v[18:19], v[18:19], s[60:61] op_sel:[0,1] op_sel_hi:[1,1]\n"
      "v_pk_add_f32 v[20:21], v[20:21], s[60:61] op_sel:[0,1] op_sel_hi:[1,1]\nv_pk_add_f32 v[22:23], v[22:23], s[60:61] op_sel:[0,1] op_sel_hi:[1,1]\n"
      "v_pk_mul_f32 v[172:173], v[172:173], v[18:19]\nv_pk_mul_f32 v[176:177], v[176:177], v[22:23]\nv_pk_fma_f32 v[172:173], v[174:175], v[16:17], v[172:173]\n"
      "v_pk_fma_f32 v[176:177], v[178:179], v[20:21], v[176:177]\nv_pk_mul_f32 v[16:17], v[16:17], v[18:19]\nv_pk_mul_f32 v[20:21], v[20:21], v[22:23]\nv_pk_mul_f32 v[18:19], v[16:17], v[20:21]\n"
      "v_pk_mul_f32 v[24:25], v[180:181], v[180:181]\nv_pk_mul_f32 v[172:173], v[172:173], v[20:21]\nv_pk_mul_f32 v[26:27], v[182:183], v[182:183]\nv_rcp_f32 v18, v18\n"
      "v_pk_mul_f32 v[28:29], v[184:185], v[184:185]\nv_rcp_f32 v19, v19\nv_pk_mul_f32 v[30:31], v[186:187], v[186:187]\nv_pk_fma_f32 v[172:173], v[176:177], v[16:17], v[172:173]\n"
      "v_pk_fma_f32 v[24:25], v[24:25], s[58:59], s[58:59] op_sel:[0,0,1] op_sel_hi:[1,0,1]\nv_pk_mul_f32 v[32:33], v[172:173], v[18:19]\n"
      "v_pk_fma_f32 v[26:27], v[26:27], s[58:59], s[58:59] op_sel:[0,0,1] op_sel_hi:[1,0,1]\nv_pk_fma_f32 v[28:29], v[28:29], s[58:59], s[58:59] op_sel:[0,0,1] op_sel_hi:[1,0,1]\n"
      "v_pk_fma_f32 v[30:31], v[30:31], s[58:59], s[58:59] op_sel:[0,0,1] op_sel_hi:[1,0,1]\nv_pk_fma_f32 v[24:25], v[24:25], v[180:181], s[60:61] op_sel_hi:[1,1,0]\n"
      "ds_read_b128 v[172:175], v252 offset:25600\nv_pk_fma_f32 v[26:27], v[26:27], v[182:183], s[60:61] op_sel_hi:[1,1,0]\nds_read_b128 v[176:179], v252 offset:26624\n"
      "v_pk_fma_f32 v[28:29], v[28:29], v[184:185], s[60:61] op_sel_hi:[1,1,0]\nv_pk_fma_f32 v[30:31], v[30:31], v[186:187], s[60:61] op_sel_hi:[1,1,0]\n"
      "v_pk_mul_f32 v[180:181], v[180:181], v[8:9]\nv_pk_mul_f32 v[182:183], v[182:183], v[10:11]\nv_pk_mul_f32 v[184:185], v[184:185], v[12:13]\nv_pk_mul_f32 v[186:187], v[186:187], v[14:15]\n"
      "v_exp_f32 v24, v24 clamp\nv_exp_f32 v25, v25 clamp\nv_exp_f32 v26, v26 clamp\nv_exp_f32 v27, v27 clamp\nv_exp_f32 v28, v28 clamp\nv_exp_f32 v29, v29 clamp\nv_exp_f32 v30, v30 clamp\n"
      "v_exp_f32 v31, v31 clamp\nv_pk_add_f32 v[24:25], v[24:25], s[60:61] op_sel:[0,1] op_sel_hi:[1,1]\nv_pk_add_f32 v[26:27], v[26:27], s[60:61] op_sel:[0,1] op_sel_hi:[1,1]\n"
      "v_pk_add_f32 v[28:29], v[28:29], s[60:61] op_sel:[0,1] op_sel_hi:[1,1]\nv_pk_add_f32 v[30:31], v[30:31], s[60:61] op_sel:[0,1] op_sel_hi:[1,1]\n"
      "v_pk_mul_f32 v[180:181], v[180:181], v[26:27]\nv_pk_mul_f32 v[184:185], v[184:185], v[30:31]\nv_pk_fma_f32 v[180:181], v[182:183], v[24:25], v[180:181]\n"
      "v_pk_fma_f32 v[184:185], v[186:187], v[28:29], v[184:185]\nv_pk_mul_f32 v[24:25], v[24:25], v[26:27]\nv_pk_mul_f32 v[28:29], v[28:29], v[30:31]\nv_pk_mul_f32 v[26:27], v[24:25], v[28:29]\n"
      "v_pk_mul_f32 v[16:17], v[188:189], v[188:189]\nv_pk_mul_f32 v[180:181], v[180:181], v[28:29]\nv_pk_mul_f32 v[18:19], v[190:191], v[190:191]\nv_rcp_f32 v26, v26\n"
      "v_pk_mul_f32 v[20:21], v[192:193], v[192:193]\nv_rcp_f32 v27, v27\nv_pk_mul_f32 v[22:23], v[194:195], v[194:195]\nv_pk_fma_f32 v[180:181], v[184:185], v[24:25], v[180:181]\n"
      "v_pk_fma_f32 v[16:17], v[16:17], s[58:59], s[58:59] op_sel:[0,0,1] op_sel_hi:[1,0,1]\nv_pk_fma_f32 v[32:33], v[180:181], v[26:27], v[32:33]\n"
      "v_pk_fma_f32 v[18:19], v[18:19], s[58:59], s[58:59] op_sel:[0,0,1] op_sel_hi:[1,0,1]\nv_add_f32 v39, v32, v33\n"
      "v_pk_fma_f32 v[20:21], v[20:21], s[58:59], s[58:59] op_sel:[0,0,1] op_sel_hi:[1,0,1]\nv_pk_fma_f32 v[22:23], v[22:23], s[58:59], s[58:59] op_sel:[0,0,1] op_sel_hi:[1,0,1]\n"
      "v_pk_fma_f32 v[16:17], v[16:17], v[188:189], s[60:61] op_sel_hi:[1,1,0]\nds_read_b128 v[180:183], v252 offset:27648\nv_pk_fma_f32 v[18:19], v[18:19], v[190:191], s[60:61] op_sel_hi:[1,1,0]\n"
      "ds_read_b128 v[184:187], v252 offset:28672\nv_pk_fma_f32 v[20:21], v[20:21], v[192:193], s[60:61] op_sel_hi:[1,1,0]\nv_pk_fma_f32 v[22:23], v[22:23], v[194:195], s[60:61] op_sel_hi:[1,1,0]\n"
      "v_pk_mul_f32 v[188:189], v[188:189], v[0:1]\nv_pk_mul_f32 v[190:191], v[190:191], v[2:3]\nv_pk_mul_f32 v[192:193], v[192:193], v[4:5]\nv_pk_mul_f32 v[194:195], v[194:195], v[6:7]\n"
      "v_exp_f32 v16, v16 clamp\nv_exp_f32 v17, v17 clamp\nv_exp_f32 v18, v18 clamp\nv_exp_f32 v19, v19 clamp\nv_exp_f32 v20, v20 clamp\nv_exp_f32 v21, v21 clamp\nv_exp_f32 v22, v22 clamp\n"
      "v_exp_f32 v23, v23 clamp\nv_pk_add_f32 v[16:17], v[16:17], s[60:61] op_sel:[0,1] op_sel_hi:[1,1]\nv_pk_add_f32 v[18:19], v[18:19], s[60:61] op_sel:[0,1] op_sel_hi:[1,1]\n"
      "v_pk_add_f32 v[20:21], v[20:21], s[60:61] op_sel:[0,1] op_sel_hi:[1,1]\nv_pk_add_f32 v[22:23], v[22:23], s[60:61] op_sel:[0,1] op_sel_hi:[1,1]\n"
      "v_pk_mul_f32 v[188:189], v[188:189], v[18:19]\nv_pk_mul_f32 v[192:193], v[192:193], v[22:23]\nv_pk_fma_f32 v[188:189], v[190:191], v[16:17], v[188:189]\n"
      "v_pk_fma_f32 v[192:193], v[194:195], v[20:21], v[192:193]\nv_pk_mul_f32 v[16:17], v[16:17], v[18:19]\nv_pk_mul_f32 v[20:21], v[20:21], v[22:23]\nv_pk_mul_f32 v[18:19], v[16:17], v[20:21]\n"
      "v_pk_mul_f32 v[24:25], v[196:197], v[196:197]\nv_pk_mul_f32 v[188:189], v[188:189], v[20:21]\nv_pk_mul_f32 v[26:27], v[198:199], v[198:199]\nv_rcp_f32 v18, v18\n"
      "v_pk_mul_f32 v[28:29], v[200:201], v[200:201]\nv_rcp_f32 v19, v19\nv_pk_mul_f32 v[30:31], v[202:203], v[202:203]\nv_pk_fma_f32 v[188:189], v[192:193], v[16:17], v[188:189]\n"
      "v_pk_fma_f32 v[24:25], v[24:25], s[58:59], s[58:59] op_sel:[0,0,1] op_sel_hi:[1,0,1]\nv_pk_mul_f32 v[32:33], v[188:189], v[18:19]\n"
      "v_pk_fma_f32 v[26:27], v[26:27], s[58:59], s[58:59] op_sel:[0,0,1] op_sel_hi:[1,0,1]\nv_pk_fma_f32 v[28:29], v[28:29], s[58:59], s[58:59] op_sel:[0,0,1] op_sel_hi:[1,0,1]\n"
      "v_pk_fma_f32 v[30:31], v[30:31], s[58:59], s[58:59] op_sel:[0,0,1] op_sel_hi:[1,0,1]\nv_pk_fma_f32 v[24:25], v[24:25], v[196:197], s[60:61] op_sel_hi:[1,1,0]\n"
      "ds_read_b128 v[188:191], v252 offset:29696\nv_pk_fma_f32 v[26:27], v[26:27], v[198:199], s[60:61] op_sel_hi:[1,1,0]\nds_read_b128 v[192:195], v252 offset:30720\n"
      "v_pk_fma_f32 v[28:29], v[28:29], v[200:201], s[60:61] op_sel_hi:[1,1,0]\nv_pk_fma_f32 v[30:31], v[30:31], v[202:203], s[60:61] op_sel_hi:[1,1,0]\n"
      "v_pk_mul_f32 v[196:197], v[196:197], v[8:9]\nv_pk_mul_f32 v[198:199], v[198:199], v[10:11]\nv_pk_mul_f32 v[200:201], v[200:201], v[12:13]\nv_pk_mul_f32 v[202:203], v[202:203], v[14:15]\n"
      "v_exp_f32 v24, v24 clamp\nv_exp_f32 v25, v25 clamp\nv_exp_f32 v26, v26 clamp\nv_exp_f32 v27, v27 clamp\nv_exp_f32 v28, v28 clamp\nv_exp_f32 v29, v29 clamp\nv_exp_f32 v30, v30 clamp\n"
      "v_exp_f32 v31, v31 clamp\nv_pk_add_f32 v[24:25], v[24:25], s[60:61] op_sel:[0,1] op_sel_hi:[1,1]\nv_pk_add_f32 v[26:27], v[26:27], s[60:61] op_sel:[0,1] op_sel_hi:[1,1]\n"
      "v_pk_add_f32 v[28:29], v[28:29], s[60:61] op_sel:[0,1] op_sel_hi:[1,1]\nv_pk_add_f32 v[30:31], v[30:31], s[60:61] op_sel:[0,1] op_sel_hi:[1,1]\n"
      "v_pk_mul_f32 v[196:197], v[196:197], v[26:27]\nv_pk_mul_f32 v[200:201], v[200:201], v[30:31]\nv_pk_fma_f32 v[196:197], v[198:199], v[24:25], v[196:197]\n"
      "v_pk_fma_f32 v[200:201], v[202:203], v[28:29], v[200:201]\nv_pk_mul_f32 v[24:25], v[24:25], v[26:27]\nv_pk_mul_f32 v[28:29], v[28:29], v[30:31]\nv_pk_mul_f32 v[26:27], v[24:25], v[28:29]\n"
      "v_pk_mul_f32 v[16:17], v[204:205], v[204:205]\nv_pk_mul_f32 v[196:197], v[196:197], v[28:29]\nv_pk_mul_f32 v[18:19], v[206:207], v[206:207]\nv_rcp_f32 v26, v26\n"
      "v_pk_mul_f32 v[20:21], v[208:209], v[208:209]\nv_rcp_f32 v27, v27\nv_pk_mul_f32 v[22:23], v[210:211], v[210:211]\nv_pk_fma_f32 v[196:197], v[200:201], v[24:25], v[196:197]\n"
      "v_pk_fma_f32 v[16:17], v[16:17], s[58:59], s[58:59] op_sel:[0,0,1] op_sel_hi:[1,0,1]\nv_pk_fma_f32 v[32:33], v[196:197], v[26:27], v[32:33]\n"
      "v_pk_fma_f32 v[18:19], v[18:19], s[58:59], s[58:59] op_sel:[0,0,1] op_sel_hi:[1,0,1]\nv_add_f32 v40, v32, v33\n"
      "v_pk_fma_f32 v[20:21], v[20:21], s[58:59], s[58:59] op_sel:[0,0,1] op_sel_hi:[1,0,1]\nv_pk_fma_f32 v[22:23], v[22:23], s[58:59], s[58:59] op_sel:[0,0,1] op_sel_hi:[1,0,1]\n"
      "v_pk_fma_f32 v[16:17], v[16:17], v[204:205], s[60:61] op_sel_hi:[1,1,0]\nds_read_b128 v[196:199], v252 offset:31744\nv_pk_fma_f32 v[18:19], v[18:19], v[206:207], s[60:61] op_sel_hi:[1,1,0]\n"
      "ds_read_b128 v[200:203], v252 offset:32768\nv_pk_fma_f32 v[20:21], v[20:21], v[208:209], s[60:61] op_sel_hi:[1,1,0]\nv_pk_fma_f32 v[22:23], v[22:23], v[210:211], s[60:61] op_sel_hi:[1,1,0]\n"
      "v_pk_mul_f32 v[204:205], v[204:205], v[0:1]\nv_pk_mul_f32 v[206:207], v[206:207], v[2:3]\nv_pk_mul_f32 v[208:209], v[208:209], v[4:5]\nv_pk_mul_f32 v[210:211], v[210:211], v[6:7]\n"
      "v_exp_f32 v16, v16 clamp\nv_exp_f32 v17, v17 clamp\nv_exp_f32 v18, v18 clamp\nv_exp_f32 v19, v19 clamp\nv_exp_f32 v20, v20 clamp\nv_exp_f32 v21, v21 clamp\nv_exp_f32 v22, v22 clamp\n"
      "v_exp_f32 v23, v23 clamp\nv_pk_add_f32 v[16:17], v[16:17], s[60:61] op_sel:[0,1] op_sel_hi:[1,1]\nv_pk_add_f32 v[18:19], v[18:19], s[60:61] op_sel:[0,1] op_sel_hi:[1,1]\n"
      "v_pk_add_f32 v[20:21], v[20:21], s[60:61] op_sel:[0,1] op_sel_hi:[1,1]\nv_pk_add_f32 v[22:23], v[22:23], s[60:61] op_sel:[0,1] op_sel_hi:[1,1]\n"
      "v_pk_mul_f32 v[204:205], v[204:205], v[18:19]\nv_pk_mul_f32 v[208:209], v[208:209], v[22:23]\nv_pk_fma_f32 v[204:205], v[206:207], v[16:17], v[204:205]\n"
      "v_pk_fma_f32 v[208:209], v[210:211], v[20:21], v[208:209]\nv_pk_mul_f32 v[16:17], v[16:17], v[18:19]\nv_pk_mul_f32 v[20:21], v[20:21], v[22:23]\nv_pk_mul_f32 v[18:19], v[16:17], v[20:21]\n"
      "v_pk_mul_f32 v[24:25], v[212:213], v[212:213]\nv_pk_mul_f32 v[204:205], v[204:205], v[20:21]\nv_pk_mul_f32 v[26:27], v[214:215], v[214:215]\nv_rcp_f32 v18, v18\n"
      "v_pk_mul_f32 v[28:29], v[216:217], v[216:217]\nv_rcp_f32 v19, v19\nv_pk_mul_f32 v[30:31], v[218:219], v[218:219]\nv_pk_fma_f32 v[204:205], v[208:209], v[16:17], v[204:205]\n"
      "v_pk_fma_f32 v[24:25], v[24:25], s[58:59], s[58:59] op_sel:[0,0,1] op_sel_hi:[1,0,1]\nv_pk_mul_f32 v[32:33], v[204:205], v[18:19]\n"
      "v_pk_fma_f32 v[26:27], v[26:27], s[58:59], s[58:59] op_sel:[0,0,1] op_sel_hi:[1,0,1]\nv_pk_fma_f32 v[28:29], v[28:29], s[58:59], s[58:59] op_sel:[0,0,1] op_sel_hi:[1,0,1]\n"
      "v_pk_fma_f32 v[30:31], v[30:31], s[58:59], s[58:59] op_sel:[0,0,1] op_sel_hi:[1,0,1]\nv_pk_fma_f32 v[24:25], v[24:25], v[212:213], s[60:61] op_sel_hi:[1,1,0]\n"
      "ds_read_b128 v[204:207], v252 offset:33792\nv_pk_fma_f32 v[26:27], v[26:27], v[214:215], s[60:61] op_sel_hi:[1,1,0]\nds_read_b128 v[208:211], v252 offset:34816\n"
      "v_pk_fma_f32 v[28:29], v[28:29], v[216:217], s[60:61] op_sel_hi:[1,1,0]\nv_pk_fma_f32 v[30:31], v[30:31], v[218:219], s[60:61] op_sel_hi:[1,1,0]\n"
      "v_pk_mul_f32 v[212:213], v[212:213], v[8:9]\nv_pk_mul_f32 v[214:215], v[214:215], v[10:11]\nv_pk_mul_f32 v[216:217], v[216:217], v[12:13]\nv_pk_mul_f32 v[218:219], v[218:219], v[14:15]\n"
      "v_exp_f32 v24, v24 clamp\nv_exp_f32 v25, v25 clamp\nv_exp_f32 v26, v26 clamp\nv_exp_f32 v27, v27 clamp\nv_exp_f32 v28, v28 clamp\nv_exp_f32 v29, v29 clamp\nv_exp_f32 v30, v30 clamp\n"
      "v_exp_f32 v31, v31 clamp\nv_pk_add_f32 v[24:25], v[24:25], s[60:61] op_sel:[0,1] op_sel_hi:[1,1]\nv_pk_add_f32 v[26:27], v[26:27], s[60:61] op_sel:[0,1] op_sel_hi:[1,1]\n"
      "v_pk_add_f32 v[28:29], v[28:29], s[60:61] op_sel:[0,1] op_sel_hi:[1,1]\nv_pk_add_f32 v[30:31], v[30:31], s[60:61] op_sel:[0,1] op_sel_hi:[1,1]\n"
      "v_pk_mul_f32 v[212:213], v[212:213], v[26:27]\nv_pk_mul_f32 v[216:217], v[216:217], v[30:31]\nv_pk_fma_f32 v[212:213], v[214:215], v[24:25], v[212:213]\n"
      "v_pk_fma_f32 v[216:217], v[218:219], v[28:29], v[216:217]\nv_pk_mul_f32 v[24:25], v[24:25], v[26:27]\nv_pk_mul_f32 v[28:29], v[28:29], v[30:31]\nv_pk_mul_f32 v[26:27], v[24:25], v[28:29]\n"
      "v_pk_mul_f32 v[212:213], v[212:213], v[28:29]\nv_rcp_f32 v26, v26\nv_rcp_f32 v27, v27\nv_pk_fma_f32 v[212:213], v[216:217], v[24:25], v[212:213]\n"
      "v_pk_fma_f32 v[32:33], v[212:213], v[26:27], v[32:33]\nv_add_f32 v41, v32, v33\ns_barrier\nds_read_b128 v[212:215], v252 offset:35840\nds_read_b128 v[216:219], v252 offset:36864\n"
      "s_branch .Lge\n"
      ".Lg13:\ns_barrier\nds_read_b128 v[0:3], v102 offset:0\nds_read_b128 v[4:7], v103 offset:0\nds_read_b64_tr_b16 v[64:65], v108 offset:0\nds_read_b64_tr_b16 v[66:67], v108 offset:1024\n"
      "ds_read_b64_tr_b16 v[68:69], v108 offset:8192\nds_read_b64_tr_b16 v[70:71], v108 offset:9216\nds_read_b64_tr_b16 v[72:73], v109 offset:0\nds_read_b64_tr_b16 v[74:75], v109 offset:1024\n"
      "ds_read_b64_tr_b16 v[76:77], v109 offset:8192\nds_read_b64_tr_b16 v[78:79], v109 offset:9216\nds_read_b64_tr_b16 v[80:81], v110 offset:0\nds_read_b64_tr_b16 v[82:83], v110 offset:1024\n"
      "ds_read_b64_tr_b16 v[84:85], v110 offset:8192\nds_read_b64_tr_b16 v[86:87], v110 offset:9216\nds_read_b64_tr_b16 v[88:89], v111 offset:0\nds_read_b64_tr_b16 v[90:91], v111 offset:1024\n"
      "ds_read_b64_tr_b16 v[92:93], v111 offset:8192\nds_read_b64_tr_b16 v[94:95], v111 offset:9216\nds_read_b128 v[8:11], v98 offset:0\nds_read_b128 v[12:15], v99 offset:0\ns_waitcnt lgkmcnt(0)\n"
      "ds_read_b128 v[16:19], v98 offset:2048\nds_read_b128 v[20:23], v99 offset:2048\n.p2align 3\n"
      ".Lgl13:\nv_mfma_f32_16x16x32_bf16 v[124:127], v[64:67], v[0:3], v[124:127]\nds_read_b128 v[24:27], v98 offset:4096\nv_mfma_f32_16x16x32_bf16 v[124:127], v[68:71], v[4:7], v[124:127]\n"
      "ds_read_b128 v[28:31], v99 offset:4096\nv_mfma_f32_16x16x32_bf16 v[128:131], v[76:79], v[4:7], v[128:131]\nv_mfma_f32_16x16x32_bf16 v[128:131], v[72:75], v[0:3], v[128:131]\n"
      "v_mfma_f32_16x16x32_bf16 v[152:155], v[88:91], v[8:11], v[152:155]\nds_read_b128 v[0:3], v98 offset:6144\nv_mfma_f32_16x16x32_bf16 v[152:155], v[92:95], v[12:15], v[152:155]\n"
      "ds_read_b128 v[4:7], v99 offset:6144\nv_mfma_f32_16x16x32_bf16 v[148:151], v[84:87], v[12:15], v[148:151]\nv_mfma_f32_16x16x32_bf16 v[148:151], v[80:83], v[8:11], v[148:151]\n"
      "v_mfma_f32_16x16x32_bf16 v[144:147], v[72:75], v[8:11], v[144:147]\nv_mfma_f32_16x16x32_bf16 v[144:147], v[76:79], v[12:15], v[144:147]\n"
      "v_mfma_f32_16x16x32_bf16 v[140:143], v[68:71], v[12:15], v[140:143]\ns_waitcnt lgkmcnt(4)\nv_mfma_f32_16x16x32_bf16 v[140:143], v[64:67], v[8:11], v[140:143]\n"
      "v_mfma_f32_16x16x32_bf16 v[156:159], v[64:67], v[16:19], v[156:159]\nds_read_b128 v[8:11], v98 offset:8192\nv_mfma_f32_16x16x32_bf16 v[156:159], v[68:71], v[20:23], v[156:159]\n"
      "ds_read_b128 v[12:15], v99 offset:8192\nv_mfma_f32_16x16x32_bf16 v[160:163], v[76:79], v[20:23], v[160:163]\nv_mfma_f32_16x16x32_bf16 v[160:163], v[72:75], v[16:19], v[160:163]\n"
      "v_mfma_f32_16x16x32_bf16 v[164:167], v[80:83], v[16:19], v[164:167]\nv_mfma_f32_16x16x32_bf16 v[164:167], v[84:87], v[20:23], v[164:167]\n"
      "v_mfma_f32_16x16x32_bf16 v[168:171], v[92:95], v[20:23], v[168:171]\ns_waitcnt lgkmcnt(2)\nv_mfma_f32_16x16x32_bf16 v[168:171], v[88:91], v[16:19], v[168:171]\n"
      "v_mfma_f32_16x16x32_bf16 v[184:187], v[88:91], v[24:27], v[184:187]\nds_read_b128 v[16:19], v98 offset:10240\nv_mfma_f32_16x16x32_bf16 v[184:187], v[92:95], v[28:31], v[184:187]\n"
      "ds_read_b128 v[20:23], v99 offset:10240\nv_mfma_f32_16x16x32_bf16 v[180:183], v[84:87], v[28:31], v[180:183]\nv_mfma_f32_16x16x32_bf16 v[180:183], v[80:83], v[24:27], v[180:183]\n"
      "v_mfma_f32_16x16x32_bf16 v[176:179], v[72:75], v[24:27], v[176:179]\nv_mfma_f32_16x16x32_bf16 v[176:179], v[76:79], v[28:31], v[176:179]\n"
      "v_mfma_f32_16x16x32_bf16 v[172:175], v[68:71], v[28:31], v[172:175]\ns_waitcnt vmcnt(0) lgkmcnt(0)\ns_barrier\nv_mfma_f32_16x16x32_bf16 v[172:175], v[64:67], v[24:27], v[172:175]\n"
      "ds_read_b64_tr_b16 v[32:33], v104 offset:0\nv_mfma_f32_16x16x32_bf16 v[188:191], v[64:67], v[0:3], v[188:191]\nds_read_b64_tr_b16 v[34:35], v104 offset:1024\n"
      "v_mfma_f32_16x16x32_bf16 v[188:191], v[68:71], v[4:7], v[188:191]\nds_read_b64_tr_b16 v[36:37], v104 offset:8192\nv_mfma_f32_16x16x32_bf16 v[192:195], v[76:79], v[4:7], v[192:195]\n"
      "ds_read_b64_tr_b16 v[38:39], v104 offset:9216\nv_mfma_f32_16x16x32_bf16 v[192:195], v[72:75], v[0:3], v[192:195]\nds_read_b64_tr_b16 v[40:41], v105 offset:0\n"
      "v_mfma_f32_16x16x32_bf16 v[196:199], v[80:83], v[0:3], v[196:199]\nds_read_b64_tr_b16 v[42:43], v105 offset:1024\nv_mfma_f32_16x16x32_bf16 v[196:199], v[84:87], v[4:7], v[196:199]\n"
      "ds_read_b64_tr_b16 v[44:45], v105 offset:8192\nv_mfma_f32_16x16x32_bf16 v[200:203], v[92:95], v[4:7], v[200:203]\nds_read_b64_tr_b16 v[46:47], v105 offset:9216\n"
      "v_mfma_f32_16x16x32_bf16 v[200:203], v[88:91], v[0:3], v[200:203]\nds_read_b64_tr_b16 v[48:49], v106 offset:0\nv_mfma_f32_16x16x32_bf16 v[216:219], v[88:91], v[8:11], v[216:219]\n"
      "ds_read_b128 v[0:3], v100 offset:0\nv_mfma_f32_16x16x32_bf16 v[216:219], v[92:95], v[12:15], v[216:219]\nds_read_b128 v[4:7], v101 offset:0\n"
      "v_mfma_f32_16x16x32_bf16 v[212:215], v[84:87], v[12:15], v[212:215]\nds_read_b64_tr_b16 v[50:51], v106 offset:1024\nv_mfma_f32_16x16x32_bf16 v[212:215], v[80:83], v[8:11], v[212:215]\n"
      "ds_read_b64_tr_b16 v[52:53], v106 offset:8192\nv_mfma_f32_16x16x32_bf16 v[208:211], v[72:75], v[8:11], v[208:211]\nds_read_b64_tr_b16 v[54:55], v106 offset:9216\n"
      "v_mfma_f32_16x16x32_bf16 v[208:211], v[76:79], v[12:15], v[208:211]\nds_read_b64_tr_b16 v[56:57], v107 offset:0\nv_mfma_f32_16x16x32_bf16 v[204:207], v[68:71], v[12:15], v[204:207]\n"
      "ds_read_b64_tr_b16 v[58:59], v107 offset:1024\nv_mfma_f32_16x16x32_bf16 v[204:207], v[64:67], v[8:11], v[204:207]\nds_read_b64_tr_b16 v[60:61], v107 offset:8192\n"
      "v_mfma_f32_16x16x32_bf16 v[220:223], v[64:67], v[16:19], v[220:223]\nds_read_b128 v[8:11], v96 offset:0\nv_mfma_f32_16x16x32_bf16 v[220:223], v[68:71], v[20:23], v[220:223]\n"
      "ds_read_b128 v[12:15], v97 offset:0\nv_mfma_f32_16x16x32_bf16 v[224:227], v[76:79], v[20:23], v[224:227]\nds_read_b64_tr_b16 v[62:63], v107 offset:9216\n"
      "v_mfma_f32_16x16x32_bf16 v[224:227], v[72:75], v[16:19], v[224:227]\nv_mfma_f32_16x16x32_bf16 v[228:231], v[80:83], v[16:19], v[228:231]\n"
      "v_mfma_f32_16x16x32_bf16 v[228:231], v[84:87], v[20:23], v[228:231]\nv_mfma_f32_16x16x32_bf16 v[232:235], v[92:95], v[20:23], v[232:235]\ns_waitcnt lgkmcnt(0)\n"
      "v_mfma_f32_16x16x32_bf16 v[232:235], v[88:91], v[16:19], v[232:235]\nds_read_b128 v[16:19], v96 offset:2048\nds_read_b128 v[20:23], v97 offset:2048\n"
      "v_mfma_f32_16x16x32_bf16 v[124:127], v[32:35], v[0:3], v[124:127]\nds_read_b128 v[24:27], v96 offset:4096\nv_mfma_f32_16x16x32_bf16 v[124:127], v[36:39], v[4:7], v[124:127]\n"
      "ds_read_b128 v[28:31], v97 offset:4096\nv_mfma_f32_16x16x32_bf16 v[128:131], v[44:47], v[4:7], v[128:131]\nv_mfma_f32_16x16x32_bf16 v[128:131], v[40:43], v[0:3], v[128:131]\n"
      "v_mfma_f32_16x16x32_bf16 v[152:155], v[56:59], v[8:11], v[152:155]\nds_read_b128 v[0:3], v96 offset:6144\nv_mfma_f32_16x16x32_bf16 v[152:155], v[60:63], v[12:15], v[152:155]\n"
      "ds_read_b128 v[4:7], v97 offset:6144\nv_mfma_f32_16x16x32_bf16 v[148:151], v[52:55], v[12:15], v[148:151]\nv_mfma_f32_16x16x32_bf16 v[148:151], v[48:51], v[8:11], v[148:151]\n"
      "v_mfma_f32_16x16x32_bf16 v[144:147], v[40:43], v[8:11], v[144:147]\nv_mfma_f32_16x16x32_bf16 v[144:147], v[44:47], v[12:15], v[144:147]\n"
      "v_mfma_f32_16x16x32_bf16 v[140:143], v[36:39], v[12:15], v[140:143]\ns_waitcnt lgkmcnt(4)\nv_mfma_f32_16x16x32_bf16 v[140:143], v[32:35], v[8:11], v[140:143]\n"
      "v_mfma_f32_16x16x32_bf16 v[156:159], v[32:35], v[16:19], v[156:159]\nds_read_b128 v[8:11], v96 offset:8192\nv_mfma_f32_16x16x32_bf16 v[156:159], v[36:39], v[20:23], v[156:159]\n"
      "ds_read_b128 v[12:15], v97 offset:8192\nv_mfma_f32_16x16x32_bf16 v[160:163], v[44:47], v[20:23], v[160:163]\nv_mfma_f32_16x16x32_bf16 v[160:163], v[40:43], v[16:19], v[160:163]\n"
      "v_mfma_f32_16x16x32_bf16 v[164:167], v[48:51], v[16:19], v[164:167]\nv_mfma_f32_16x16x32_bf16 v[164:167], v[52:55], v[20:23], v[164:167]\n"
      "v_mfma_f32_16x16x32_bf16 v[168:171], v[60:63], v[20:23], v[168:171]\ns_waitcnt lgkmcnt(2)\nv_mfma_f32_16x16x32_bf16 v[168:171], v[56:59], v[16:19], v[168:171]\n"
      "v_mfma_f32_16x16x32_bf16 v[184:187], v[56:59], v[24:27], v[184:187]\nds_read_b128 v[16:19], v96 offset:10240\nv_mfma_f32_16x16x32_bf16 v[184:187], v[60:63], v[28:31], v[184:187]\n"
      "ds_read_b128 v[20:23], v97 offset:10240\nv_mfma_f32_16x16x32_bf16 v[180:183], v[52:55], v[28:31], v[180:183]\nv_mfma_f32_16x16x32_bf16 v[180:183], v[48:51], v[24:27], v[180:183]\n"
      "v_mfma_f32_16x16x32_bf16 v[176:179], v[40:43], v[24:27], v[176:179]\nv_mfma_f32_16x16x32_bf16 v[176:179], v[44:47], v[28:31], v[176:179]\n"
      "v_mfma_f32_16x16x32_bf16 v[172:175], v[36:39], v[28:31], v[172:175]\ns_waitcnt vmcnt(0) lgkmcnt(0)\ns_barrier\nv_mfma_f32_16x16x32_bf16 v[172:175], v[32:35], v[24:27], v[172:175]\n"
      "ds_read_b64_tr_b16 v[64:65], v104 offset:45056\nv_mfma_f32_16x16x32_bf16 v[188:191], v[32:35], v[0:3], v[188:191]\nds_read_b64_tr_b16 v[66:67], v104 offset:46080\n"
      "v_mfma_f32_16x16x32_bf16 v[188:191], v[36:39], v[4:7], v[188:191]\nds_read_b64_tr_b16 v[68:69], v104 offset:53248\nv_mfma_f32_16x16x32_bf16 v[192:195], v[44:47], v[4:7], v[192:195]\n"
      "ds_read_b64_tr_b16 v[70:71], v104 offset:54272\nv_mfma_f32_16x16x32_bf16 v[192:195], v[40:43], v[0:3], v[192:195]\nds_read_b64_tr_b16 v[72:73], v105 offset:45056\n"
      "v_mfma_f32_16x16x32_bf16 v[196:199], v[48:51], v[0:3], v[196:199]\nds_read_b64_tr_b16 v[74:75], v105 offset:46080\nv_mfma_f32_16x16x32_bf16 v[196:199], v[52:55], v[4:7], v[196:199]\n"
      "ds_read_b64_tr_b16 v[76:77], v105 offset:53248\nv_mfma_f32_16x16x32_bf16 v[200:203], v[60:63], v[4:7], v[200:203]\nds_read_b64_tr_b16 v[78:79], v105 offset:54272\n"
      "v_mfma_f32_16x16x32_bf16 v[200:203], v[56:59], v[0:3], v[200:203]\nds_read_b64_tr_b16 v[80:81], v106 offset:45056\nv_mfma_f32_16x16x32_bf16 v[216:219], v[56:59], v[8:11], v[216:219]\n"
      "ds_read_b128 v[0:3], v100 offset:45056\nv_mfma_f32_16x16x32_bf16 v[216:219], v[60:63], v[12:15], v[216:219]\nds_read_b128 v[4:7], v101 offset:45056\n"
      "v_mfma_f32_16x16x32_bf16 v[212:215], v[52:55], v[12:15], v[212:215]\nds_read_b64_tr_b16 v[82:83], v106 offset:46080\nv_mfma_f32_16x16x32_bf16 v[212:215], v[48:51], v[8:11], v[212:215]\n"
      "ds_read_b64_tr_b16 v[84:85], v106 offset:53248\nv_mfma_f32_16x16x32_bf16 v[208:211], v[40:43], v[8:11], v[208:211]\nds_read_b64_tr_b16 v[86:87], v106 offset:54272\n"
      "v_mfma_f32_16x16x32_bf16 v[208:211], v[44:47], v[12:15], v[208:211]\nds_read_b64_tr_b16 v[88:89], v107 offset:45056\nv_mfma_f32_16x16x32_bf16 v[204:207], v[36:39], v[12:15], v[204:207]\n"
      "ds_read_b64_tr_b16 v[90:91], v107 offset:46080\nv_mfma_f32_16x16x32_bf16 v[204:207], v[32:35], v[8:11], v[204:207]\nds_read_b64_tr_b16 v[92:93], v107 offset:53248\n"
      "v_mfma_f32_16x16x32_bf16 v[220:223], v[32:35], v[16:19], v[220:223]\nds_read_b128 v[8:11], v96 offset:45056\nv_mfma_f32_16x16x32_bf16 v[220:223], v[36:39], v[20:23], v[220:223]\n"
      "ds_read_b128 v[12:15], v97 offset:45056\nv_mfma_f32_16x16x32_bf16 v[224:227], v[44:47], v[20:23], v[224:227]\nds_read_b64_tr_b16 v[94:95], v107 offset:54272\n"
      "v_mfma_f32_16x16x32_bf16 v[224:227], v[40:43], v[16:19], v[224:227]\nv_mfma_f32_16x16x32_bf16 v[228:231], v[48:51], v[16:19], v[228:231]\n"
      "v_mfma_f32_16x16x32_bf16 v[228:231], v[52:55], v[20:23], v[228:231]\nv_mfma_f32_16x16x32_bf16 v[232:235], v[60:63], v[20:23], v[232:235]\ns_waitcnt lgkmcnt(0)\n"
      "v_mfma_f32_16x16x32_bf16 v[232:235], v[56:59], v[16:19], v[232:235]\nds_read_b128 v[16:19], v96 offset:47104\nds_read_b128 v[20:23], v97 offset:47104\n"
      "v_mfma_f32_16x16x32_bf16 v[124:127], v[64:67], v[0:3], v[124:127]\nds_read_b128 v[24:27], v96 offset:49152\nv_mfma_f32_16x16x32_bf16 v[124:127], v[68:71], v[4:7], v[124:127]\n"
      "ds_read_b128 v[28:31], v97 offset:49152\nv_mfma_f32_16x16x32_bf16 v[128:131], v[76:79], v[4:7], v[128:131]\nv_mfma_f32_16x16x32_bf16 v[128:131], v[72:75], v[0:3], v[128:131]\n"
      "v_mfma_f32_16x16x32_bf16 v[152:155], v[88:91], v[8:11], v[152:155]\nds_read_b128 v[0:3], v96 offset:51200\nv_mfma_f32_16x16x32_bf16 v[152:155], v[92:95], v[12:15], v[152:155]\n"
      "ds_read_b128 v[4:7], v97 offset:51200\nv_mfma_f32_16x16x32_bf16 v[148:151], v[84:87], v[12:15], v[148:151]\nv_mfma_f32_16x16x32_bf16 v[148:151], v[80:83], v[8:11], v[148:151]\n"
      "v_mfma_f32_16x16x32_bf16 v[144:147], v[72:75], v[8:11], v[144:147]\nv_mfma_f32_16x16x32_bf16 v[144:147], v[76:79], v[12:15], v[144:147]\n"
      "v_mfma_f32_16x16x32_bf16 v[140:143], v[68:71], v[12:15], v[140:143]\ns_waitcnt lgkmcnt(4)\nv_mfma_f32_16x16x32_bf16 v[140:143], v[64:67], v[8:11], v[140:143]\n"
      "v_mfma_f32_16x16x32_bf16 v[156:159], v[64:67], v[16:19], v[156:159]\nds_read_b128 v[8:11], v96 offset:53248\nv_mfma_f32_16x16x32_bf16 v[156:159], v[68:71], v[20:23], v[156:159]\n"
      "ds_read_b128 v[12:15], v97 offset:53248\nv_mfma_f32_16x16x32_bf16 v[160:163], v[76:79], v[20:23], v[160:163]\nv_mfma_f32_16x16x32_bf16 v[160:163], v[72:75], v[16:19], v[160:163]\n"
      "v_mfma_f32_16x16x32_bf16 v[164:167], v[80:83], v[16:19], v[164:167]\nv_mfma_f32_16x16x32_bf16 v[164:167], v[84:87], v[20:23], v[164:167]\n"
      "v_mfma_f32_16x16x32_bf16 v[168:171], v[92:95], v[20:23], v[168:171]\ns_waitcnt lgkmcnt(2)\nv_mfma_f32_16x16x32_bf16 v[168:171], v[88:91], v[16:19], v[168:171]\n"
      "v_mfma_f32_16x16x32_bf16 v[184:187], v[88:91], v[24:27], v[184:187]\nds_read_b128 v[16:19], v96 offset:55296\nv_mfma_f32_16x16x32_bf16 v[184:187], v[92:95], v[28:31], v[184:187]\n"
      "ds_read_b128 v[20:23], v97 offset:55296\nv_mfma_f32_16x16x32_bf16 v[180:183], v[84:87], v[28:31], v[180:183]\nv_mfma_f32_16x16x32_bf16 v[180:183], v[80:83], v[24:27], v[180:183]\n"
      "v_mfma_f32_16x16x32_bf16 v[176:179], v[72:75], v[24:27], v[176:179]\nv_mfma_f32_16x16x32_bf16 v[176:179], v[76:79], v[28:31], v[176:179]\n"
      "v_mfma_f32_16x16x32_bf16 v[172:175], v[68:71], v[28:31], v[172:175]\ns_waitcnt vmcnt(0) lgkmcnt(0)\ns_barrier\nv_mfma_f32_16x16x32_bf16 v[172:175], v[64:67], v[24:27], v[172:175]\n"
      "ds_read_b64_tr_b16 v[32:33], v108 offset:0\nv_mfma_f32_16x16x32_bf16 v[188:191], v[64:67], v[0:3], v[188:191]\nds_read_b64_tr_b16 v[34:35], v108 offset:1024\n"
      "v_mfma_f32_16x16x32_bf16 v[188:191], v[68:71], v[4:7], v[188:191]\nds_read_b64_tr_b16 v[36:37], v108 offset:8192\nv_mfma_f32_16x16x32_bf16 v[192:195], v[76:79], v[4:7], v[192:195]\n"
      "ds_read_b64_tr_b16 v[38:39], v108 offset:9216\nv_mfma_f32_16x16x32_bf16 v[192:195], v[72:75], v[0:3], v[192:195]\nds_read_b64_tr_b16 v[40:41], v109 offset:0\n"
      "v_mfma_f32_16x16x32_bf16 v[196:199], v[80:83], v[0:3], v[196:199]\nds_read_b64_tr_b16 v[42:43], v109 offset:1024\nv_mfma_f32_16x16x32_bf16 v[196:199], v[84:87], v[4:7], v[196:199]\n"
      "ds_read_b64_tr_b16 v[44:45], v109 offset:8192\nv_mfma_f32_16x16x32_bf16 v[200:203], v[92:95], v[4:7], v[200:203]\nds_read_b64_tr_b16 v[46:47], v109 offset:9216\n"
      "v_mfma_f32_16x16x32_bf16 v[200:203], v[88:91], v[0:3], v[200:203]\nds_read_b64_tr_b16 v[48:49], v110 offset:0\nv_mfma_f32_16x16x32_bf16 v[216:219], v[88:91], v[8:11], v[216:219]\n"
      "ds_read_b128 v[0:3], v102 offset:0\nv_mfma_f32_16x16x32_bf16 v[216:219], v[92:95], v[12:15], v[216:219]\nds_read_b128 v[4:7], v103 offset:0\n"
      "v_mfma_f32_16x16x32_bf16 v[212:215], v[84:87], v[12:15], v[212:215]\nds_read_b64_tr_b16 v[50:51], v110 offset:1024\nv_mfma_f32_16x16x32_bf16 v[212:215], v[80:83], v[8:11], v[212:215]\n"
      "ds_read_b64_tr_b16 v[52:53], v110 offset:8192\nv_mfma_f32_16x16x32_bf16 v[208:211], v[72:75], v[8:11], v[208:211]\nds_read_b64_tr_b16 v[54:55], v110 offset:9216\n"
      "v_mfma_f32_16x16x32_bf16 v[208:211], v[76:79], v[12:15], v[208:211]\nds_read_b64_tr_b16 v[56:57], v111 offset:0\nv_mfma_f32_16x16x32_bf16 v[204:207], v[68:71], v[12:15], v[204:207]\n"
      "ds_read_b64_tr_b16 v[58:59], v111 offset:1024\nv_mfma_f32_16x16x32_bf16 v[204:207], v[64:67], v[8:11], v[204:207]\nds_read_b64_tr_b16 v[60:61], v111 offset:8192\n"
      "v_mfma_f32_16x16x32_bf16 v[220:223], v[64:67], v[16:19], v[220:223]\nds_read_b128 v[8:11], v98 offset:0\nv_mfma_f32_16x16x32_bf16 v[220:223], v[68:71], v[20:23], v[220:223]\n"
      "ds_read_b128 v[12:15], v99 offset:0\nv_mfma_f32_16x16x32_bf16 v[224:227], v[76:79], v[20:23], v[224:227]\nds_read_b64_tr_b16 v[62:63], v111 offset:9216\n"
      "v_mfma_f32_16x16x32_bf16 v[224:227], v[72:75], v[16:19], v[224:227]\nv_mfma_f32_16x16x32_bf16 v[228:231], v[80:83], v[16:19], v[228:231]\n"
      "v_mfma_f32_16x16x32_bf16 v[228:231], v[84:87], v[20:23], v[228:231]\nv_mfma_f32_16x16x32_bf16 v[232:235], v[92:95], v[20:23], v[232:235]\ns_waitcnt lgkmcnt(0)\n"
      "v_mfma_f32_16x16x32_bf16 v[232:235], v[88:91], v[16:19], v[232:235]\nds_read_b128 v[16:19], v98 offset:2048\nds_read_b128 v[20:23], v99 offset:2048\n"
      "v_mfma_f32_16x16x32_bf16 v[124:127], v[32:35], v[0:3], v[124:127]\nds_read_b128 v[24:27], v98 offset:4096\nv_mfma_f32_16x16x32_bf16 v[124:127], v[36:39], v[4:7], v[124:127]\n"
      "ds_read_b128 v[28:31], v99 offset:4096\nv_mfma_f32_16x16x32_bf16 v[128:131], v[44:47], v[4:7], v[128:131]\nv_mfma_f32_16x16x32_bf16 v[128:131], v[40:43], v[0:3], v[128:131]\n"
      "v_mfma_f32_16x16x32_bf16 v[152:155], v[56:59], v[8:11], v[152:155]\nds_read_b128 v[0:3], v98 offset:6144\nv_mfma_f32_16x16x32_bf16 v[152:155], v[60:63], v[12:15], v[152:155]\n"
      "ds_read_b128 v[4:7], v99 offset:6144\nv_mfma_f32_16x16x32_bf16 v[148:151], v[52:55], v[12:15], v[148:151]\nv_mfma_f32_16x16x32_bf16 v[148:151], v[48:51], v[8:11], v[148:151]\n"
      "v_mfma_f32_16x16x32_bf16 v[144:147], v[40:43], v[8:11], v[144:147]\nv_mfma_f32_16x16x32_bf16 v[144:147], v[44:47], v[12:15], v[144:147]\n"
      "v_mfma_f32_16x16x32_bf16 v[140:143], v[36:39], v[12:15], v[140:143]\ns_waitcnt lgkmcnt(4)\nv_mfma_f32_16x16x32_bf16 v[140:143], v[32:35], v[8:11], v[140:143]\n"
      "v_mfma_f32_16x16x32_bf16 v[156:159], v[32:35], v[16:19], v[156:159]\nds_read_b128 v[8:11], v98 offset:8192\nv_mfma_f32_16x16x32_bf16 v[156:159], v[36:39], v[20:23], v[156:159]\n"
      "ds_read_b128 v[12:15], v99 offset:8192\nv_mfma_f32_16x16x32_bf16 v[160:163], v[44:47], v[20:23], v[160:163]\nv_mfma_f32_16x16x32_bf16 v[160:163], v[40:43], v[16:19], v[160:163]\n"
      "v_mfma_f32_16x16x32_bf16 v[164:167], v[48:51], v[16:19], v[164:167]\nv_mfma_f32_16x16x32_bf16 v[164:167], v[52:55], v[20:23], v[164:167]\n"
      "v_mfma_f32_16x16x32_bf16 v[168:171], v[60:63], v[20:23], v[168:171]\ns_waitcnt lgkmcnt(2)\nv_mfma_f32_16x16x32_bf16 v[168:171], v[56:59], v[16:19], v[168:171]\n"
      "v_mfma_f32_16x16x32_bf16 v[184:187], v[56:59], v[24:27], v[184:187]\nds_read_b128 v[16:19], v98 offset:10240\nv_mfma_f32_16x16x32_bf16 v[184:187], v[60:63], v[28:31], v[184:187]\n"
      "ds_read_b128 v[20:23], v99 offset:10240\nv_mfma_f32_16x16x32_bf16 v[180:183], v[52:55], v[28:31], v[180:183]\nv_mfma_f32_16x16x32_bf16 v[180:183], v[48:51], v[24:27], v[180:183]\n"
      "v_mfma_f32_16x16x32_bf16 v[176:179], v[40:43], v[24:27], v[176:179]\nv_mfma_f32_16x16x32_bf16 v[176:179], v[44:47], v[28:31], v[176:179]\n"
      "v_mfma_f32_16x16x32_bf16 v[172:175], v[36:39], v[28:31], v[172:175]\ns_waitcnt vmcnt(0) lgkmcnt(0)\ns_barrier\nv_mfma_f32_16x16x32_bf16 v[172:175], v[32:35], v[24:27], v[172:175]\n"
      "ds_read_b64_tr_b16 v[64:65], v104 offset:0\nv_mfma_f32_16x16x32_bf16 v[188:191], v[32:35], v[0:3], v[188:191]\nds_read_b64_tr_b16 v[66:67], v104 offset:1024\n"
      "v_mfma_f32_16x16x32_bf16 v[188:191], v[36:39], v[4:7], v[188:191]\nds_read_b64_tr_b16 v[68:69], v104 offset:8192\nv_mfma_f32_16x16x32_bf16 v[192:195], v[44:47], v[4:7], v[192:195]\n"
      "ds_read_b64_tr_b16 v[70:71], v104 offset:9216\nv_mfma_f32_16x16x32_bf16 v[192:195], v[40:43], v[0:3], v[192:195]\nds_read_b64_tr_b16 v[72:73], v105 offset:0\n"
      "v_mfma_f32_16x16x32_bf16 v[196:199], v[48:51], v[0:3], v[196:199]\nds_read_b64_tr_b16 v[74:75], v105 offset:1024\nv_mfma_f32_16x16x32_bf16 v[196:199], v[52:55], v[4:7], v[196:199]\n"
      "ds_read_b64_tr_b16 v[76:77], v105 offset:8192\nv_mfma_f32_16x16x32_bf16 v[200:203], v[60:63], v[4:7], v[200:203]\nds_read_b64_tr_b16 v[78:79], v105 offset:9216\n"
      "v_mfma_f32_16x16x32_bf16 v[200:203], v[56:59], v[0:3], v[200:203]\nds_read_b64_tr_b16 v[80:81], v106 offset:0\nv_mfma_f32_16x16x32_bf16 v[216:219], v[56:59], v[8:11], v[216:219]\n"
      "ds_read_b128 v[0:3], v100 offset:0\nv_mfma_f32_16x16x32_bf16 v[216:219], v[60:63], v[12:15], v[216:219]\nds_read_b128 v[4:7], v101 offset:0\n"
      "v_mfma_f32_16x16x32_bf16 v[212:215], v[52:55], v[12:15], v[212:215]\nds_read_b64_tr_b16 v[82:83], v106 offset:1024\nv_mfma_f32_16x16x32_bf16 v[212:215], v[48:51], v[8:11], v[212:215]\n"
      "ds_read_b64_tr_b16 v[84:85], v106 offset:8192\nv_mfma_f32_16x16x32_bf16 v[208:211], v[40:43], v[8:11], v[208:211]\nds_read_b64_tr_b16 v[86:87], v106 offset:9216\n"
      "v_mfma_f32_16x16x32_bf16 v[208:211], v[44:47], v[12:15], v[208:211]\nds_read_b64_tr_b16 v[88:89], v107 offset:0\nv_mfma_f32_16x16x32_bf16 v[204:207], v[36:39], v[12:15], v[204:207]\n"
      "ds_read_b64_tr_b16 v[90:91], v107 offset:1024\nv_mfma_f32_16x16x32_bf16 v[204:207], v[32:35], v[8:11], v[204:207]\nds_read_b64_tr_b16 v[92:93], v107 offset:8192\n"
      "v_mfma_f32_16x16x32_bf16 v[220:223], v[32:35], v[16:19], v[220:223]\nds_read_b128 v[8:11], v96 offset:0\nv_mfma_f32_16x16x32_bf16 v[220:223], v[36:39], v[20:23], v[220:223]\n"
      "ds_read_b128 v[12:15], v97 offset:0\nv_mfma_f32_16x16x32_bf16 v[224:227], v[44:47], v[20:23], v[224:227]\nds_read_b64_tr_b16 v[94:95], v107 offset:9216\n"
      "v_mfma_f32_16x16x32_bf16 v[224:227], v[40:43], v[16:19], v[224:227]\nv_mfma_f32_16x16x32_bf16 v[228:231], v[48:51], v[16:19], v[228:231]\n"
      "v_mfma_f32_16x16x32_bf16 v[228:231], v[52:55], v[20:23], v[228:231]\nv_mfma_f32_16x16x32_bf16 v[232:235], v[60:63], v[20:23], v[232:235]\ns_waitcnt lgkmcnt(0)\n"
      "v_mfma_f32_16x16x32_bf16 v[232:235], v[56:59], v[16:19], v[232:235]\nds_read_b128 v[16:19], v96 offset:2048\nds_read_b128 v[20:23], v97 offset:2048\n"
      "v_mfma_f32_16x16x32_bf16 v[124:127], v[64:67], v[0:3], v[124:127]\nds_read_b128 v[24:27], v96 offset:4096\nv_mfma_f32_16x16x32_bf16 v[124:127], v[68:71], v[4:7], v[124:127]\n"
      "ds_read_b128 v[28:31], v97 offset:4096\nv_mfma_f32_16x16x32_bf16 v[128:131], v[76:79], v[4:7], v[128:131]\nv_mfma_f32_16x16x32_bf16 v[128:131], v[72:75], v[0:3], v[128:131]\n"
      "v_mfma_f32_16x16x32_bf16 v[152:155], v[88:91], v[8:11], v[152:155]\nds_read_b128 v[0:3], v96 offset:6144\nv_mfma_f32_16x16x32_bf16 v[152:155], v[92:95], v[12:15], v[152:155]\n"
      "ds_read_b128 v[4:7], v97 offset:6144\nv_mfma_f32_16x16x32_bf16 v[148:151], v[84:87], v[12:15], v[148:151]\nv_mfma_f32_16x16x32_bf16 v[148:151], v[80:83], v[8:11], v[148:151]\n"
      "v_mfma_f32_16x16x32_bf16 v[144:147], v[72:75], v[8:11], v[144:147]\nv_mfma_f32_16x16x32_bf16 v[144:147], v[76:79], v[12:15], v[144:147]\n"
      "v_mfma_f32_16x16x32_bf16 v[140:143], v[68:71], v[12:15], v[140:143]\ns_waitcnt lgkmcnt(4)\nv_mfma_f32_16x16x32_bf16 v[140:143], v[64:67], v[8:11], v[140:143]\n"
      "v_mfma_f32_16x16x32_bf16 v[156:159], v[64:67], v[16:19], v[156:159]\nds_read_b128 v[8:11], v96 offset:8192\nv_mfma_f32_16x16x32_bf16 v[156:159], v[68:71], v[20:23], v[156:159]\n"
      "ds_read_b128 v[12:15], v97 offset:8192\nv_mfma_f32_16x16x32_bf16 v[160:163], v[76:79], v[20:23], v[160:163]\nv_mfma_f32_16x16x32_bf16 v[160:163], v[72:75], v[16:19], v[160:163]\n"
      "v_mfma_f32_16x16x32_bf16 v[164:167], v[80:83], v[16:19], v[164:167]\nv_mfma_f32_16x16x32_bf16 v[164:167], v[84:87], v[20:23], v[164:167]\n"
      "v_mfma_f32_16x16x32_bf16 v[168:171], v[92:95], v[20:23], v[168:171]\ns_waitcnt lgkmcnt(2)\nv_mfma_f32_16x16x32_bf16 v[168:171], v[88:91], v[16:19], v[168:171]\n"
      "v_mfma_f32_16x16x32_bf16 v[184:187], v[88:91], v[24:27], v[184:187]\nds_read_b128 v[16:19], v96 offset:10240\nv_mfma_f32_16x16x32_bf16 v[184:187], v[92:95], v[28:31], v[184:187]\n"
      "ds_read_b128 v[20:23], v97 offset:10240\nv_mfma_f32_16x16x32_bf16 v[180:183], v[84:87], v[28:31], v[180:183]\nv_mfma_f32_16x16x32_bf16 v[180:183], v[80:83], v[24:27], v[180:183]\n"
      "v_mfma_f32_16x16x32_bf16 v[176:179], v[72:75], v[24:27], v[176:179]\nv_mfma_f32_16x16x32_bf16 v[176:179], v[76:79], v[28:31], v[176:179]\n"
      "v_mfma_f32_16x16x32_bf16 v[172:175], v[68:71], v[28:31], v[172:175]\ns_waitcnt vmcnt(0) lgkmcnt(0)\ns_barrier\nv_mfma_f32_16x16x32_bf16 v[172:175], v[64:67], v[24:27], v[172:175]\n"
      "ds_read_b64_tr_b16 v[32:33], v104 offset:45056\nv_mfma_f32_16x16x32_bf16 v[188:191], v[64:67], v[0:3], v[188:191]\nds_read_b64_tr_b16 v[34:35], v104 offset:46080\n"
      "v_mfma_f32_16x16x32_bf16 v[188:191], v[68:71], v[4:7], v[188:191]\nds_read_b64_tr_b16 v[36:37], v104 offset:53248\nv_mfma_f32_16x16x32_bf16 v[192:195], v[76:79], v[4:7], v[192:195]\n"
      "ds_read_b64_tr_b16 v[38:39], v104 offset:54272\nv_mfma_f32_16x16x32_bf16 v[192:195], v[72:75], v[0:3], v[192:195]\nds_read_b64_tr_b16 v[40:41], v105 offset:45056\n"
      "v_mfma_f32_16x16x32_bf16 v[196:199], v[80:83], v[0:3], v[196:199]\nds_read_b64_tr_b16 v[42:43], v105 offset:46080\nv_mfma_f32_16x16x32_bf16 v[196:199], v[84:87], v[4:7], v[196:199]\n"
      "ds_read_b64_tr_b16 v[44:45], v105 offset:53248\nv_mfma_f32_16x16x32_bf16 v[200:203], v[92:95], v[4:7], v[200:203]\nds_read_b64_tr_b16 v[46:47], v105 offset:54272\n"
      "v_mfma_f32_16x16x32_bf16 v[200:203], v[88:91], v[0:3], v[200:203]\nds_read_b64_tr_b16 v[48:49], v106 offset:45056\nv_mfma_f32_16x16x32_bf16 v[216:219], v[88:91], v[8:11], v[216:219]\n"
      "ds_read_b128 v[0:3], v100 offset:45056\nv_mfma_f32_16x16x32_bf16 v[216:219], v[92:95], v[12:15], v[216:219]\nds_read_b128 v[4:7], v101 offset:45056\n"
      "v_mfma_f32_16x16x32_bf16 v[212:215], v[84:87], v[12:15], v[212:215]\nds_read_b64_tr_b16 v[50:51], v106 offset:46080\nv_mfma_f32_16x16x32_bf16 v[212:215], v[80:83], v[8:11], v[212:215]\n"
      "ds_read_b64_tr_b16 v[52:53], v106 offset:53248\nv_mfma_f32_16x16x32_bf16 v[208:211], v[72:75], v[8:11], v[208:211]\nds_read_b64_tr_b16 v[54:55], v106 offset:54272\n"
      "v_mfma_f32_16x16x32_bf16 v[208:211], v[76:79], v[12:15], v[208:211]\nds_read_b64_tr_b16 v[56:57], v107 offset:45056\nv_mfma_f32_16x16x32_bf16 v[204:207], v[68:71], v[12:15], v[204:207]\n"
      "ds_read_b64_tr_b16 v[58:59], v107 offset:46080\nv_mfma_f32_16x16x32_bf16 v[204:207], v[64:67], v[8:11], v[204:207]\nds_read_b64_tr_b16 v[60:61], v107 offset:53248\n"
      "v_mfma_f32_16x16x32_bf16 v[220:223], v[64:67], v[16:19], v[220:223]\nds_read_b128 v[8:11], v96 offset:45056\nv_mfma_f32_16x16x32_bf16 v[220:223], v[68:71], v[20:23], v[220:223]\n"
      "ds_read_b128 v[12:15], v97 offset:45056\nv_mfma_f32_16x16x32_bf16 v[224:227], v[76:79], v[20:23], v[224:227]\nds_read_b64_tr_b16 v[62:63], v107 offset:54272\n"
      "v_mfma_f32_16x16x32_bf16 v[224:227], v[72:75], v[16:19], v[224:227]\nv_mfma_f32_16x16x32_bf16 v[228:231], v[80:83], v[16:19], v[228:231]\n"
      "v_mfma_f32_16x16x32_bf16 v[228:231], v[84:87], v[20:23], v[228:231]\nv_mfma_f32_16x16x32_bf16 v[232:235], v[92:95], v[20:23], v[232:235]\ns_waitcnt lgkmcnt(0)\n"
      "v_mfma_f32_16x16x32_bf16 v[232:235], v[88:91], v[16:19], v[232:235]\nds_read_b128 v[16:19], v96 offset:47104\nds_read_b128 v[20:23], v97 offset:47104\n"
      "v_mfma_f32_16x16x32_bf16 v[124:127], v[32:35], v[0:3], v[124:127]\nds_read_b128 v[24:27], v96 offset:49152\nv_mfma_f32_16x16x32_bf16 v[124:127], v[36:39], v[4:7], v[124:127]\n"
      "ds_read_b128 v[28:31], v97 offset:49152\nv_mfma_f32_16x16x32_bf16 v[128:131], v[44:47], v[4:7], v[128:131]\nv_mfma_f32_16x16x32_bf16 v[128:131], v[40:43], v[0:3], v[128:131]\n"
      "v_mfma_f32_16x16x32_bf16 v[152:155], v[56:59], v[8:11], v[152:155]\nds_read_b128 v[0:3], v96 offset:51200\nv_mfma_f32_16x16x32_bf16 v[152:155], v[60:63], v[12:15], v[152:155]\n"
      "ds_read_b128 v[4:7], v97 offset:51200\nv_mfma_f32_16x16x32_bf16 v[148:151], v[52:55], v[12:15], v[148:151]\nv_mfma_f32_16x16x32_bf16 v[148:151], v[48:51], v[8:11], v[148:151]\n"
      "v_mfma_f32_16x16x32_bf16 v[144:147], v[40:43], v[8:11], v[144:147]\nv_mfma_f32_16x16x32_bf16 v[144:147], v[44:47], v[12:15], v[144:147]\n"
      "v_mfma_f32_16x16x32_bf16 v[140:143], v[36:39], v[12:15], v[140:143]\ns_waitcnt lgkmcnt(4)\nv_mfma_f32_16x16x32_bf16 v[140:143], v[32:35], v[8:11], v[140:143]\n"
      "v_mfma_f32_16x16x32_bf16 v[156:159], v[32:35], v[16:19], v[156:159]\nds_read_b128 v[8:11], v96 offset:53248\nv_mfma_f32_16x16x32_bf16 v[156:159], v[36:39], v[20:23], v[156:159]\n"
      "ds_read_b128 v[12:15], v97 offset:53248\nv_mfma_f32_16x16x32_bf16 v[160:163], v[44:47], v[20:23], v[160:163]\nv_mfma_f32_16x16x32_bf16 v[160:163], v[40:43], v[16:19], v[160:163]\n"
      "v_mfma_f32_16x16x32_bf16 v[164:167], v[48:51], v[16:19], v[164:167]\nv_mfma_f32_16x16x32_bf16 v[164:167], v[52:55], v[20:23], v[164:167]\n"
      "v_mfma_f32_16x16x32_bf16 v[168:171], v[60:63], v[20:23], v[168:171]\ns_waitcnt lgkmcnt(2)\nv_mfma_f32_16x16x32_bf16 v[168:171], v[56:59], v[16:19], v[168:171]\n"
      "v_mfma_f32_16x16x32_bf16 v[184:187], v[56:59], v[24:27], v[184:187]\nds_read_b128 v[16:19], v96 offset:55296\nv_mfma_f32_16x16x32_bf16 v[184:187], v[60:63], v[28:31], v[184:187]\n"
      "ds_read_b128 v[20:23], v97 offset:55296\nv_mfma_f32_16x16x32_bf16 v[180:183], v[52:55], v[28:31], v[180:183]\nv_mfma_f32_16x16x32_bf16 v[180:183], v[48:51], v[24:27], v[180:183]\n"
      "v_mfma_f32_16x16x32_bf16 v[176:179], v[40:43], v[24:27], v[176:179]\nv_mfma_f32_16x16x32_bf16 v[176:179], v[44:47], v[28:31], v[176:179]\n"
      "v_mfma_f32_16x16x32_bf16 v[172:175], v[36:39], v[28:31], v[172:175]\ns_waitcnt vmcnt(0) lgkmcnt(0)\ns_barrier\nv_mfma_f32_16x16x32_bf16 v[172:175], v[32:35], v[24:27], v[172:175]\n"
      "ds_read_b64_tr_b16 v[64:65], v108 offset:0\nv_mfma_f32_16x16x32_bf16 v[188:191], v[32:35], v[0:3], v[188:191]\nds_read_b64_tr_b16 v[66:67], v108 offset:1024\n"
      "v_mfma_f32_16x16x32_bf16 v[188:191], v[36:39], v[4:7], v[188:191]\nds_read_b64_tr_b16 v[68:69], v108 offset:8192\nv_mfma_f32_16x16x32_bf16 v[192:195], v[44:47], v[4:7], v[192:195]\n"
      "ds_read_b64_tr_b16 v[70:71], v108 offset:9216\nv_mfma_f32_16x16x32_bf16 v[192:195], v[40:43], v[0:3], v[192:195]\nds_read_b64_tr_b16 v[72:73], v109 offset:0\n"
      "v_mfma_f32_16x16x32_bf16 v[196:199], v[48:51], v[0:3], v[196:199]\nds_read_b64_tr_b16 v[74:75], v109 offset:1024\nv_mfma_f32_16x16x32_bf16 v[196:199], v[52:55], v[4:7], v[196:199]\n"
      "ds_read_b64_tr_b16 v[76:77], v109 offset:8192\nv_mfma_f32_16x16x32_bf16 v[200:203], v[60:63], v[4:7], v[200:203]\nds_read_b64_tr_b16 v[78:79], v109 offset:9216\n"
      "v_mfma_f32_16x16x32_bf16 v[200:203], v[56:59], v[0:3], v[200:203]\nds_read_b64_tr_b16 v[80:81], v110 offset:0\nv_mfma_f32_16x16x32_bf16 v[216:219], v[56:59], v[8:11], v[216:219]\n"
      "ds_read_b128 v[0:3], v102 offset:0\nv_mfma_f32_16x16x32_bf16 v[216:219], v[60:63], v[12:15], v[216:219]\nds_read_b128 v[4:7], v103 offset:0\n"
      "v_mfma_f32_16x16x32_bf16 v[212:215], v[52:55], v[12:15], v[212:215]\nds_read_b64_tr_b16 v[82:83], v110 offset:1024\nv_mfma_f32_16x16x32_bf16 v[212:215], v[48:51], v[8:11], v[212:215]\n"
      "ds_read_b64_tr_b16 v[84:85], v110 offset:8192\nv_mfma_f32_16x16x32_bf16 v[208:211], v[40:43], v[8:11], v[208:211]\nds_read_b64_tr_b16 v[86:87], v110 offset:9216\n"
      "v_mfma_f32_16x16x32_bf16 v[208:211], v[44:47], v[12:15], v[208:211]\nds_read_b64_tr_b16 v[88:89], v111 offset:0\nv_mfma_f32_16x16x32_bf16 v[204:207], v[36:39], v[12:15], v[204:207]\n"
      "ds_read_b64_tr_b16 v[90:91], v111 offset:1024\nv_mfma_f32_16x16x32_bf16 v[204:207], v[32:35], v[8:11], v[204:207]\nds_read_b64_tr_b16 v[92:93], v111 offset:8192\n"
      "v_mfma_f32_16x16x32_bf16 v[220:223], v[32:35], v[16:19], v[220:223]\nds_read_b128 v[8:11], v98 offset:0\nv_mfma_f32_16x16x32_bf16 v[220:223], v[36:39], v[20:23], v[220:223]\n"
      "ds_read_b128 v[12:15], v99 offset:0\nv_mfma_f32_16x16x32_bf16 v[224:227], v[44:47], v[20:23], v[224:227]\nds_read_b64_tr_b16 v[94:95], v111 offset:9216\n"
      "v_mfma_f32_16x16x32_bf16 v[224:227], v[40:43], v[16:19], v[224:227]\nv_mfma_f32_16x16x32_bf16 v[228:231], v[48:51], v[16:19], v[228:231]\ns_sub_u32 s58, s58, 1\ns_cmp_lg_u32 s58, 0\n"
      "v_mfma_f32_16x16x32_bf16 v[228:231], v[52:55], v[20:23], v[228:231]\nv_mfma_f32_16x16x32_bf16 v[232:235], v[60:63], v[20:23], v[232:235]\ns_waitcnt lgkmcnt(0)\n"
      "v_mfma_f32_16x16x32_bf16 v[232:235], v[56:59], v[16:19], v[232:235]\nds_read_b128 v[16:19], v98 offset:2048\nds_read_b128 v[20:23], v99 offset:2048\ns_cbranch_scc1 .Lgl13\n"
      "v_mfma_f32_16x16x32_bf16 v[124:127], v[64:67], v[0:3], v[124:127]\nds_read_b128 v[24:27], v98 offset:4096\nv_cmp_eq_u32 s[62:63], 0, v240\n"
      "v_mfma_f32_16x16x32_bf16 v[124:127], v[68:71], v[4:7], v[124:127]\nds_read_b128 v[28:31], v99 offset:4096\nv_mov_b32 v248, 262144\n"
      "v_mfma_f32_16x16x32_bf16 v[128:131], v[76:79], v[4:7], v[128:131]\nv_mov_b32 v247, 162176\nv_mov_b32 v241, 1\nv_mfma_f32_16x16x32_bf16 v[128:131], v[72:75], v[0:3], v[128:131]\n"
      "v_cndmask_b32 v253, v248, v247, s[62:63]\ns_lshl_b32 s42, s36, 2\nv_mfma_f32_16x16x32_bf16 v[152:155], v[88:91], v[8:11], v[152:155]\nds_read_b128 v[0:3], v98 offset:6144\n"
      "v_add_u32 v245, s42, v253\nv_mfma_f32_16x16x32_bf16 v[152:155], v[92:95], v[12:15], v[152:155]\nds_read_b128 v[4:7], v99 offset:6144\nv_lshrrev_b32 v112, 4, v240\n"
      "v_mfma_f32_16x16x32_bf16 v[148:151], v[84:87], v[12:15], v[148:151]\nv_bfe_u32 v113, v240, 2, 2\nv_and_b32 v114, 3, v240\nv_mfma_f32_16x16x32_bf16 v[148:151], v[80:83], v[8:11], v[148:151]\n"
      "v_mul_u32_u24 v114, 0x80008, v114\nv_add_u32 v242, v112, v113\nv_mfma_f32_16x16x32_bf16 v[144:147], v[72:75], v[8:11], v[144:147]\nv_and_b32 v242, 3, v242\nv_add_u32 v116, 22, v113\n"
      "v_mfma_f32_16x16x32_bf16 v[144:147], v[76:79], v[12:15], v[144:147]\nv_cmp_gt_u32 s[62:63], 2, v113\nv_and_b32 v113, 60, v240\n"
      "v_mfma_f32_16x16x32_bf16 v[140:143], v[68:71], v[12:15], v[140:143]\ns_waitcnt lgkmcnt(4)\ns_mov_b32 s42, 45056\nv_mfma_f32_16x16x32_bf16 v[140:143], v[64:67], v[8:11], v[140:143]\n"
      "v_cndmask_b32 v116, v116, v242, s[62:63]\nv_lshl_or_b32 v242, v242, 16, v116\nv_mfma_f32_16x16x32_bf16 v[156:159], v[64:67], v[16:19], v[156:159]\nds_read_b128 v[8:11], v98 offset:8192\n"
      "v_lshl_add_u32 v242, v242, 5, v114\nv_mfma_f32_16x16x32_bf16 v[156:159], v[68:71], v[20:23], v[156:159]\nds_read_b128 v[12:15], v99 offset:8192\nv_lshl_add_u32 v252, v240, 4, s42\n"
      "v_mfma_f32_16x16x32_bf16 v[160:163], v[76:79], v[20:23], v[160:163]\ns_mov_b32 s42, 0x76325410\ns_mov_b32 s43, 0x21005410\n"
      "v_mfma_f32_16x16x32_bf16 v[160:163], v[72:75], v[16:19], v[160:163]\nv_lshrrev_b64 v[116:117], v113, s[42:43]\nv_and_b32 v116, 7, v116\n"
      "v_mfma_f32_16x16x32_bf16 v[164:167], v[80:83], v[16:19], v[164:167]\nv_lshl_add_u32 v116, v112, 3, v116\ns_mov_b32 s42, 0x54107632\n"
      "v_mfma_f32_16x16x32_bf16 v[164:167], v[84:87], v[20:23], v[164:167]\ns_mov_b32 s43, 0x21107632\nv_lshrrev_b64 v[118:119], v113, s[42:43]\n"
      "v_mfma_f32_16x16x32_bf16 v[168:171], v[92:95], v[20:23], v[168:171]\ns_waitcnt lgkmcnt(2)\nv_and_b32 v118, 7, v118\nv_mfma_f32_16x16x32_bf16 v[168:171], v[88:91], v[16:19], v[168:171]\n"
      "v_lshl_add_u32 v118, v112, 3, v118\nv_lshl_or_b32 v243, v118, 16, v116\nv_mfma_f32_16x16x32_bf16 v[184:187], v[88:91], v[24:27], v[184:187]\nds_read_b128 v[16:19], v98 offset:10240\n"
      "v_lshl_add_u32 v243, v243, 5, v114\nv_mfma_f32_16x16x32_bf16 v[184:187], v[92:95], v[28:31], v[184:187]\nds_read_b128 v[20:23], v99 offset:10240\nv_and_b32 v113, 1, v240\n"
      "v_mfma_f32_16x16x32_bf16 v[180:183], v[84:87], v[28:31], v[180:183]\nv_mul_u32_u24 v113, 0xaaaa, v113\nv_add_u32 v244, 0x4444, v113\n"
      "v_mfma_f32_16x16x32_bf16 v[180:183], v[80:83], v[24:27], v[180:183]\ns_lshl_b32 s42, s36, 4\ns_add_u32 s42, s42, 160896\nv_mfma_f32_16x16x32_bf16 v[176:179], v[72:75], v[24:27], v[176:179]\n"
      "v_lshl_add_u32 v246, v112, 2, s42\nv_and_b32 v112, 1, v112\nv_mfma_f32_16x16x32_bf16 v[176:179], v[76:79], v[28:31], v[176:179]\nv_lshl_add_u32 v112, v112, 3, s24\ns_sub_u32 s42, s64, 1\n"
      "v_mfma_f32_16x16x32_bf16 v[172:175], v[68:71], v[28:31], v[172:175]\ns_waitcnt vmcnt(0) lgkmcnt(0)\ns_barrier\nv_mfma_f32_16x16x32_bf16 v[172:175], v[64:67], v[24:27], v[172:175]\n"
      "ds_read_b64_tr_b16 v[32:33], v104 offset:0\nv_min_u32 v112, s42, v112\nv_mfma_f32_16x16x32_bf16 v[188:191], v[64:67], v[0:3], v[188:191]\nds_read_b64_tr_b16 v[34:35], v104 offset:1024\n"
      "v_lshlrev_b32 v112, 3, v112\nv_mfma_f32_16x16x32_bf16 v[188:191], v[68:71], v[4:7], v[188:191]\nds_read_b64_tr_b16 v[36:37], v104 offset:8192\nv_add_u32 v250, 160640, v112\n"
      "v_mfma_f32_16x16x32_bf16 v[192:195], v[76:79], v[4:7], v[192:195]\nds_read_b64_tr_b16 v[38:39], v104 offset:9216\nv_and_b32 v113, 15, v240\n"
      "v_mfma_f32_16x16x32_bf16 v[192:195], v[72:75], v[0:3], v[192:195]\nds_read_b64_tr_b16 v[40:41], v105 offset:0\nv_min_u32 v113, 12, v113\n"
      "v_mfma_f32_16x16x32_bf16 v[196:199], v[80:83], v[0:3], v[196:199]\nds_read_b64_tr_b16 v[42:43], v105 offset:1024\ns_lshl_b32 s42, s30, 7\n"
      "v_mfma_f32_16x16x32_bf16 v[196:199], v[84:87], v[4:7], v[196:199]\nds_read_b64_tr_b16 v[44:45], v105 offset:8192\nv_lshl_add_u32 v113, v113, 15, s42\n"
      "v_mfma_f32_16x16x32_bf16 v[200:203], v[92:95], v[4:7], v[200:203]\nds_read_b64_tr_b16 v[46:47], v105 offset:9216\nv_add_u32 v249, v113, v112\n"
      "v_mfma_f32_16x16x32_bf16 v[200:203], v[88:91], v[0:3], v[200:203]\nds_read_b64_tr_b16 v[48:49], v106 offset:0\ns_mul_i32 s65, s36, 17632\n"
      "v_mfma_f32_16x16x32_bf16 v[216:219], v[88:91], v[8:11], v[216:219]\nds_read_b128 v[0:3], v100 offset:0\ns_add_u32 s65, s65, 90112\n"
      "v_mfma_f32_16x16x32_bf16 v[216:219], v[92:95], v[12:15], v[216:219]\nds_read_b128 v[4:7], v101 offset:0\ns_add_u32 s68, s64, 6\n"
      "v_mfma_f32_16x16x32_bf16 v[212:215], v[84:87], v[12:15], v[212:215]\nds_read_b64_tr_b16 v[50:51], v106 offset:1024\ns_min_u32 s68, s68, 19\n"
      "v_mfma_f32_16x16x32_bf16 v[212:215], v[80:83], v[8:11], v[212:215]\nds_read_b64_tr_b16 v[52:53], v106 offset:8192\ns_lshl_b32 s38, s38, 16\n"
      "v_mfma_f32_16x16x32_bf16 v[208:211], v[72:75], v[8:11], v[208:211]\nds_read_b64_tr_b16 v[54:55], v106 offset:9216\ns_lshr_b32 s42, s24, 2\n"
      "v_mfma_f32_16x16x32_bf16 v[208:211], v[76:79], v[12:15], v[208:211]\nds_read_b64_tr_b16 v[56:57], v107 offset:0\ns_mul_i32 s43, s42, 3\n"
      "v_mfma_f32_16x16x32_bf16 v[204:207], v[68:71], v[12:15], v[204:207]\nds_read_b64_tr_b16 v[58:59], v107 offset:1024\ns_xor_b32 s43, s43, s36\n"
      "v_mfma_f32_16x16x32_bf16 v[204:207], v[64:67], v[8:11], v[204:207]\nds_read_b64_tr_b16 v[60:61], v107 offset:8192\ns_add_u32 s42, s42, s64\n"
      "v_mfma_f32_16x16x32_bf16 v[220:223], v[64:67], v[16:19], v[220:223]\nds_read_b128 v[8:11], v96 offset:0\ns_add_u32 s42, s42, 7\n"
      "v_mfma_f32_16x16x32_bf16 v[220:223], v[68:71], v[20:23], v[220:223]\nds_read_b128 v[12:15], v97 offset:0\ns_and_b32 s42, s42, 1\n"
      "v_mfma_f32_16x16x32_bf16 v[224:227], v[76:79], v[20:23], v[224:227]\nds_read_b64_tr_b16 v[62:63], v107 offset:9216\ns_lshl1_add_u32 s67, s43, s42\n"
      "v_mfma_f32_16x16x32_bf16 v[224:227], v[72:75], v[16:19], v[224:227]\ns_add_u32 s66, s64, s67\ns_cmp_lt_u32 s66, s68\nv_mfma_f32_16x16x32_bf16 v[228:231], v[80:83], v[16:19], v[228:231]\n"
      "s_cselect_b32 s66, s66, 127\ns_sub_u32 s66, s66, 7\nv_mfma_f32_16x16x32_bf16 v[228:231], v[84:87], v[20:23], v[228:231]\ns_getpc_b64 s[4:5]\n"
      ".Lk1064:\nv_mfma_f32_16x16x32_bf16 v[232:235], v[92:95], v[20:23], v[232:235]\ns_waitcnt lgkmcnt(0)\ns_add_u32 s34, s4, .Lt_g-.Lk1064\n"
      "v_mfma_f32_16x16x32_bf16 v[232:235], v[88:91], v[16:19], v[232:235]\nds_read_b128 v[16:19], v96 offset:2048\nds_read_b128 v[20:23], v97 offset:2048\n"
      "v_mfma_f32_16x16x32_bf16 v[124:127], v[32:35], v[0:3], v[124:127]\nds_read_b128 v[24:27], v96 offset:4096\ns_addc_u32 s35, s5, 0\n"
      "v_mfma_f32_16x16x32_bf16 v[124:127], v[36:39], v[4:7], v[124:127]\nds_read_b128 v[28:31], v97 offset:4096\ns_add_u32 s4, s4, .Lc_back-.Lk1064\n"
      "v_mfma_f32_16x16x32_bf16 v[128:131], v[44:47], v[4:7], v[128:131]\ns_addc_u32 s5, s5, 0\ns_lshr_b32 s42, %[blk], 3\nv_mfma_f32_16x16x32_bf16 v[128:131], v[40:43], v[0:3], v[128:131]\n"
      "s_lshl1_add_u32 s42, %[blk], s42\ns_and_b32 s69, s42, 15\nv_mfma_f32_16x16x32_bf16 v[152:155], v[56:59], v[8:11], v[152:155]\nds_read_b128 v[0:3], v96 offset:6144\ns_lshl_b32 s43, s69, 7\n"
      "v_mfma_f32_16x16x32_bf16 v[152:155], v[60:63], v[12:15], v[152:155]\nds_read_b128 v[4:7], v97 offset:6144\ns_add_u32 s43, s43, 32768\n"
      "v_mfma_f32_16x16x32_bf16 v[148:151], v[52:55], v[12:15], v[148:151]\ns_add_u32 s8, s20, s43\ns_addc_u32 s9, s21, 0\nv_mfma_f32_16x16x32_bf16 v[148:151], v[48:51], v[8:11], v[148:151]\n"
      "s_add_u32 s12, s20, 65536\ns_addc_u32 s13, s21, 0\nv_mfma_f32_16x16x32_bf16 v[144:147], v[40:43], v[8:11], v[144:147]\ns_add_u32 s16, s20, 2052096\ns_addc_u32 s17, s21, 0\n"
      "v_mfma_f32_16x16x32_bf16 v[144:147], v[44:47], v[12:15], v[144:147]\ns_lshl_b32 s42, s69, 4\ns_max_u32 s42, s42, 24\nv_mfma_f32_16x16x32_bf16 v[140:143], v[36:39], v[12:15], v[140:143]\n"
      "s_waitcnt lgkmcnt(4)\ns_sub_u32 s42, s42, 24\nv_mfma_f32_16x16x32_bf16 v[140:143], v[32:35], v[8:11], v[140:143]\ns_min_u32 s69, s42, 192\ns_lshl_b32 s42, s69, 3\n"
      "v_mfma_f32_16x16x32_bf16 v[156:159], v[32:35], v[16:19], v[156:159]\nds_read_b128 v[8:11], v96 offset:8192\ns_add_u32 s42, s42, 24576\n"
      "v_mfma_f32_16x16x32_bf16 v[156:159], v[36:39], v[20:23], v[156:159]\nds_read_b128 v[12:15], v97 offset:8192\ns_add_u32 s14, s20, s42\n"
      "v_mfma_f32_16x16x32_bf16 v[160:163], v[44:47], v[20:23], v[160:163]\ns_addc_u32 s15, s21, 0\nv_mfma_f32_16x16x32_bf16 v[160:163], v[40:43], v[16:19], v[160:163]\n"
      "v_mfma_f32_16x16x32_bf16 v[164:167], v[48:51], v[16:19], v[164:167]\nv_mfma_f32_16x16x32_bf16 v[164:167], v[52:55], v[20:23], v[164:167]\n"
      "v_mfma_f32_16x16x32_bf16 v[168:171], v[60:63], v[20:23], v[168:171]\ns_waitcnt lgkmcnt(2)\nv_mfma_f32_16x16x32_bf16 v[168:171], v[56:59], v[16:19], v[168:171]\n"
      "v_mfma_f32_16x16x32_bf16 v[184:187], v[56:59], v[24:27], v[184:187]\nds_read_b128 v[16:19], v96 offset:10240\nv_mfma_f32_16x16x32_bf16 v[184:187], v[60:63], v[28:31], v[184:187]\n"
      "ds_read_b128 v[20:23], v97 offset:10240\nv_mfma_f32_16x16x32_bf16 v[180:183], v[52:55], v[28:31], v[180:183]\nv_mfma_f32_16x16x32_bf16 v[180:183], v[48:51], v[24:27], v[180:183]\n"
      "v_mfma_f32_16x16x32_bf16 v[176:179], v[40:43], v[24:27], v[176:179]\nv_mfma_f32_16x16x32_bf16 v[176:179], v[44:47], v[28:31], v[176:179]\n"
      "v_mfma_f32_16x16x32_bf16 v[172:175], v[36:39], v[28:31], v[172:175]\ns_waitcnt vmcnt(0) lgkmcnt(0)\ns_barrier\nv_mfma_f32_16x16x32_bf16 v[172:175], v[32:35], v[24:27], v[172:175]\n"
      "ds_read_b128 v[72:75], v252 offset:0\nv_lshl_add_u32 v65, v240, 1, v247\nv_mfma_f32_16x16x32_bf16 v[188:191], v[32:35], v[0:3], v[188:191]\nds_read_b128 v[76:79], v252 offset:1024\n"
      "ds_read_u16 v251, v65 offset:64\nv_mfma_f32_16x16x32_bf16 v[188:191], v[36:39], v[4:7], v[188:191]\nds_read_b128 v[80:83], v252 offset:2048\nds_read_b32 v66, v247 offset:24\n"
      "v_mfma_f32_16x16x32_bf16 v[192:195], v[44:47], v[4:7], v[192:195]\nds_read_b128 v[84:87], v252 offset:3072\nv_and_b32 v64, 48, v240\n"
      "v_mfma_f32_16x16x32_bf16 v[192:195], v[40:43], v[0:3], v[192:195]\nds_read_b128 v[88:91], v252 offset:4096\ns_mov_b32 s42, 87296\n"
      "v_mfma_f32_16x16x32_bf16 v[196:199], v[48:51], v[0:3], v[196:199]\nds_read_b128 v[92:95], v252 offset:5120\nv_lshl_add_u32 v64, v64, 2, s42\n"
      "v_mfma_f32_16x16x32_bf16 v[196:199], v[52:55], v[4:7], v[196:199]\nds_read_b128 v[96:99], v252 offset:6144\ns_mov_b32 s58, 0xbdd2d3e7\n"
      "v_mfma_f32_16x16x32_bf16 v[200:203], v[60:63], v[4:7], v[200:203]\nds_read_b128 v[100:103], v252 offset:7168\ns_mov_b32 s59, 0xc0135761\n"
      "v_mfma_f32_16x16x32_bf16 v[200:203], v[56:59], v[0:3], v[200:203]\nds_read_b128 v[104:107], v252 offset:8192\ns_mov_b32 s60, 0xc1f00000\n"
      "v_mfma_f32_16x16x32_bf16 v[216:219], v[56:59], v[8:11], v[216:219]\nds_read_b128 v[0:3], v236 offset:0\ns_mov_b32 s61, 0x30800000\n"
      "v_mfma_f32_16x16x32_bf16 v[216:219], v[60:63], v[12:15], v[216:219]\nds_read_b128 v[4:7], v236 offset:64\ns_add_u32 s49, s65, 928\n"
      "v_mfma_f32_16x16x32_bf16 v[212:215], v[52:55], v[12:15], v[212:215]\nds_read_b128 v[108:111], v252 offset:9216\ns_mov_b32 s45, 1\n"
      "v_mfma_f32_16x16x32_bf16 v[212:215], v[48:51], v[8:11], v[212:215]\nds_read_b128 v[112:115], v252 offset:10240\ns_mov_b32 s46, 0\n"
      "v_mfma_f32_16x16x32_bf16 v[208:211], v[40:43], v[8:11], v[208:211]\nds_read_b128 v[116:119], v252 offset:11264\ns_waitcnt lgkmcnt(8)\n"
      "v_mfma_f32_16x16x32_bf16 v[208:211], v[44:47], v[12:15], v[208:211]\nds_read_b128 v[120:123], v252 offset:12288\nv_readfirstlane_b32 s39, v66\n"
      "v_mfma_f32_16x16x32_bf16 v[204:207], v[36:39], v[12:15], v[204:207]\nds_read_b128 v[132:135], v252 offset:15360\nv_readlane_b32 s55, v251, 0\n"
      "v_mfma_f32_16x16x32_bf16 v[204:207], v[32:35], v[8:11], v[204:207]\nds_read_b128 v[136:139], v252 offset:16384\ns_setprio 3\n"
      "v_mfma_f32_16x16x32_bf16 v[220:223], v[32:35], v[16:19], v[220:223]\nds_read_b128 v[8:11], v237 offset:0\ns_xor_b32 s40, s39, 0xa5c30f96\n"
      "v_mfma_f32_16x16x32_bf16 v[220:223], v[36:39], v[20:23], v[220:223]\nds_read_b128 v[12:15], v237 offset:64\ns_mul_i32 s41, s39, 0x9E3779B1\n"
      "v_mfma_f32_16x16x32_bf16 v[224:227], v[44:47], v[20:23], v[224:227]\ns_lshr_b32 s41, s41, 24\nv_mfma_f32_16x16x32_bf16 v[224:227], v[40:43], v[16:19], v[224:227]\n"
      "v_add_u32_sdwa v239, s49, v243 dst_sel:DWORD dst_unused:UNUSED_PAD src0_sel:DWORD src1_sel:WORD_1\nv_mfma_f32_16x16x32_bf16 v[228:231], v[48:51], v[16:19], v[228:231]\n"
      "v_add_u32_sdwa v236, s49, v242 dst_sel:DWORD dst_unused:UNUSED_PAD src0_sel:DWORD src1_sel:WORD_0\nv_mfma_f32_16x16x32_bf16 v[228:231], v[52:55], v[20:23], v[228:231]\n"
      "v_add_u32_sdwa v237, s49, v242 dst_sel:DWORD dst_unused:UNUSED_PAD src0_sel:DWORD src1_sel:WORD_1\nv_mfma_f32_16x16x32_bf16 v[232:235], v[60:63], v[20:23], v[232:235]\n"
      "v_add_u32_sdwa v238, s49, v243 dst_sel:DWORD dst_unused:UNUSED_PAD src0_sel:DWORD src1_sel:WORD_0\ns_waitcnt lgkmcnt(0)\nv_mfma_f32_16x16x32_bf16 v[232:235], v[56:59], v[16:19], v[232:235]\n"
      "v_pk_mul_f32 v[16:17], v[124:125], v[124:125]\nv_pk_mul_f32 v[18:19], v[126:127], v[126:127]\nv_pk_mul_f32 v[20:21], v[128:129], v[128:129]\nv_pk_mul_f32 v[22:23], v[130:131], v[130:131]\n"
      "v_pk_fma_f32 v[16:17], v[16:17], s[58:59], s[58:59] op_sel:[0,0,1] op_sel_hi:[1,0,1]\nv_pk_fma_f32 v[18:19], v[18:19], s[58:59], s[58:59] op_sel:[0,0,1] op_sel_hi:[1,0,1]\n"
      "v_pk_fma_f32 v[20:21], v[20:21], s[58:59], s[58:59] op_sel:[0,0,1] op_sel_hi:[1,0,1]\nv_pk_fma_f32 v[22:23], v[22:23], s[58:59], s[58:59] op_sel:[0,0,1] op_sel_hi:[1,0,1]\n"
      "v_pk_fma_f32 v[16:17], v[16:17], v[124:125], s[60:61] op_sel_hi:[1,1,0]\nv_pk_fma_f32 v[18:19], v[18:19], v[126:127], s[60:61] op_sel_hi:[1,1,0]\n"
      "v_pk_fma_f32 v[20:21], v[20:21], v[128:129], s[60:61] op_sel_hi:[1,1,0]\nv_pk_fma_f32 v[22:23], v[22:23], v[130:131], s[60:61] op_sel_hi:[1,1,0]\n"
      "v_pk_mul_f32 v[124:125], v[124:125], v[0:1]\nv_pk_mul_f32 v[126:127], v[126:127], v[2:3]\nv_pk_mul_f32 v[128:129], v[128:129], v[4:5]\nv_pk_mul_f32 v[130:131], v[130:131], v[6:7]\n"
      "v_exp_f32 v16, v16 clamp\nv_exp_f32 v17, v17 clamp\nv_exp_f32 v18, v18 clamp\nv_exp_f32 v19, v19 clamp\nv_exp_f32 v20, v20 clamp\nv_exp_f32 v21, v21 clamp\nv_exp_f32 v22, v22 clamp\n"
      "v_exp_f32 v23, v23 clamp\nv_pk_add_f32 v[16:17], v[16:17], s[60:61] op_sel:[0,1] op_sel_hi:[1,1]\nv_pk_add_f32 v[18:19], v[18:19], s[60:61] op_sel:[0,1] op_sel_hi:[1,1]\n"
      "v_pk_add_f32 v[20:21], v[20:21], s[60:61] op_sel:[0,1] op_sel_hi:[1,1]\nv_pk_add_f32 v[22:23], v[22:23], s[60:61] op_sel:[0,1] op_sel_hi:[1,1]\n"
      "v_pk_mul_f32 v[124:125], v[124:125], v[18:19]\nv_pk_mul_f32 v[128:129], v[128:129], v[22:23]\nv_pk_fma_f32 v[124:125], v[126:127], v[16:17], v[124:125]\n"
      "v_pk_fma_f32 v[128:129], v[130:131], v[20:21], v[128:129]\nv_pk_mul_f32 v[16:17], v[16:17], v[18:19]\nv_pk_mul_f32 v[20:21], v[20:21], v[22:23]\nv_pk_mul_f32 v[18:19], v[16:17], v[20:21]\n"
      "v_pk_mul_f32 v[24:25], v[140:141], v[140:141]\nv_pk_mul_f32 v[124:125], v[124:125], v[20:21]\nv_pk_mul_f32 v[26:27], v[142:143], v[142:143]\nv_rcp_f32 v18, v18\n"
      "v_pk_mul_f32 v[28:29], v[144:145], v[144:145]\nv_rcp_f32 v19, v19\nv_pk_mul_f32 v[30:31], v[146:147], v[146:147]\nv_pk_fma_f32 v[124:125], v[128:129], v[16:17], v[124:125]\n"
      "v_pk_fma_f32 v[24:25], v[24:25], s[58:59], s[58:59] op_sel:[0,0,1] op_sel_hi:[1,0,1]\nv_pk_mul_f32 v[32:33], v[124:125], v[18:19]\n"
      "v_pk_fma_f32 v[26:27], v[26:27], s[58:59], s[58:59] op_sel:[0,0,1] op_sel_hi:[1,0,1]\nv_add_f32 v42, v32, v33\n"
      "v_pk_fma_f32 v[28:29], v[28:29], s[58:59], s[58:59] op_sel:[0,0,1] op_sel_hi:[1,0,1]\nv_pk_fma_f32 v[30:31], v[30:31], s[58:59], s[58:59] op_sel:[0,0,1] op_sel_hi:[1,0,1]\n"
      "v_pk_fma_f32 v[24:25], v[24:25], v[140:141], s[60:61] op_sel_hi:[1,1,0]\nds_read_b128 v[124:127], v252 offset:13312\nv_pk_fma_f32 v[26:27], v[26:27], v[142:143], s[60:61] op_sel_hi:[1,1,0]\n"
      "ds_read_b128 v[128:131], v252 offset:14336\nv_pk_fma_f32 v[28:29], v[28:29], v[144:145], s[60:61] op_sel_hi:[1,1,0]\nv_pk_fma_f32 v[30:31], v[30:31], v[146:147], s[60:61] op_sel_hi:[1,1,0]\n"
      "v_pk_mul_f32 v[140:141], v[140:141], v[0:1]\nv_pk_mul_f32 v[142:143], v[142:143], v[2:3]\nv_pk_mul_f32 v[144:145], v[144:145], v[4:5]\nv_pk_mul_f32 v[146:147], v[146:147], v[6:7]\n"
      "v_exp_f32 v24, v24 clamp\nv_exp_f32 v25, v25 clamp\nv_exp_f32 v26, v26 clamp\nv_exp_f32 v27, v27 clamp\nv_exp_f32 v28, v28 clamp\nv_exp_f32 v29, v29 clamp\nv_exp_f32 v30, v30 clamp\n"
      "v_exp_f32 v31, v31 clamp\nv_pk_add_f32 v[24:25], v[24:25], s[60:61] op_sel:[0,1] op_sel_hi:[1,1]\nv_pk_add_f32 v[26:27], v[26:27], s[60:61] op_sel:[0,1] op_sel_hi:[1,1]\n"
      "v_pk_add_f32 v[28:29], v[28:29], s[60:61] op_sel:[0,1] op_sel_hi:[1,1]\nv_pk_add_f32 v[30:31], v[30:31], s[60:61] op_sel:[0,1] op_sel_hi:[1,1]\n"
      "v_pk_mul_f32 v[140:141], v[140:141], v[26:27]\nv_pk_mul_f32 v[144:145], v[144:145], v[30:31]\nv_pk_fma_f32 v[140:141], v[142:143], v[24:25], v[140:141]\n"
      "v_pk_fma_f32 v[144:145], v[146:147], v[28:29], v[144:145]\nv_pk_mul_f32 v[24:25], v[24:25], v[26:27]\nv_pk_mul_f32 v[28:29], v[28:29], v[30:31]\nv_pk_mul_f32 v[26:27], v[24:25], v[28:29]\n"
      "v_pk_mul_f32 v[16:17], v[148:149], v[148:149]\nv_pk_mul_f32 v[140:141], v[140:141], v[28:29]\nv_pk_mul_f32 v[18:19], v[150:151], v[150:151]\nv_rcp_f32 v26, v26\n"
      "v_pk_mul_f32 v[20:21], v[152:153], v[152:153]\nv_rcp_f32 v27, v27\nv_pk_mul_f32 v[22:23], v[154:155], v[154:155]\nv_pk_fma_f32 v[140:141], v[144:145], v[24:25], v[140:141]\n"
      "v_pk_fma_f32 v[16:17], v[16:17], s[58:59], s[58:59] op_sel:[0,0,1] op_sel_hi:[1,0,1]\nv_pk_mul_f32 v[32:33], v[140:141], v[26:27]\n"
      "v_pk_fma_f32 v[18:19], v[18:19], s[58:59], s[58:59] op_sel:[0,0,1] op_sel_hi:[1,0,1]\nv_pk_fma_f32 v[20:21], v[20:21], s[58:59], s[58:59] op_sel:[0,0,1] op_sel_hi:[1,0,1]\n"
      "v_pk_fma_f32 v[22:23], v[22:23], s[58:59], s[58:59] op_sel:[0,0,1] op_sel_hi:[1,0,1]\nv_pk_fma_f32 v[16:17], v[16:17], v[148:149], s[60:61] op_sel_hi:[1,1,0]\n"
      "ds_read_b128 v[140:143], v252 offset:17408\nv_pk_fma_f32 v[18:19], v[18:19], v[150:151], s[60:61] op_sel_hi:[1,1,0]\nds_read_b128 v[144:147], v252 offset:18432\n"
      "v_pk_fma_f32 v[20:21], v[20:21], v[152:153], s[60:61] op_sel_hi:[1,1,0]\nv_pk_fma_f32 v[22:23], v[22:23], v[154:155], s[60:61] op_sel_hi:[1,1,0]\n"
      "v_pk_mul_f32 v[148:149], v[148:149], v[8:9]\nv_pk_mul_f32 v[150:151], v[150:151], v[10:11]\nv_pk_mul_f32 v[152:153], v[152:153], v[12:13]\nv_pk_mul_f32 v[154:155], v[154:155], v[14:15]\n"
      "v_exp_f32 v16, v16 clamp\nv_exp_f32 v17, v17 clamp\nv_exp_f32 v18, v18 clamp\nv_exp_f32 v19, v19 clamp\nv_exp_f32 v20, v20 clamp\nv_exp_f32 v21, v21 clamp\nv_exp_f32 v22, v22 clamp\n"
      "v_exp_f32 v23, v23 clamp\nv_pk_add_f32 v[16:17], v[16:17], s[60:61] op_sel:[0,1] op_sel_hi:[1,1]\nv_pk_add_f32 v[18:19], v[18:19], s[60:61] op_sel:[0,1] op_sel_hi:[1,1]\n"
      "v_pk_add_f32 v[20:21], v[20:21], s[60:61] op_sel:[0,1] op_sel_hi:[1,1]\nv_pk_add_f32 v[22:23], v[22:23], s[60:61] op_sel:[0,1] op_sel_hi:[1,1]\n"
      "v_pk_mul_f32 v[148:149], v[148:149], v[18:19]\nv_pk_mul_f32 v[152:153], v[152:153], v[22:23]\nv_pk_fma_f32 v[148:149], v[150:151], v[16:17], v[148:149]\n"
      "v_pk_fma_f32 v[152:153], v[154:155], v[20:21], v[152:153]\nv_pk_mul_f32 v[16:17], v[16:17], v[18:19]\nv_pk_mul_f32 v[20:21], v[20:21], v[22:23]\nv_pk_mul_f32 v[18:19], v[16:17], v[20:21]\n"
      "v_pk_mul_f32 v[24:25], v[156:157], v[156:157]\nv_pk_mul_f32 v[148:149], v[148:149], v[20:21]\nv_pk_mul_f32 v[26:27], v[158:159], v[158:159]\nv_rcp_f32 v18, v18\n"
      "v_pk_mul_f32 v[28:29], v[160:161], v[160:161]\nv_rcp_f32 v19, v19\nv_pk_mul_f32 v[30:31], v[162:163], v[162:163]\nv_pk_fma_f32 v[148:149], v[152:153], v[16:17], v[148:149]\n"
      "v_pk_fma_f32 v[24:25], v[24:25], s[58:59], s[58:59] op_sel:[0,0,1] op_sel_hi:[1,0,1]\nv_pk_fma_f32 v[32:33], v[148:149], v[18:19], v[32:33]\n"
      "v_pk_fma_f32 v[26:27], v[26:27], s[58:59], s[58:59] op_sel:[0,0,1] op_sel_hi:[1,0,1]\nv_add_f32 v36, v32, v33\n"
      "v_pk_fma_f32 v[28:29], v[28:29], s[58:59], s[58:59] op_sel:[0,0,1] op_sel_hi:[1,0,1]\nv_pk_fma_f32 v[30:31], v[30:31], s[58:59], s[58:59] op_sel:[0,0,1] op_sel_hi:[1,0,1]\n"
      "v_pk_fma_f32 v[24:25], v[24:25], v[156:157], s[60:61] op_sel_hi:[1,1,0]\nds_read_b128 v[148:151], v252 offset:19456\nv_pk_fma_f32 v[26:27], v[26:27], v[158:159], s[60:61] op_sel_hi:[1,1,0]\n"
      "ds_read_b128 v[152:155], v252 offset:20480\nv_pk_fma_f32 v[28:29], v[28:29], v[160:161], s[60:61] op_sel_hi:[1,1,0]\nv_pk_fma_f32 v[30:31], v[30:31], v[162:163], s[60:61] op_sel_hi:[1,1,0]\n"
      "v_pk_mul_f32 v[156:157], v[156:157], v[0:1]\nv_pk_mul_f32 v[158:159], v[158:159], v[2:3]\nv_pk_mul_f32 v[160:161], v[160:161], v[4:5]\nv_pk_mul_f32 v[162:163], v[162:163], v[6:7]\n"
      "v_exp_f32 v24, v24 clamp\nv_exp_f32 v25, v25 clamp\nv_exp_f32 v26, v26 clamp\nv_exp_f32 v27, v27 clamp\nv_exp_f32 v28, v28 clamp\nv_exp_f32 v29, v29 clamp\nv_exp_f32 v30, v30 clamp\n"
      "v_exp_f32 v31, v31 clamp\nv_pk_add_f32 v[24:25], v[24:25], s[60:61] op_sel:[0,1] op_sel_hi:[1,1]\nv_pk_add_f32 v[26:27], v[26:27], s[60:61] op_sel:[0,1] op_sel_hi:[1,1]\n"
      "v_pk_add_f32 v[28:29], v[28:29], s[60:61] op_sel:[0,1] op_sel_hi:[1,1]\nv_pk_add_f32 v[30:31], v[30:31], s[60:61] op_sel:[0,1] op_sel_hi:[1,1]\n"
      "v_pk_mul_f32 v[156:157], v[156:157], v[26:27]\nv_pk_mul_f32 v[160:161], v[160:161], v[30:31]\nv_pk_fma_f32 v[156:157], v[158:159], v[24:25], v[156:157]\n"
      "v_pk_fma_f32 v[160:161], v[162:163], v[28:29], v[160:161]\nv_pk_mul_f32 v[24:25], v[24:25], v[26:27]\nv_pk_mul_f32 v[28:29], v[28:29], v[30:31]\nv_pk_mul_f32 v[26:27], v[24:25], v[28:29]\n"
      "v_pk_mul_f32 v[16:17], v[164:165], v[164:165]\nv_pk_mul_f32 v[156:157], v[156:157], v[28:29]\nv_pk_mul_f32 v[18:19], v[166:167], v[166:167]\nv_rcp_f32 v26, v26\n"
      "v_pk_mul_f32 v[20:21], v[168:169], v[168:169]\nv_rcp_f32 v27, v27\nv_pk_mul_f32 v[22:23], v[170:171], v[170:171]\nv_pk_fma_f32 v[156:157], v[160:161], v[24:25], v[156:157]\n"
      "v_pk_fma_f32 v[16:17], v[16:17], s[58:59], s[58:59] op_sel:[0,0,1] op_sel_hi:[1,0,1]\nv_pk_mul_f32 v[32:33], v[156:157], v[26:27]\n"
      "v_pk_fma_f32 v[18:19], v[18:19], s[58:59], s[58:59] op_sel:[0,0,1] op_sel_hi:[1,0,1]\nv_pk_fma_f32 v[20:21], v[20:21], s[58:59], s[58:59] op_sel:[0,0,1] op_sel_hi:[1,0,1]\n"
      "v_pk_fma_f32 v[22:23], v[22:23], s[58:59], s[58:59] op_sel:[0,0,1] op_sel_hi:[1,0,1]\nv_pk_fma_f32 v[16:17], v[16:17], v[164:165], s[60:61] op_sel_hi:[1,1,0]\n"
      "ds_read_b128 v[156:159], v252 offset:21504\nv_pk_fma_f32 v[18:19], v[18:19], v[166:167], s[60:61] op_sel_hi:[1,1,0]\nds_read_b128 v[160:163], v252 offset:22528\n"
      "v_pk_fma_f32 v[20:21], v[20:21], v[168:169], s[60:61] op_sel_hi:[1,1,0]\nv_pk_fma_f32 v[22:23], v[22:23], v[170:171], s[60:61] op_sel_hi:[1,1,0]\n"
      "v_pk_mul_f32 v[164:165], v[164:165], v[8:9]\nv_pk_mul_f32 v[166:167], v[166:167], v[10:11]\nv_pk_mul_f32 v[168:169], v[168:169], v[12:13]\nv_pk_mul_f32 v[170:171], v[170:171], v[14:15]\n"
      "v_exp_f32 v16, v16 clamp\nv_exp_f32 v17, v17 clamp\nv_exp_f32 v18, v18 clamp\nv_exp_f32 v19, v19 clamp\nv_exp_f32 v20, v20 clamp\nv_exp_f32 v21, v21 clamp\nv_exp_f32 v22, v22 clamp\n"
      "v_exp_f32 v23, v23 clamp\nv_pk_add_f32 v[16:17], v[16:17], s[60:61] op_sel:[0,1] op_sel_hi:[1,1]\nv_pk_add_f32 v[18:19], v[18:19], s[60:61] op_sel:[0,1] op_sel_hi:[1,1]\n"
      "v_pk_add_f32 v[20:21], v[20:21], s[60:61] op_sel:[0,1] op_sel_hi:[1,1]\nv_pk_add_f32 v[22:23], v[22:23], s[60:61] op_sel:[0,1] op_sel_hi:[1,1]\n"
      "v_pk_mul_f32 v[164:165], v[164:165], v[18:19]\nv_pk_mul_f32 v[168:169], v[168:169], v[22:23]\nv_pk_fma_f32 v[164:165], v[166:167], v[16:17], v[164:165]\n"
      "v_pk_fma_f32 v[168:169], v[170:171], v[20:21], v[168:169]\nv_pk_mul_f32 v[16:17], v[16:17], v[18:19]\nv_pk_mul_f32 v[20:21], v[20:21], v[22:23]\nv_pk_mul_f32 v[18:19], v[16:17], v[20:21]\n"
      "v_pk_mul_f32 v[24:25], v[172:173], v[172:173]\nv_pk_mul_f32 v[164:165], v[164:165], v[20:21]\nv_pk_mul_f32 v[26:27], v[174:175], v[174:175]\nv_rcp_f32 v18, v18\n"
      "v_pk_mul_f32 v[28:29], v[176:177], v[176:177]\nv_rcp_f32 v19, v19\nv_pk_mul_f32 v[30:31], v[178:179], v[178:179]\nv_pk_fma_f32 v[164:165], v[168:169], v[16:17], v[164:165]\n"
      "v_pk_fma_f32 v[24:25], v[24:25], s[58:59], s[58:59] op_sel:[0,0,1] op_sel_hi:[1,0,1]\nv_pk_fma_f32 v[32:33], v[164:165], v[18:19], v[32:33]\n"
      "v_pk_fma_f32 v[26:27], v[26:27], s[58:59], s[58:59] op_sel:[0,0,1] op_sel_hi:[1,0,1]\nv_add_f32 v37, v32, v33\n"
      "v_pk_fma_f32 v[28:29], v[28:29], s[58:59], s[58:59] op_sel:[0,0,1] op_sel_hi:[1,0,1]\nv_pk_fma_f32 v[30:31], v[30:31], s[58:59], s[58:59] op_sel:[0,0,1] op_sel_hi:[1,0,1]\n"
      "v_pk_fma_f32 v[24:25], v[24:25], v[172:173], s[60:61] op_sel_hi:[1,1,0]\nds_read_b128 v[164:167], v252 offset:23552\nv_pk_fma_f32 v[26:27], v[26:27], v[174:175], s[60:61] op_sel_hi:[1,1,0]\n"
      "ds_read_b128 v[168:171], v252 offset:24576\nv_pk_fma_f32 v[28:29], v[28:29], v[176:177], s[60:61] op_sel_hi:[1,1,0]\nv_pk_fma_f32 v[30:31], v[30:31], v[178:179], s[60:61] op_sel_hi:[1,1,0]\n"
      "v_pk_mul_f32 v[172:173], v[172:173], v[0:1]\nv_pk_mul_f32 v[174:175], v[174:175], v[2:3]\nv_pk_mul_f32 v[176:177], v[176:177], v[4:5]\nv_pk_mul_f32 v[178:179], v[178:179], v[6:7]\n"
      "v_exp_f32 v24, v24 clamp\nv_exp_f32 v25, v25 clamp\nv_exp_f32 v26, v26 clamp\nv_exp_f32 v27, v27 clamp\nv_exp_f32 v28, v28 clamp\nv_exp_f32 v29, v29 clamp\nv_exp_f32 v30, v30 clamp\n"
      "v_exp_f32 v31, v31 clamp\nv_pk_add_f32 v[24:25], v[24:25], s[60:61] op_sel:[0,1] op_sel_hi:[1,1]\nv_pk_add_f32 v[26:27], v[26:27], s[60:61] op_sel:[0,1] op_sel_hi:[1,1]\n"
      "v_pk_add_f32 v[28:29], v[28:29], s[60:61] op_sel:[0,1] op_sel_hi:[1,1]\nv_pk_add_f32 v[30:31], v[30:31], s[60:61] op_sel:[0,1] op_sel_hi:[1,1]\n"
      "v_pk_mul_f32 v[172:173], v[172:173], v[26:27]\nv_pk_mul_f32 v[176:177], v[176:177], v[30:31]\nv_pk_fma_f32 v[172:173], v[174:175], v[24:25], v[172:173]\n"
      "v_pk_fma_f32 v[176:177], v[178:179], v[28:29], v[176:177]\nv_pk_mul_f32 v[24:25], v[24:25], v[26:27]\nv_pk_mul_f32 v[28:29], v[28:29], v[30:31]\nv_pk_mul_f32 v[26:27], v[24:25], v[28:29]\n"
      "v_pk_mul_f32 v[16:17], v[180:181], v[180:181]\nv_pk_mul_f32 v[172:173], v[172:173], v[28:29]\nv_pk_mul_f32 v[18:19], v[182:183], v[182:183]\nv_rcp_f32 v26, v26\n"
      "v_pk_mul_f32 v[20:21], v[184:185], v[184:185]\nv_rcp_f32 v27, v27\nv_pk_mul_f32 v[22:23], v[186:187], v[186:187]\nv_pk_fma_f32 v[172:173], v[176:177], v[24:25], v[172:173]\n"
      "v_pk_fma_f32 v[16:17], v[16:17], s[58:59], s[58:59] op_sel:[0,0,1] op_sel_hi:[1,0,1]\nv_pk_mul_f32 v[32:33], v[172:173], v[26:27]\n"
      "v_pk_fma_f32 v[18:19], v[18:19], s[58:59], s[58:59] op_sel:[0,0,1] op_sel_hi:[1,0,1]\nv_pk_fma_f32 v[20:21], v[20:21], s[58:59], s[58:59] op_sel:[0,0,1] op_sel_hi:[1,0,1]\n"
      "v_pk_fma_f32 v[22:23], v[22:23], s[58:59], s[58:59] op_sel:[0,0,1] op_sel_hi:[1,0,1]\nv_pk_fma_f32 v[16:17], v[16:17], v[180:181], s[60:61] op_sel_hi:[1,1,0]\n"
      "ds_read_b128 v[172:175], v252 offset:25600\nv_pk_fma_f32 v[18:19], v[18:19], v[182:183], s[60:61] op_sel_hi:[1,1,0]\nds_read_b128 v[176:179], v252 offset:26624\n"
      "v_pk_fma_f32 v[20:21], v[20:21], v[184:185], s[60:61] op_sel_hi:[1,1,0]\nv_pk_fma_f32 v[22:23], v[22:23], v[186:187], s[60:61] op_sel_hi:[1,1,0]\n"
      "v_pk_mul_f32 v[180:181], v[180:181], v[8:9]\nv_pk_mul_f32 v[182:183], v[182:183], v[10:11]\nv_pk_mul_f32 v[184:185], v[184:185], v[12:13]\nv_pk_mul_f32 v[186:187], v[186:187], v[14:15]\n"
      "v_exp_f32 v16, v16 clamp\nv_exp_f32 v17, v17 clamp\nv_exp_f32 v18, v18 clamp\nv_exp_f32 v19, v19 clamp\nv_exp_f32 v20, v20 clamp\nv_exp_f32 v21, v21 clamp\nv_exp_f32 v22, v22 clamp\n"
      "v_exp_f32 v23, v23 clamp\nv_pk_add_f32 v[16:17], v[16:17], s[60:61] op_sel:[0,1] op_sel_hi:[1,1]\nv_pk_add_f32 v[18:19], v[18:19], s[60:61] op_sel:[0,1] op_sel_hi:[1,1]\n"
      "v_pk_add_f32 v[20:21], v[20:21], s[60:61] op_sel:[0,1] op_sel_hi:[1,1]\nv_pk_add_f32 v[22:23], v[22:23], s[60:61] op_sel:[0,1] op_sel_hi:[1,1]\n"
      "v_pk_mul_f32 v[180:181], v[180:181], v[18:19]\nv_pk_mul_f32 v[184:185], v[184:185], v[22:23]\nv_pk_fma_f32 v[180:181], v[182:183], v[16:17], v[180:181]\n"
      "v_pk_fma_f32 v[184:185], v[186:187], v[20:21], v[184:185]\nv_pk_mul_f32 v[16:17], v[16:17], v[18:19]\nv_pk_mul_f32 v[20:21], v[20:21], v[22:23]\nv_pk_mul_f32 v[18:19], v[16:17], v[20:21]\n"
      "v_pk_mul_f32 v[24:25], v[188:189], v[188:189]\nv_pk_mul_f32 v[180:181], v[180:181], v[20:21]\nv_pk_mul_f32 v[26:27], v[190:191], v[190:191]\nv_rcp_f32 v18, v18\n"
      "v_pk_mul_f32 v[28:29], v[192:193], v[192:193]\nv_rcp_f32 v19, v19\nv_pk_mul_f32 v[30:31], v[194:195], v[194:195]\nv_pk_fma_f32 v[180:181], v[184:185], v[16:17], v[180:181]\n"
      "v_pk_fma_f32 v[24:25], v[24:25], s[58:59], s[58:59] op_sel:[0,0,1] op_sel_hi:[1,0,1]\nv_pk_fma_f32 v[32:33], v[180:181], v[18:19], v[32:33]\n"
      "v_pk_fma_f32 v[26:27], v[26:27], s[58:59], s[58:59] op_sel:[0,0,1] op_sel_hi:[1,0,1]\nv_add_f32 v38, v32, v33\n"
      "v_pk_fma_f32 v[28:29], v[28:29], s[58:59], s[58:59] op_sel:[0,0,1] op_sel_hi:[1,0,1]\nv_pk_fma_f32 v[30:31], v[30:31], s[58:59], s[58:59] op_sel:[0,0,1] op_sel_hi:[1,0,1]\n"
      "v_pk_fma_f32 v[24:25], v[24:25], v[188:189], s[60:61] op_sel_hi:[1,1,0]\nds_read_b128 v[180:183], v252 offset:27648\nv_pk_fma_f32 v[26:27], v[26:27], v[190:191], s[60:61] op_sel_hi:[1,1,0]\n"
      "ds_read_b128 v[184:187], v252 offset:28672\nv_pk_fma_f32 v[28:29], v[28:29], v[192:193], s[60:61] op_sel_hi:[1,1,0]\nv_pk_fma_f32 v[30:31], v[30:31], v[194:195], s[60:61] op_sel_hi:[1,1,0]\n"
      "v_pk_mul_f32 v[188:189], v[188:189], v[0:1]\nv_pk_mul_f32 v[190:191], v[190:191], v[2:3]\nv_pk_mul_f32 v[192:193], v[192:193], v[4:5]\nv_pk_mul_f32 v[194:195], v[194:195], v[6:7]\n"
      "v_exp_f32 v24, v24 clamp\nv_exp_f32 v25, v25 clamp\nv_exp_f32 v26, v26 clamp\nv_exp_f32 v27, v27 clamp\nv_exp_f32 v28, v28 clamp\nv_exp_f32 v29, v29 clamp\nv_exp_f32 v30, v30 clamp\n"
      "v_exp_f32 v31, v31 clamp\nv_pk_add_f32 v[24:25], v[24:25], s[60:61] op_sel:[0,1] op_sel_hi:[1,1]\nv_pk_add_f32 v[26:27], v[26:27], s[60:61] op_sel:[0,1] op_sel_hi:[1,1]\n"
      "v_pk_add_f32 v[28:29], v[28:29], s[60:61] op_sel:[0,1] op_sel_hi:[1,1]\nv_pk_add_f32 v[30:31], v[30:31], s[60:61] op_sel:[0,1] op_sel_hi:[1,1]\n"
      "v_pk_mul_f32 v[188:189], v[188:189], v[26:27]\nv_pk_mul_f32 v[192:193], v[192:193], v[30:31]\nv_pk_fma_f32 v[188:189], v[190:191], v[24:25], v[188:189]\n"
      "v_pk_fma_f32 v[192:193], v[194:195], v[28:29], v[192:193]\nv_pk_mul_f32 v[24:25], v[24:25], v[26:27]\nv_pk_mul_f32 v[28:29], v[28:29], v[30:31]\nv_pk_mul_f32 v[26:27], v[24:25], v[28:29]\n"
      "v_pk_mul_f32 v[16:17], v[196:197], v[196:197]\nv_pk_mul_f32 v[188:189], v[188:189], v[28:29]\nv_pk_mul_f32 v[18:19], v[198:199], v[198:199]\nv_rcp_f32 v26, v26\n"
      "v_pk_mul_f32 v[20:21], v[200:201], v[200:201]\nv_rcp_f32 v27, v27\nv_pk_mul_f32 v[22:23], v[202:203], v[202:203]\nv_pk_fma_f32 v[188:189], v[192:193], v[24:25], v[188:189]\n"
      "v_pk_fma_f32 v[16:17], v[16:17], s[58:59], s[58:59] op_sel:[0,0,1] op_sel_hi:[1,0,1]\nv_pk_mul_f32 v[32:33], v[188:189], v[26:27]\n"
      "v_pk_fma_f32 v[18:19], v[18:19], s[58:59], s[58:59] op_sel:[0,0,1] op_sel_hi:[1,0,1]\nv_pk_fma_f32 v[20:21], v[20:21], s[58:59], s[58:59] op_sel:[0,0,1] op_sel_hi:[1,0,1]\n"
      "v_pk_fma_f32 v[22:23], v[22:23], s[58:59], s[58:59] op_sel:[0,0,1] op_sel_hi:[1,0,1]\nv_pk_fma_f32 v[16:17], v[16:17], v[196:197], s[60:61] op_sel_hi:[1,1,0]\n"
      "ds_read_b128 v[188:191], v252 offset:29696\nv_pk_fma_f32 v[18:19], v[18:19], v[198:199], s[60:61] op_sel_hi:[1,1,0]\nds_read_b128 v[192:195], v252 offset:30720\n"
      "v_pk_fma_f32 v[20:21], v[20:21], v[200:201], s[60:61] op_sel_hi:[1,1,0]\nv_pk_fma_f32 v[22:23], v[22:23], v[202:203], s[60:61] op_sel_hi:[1,1,0]\n"
      "v_pk_mul_f32 v[196:197], v[196:197], v[8:9]\nv_pk_mul_f32 v[198:199], v[198:199], v[10:11]\nv_pk_mul_f32 v[200:201], v[200:201], v[12:13]\nv_pk_mul_f32 v[202:203], v[202:203], v[14:15]\n"
      "v_exp_f32 v16, v16 clamp\nv_exp_f32 v17, v17 clamp\nv_exp_f32 v18, v18 clamp\nv_exp_f32 v19, v19 clamp\nv_exp_f32 v20, v20 clamp\nv_exp_f32 v21, v21 clamp\nv_exp_f32 v22, v22 clamp\n"
      "v_exp_f32 v23, v23 clamp\nv_pk_add_f32 v[16:17], v[16:17], s[60:61] op_sel:[0,1] op_sel_hi:[1,1]\nv_pk_add_f32 v[18:19], v[18:19], s[60:61] op_sel:[0,1] op_sel_hi:[1,1]\n"
      "v_pk_add_f32 v[20:21], v[20:21], s[60:61] op_sel:[0,1] op_sel_hi:[1,1]\nv_pk_add_f32 v[22:23], v[22:23], s[60:61] op_sel:[0,1] op_sel_hi:[1,1]\n"
      "v_pk_mul_f32 v[196:197], v[196:197], v[18:19]\nv_pk_mul_f32 v[200:201], v[200:201], v[22:23]\nv_pk_fma_f32 v[196:197], v[198:199], v[16:17], v[196:197]\n"
      "v_pk_fma_f32 v[200:201], v[202:203], v[20:21], v[200:201]\nv_pk_mul_f32 v[16:17], v[16:17], v[18:19]\nv_pk_mul_f32 v[20:21], v[20:21], v[22:23]\nv_pk_mul_f32 v[18:19], v[16:17], v[20:21]\n"
      "v_pk_mul_f32 v[24:25], v[204:205], v[204:205]\nv_pk_mul_f32 v[196:197], v[196:197], v[20:21]\nv_pk_mul_f32 v[26:27], v[206:207], v[206:207]\nv_rcp_f32 v18, v18\n"
      "v_pk_mul_f32 v[28:29], v[208:209], v[208:209]\nv_rcp_f32 v19, v19\nv_pk_mul_f32 v[30:31], v[210:211], v[210:211]\nv_pk_fma_f32 v[196:197], v[200:201], v[16:17], v[196:197]\n"
      "v_pk_fma_f32 v[24:25], v[24:25], s[58:59], s[58:59] op_sel:[0,0,1] op_sel_hi:[1,0,1]\nv_pk_fma_f32 v[32:33], v[196:197], v[18:19], v[32:33]\n"
      "v_pk_fma_f32 v[26:27], v[26:27], s[58:59], s[58:59] op_sel:[0,0,1] op_sel_hi:[1,0,1]\nv_add_f32 v39, v32, v33\n"
      "v_pk_fma_f32 v[28:29], v[28:29], s[58:59], s[58:59] op_sel:[0,0,1] op_sel_hi:[1,0,1]\nv_pk_fma_f32 v[30:31], v[30:31], s[58:59], s[58:59] op_sel:[0,0,1] op_sel_hi:[1,0,1]\n"
      "v_pk_fma_f32 v[24:25], v[24:25], v[204:205], s[60:61] op_sel_hi:[1,1,0]\nds_read_b128 v[196:199], v252 offset:31744\nv_pk_fma_f32 v[26:27], v[26:27], v[206:207], s[60:61] op_sel_hi:[1,1,0]\n"
      "ds_read_b128 v[200:203], v252 offset:32768\nv_pk_fma_f32 v[28:29], v[28:29], v[208:209], s[60:61] op_sel_hi:[1,1,0]\nv_pk_fma_f32 v[30:31], v[30:31], v[210:211], s[60:61] op_sel_hi:[1,1,0]\n"
      "v_pk_mul_f32 v[204:205], v[204:205], v[0:1]\nv_pk_mul_f32 v[206:207], v[206:207], v[2:3]\nv_pk_mul_f32 v[208:209], v[208:209], v[4:5]\nv_pk_mul_f32 v[210:211], v[210:211], v[6:7]\n"
      "v_exp_f32 v24, v24 clamp\nv_exp_f32 v25, v25 clamp\nv_exp_f32 v26, v26 clamp\nv_exp_f32 v27, v27 clamp\nv_exp_f32 v28, v28 clamp\nv_exp_f32 v29, v29 clamp\nv_exp_f32 v30, v30 clamp\n"
      "v_exp_f32 v31, v31 clamp\nv_pk_add_f32 v[24:25], v[24:25], s[60:61] op_sel:[0,1] op_sel_hi:[1,1]\nv_pk_add_f32 v[26:27], v[26:27], s[60:61] op_sel:[0,1] op_sel_hi:[1,1]\n"
      "v_pk_add_f32 v[28:29], v[28:29], s[60:61] op_sel:[0,1] op_sel_hi:[1,1]\nv_pk_add_f32 v[30:31], v[30:31], s[60:61] op_sel:[0,1] op_sel_hi:[1,1]\n"
      "v_pk_mul_f32 v[204:205], v[204:205], v[26:27]\nv_pk_mul_f32 v[208:209], v[208:209], v[30:31]\nv_pk_fma_f32 v[204:205], v[206:207], v[24:25], v[204:205]\n"
      "v_pk_fma_f32 v[208:209], v[210:211], v[28:29], v[208:209]\nv_pk_mul_f32 v[24:25], v[24:25], v[26:27]\nv_pk_mul_f32 v[28:29], v[28:29], v[30:31]\nv_pk_mul_f32 v[26:27], v[24:25], v[28:29]\n"
      "v_pk_mul_f32 v[16:17], v[212:213], v[212:213]\nv_pk_mul_f32 v[204:205], v[204:205], v[28:29]\nv_pk_mul_f32 v[18:19], v[214:215], v[214:215]\nv_rcp_f32 v26, v26\n"
      "v_pk_mul_f32 v[20:21], v[216:217], v[216:217]\nv_rcp_f32 v27, v27\nv_pk_mul_f32 v[22:23], v[218:219], v[218:219]\nv_pk_fma_f32 v[204:205], v[208:209], v[24:25], v[204:205]\n"
      "v_pk_fma_f32 v[16:17], v[16:17], s[58:59], s[58:59] op_sel:[0,0,1] op_sel_hi:[1,0,1]\nv_pk_mul_f32 v[32:33], v[204:205], v[26:27]\n"
      "v_pk_fma_f32 v[18:19], v[18:19], s[58:59], s[58:59] op_sel:[0,0,1] op_sel_hi:[1,0,1]\nv_pk_fma_f32 v[20:21], v[20:21], s[58:59], s[58:59] op_sel:[0,0,1] op_sel_hi:[1,0,1]\n"
      "v_pk_fma_f32 v[22:23], v[22:23], s[58:59], s[58:59] op_sel:[0,0,1] op_sel_hi:[1,0,1]\nv_pk_fma_f32 v[16:17], v[16:17], v[212:213], s[60:61] op_sel_hi:[1,1,0]\n"
      "ds_read_b128 v[204:207], v252 offset:33792\nv_pk_fma_f32 v[18:19], v[18:19], v[214:215], s[60:61] op_sel_hi:[1,1,0]\nds_read_b128 v[208:211], v252 offset:34816\n"
      "v_pk_fma_f32 v[20:21], v[20:21], v[216:217], s[60:61] op_sel_hi:[1,1,0]\nv_pk_fma_f32 v[22:23], v[22:23], v[218:219], s[60:61] op_sel_hi:[1,1,0]\n"
      "v_pk_mul_f32 v[212:213], v[212:213], v[8:9]\nv_pk_mul_f32 v[214:215], v[214:215], v[10:11]\nv_pk_mul_f32 v[216:217], v[216:217], v[12:13]\nv_pk_mul_f32 v[218:219], v[218:219], v[14:15]\n"
      "v_exp_f32 v16, v16 clamp\nv_exp_f32 v17, v17 clamp\nv_exp_f32 v18, v18 clamp\nv_exp_f32 v19, v19 clamp\nv_exp_f32 v20, v20 clamp\nv_exp_f32 v21, v21 clamp\nv_exp_f32 v22, v22 clamp\n"
      "v_exp_f32 v23, v23 clamp\nv_pk_add_f32 v[16:17], v[16:17], s[60:61] op_sel:[0,1] op_sel_hi:[1,1]\nv_pk_add_f32 v[18:19], v[18:19], s[60:61] op_sel:[0,1] op_sel_hi:[1,1]\n"
      "v_pk_add_f32 v[20:21], v[20:21], s[60:61] op_sel:[0,1] op_sel_hi:[1,1]\nv_pk_add_f32 v[22:23], v[22:23], s[60:61] op_sel:[0,1] op_sel_hi:[1,1]\n"
      "v_pk_mul_f32 v[212:213], v[212:213], v[18:19]\nv_pk_mul_f32 v[216:217], v[216:217], v[22:23]\nv_pk_fma_f32 v[212:213], v[214:215], v[16:17], v[212:213]\n"
      "v_pk_fma_f32 v[216:217], v[218:219], v[20:21], v[216:217]\nv_pk_mul_f32 v[16:17], v[16:17], v[18:19]\nv_pk_mul_f32 v[20:21], v[20:21], v[22:23]\nv_pk_mul_f32 v[18:19], v[16:17], v[20:21]\n"
      "v_pk_mul_f32 v[24:25], v[220:221], v[220:221]\nv_pk_mul_f32 v[212:213], v[212:213], v[20:21]\nv_pk_mul_f32 v[26:27], v[222:223], v[222:223]\nv_rcp_f32 v18, v18\n"
      "v_pk_mul_f32 v[28:29], v[224:225], v[224:225]\nv_rcp_f32 v19, v19\nv_pk_mul_f32 v[30:31], v[226:227], v[226:227]\nv_pk_fma_f32 v[212:213], v[216:217], v[16:17], v[212:213]\n"
      "v_pk_fma_f32 v[24:25], v[24:25], s[58:59], s[58:59] op_sel:[0,0,1] op_sel_hi:[1,0,1]\nv_pk_fma_f32 v[32:33], v[212:213], v[18:19], v[32:33]\n"
      "v_pk_fma_f32 v[26:27], v[26:27], s[58:59], s[58:59] op_sel:[0,0,1] op_sel_hi:[1,0,1]\nv_add_f32 v40, v32, v33\n"
      "v_pk_fma_f32 v[28:29], v[28:29], s[58:59], s[58:59] op_sel:[0,0,1] op_sel_hi:[1,0,1]\nv_pk_fma_f32 v[30:31], v[30:31], s[58:59], s[58:59] op_sel:[0,0,1] op_sel_hi:[1,0,1]\n"
      "v_pk_fma_f32 v[24:25], v[24:25], v[220:221], s[60:61] op_sel_hi:[1,1,0]\nds_read_b128 v[212:215], v252 offset:35840\nv_pk_fma_f32 v[26:27], v[26:27], v[222:223], s[60:61] op_sel_hi:[1,1,0]\n"
      "ds_read_b128 v[216:219], v252 offset:36864\nv_pk_fma_f32 v[28:29], v[28:29], v[224:225], s[60:61] op_sel_hi:[1,1,0]\nv_pk_fma_f32 v[30:31], v[30:31], v[226:227], s[60:61] op_sel_hi:[1,1,0]\n"
      "v_pk_mul_f32 v[220:221], v[220:221], v[0:1]\nv_pk_mul_f32 v[222:223], v[222:223], v[2:3]\nv_pk_mul_f32 v[224:225], v[224:225], v[4:5]\nv_pk_mul_f32 v[226:227], v[226:227], v[6:7]\n"
      "v_exp_f32 v24, v24 clamp\nv_exp_f32 v25, v25 clamp\nv_exp_f32 v26, v26 clamp\nv_exp_f32 v27, v27 clamp\nv_exp_f32 v28, v28 clamp\nv_exp_f32 v29, v29 clamp\nv_exp_f32 v30, v30 clamp\n"
      "v_exp_f32 v31, v31 clamp\nv_pk_add_f32 v[24:25], v[24:25], s[60:61] op_sel:[0,1] op_sel_hi:[1,1]\nv_pk_add_f32 v[26:27], v[26:27], s[60:61] op_sel:[0,1] op_sel_hi:[1,1]\n"
      "v_pk_add_f32 v[28:29], v[28:29], s[60:61] op_sel:[0,1] op_sel_hi:[1,1]\nv_pk_add_f32 v[30:31], v[30:31], s[60:61] op_sel:[0,1] op_sel_hi:[1,1]\n"
      "v_pk_mul_f32 v[220:221], v[220:221], v[26:27]\nv_pk_mul_f32 v[224:225], v[224:225], v[30:31]\nv_pk_fma_f32 v[220:221], v[222:223], v[24:25], v[220:221]\n"
      "v_pk_fma_f32 v[224:225], v[226:227], v[28:29], v[224:225]\nv_pk_mul_f32 v[24:25], v[24:25], v[26:27]\nv_pk_mul_f32 v[28:29], v[28:29], v[30:31]\nv_pk_mul_f32 v[26:27], v[24:25], v[28:29]\n"
      "v_pk_mul_f32 v[16:17], v[228:229], v[228:229]\nv_pk_mul_f32 v[220:221], v[220:221], v[28:29]\nv_pk_mul_f32 v[18:19], v[230:231], v[230:231]\nv_rcp_f32 v26, v26\n"
      "v_pk_mul_f32 v[20:21], v[232:233], v[232:233]\nv_rcp_f32 v27, v27\nv_pk_mul_f32 v[22:23], v[234:235], v[234:235]\nv_pk_fma_f32 v[220:221], v[224:225], v[24:25], v[220:221]\n"
      "v_pk_fma_f32 v[16:17], v[16:17], s[58:59], s[58:59] op_sel:[0,0,1] op_sel_hi:[1,0,1]\nv_pk_mul_f32 v[32:33], v[220:221], v[26:27]\n"
      "v_pk_fma_f32 v[18:19], v[18:19], s[58:59], s[58:59] op_sel:[0,0,1] op_sel_hi:[1,0,1]\nv_pk_fma_f32 v[20:21], v[20:21], s[58:59], s[58:59] op_sel:[0,0,1] op_sel_hi:[1,0,1]\n"
      "v_pk_fma_f32 v[22:23], v[22:23], s[58:59], s[58:59] op_sel:[0,0,1] op_sel_hi:[1,0,1]\nv_pk_fma_f32 v[16:17], v[16:17], v[228:229], s[60:61] op_sel_hi:[1,1,0]\n"
      "ds_read_b128 v[220:223], v252 offset:37888\nv_pk_fma_f32 v[18:19], v[18:19], v[230:231], s[60:61] op_sel_hi:[1,1,0]\nds_read_b128 v[224:227], v252 offset:38912\n"
      "v_pk_fma_f32 v[20:21], v[20:21], v[232:233], s[60:61] op_sel_hi:[1,1,0]\nv_pk_fma_f32 v[22:23], v[22:23], v[234:235], s[60:61] op_sel_hi:[1,1,0]\n"
      "v_pk_mul_f32 v[228:229], v[228:229], v[8:9]\nv_pk_mul_f32 v[230:231], v[230:231], v[10:11]\nv_pk_mul_f32 v[232:233], v[232:233], v[12:13]\nv_pk_mul_f32 v[234:235], v[234:235], v[14:15]\n"
      "v_exp_f32 v16, v16 clamp\nv_exp_f32 v17, v17 clamp\nv_exp_f32 v18, v18 clamp\nv_exp_f32 v19, v19 clamp\nv_exp_f32 v20, v20 clamp\nv_exp_f32 v21, v21 clamp\nv_exp_f32 v22, v22 clamp\n"
      "v_exp_f32 v23, v23 clamp\nv_pk_add_f32 v[16:17], v[16:17], s[60:61] op_sel:[0,1] op_sel_hi:[1,1]\nv_pk_add_f32 v[18:19], v[18:19], s[60:61] op_sel:[0,1] op_sel_hi:[1,1]\n"
      "v_pk_add_f32 v[20:21], v[20:21], s[60:61] op_sel:[0,1] op_sel_hi:[1,1]\nv_pk_add_f32 v[22:23], v[22:23], s[60:61] op_sel:[0,1] op_sel_hi:[1,1]\n"
      "v_pk_mul_f32 v[228:229], v[228:229], v[18:19]\nv_pk_mul_f32 v[232:233], v[232:233], v[22:23]\nv_pk_fma_f32 v[228:229], v[230:231], v[16:17], v[228:229]\n"
      "v_pk_fma_f32 v[232:233], v[234:235], v[20:21], v[232:233]\nv_pk_mul_f32 v[16:17], v[16:17], v[18:19]\nv_pk_mul_f32 v[20:21], v[20:21], v[22:23]\nv_pk_mul_f32 v[18:19], v[16:17], v[20:21]\n"
      "v_pk_mul_f32 v[228:229], v[228:229], v[20:21]\nv_rcp_f32 v18, v18\nv_rcp_f32 v19, v19\nv_pk_fma_f32 v[228:229], v[232:233], v[16:17], v[228:229]\n"
      "v_pk_fma_f32 v[32:33], v[228:229], v[18:19], v[32:33]\nv_add_f32 v41, v32, v33\ns_barrier\nds_read_b128 v[228:231], v252 offset:39936\nds_read_b128 v[232:235], v252 offset:40960\n"
      ".Lge:\nv_mov_b32 v43, v42\nds_read_b128 v[48:51], v64\nds_read_b128 v[52:55], v64 offset:16\nds_read_b64_tr_b16 v[68:69], v238\nds_read_b64_tr_b16 v[70:71], v239\n"
      "ds_read_b64_tr_b16 v[56:57], v236 offset:0\nds_read_b64_tr_b16 v[58:59], v237 offset:128\nds_read_b64_tr_b16 v[60:61], v237 offset:256\nds_read_b64_tr_b16 v[62:63], v237 offset:384\n"
      "ds_read_b64_tr_b16 v[64:65], v237 offset:512\nds_read_b64_tr_b16 v[66:67], v237 offset:640\nds_read_b128 v[44:47], v247 offset:48\nv_permlane32_swap_b32 v36, v38\n"
      "v_permlane32_swap_b32 v37, v39\nv_permlane32_swap_b32 v40, v42\nv_permlane32_swap_b32 v41, v43\nv_add_f32 v36, v36, v38\nv_add_f32 v37, v37, v39\nv_add_f32 v40, v40, v42\n"
      "v_add_f32 v41, v41, v43\ns_nop 0\nv_permlane16_swap_b32 v36, v37\nv_permlane16_swap_b32 v40, v41\nv_add_f32 v36, v36, v37\nv_add_f32 v40, v40, v41\nds_write_b32 v254, v36\n"
      "ds_write_b32 v255, v40 offset:256\nds_add_rtn_u32 v2, v253, v241 offset:20\ns_waitcnt lgkmcnt(4)\n"
      ".Lcin:\nv_readlane_b32 s48, v69, 48\ns_mov_b32 s56, 0\ns_mov_b32 s47, 1\nv_pk_mul_f16 v20, v68, v70\nv_pk_mul_f16 v21, v69, v71\nv_pk_mul_f16 v22, v56, v56 op_sel_hi:[0,1]\n"
      "v_pk_mul_f16 v23, v56, v57 op_sel_hi:[0,1]\nv_pk_mul_f16 v16, v68, s48 op_sel:[0,1]\nv_pk_mul_f16 v17, v69, s48 op_sel:[0,1]\nv_pk_mul_f16 v18, v70, s48 op_sel:[0,1]\n"
      "v_pk_mul_f16 v19, v71, s48 op_sel:[0,1]\ns_cmp_ge_u32 s24, 4\ns_cbranch_scc1 .Lc_top\n.p2align 6\ns_nop 0\ns_barrier\n"
      ".Lc_top:\nv_mfma_f32_16x16x32_f16 v[36:39], v[72:75], v[16:19], 0\nds_add_rtn_u32 v4, v245, v241\nv_pk_mul_f16 v24, v66, v66 op_sel_hi:[0,1]\nv_pk_mul_f16 v25, v66, v57 op_sel_hi:[0,1]\n"
      "v_mfma_f32_16x16x32_f16 v[36:39], v[84:87], v[20:23], v[36:39]\nv_pk_mul_f16 v26, v56, v66 op_sel_hi:[0,1]\nv_add_f32_dpp v55, v55, v55 quad_perm:[1,0,3,2] row_mask:0xf bank_mask:0xf\n"
      "v_mfma_f32_16x16x32_f16 v[40:43], v[88:91], v[20:23], 0\nv_pk_mul_f16 v27, v56, v67 op_sel_hi:[0,1]\nv_add_f32_dpp v55, v55, v55 quad_perm:[2,3,0,1] row_mask:0xf bank_mask:0xf\ns_setprio 0\n"
      "v_mfma_f32_16x16x32_f16 v[40:43], v[76:79], v[16:19], v[40:43]\nv_pk_mul_f16 v28, v58, v66 op_sel_hi:[0,1]\nv_add_f32_dpp v55, v55, v55 row_half_mirror row_mask:0xf bank_mask:0xf\n"
      "v_mfma_f32_16x16x32_f16 v[32:35], v[80:83], v[20:23], 0\nv_pk_mul_f16 v29, v58, v67 op_sel_hi:[0,1]\nv_add_f32_dpp v55, v55, v55 row_mirror row_mask:0xf bank_mask:0xf\n"
      "ds_write_b32 v248, v55\ns_waitcnt lgkmcnt(1)\nv_smfmac_f32_16x16x64_f16 v[44:47], v[92:95], v[16:23], v244\nv_pk_mul_f16 v30, v60, v66 op_sel_hi:[0,1]\n"
      "v_pk_mul_f16 v31, v60, v67 op_sel_hi:[0,1]\ns_lshl_b32 s50, s45, 6\nv_mfma_f32_16x16x32_f16 v[32:35], v[96:99], v[24:27], v[32:35]\nv_pk_mul_f16 v16, v62, v66 op_sel_hi:[0,1]\n"
      "v_pk_mul_f16 v17, v62, v67 op_sel_hi:[0,1]\nv_mfma_f32_16x16x32_f16 v[32:35], v[108:111], v[28:31], v[32:35]\nv_pk_mul_f16 v18, v64, v66 op_sel_hi:[0,1]\nv_readfirstlane_b32 s44, v4\n"
      "v_mfma_f32_16x16x32_f16 v[36:39], v[112:115], v[28:31], v[36:39]\nv_pk_mul_f16 v19, v64, v67 op_sel_hi:[0,1]\nv_add_u32 v248, s50, v246\ns_and_b32 s45, s44, 31\ns_min_u32 s49, s45, 18\n"
      "s_mulk_i32 s49, 928\ns_add_u32 s49, s49, s65\nv_mfma_f32_16x16x32_f16 v[36:39], v[100:103], v[24:27], v[36:39]\nv_pk_mul_f16 v20, v64, v64 op_sel_hi:[0,1]\n"
      "v_add_u32_sdwa v238, s49, v243 dst_sel:DWORD dst_unused:UNUSED_PAD src0_sel:DWORD src1_sel:WORD_0\nv_mfma_f32_16x16x32_f16 v[40:43], v[104:107], v[24:27], v[40:43]\n"
      "v_pk_mul_f16 v21, v64, v57 op_sel_hi:[0,1]\nv_add_u32_sdwa v239, s49, v243 dst_sel:DWORD dst_unused:UNUSED_PAD src0_sel:DWORD src1_sel:WORD_1\n"
      "v_mfma_f32_16x16x32_f16 v[40:43], v[116:119], v[28:31], v[40:43]\nv_pk_mul_f16 v22, v56, v64 op_sel_hi:[0,1]\n"
      "v_add_u32_sdwa v236, s49, v242 dst_sel:DWORD dst_unused:UNUSED_PAD src0_sel:DWORD src1_sel:WORD_0\nv_smfmac_f32_16x16x64_f16 v[44:47], v[120:123], v[24:31], v244\n"
      "v_pk_mul_f16 v23, v56, v65 op_sel_hi:[0,1]\nv_add_u32_sdwa v237, s49, v242 dst_sel:DWORD dst_unused:UNUSED_PAD src0_sel:DWORD src1_sel:WORD_1\n"
      "v_mfma_f32_16x16x32_f16 v[32:35], v[124:127], v[16:19], v[32:35]\nv_pk_mul_f16 v24, v58, v64 op_sel_hi:[0,1]\nv_pk_mul_f16 v25, v58, v65 op_sel_hi:[0,1]\nds_read_b64_tr_b16 v[70:71], v239\n"
      "v_mfma_f32_16x16x32_f16 v[32:35], v[136:139], v[20:23], v[32:35]\nv_pk_mul_f16 v26, v60, v64 op_sel_hi:[0,1]\ns_setprio 1\nds_read_b64_tr_b16 v[68:69], v238\n"
      "v_mfma_f32_16x16x32_f16 v[36:39], v[140:143], v[20:23], v[36:39]\nv_pk_mul_f16 v27, v60, v65 op_sel_hi:[0,1]\nds_read_b64_tr_b16 v[66:67], v237 offset:640\n"
      "v_mfma_f32_16x16x32_f16 v[36:39], v[128:131], v[16:19], v[36:39]\nv_pk_mul_f16 v28, v62, v64 op_sel_hi:[0,1]\nv_mfma_f32_16x16x32_f16 v[40:43], v[132:135], v[16:19], v[40:43]\n"
      "v_pk_mul_f16 v29, v62, v65 op_sel_hi:[0,1]\ns_cmp_ge_u32 s45, s46\ns_cbranch_scc1 .Lc_task\n"
      ".Lc_back:\nv_mfma_f32_16x16x32_f16 v[40:43], v[144:147], v[20:23], v[40:43]\nv_pk_mul_f16 v30, v62, v62 op_sel_hi:[0,1]\nv_smfmac_f32_16x16x64_f16 v[44:47], v[148:151], v[16:23], v244\n"
      "v_pk_mul_f16 v31, v62, v57 op_sel_hi:[0,1]\nv_mfma_f32_16x16x32_f16 v[32:35], v[152:155], v[24:27], v[32:35]\nv_pk_mul_f16 v16, v56, v62 op_sel_hi:[0,1]\n"
      "v_pk_mul_f16 v17, v56, v63 op_sel_hi:[0,1]\nds_read_b64_tr_b16 v[64:65], v237 offset:512\nv_mfma_f32_16x16x32_f16 v[32:35], v[164:167], v[28:31], v[32:35]\n"
      "v_pk_mul_f16 v18, v58, v62 op_sel_hi:[0,1]\ns_lshr_b32 s53, s44, 8\nv_mfma_f32_16x16x32_f16 v[36:39], v[168:171], v[28:31], v[36:39]\nv_pk_mul_f16 v19, v58, v63 op_sel_hi:[0,1]\n"
      "s_ff0_i32_b32 s53, s53\nv_mfma_f32_16x16x32_f16 v[36:39], v[156:159], v[24:27], v[36:39]\nv_pk_mul_f16 v20, v60, v62 op_sel_hi:[0,1]\ns_waitcnt lgkmcnt(1)\nv_readlane_b32 s48, v69, 48\n"
      "v_mfma_f32_16x16x32_f16 v[40:43], v[160:163], v[24:27], v[40:43]\nv_pk_mul_f16 v21, v60, v63 op_sel_hi:[0,1]\ns_add_u32 s53, s53, s64\n"
      "v_mfma_f32_16x16x32_f16 v[40:43], v[172:175], v[28:31], v[40:43]\nv_pk_mul_f16 v22, v60, v60 op_sel_hi:[0,1]\nv_smfmac_f32_16x16x64_f16 v[44:47], v[176:179], v[24:31], v244\n"
      "v_pk_mul_f16 v23, v60, v57 op_sel_hi:[0,1]\nv_mfma_f32_16x16x32_f16 v[32:35], v[180:183], v[16:19], v[32:35]\nv_pk_mul_f16 v24, v56, v60 op_sel_hi:[0,1]\n"
      "v_pk_mul_f16 v25, v56, v61 op_sel_hi:[0,1]\ns_setprio 2\nds_read_b64_tr_b16 v[62:63], v237 offset:384\nv_mfma_f32_16x16x32_f16 v[32:35], v[192:195], v[20:23], v[32:35]\n"
      "v_pk_mul_f16 v26, v58, v60 op_sel_hi:[0,1]\nv_pk_mul_f16 v27, v58, v61 op_sel_hi:[0,1]\nv_mfma_f32_16x16x32_f16 v[36:39], v[196:199], v[20:23], v[36:39]\n"
      "v_pk_mul_f16 v28, v58, v58 op_sel_hi:[0,1]\nv_mfma_f32_16x16x32_f16 v[36:39], v[184:187], v[16:19], v[36:39]\nv_pk_mul_f16 v29, v58, v57 op_sel_hi:[0,1]\n"
      "ds_read_b64_tr_b16 v[60:61], v237 offset:256\nv_mfma_f32_16x16x32_f16 v[40:43], v[188:191], v[16:19], v[40:43]\nv_pk_mul_f16 v30, v56, v58 op_sel_hi:[0,1]\n"
      "v_mfma_f32_16x16x32_f16 v[40:43], v[200:203], v[20:23], v[40:43]\nv_pk_mul_f16 v31, v56, v59 op_sel_hi:[0,1]\nv_smfmac_f32_16x16x64_f16 v[44:47], v[204:207], v[16:23], v244\n"
      "ds_read_b64_tr_b16 v[56:57], v236 offset:0\nds_read_b64_tr_b16 v[58:59], v237 offset:128\nv_smfmac_f32_16x16x64_f16 v[44:47], v[232:235], v[24:31], v244\n"
      "v_pk_mul_f16 v16, v68, s48 op_sel:[0,1]\nv_pk_mul_f16 v17, v69, s48 op_sel:[0,1]\nv_mfma_f32_16x16x32_f16 v[40:43], v[216:219], v[24:27], v[40:43]\nv_pk_mul_f16 v18, v70, s48 op_sel:[0,1]\n"
      "v_pk_mul_f16 v19, v71, s48 op_sel:[0,1]\nv_mfma_f32_16x16x32_f16 v[40:43], v[228:231], v[28:31], v[40:43]\nv_pk_mul_f16 v20, v68, v70\nv_pk_mul_f16 v21, v69, v71\n"
      "v_mfma_f32_16x16x32_f16 v[32:35], v[220:223], v[28:31], v[32:35]\nv_add_f32 v44, v44, v45\nv_add_f32 v46, v46, v47\nv_mfma_f32_16x16x32_f16 v[32:35], v[208:211], v[24:27], v[32:35]\n"
      "v_mul_f32 v55, v44, v54\nv_mul_f32 v55, v55, v46\nds_read_b128 v[44:47], v247 offset:48\nv_mfma_f32_16x16x32_f16 v[36:39], v[212:215], v[24:27], v[36:39]\nv_mul_f32 v0, v40, v52\n"
      "v_fmac_f32 v55, v0, v42\nv_mfma_f32_16x16x32_f16 v[36:39], v[224:227], v[28:31], v[36:39]\nv_mul_f32 v0, v41, v53\nv_fmac_f32 v55, v0, v43\nv_mul_f32 v32, v32, v48\nv_mul_f32 v33, v33, v49\n"
      "v_mul_f32 v34, v34, v50\nv_mul_f32 v35, v35, v51\ns_waitcnt lgkmcnt(1)\nv_pk_mul_f16 v22, v56, v56 op_sel_hi:[0,1]\nv_pk_mul_f16 v23, v56, v57 op_sel_hi:[0,1]\ns_cmp_lt_u32 s45, s53\n"
      "v_fmac_f32 v55, v32, v36\nv_fmac_f32 v55, v33, v37\nv_fmac_f32 v55, v34, v38\nv_fmac_f32 v55, v35, v39\ns_cbranch_scc1 .Lc_top\n"
      ".Lc_slow:\ns_sub_u32 s49, s45, s64\ns_lshr_b32 s50, s44, 16\ns_or_b32 s50, s50, s56\ns_ashr_i32 s50, s50, s49\nv_add_f32_dpp v55, v55, v55 quad_perm:[1,0,3,2] row_mask:0xf bank_mask:0xf\n"
      "s_cmp_lg_u32 s50, -1\ns_cbranch_scc1 .Lc_more\n"
      ".Lc_fin:\nv_add_f32_dpp v55, v55, v55 quad_perm:[2,3,0,1] row_mask:0xf bank_mask:0xf\ns_nop 1\nv_add_f32_dpp v55, v55, v55 row_half_mirror row_mask:0xf bank_mask:0xf\ns_nop 1\n"
      "v_add_f32_dpp v55, v55, v55 row_mirror row_mask:0xf bank_mask:0xf\nds_write_b32 v248, v55\ns_cmp_gt_u32 s24, 1\ns_waitcnt lgkmcnt(0)\ns_barrier\ns_cbranch_scc1 .Lc_end\n"
      "ds_read_b128 v[4:7], v10\nds_read_b32 v3, v11\nds_read_b32 v2, v247\nds_read_b64 v[8:9], v13\ns_mov_b32 s42, 0x11111111\ns_cmp_eq_u32 s24, 0\ns_cselect_b32 s43, s42, 0\n"
      "s_cselect_b32 s42, s42, 0x111\ns_waitcnt lgkmcnt(2)\nv_add_f32 v4, v4, v5\nv_add_f32 v6, v6, v7\nv_add_f32 v4, v4, v6\nv_ldexp_f32 v4, v4, v3\ns_waitcnt lgkmcnt(1)\n"
      "v_and_b32 v2, 0x3f00, v2\nv_add_f32_dpp v4, v4, v4 quad_perm:[1,0,3,2] row_mask:0xf bank_mask:0xf\nv_lshrrev_b32 v2, v14, v2\nv_or_b32 v2, v2, v15\n"
      "v_add_f32_dpp v4, v4, v4 quad_perm:[2,3,0,1] row_mask:0xf bank_mask:0xf\nv_and_b32 v2, 1, v2\nv_cmp_eq_u32 vcc, 1, v2\ns_waitcnt lgkmcnt(0)\nv_add_f32 v4, v4, v9\nv_and_b32 v8, 0x1ffe, v8\n"
      "v_cvt_pk_bf16_f32 v4, v4, v4\ns_and_b64 exec, vcc, s[42:43]\ns_cbranch_execz .Lc_end\nglobal_store_short v8, v4, s[22:23]\ns_branch .Lc_end\n"
      ".Lc_more:\ns_mov_b32 s51, 0\ns_add_u32 s4, s4, .Lc_chk-.Lc_back\ns_addc_u32 s5, s5, 0\n"
      ".Lc_chk:\ns_sub_u32 s49, s45, s64\ns_lshr_b32 s50, s44, s49\ns_bitcmp1_b32 s50, 8\ns_cbranch_scc1 .Lc_have\ns_sub_u32 s53, s68, s64\ns_lshl_b32 s53, -1, s53\ns_lshr_b32 s50, s44, 16\n"
      "s_or_b32 s50, s50, s53\ns_ashr_i32 s50, s50, s49\ns_add_u32 s51, s51, 1\ns_mov_b32 s47, 0\ns_bitcmp1_b32 s51, 22\ns_cbranch_scc1 .Lc_fin\ns_cmp_eq_u32 s50, -1\ns_cbranch_scc0 .Lc_rd\n"
      "s_cmp_lg_u32 s56, 0\ns_cbranch_scc1 .Lc_fin\n"
      ".Lc_task:\ns_setpc_b64 s[34:35]\n"
      ".Lc_rd:\ns_and_b32 s50, s50, 1\nv_mov_b32 v0, s50\nds_add_rtn_u32 v4, v245, v0\ns_waitcnt lgkmcnt(0)\nv_readfirstlane_b32 s44, v4\ns_and_b32 s49, s44, 31\ns_cmp_eq_u32 s50, 0\n"
      "s_cselect_b32 s45, s45, s49\ns_cmp_lg_u32 s56, 0\ns_cbranch_scc1 .Lc_chk\ns_setpc_b64 s[34:35]\n"
      ".Lc_have:\ns_cmp_eq_u32 s47, 1\ns_cbranch_scc1 .Lc_go\ns_min_u32 s49, s45, 18\ns_mulk_i32 s49, 928\ns_add_u32 s49, s49, s65\n"
      "v_add_u32_sdwa v238, s49, v243 dst_sel:DWORD dst_unused:UNUSED_PAD src0_sel:DWORD src1_sel:WORD_0\n"
      "v_add_u32_sdwa v239, s49, v243 dst_sel:DWORD dst_unused:UNUSED_PAD src0_sel:DWORD src1_sel:WORD_1\n"
      "v_add_u32_sdwa v236, s49, v242 dst_sel:DWORD dst_unused:UNUSED_PAD src0_sel:DWORD src1_sel:WORD_0\n"
      "v_add_u32_sdwa v237, s49, v242 dst_sel:DWORD dst_unused:UNUSED_PAD src0_sel:DWORD src1_sel:WORD_1\nds_read_b64_tr_b16 v[68:69], v238\nds_read_b64_tr_b16 v[70:71], v239\n"
      "ds_read_b64_tr_b16 v[56:57], v236 offset:0\nds_read_b64_tr_b16 v[58:59], v237 offset:128\nds_read_b64_tr_b16 v[60:61], v237 offset:256\nds_read_b64_tr_b16 v[62:63], v237 offset:384\n"
      "ds_read_b64_tr_b16 v[64:65], v237 offset:512\nds_read_b64_tr_b16 v[66:67], v237 offset:640\nds_read_b128 v[44:47], v247 offset:48\ns_mov_b32 s47, 1\ns_waitcnt lgkmcnt(0)\n"
      "v_readlane_b32 s48, v69, 48\nv_pk_mul_f16 v20, v68, v70\nv_pk_mul_f16 v21, v69, v71\nv_pk_mul_f16 v22, v56, v56 op_sel_hi:[0,1]\nv_pk_mul_f16 v23, v56, v57 op_sel_hi:[0,1]\n"
      "v_pk_mul_f16 v16, v68, s48 op_sel:[0,1]\nv_pk_mul_f16 v17, v69, s48 op_sel:[0,1]\nv_pk_mul_f16 v18, v70, s48 op_sel:[0,1]\nv_pk_mul_f16 v19, v71, s48 op_sel:[0,1]\n"
      ".Lc_go:\nv_mul_f32 v55, 0.5, v55\ns_sub_u32 s4, s4, .Lc_chk-.Lc_back\ns_subb_u32 s5, s5, 0\ns_branch .Lc_top\n"
      ".Lt_g:\nv_readfirstlane_b32 s42, v2\ns_mov_b32 s43, 88064\nv_lshl_add_u32 v1, v240, 2, s43\ns_cmp_lg_u32 s42, 3\ns_cbranch_scc1 .Lt_p\nds_read2st64_b32 v[6:7], v1 offset0:0 offset1:4\n"
      "ds_read2st64_b32 v[8:9], v1 offset0:1 offset1:5\nds_read2st64_b32 v[10:11], v1 offset0:2 offset1:6\nds_read2st64_b32 v[12:13], v1 offset0:3 offset1:7\ns_lshl_b32 s42, s28, 8\n"
      "s_add_u32 s42, s42, s29\ns_lshl_b32 s42, s42, 7\ns_add_u32 s58, s12, s42\ns_addc_u32 s59, s13, 0\nv_lshlrev_b32 v14, 3, v240\ns_cmp_eq_u32 s25, 2\ns_cselect_b64 vcc, -1, 0\n"
      "s_movk_i32 s42, 0xff00\nv_mov_b32 v1, s41\ns_lshl_b32 s43, s25, 3\ns_waitcnt lgkmcnt(0)\nv_add_f32 v12, v12, v13\nv_add_f32 v6, v6, v7\nv_add_f32 v8, v8, v9\nv_add_f32 v10, v10, v11\n"
      "v_mov_b32 v13, v12\nv_mov_b32 v15, v12\ns_nop 1\nv_permlane16_swap_b32 v13, v15\nv_add_f32 v13, v13, v15\nv_cndmask_b32 v12, v12, v13, vcc\nv_mul_f32 v6, 0x1a000000, v6\n"
      "v_mul_f32 v8, 0x1a000000, v8\nv_mul_f32 v10, 0x1a000000, v10\nv_mul_f32 v12, 0x1a000000, v12\nv_add_u32 v6, 0x80, v6\nv_and_or_b32 v6, v6, s42, v1\nv_mov_b32 v7, s40\n"
      "v_add_u32 v8, 0x80, v8\nv_and_or_b32 v8, v8, s42, v1\nv_mov_b32 v9, s40\nv_add_u32 v10, 0x80, v10\nv_and_or_b32 v10, v10, s42, v1\nv_mov_b32 v11, s40\nv_add_u32 v12, 0x80, v12\n"
      "v_and_or_b32 v12, v12, s42, v1\nv_mov_b32 v13, s40\nglobal_store_dwordx2 v14, v[6:7], s[58:59] offset:0 sc1\nglobal_store_dwordx2 v14, v[8:9], s[58:59] offset:512 sc1\n"
      "global_store_dwordx2 v14, v[10:11], s[58:59] offset:1024 sc1\ns_cmp_eq_u32 s43, 0\ns_cbranch_scc1 .Lt_p\ns_bfm_b64 exec, s43, 0\n"
      "global_store_dwordx2 v14, v[12:13], s[58:59] offset:1536 sc1\ns_mov_b64 exec, -1\n"
      ".Lt_p:\ns_cmp_gt_u32 s66, 100\ns_cbranch_scc1 .Lt_d\ns_mov_b32 s46, s66\ns_swappc_b64 s[34:35], s[4:5]\ns_waitcnt vmcnt(0)\nv_lshl_add_u32 v6, v240, 2, v247\ns_lshl_b32 s43, 0x10000, s67\n"
      "s_cmp_gt_u32 s44, 0xffff\ns_cbranch_scc1 .Lt_a\nv_lshlrev_b32 v1, 3, v240\ns_mov_b64 exec, 1\nglobal_atomic_add v5, v1, v241, s[8:9] sc0 sc1\ns_mov_b64 exec, -1\n"
      ".Lt_f:\nglobal_load_dwordx2 v[2:3], v1, s[14:15] sc1\ns_swappc_b64 s[34:35], s[4:5]\ns_waitcnt vmcnt(0)\nv_readfirstlane_b32 s50, v5\nv_cmp_eq_u32 vcc, s41, v2\n"
      "v_cmp_eq_u32 s[62:63], s40, v3\ns_cmp_ge_u32 s50, s55\ns_cbranch_scc1 .Lt_a\ns_add_u32 s50, s50, 1\nv_readlane_b32 s54, v251, s50\ns_and_b64 s[62:63], s[62:63], vcc\n"
      "s_bfe_u32 s50, s54, 0x50004\ns_lshr_b32 s49, s54, 9\ns_lshl3_add_u32 s50, s50, s49\ns_sub_u32 s50, s50, s69\ns_bitcmp0_b64 s[62:63], s50\ns_cbranch_scc1 .Lt_f\ns_mul_i32 s50, s54, 3584\n"
      "s_add_u32 s62, s16, s50\ns_addc_u32 s63, s17, 0\ns_add_u32 s50, s64, s67\ns_mul_i32 s49, s50, 928\ns_add_u32 m0, s49, 90112\ns_mov_b32 exec_hi, 0x7fffff\n"
      "global_load_lds_dwordx4 v252, s[62:63] offset:0 sc1\ns_add_u32 m0, m0, 16736\ns_lshl_b32 s54, s54, 3\nglobal_load_lds_dwordx4 v252, s[62:63] offset:896 sc1\ns_add_u32 m0, m0, 16736\n"
      "s_lshl3_add_u32 s50, s50, 160640\nglobal_load_lds_dwordx4 v252, s[62:63] offset:1792 sc1\ns_add_u32 m0, m0, 16736\ns_lshl_b32 s43, 0x100, s67\n"
      "global_load_lds_dwordx4 v252, s[62:63] offset:2688 sc1\ns_mov_b32 exec_hi, -1\ns_mov_b32 exec_lo, 0\nv_and_b32 v249, 0xffff8000, v249\nv_or_b32 v249, s54, v249\nv_mov_b32 v250, s50\n"
      "s_mov_b32 exec_lo, -1\ns_swappc_b64 s[34:35], s[4:5]\ns_waitcnt vmcnt(0)\n"
      ".Lt_a:\nv_mov_b32 v7, s43\ns_mov_b64 exec, 15\nds_add_u32 v6, v7\ns_mov_b64 exec, -1\n"
      ".Lt_d:\ns_sub_u32 s46, s64, 4\ns_cmp_ge_u32 s45, s46\ns_cbranch_scc1 .Lt_l\ns_swappc_b64 s[34:35], s[4:5]\n"
      ".Lt_l:\nglobal_load_dwordx2 v[254:255], v249, s[12:13] sc1\ns_swappc_b64 s[34:35], s[4:5]\ns_waitcnt vmcnt(0)\nv_and_b32 v6, 0xff, v254\nv_cmp_ne_u32 vcc, s40, v255\n"
      "v_cmp_ne_u32 s[62:63], s41, v6\nv_and_b32 v9, 0xffffff00, v254\ns_or_b64 vcc, vcc, s[62:63]\ns_cbranch_vccnz .Lt_l\ns_mov_b32 vcc_lo, 0x1fff1fff\ns_mov_b32 vcc_hi, vcc_lo\n"
      "v_bfe_u32 v8, v249, 2, 13\nv_cndmask_b32 v9, 0, v9, vcc\ns_sub_u32 s56, s68, s64\ns_lshl_b32 s56, -1, s56\nv_add_f32_dpp v9, v9, v9 quad_perm:[1,0,3,2] row_mask:0xf bank_mask:0xf\n"
      "s_movk_i32 s46, 99\ns_nop 0\nv_add_f32_dpp v9, v9, v9 quad_perm:[2,3,0,1] row_mask:0xf bank_mask:0xf\ns_nop 1\nv_add_f32_dpp v9, v9, v9 row_half_mirror row_mask:0xf bank_mask:0xf\ns_nop 1\n"
      "v_add_f32_dpp v9, v9, v9 row_mirror row_mask:0xf bank_mask:0xf\nv_add_f32 v9, s38, v9\nds_write_b64 v250, v[8:9]\ns_setpc_b64 s[4:5]\n"
      ".Lc_end:\n"
      ".Lxd_end:"
      :: "{v[236:239]}"(boff), "{v240}"(lane), "{v[248:251]}"(mrow), "{v252}"(msc), "{v[254:255]}"(rsp), "{s[4:19]}"(ka0), "{s[20:23]}"(ka1), "{s[24:31]}"(gs), [blk] "s"(blockIdx.x), [pl]
        "s"(g_tab.pool[group(blockIdx.x)]), [kp] "i"(KP[0] | KP[1] << 5 | KP[2] << 10 | KP[3] << 15 | KP[4] << 20)
      : "memory", "vcc", "scc", "s33", "s34", "s35", "s36", "s37", "s38", "s39", "s40", "s41", "s42", "s43", "s44", "s45", "s46", "s47", "s48", "s49", "s50", "s51", "s52", "s53", "s54", "s55", "s56", "s57", "s58", "s59", "s60", "s61", "s62", "s63", "s64", "s65", "s66", "s67", "s68", "s69",
        "v0", "v1", "v2", "v3", "v4", "v5", "v6", "v7", "v8", "v9", "v10", "v11", "v12", "v13", "v14", "v15", "v16", "v17", "v18", "v19", "v20", "v21", "v22", "v23", "v24", "v25", "v26", "v27",
        "v28", "v29", "v30", "v31", "v32", "v33", "v34", "v35", "v36", "v37", "v38", "v39", "v40", "v41", "v42", "v43", "v44", "v45", "v46", "v47", "v48", "v49", "v50", "v51", "v52", "v53", "v54",
        "v55", "v56", "v57", "v58", "v59", "v60", "v61", "v62", "v63", "v64", "v65", "v66", "v67", "v68", "v69", "v70", "v71", "v72", "v73", "v74", "v75", "v76", "v77", "v78", "v79", "v80", "v81",
        "v82", "v83", "v84", "v85", "v86", "v87", "v88", "v89", "v90", "v91", "v92", "v93", "v94", "v95", "v96", "v97", "v98", "v99", "v100", "v101", "v102", "v103", "v104", "v105", "v106", "v107",
        "v108", "v109", "v110", "v111", "v112", "v113", "v114", "v115", "v116", "v117", "v118", "v119", "v120", "v121", "v122", "v123", "v124", "v125", "v126", "v127", "v128", "v129", "v130",
        "v131", "v132", "v133", "v134", "v135", "v136", "v137", "v138", "v139", "v140", "v141", "v142", "v143", "v144", "v145", "v146", "v147", "v148", "v149", "v150", "v151", "v152", "v153",
        "v154", "v155", "v156", "v157", "v158", "v159", "v160", "v161", "v162", "v163", "v164", "v165", "v166", "v167", "v168", "v169", "v170", "v171", "v172", "v173", "v174", "v175", "v176",
        "v177", "v178", "v179", "v180", "v181", "v182", "v183", "v184", "v185", "v186", "v187", "v188", "v189", "v190", "v191", "v192", "v193", "v194", "v195", "v196", "v197", "v198", "v199",
        "v200", "v201", "v202", "v203", "v204", "v205", "v206", "v207", "v208", "v209", "v210", "v211", "v212", "v213", "v214", "v215", "v216", "v217", "v218", "v219", "v220", "v221", "v222",
        "v223", "v224", "v225", "v226", "v227", "v228", "v229", "v230", "v231", "v232", "v233", "v234", "v235", "v241", "v242", "v243", "v244", "v245", "v246", "v247", "v253");
}

extern "C" void kernel_launch(void* const* d_in, const int* in_sizes, int n_in, void* d_out, int out_size,
                              void* d_ws, size_t ws_size, hipStream_t stream) {
  const int* idx = (const int*)d_in[0];
  const u16* table = (const u16*)d_in[1];
  const u16* ws_cin = (const u16*)d_in[2];
  const u16* wout = (const u16*)d_in[3];
  const u16* w1 = (const u16*)d_in[4];
  const u16* b1 = (const u16*)d_in[5];
  const u16* w2 = (const u16*)d_in[6];
  const u16* b_out = (const u16*)d_in[7];
  dnn_kernel<<<256, 512, 0, stream>>>(idx, table, ws_cin, w1, b1, w2, wout, b_out, (char*)d_ws, (u16*)d_out);
}
```

```cpp
#include <hip/hip_runtime.h>
#include <hip/hip_bf16.h>
#include <stdint.h>
#include <utility>

typedef __attribute__((ext_vector_type(8))) short bf16x8;
typedef __attribute__((ext_vector_type(4))) short s16x4;
typedef __attribute__((ext_vector_type(4))) float f32x4;
typedef __attribute__((ext_vector_type(2))) float f32x2;
typedef __attribute__((ext_vector_type(2))) __bf16 bf16x2;
typedef __attribute__((ext_vector_type(16))) float f32x16;
typedef unsigned short u16;
typedef __attribute__((ext_vector_type(2))) unsigned u32x2;
typedef __attribute__((ext_vector_type(4))) unsigned u32x4;

constexpr int NB = 4096, NF = 26, ND = 64, DIN = 1664;

#define STAMP(k,i)
#define WSTAMP(i)
template <class F, int... I>
__device__ __forceinline__ void static_for(F& f, std::integer_sequence<int, I...>) { (f(std::integral_constant<int, I>{}), ...); }
template <int CTRL, int RM = 0xf>
__device__ __forceinline__ float dpp(float v) { return __builtin_bit_cast(float, __builtin_amdgcn_update_dpp(0, __builtin_bit_cast(int, v), CTRL, RM, 0xf, false)); }
__device__ __forceinline__ unsigned pk2(float a, float b) { return __builtin_bit_cast(unsigned, __builtin_convertvector((f32x2){a, b}, bf16x2)); }

template <int V> using IC = std::integral_constant<int, V>;
typedef __attribute__((ext_vector_type(8))) unsigned u32x8;
typedef __attribute__((ext_vector_type(16))) unsigned u32x16;
constexpr int STAGE = 224 * 128 + 64 * 256, G_LDS = 163840;
constexpr int MC = 41 * 1024;
constexpr int W3S = 0, U_OFF = W3S + 35840, NC_OFF = U_OFF + 2048, GT_OFF = NC_OFF + 1024, WO_OFF = GT_OFF + 2048, DP = 4352, W2S = 2 * STAGE;
static_assert(WO_OFF + 256 <= STAGE, "lds");
constexpr int WS_RESP = 4096, WS_POOL = 32768, WS_M = 1 << 20;
#define NCLV 8
constexpr int NSLOT = 21, NCL = NCLV;

struct Geo { unsigned nt, s0, ns; };
constexpr __host__ __device__ Geo geo(unsigned e) {
  const unsigned i = (e * 79) >> 10, nt = e - 13 * i, c = nt < 9 ? 13108 : 13798, t = i * c + ((nt * 247) & 1023);
  return Geo{nt, t >> 10, ((t + c) >> 10) - (t >> 10)};
}
constexpr unsigned long long builders() {
  unsigned long long k = 0;
  for (unsigned x = 0; x < 8; ++x)
    for (unsigned s = 32; s-- > 0;)
      if (geo(32 * x + s).ns == 12) k = (k & ~(31ull << (5 * x))) | (unsigned long long)s << (5 * x);
  return k;
}
constexpr unsigned long long BK = builders();
#define KEEP 10, 13, 15, 11, 8
constexpr int KP[5] = {KEEP};
constexpr int WANT[5] = {138, 163, 177, 135, 113};
constexpr int kind(unsigned b) { return ((BK >> (5 * (b & 7))) & 31) == b >> 3 ? 3 + ((b & 7) >> 2) : 14 - geo((b & 7) * 32 + (b >> 3)).ns; }
constexpr __host__ __device__ unsigned group(unsigned b) { return ((b >> 3) + 2 * b) & 15; }
constexpr bool geo_ok() {
  for (unsigned nt = 0; nt < 13; ++nt) {
    unsigned s = 0;
    for (unsigned e = nt; e < 256; e += 13) {
      const Geo g = geo(e);
      if (g.nt != nt || g.s0 != s || g.ns < 12 || g.ns > 14 || e < g.s0 || e >= g.s0 + g.ns) return false;
      s += g.ns;
    }
    if (s != 256) return false;
  }
  return true;
}
static_assert(geo_ok(), "tiles");

struct PI { int a, b; };
constexpr PI prod(int n) {
  int m = n - 2;
  for (int jj = 0; jj < 6; ++jj) {
    const int j = jj ? 6 - jj : 0;
    if (m < 2 + 2 * j) return m == 0 ? PI{2 * j, 2 * j} : m == 1 ? PI{2 * j, 1} : PI{(m - 2) & ~1, 2 * j + (m & 1)};
    m -= 2 + 2 * j;
  }
  return PI{0, 0};
}
constexpr int xfield(int k, int h, int g) { return k == 1 ? 24 + h : 4 * (k >> 1) + ((g + 2 * (k & 1) + h) & 3); }
constexpr int pqfield(int q, int sl, int g) { return g < 3 ? 8 * g + 4 * (sl >> 1) + (sl & 1) + (((g ^ q) & 1) << 1) : sl == 3 ? 26 : 24 + ((sl + q) >> 1); }
constexpr unsigned short slot_code(int f, int t, int g, int h) {
  if (f == 0) {
    const int sl = 2 * (t & 1) + h, q = t >> 1;
    return g == 3 && (q || (sl & 1)) ? 0x8000 : 0x4000 | 6 << 10 | (26 * 33 + pqfield(q, sl, g));
  }
  const int n = 4 * (f - 1) + t;
  const int p = n < 2 ? pqfield(0, 2 * n + h, g) : xfield(prod(n).a, 0, g), q = n < 2 ? pqfield(1, 2 * n + h, g) : xfield(prod(n).b, h, g);
  return (p == 26 ? 0x4000 | 10 << 10 : p == q ? 1 << 10 : 2 << 10) | (p * 33 + q);
}
struct GT { unsigned short t[2][64][8], pool[16][64]; };
constexpr GT make_gt() {
  GT r{};
  for (int l = 0; l < 64; ++l)
    for (int e = 0; e < 8; ++e) {
      r.t[0][l][e] = l < 48 ? slot_code(l >> 2, e >> 1, l & 3, e & 1) : 0x8000;
      r.t[1][l][e] = l < 48 ? slot_code(2 * (l >> 3) + ((l >> 1) & 1), 2 * ((e >> 1) & 1) + ((l >> 2) & 1), 2 * (l & 1) + (e >> 2), e & 1) : 0x8000;
    }
  int tot = 0, dem = 0, acc = 0, k = 15, b = 0;
  for (int i = 0; i < 256; ++i) { tot += 16 - KP[kind(i)]; dem += WANT[kind(i)] - 10 * KP[kind(i)]; }
  for (int q = 0, n = 0; q < 16; ++q) {
    for (int i = 0; i < 256; ++i) acc += group(i) == q ? WANT[kind(i)] - 10 * KP[kind(i)] : 0;
    const int len = (acc * tot + dem / 2) / dem - n;
    n += len;
    r.pool[q][0] = len;
    for (int i = 1; i <= len; ++i) {
      while (k < KP[kind(b)]) { ++b; k = 15; }
      r.pool[q][i] = ((b & 7) * 32 + (b >> 3)) * 16 + k--;
    }
  }
  return r;
}
constexpr bool pool_ok() {
  const GT t = make_gt();
  for (int q = 0; q < 16; ++q) {
    int n = 0;
    for (int i = 0; i < 256; ++i) n += group(i) != q ? 0 : NSLOT - KP[kind(i)] < NCL ? NSLOT - KP[kind(i)] : NCL;
    if (t.pool[q][0] > 62 || t.pool[q][0] > n) return false;
    const int fb = 16 * q < 24 ? 0 : 16 * q - 24 > 192 ? 192 : 16 * q - 24;
    for (int i = 1; i <= t.pool[q][0]; ++i) {
      const int b = ((t.pool[q][i] >> 4) & 31) * 8 + (t.pool[q][i] >> 9);
      if (b < fb || b >= fb + 64) return false;
    }
  }
  for (int k = 0; k < 5; ++k) if (KP[k] < 8 || KP[k] > 16) return false;
  return true;
}
static_assert(pool_ok(), "pools");
static __device__ const __attribute__((aligned(16))) GT g_tab = make_gt();

__global__ __launch_bounds__(512, 2) void dnn_kernel(const int* __restrict__ idx, const u16* __restrict__ table,
                                                     const u16* __restrict__ ws_cin, const u16* __restrict__ w1,
                                                     const u16* __restrict__ b1, const u16* __restrict__ w2,
                                                     const u16* __restrict__ wout, const u16* __restrict__ b_out,
                                                     char* __restrict__ ws, u16* __restrict__ out) {
  __shared__ __attribute__((aligned(1024))) char shm[G_LDS];
  u32x4 ka1;
  u32x8 gs;
  u32x16 ka0;
  unsigned tch, blk = blockIdx.x;
  int lane;
  asm volatile(
      "s_load_dwordx16 s[4:19], %[ka], 0\ns_load_dwordx4 s[20:23], %[ka], 64\nv_readfirstlane_b32 s24, %[tid]\ns_lshr_b32 s37, s2, 3\ns_cmp_lt_u32 s24, 448\ns_cbranch_scc1 .Lf3\n"
      "s_getpc_b64 s[42:43]\n"
      ".Lxd_pc0:\nv_and_b32 v245, 15, %[tid]\nv_lshl_add_u32 v245, s37, 4, v245\nv_lshlrev_b32 v245, 7, v245\nv_min_u32 v245, .Lxd_end-.Lxd_pc0-4, v245\nglobal_load_dword v253, v245, s[42:43]\n"
      ".Lf3:\ns_and_b32 s36, s2, 7\ns_lshl_b32 s30, s36, 5\ns_add_u32 s30, s30, s37\ns_lshr_b32 s24, s24, 6\ns_mul_i32 s38, s30, 79\ns_lshr_b32 s38, s38, 10\ns_mul_i32 s28, s38, 13\n"
      "s_sub_u32 s28, s30, s28\nv_and_b32 v240, 63, %[tid]\ns_lshr_b32 s27, s36, 1\ns_add_u32 s27, s27, s36\ns_and_b32 s39, s28, 3\ns_mul_i32 s39, s39, 3\ns_add_u32 s27, s27, s39\n"
      "s_sub_u32 s39, s27, 12\ns_min_u32 s27, s27, s39\ns_lshl_b32 s27, s27, 1\ns_cmp_ge_u32 s24, 4\ns_cbranch_scc1 .Lf1\ns_movk_i32 s39, 13798\nv_mul_u32_u24 v245, 9363, v240\n"
      "s_cmp_lt_u32 s28, 9\ns_cselect_b32 s39, 13108, s39\ns_or_b32 s41, s27, 2\ns_mul_i32 s40, s28, 247\nv_lshrrev_b32 v245, 16, v245\ns_and_b32 s40, s40, 1023\n"
      "v_mad_i32_i24 v246, v245, -7, v240\ns_mul_i32 s38, s38, s39\nv_lshl_add_u32 v246, v246, 2, s41\ns_add_u32 s38, s38, s40\nv_subrev_u32 v247, 26, v246\ns_lshr_b32 s29, s38, 10\n"
      "v_mul_u32_u24 v245, 26, v245\ns_add_u32 s38, s38, s39\nv_min_u32 v246, v246, v247\ns_lshr_b32 s38, s38, 10\nv_add_lshl_u32 v241, v246, v245, 2\ns_sub_u32 s38, s38, s29\n"
      "v_add_u32 v244, 192, v241\ns_lshl_b32 s38, s38, 1\nv_add_u32 v243, 128, v241\ns_sub_u32 s25, s38, 24\nv_add_u32 v242, 64, v241\ns_mul_i32 s38, s24, 6272\ns_add_u32 m0, s38, 139648\n"
      "s_lshl1_add_u32 s38, s29, s24\ns_mul_i32 s38, s38, 832\ns_mul_i32 s40, s24, 3328\ns_add_u32 s38, s38, 16640\ns_sub_u32 s40, 16896, s40\ns_mov_b32 exec_hi, 0xffffff\n"
      "v_add_u32 v245, 256, v241\nv_add_u32 v246, 2432, v241\ns_waitcnt lgkmcnt(0)\ns_add_u32 s38, s4, s38\ns_addc_u32 s39, s5, 0\ns_cmp_ge_u32 s24, s25\nglobal_load_lds_dwordx4 v241, s[38:39]\n"
      "s_cbranch_scc1 .Li0\nglobal_load_lds_dwordx4 v246, s[38:39] offset:896\n"
      ".Li0:\ns_sub_u32 m0, m0, 4480\ns_sub_u32 s38, s38, s40\ns_subb_u32 s39, s39, 0\nglobal_load_lds_dwordx4 v245, s[38:39] offset:0\nglobal_load_lds_dwordx4 v244, s[38:39] offset:896\n"
      "global_load_lds_dwordx4 v243, s[38:39] offset:1792\nglobal_load_lds_dwordx4 v242, s[38:39] offset:2688\nglobal_load_lds_dwordx4 v241, s[38:39] offset:3584\ns_mov_b32 exec_hi, -1\n"
      "s_add_u32 s27, s27, 2\ns_mul_i32 s36, s36, 5\ns_mov_b32 s38, %[bkl]\ns_mov_b32 s39, %[bkh]\ns_lshr_b64 s[38:39], s[38:39], s36\ns_and_b32 s38, s38, 31\ns_xor_b32 s31, s38, s37\n"
      "s_cbranch_scc0 .Lbld\n"
      ".Lbp:\ns_mov_b32 s36, s24\ns_mul_i32 s42, s36, 6272\ns_cmp_ge_u32 s36, s25\ns_cselect_b64 vcc, 0, -1\ns_and_b32 s44, s27, 2\ns_xor_b32 s44, s44, 2\ns_lshl_b32 s44, s44, 2\n"
      "s_add_u32 s42, s42, s44\ns_add_u32 s42, s42, 135168\nv_lshrrev_b32 v22, 3, v240\nv_mul_u32_u24 v6, 112, v22\nv_add_u32 v6, s42, v6\nv_lshrrev_b32 v22, 4, v240\ns_lshl_b32 s42, s36, 2\n"
      "v_xor_b32 v22, v22, v240\nv_xor_b32 v22, s42, v22\nv_and_b32 v22, 7, v22\nv_lshlrev_b32 v4, 4, v22\nv_xor_b32 v5, 64, v4\ns_sub_u32 s44, s6, 1024\ns_subb_u32 s45, s7, 0\n"
      "s_sub_u32 s46, s6, 2048\ns_subb_u32 s47, s7, 0\ns_sub_u32 s48, s6, 3072\ns_subb_u32 s49, s7, 0\ns_mul_i32 s53, s36, 5120\ns_lshl_b32 s26, s36, 10\ns_lshl_b32 s54, s36, 12\n"
      "s_add_u32 s54, s54, 28672\nv_add_u32 v7, 1792, v6\nv_add_u32 v8, 3584, v6\nv_add_u32 v9, 5376, v6\ns_waitcnt vmcnt(0)\nds_read2_b64 v[196:199], v8 offset0:112 offset1:113\n"
      "ds_read2_b64 v[200:203], v9 offset0:0 offset1:1\nds_read2_b64 v[176:179], v6 offset0:0 offset1:1\nds_read2_b64 v[180:183], v6 offset0:112 offset1:113\n"
      "ds_read2_b64 v[184:187], v7 offset0:0 offset1:1\nds_read2_b64 v[188:191], v7 offset0:112 offset1:113\nds_read2_b64 v[192:195], v8 offset0:0 offset1:1\ns_waitcnt lgkmcnt(0)\n"
      "v_lshl_add_u32 v22, v176, 7, v4\nv_lshl_add_u32 v23, v180, 7, v5\nv_lshl_add_u32 v24, v184, 7, v4\nv_lshl_add_u32 v25, v188, 7, v5\nv_lshl_add_u32 v26, v192, 7, v4\n"
      "v_lshl_add_u32 v27, v196, 7, v4\nv_lshl_add_u32 v28, v200, 7, v4\ns_add_u32 m0, s26, 65536\ns_nop 0\nglobal_load_lds_dwordx4 v27, s[6:7] offset:0\nv_lshl_add_u32 v27, v197, 7, v4\n"
      "ds_read2_b64 v[196:199], v8 offset0:113 offset1:114\ns_cbranch_vccz .La1001\ns_add_u32 m0, s26, 69632\ns_nop 0\nglobal_load_lds_dwordx4 v28, s[6:7] offset:0\n"
      ".La1001:\nv_lshl_add_u32 v28, v201, 7, v4\ns_add_u32 m0, s53, 45056\ns_nop 0\nglobal_load_lds_dwordx4 v22, s[6:7] offset:0\nv_lshl_add_u32 v22, v177, 7, v4\n"
      "global_load_lds_dwordx4 v23, s[44:45] offset:1024\nv_lshl_add_u32 v23, v181, 7, v5\nds_read2_b64 v[180:183], v6 offset0:113 offset1:114\nglobal_load_lds_dwordx4 v24, s[46:47] offset:2048\n"
      "v_lshl_add_u32 v24, v185, 7, v4\nglobal_load_lds_dwordx4 v25, s[48:49] offset:3072\nv_lshl_add_u32 v25, v189, 7, v5\nds_read2_b64 v[188:191], v7 offset0:113 offset1:114\n"
      "s_add_u32 m0, s53, 49152\ns_nop 0\nglobal_load_lds_dwordx4 v26, s[6:7] offset:0\nv_lshl_add_u32 v26, v193, 7, v4\ns_waitcnt lgkmcnt(0)\ns_setprio 3\ns_barrier\ns_add_u32 m0, s26, 110592\n"
      "s_nop 0\nglobal_load_lds_dwordx4 v27, s[6:7] offset:0\nv_lshl_add_u32 v27, v196, 7, v4\ns_cbranch_vccz .La1002\ns_add_u32 m0, s26, 114688\ns_nop 0\n"
      "global_load_lds_dwordx4 v28, s[6:7] offset:0\n"
      ".La1002:\nv_lshl_add_u32 v28, v202, 7, v4\ns_add_u32 m0, s53, 90112\ns_nop 0\nglobal_load_lds_dwordx4 v22, s[6:7] offset:0\nv_lshl_add_u32 v22, v178, 7, v4\n"
      "global_load_lds_dwordx4 v23, s[44:45] offset:1024\nv_lshl_add_u32 v23, v180, 7, v5\nglobal_load_lds_dwordx4 v24, s[46:47] offset:2048\nv_lshl_add_u32 v24, v186, 7, v4\n"
      "global_load_lds_dwordx4 v25, s[48:49] offset:3072\nv_lshl_add_u32 v25, v188, 7, v5\ns_add_u32 m0, s53, 94208\ns_nop 0\nglobal_load_lds_dwordx4 v26, s[6:7] offset:0\n"
      "v_lshl_add_u32 v26, v194, 7, v4\nv_lshrrev_b32 v30, 2, v240\nv_bfe_u32 v31, v240, 1, 1\ns_lshl_b32 s42, s36, 1\nv_lshrrev_b32 v32, 3, v240\nv_or_b32 v31, s42, v31\ns_sub_u32 s33, s30, s29\n"
      "s_lshl_b32 s42, s33, 11\nv_xor_b32 v31, v31, v32\nv_lshl_add_u32 v30, v30, 7, s42\nv_and_b32 v32, 1, v240\nv_lshl_add_u32 v30, v31, 4, v30\nv_lshl_add_u32 v30, v32, 3, v30\n"
      "v_add_u32 v31, 90112, v30\nv_mov_b32 v32, 0\nv_mov_b32 v33, 0x3d800000\ns_mov_b32 s55, 0x80008000\ns_and_b32 s41, s24, 3\nv_lshrrev_b32 v245, 4, v240\nv_and_b32 v246, 15, v240\n"
      "s_mul_i32 s40, s41, 53248\nv_lshlrev_b32 v238, 1, v245\nv_mul_u32_u24 v245, 3328, v245\nv_xor_b32 v247, v246, v238\nv_add_u32 v245, s40, v245\nv_lshl_add_u32 v236, v247, 4, v245\n"
      "v_xor_b32 v238, 128, v236\nv_add_u32 v237, 12288, v236\nv_add_u32 v239, 36864, v238\nv_add_u32 v238, 24576, v238\ns_lshl_b32 s40, s28, 8\ns_add_u32 s10, s10, s40\ns_addc_u32 s11, s11, 0\n"
      "s_add_u32 s52, s27, 1\ns_mul_i32 s52, s52, 212992\ns_add_u32 s50, s10, s52\ns_addc_u32 s51, s11, 0\ns_add_u32 s50, s50, 212992\ns_addc_u32 s51, s51, 0\ns_cmp_eq_u32 s27, 24\n"
      "s_cselect_b64 s[50:51], s[10:11], s[50:51]\ns_lshl_b32 s42, s2, 2\ns_load_dword s37, s[20:21], s42\ns_load_dword s38, s[18:19], 0\ns_bfe_u32 s42, s2, 0x10002\ns_add_u32 s42, s42, 3\n"
      "s_lshr_b32 s43, s25, 1\ns_sub_u32 s43, 2, s43\ns_cmp_eq_u32 s31, 0\ns_cselect_b32 s42, s42, s43\ns_mul_i32 s42, s42, 5\ns_lshr_b32 s64, %[kp], s42\ns_and_b32 s64, s64, 31\ns_branch .Lfeed\n"
      ".Lf1:\ns_and_b32 s41, s24, 3\nv_lshrrev_b32 v245, 4, v240\nv_and_b32 v246, 15, v240\ns_mul_i32 s40, s41, 53248\nv_lshlrev_b32 v238, 1, v245\nv_mul_u32_u24 v245, 3328, v245\n"
      "v_xor_b32 v247, v246, v238\nv_add_u32 v245, s40, v245\nv_lshl_add_u32 v236, v247, 4, v245\nv_xor_b32 v238, 128, v236\nv_add_u32 v237, 12288, v236\nv_add_u32 v239, 36864, v238\n"
      "v_add_u32 v238, 24576, v238\ns_lshl_b32 s41, s41, 12\ns_mov_b32 m0, 160512\ns_lshl_b32 s40, s28, 8\ns_add_u32 s39, s27, 2\ns_mul_i32 s39, s39, 212992\ns_add_u32 s39, s39, s40\n"
      "v_lshl_add_u32 v244, v240, 4, s40\nv_lshl_add_u32 v243, v240, 2, s40\ns_mov_b64 exec, 0xffff\ns_cmp_lg_u32 s24, 5\ns_waitcnt lgkmcnt(0)\nglobal_load_lds_dwordx4 v244, s[12:13]\n"
      "s_mov_b64 exec, -1\ns_cbranch_scc1 .Lf4\nglobal_load_dword v253, v243, s[14:15]\n"
      ".Lf4:\ns_add_u32 m0, s41, 73728\ns_add_u32 s42, s10, s39\ns_addc_u32 s43, s11, 0\nglobal_load_lds_dwordx4 v236, s[42:43] offset:0\nglobal_load_lds_dwordx4 v237, s[42:43] offset:1024\n"
      "global_load_lds_dwordx4 v238, s[42:43] offset:2048\nglobal_load_lds_dwordx4 v239, s[42:43] offset:3072\ns_add_u32 m0, s41, 118784\ns_add_u32 s42, s42, 212992\ns_addc_u32 s43, s43, 0\n"
      "global_load_lds_dwordx4 v236, s[42:43] offset:0\nglobal_load_lds_dwordx4 v237, s[42:43] offset:1024\nglobal_load_lds_dwordx4 v238, s[42:43] offset:2048\n"
      "global_load_lds_dwordx4 v239, s[42:43] offset:3072\ns_movk_i32 s39, 13798\ns_cmp_lt_u32 s28, 9\ns_cselect_b32 s39, 13108, s39\ns_mul_i32 s40, s28, 247\ns_and_b32 s40, s40, 1023\n"
      "s_mul_i32 s38, s38, s39\ns_add_u32 s38, s38, s40\ns_lshr_b32 s29, s38, 10\ns_add_u32 s38, s38, s39\ns_lshr_b32 s38, s38, 10\ns_sub_u32 s38, s38, s29\ns_lshl_b32 s38, s38, 1\n"
      "s_sub_u32 s25, s38, 24\ns_add_u32 s27, s27, 2\ns_mul_i32 s36, s36, 5\ns_mov_b32 s38, %[bkl]\ns_mov_b32 s39, %[bkh]\ns_lshr_b64 s[38:39], s[38:39], s36\ns_and_b32 s38, s38, 31\n"
      "s_xor_b32 s31, s38, s37\ns_cbranch_scc0 .Lbld\n"
      ".Lbg:\ns_and_b32 s36, s24, 3\ns_lshl_b32 s42, s2, 2\ns_load_dword s37, s[20:21], s42\ns_load_dword s38, s[18:19], 0\ns_bfe_u32 s42, s2, 0x10002\ns_add_u32 s42, s42, 3\n"
      "s_lshr_b32 s43, s25, 1\ns_sub_u32 s43, 2, s43\ns_cmp_eq_u32 s31, 0\ns_cselect_b32 s42, s42, s43\ns_mul_i32 s42, s42, 5\ns_lshr_b32 s64, %[kp], s42\ns_and_b32 s64, s64, 31\ns_setprio 2\n"
      "s_and_b32 s56, s36, 1\ns_lshr_b32 s57, s36, 1\nv_lshrrev_b32 v1, 4, v240\nv_and_b32 v2, 15, v240\nv_lshlrev_b32 v3, 2, v2\nv_add_u32 v3, 162048, v3\nv_mov_b32 v4, 0\nds_write_b32 v3, v4\n"
      "s_mul_i32 s42, s56, 10240\nv_lshrrev_b32 v3, 1, v2\nv_xor_b32 v3, v3, v1\nv_lshl_add_u32 v4, v2, 7, s42\nv_lshl_add_u32 v96, v3, 4, v4\nv_xor_b32 v97, 64, v96\nv_add_u32 v98, 90112, v96\n"
      "v_add_u32 v99, 90112, v97\ns_cmp_eq_u32 s25, 4\ns_cselect_b32 s42, 4, 5\ns_mul_i32 s42, s42, s56\ns_sub_u32 s42, 12, s42\ns_lshl_b32 s42, s42, 11\ns_lshl_b32 s43, s56, 13\n"
      "s_sub_u32 s43, 20480, s43\nv_add_u32 v100, s42, v96\nv_add_u32 v120, s43, v96\nv_add_u32 v101, s42, v97\nv_add_u32 v121, s43, v97\nv_add_u32 v102, s42, v98\nv_add_u32 v122, s43, v98\n"
      "v_add_u32 v103, s42, v99\nv_add_u32 v123, s43, v99\nv_lshrrev_b32 v3, 2, v2\nv_lshl_add_u32 v4, v1, 3, v3\nv_and_b32 v5, 1, v1\nv_lshl_add_u32 v3, v5, 2, v3\ns_lshl_b32 s42, s36, 1\n"
      "v_xor_b32 v3, s42, v3\nv_and_b32 v5, 3, v2\nv_lshlrev_b32 v5, 3, v5\nv_lshl_add_u32 v4, v4, 8, v5\nv_lshl_add_u32 v4, v3, 5, v4\nv_add_u32 v104, 28672, v4\nv_xor_b32 v105, 32, v104\n"
      "v_xor_b32 v106, 64, v104\nv_xor_b32 v107, 96, v104\nv_add_u32 v108, 90112, v104\nv_add_u32 v109, 90112, v105\nv_add_u32 v110, 90112, v106\nv_add_u32 v111, 90112, v107\n"
      "s_lshl_b32 s42, s57, 10\ns_add_u32 s42, s42, 43008\ns_mul_i32 s43, s56, 320\ns_add_u32 s43, s43, s42\nv_lshl_add_u32 v254, v240, 2, s43\nv_cmp_gt_u32 vcc, 16, v240\ns_lshl_b32 s43, s56, 6\n"
      "s_add_u32 s42, s42, s43\ns_add_u32 s42, s42, 256\nv_min_u32 v5, 2, v1\nv_lshl_add_u32 v255, v2, 2, s42\nv_lshl_add_u32 v255, v5, 7, v255\nv_cndmask_b32 v255, v255, v254, vcc\n"
      "s_lshl_b32 s42, s36, 6\ns_add_u32 s42, s42, 160512\nv_lshl_add_u32 v3, v1, 3, s42\nv_xor_b32 v4, 64, v3\ns_lshl_b32 s42, s36, 7\ns_add_u32 s42, s42, 162816\nv_lshl_add_u32 v236, v1, 4, s42\n"
      "v_xor_b32 v237, 128, v236\ns_cmp_lg_u32 s24, 5\ns_cbranch_scc1 .Lj1063\ns_waitcnt vmcnt(8)\nv_lshlrev_b32 v16, 16, v253\nv_and_b32 v17, 0xffff0000, v253\ns_mov_b32 s42, 0xf000000\n"
      "v_max3_f32 v18, |v16|, |v17|, s42\ns_nop 1\nv_max_u32_dpp v18, v18, v18 quad_perm:[1,0,3,2] row_mask:0xf bank_mask:0xf\ns_nop 1\n"
      "v_max_u32_dpp v18, v18, v18 quad_perm:[2,3,0,1] row_mask:0xf bank_mask:0xf\ns_nop 1\nv_max_u32_dpp v18, v18, v18 row_half_mirror row_mask:0xf bank_mask:0xf\ns_nop 1\n"
      "v_max_u32_dpp v18, v18, v18 row_mirror row_mask:0xf bank_mask:0xf\nv_mov_b32 v19, v18\ns_mov_b32 s42, 162816\nv_lshl_add_u32 v20, v240, 3, s42\nv_permlane16_swap_b32 v18, v19\n"
      "v_max_u32 v18, v18, v19\nv_mov_b32 v19, v18\nv_mov_b32 v21, 162812\ns_nop 0\nv_permlane32_swap_b32 v18, v19\nv_max_u32 v18, v18, v19\nv_and_b32 v18, 0x7f800000, v18\n"
      "v_subrev_u32 v19, 0xc000000, v18\nds_write_b32 v21, v19\nv_lshrrev_b32 v18, 23, v18\nv_sub_u32 v18, 123, v18\nv_ldexp_f32 v16, v16, v18\nv_ldexp_f32 v17, v17, v18\n"
      "s_mov_b32 s42, 0x32000000\nv_max_f32 v18, |v16|, s42\nv_max_f32 v19, |v17|, s42\ns_brev_b32 s42, -2\nv_bfi_b32 v16, s42, v18, v16\nv_bfi_b32 v17, s42, v19, v17\nv_rcp_f32 v16, v16\n"
      "v_rcp_f32 v17, v17\ns_nop 0\nv_mul_f32 v18, 0x31800000, v16\nv_mul_f32 v19, 0x31800000, v17\nds_write2st64_b64 v20, v[16:17], v[18:19] offset1:1\n"
      ".Lj1063:\ns_barrier\ns_waitcnt vmcnt(8)\nds_read_b64 v[8:9], v3 offset:0\nds_read_b64 v[10:11], v3 offset:32\nds_read_b64 v[12:13], v4 offset:0\nds_read_b64 v[14:15], v4 offset:32\n"
      "s_waitcnt lgkmcnt(0)\nv_lshlrev_b32 v124, 16, v8\nv_and_b32 v125, 0xffff0000, v8\nv_lshlrev_b32 v126, 16, v9\nv_and_b32 v127, 0xffff0000, v9\nv_lshlrev_b32 v128, 16, v10\n"
      "v_and_b32 v129, 0xffff0000, v10\nv_lshlrev_b32 v130, 16, v11\nv_and_b32 v131, 0xffff0000, v11\nv_lshlrev_b32 v132, 16, v12\nv_and_b32 v133, 0xffff0000, v12\nv_lshlrev_b32 v134, 16, v13\n"
      "v_and_b32 v135, 0xffff0000, v13\nv_lshlrev_b32 v136, 16, v14\nv_and_b32 v137, 0xffff0000, v14\nv_lshlrev_b32 v138, 16, v15\nv_and_b32 v139, 0xffff0000, v15\n"
      "v_mov_b64 v[140:141], v[124:125]\nv_mov_b64 v[142:143], v[126:127]\nv_mov_b64 v[144:145], v[128:129]\nv_mov_b64 v[146:147], v[130:131]\nv_mov_b64 v[148:149], v[132:133]\n"
      "v_mov_b64 v[150:151], v[134:135]\nv_mov_b64 v[152:153], v[136:137]\nv_mov_b64 v[154:155], v[138:139]\nv_mov_b64 v[156:157], v[124:125]\nv_mov_b64 v[158:159], v[126:127]\n"
      "v_mov_b64 v[160:161], v[128:129]\nv_mov_b64 v[162:163], v[130:131]\nv_mov_b64 v[164:165], v[132:133]\nv_mov_b64 v[166:167], v[134:135]\nv_mov_b64 v[168:169], v[136:137]\n"
      "v_mov_b64 v[170:171], v[138:139]\nv_mov_b64 v[172:173], v[124:125]\nv_mov_b64 v[174:175], v[126:127]\nv_mov_b64 v[176:177], v[128:129]\nv_mov_b64 v[178:179], v[130:131]\n"
      "v_mov_b64 v[180:181], v[132:133]\nv_mov_b64 v[182:183], v[134:135]\nv_mov_b64 v[184:185], v[136:137]\nv_mov_b64 v[186:187], v[138:139]\nv_mov_b64 v[188:189], v[124:125]\n"
      "v_mov_b64 v[190:191], v[126:127]\nv_mov_b64 v[192:193], v[128:129]\nv_mov_b64 v[194:195], v[130:131]\nv_mov_b64 v[196:197], v[132:133]\nv_mov_b64 v[198:199], v[134:135]\n"
      "v_mov_b64 v[200:201], v[136:137]\nv_mov_b64 v[202:203], v[138:139]\nv_mov_b64 v[204:205], v[124:125]\nv_mov_b64 v[206:207], v[126:127]\nv_mov_b64 v[208:209], v[128:129]\n"
      "v_mov_b64 v[210:211], v[130:131]\nv_mov_b64 v[212:213], v[132:133]\nv_mov_b64 v[214:215], v[134:135]\nv_mov_b64 v[216:217], v[136:137]\nv_mov_b64 v[218:219], v[138:139]\n"
      "v_mov_b64 v[220:221], v[124:125]\nv_mov_b64 v[222:223], v[126:127]\nv_mov_b64 v[224:225], v[128:129]\nv_mov_b64 v[226:227], v[130:131]\nv_mov_b64 v[228:229], v[132:133]\n"
      "v_mov_b64 v[230:231], v[134:135]\nv_mov_b64 v[232:233], v[136:137]\nv_mov_b64 v[234:235], v[138:139]\ns_mov_b32 s58, 4\ns_cmp_eq_u32 s25, 2\ns_cbranch_scc1 .Lg13\ns_cmp_eq_u32 s25, 0\n"
      "s_cbranch_scc1 .Lg12\ns_branch .Lg14\n"
      ".Lbld:\n"
      : "=&{v240}"(lane), "=&{v253}"(tch), "=&{s[4:19]}"(ka0), "=&{s[20:23]}"(ka1), "=&{s[24:31]}"(gs), "+{s2}"(blk)
      : [ka] "s"(__builtin_amdgcn_kernarg_segment_ptr()), [tid] "v"(threadIdx.x), [bkl] "i"((unsigned)BK), [bkh] "i"((unsigned)(BK >> 32)), [kp] "i"(KP[0] | KP[1] << 5 | KP[2] << 10 | KP[3] << 15 | KP[4] << 20)
      : "memory", "scc", "s36", "s37", "s38", "s39", "s40", "s41", "s42", "s43", "v236", "v237", "v238", "v239", "v241", "v242", "v243", "v244", "v245", "v246", "v247");
#define SB __builtin_amdgcn_sched_barrier(0)
#define PIN(x) asm volatile("" : "+v"(x))
  const int w = gs[0], tid = w * 64 + lane;
  ws_cin = (const u16*)((unsigned long long)ka0[5] << 32 | ka0[4]);
  wout = (const u16*)((unsigned long long)ka0[13] << 32 | ka0[12]);
  ws = (char*)((unsigned long long)ka1[1] << 32 | ka1[0]);
  const unsigned sb = (unsigned)(size_t)(__attribute__((address_space(3))) char*)shm;
  const int g = lane >> 4, fr = lane & 15, hf = lane >> 5, l31 = lane & 31;
  constexpr unsigned TX = 0xA5C30F96u;
  auto chk = [](unsigned t) -> unsigned { return (t * 0x9E3779B1u) >> 24; };
  const int bid = blk & 7;
  const bool rblk = bid >= 4;
  const int fi = (bid & 3) * 8 + w, mi_ = min(fi, NF - 1);
  const unsigned tc = sb + (g & 1) * 32 + (fr & 3) * 8, h0 = 8 * (g >> 1) + (fr >> 2);
  u32x4 mrow;
  u32x2 rsp;
  unsigned msc;
  {
    unsigned ma_, ur[2], uw[6], fa[2][2];
    if (rblk) {
      const unsigned o3 = min(w * 4480, NF * NF * NF * 2 - 4480);
      asm volatile("s_mov_b32 m0, %2\n\ts_nop 0\n\tglobal_load_lds_dwordx4 %0, %1\n\tglobal_load_lds_dwordx4 %0, %1 offset:1024\n\tglobal_load_lds_dwordx4 %0, %1 offset:2048\n\t"
                   "global_load_lds_dwordx4 %0, %1 offset:3072\n\tglobal_load_lds_dwordx4 %0, %1 offset:3456"
                   :: "v"(lane * 16), "s"((const char*)(ws_cin + 2 * NF * NF * NF) + o3), "s"(sb + W3S + o3) : "memory");
      const unsigned rr = (bid & 3) * 8 + ((lane >> 2) & 7);
      const unsigned vo = (hf * NF * NF + min(rr, 25u) * NF) * 2 + (lane & 3) * 16;
#pragma unroll
      for (int m = 0; m < 2; ++m) {
        const unsigned p = min(w + 8 * m, 12);
        asm volatile("s_mov_b32 m0, %2\n\ts_nop 0\n\tglobal_load_lds_dwordx4 %0, %1" :: "v"(vo), "s"(ws_cin + (NF + 2 * p) * NF * NF), "s"(sb + W2S + p * 1024) : "memory");
      }
      asm volatile("ds_write_b32 %0, %1" :: "v"(sb + U_OFF + tid * 4), "v"(0) : "memory");
    } else {
#pragma unroll
      for (int s = 0; s < 2; ++s)
        asm volatile("s_mov_b32 m0, %2\n\ts_nop 0\n\tglobal_load_lds_dwordx4 %0, %1" :: "v"(min(l31, NF - 1) * NF * 2 + hf * 16), "s"(ws_cin + mi_ * NF * NF + s * 16), "s"(sb + W3S + w * DP + s * 1024) : "memory");
    }
    if (w < 3) asm volatile("s_mov_b32 m0, %2\n\ts_nop 0\n\tglobal_load_lds_dwordx4 %0, %1 sc1" :: "v"(lane * 16), "s"(w ? (const char*)&g_tab + (w - 1) * 1024 : ws), "s"(sb + NC_OFF + w * 1024) : "memory");
    if (w == 3) asm volatile("s_mov_b32 m0, %2\n\ts_nop 0\n\tglobal_load_lds_dword %0, %1" :: "v"(min(lane, 38) * 4), "s"(wout), "s"(sb + WO_OFF) : "memory");
    {
      const int ty = bid >> 2, f = lane >> 2;
      const bool sp = fi >= 24;
      int c0 = sp ? 5 : fi < 16 ? ty - 1 : 1, c1 = sp ? 16 : 3072, rho = sp ? 4 * (fi - 24) + 2 * ty : fi < 16 ? fi : 2 * (fi & 6) + (fi & 1) + 2 * ty;
      asm volatile("" : "+s"(c0), "+s"(c1), "+s"(rho));
      int t = (7 * (f >> 1) + c0) * 1024 + (f & 1) * c1 + ((lane & 3) * 16 + rho) * 16;
      PIN(t);
      ma_ = min((unsigned)t | (fi < 28 ? 0u : ~0u), 41u * 1024 + 1024);
      ma_ = lane < 48 ? ma_ : 43008;
    }
#pragma unroll
    for (int p = 0; p < 2; ++p) {
      ur[p] = sb + W3S + min(8 * g + 4 * p + (fr >> 2), NF - 1) * (NF * NF * 2) + w * 32 + (fr & 3) * 8;
      fa[0][p] = tc + W2S + ((h0 + 4 * p) * 8 + w) * 64; fa[1][p] = tc + W2S + (min(h0 + 16 + 4 * p, 25u) * 8 + w) * 64;
      PIN(ur[p]); PIN(fa[0][p]); PIN(fa[1][p]);
    }
#pragma unroll
    for (int m = 0; m < 6; ++m) {
      const unsigned e = (w + 8 * m) * 16 + fr;
      uw[m] = sb + U_OFF + e * 2 + ((e * 2521u) >> 16) * 12;
      PIN(uw[m]);
      if (m == 5) uw[m] = e < NF * NF ? uw[m] : 0x40000u;
    }
    PIN(uw[5]); PIN(ma_);
    asm volatile("s_waitcnt vmcnt(0) lgkmcnt(0)\n\ts_barrier" :: "{v253}"(tch) : "memory");
    const unsigned wov = *(const unsigned*)(shm + WO_OFF + lane * 4);
    const u32x4 tb = *(const u32x4*)(shm + GT_OFF + (fi >= 24) * 1024 + lane * 16);
    bf16x8 P[2], Q[2];
    if (rblk) {
      s16x4 wl[6][2];
      u32x4 wa;
      unsigned wo2;
      asm volatile("ds_read2_b64 %0, %1 offset0:13 offset1:14" : "=v"(wa) : "v"(sb + WO_OFF + g * 16));
      asm volatile("ds_read_u16 %0, %1 offset:52" : "=v"(wo2) : "v"(sb + WO_OFF + lane * 2));
#pragma unroll
      for (int m = 0; m < 6; ++m)
#pragma unroll
        for (int p = 0; p < 2; ++p) asm volatile("ds_read_b64_tr_b16 %0, %1 offset:%2" : "=v"(wl[m][p]) : "v"(ur[p]), "i"(m * 256));
      u32x2 fl[2][2];
#pragma unroll
      for (int s = 0; s < 2; ++s)
#pragma unroll
        for (int p = 0; p < 2; ++p) asm volatile("ds_read_b64_tr_b16 %0, %1" : "=v"(fl[s][p]) : "v"(fa[s][p]));
      asm volatile("s_waitcnt lgkmcnt(0)" : "+v"(wa), "+v"(wo2), "+v"(wl[0][0]), "+v"(wl[0][1]), "+v"(wl[1][0]), "+v"(wl[1][1]), "+v"(wl[2][0]), "+v"(wl[2][1]), "+v"(wl[3][0]), "+v"(wl[3][1]),
                   "+v"(wl[4][0]), "+v"(wl[4][1]), "+v"(wl[5][0]), "+v"(wl[5][1]), "+v"(fl[0][0]), "+v"(fl[0][1]), "+v"(fl[1][0]), "+v"(fl[1][1]));
      const unsigned km = g < 3 ? ~0u : 0u;
      const u32x4 wm = {wa[0], wa[1] & km, wa[2] & km, wa[3] & km};
      f32x4 du[6];
#pragma unroll
      for (int m = 0; m < 6; ++m)
        du[m] = __builtin_amdgcn_mfma_f32_16x16x32_bf16(__builtin_bit_cast(bf16x8, wm), __builtin_shufflevector(wl[m][0], wl[m][1], 0, 1, 2, 3, 4, 5, 6, 7), (f32x4){0.f, 0.f, 0.f, 0.f}, 0, 0, 0);
      asm volatile("ds_write_b16 %0, %1" :: "v"(lane < NF ? sb + U_OFF + lane * 64 + 52 : 0x40000u), "v"(wo2) : "memory");
#pragma unroll
      for (int m = 0; m < 6; ++m) {
        asm volatile("ds_write_b16 %0, %1" :: "v"(uw[m]), "v"(pk2(du[m][0], du[m][0])) : "memory");
        asm volatile("" :: "v"(du[m]));
      }
      asm volatile("s_waitcnt lgkmcnt(0)\n\ts_barrier" ::: "memory");
      s16x4 ul[2][2];
#pragma unroll
      for (int s = 0; s < 2; ++s)
#pragma unroll
        for (int p = 0; p < 2; ++p)
          asm volatile("ds_read_b64_tr_b16 %0, %1 offset:%2" : "=v"(ul[s][p]) : "v"(tc + U_OFF + h0 * 64), "i"(s * 1024 + p * 256));
      const unsigned fmask = l31 < NF ? ~0u : 0u;
#pragma unroll
      for (int s = 0; s < 2; ++s) {
        u32x4 q4 = {fl[s][0][0] & fmask, fl[s][0][1] & fmask, fl[s][1][0] & fmask, fl[s][1][1] & fmask};
        Q[s] = __builtin_bit_cast(bf16x8, q4);
      }
      asm volatile("s_waitcnt lgkmcnt(0)" : "+v"(ul[0][0]), "+v"(ul[0][1]), "+v"(ul[1][0]), "+v"(ul[1][1]));
#pragma unroll
      for (int s = 0; s < 2; ++s) P[s] = __builtin_shufflevector(ul[s][0], ul[s][1], 0, 1, 2, 3, 4, 5, 6, 7);
    } else {
#pragma unroll
      for (int s = 0; s < 2; ++s) {
        u32x4 xf = *(const u32x4*)(shm + W3S + w * DP + s * 1024 + lane * 16);
        if (s == 1) { const unsigned m = hf ? 0u : ~0u; xf[1] &= m; xf[2] &= m; xf[3] &= m; }
        Q[s] = __builtin_bit_cast(bf16x8, xf);
        const unsigned e = (l31 >> 1) - 8 * s - 4 * hf, v = 0x3f80u << ((lane & 1) * 16);
        u32x4 q4 = {e == 0 ? v : 0u, e == 1 ? v : 0u, e == 2 ? v : 0u, e == 3 ? v : 0u};
        P[s] = __builtin_bit_cast(bf16x8, q4);
      }
    }
    f32x16 D = {};
#pragma unroll
    for (int s = 0; s < 2; ++s) {
      D = __builtin_amdgcn_mfma_f32_32x32x16_bf16(P[s], Q[s], D, 0, 0, 0);
      D = __builtin_amdgcn_mfma_f32_32x32x16_bf16(Q[s], P[s], D, 0, 0, 0);
    }
    float* dl = (float*)(shm + W3S + w * DP);
    D[14] = lane == NF ? __uint_as_float(((unsigned)__builtin_amdgcn_readlane(wov, mi_ >> 1) >> ((mi_ & 1) * 16)) << 16) : D[14];
#pragma unroll
    for (int r = 0; r < 15; ++r) dl[((r & 3) + 8 * (r >> 2) + 4 * hf) * 33 + l31] = D[r];
    __builtin_amdgcn_wave_barrier();
    float v[8], mx = 0.f;
    const unsigned zm = fi >= NF ? 0xffff : rblk ? 0x8000 : 0xc000;
#pragma unroll
    for (int e = 0; e < 8; ++e) v[e] = dl[(tb[e >> 1] >> (16 * (e & 1))) & 0x3ff];
#pragma unroll
    for (int e = 0; e < 8; ++e) {
      const unsigned c = tb[e >> 1] >> (16 * (e & 1));
      float x = __builtin_amdgcn_ldexpf(v[e], ((c >> 10) & 15) - 2);
      PIN(x);
      v[e] = c & zm ? 0.f : x;
      mx = fmaxf(mx, fabsf(v[e]));
    }
    mx = fmaxf(mx, dpp<0xB1>(mx)); mx = fmaxf(mx, dpp<0x4E>(mx)); mx = fmaxf(mx, dpp<0x141>(mx)); mx = fmaxf(mx, dpp<0x140>(mx));
    const unsigned mxu = max(max((unsigned)__builtin_amdgcn_readlane(__float_as_int(mx), 0), (unsigned)__builtin_amdgcn_readlane(__float_as_int(mx), 16)), (unsigned)__builtin_amdgcn_readlane(__float_as_int(mx), 32));
    const unsigned ex = min(max(mxu >> 23, 40u), 250u);
    const float al = __uint_as_float((268 - ex) << 23);
#pragma unroll
    for (int e = 0; e < 4; ++e) {
      const unsigned q = pk2(v[2 * e] * al, v[2 * e + 1] * al);
      mrow[e] = __builtin_bit_cast(unsigned, __builtin_amdgcn_cvt_pkrtz(__uint_as_float(q << 16), __uint_as_float(q & 0xffff0000u)));
    }
    msc = fi < NF ? (ex - 14) << 23 : 0u;
    PIN(msc);
    asm volatile("global_store_dwordx4 %0, %1, %2 sc1" :: "v"(ma_), "{v[248:251]}"(mrow), "s"(ws + WS_M) : "memory");
    asm volatile("global_store_dword %0, %1, %2 sc1" :: "v"((fi < 16 ? (fi >> 2) * 64 + (fi & 3) * 4 : fi < 24 ? ((fi & 6) << 5) + (fi & 1) * 4 + 16 : (fi & 3) * 64 + (fi & 4) + 24) + (bid & 4) * 8), "{v252}"(msc), "s"(ws + WS_M + MC) : "memory");
    if (fi == 31) asm volatile("global_store_dword %0, %1, %2 sc1\n\ts_waitcnt vmcnt(0)" :: "v"((lane & 15) * 128), "{v252}"(msc), "s"(ws + WS_POOL) : "memory");
    const unsigned mval = *(const unsigned*)(shm + NC_OFF + (tid & 255) * 4), qval = *(const unsigned*)(shm + NC_OFF);
    rsp = (u32x2){qval << 8 | chk(mval), mval ^ TX};
    PIN(rsp);
    asm volatile("s_waitcnt lgkmcnt(0)\n\ts_barrier" ::: "memory");
  }
  static_assert(STAGE == 45056 && MC == 41984 && NSLOT == 21 && WS_RESP == 4096 && WS_POOL == 32768 && WS_M == 1048576 && NCL == 8 && sizeof(g_tab.t) == 2048, "constants of the code below");
  asm volatile(
      "s_cmp_lt_u32 s24, 4\ns_cbranch_scc1 .Lbp\ns_branch .Lbg\n"
      ".Lfeed:\ns_waitcnt vmcnt(7) lgkmcnt(0)\ns_cbranch_vccnz .Lw1004\ns_waitcnt vmcnt(6)\n"
      ".Lw1004:\ns_barrier\nds_read_b64 v[44:45], v30 offset:45056\ns_add_u32 m0, s54, 0\ns_nop 0\nglobal_load_lds_dwordx4 v236, s[50:51] offset:0\n"
      "global_load_lds_dwordx4 v237, s[50:51] offset:1024\nglobal_load_lds_dwordx4 v238, s[50:51] offset:2048\nglobal_load_lds_dwordx4 v239, s[50:51] offset:3072\ns_add_u32 s50, s50, 212992\n"
      "s_addc_u32 s51, s51, 0\ns_add_u32 m0, s26, 20480\ns_nop 0\nglobal_load_lds_dwordx4 v27, s[6:7] offset:0\nv_lshl_add_u32 v27, v197, 7, v4\ns_cbranch_vccz .La1003\ns_add_u32 m0, s26, 24576\n"
      "s_nop 0\nglobal_load_lds_dwordx4 v28, s[6:7] offset:0\n"
      ".La1003:\nv_lshl_add_u32 v28, v203, 7, v4\nds_read2_b64 v[200:203], v9 offset0:2 offset1:3\ns_add_u32 m0, s53, 0\ns_nop 0\nglobal_load_lds_dwordx4 v22, s[6:7] offset:0\n"
      "v_lshl_add_u32 v22, v179, 7, v4\nds_read2_b64 v[176:179], v6 offset0:2 offset1:3\nglobal_load_lds_dwordx4 v23, s[44:45] offset:1024\nv_lshl_add_u32 v23, v181, 7, v5\n"
      "global_load_lds_dwordx4 v24, s[46:47] offset:2048\nv_lshl_add_u32 v24, v187, 7, v4\nds_read2_b64 v[184:187], v7 offset0:2 offset1:3\nglobal_load_lds_dwordx4 v25, s[48:49] offset:3072\n"
      "v_lshl_add_u32 v25, v189, 7, v5\ns_add_u32 m0, s53, 4096\ns_nop 0\nglobal_load_lds_dwordx4 v26, s[6:7] offset:0\nv_lshl_add_u32 v26, v195, 7, v4\n"
      "ds_read2_b64 v[192:195], v8 offset0:2 offset1:3\ns_waitcnt vmcnt(11) lgkmcnt(0)\ns_cbranch_vccnz .Lw1006\ns_waitcnt vmcnt(10)\n"
      ".Lw1006:\ns_barrier\ns_add_u32 m0, s54, 45056\ns_nop 0\nglobal_load_lds_dwordx4 v236, s[50:51] offset:0\nds_read_b64 v[46:47], v31 offset:0\n"
      "global_load_lds_dwordx4 v237, s[50:51] offset:1024\nglobal_load_lds_dwordx4 v238, s[50:51] offset:2048\nglobal_load_lds_dwordx4 v239, s[50:51] offset:3072\ns_add_u32 s50, s50, 212992\n"
      "s_addc_u32 s51, s51, 0\ns_cmp_eq_u32 s27, 22\ns_cselect_b64 s[50:51], s[10:11], s[50:51]\ns_add_u32 m0, s26, 65536\nv_max3_f16 v32, |v44|, |v45|, v32\n"
      "global_load_lds_dwordx4 v27, s[6:7] offset:0\nv_lshl_add_u32 v27, v198, 7, v4\nv_max3_f32 v33, |v44|, |v45|, v33\ns_cbranch_vccz .La1007\ns_add_u32 m0, s26, 69632\ns_nop 0\n"
      "global_load_lds_dwordx4 v28, s[6:7] offset:0\n"
      ".La1007:\nv_lshl_add_u32 v28, v200, 7, v4\ns_add_u32 m0, s53, 45056\nv_and_b32 v96, s55, v44\nglobal_load_lds_dwordx4 v22, s[6:7] offset:0\nv_lshl_add_u32 v22, v176, 7, v4\n"
      "v_xor_b32 v44, v44, v96\nglobal_load_lds_dwordx4 v23, s[44:45] offset:1024\nv_lshl_add_u32 v23, v182, 7, v5\nv_and_b32 v97, s55, v45\nglobal_load_lds_dwordx4 v24, s[46:47] offset:2048\n"
      "v_lshl_add_u32 v24, v184, 7, v4\nv_xor_b32 v45, v45, v97\nglobal_load_lds_dwordx4 v25, s[48:49] offset:3072\ns_add_u32 m0, s53, 49152\nv_lshl_add_u32 v25, v190, 7, v5\n"
      "global_load_lds_dwordx4 v26, s[6:7] offset:0\nv_lshl_add_u32 v26, v192, 7, v4\ns_cmp_lg_u32 s31, 0\ns_cbranch_scc1 .Lb1008\ns_lshl_b32 s42, s24, 9\nv_lshl_add_u32 v39, v240, 3, s42\n"
      "s_and_b32 s42, s2, 7\ns_lshl_b32 s42, s42, 11\ns_add_u32 s42, s42, 4096\ns_add_u32 s58, s20, s42\ns_addc_u32 s59, s21, 0\nglobal_store_dwordx2 v39, v[254:255], s[58:59] sc1\n"
      ".Lb1008:\ns_waitcnt vmcnt(11) lgkmcnt(0)\ns_cbranch_vccnz .Lw1009\ns_waitcnt vmcnt(10)\n"
      ".Lw1009:\ns_barrier\ns_add_u32 m0, s54, 90112\ns_nop 0\nglobal_load_lds_dwordx4 v236, s[50:51] offset:0\nds_read_b64 v[48:49], v30 offset:0\n"
      "global_load_lds_dwordx4 v237, s[50:51] offset:1024\nglobal_load_lds_dwordx4 v238, s[50:51] offset:2048\nglobal_load_lds_dwordx4 v239, s[50:51] offset:3072\ns_add_u32 s50, s50, 212992\n"
      "s_addc_u32 s51, s51, 0\ns_add_u32 m0, s26, 110592\nv_max3_f16 v32, |v46|, |v47|, v32\nglobal_load_lds_dwordx4 v27, s[6:7] offset:0\nv_lshl_add_u32 v27, v199, 7, v4\n"
      "ds_read2_b64 v[196:199], v8 offset0:115 offset1:116\nv_max3_f32 v33, |v46|, |v47|, v33\ns_cbranch_vccz .La1010\ns_add_u32 m0, s26, 114688\ns_nop 0\n"
      "global_load_lds_dwordx4 v28, s[6:7] offset:0\n"
      ".La1010:\nv_lshl_add_u32 v28, v201, 7, v4\ns_add_u32 m0, s53, 90112\nv_and_b32 v98, s55, v46\nglobal_load_lds_dwordx4 v22, s[6:7] offset:0\nv_lshl_add_u32 v22, v177, 7, v4\n"
      "v_xor_b32 v46, v46, v98\nglobal_load_lds_dwordx4 v23, s[44:45] offset:1024\nv_lshl_add_u32 v23, v183, 7, v5\nds_read2_b64 v[180:183], v6 offset0:115 offset1:116\nv_and_b32 v99, s55, v47\n"
      "global_load_lds_dwordx4 v24, s[46:47] offset:2048\nv_lshl_add_u32 v24, v185, 7, v4\nv_xor_b32 v47, v47, v99\nglobal_load_lds_dwordx4 v25, s[48:49] offset:3072\n"
      "v_lshl_add_u32 v25, v191, 7, v5\ns_add_u32 m0, s53, 94208\nds_read2_b64 v[188:191], v7 offset0:115 offset1:116\nglobal_load_lds_dwordx4 v26, s[6:7] offset:0\n"
      "v_lshl_add_u32 v26, v193, 7, v4\ns_cmp_lg_u32 s24, 2\ns_cbranch_scc1 .Lo1011\ns_getpc_b64 s[62:63]\ns_add_u32 s62, s62, _ZL5g_tab@rel32@lo+2052\n"
      "s_addc_u32 s63, s63, _ZL5g_tab@rel32@hi+2060\ns_lshr_b32 s42, s2, 3\ns_lshl1_add_u32 s42, s2, s42\ns_and_b32 s42, s42, 15\ns_lshl_b32 s42, s42, 7\nv_lshl_add_u32 v40, v240, 4, s42\n"
      "s_mov_b32 m0, 162112\ns_mov_b64 exec, 0xff\nglobal_load_lds_dwordx4 v40, s[62:63]\ns_mov_b64 exec, -1\n"
      ".Lo1011:\ns_waitcnt vmcnt(11) lgkmcnt(0)\ns_cbranch_vccnz .Lw1012\ns_waitcnt vmcnt(10)\n"
      ".Lw1012:\ns_barrier\ns_add_u32 m0, s54, 0\ns_nop 0\nglobal_load_lds_dwordx4 v236, s[50:51] offset:0\nds_read_b64 v[50:51], v30 offset:45056\n"
      "global_load_lds_dwordx4 v237, s[50:51] offset:1024\nglobal_load_lds_dwordx4 v238, s[50:51] offset:2048\nglobal_load_lds_dwordx4 v239, s[50:51] offset:3072\ns_add_u32 s50, s50, 212992\n"
      "s_addc_u32 s51, s51, 0\ns_cmp_eq_u32 s27, 20\ns_cselect_b64 s[50:51], s[10:11], s[50:51]\ns_add_u32 m0, s26, 20480\nv_max3_f16 v32, |v48|, |v49|, v32\n"
      "global_load_lds_dwordx4 v27, s[6:7] offset:0\nv_lshl_add_u32 v27, v196, 7, v4\nv_max3_f32 v33, |v48|, |v49|, v33\ns_cbranch_vccz .La1013\ns_add_u32 m0, s26, 24576\ns_nop 0\n"
      "global_load_lds_dwordx4 v28, s[6:7] offset:0\n"
      ".La1013:\nv_lshl_add_u32 v28, v202, 7, v4\ns_add_u32 m0, s53, 0\nv_and_b32 v100, s55, v48\nglobal_load_lds_dwordx4 v22, s[6:7] offset:0\nv_lshl_add_u32 v22, v178, 7, v4\n"
      "v_xor_b32 v48, v48, v100\nglobal_load_lds_dwordx4 v23, s[44:45] offset:1024\nv_lshl_add_u32 v23, v180, 7, v5\nv_and_b32 v101, s55, v49\nglobal_load_lds_dwordx4 v24, s[46:47] offset:2048\n"
      "v_lshl_add_u32 v24, v186, 7, v4\nv_xor_b32 v49, v49, v101\nglobal_load_lds_dwordx4 v25, s[48:49] offset:3072\ns_add_u32 m0, s53, 4096\nv_lshl_add_u32 v25, v188, 7, v5\n"
      "global_load_lds_dwordx4 v26, s[6:7] offset:0\nv_lshl_add_u32 v26, v194, 7, v4\ns_waitcnt vmcnt(11) lgkmcnt(0)\ns_cbranch_vccnz .Lw1014\ns_waitcnt vmcnt(10)\n"
      ".Lw1014:\ns_barrier\ns_add_u32 m0, s54, 45056\ns_nop 0\nglobal_load_lds_dwordx4 v236, s[50:51] offset:0\nds_read_b64 v[52:53], v31 offset:0\n"
      "global_load_lds_dwordx4 v237, s[50:51] offset:1024\nglobal_load_lds_dwordx4 v238, s[50:51] offset:2048\nglobal_load_lds_dwordx4 v239, s[50:51] offset:3072\ns_add_u32 s50, s50, 212992\n"
      "s_addc_u32 s51, s51, 0\ns_add_u32 m0, s26, 65536\nv_max3_f16 v32, |v50|, |v51|, v32\nglobal_load_lds_dwordx4 v27, s[6:7] offset:0\nv_lshl_add_u32 v27, v197, 7, v4\n"
      "v_max3_f32 v33, |v50|, |v51|, v33\ns_cbranch_vccz .La1015\ns_add_u32 m0, s26, 69632\ns_nop 0\nglobal_load_lds_dwordx4 v28, s[6:7] offset:0\n"
      ".La1015:\nv_lshl_add_u32 v28, v203, 7, v4\nds_read2_b64 v[200:203], v9 offset0:4 offset1:5\ns_add_u32 m0, s53, 45056\nv_and_b32 v102, s55, v50\nglobal_load_lds_dwordx4 v22, s[6:7] offset:0\n"
      "v_lshl_add_u32 v22, v179, 7, v4\nds_read2_b64 v[176:179], v6 offset0:4 offset1:5\nv_xor_b32 v50, v50, v102\nglobal_load_lds_dwordx4 v23, s[44:45] offset:1024\n"
      "v_lshl_add_u32 v23, v181, 7, v5\nv_and_b32 v103, s55, v51\nglobal_load_lds_dwordx4 v24, s[46:47] offset:2048\nv_lshl_add_u32 v24, v187, 7, v4\n"
      "ds_read2_b64 v[184:187], v7 offset0:4 offset1:5\nv_xor_b32 v51, v51, v103\nglobal_load_lds_dwordx4 v25, s[48:49] offset:3072\ns_add_u32 m0, s53, 49152\nv_lshl_add_u32 v25, v189, 7, v5\n"
      "global_load_lds_dwordx4 v26, s[6:7] offset:0\nv_lshl_add_u32 v26, v195, 7, v4\nds_read2_b64 v[192:195], v8 offset0:4 offset1:5\ns_waitcnt vmcnt(11) lgkmcnt(0)\ns_cbranch_vccnz .Lw1016\n"
      "s_waitcnt vmcnt(10)\n"
      ".Lw1016:\ns_barrier\ns_add_u32 m0, s54, 90112\ns_nop 0\nglobal_load_lds_dwordx4 v236, s[50:51] offset:0\nds_read_b64 v[54:55], v30 offset:0\n"
      "global_load_lds_dwordx4 v237, s[50:51] offset:1024\nglobal_load_lds_dwordx4 v238, s[50:51] offset:2048\nglobal_load_lds_dwordx4 v239, s[50:51] offset:3072\ns_add_u32 s50, s50, 212992\n"
      "s_addc_u32 s51, s51, 0\ns_cmp_eq_u32 s27, 18\ns_cselect_b64 s[50:51], s[10:11], s[50:51]\ns_add_u32 m0, s26, 110592\nv_max3_f16 v32, |v52|, |v53|, v32\n"
      "global_load_lds_dwordx4 v27, s[6:7] offset:0\nv_lshl_add_u32 v27, v198, 7, v4\nv_max3_f32 v33, |v52|, |v53|, v33\ns_cbranch_vccz .La1017\ns_add_u32 m0, s26, 114688\ns_nop 0\n"
      "global_load_lds_dwordx4 v28, s[6:7] offset:0\n"
      ".La1017:\nv_lshl_add_u32 v28, v200, 7, v4\ns_add_u32 m0, s53, 90112\nv_and_b32 v104, s55, v52\nglobal_load_lds_dwordx4 v22, s[6:7] offset:0\nv_lshl_add_u32 v22, v176, 7, v4\n"
      "v_xor_b32 v52, v52, v104\nglobal_load_lds_dwordx4 v23, s[44:45] offset:1024\nv_lshl_add_u32 v23, v182, 7, v5\nv_and_b32 v105, s55, v53\nglobal_load_lds_dwordx4 v24, s[46:47] offset:2048\n"
      "v_lshl_add_u32 v24, v184, 7, v4\nv_xor_b32 v53, v53, v105\nglobal_load_lds_dwordx4 v25, s[48:49] offset:3072\ns_add_u32 m0, s53, 94208\nv_lshl_add_u32 v25, v190, 7, v5\n"
      "global_load_lds_dwordx4 v26, s[6:7] offset:0\nv_lshl_add_u32 v26, v192, 7, v4\ns_waitcnt vmcnt(11) lgkmcnt(0)\ns_cbranch_vccnz .Lw1018\ns_waitcnt vmcnt(10)\n"
      ".Lw1018:\ns_barrier\ns_add_u32 m0, s54, 0\ns_nop 0\nglobal_load_lds_dwordx4 v236, s[50:51] offset:0\nds_read_b64 v[56:57], v30 offset:45056\n"
      "global_load_lds_dwordx4 v237, s[50:51] offset:1024\nglobal_load_lds_dwordx4 v238, s[50:51] offset:2048\nglobal_load_lds_dwordx4 v239, s[50:51] offset:3072\ns_add_u32 s50, s50, 212992\n"
      "s_addc_u32 s51, s51, 0\nv_max3_f16 v32, |v54|, |v55|, v32\nv_max3_f32 v33, |v54|, |v55|, v33\ns_add_u32 m0, s26, 20480\nv_cmp_eq_u32 s[62:63], 0, v240\n"
      "global_load_lds_dwordx4 v27, s[6:7] offset:0\nv_lshl_add_u32 v27, v199, 7, v4\nds_read2_b64 v[196:199], v8 offset0:117 offset1:118\nv_mov_b32 v253, 262144\nv_mov_b32 v248, 162048\n"
      "v_mov_b32 v241, 1\ns_cbranch_vccz .La1019\ns_add_u32 m0, s26, 24576\ns_nop 0\nglobal_load_lds_dwordx4 v28, s[6:7] offset:0\n"
      ".La1019:\nv_lshl_add_u32 v28, v201, 7, v4\nv_cndmask_b32 v253, v253, v248, s[62:63]\ns_add_u32 m0, s53, 0\nv_and_b32 v106, s55, v54\nglobal_load_lds_dwordx4 v22, s[6:7] offset:0\n"
      "v_lshl_add_u32 v22, v177, 7, v4\nv_xor_b32 v54, v54, v106\nv_and_b32 v107, s55, v55\nglobal_load_lds_dwordx4 v23, s[44:45] offset:1024\nv_lshl_add_u32 v23, v183, 7, v5\n"
      "ds_read2_b64 v[180:183], v6 offset0:117 offset1:118\nv_xor_b32 v55, v55, v107\nv_lshrrev_b32 v164, 4, v240\nglobal_load_lds_dwordx4 v24, s[46:47] offset:2048\n"
      "v_lshl_add_u32 v24, v185, 7, v4\nv_bfe_u32 v165, v240, 2, 2\nv_and_b32 v166, 3, v240\nglobal_load_lds_dwordx4 v25, s[48:49] offset:3072\nv_lshl_add_u32 v25, v191, 7, v5\n"
      "ds_read2_b64 v[188:191], v7 offset0:117 offset1:118\nv_mul_u32_u24 v166, 0x80008, v166\ns_add_u32 m0, s53, 4096\nv_add_u32 v244, v164, v165\nglobal_load_lds_dwordx4 v26, s[6:7] offset:0\n"
      "v_lshl_add_u32 v26, v193, 7, v4\nv_and_b32 v244, 3, v244\nv_add_u32 v168, 22, v165\ns_waitcnt vmcnt(11) lgkmcnt(0)\ns_cbranch_vccnz .Lw1020\ns_waitcnt vmcnt(10)\n"
      ".Lw1020:\ns_barrier\ns_add_u32 m0, s54, 45056\ns_nop 0\nglobal_load_lds_dwordx4 v236, s[50:51] offset:0\nds_read_b64 v[58:59], v31 offset:0\n"
      "global_load_lds_dwordx4 v237, s[50:51] offset:1024\nglobal_load_lds_dwordx4 v238, s[50:51] offset:2048\nglobal_load_lds_dwordx4 v239, s[50:51] offset:3072\ns_add_u32 s50, s50, 212992\n"
      "s_addc_u32 s51, s51, 0\ns_cmp_eq_u32 s27, 16\ns_cselect_b64 s[50:51], s[10:11], s[50:51]\nv_max3_f16 v32, |v56|, |v57|, v32\nv_max3_f32 v33, |v56|, |v57|, v33\ns_add_u32 m0, s26, 65536\n"
      "v_cmp_gt_u32 s[62:63], 2, v165\nglobal_load_lds_dwordx4 v27, s[6:7] offset:0\nv_lshl_add_u32 v27, v196, 7, v4\nv_and_b32 v165, 60, v240\ns_mov_b32 s42, 0\n"
      "v_cndmask_b32 v168, v168, v244, s[62:63]\ns_cbranch_vccz .La1021\ns_add_u32 m0, s26, 69632\ns_nop 0\nglobal_load_lds_dwordx4 v28, s[6:7] offset:0\n"
      ".La1021:\nv_lshl_add_u32 v28, v202, 7, v4\nv_lshl_or_b32 v244, v244, 16, v168\nv_lshl_add_u32 v244, v244, 5, v166\ns_add_u32 m0, s53, 45056\nv_lshl_add_u32 v252, v240, 4, s42\n"
      "global_load_lds_dwordx4 v22, s[6:7] offset:0\nv_lshl_add_u32 v22, v178, 7, v4\nv_and_b32 v108, s55, v56\nv_xor_b32 v56, v56, v108\nv_and_b32 v109, s55, v57\n"
      "global_load_lds_dwordx4 v23, s[44:45] offset:1024\nv_lshl_add_u32 v23, v180, 7, v5\nv_xor_b32 v57, v57, v109\ns_mov_b32 s42, 0x76325410\ns_mov_b32 s43, 0x21005410\n"
      "global_load_lds_dwordx4 v24, s[46:47] offset:2048\nv_lshl_add_u32 v24, v186, 7, v4\nv_lshrrev_b64 v[168:169], v165, s[42:43]\nv_and_b32 v168, 7, v168\nv_lshl_add_u32 v168, v164, 3, v168\n"
      "global_load_lds_dwordx4 v25, s[48:49] offset:3072\nv_lshl_add_u32 v25, v188, 7, v5\ns_mov_b32 s42, 0x54107632\ns_mov_b32 s43, 0x21107632\ns_add_u32 m0, s53, 49152\n"
      "v_lshrrev_b64 v[170:171], v165, s[42:43]\nglobal_load_lds_dwordx4 v26, s[6:7] offset:0\nv_lshl_add_u32 v26, v194, 7, v4\nv_and_b32 v170, 7, v170\nv_lshl_add_u32 v170, v164, 3, v170\n"
      "s_waitcnt vmcnt(11) lgkmcnt(0)\ns_cbranch_vccnz .Lw1022\ns_waitcnt vmcnt(10)\n"
      ".Lw1022:\ns_barrier\ns_add_u32 m0, s54, 90112\ns_nop 0\nglobal_load_lds_dwordx4 v236, s[50:51] offset:0\nds_read_b64 v[60:61], v30 offset:0\n"
      "global_load_lds_dwordx4 v237, s[50:51] offset:1024\nglobal_load_lds_dwordx4 v238, s[50:51] offset:2048\nglobal_load_lds_dwordx4 v239, s[50:51] offset:3072\ns_add_u32 s50, s50, 212992\n"
      "s_addc_u32 s51, s51, 0\nv_max3_f16 v32, |v58|, |v59|, v32\ns_add_u32 m0, s26, 110592\nv_max3_f32 v33, |v58|, |v59|, v33\nglobal_load_lds_dwordx4 v27, s[6:7] offset:0\n"
      "v_lshl_add_u32 v27, v197, 7, v4\nv_lshl_or_b32 v245, v170, 16, v168\nv_lshl_add_u32 v245, v245, 5, v166\ns_cbranch_vccz .La1023\ns_add_u32 m0, s26, 114688\ns_nop 0\n"
      "global_load_lds_dwordx4 v28, s[6:7] offset:0\n"
      ".La1023:\nv_lshl_add_u32 v28, v203, 7, v4\nds_read2_b64 v[200:203], v9 offset0:6 offset1:7\nv_and_b32 v165, 1, v240\ns_add_u32 m0, s53, 90112\nv_mul_u32_u24 v165, 0xaaaa, v165\n"
      "global_load_lds_dwordx4 v22, s[6:7] offset:0\nv_lshl_add_u32 v22, v179, 7, v4\nds_read2_b64 v[176:179], v6 offset0:6 offset1:7\nv_add_u32 v242, 0x4444, v165\ns_lshl_b32 s42, s36, 4\n"
      "global_load_lds_dwordx4 v23, s[44:45] offset:1024\nv_lshl_add_u32 v23, v181, 7, v5\ns_add_u32 s42, s42, 160448\nv_lshl_add_u32 v243, v164, 2, s42\n"
      "global_load_lds_dwordx4 v24, s[46:47] offset:2048\nv_lshl_add_u32 v24, v187, 7, v4\nds_read2_b64 v[184:187], v7 offset0:6 offset1:7\nv_and_b32 v110, s55, v58\nv_xor_b32 v58, v58, v110\n"
      "global_load_lds_dwordx4 v25, s[48:49] offset:3072\nv_lshl_add_u32 v25, v189, 7, v5\ns_add_u32 m0, s53, 94208\nv_and_b32 v111, s55, v59\nglobal_load_lds_dwordx4 v26, s[6:7] offset:0\n"
      "v_lshl_add_u32 v26, v195, 7, v4\nds_read2_b64 v[192:195], v8 offset0:6 offset1:7\nv_xor_b32 v59, v59, v111\ns_waitcnt vmcnt(11) lgkmcnt(0)\ns_cbranch_vccnz .Lw1024\ns_waitcnt vmcnt(10)\n"
      ".Lw1024:\ns_barrier\ns_add_u32 m0, s54, 0\ns_nop 0\nglobal_load_lds_dwordx4 v236, s[50:51] offset:0\nds_read_b64 v[62:63], v30 offset:45056\n"
      "global_load_lds_dwordx4 v237, s[50:51] offset:1024\nglobal_load_lds_dwordx4 v238, s[50:51] offset:2048\nglobal_load_lds_dwordx4 v239, s[50:51] offset:3072\ns_add_u32 s50, s50, 212992\n"
      "s_addc_u32 s51, s51, 0\ns_cmp_eq_u32 s27, 14\ns_cselect_b64 s[50:51], s[10:11], s[50:51]\nv_max3_f16 v32, |v60|, |v61|, v32\nv_max3_f32 v33, |v60|, |v61|, v33\ns_add_u32 m0, s26, 20480\n"
      "v_and_b32 v164, 1, v164\nglobal_load_lds_dwordx4 v27, s[6:7] offset:0\nv_lshl_add_u32 v27, v198, 7, v4\nv_lshl_add_u32 v164, v164, 3, s24\ns_sub_u32 s42, s64, 1\ns_cbranch_vccz .La1025\n"
      "s_add_u32 m0, s26, 24576\ns_nop 0\nglobal_load_lds_dwordx4 v28, s[6:7] offset:0\n"
      ".La1025:\nv_lshl_add_u32 v28, v200, 7, v4\nv_min_u32 v164, s42, v164\ns_add_u32 m0, s53, 0\nv_lshlrev_b32 v164, 3, v164\nglobal_load_lds_dwordx4 v22, s[6:7] offset:0\n"
      "v_lshl_add_u32 v22, v176, 7, v4\nv_add_u32 v250, 160256, v164\nv_and_b32 v165, 15, v240\nglobal_load_lds_dwordx4 v23, s[44:45] offset:1024\nv_lshl_add_u32 v23, v182, 7, v5\n"
      "v_min_u32 v165, 12, v165\ns_lshl_b32 s42, s30, 7\nglobal_load_lds_dwordx4 v24, s[46:47] offset:2048\nv_lshl_add_u32 v24, v184, 7, v4\nv_lshl_add_u32 v165, v165, 15, s42\n"
      "v_add_u32 v249, v165, v164\nglobal_load_lds_dwordx4 v25, s[48:49] offset:3072\nv_lshl_add_u32 v25, v190, 7, v5\nv_and_b32 v112, s55, v60\ns_add_u32 m0, s53, 4096\nv_xor_b32 v60, v60, v112\n"
      "global_load_lds_dwordx4 v26, s[6:7] offset:0\nv_lshl_add_u32 v26, v192, 7, v4\nv_and_b32 v113, s55, v61\nv_xor_b32 v61, v61, v113\ns_waitcnt vmcnt(11) lgkmcnt(0)\ns_cbranch_vccnz .Lw1026\n"
      "s_waitcnt vmcnt(10)\n"
      ".Lw1026:\ns_barrier\ns_add_u32 m0, s54, 45056\ns_nop 0\nglobal_load_lds_dwordx4 v236, s[50:51] offset:0\nds_read_b64 v[64:65], v31 offset:0\n"
      "global_load_lds_dwordx4 v237, s[50:51] offset:1024\nglobal_load_lds_dwordx4 v238, s[50:51] offset:2048\nglobal_load_lds_dwordx4 v239, s[50:51] offset:3072\ns_add_u32 s50, s50, 212992\n"
      "s_addc_u32 s51, s51, 0\nv_max3_f16 v32, |v62|, |v63|, v32\ns_add_u32 m0, s26, 65536\nv_max3_f32 v33, |v62|, |v63|, v33\nglobal_load_lds_dwordx4 v27, s[6:7] offset:0\n"
      "v_lshl_add_u32 v27, v199, 7, v4\nds_read2_b64 v[196:199], v8 offset0:119 offset1:120\ns_mul_i32 s65, s36, 22272\ns_add_u32 s65, s65, 45056\ns_cbranch_vccz .La1027\ns_add_u32 m0, s26, 69632\n"
      "s_nop 0\nglobal_load_lds_dwordx4 v28, s[6:7] offset:0\n"
      ".La1027:\nv_lshl_add_u32 v28, v201, 7, v4\ns_add_u32 s68, s64, 8\ns_min_u32 s68, s68, 21\ns_add_u32 m0, s53, 45056\ns_nop 0\nglobal_load_lds_dwordx4 v22, s[6:7] offset:0\n"
      "v_lshl_add_u32 v22, v177, 7, v4\ns_lshl_b32 s38, s38, 16\nglobal_load_lds_dwordx4 v23, s[44:45] offset:1024\nv_lshl_add_u32 v23, v183, 7, v5\n"
      "ds_read2_b64 v[180:183], v6 offset0:119 offset1:120\nv_and_b32 v114, s55, v62\nglobal_load_lds_dwordx4 v24, s[46:47] offset:2048\nv_lshl_add_u32 v24, v185, 7, v4\nv_xor_b32 v62, v62, v114\n"
      "global_load_lds_dwordx4 v25, s[48:49] offset:3072\nv_lshl_add_u32 v25, v191, 7, v5\nds_read2_b64 v[188:191], v7 offset0:119 offset1:120\ns_add_u32 m0, s53, 49152\nv_and_b32 v115, s55, v63\n"
      "global_load_lds_dwordx4 v26, s[6:7] offset:0\nv_lshl_add_u32 v26, v193, 7, v4\nv_xor_b32 v63, v63, v115\ns_waitcnt vmcnt(11) lgkmcnt(0)\ns_cbranch_vccnz .Lw1028\ns_waitcnt vmcnt(10)\n"
      ".Lw1028:\ns_barrier\ns_add_u32 m0, s54, 90112\ns_nop 0\nglobal_load_lds_dwordx4 v236, s[50:51] offset:0\nds_read_b64 v[66:67], v30 offset:0\n"
      "global_load_lds_dwordx4 v237, s[50:51] offset:1024\nglobal_load_lds_dwordx4 v238, s[50:51] offset:2048\nglobal_load_lds_dwordx4 v239, s[50:51] offset:3072\ns_add_u32 s50, s50, 212992\n"
      "s_addc_u32 s51, s51, 0\ns_cmp_eq_u32 s27, 12\ns_cselect_b64 s[50:51], s[10:11], s[50:51]\nv_max3_f16 v32, |v64|, |v65|, v32\nv_max3_f32 v33, |v64|, |v65|, v33\ns_lshr_b32 s42, s24, 2\n"
      "s_add_u32 m0, s26, 110592\ns_nop 0\nglobal_load_lds_dwordx4 v27, s[6:7] offset:0\nv_lshl_add_u32 v27, v196, 7, v4\ns_mul_i32 s43, s42, 3\ns_xor_b32 s43, s43, s36\ns_cbranch_vccz .La1029\n"
      "s_add_u32 m0, s26, 114688\ns_nop 0\nglobal_load_lds_dwordx4 v28, s[6:7] offset:0\n"
      ".La1029:\nv_lshl_add_u32 v28, v202, 7, v4\ns_add_u32 s42, s42, s64\ns_add_u32 s42, s42, 9\ns_add_u32 m0, s53, 90112\ns_nop 0\nglobal_load_lds_dwordx4 v22, s[6:7] offset:0\n"
      "v_lshl_add_u32 v22, v178, 7, v4\ns_and_b32 s42, s42, 1\ns_lshl1_add_u32 s67, s43, s42\nglobal_load_lds_dwordx4 v23, s[44:45] offset:1024\nv_lshl_add_u32 v23, v180, 7, v5\n"
      "s_add_u32 s66, s64, s67\ns_cmp_lt_u32 s66, s68\ns_cselect_b32 s66, s66, 127\nglobal_load_lds_dwordx4 v24, s[46:47] offset:2048\nv_lshl_add_u32 v24, v186, 7, v4\ns_sub_u32 s66, s66, 8\n"
      "s_max_i32 s66, s66, 0\nglobal_load_lds_dwordx4 v25, s[48:49] offset:3072\nv_lshl_add_u32 v25, v188, 7, v5\nv_and_b32 v116, s55, v64\ns_add_u32 m0, s53, 94208\nv_xor_b32 v64, v64, v116\n"
      "global_load_lds_dwordx4 v26, s[6:7] offset:0\nv_lshl_add_u32 v26, v194, 7, v4\nv_and_b32 v117, s55, v65\nv_xor_b32 v65, v65, v117\ns_waitcnt vmcnt(11) lgkmcnt(0)\ns_cbranch_vccnz .Lw1030\n"
      "s_waitcnt vmcnt(10)\n"
      ".Lw1030:\ns_barrier\ns_add_u32 m0, s54, 0\ns_nop 0\nglobal_load_lds_dwordx4 v236, s[50:51] offset:0\nds_read_b64 v[68:69], v30 offset:45056\n"
      "global_load_lds_dwordx4 v237, s[50:51] offset:1024\nglobal_load_lds_dwordx4 v238, s[50:51] offset:2048\nglobal_load_lds_dwordx4 v239, s[50:51] offset:3072\ns_add_u32 s50, s50, 212992\n"
      "s_addc_u32 s51, s51, 0\ns_add_u32 m0, s26, 20480\nv_max3_f16 v32, |v66|, |v67|, v32\nglobal_load_lds_dwordx4 v27, s[6:7] offset:0\nv_lshl_add_u32 v27, v197, 7, v4\n"
      "v_max3_f32 v33, |v66|, |v67|, v33\ns_cbranch_vccz .La1031\ns_add_u32 m0, s26, 24576\ns_nop 0\nglobal_load_lds_dwordx4 v28, s[6:7] offset:0\n"
      ".La1031:\nv_lshl_add_u32 v28, v203, 7, v4\nds_read2_b64 v[200:203], v9 offset0:8 offset1:9\ns_getpc_b64 s[34:35]\n"
      ".Lk1005:\ns_add_u32 m0, s53, 0\ns_nop 0\nglobal_load_lds_dwordx4 v22, s[6:7] offset:0\nv_lshl_add_u32 v22, v179, 7, v4\nds_read2_b64 v[176:179], v6 offset0:8 offset1:9\n"
      "s_add_u32 s34, s34, .Lt_p-.Lk1005\ns_addc_u32 s35, s35, 0\nglobal_load_lds_dwordx4 v23, s[44:45] offset:1024\nv_lshl_add_u32 v23, v181, 7, v5\nv_and_b32 v118, s55, v66\n"
      "global_load_lds_dwordx4 v24, s[46:47] offset:2048\nv_lshl_add_u32 v24, v187, 7, v4\nds_read2_b64 v[184:187], v7 offset0:8 offset1:9\nv_xor_b32 v66, v66, v118\n"
      "global_load_lds_dwordx4 v25, s[48:49] offset:3072\nv_lshl_add_u32 v25, v189, 7, v5\ns_add_u32 m0, s53, 4096\nv_and_b32 v119, s55, v67\nglobal_load_lds_dwordx4 v26, s[6:7] offset:0\n"
      "v_lshl_add_u32 v26, v195, 7, v4\nds_read2_b64 v[192:195], v8 offset0:8 offset1:9\nv_xor_b32 v67, v67, v119\ns_waitcnt vmcnt(11) lgkmcnt(0)\ns_cbranch_vccnz .Lw1032\ns_waitcnt vmcnt(10)\n"
      ".Lw1032:\ns_barrier\ns_add_u32 m0, s54, 45056\ns_nop 0\nglobal_load_lds_dwordx4 v236, s[50:51] offset:0\nds_read_b64 v[70:71], v31 offset:0\n"
      "global_load_lds_dwordx4 v237, s[50:51] offset:1024\nglobal_load_lds_dwordx4 v238, s[50:51] offset:2048\nglobal_load_lds_dwordx4 v239, s[50:51] offset:3072\ns_add_u32 s50, s50, 212992\n"
      "s_addc_u32 s51, s51, 0\ns_cmp_eq_u32 s27, 10\ns_cselect_b64 s[50:51], s[10:11], s[50:51]\nv_max3_f16 v32, |v68|, |v69|, v32\ns_add_u32 m0, s26, 65536\nv_max3_f32 v33, |v68|, |v69|, v33\n"
      "global_load_lds_dwordx4 v27, s[6:7] offset:0\nv_lshl_add_u32 v27, v198, 7, v4\ns_lshr_b32 s42, s2, 3\ns_lshl1_add_u32 s42, s2, s42\ns_cbranch_vccz .La1033\ns_add_u32 m0, s26, 69632\n"
      "s_nop 0\nglobal_load_lds_dwordx4 v28, s[6:7] offset:0\n"
      ".La1033:\nv_lshl_add_u32 v28, v200, 7, v4\ns_and_b32 s69, s42, 15\ns_lshl_b32 s43, s69, 7\ns_add_u32 m0, s53, 45056\ns_nop 0\nglobal_load_lds_dwordx4 v22, s[6:7] offset:0\n"
      "v_lshl_add_u32 v22, v176, 7, v4\ns_add_u32 s43, s43, 32768\ns_add_u32 s8, s20, s43\ns_addc_u32 s9, s21, 0\nglobal_load_lds_dwordx4 v23, s[44:45] offset:1024\n"
      "v_lshl_add_u32 v23, v182, 7, v5\ns_add_u32 s12, s20, 65536\ns_addc_u32 s13, s21, 0\ns_add_u32 s16, s20, 2097152\ns_addc_u32 s17, s21, 0\nglobal_load_lds_dwordx4 v24, s[46:47] offset:2048\n"
      "v_lshl_add_u32 v24, v184, 7, v4\nv_and_b32 v120, s55, v68\nv_xor_b32 v68, v68, v120\nglobal_load_lds_dwordx4 v25, s[48:49] offset:3072\nv_lshl_add_u32 v25, v190, 7, v5\n"
      "s_add_u32 m0, s53, 49152\nv_and_b32 v121, s55, v69\nglobal_load_lds_dwordx4 v26, s[6:7] offset:0\nv_lshl_add_u32 v26, v192, 7, v4\nv_xor_b32 v69, v69, v121\ns_waitcnt vmcnt(11) lgkmcnt(0)\n"
      "s_cbranch_vccnz .Lw1034\ns_waitcnt vmcnt(10)\n"
      ".Lw1034:\ns_barrier\ns_add_u32 m0, s54, 90112\ns_nop 0\nglobal_load_lds_dwordx4 v236, s[50:51] offset:0\nds_read_b64 v[72:73], v30 offset:0\n"
      "global_load_lds_dwordx4 v237, s[50:51] offset:1024\nglobal_load_lds_dwordx4 v238, s[50:51] offset:2048\nglobal_load_lds_dwordx4 v239, s[50:51] offset:3072\ns_add_u32 s50, s50, 212992\n"
      "s_addc_u32 s51, s51, 0\nv_max3_f16 v32, |v70|, |v71|, v32\ns_add_u32 m0, s26, 110592\nv_max3_f32 v33, |v70|, |v71|, v33\nglobal_load_lds_dwordx4 v27, s[6:7] offset:0\n"
      "v_lshl_add_u32 v27, v199, 7, v4\nds_read2_b64 v[196:199], v8 offset0:121 offset1:122\ns_lshl_b32 s42, s69, 4\ns_max_u32 s42, s42, 24\ns_cbranch_vccz .La1035\ns_add_u32 m0, s26, 114688\n"
      "s_nop 0\nglobal_load_lds_dwordx4 v28, s[6:7] offset:0\n"
      ".La1035:\nv_lshl_add_u32 v28, v201, 7, v4\ns_sub_u32 s42, s42, 24\ns_min_u32 s69, s42, 192\ns_add_u32 m0, s53, 90112\ns_nop 0\nglobal_load_lds_dwordx4 v22, s[6:7] offset:0\n"
      "v_lshl_add_u32 v22, v177, 7, v4\ns_lshl_b32 s42, s69, 3\ns_add_u32 s42, s42, 24576\nglobal_load_lds_dwordx4 v23, s[44:45] offset:1024\nv_lshl_add_u32 v23, v183, 7, v5\n"
      "ds_read2_b64 v[180:183], v6 offset0:121 offset1:122\ns_add_u32 s14, s20, s42\ns_addc_u32 s15, s21, 0\nv_and_b32 v122, s55, v70\nglobal_load_lds_dwordx4 v24, s[46:47] offset:2048\n"
      "v_lshl_add_u32 v24, v185, 7, v4\nv_xor_b32 v70, v70, v122\nglobal_load_lds_dwordx4 v25, s[48:49] offset:3072\nv_lshl_add_u32 v25, v191, 7, v5\n"
      "ds_read2_b64 v[188:191], v7 offset0:121 offset1:122\ns_add_u32 m0, s53, 94208\nv_and_b32 v123, s55, v71\nglobal_load_lds_dwordx4 v26, s[6:7] offset:0\nv_lshl_add_u32 v26, v193, 7, v4\n"
      "v_xor_b32 v71, v71, v123\ns_waitcnt vmcnt(11) lgkmcnt(0)\ns_cbranch_vccnz .Lw1036\ns_waitcnt vmcnt(10)\n"
      ".Lw1036:\ns_barrier\ns_add_u32 m0, s54, 0\ns_nop 0\nglobal_load_lds_dwordx4 v236, s[50:51] offset:0\nds_read_b64 v[74:75], v30 offset:45056\n"
      "global_load_lds_dwordx4 v237, s[50:51] offset:1024\nglobal_load_lds_dwordx4 v238, s[50:51] offset:2048\nglobal_load_lds_dwordx4 v239, s[50:51] offset:3072\ns_add_u32 s50, s50, 212992\n"
      "s_addc_u32 s51, s51, 0\ns_cmp_eq_u32 s27, 8\ns_cselect_b64 s[50:51], s[10:11], s[50:51]\nv_max3_f16 v32, |v72|, |v73|, v32\ns_add_u32 m0, s26, 20480\nv_max3_f32 v33, |v72|, |v73|, v33\n"
      "global_load_lds_dwordx4 v27, s[6:7] offset:0\nv_lshl_add_u32 v27, v196, 7, v4\ns_lshl_b32 s42, s2, 3\ns_add_u32 s42, s42, 4096\ns_cbranch_vccz .La1037\ns_add_u32 m0, s26, 24576\ns_nop 0\n"
      "global_load_lds_dwordx4 v28, s[6:7] offset:0\n"
      ".La1037:\nv_lshl_add_u32 v28, v202, 7, v4\ns_add_u32 s58, s20, s42\ns_addc_u32 s59, s21, 0\ns_add_u32 m0, s53, 0\nv_and_b32 v38, 7, v240\nglobal_load_lds_dwordx4 v22, s[6:7] offset:0\n"
      "v_lshl_add_u32 v22, v178, 7, v4\nv_lshlrev_b32 v38, 11, v38\ns_xor_b32 s40, s37, 0xa5c30f96\nglobal_load_lds_dwordx4 v23, s[44:45] offset:1024\nv_lshl_add_u32 v23, v180, 7, v5\n"
      "s_mul_i32 s41, s37, 0x9E3779B1\ns_lshr_b32 s41, s41, 24\nglobal_load_lds_dwordx4 v24, s[46:47] offset:2048\nv_lshl_add_u32 v24, v186, 7, v4\nv_and_b32 v124, s55, v72\n"
      "v_xor_b32 v72, v72, v124\nglobal_load_lds_dwordx4 v25, s[48:49] offset:3072\nv_lshl_add_u32 v25, v188, 7, v5\ns_add_u32 m0, s53, 4096\nv_and_b32 v125, s55, v73\n"
      "global_load_lds_dwordx4 v26, s[6:7] offset:0\nv_lshl_add_u32 v26, v194, 7, v4\nv_xor_b32 v73, v73, v125\ns_waitcnt vmcnt(11) lgkmcnt(0)\ns_cbranch_vccnz .Lw1038\ns_waitcnt vmcnt(10)\n"
      ".Lw1038:\ns_barrier\ns_add_u32 m0, s54, 45056\ns_nop 0\nglobal_load_lds_dwordx4 v236, s[50:51] offset:0\nds_read_b64 v[76:77], v31 offset:0\n"
      "global_load_lds_dwordx4 v237, s[50:51] offset:1024\nglobal_load_lds_dwordx4 v238, s[50:51] offset:2048\nglobal_load_lds_dwordx4 v239, s[50:51] offset:3072\ns_add_u32 s50, s50, 212992\n"
      "s_addc_u32 s51, s51, 0\nv_max3_f16 v32, |v74|, |v75|, v32\ns_add_u32 m0, s26, 65536\nv_max3_f32 v33, |v74|, |v75|, v33\nglobal_load_lds_dwordx4 v27, s[6:7] offset:0\n"
      "v_lshl_add_u32 v27, v197, 7, v4\nv_lshrrev_b32 v164, 2, v240\nv_and_b32 v148, 3, v240\ns_cbranch_vccz .La1039\ns_add_u32 m0, s26, 69632\ns_nop 0\n"
      "global_load_lds_dwordx4 v28, s[6:7] offset:0\n"
      ".La1039:\nv_lshl_add_u32 v28, v203, 7, v4\nds_read2_b64 v[200:203], v9 offset0:10 offset1:11\nv_lshl_add_u32 v148, v148, 3, s65\ns_add_u32 m0, s53, 45056\nv_mul_u32_u24 v164, 928, v164\n"
      "global_load_lds_dwordx4 v22, s[6:7] offset:0\nv_lshl_add_u32 v22, v179, 7, v4\nds_read2_b64 v[176:179], v6 offset0:10 offset1:11\nv_add_u32 v149, s65, v164\nv_add_u32 v148, v148, v164\n"
      "global_load_lds_dwordx4 v23, s[44:45] offset:1024\nv_lshl_add_u32 v23, v181, 7, v5\ns_add_u32 s42, s65, 7424\nv_add_u32 v163, s42, v252\nglobal_load_lds_dwordx4 v24, s[46:47] offset:2048\n"
      "v_lshl_add_u32 v24, v187, 7, v4\nds_read2_b64 v[184:187], v7 offset0:10 offset1:11\nv_and_b32 v126, s55, v74\nv_xor_b32 v74, v74, v126\nglobal_load_lds_dwordx4 v25, s[48:49] offset:3072\n"
      "v_lshl_add_u32 v25, v189, 7, v5\ns_add_u32 m0, s53, 49152\nv_and_b32 v127, s55, v75\nglobal_load_lds_dwordx4 v26, s[6:7] offset:0\nv_lshl_add_u32 v26, v195, 7, v4\n"
      "ds_read2_b64 v[192:195], v8 offset0:10 offset1:11\nv_xor_b32 v75, v75, v127\ns_waitcnt vmcnt(11) lgkmcnt(0)\ns_cbranch_vccnz .Lw1040\ns_waitcnt vmcnt(10)\n"
      ".Lw1040:\ns_barrier\ns_add_u32 m0, s54, 90112\ns_nop 0\nglobal_load_lds_dwordx4 v236, s[50:51] offset:0\nds_read_b64 v[78:79], v30 offset:0\n"
      "global_load_lds_dwordx4 v237, s[50:51] offset:1024\nglobal_load_lds_dwordx4 v238, s[50:51] offset:2048\nglobal_load_lds_dwordx4 v239, s[50:51] offset:3072\ns_add_u32 s50, s50, 212992\n"
      "s_addc_u32 s51, s51, 0\ns_cmp_eq_u32 s27, 6\ns_cselect_b64 s[50:51], s[10:11], s[50:51]\nv_max3_f16 v32, |v76|, |v77|, v32\ns_add_u32 m0, s26, 110592\nv_max3_f32 v33, |v76|, |v77|, v33\n"
      "global_load_lds_dwordx4 v27, s[6:7] offset:0\nv_lshl_add_u32 v27, v198, 7, v4\ns_mul_i32 s42, s36, 10752\ns_add_u32 s56, s42, 0\ns_cbranch_vccz .La1041\ns_add_u32 m0, s26, 114688\ns_nop 0\n"
      "global_load_lds_dwordx4 v28, s[6:7] offset:0\n"
      ".La1041:\nv_lshl_add_u32 v28, v200, 7, v4\ns_add_u32 s42, s42, 1048576\ns_add_u32 m0, s53, 90112\ns_nop 0\nglobal_load_lds_dwordx4 v22, s[6:7] offset:0\nv_lshl_add_u32 v22, v176, 7, v4\n"
      "s_add_u32 s60, s20, s42\ns_addc_u32 s61, s21, 0\nglobal_load_lds_dwordx4 v23, s[44:45] offset:1024\nv_lshl_add_u32 v23, v182, 7, v5\nv_and_b32 v128, s55, v76\n"
      "global_load_lds_dwordx4 v24, s[46:47] offset:2048\nv_lshl_add_u32 v24, v184, 7, v4\nv_xor_b32 v76, v76, v128\nglobal_load_lds_dwordx4 v25, s[48:49] offset:3072\n"
      "v_lshl_add_u32 v25, v190, 7, v5\ns_add_u32 m0, s53, 94208\nv_and_b32 v129, s55, v77\nglobal_load_lds_dwordx4 v26, s[6:7] offset:0\nv_lshl_add_u32 v26, v192, 7, v4\nv_xor_b32 v77, v77, v129\n"
      "s_waitcnt vmcnt(11) lgkmcnt(0)\ns_cbranch_vccnz .Lw1042\ns_waitcnt vmcnt(10)\n"
      ".Lw1042:\ns_barrier\ns_add_u32 m0, s54, 0\ns_nop 0\nglobal_load_lds_dwordx4 v236, s[50:51] offset:0\nds_read_b64 v[80:81], v30 offset:45056\n"
      "global_load_lds_dwordx4 v237, s[50:51] offset:1024\nglobal_load_lds_dwordx4 v238, s[50:51] offset:2048\nglobal_load_lds_dwordx4 v239, s[50:51] offset:3072\ns_add_u32 s50, s50, 212992\n"
      "s_addc_u32 s51, s51, 0\nv_max3_f16 v32, |v78|, |v79|, v32\nv_max3_f32 v33, |v78|, |v79|, v33\ns_add_u32 s42, s27, 0\ns_add_u32 m0, s26, 20480\ns_nop 0\n"
      "global_load_lds_dwordx4 v27, s[6:7] offset:0\nv_lshl_add_u32 v27, v199, 7, v4\nds_read2_b64 v[196:199], v8 offset0:123 offset1:124\ns_sub_u32 s43, s42, 26\ns_min_u32 s42, s42, s43\n"
      "v_lshl_add_u32 v150, s42, 5, v148\ns_cbranch_vccz .La1043\ns_add_u32 m0, s26, 24576\ns_nop 0\nglobal_load_lds_dwordx4 v28, s[6:7] offset:0\n"
      ".La1043:\nv_lshl_add_u32 v28, v201, 7, v4\ns_add_u32 s42, s27, 2\ns_sub_u32 s43, s42, 26\ns_add_u32 m0, s53, 0\ns_nop 0\nglobal_load_lds_dwordx4 v22, s[6:7] offset:0\n"
      "v_lshl_add_u32 v22, v177, 7, v4\ns_min_u32 s42, s42, s43\nv_lshl_add_u32 v151, s42, 5, v148\nglobal_load_lds_dwordx4 v23, s[44:45] offset:1024\nv_lshl_add_u32 v23, v183, 7, v5\n"
      "ds_read2_b64 v[180:183], v6 offset0:123 offset1:124\ns_add_u32 s42, s27, 4\ns_sub_u32 s43, s42, 26\nglobal_load_lds_dwordx4 v24, s[46:47] offset:2048\nv_lshl_add_u32 v24, v185, 7, v4\n"
      "s_min_u32 s42, s42, s43\nv_lshl_add_u32 v152, s42, 5, v148\nglobal_load_lds_dwordx4 v25, s[48:49] offset:3072\nv_lshl_add_u32 v25, v191, 7, v5\n"
      "ds_read2_b64 v[188:191], v7 offset0:123 offset1:124\nv_and_b32 v130, s55, v78\ns_add_u32 m0, s53, 4096\nv_xor_b32 v78, v78, v130\nglobal_load_lds_dwordx4 v26, s[6:7] offset:0\n"
      "v_lshl_add_u32 v26, v193, 7, v4\nv_and_b32 v131, s55, v79\nv_xor_b32 v79, v79, v131\ns_waitcnt vmcnt(11) lgkmcnt(0)\ns_cbranch_vccnz .Lw1044\ns_waitcnt vmcnt(10)\n"
      ".Lw1044:\ns_barrier\ns_add_u32 m0, s54, 45056\ns_nop 0\nglobal_load_lds_dwordx4 v236, s[50:51] offset:0\nds_read_b64 v[82:83], v31 offset:0\n"
      "global_load_lds_dwordx4 v237, s[50:51] offset:1024\nglobal_load_lds_dwordx4 v238, s[50:51] offset:2048\nglobal_load_lds_dwordx4 v239, s[50:51] offset:3072\ns_add_u32 s50, s50, 212992\n"
      "s_addc_u32 s51, s51, 0\ns_cmp_eq_u32 s27, 4\ns_cselect_b64 s[50:51], s[10:11], s[50:51]\nv_max3_f16 v32, |v80|, |v81|, v32\nv_max3_f32 v33, |v80|, |v81|, v33\ns_add_u32 s42, s27, 6\n"
      "s_add_u32 m0, s26, 65536\ns_nop 0\nglobal_load_lds_dwordx4 v27, s[6:7] offset:0\nv_lshl_add_u32 v27, v196, 7, v4\ns_sub_u32 s43, s42, 26\ns_min_u32 s42, s42, s43\n"
      "v_lshl_add_u32 v153, s42, 5, v148\ns_cbranch_vccz .La1045\ns_add_u32 m0, s26, 69632\ns_nop 0\nglobal_load_lds_dwordx4 v28, s[6:7] offset:0\n"
      ".La1045:\nv_lshl_add_u32 v28, v202, 7, v4\ns_add_u32 s42, s27, 8\ns_sub_u32 s43, s42, 26\ns_add_u32 m0, s53, 45056\ns_nop 0\nglobal_load_lds_dwordx4 v22, s[6:7] offset:0\n"
      "v_lshl_add_u32 v22, v178, 7, v4\ns_min_u32 s42, s42, s43\nv_lshl_add_u32 v154, s42, 5, v148\nglobal_load_lds_dwordx4 v23, s[44:45] offset:1024\nv_lshl_add_u32 v23, v180, 7, v5\n"
      "s_add_u32 s42, s27, 10\ns_sub_u32 s43, s42, 26\nglobal_load_lds_dwordx4 v24, s[46:47] offset:2048\nv_lshl_add_u32 v24, v186, 7, v4\ns_min_u32 s42, s42, s43\n"
      "v_lshl_add_u32 v155, s42, 5, v148\nglobal_load_lds_dwordx4 v25, s[48:49] offset:3072\nv_lshl_add_u32 v25, v188, 7, v5\nv_and_b32 v132, s55, v80\ns_add_u32 m0, s53, 49152\n"
      "v_xor_b32 v80, v80, v132\nglobal_load_lds_dwordx4 v26, s[6:7] offset:0\nv_lshl_add_u32 v26, v194, 7, v4\nv_and_b32 v133, s55, v81\nv_xor_b32 v81, v81, v133\ns_cmp_lg_u32 s24, 3\n"
      "s_cbranch_scc1 .Lo1046\nglobal_load_dwordx2 v[36:37], v38, s[58:59] sc1\n"
      ".Lo1046:\ns_waitcnt vmcnt(11) lgkmcnt(0)\ns_cbranch_vccnz .Lw1047\ns_waitcnt vmcnt(10)\n"
      ".Lw1047:\ns_barrier\ns_add_u32 m0, s54, 90112\ns_nop 0\nglobal_load_lds_dwordx4 v236, s[50:51] offset:0\nds_read_b64 v[84:85], v30 offset:0\n"
      "global_load_lds_dwordx4 v237, s[50:51] offset:1024\nglobal_load_lds_dwordx4 v238, s[50:51] offset:2048\nglobal_load_lds_dwordx4 v239, s[50:51] offset:3072\ns_add_u32 s50, s50, 212992\n"
      "s_addc_u32 s51, s51, 0\nv_max3_f16 v32, |v82|, |v83|, v32\nv_max3_f32 v33, |v82|, |v83|, v33\ns_add_u32 s42, s27, 12\ns_add_u32 m0, s26, 110592\ns_nop 0\n"
      "global_load_lds_dwordx4 v27, s[6:7] offset:0\nv_lshl_add_u32 v27, v197, 7, v4\ns_sub_u32 s43, s42, 26\ns_min_u32 s42, s42, s43\nv_lshl_add_u32 v156, s42, 5, v148\ns_cbranch_vccz .La1048\n"
      "s_add_u32 m0, s26, 114688\ns_nop 0\nglobal_load_lds_dwordx4 v28, s[6:7] offset:0\n"
      ".La1048:\nv_lshl_add_u32 v28, v203, 7, v4\nds_read2_b64 v[200:203], v9 offset0:12 offset1:13\ns_add_u32 s42, s27, 14\ns_sub_u32 s43, s42, 26\ns_add_u32 m0, s53, 90112\ns_nop 0\n"
      "global_load_lds_dwordx4 v22, s[6:7] offset:0\nv_lshl_add_u32 v22, v179, 7, v4\nds_read2_b64 v[176:179], v6 offset0:12 offset1:13\ns_min_u32 s42, s42, s43\nv_lshl_add_u32 v157, s42, 5, v148\n"
      "global_load_lds_dwordx4 v23, s[44:45] offset:1024\nv_lshl_add_u32 v23, v181, 7, v5\ns_add_u32 s42, s27, 16\ns_sub_u32 s43, s42, 26\nglobal_load_lds_dwordx4 v24, s[46:47] offset:2048\n"
      "v_lshl_add_u32 v24, v187, 7, v4\nds_read2_b64 v[184:187], v7 offset0:12 offset1:13\ns_min_u32 s42, s42, s43\nv_lshl_add_u32 v158, s42, 5, v148\n"
      "global_load_lds_dwordx4 v25, s[48:49] offset:3072\nv_lshl_add_u32 v25, v189, 7, v5\nv_and_b32 v134, s55, v82\ns_add_u32 m0, s53, 94208\nv_xor_b32 v82, v82, v134\n"
      "global_load_lds_dwordx4 v26, s[6:7] offset:0\nv_lshl_add_u32 v26, v195, 7, v4\nds_read2_b64 v[192:195], v8 offset0:12 offset1:13\nv_and_b32 v135, s55, v83\nv_xor_b32 v83, v83, v135\n"
      "s_waitcnt vmcnt(11) lgkmcnt(0)\ns_cbranch_vccnz .Lw1049\ns_waitcnt vmcnt(10)\n"
      ".Lw1049:\ns_barrier\ns_add_u32 m0, s54, 0\ns_nop 0\nglobal_load_lds_dwordx4 v236, s[50:51] offset:0\nds_read_b64 v[86:87], v30 offset:45056\n"
      "global_load_lds_dwordx4 v237, s[50:51] offset:1024\nglobal_load_lds_dwordx4 v238, s[50:51] offset:2048\nglobal_load_lds_dwordx4 v239, s[50:51] offset:3072\ns_add_u32 s50, s50, 212992\n"
      "s_addc_u32 s51, s51, 0\ns_cmp_eq_u32 s27, 2\ns_cselect_b64 s[50:51], s[10:11], s[50:51]\nv_max3_f16 v32, |v84|, |v85|, v32\nv_max3_f32 v33, |v84|, |v85|, v33\ns_add_u32 s42, s27, 18\n"
      "s_add_u32 m0, s26, 20480\ns_nop 0\nglobal_load_lds_dwordx4 v27, s[6:7] offset:0\nv_lshl_add_u32 v27, v198, 7, v4\ns_sub_u32 s43, s42, 26\ns_min_u32 s42, s42, s43\n"
      "v_lshl_add_u32 v159, s42, 5, v148\ns_cbranch_vccz .La1050\ns_add_u32 m0, s26, 24576\ns_nop 0\nglobal_load_lds_dwordx4 v28, s[6:7] offset:0\n"
      ".La1050:\nv_lshl_add_u32 v28, v200, 7, v4\ns_add_u32 s42, s27, 20\ns_sub_u32 s43, s42, 26\ns_add_u32 m0, s53, 0\ns_nop 0\nglobal_load_lds_dwordx4 v22, s[6:7] offset:0\n"
      "v_lshl_add_u32 v22, v176, 7, v4\ns_min_u32 s42, s42, s43\nv_lshl_add_u32 v160, s42, 5, v148\nglobal_load_lds_dwordx4 v23, s[44:45] offset:1024\nv_lshl_add_u32 v23, v182, 7, v5\n"
      "s_add_u32 s42, s27, 22\ns_sub_u32 s43, s42, 26\nglobal_load_lds_dwordx4 v24, s[46:47] offset:2048\nv_lshl_add_u32 v24, v184, 7, v4\ns_min_u32 s42, s42, s43\n"
      "v_lshl_add_u32 v161, s42, 5, v148\nglobal_load_lds_dwordx4 v25, s[48:49] offset:3072\nv_lshl_add_u32 v25, v190, 7, v5\nv_and_b32 v136, s55, v84\ns_add_u32 m0, s53, 4096\n"
      "v_xor_b32 v84, v84, v136\nglobal_load_lds_dwordx4 v26, s[6:7] offset:0\nv_lshl_add_u32 v26, v192, 7, v4\nv_and_b32 v137, s55, v85\nv_xor_b32 v85, v85, v137\ns_waitcnt vmcnt(11) lgkmcnt(0)\n"
      "s_cbranch_vccnz .Lw1051\ns_waitcnt vmcnt(10)\n"
      ".Lw1051:\ns_barrier\ns_add_u32 m0, s54, 45056\ns_nop 0\nglobal_load_lds_dwordx4 v236, s[50:51] offset:0\nds_read_b64 v[88:89], v31 offset:0\n"
      "global_load_lds_dwordx4 v237, s[50:51] offset:1024\nglobal_load_lds_dwordx4 v238, s[50:51] offset:2048\nglobal_load_lds_dwordx4 v239, s[50:51] offset:3072\ns_add_u32 s50, s50, 212992\n"
      "s_addc_u32 s51, s51, 0\nv_max3_f16 v32, |v86|, |v87|, v32\ns_add_u32 m0, s26, 65536\nv_max3_f32 v33, |v86|, |v87|, v33\nglobal_load_lds_dwordx4 v27, s[6:7] offset:0\n"
      "v_lshl_add_u32 v27, v199, 7, v4\ns_add_u32 s42, s27, 24\ns_sub_u32 s43, s42, 26\ns_cbranch_vccz .La1052\ns_add_u32 m0, s26, 69632\ns_nop 0\nglobal_load_lds_dwordx4 v28, s[6:7] offset:0\n"
      ".La1052:\nv_lshl_add_u32 v28, v201, 7, v4\ns_min_u32 s42, s42, s43\ns_add_u32 m0, s53, 45056\ns_nop 0\nglobal_load_lds_dwordx4 v22, s[6:7] offset:0\nv_lshl_add_u32 v22, v177, 7, v4\n"
      "v_lshl_add_u32 v162, s42, 5, v148\nglobal_load_lds_dwordx4 v23, s[44:45] offset:1024\nv_lshl_add_u32 v23, v183, 7, v5\nv_and_b32 v138, s55, v86\n"
      "global_load_lds_dwordx4 v24, s[46:47] offset:2048\nv_lshl_add_u32 v24, v185, 7, v4\nv_xor_b32 v86, v86, v138\nglobal_load_lds_dwordx4 v25, s[48:49] offset:3072\n"
      "v_lshl_add_u32 v25, v191, 7, v5\ns_add_u32 m0, s53, 49152\nv_and_b32 v139, s55, v87\nglobal_load_lds_dwordx4 v26, s[6:7] offset:0\nv_lshl_add_u32 v26, v193, 7, v4\nv_xor_b32 v87, v87, v139\n"
      "s_cmp_lg_u32 s24, 3\ns_cbranch_scc1 .Lo1055\ns_mov_b32 s42, 0\n"
      ".Lq1053:\nv_and_b32 v164, 0xff, v36\nv_xor_b32 v164, s41, v164\nv_xor_b32 v165, s40, v37\nv_or_b32 v164, v164, v165\nv_cmp_ne_u32 s[62:63], 0, v164\nv_lshrrev_b32 v164, 8, v36\n"
      "s_cmp_eq_u64 s[62:63], 0\ns_cbranch_scc1 .Lr1054\ns_sleep 1\nglobal_load_dwordx2 v[36:37], v38, s[58:59] sc1\ns_add_u32 s42, s42, 1\ns_bitcmp0_b32 s42, 22\ns_waitcnt vmcnt(0)\n"
      "s_cbranch_scc1 .Lq1053\n"
      ".Lr1054:\nds_write_b32 v253, v164 offset:24\n"
      ".Lo1055:\ns_waitcnt vmcnt(11) lgkmcnt(0)\ns_cbranch_vccnz .Lw1056\ns_waitcnt vmcnt(10)\n"
      ".Lw1056:\ns_barrier\ns_add_u32 m0, s54, 90112\ns_nop 0\nglobal_load_lds_dwordx4 v236, s[50:51] offset:0\nds_read_b64 v[90:91], v30 offset:0\n"
      "global_load_lds_dwordx4 v237, s[50:51] offset:1024\nglobal_load_lds_dwordx4 v238, s[50:51] offset:2048\nglobal_load_lds_dwordx4 v239, s[50:51] offset:3072\n"
      "v_max3_f16 v32, |v88|, |v89|, v32\nv_max3_f32 v33, |v88|, |v89|, v33\nv_and_b32 v140, s55, v88\ns_add_u32 m0, s26, 110592\nv_xor_b32 v88, v88, v140\n"
      "global_load_lds_dwordx4 v27, s[6:7] offset:0\nv_and_b32 v141, s55, v89\nv_xor_b32 v89, v89, v141\ns_lshl_b32 s42, s24, 6\nv_add_u32 v10, s42, v240\ns_cbranch_vccz .La1057\n"
      "s_add_u32 m0, s26, 114688\ns_nop 0\nglobal_load_lds_dwordx4 v28, s[6:7] offset:0\n"
      ".La1057:\nv_min_u32 v10, 83, v10\nv_and_b32 v11, 3, v10\nv_lshrrev_b32 v12, 2, v10\ns_add_u32 m0, s53, 90112\nv_mul_u32_u24 v11, 22272, v11\nglobal_load_lds_dwordx4 v22, s[6:7] offset:0\n"
      "v_mul_u32_u24 v13, 928, v12\ns_mov_b32 s42, 45920\nv_add3_u32 v11, v11, v13, s42\nglobal_load_lds_dwordx4 v23, s[44:45] offset:1024\ns_mov_b32 s42, 160256\nv_lshl_add_u32 v13, v12, 3, s42\n"
      "s_mov_b32 s42, 160448\nglobal_load_lds_dwordx4 v24, s[46:47] offset:2048\nv_lshl_add_u32 v10, v10, 4, s42\nv_min_u32 v14, 1, v12\nv_lshl_add_u32 v10, v14, 6, v10\n"
      "global_load_lds_dwordx4 v25, s[48:49] offset:3072\ns_bfm_b32 s42, s64, 0\nv_lshrrev_b32 v15, v12, s42\ns_add_u32 m0, s53, 94208\nv_subrev_u32 v14, s64, v12\n"
      "global_load_lds_dwordx4 v26, s[6:7] offset:0\nv_lshl_add_u32 v12, v240, 1, v248\nds_read_u16 v251, v12 offset:64\nds_read_b32 v9, v248 offset:24\ns_waitcnt vmcnt(11) lgkmcnt(0)\n"
      "s_cbranch_vccnz .Lw1058\ns_waitcnt vmcnt(10)\n"
      ".Lw1058:\ns_barrier\ns_add_u32 m0, s56, 0\ns_nop 0\nglobal_load_lds_dwordx4 v252, s[60:61] offset:0 sc1\nds_read_b64 v[92:93], v30 offset:45056\n"
      "global_load_lds_dwordx4 v252, s[60:61] offset:1024 sc1\nglobal_load_lds_dwordx4 v252, s[60:61] offset:2048 sc1\nglobal_load_lds_dwordx4 v252, s[60:61] offset:3072 sc1\n"
      "s_add_u32 s60, s60, 4096\ns_addc_u32 s61, s61, 0\nv_max3_f16 v32, |v90|, |v91|, v32\nv_max3_f32 v33, |v90|, |v91|, v33\nv_and_b32 v142, s55, v90\ns_add_u32 m0, s56, 4096\n"
      "v_xor_b32 v90, v90, v142\nglobal_load_lds_dwordx4 v252, s[60:61] offset:0 sc1\nv_and_b32 v143, s55, v91\nv_xor_b32 v91, v91, v143\nv_readfirstlane_b32 s39, v9\nv_readlane_b32 s57, v251, 0\n"
      "global_load_lds_dwordx4 v252, s[60:61] offset:1024 sc1\ns_xor_b32 s40, s39, 0xa5c30f96\ns_mul_i32 s41, s39, 0x9E3779B1\ns_lshr_b32 s41, s41, 24\ns_lshl_b32 s42, s2, 3\n"
      "global_load_lds_dwordx4 v252, s[60:61] offset:2048 sc1\ns_add_u32 s42, s42, 24576\nv_mov_b32 v7, s42\nv_mov_b32 v8, s41\nv_mov_b32 v9, s40\n"
      "global_load_lds_dwordx4 v252, s[60:61] offset:3072 sc1\ns_add_u32 s60, s60, 4096\ns_addc_u32 s61, s61, 0\ns_lshl_b32 s42, s2, 2\nv_mov_b32 v12, s42\ns_add_u32 s42, s37, 1\n"
      "s_add_u32 m0, s56, 8192\ns_nop 0\nglobal_load_lds_dwordx4 v252, s[60:61] offset:0 sc1\nv_mov_b32 v6, s42\nv_and_b32 v1, 48, v240\ns_mov_b32 s42, 41984\n"
      "global_load_lds_dwordx4 v252, s[60:61] offset:1024 sc1\nv_lshl_add_u32 v1, v1, 2, s42\ns_sub_u32 s49, s65, 928\n"
      "v_add_u32_sdwa v236, s49, v244 dst_sel:DWORD dst_unused:UNUSED_PAD src0_sel:DWORD src1_sel:WORD_0\nglobal_load_lds_dwordx4 v252, s[60:61] offset:1536 sc1\n"
      "v_add_u32_sdwa v237, s49, v244 dst_sel:DWORD dst_unused:UNUSED_PAD src0_sel:DWORD src1_sel:WORD_1\n"
      "v_add_u32_sdwa v238, s49, v245 dst_sel:DWORD dst_unused:UNUSED_PAD src0_sel:DWORD src1_sel:WORD_0\n"
      "v_add_u32_sdwa v239, s49, v245 dst_sel:DWORD dst_unused:UNUSED_PAD src0_sel:DWORD src1_sel:WORD_1\ns_waitcnt vmcnt(11) lgkmcnt(0)\ns_barrier\ns_setprio 3\n"
      "ds_read_b64 v[94:95], v31 offset:0\nv_max3_f16 v32, |v92|, |v93|, v32\nv_max3_f32 v33, |v92|, |v93|, v33\nv_and_b32 v144, s55, v92\nv_xor_b32 v92, v92, v144\nv_and_b32 v145, s55, v93\n"
      "v_xor_b32 v93, v93, v145\ns_lshl_b32 s42, s30, 6\ns_add_u32 s42, s42, s36\ns_add_u32 s42, s42, 60\ns_mulk_i32 s42, 896\ns_add_u32 s44, s16, s42\ns_addc_u32 s45, s17, 0\n"
      "s_waitcnt lgkmcnt(0)\nv_max3_f16 v32, |v94|, |v95|, v32\nv_max3_f32 v33, |v94|, |v95|, v33\nv_and_b32 v146, s55, v94\nv_xor_b32 v94, v94, v146\nv_and_b32 v147, s55, v95\n"
      "v_xor_b32 v95, v95, v147\nv_lshlrev_b32 v32, 16, v32\nv_max_u32 v33, v32, v33\ns_nop 1\nv_max_u32_dpp v33, v33, v33 quad_perm:[1,0,3,2] row_mask:0xf bank_mask:0xf\ns_nop 1\n"
      "v_max_u32_dpp v33, v33, v33 quad_perm:[2,3,0,1] row_mask:0xf bank_mask:0xf\nv_lshrrev_b32 v33, 23, v33\nv_subrev_u32 v32, 22, v33\nv_mul_u32_u24 v32, 0x800080, v32\n"
      "v_pk_sub_u16 v44, v44, v32 clamp\nv_pk_sub_u16 v45, v45, v32 clamp\nv_pk_sub_u16 v46, v46, v32 clamp\nv_pk_sub_u16 v47, v47, v32 clamp\nv_lshl_or_b32 v44, v44, 3, v96\n"
      "v_lshl_or_b32 v45, v45, 3, v97\nv_lshl_or_b32 v46, v46, 3, v98\nv_lshl_or_b32 v47, v47, 3, v99\nv_pk_sub_u16 v48, v48, v32 clamp\nv_pk_sub_u16 v49, v49, v32 clamp\n"
      "v_pk_sub_u16 v50, v50, v32 clamp\nv_pk_sub_u16 v51, v51, v32 clamp\nv_lshl_or_b32 v48, v48, 3, v100\nv_lshl_or_b32 v49, v49, 3, v101\nv_lshl_or_b32 v50, v50, 3, v102\n"
      "v_lshl_or_b32 v51, v51, 3, v103\nv_pk_sub_u16 v52, v52, v32 clamp\nv_pk_sub_u16 v53, v53, v32 clamp\nv_pk_sub_u16 v54, v54, v32 clamp\nv_pk_sub_u16 v55, v55, v32 clamp\n"
      "v_lshl_or_b32 v52, v52, 3, v104\nv_lshl_or_b32 v53, v53, 3, v105\nv_lshl_or_b32 v54, v54, 3, v106\nv_lshl_or_b32 v55, v55, 3, v107\nv_pk_sub_u16 v56, v56, v32 clamp\n"
      "v_pk_sub_u16 v57, v57, v32 clamp\nv_pk_sub_u16 v58, v58, v32 clamp\nv_pk_sub_u16 v59, v59, v32 clamp\nv_lshl_or_b32 v56, v56, 3, v108\nv_lshl_or_b32 v57, v57, 3, v109\n"
      "v_lshl_or_b32 v58, v58, 3, v110\nv_lshl_or_b32 v59, v59, 3, v111\nv_pk_sub_u16 v60, v60, v32 clamp\nv_pk_sub_u16 v61, v61, v32 clamp\nv_pk_sub_u16 v62, v62, v32 clamp\n"
      "v_pk_sub_u16 v63, v63, v32 clamp\nv_lshl_or_b32 v60, v60, 3, v112\nv_lshl_or_b32 v61, v61, 3, v113\nv_lshl_or_b32 v62, v62, 3, v114\nv_lshl_or_b32 v63, v63, 3, v115\n"
      "v_pk_sub_u16 v64, v64, v32 clamp\nv_pk_sub_u16 v65, v65, v32 clamp\nv_pk_sub_u16 v66, v66, v32 clamp\nv_pk_sub_u16 v67, v67, v32 clamp\nv_lshl_or_b32 v64, v64, 3, v116\n"
      "v_lshl_or_b32 v65, v65, 3, v117\nv_lshl_or_b32 v66, v66, 3, v118\nv_lshl_or_b32 v67, v67, 3, v119\ns_waitcnt vmcnt(0)\ns_barrier\nv_pk_sub_u16 v68, v68, v32 clamp\n"
      "v_pk_sub_u16 v69, v69, v32 clamp\nv_pk_sub_u16 v70, v70, v32 clamp\nv_pk_sub_u16 v71, v71, v32 clamp\nv_lshl_or_b32 v68, v68, 3, v120\nv_lshl_or_b32 v69, v69, 3, v121\n"
      "v_lshl_or_b32 v70, v70, 3, v122\nv_lshl_or_b32 v71, v71, 3, v123\nv_pk_sub_u16 v72, v72, v32 clamp\nv_pk_sub_u16 v73, v73, v32 clamp\nv_pk_sub_u16 v74, v74, v32 clamp\n"
      "v_pk_sub_u16 v75, v75, v32 clamp\nv_lshl_or_b32 v72, v72, 3, v124\nv_lshl_or_b32 v73, v73, 3, v125\nv_lshl_or_b32 v74, v74, 3, v126\nv_lshl_or_b32 v75, v75, 3, v127\n"
      "v_pk_sub_u16 v76, v76, v32 clamp\nv_pk_sub_u16 v77, v77, v32 clamp\nv_pk_sub_u16 v78, v78, v32 clamp\nv_pk_sub_u16 v79, v79, v32 clamp\nv_lshl_or_b32 v76, v76, 3, v128\n"
      "v_lshl_or_b32 v77, v77, 3, v129\nv_lshl_or_b32 v78, v78, 3, v130\nv_lshl_or_b32 v79, v79, 3, v131\nv_pk_sub_u16 v80, v80, v32 clamp\nv_pk_sub_u16 v81, v81, v32 clamp\n"
      "v_pk_sub_u16 v82, v82, v32 clamp\nv_pk_sub_u16 v83, v83, v32 clamp\nv_lshl_or_b32 v80, v80, 3, v132\nv_lshl_or_b32 v81, v81, 3, v133\nv_lshl_or_b32 v82, v82, 3, v134\n"
      "v_lshl_or_b32 v83, v83, 3, v135\nv_pk_sub_u16 v84, v84, v32 clamp\nv_pk_sub_u16 v85, v85, v32 clamp\nv_pk_sub_u16 v86, v86, v32 clamp\nv_pk_sub_u16 v87, v87, v32 clamp\n"
      "v_lshl_or_b32 v84, v84, 3, v136\nv_lshl_or_b32 v85, v85, 3, v137\nv_lshl_or_b32 v86, v86, 3, v138\nv_lshl_or_b32 v87, v87, 3, v139\nv_pk_sub_u16 v88, v88, v32 clamp\n"
      "v_pk_sub_u16 v89, v89, v32 clamp\nv_pk_sub_u16 v90, v90, v32 clamp\nv_pk_sub_u16 v91, v91, v32 clamp\nv_lshl_or_b32 v88, v88, 3, v140\nv_lshl_or_b32 v89, v89, 3, v141\n"
      "v_lshl_or_b32 v90, v90, 3, v142\nv_lshl_or_b32 v91, v91, 3, v143\nv_pk_sub_u16 v92, v92, v32 clamp\nv_pk_sub_u16 v93, v93, v32 clamp\nv_pk_sub_u16 v94, v94, v32 clamp\n"
      "v_pk_sub_u16 v95, v95, v32 clamp\nv_lshl_or_b32 v92, v92, 3, v144\nv_lshl_or_b32 v93, v93, 3, v145\nv_lshl_or_b32 v94, v94, 3, v146\nv_lshl_or_b32 v95, v95, 3, v147\nv_sub_u32 v4, 257, v33\n"
      "v_lshlrev_b32 v4, 23, v4\nv_cvt_pkrtz_f16_f32 v4, v4, v4\nv_lshlrev_b32 v33, 2, v33\nv_mov_b32 v5, v4\nv_subrev_u32 v33, 536, v33\ns_setprio 0\nds_write_b64 v148, v[4:5] offset:832\n"
      "ds_write_b32 v149, v33 offset:864\nds_write2_b64 v150, v[44:45], v[46:47] offset1:4\nds_write2_b64 v151, v[48:49], v[50:51] offset1:4\nds_write2_b64 v152, v[52:53], v[54:55] offset1:4\n"
      "ds_write2_b64 v153, v[56:57], v[58:59] offset1:4\nds_write2_b64 v154, v[60:61], v[62:63] offset1:4\nds_write2_b64 v155, v[64:65], v[66:67] offset1:4\n"
      "ds_write2_b64 v156, v[68:69], v[70:71] offset1:4\nds_write2_b64 v157, v[72:73], v[74:75] offset1:4\nds_write2_b64 v158, v[76:77], v[78:79] offset1:4\n"
      "ds_write2_b64 v159, v[80:81], v[82:83] offset1:4\nds_write2_b64 v160, v[84:85], v[86:87] offset1:4\nds_write2_b64 v161, v[88:89], v[90:91] offset1:4\n"
      "ds_write2_b64 v162, v[92:93], v[94:95] offset1:4\nds_read_b128 v[16:19], v163 offset:6496\nds_read_b128 v[20:23], v163 offset:5568\nds_read_b128 v[24:27], v163 offset:4640\n"
      "s_cmp_gt_u32 s64, 12\ns_cbranch_scc1 .Ld1060\nds_read_b128 v[28:31], v163 offset:3712\nds_read_b128 v[32:35], v163 offset:2784\nds_read_b128 v[36:39], v163 offset:1856\n"
      "ds_read_b128 v[40:43], v163 offset:928\nds_read_b128 v[44:47], v163 offset:0\n"
      ".Ld1060:\ns_mov_b32 exec_hi, 0x7fffff\ns_waitcnt lgkmcnt(0)\ns_cmp_gt_u32 s64, 15\ns_cbranch_scc1 .Ld1061\nglobal_store_dwordx4 v252, v[16:19], s[44:45] sc1\ns_sub_u32 s44, s44, 3584\n"
      "s_subb_u32 s45, s45, 0\ns_cmp_gt_u32 s64, 14\ns_cbranch_scc1 .Ld1061\nglobal_store_dwordx4 v252, v[20:23], s[44:45] sc1\ns_sub_u32 s44, s44, 3584\ns_subb_u32 s45, s45, 0\n"
      "s_cmp_gt_u32 s64, 13\ns_cbranch_scc1 .Ld1061\nglobal_store_dwordx4 v252, v[24:27], s[44:45] sc1\ns_sub_u32 s44, s44, 3584\ns_subb_u32 s45, s45, 0\ns_cmp_gt_u32 s64, 12\n"
      "s_cbranch_scc1 .Ld1061\nglobal_store_dwordx4 v252, v[28:31], s[44:45] sc1\ns_sub_u32 s44, s44, 3584\ns_subb_u32 s45, s45, 0\ns_cmp_gt_u32 s64, 11\ns_cbranch_scc1 .Ld1061\n"
      "global_store_dwordx4 v252, v[32:35], s[44:45] sc1\ns_sub_u32 s44, s44, 3584\ns_subb_u32 s45, s45, 0\ns_cmp_gt_u32 s64, 10\ns_cbranch_scc1 .Ld1061\n"
      "global_store_dwordx4 v252, v[36:39], s[44:45] sc1\ns_sub_u32 s44, s44, 3584\ns_subb_u32 s45, s45, 0\ns_cmp_gt_u32 s64, 9\ns_cbranch_scc1 .Ld1061\n"
      "global_store_dwordx4 v252, v[40:43], s[44:45] sc1\ns_sub_u32 s44, s44, 3584\ns_subb_u32 s45, s45, 0\ns_cmp_gt_u32 s64, 8\ns_cbranch_scc1 .Ld1061\n"
      "global_store_dwordx4 v252, v[44:47], s[44:45] sc1\ns_sub_u32 s44, s44, 3584\ns_subb_u32 s45, s45, 0\n"
      ".Ld1061:\ns_mov_b32 exec_hi, -1\nds_read_b128 v[48:51], v1 offset:0\nds_read_b128 v[52:55], v1 offset:16\nds_read_b128 v[56:59], v1 offset:32\nds_read_b128 v[60:63], v1 offset:48\n"
      "ds_read_b128 v[72:75], v252 offset:0\nds_read_b128 v[76:79], v252 offset:1024\nds_read_b128 v[80:83], v252 offset:2048\nds_read_b128 v[84:87], v252 offset:3072\n"
      "ds_read_b128 v[88:91], v252 offset:4096\nds_read_b128 v[92:95], v252 offset:5120\nds_read_b128 v[96:99], v252 offset:6144\nds_read_b128 v[100:103], v252 offset:7168\n"
      "ds_read_b128 v[104:107], v252 offset:8192\nds_read_b128 v[108:111], v252 offset:9216\nds_read_b128 v[112:115], v252 offset:10240\nds_read_b128 v[116:119], v252 offset:11264\n"
      "ds_read_b128 v[120:123], v252 offset:12288\nds_read_b128 v[124:127], v252 offset:13312\nds_read_b128 v[128:131], v252 offset:14336\nds_read_b128 v[132:135], v252 offset:15360\n"
      "ds_read_b128 v[136:139], v252 offset:16384\nds_read_b128 v[140:143], v252 offset:17408\nds_read_b128 v[144:147], v252 offset:18432\nds_read_b128 v[148:151], v252 offset:19456\n"
      "ds_read_b128 v[152:155], v252 offset:20480\nds_read_b128 v[156:159], v252 offset:21504\nds_read_b128 v[160:163], v252 offset:22528\nds_read_b128 v[164:167], v252 offset:23552\n"
      "ds_read_b128 v[168:171], v252 offset:24576\nds_read_b128 v[172:175], v252 offset:25600\nds_read_b128 v[176:179], v252 offset:26624\nds_read_b128 v[180:183], v252 offset:27648\n"
      "ds_read_b128 v[184:187], v252 offset:28672\nds_read_b128 v[188:191], v252 offset:29696\nds_read_b128 v[192:195], v252 offset:30720\nds_read_b128 v[196:199], v252 offset:31744\n"
      "ds_read_b128 v[200:203], v252 offset:32768\nds_read_b128 v[204:207], v252 offset:33792\nds_read_b128 v[208:211], v252 offset:34816\nds_read_b128 v[212:215], v252 offset:35840\n"
      "ds_read_b128 v[216:219], v252 offset:36864\nds_read_b128 v[220:223], v252 offset:37888\nds_read_b128 v[224:227], v252 offset:38912\nds_read_b128 v[228:231], v252 offset:39936\n"
      "ds_read_b128 v[232:235], v252 offset:40960\ns_mov_b32 s55, s57\ns_mov_b32 s45, -1\ns_mov_b32 s46, 0\ns_setprio 3\ns_waitcnt lgkmcnt(15)\nv_mul_f32 v48, v48, v56\nv_mul_f32 v49, v49, v57\n"
      "v_mul_f32 v50, v50, v58\nv_mul_f32 v51, v51, v59\nv_mul_f32 v52, v52, v60\nv_mul_f32 v53, v53, v61\nv_mul_f32 v54, v54, v62\nds_write_b128 v1, v[48:51] offset:256\n"
      "ds_write_b128 v1, v[52:55] offset:272\nds_read_b64_tr_b16 v[68:69], v238 offset:928\nds_read_b64_tr_b16 v[70:71], v239 offset:928\nds_read_b64_tr_b16 v[56:57], v236 offset:928\n"
      "ds_read_b64_tr_b16 v[58:59], v237 offset:1056\nds_read_b64_tr_b16 v[60:61], v237 offset:1184\nds_read_b64_tr_b16 v[62:63], v237 offset:1312\nds_read_b64_tr_b16 v[64:65], v237 offset:1440\n"
      "ds_read_b64_tr_b16 v[66:67], v237 offset:1568\ns_waitcnt vmcnt(0)\nds_read_b128 v[44:47], v248 offset:48\nds_add_rtn_u32 v0, v253, v241 offset:32\ns_waitcnt lgkmcnt(0)\n"
      "v_readfirstlane_b32 s42, v0\ns_cmp_lg_u32 s42, 3\ns_cbranch_scc1 .Lf1062\ns_mov_b64 exec, 1\nglobal_store_dwordx2 v7, v[8:9], s[20:21] sc1\nglobal_store_dword v12, v6, s[20:21] sc1\n"
      "s_mov_b64 exec, -1\n"
      ".Lf1062:\ns_branch .Lcin\n"
      ".Lg14:\ns_waitcnt vmcnt(4)\ns_barrier\nds_read_b64_tr_b16 v[64:65], v104 offset:45056\nds_read_b64_tr_b16 v[66:67], v104 offset:46080\nds_read_b64_tr_b16 v[68:69], v104 offset:53248\n"
      "ds_read_b64_tr_b16 v[70:71], v104 offset:54272\nds_read_b64_tr_b16 v[72:73], v105 offset:45056\nds_read_b64_tr_b16 v[74:75], v105 offset:46080\n"
      "ds_read_b64_tr_b16 v[76:77], v105 offset:53248\nds_read_b64_tr_b16 v[78:79], v105 offset:54272\nds_read_b64_tr_b16 v[80:81], v106 offset:45056\n"
      "ds_read_b64_tr_b16 v[82:83], v106 offset:46080\nds_read_b64_tr_b16 v[84:85], v106 offset:53248\nds_read_b64_tr_b16 v[86:87], v106 offset:54272\n"
      "ds_read_b64_tr_b16 v[88:89], v107 offset:45056\nds_read_b64_tr_b16 v[90:91], v107 offset:46080\nds_read_b64_tr_b16 v[92:93], v107 offset:53248\n"
      "ds_read_b64_tr_b16 v[94:95], v107 offset:54272\nds_read_b128 v[0:3], v100 offset:45056\nds_read_b128 v[4:7], v101 offset:45056\nds_read_b128 v[8:11], v120 offset:45056\n"
      "ds_read_b128 v[12:15], v121 offset:45056\nds_read_b128 v[16:19], v96 offset:45056\nds_read_b128 v[20:23], v97 offset:45056\ns_waitcnt lgkmcnt(2)\n.p2align 3\n"
      ".Lgl14:\nv_mfma_f32_16x16x32_bf16 v[124:127], v[64:67], v[0:3], v[124:127]\nds_read_b128 v[24:27], v96 offset:47104\nv_mfma_f32_16x16x32_bf16 v[124:127], v[68:71], v[4:7], v[124:127]\n"
      "ds_read_b128 v[28:31], v97 offset:47104\nv_mfma_f32_16x16x32_bf16 v[128:131], v[76:79], v[4:7], v[128:131]\nv_mfma_f32_16x16x32_bf16 v[128:131], v[72:75], v[0:3], v[128:131]\n"
      "v_mfma_f32_16x16x32_bf16 v[132:135], v[80:83], v[0:3], v[132:135]\nv_mfma_f32_16x16x32_bf16 v[132:135], v[84:87], v[4:7], v[132:135]\n"
      "v_mfma_f32_16x16x32_bf16 v[136:139], v[92:95], v[4:7], v[136:139]\nv_mfma_f32_16x16x32_bf16 v[136:139], v[88:91], v[0:3], v[136:139]\n"
      "v_mfma_f32_16x16x32_bf16 v[152:155], v[88:91], v[8:11], v[152:155]\nds_read_b128 v[0:3], v96 offset:49152\nv_mfma_f32_16x16x32_bf16 v[152:155], v[92:95], v[12:15], v[152:155]\n"
      "ds_read_b128 v[4:7], v97 offset:49152\nv_mfma_f32_16x16x32_bf16 v[148:151], v[84:87], v[12:15], v[148:151]\nv_mfma_f32_16x16x32_bf16 v[148:151], v[80:83], v[8:11], v[148:151]\n"
      "v_mfma_f32_16x16x32_bf16 v[144:147], v[72:75], v[8:11], v[144:147]\nv_mfma_f32_16x16x32_bf16 v[144:147], v[76:79], v[12:15], v[144:147]\n"
      "v_mfma_f32_16x16x32_bf16 v[140:143], v[68:71], v[12:15], v[140:143]\ns_waitcnt lgkmcnt(4)\nv_mfma_f32_16x16x32_bf16 v[140:143], v[64:67], v[8:11], v[140:143]\n"
      "v_mfma_f32_16x16x32_bf16 v[156:159], v[64:67], v[16:19], v[156:159]\nds_read_b128 v[8:11], v96 offset:51200\nv_mfma_f32_16x16x32_bf16 v[156:159], v[68:71], v[20:23], v[156:159]\n"
      "ds_read_b128 v[12:15], v97 offset:51200\nv_mfma_f32_16x16x32_bf16 v[160:163], v[76:79], v[20:23], v[160:163]\nv_mfma_f32_16x16x32_bf16 v[160:163], v[72:75], v[16:19], v[160:163]\n"
      "v_mfma_f32_16x16x32_bf16 v[164:167], v[80:83], v[16:19], v[164:167]\nv_mfma_f32_16x16x32_bf16 v[164:167], v[84:87], v[20:23], v[164:167]\n"
      "v_mfma_f32_16x16x32_bf16 v[168:171], v[92:95], v[20:23], v[168:171]\ns_waitcnt lgkmcnt(2)\nv_mfma_f32_16x16x32_bf16 v[168:171], v[88:91], v[16:19], v[168:171]\n"
      "v_mfma_f32_16x16x32_bf16 v[184:187], v[88:91], v[24:27], v[184:187]\nds_read_b128 v[16:19], v96 offset:53248\nv_mfma_f32_16x16x32_bf16 v[184:187], v[92:95], v[28:31], v[184:187]\n"
      "ds_read_b128 v[20:23], v97 offset:53248\nv_mfma_f32_16x16x32_bf16 v[180:183], v[84:87], v[28:31], v[180:183]\nv_mfma_f32_16x16x32_bf16 v[180:183], v[80:83], v[24:27], v[180:183]\n"
      "v_mfma_f32_16x16x32_bf16 v[176:179], v[72:75], v[24:27], v[176:179]\nv_mfma_f32_16x16x32_bf16 v[176:179], v[76:79], v[28:31], v[176:179]\n"
      "v_mfma_f32_16x16x32_bf16 v[172:175], v[68:71], v[28:31], v[172:175]\ns_waitcnt vmcnt(0) lgkmcnt(0)\ns_barrier\nv_mfma_f32_16x16x32_bf16 v[172:175], v[64:67], v[24:27], v[172:175]\n"
      "ds_read_b64_tr_b16 v[32:33], v108 offset:0\nv_mfma_f32_16x16x32_bf16 v[188:191], v[64:67], v[0:3], v[188:191]\nds_read_b64_tr_b16 v[34:35], v108 offset:1024\n"
      "v_mfma_f32_16x16x32_bf16 v[188:191], v[68:71], v[4:7], v[188:191]\nds_read_b64_tr_b16 v[36:37], v108 offset:8192\nv_mfma_f32_16x16x32_bf16 v[192:195], v[76:79], v[4:7], v[192:195]\n"
      "ds_read_b64_tr_b16 v[38:39], v108 offset:9216\nv_mfma_f32_16x16x32_bf16 v[192:195], v[72:75], v[0:3], v[192:195]\nds_read_b64_tr_b16 v[40:41], v109 offset:0\n"
      "v_mfma_f32_16x16x32_bf16 v[196:199], v[80:83], v[0:3], v[196:199]\nds_read_b64_tr_b16 v[42:43], v109 offset:1024\nv_mfma_f32_16x16x32_bf16 v[196:199], v[84:87], v[4:7], v[196:199]\n"
      "ds_read_b64_tr_b16 v[44:45], v109 offset:8192\nv_mfma_f32_16x16x32_bf16 v[200:203], v[92:95], v[4:7], v[200:203]\nds_read_b64_tr_b16 v[46:47], v109 offset:9216\n"
      "v_mfma_f32_16x16x32_bf16 v[200:203], v[88:91], v[0:3], v[200:203]\nds_read_b64_tr_b16 v[48:49], v110 offset:0\nv_mfma_f32_16x16x32_bf16 v[216:219], v[88:91], v[8:11], v[216:219]\n"
      "ds_read_b128 v[0:3], v102 offset:0\nv_mfma_f32_16x16x32_bf16 v[216:219], v[92:95], v[12:15], v[216:219]\nds_read_b128 v[4:7], v103 offset:0\n"
      "v_mfma_f32_16x16x32_bf16 v[212:215], v[84:87], v[12:15], v[212:215]\nds_read_b64_tr_b16 v[50:51], v110 offset:1024\nv_mfma_f32_16x16x32_bf16 v[212:215], v[80:83], v[8:11], v[212:215]\n"
      "ds_read_b64_tr_b16 v[52:53], v110 offset:8192\nv_mfma_f32_16x16x32_bf16 v[208:211], v[72:75], v[8:11], v[208:211]\nds_read_b64_tr_b16 v[54:55], v110 offset:9216\n"
      "v_mfma_f32_16x16x32_bf16 v[208:211], v[76:79], v[12:15], v[208:211]\nds_read_b64_tr_b16 v[56:57], v111 offset:0\nv_mfma_f32_16x16x32_bf16 v[204:207], v[68:71], v[12:15], v[204:207]\n"
      "ds_read_b64_tr_b16 v[58:59], v111 offset:1024\nv_mfma_f32_16x16x32_bf16 v[204:207], v[64:67], v[8:11], v[204:207]\nds_read_b64_tr_b16 v[60:61], v111 offset:8192\n"
      "v_mfma_f32_16x16x32_bf16 v[220:223], v[64:67], v[16:19], v[220:223]\nds_read_b128 v[8:11], v122 offset:0\nv_mfma_f32_16x16x32_bf16 v[220:223], v[68:71], v[20:23], v[220:223]\n"
      "ds_read_b128 v[12:15], v123 offset:0\nv_mfma_f32_16x16x32_bf16 v[224:227], v[76:79], v[20:23], v[224:227]\nds_read_b64_tr_b16 v[62:63], v111 offset:9216\n"
      "v_mfma_f32_16x16x32_bf16 v[224:227], v[72:75], v[16:19], v[224:227]\nv_mfma_f32_16x16x32_bf16 v[228:231], v[80:83], v[16:19], v[228:231]\n"
      "v_mfma_f32_16x16x32_bf16 v[228:231], v[84:87], v[20:23], v[228:231]\nv_mfma_f32_16x16x32_bf16 v[232:235], v[92:95], v[20:23], v[232:235]\ns_waitcnt lgkmcnt(0)\n"
      "v_mfma_f32_16x16x32_bf16 v[232:235], v[88:91], v[16:19], v[232:235]\nds_read_b128 v[16:19], v98 offset:0\nds_read_b128 v[20:23], v99 offset:0\n"
      "v_mfma_f32_16x16x32_bf16 v[124:127], v[32:35], v[0:3], v[124:127]\nds_read_b128 v[24:27], v98 offset:2048\nv_mfma_f32_16x16x32_bf16 v[124:127], v[36:39], v[4:7], v[124:127]\n"
      "ds_read_b128 v[28:31], v99 offset:2048\nv_mfma_f32_16x16x32_bf16 v[128:131], v[44:47], v[4:7], v[128:131]\nv_mfma_f32_16x16x32_bf16 v[128:131], v[40:43], v[0:3], v[128:131]\n"
      "v_mfma_f32_16x16x32_bf16 v[132:135], v[48:51], v[0:3], v[132:135]\nv_mfma_f32_16x16x32_bf16 v[132:135], v[52:55], v[4:7], v[132:135]\n"
      "v_mfma_f32_16x16x32_bf16 v[136:139], v[60:63], v[4:7], v[136:139]\nv_mfma_f32_16x16x32_bf16 v[136:139], v[56:59], v[0:3], v[136:139]\n"
      "v_mfma_f32_16x16x32_bf16 v[152:155], v[56:59], v[8:11], v[152:155]\nds_read_b128 v[0:3], v98 offset:4096\nv_mfma_f32_16x16x32_bf16 v[152:155], v[60:63], v[12:15], v[152:155]\n"
      "ds_read_b128 v[4:7], v99 offset:4096\nv_mfma_f32_16x16x32_bf16 v[148:151], v[52:55], v[12:15], v[148:151]\nv_mfma_f32_16x16x32_bf16 v[148:151], v[48:51], v[8:11], v[148:151]\n"
      "v_mfma_f32_16x16x32_bf16 v[144:147], v[40:43], v[8:11], v[144:147]\nv_mfma_f32_16x16x32_bf16 v[144:147], v[44:47], v[12:15], v[144:147]\n"
      "v_mfma_f32_16x16x32_bf16 v[140:143], v[36:39], v[12:15], v[140:143]\ns_waitcnt lgkmcnt(4)\nv_mfma_f32_16x16x32_bf16 v[140:143], v[32:35], v[8:11], v[140:143]\n"
      "v_mfma_f32_16x16x32_bf16 v[156:159], v[32:35], v[16:19], v[156:159]\nds_read_b128 v[8:11], v98 offset:6144\nv_mfma_f32_16x16x32_bf16 v[156:159], v[36:39], v[20:23], v[156:159]\n"
      "ds_read_b128 v[12:15], v99 offset:6144\nv_mfma_f32_16x16x32_bf16 v[160:163], v[44:47], v[20:23], v[160:163]\nv_mfma_f32_16x16x32_bf16 v[160:163], v[40:43], v[16:19], v[160:163]\n"
      "v_mfma_f32_16x16x32_bf16 v[164:167], v[48:51], v[16:19], v[164:167]\nv_mfma_f32_16x16x32_bf16 v[164:167], v[52:55], v[20:23], v[164:167]\n"
      "v_mfma_f32_16x16x32_bf16 v[168:171], v[60:63], v[20:23], v[168:171]\ns_waitcnt lgkmcnt(2)\nv_mfma_f32_16x16x32_bf16 v[168:171], v[56:59], v[16:19], v[168:171]\n"
      "v_mfma_f32_16x16x32_bf16 v[184:187], v[56:59], v[24:27], v[184:187]\nds_read_b128 v[16:19], v98 offset:8192\nv_mfma_f32_16x16x32_bf16 v[184:187], v[60:63], v[28:31], v[184:187]\n"
      "ds_read_b128 v[20:23], v99 offset:8192\nv_mfma_f32_16x16x32_bf16 v[180:183], v[52:55], v[28:31], v[180:183]\nv_mfma_f32_16x16x32_bf16 v[180:183], v[48:51], v[24:27], v[180:183]\n"
      "v_mfma_f32_16x16x32_bf16 v[176:179], v[40:43], v[24:27], v[176:179]\nv_mfma_f32_16x16x32_bf16 v[176:179], v[44:47], v[28:31], v[176:179]\n"
      "v_mfma_f32_16x16x32_bf16 v[172:175], v[36:39], v[28:31], v[172:175]\ns_waitcnt vmcnt(0) lgkmcnt(0)\ns_barrier\nv_mfma_f32_16x16x32_bf16 v[172:175], v[32:35], v[24:27], v[172:175]\n"
      "ds_read_b64_tr_b16 v[64:65], v104 offset:0\nv_mfma_f32_16x16x32_bf16 v[188:191], v[32:35], v[0:3], v[188:191]\nds_read_b64_tr_b16 v[66:67], v104 offset:1024\n"
      "v_mfma_f32_16x16x32_bf16 v[188:191], v[36:39], v[4:7], v[188:191]\nds_read_b64_tr_b16 v[68:69], v104 offset:8192\nv_mfma_f32_16x16x32_bf16 v[192:195], v[44:47], v[4:7], v[192:195]\n"
      "ds_read_b64_tr_b16 v[70:71], v104 offset:9216\nv_mfma_f32_16x16x32_bf16 v[192:195], v[40:43], v[0:3], v[192:195]\nds_read_b64_tr_b16 v[72:73], v105 offset:0\n"
      "v_mfma_f32_16x16x32_bf16 v[196:199], v[48:51], v[0:3], v[196:199]\nds_read_b64_tr_b16 v[74:75], v105 offset:1024\nv_mfma_f32_16x16x32_bf16 v[196:199], v[52:55], v[4:7], v[196:199]\n"
      "ds_read_b64_tr_b16 v[76:77], v105 offset:8192\nv_mfma_f32_16x16x32_bf16 v[200:203], v[60:63], v[4:7], v[200:203]\nds_read_b64_tr_b16 v[78:79], v105 offset:9216\n"
      "v_mfma_f32_16x16x32_bf16 v[200:203], v[56:59], v[0:3], v[200:203]\nds_read_b64_tr_b16 v[80:81], v106 offset:0\nv_mfma_f32_16x16x32_bf16 v[216:219], v[56:59], v[8:11], v[216:219]\n"
      "ds_read_b128 v[0:3], v100 offset:0\nv_mfma_f32_16x16x32_bf16 v[216:219], v[60:63], v[12:15], v[216:219]\nds_read_b128 v[4:7], v101 offset:0\n"
      "v_mfma_f32_16x16x32_bf16 v[212:215], v[52:55], v[12:15], v[212:215]\nds_read_b64_tr_b16 v[82:83], v106 offset:1024\nv_mfma_f32_16x16x32_bf16 v[212:215], v[48:51], v[8:11], v[212:215]\n"
      "ds_read_b64_tr_b16 v[84:85], v106 offset:8192\nv_mfma_f32_16x16x32_bf16 v[208:211], v[40:43], v[8:11], v[208:211]\nds_read_b64_tr_b16 v[86:87], v106 offset:9216\n"
      "v_mfma_f32_16x16x32_bf16 v[208:211], v[44:47], v[12:15], v[208:211]\nds_read_b64_tr_b16 v[88:89], v107 offset:0\nv_mfma_f32_16x16x32_bf16 v[204:207], v[36:39], v[12:15], v[204:207]\n"
      "ds_read_b64_tr_b16 v[90:91], v107 offset:1024\nv_mfma_f32_16x16x32_bf16 v[204:207], v[32:35], v[8:11], v[204:207]\nds_read_b64_tr_b16 v[92:93], v107 offset:8192\n"
      "v_mfma_f32_16x16x32_bf16 v[220:223], v[32:35], v[16:19], v[220:223]\nds_read_b128 v[8:11], v120 offset:0\nv_mfma_f32_16x16x32_bf16 v[220:223], v[36:39], v[20:23], v[220:223]\n"
      "ds_read_b128 v[12:15], v121 offset:0\nv_mfma_f32_16x16x32_bf16 v[224:227], v[44:47], v[20:23], v[224:227]\nds_read_b64_tr_b16 v[94:95], v107 offset:9216\n"
      "v_mfma_f32_16x16x32_bf16 v[224:227], v[40:43], v[16:19], v[224:227]\nv_mfma_f32_16x16x32_bf16 v[228:231], v[48:51], v[16:19], v[228:231]\n"
      "v_mfma_f32_16x16x32_bf16 v[228:231], v[52:55], v[20:23], v[228:231]\nv_mfma_f32_16x16x32_bf16 v[232:235], v[60:63], v[20:23], v[232:235]\ns_waitcnt lgkmcnt(0)\n"
      "v_mfma_f32_16x16x32_bf16 v[232:235], v[56:59], v[16:19], v[232:235]\nds_read_b128 v[16:19], v96 offset:0\nds_read_b128 v[20:23], v97 offset:0\n"
      "v_mfma_f32_16x16x32_bf16 v[124:127], v[64:67], v[0:3], v[124:127]\nds_read_b128 v[24:27], v96 offset:2048\nv_mfma_f32_16x16x32_bf16 v[124:127], v[68:71], v[4:7], v[124:127]\n"
      "ds_read_b128 v[28:31], v97 offset:2048\nv_mfma_f32_16x16x32_bf16 v[128:131], v[76:79], v[4:7], v[128:131]\nv_mfma_f32_16x16x32_bf16 v[128:131], v[72:75], v[0:3], v[128:131]\n"
      "v_mfma_f32_16x16x32_bf16 v[132:135], v[80:83], v[0:3], v[132:135]\nv_mfma_f32_16x16x32_bf16 v[132:135], v[84:87], v[4:7], v[132:135]\n"
      "v_mfma_f32_16x16x32_bf16 v[136:139], v[92:95], v[4:7], v[136:139]\nv_mfma_f32_16x16x32_bf16 v[136:139], v[88:91], v[0:3], v[136:139]\n"
      "v_mfma_f32_16x16x32_bf16 v[152:155], v[88:91], v[8:11], v[152:155]\nds_read_b128 v[0:3], v96 offset:4096\nv_mfma_f32_16x16x32_bf16 v[152:155], v[92:95], v[12:15], v[152:155]\n"
      "ds_read_b128 v[4:7], v97 offset:4096\nv_mfma_f32_16x16x32_bf16 v[148:151], v[84:87], v[12:15], v[148:151]\nv_mfma_f32_16x16x32_bf16 v[148:151], v[80:83], v[8:11], v[148:151]\n"
      "v_mfma_f32_16x16x32_bf16 v[144:147], v[72:75], v[8:11], v[144:147]\nv_mfma_f32_16x16x32_bf16 v[144:147], v[76:79], v[12:15], v[144:147]\n"
      "v_mfma_f32_16x16x32_bf16 v[140:143], v[68:71], v[12:15], v[140:143]\ns_waitcnt lgkmcnt(4)\nv_mfma_f32_16x16x32_bf16 v[140:143], v[64:67], v[8:11], v[140:143]\n"
      "v_mfma_f32_16x16x32_bf16 v[156:159], v[64:67], v[16:19], v[156:159]\nds_read_b128 v[8:11], v96 offset:6144\nv_mfma_f32_16x16x32_bf16 v[156:159], v[68:71], v[20:23], v[156:159]\n"
      "ds_read_b128 v[12:15], v97 offset:6144\nv_mfma_f32_16x16x32_bf16 v[160:163], v[76:79], v[20:23], v[160:163]\nv_mfma_f32_16x16x32_bf16 v[160:163], v[72:75], v[16:19], v[160:163]\n"
      "v_mfma_f32_16x16x32_bf16 v[164:167], v[80:83], v[16:19], v[164:167]\nv_mfma_f32_16x16x32_bf16 v[164:167], v[84:87], v[20:23], v[164:167]\n"
      "v_mfma_f32_16x16x32_bf16 v[168:171], v[92:95], v[20:23], v[168:171]\ns_waitcnt lgkmcnt(2)\nv_mfma_f32_16x16x32_bf16 v[168:171], v[88:91], v[16:19], v[168:171]\n"
      "v_mfma_f32_16x16x32_bf16 v[184:187], v[88:91], v[24:27], v[184:187]\nds_read_b128 v[16:19], v96 offset:8192\nv_mfma_f32_16x16x32_bf16 v[184:187], v[92:95], v[28:31], v[184:187]\n"
      "ds_read_b128 v[20:23], v97 offset:8192\nv_mfma_f32_16x16x32_bf16 v[180:183], v[84:87], v[28:31], v[180:183]\nv_mfma_f32_16x16x32_bf16 v[180:183], v[80:83], v[24:27], v[180:183]\n"
      "v_mfma_f32_16x16x32_bf16 v[176:179], v[72:75], v[24:27], v[176:179]\nv_mfma_f32_16x16x32_bf16 v[176:179], v[76:79], v[28:31], v[176:179]\n"
      "v_mfma_f32_16x16x32_bf16 v[172:175], v[68:71], v[28:31], v[172:175]\ns_waitcnt vmcnt(0) lgkmcnt(0)\ns_barrier\nv_mfma_f32_16x16x32_bf16 v[172:175], v[64:67], v[24:27], v[172:175]\n"
      "ds_read_b64_tr_b16 v[32:33], v104 offset:45056\nv_mfma_f32_16x16x32_bf16 v[188:191], v[64:67], v[0:3], v[188:191]\nds_read_b64_tr_b16 v[34:35], v104 offset:46080\n"
      "v_mfma_f32_16x16x32_bf16 v[188:191], v[68:71], v[4:7], v[188:191]\nds_read_b64_tr_b16 v[36:37], v104 offset:53248\nv_mfma_f32_16x16x32_bf16 v[192:195], v[76:79], v[4:7], v[192:195]\n"
      "ds_read_b64_tr_b16 v[38:39], v104 offset:54272\nv_mfma_f32_16x16x32_bf16 v[192:195], v[72:75], v[0:3], v[192:195]\nds_read_b64_tr_b16 v[40:41], v105 offset:45056\n"
      "v_mfma_f32_16x16x32_bf16 v[196:199], v[80:83], v[0:3], v[196:199]\nds_read_b64_tr_b16 v[42:43], v105 offset:46080\nv_mfma_f32_16x16x32_bf16 v[196:199], v[84:87], v[4:7], v[196:199]\n"
      "ds_read_b64_tr_b16 v[44:45], v105 offset:53248\nv_mfma_f32_16x16x32_bf16 v[200:203], v[92:95], v[4:7], v[200:203]\nds_read_b64_tr_b16 v[46:47], v105 offset:54272\n"
      "v_mfma_f32_16x16x32_bf16 v[200:203], v[88:91], v[0:3], v[200:203]\nds_read_b64_tr_b16 v[48:49], v106 offset:45056\nv_mfma_f32_16x16x32_bf16 v[216:219], v[88:91], v[8:11], v[216:219]\n"
      "ds_read_b128 v[0:3], v100 offset:45056\nv_mfma_f32_16x16x32_bf16 v[216:219], v[92:95], v[12:15], v[216:219]\nds_read_b128 v[4:7], v101 offset:45056\n"
      "v_mfma_f32_16x16x32_bf16 v[212:215], v[84:87], v[12:15], v[212:215]\nds_read_b64_tr_b16 v[50:51], v106 offset:46080\nv_mfma_f32_16x16x32_bf16 v[212:215], v[80:83], v[8:11], v[212:215]\n"
      "ds_read_b64_tr_b16 v[52:53], v106 offset:53248\nv_mfma_f32_16x16x32_bf16 v[208:211], v[72:75], v[8:11], v[208:211]\nds_read_b64_tr_b16 v[54:55], v106 offset:54272\n"
      "v_mfma_f32_16x16x32_bf16 v[208:211], v[76:79], v[12:15], v[208:211]\nds_read_b64_tr_b16 v[56:57], v107 offset:45056\nv_mfma_f32_16x16x32_bf16 v[204:207], v[68:71], v[12:15], v[204:207]\n"
      "ds_read_b64_tr_b16 v[58:59], v107 offset:46080\nv_mfma_f32_16x16x32_bf16 v[204:207], v[64:67], v[8:11], v[204:207]\nds_read_b64_tr_b16 v[60:61], v107 offset:53248\n"
      "v_mfma_f32_16x16x32_bf16 v[220:223], v[64:67], v[16:19], v[220:223]\nds_read_b128 v[8:11], v120 offset:45056\nv_mfma_f32_16x16x32_bf16 v[220:223], v[68:71], v[20:23], v[220:223]\n"
      "ds_read_b128 v[12:15], v121 offset:45056\nv_mfma_f32_16x16x32_bf16 v[224:227], v[76:79], v[20:23], v[224:227]\nds_read_b64_tr_b16 v[62:63], v107 offset:54272\n"
      "v_mfma_f32_16x16x32_bf16 v[224:227], v[72:75], v[16:19], v[224:227]\nv_mfma_f32_16x16x32_bf16 v[228:231], v[80:83], v[16:19], v[228:231]\n"
      "v_mfma_f32_16x16x32_bf16 v[228:231], v[84:87], v[20:23], v[228:231]\nv_mfma_f32_16x16x32_bf16 v[232:235], v[92:95], v[20:23], v[232:235]\ns_waitcnt lgkmcnt(0)\n"
      "v_mfma_f32_16x16x32_bf16 v[232:235], v[88:91], v[16:19], v[232:235]\nds_read_b128 v[16:19], v96 offset:45056\nds_read_b128 v[20:23], v97 offset:45056\n"
      "v_mfma_f32_16x16x32_bf16 v[124:127], v[32:35], v[0:3], v[124:127]\nds_read_b128 v[24:27], v96 offset:47104\nv_mfma_f32_16x16x32_bf16 v[124:127], v[36:39], v[4:7], v[124:127]\n"
      "ds_read_b128 v[28:31], v97 offset:47104\nv_mfma_f32_16x16x32_bf16 v[128:131], v[44:47], v[4:7], v[128:131]\nv_mfma_f32_16x16x32_bf16 v[128:131], v[40:43], v[0:3], v[128:131]\n"
      "v_mfma_f32_16x16x32_bf16 v[132:135], v[48:51], v[0:3], v[132:135]\nv_mfma_f32_16x16x32_bf16 v[132:135], v[52:55], v[4:7], v[132:135]\n"
      "v_mfma_f32_16x16x32_bf16 v[136:139], v[60:63], v[4:7], v[136:139]\nv_mfma_f32_16x16x32_bf16 v[136:139], v[56:59], v[0:3], v[136:139]\n"
      "v_mfma_f32_16x16x32_bf16 v[152:155], v[56:59], v[8:11], v[152:155]\nds_read_b128 v[0:3], v96 offset:49152\nv_mfma_f32_16x16x32_bf16 v[152:155], v[60:63], v[12:15], v[152:155]\n"
      "ds_read_b128 v[4:7], v97 offset:49152\nv_mfma_f32_16x16x32_bf16 v[148:151], v[52:55], v[12:15], v[148:151]\nv_mfma_f32_16x16x32_bf16 v[148:151], v[48:51], v[8:11], v[148:151]\n"
      "v_mfma_f32_16x16x32_bf16 v[144:147], v[40:43], v[8:11], v[144:147]\nv_mfma_f32_16x16x32_bf16 v[144:147], v[44:47], v[12:15], v[144:147]\n"
      "v_mfma_f32_16x16x32_bf16 v[140:143], v[36:39], v[12:15], v[140:143]\ns_waitcnt lgkmcnt(4)\nv_mfma_f32_16x16x32_bf16 v[140:143], v[32:35], v[8:11], v[140:143]\n"
      "v_mfma_f32_16x16x32_bf16 v[156:159], v[32:35], v[16:19], v[156:159]\nds_read_b128 v[8:11], v96 offset:51200\nv_mfma_f32_16x16x32_bf16 v[156:159], v[36:39], v[20:23], v[156:159]\n"
      "ds_read_b128 v[12:15], v97 offset:51200\nv_mfma_f32_16x16x32_bf16 v[160:163], v[44:47], v[20:23], v[160:163]\nv_mfma_f32_16x16x32_bf16 v[160:163], v[40:43], v[16:19], v[160:163]\n"
      "v_mfma_f32_16x16x32_bf16 v[164:167], v[48:51], v[16:19], v[164:167]\nv_mfma_f32_16x16x32_bf16 v[164:167], v[52:55], v[20:23], v[164:167]\n"
      "v_mfma_f32_16x16x32_bf16 v[168:171], v[60:63], v[20:23], v[168:171]\ns_waitcnt lgkmcnt(2)\nv_mfma_f32_16x16x32_bf16 v[168:171], v[56:59], v[16:19], v[168:171]\n"
      "v_mfma_f32_16x16x32_bf16 v[184:187], v[56:59], v[24:27], v[184:187]\nds_read_b128 v[16:19], v96 offset:53248\nv_mfma_f32_16x16x32_bf16 v[184:187], v[60:63], v[28:31], v[184:187]\n"
      "ds_read_b128 v[20:23], v97 offset:53248\nv_mfma_f32_16x16x32_bf16 v[180:183], v[52:55], v[28:31], v[180:183]\nv_mfma_f32_16x16x32_bf16 v[180:183], v[48:51], v[24:27], v[180:183]\n"
      "v_mfma_f32_16x16x32_bf16 v[176:179], v[40:43], v[24:27], v[176:179]\nv_mfma_f32_16x16x32_bf16 v[176:179], v[44:47], v[28:31], v[176:179]\n"
      "v_mfma_f32_16x16x32_bf16 v[172:175], v[36:39], v[28:31], v[172:175]\ns_waitcnt vmcnt(0) lgkmcnt(0)\ns_barrier\nv_mfma_f32_16x16x32_bf16 v[172:175], v[32:35], v[24:27], v[172:175]\n"
      "ds_read_b64_tr_b16 v[64:65], v108 offset:0\nv_mfma_f32_16x16x32_bf16 v[188:191], v[32:35], v[0:3], v[188:191]\nds_read_b64_tr_b16 v[66:67], v108 offset:1024\n"
      "v_mfma_f32_16x16x32_bf16 v[188:191], v[36:39], v[4:7], v[188:191]\nds_read_b64_tr_b16 v[68:69], v108 offset:8192\nv_mfma_f32_16x16x32_bf16 v[192:195], v[44:47], v[4:7], v[192:195]\n"
      "ds_read_b64_tr_b16 v[70:71], v108 offset:9216\nv_mfma_f32_16x16x32_bf16 v[192:195], v[40:43], v[0:3], v[192:195]\nds_read_b64_tr_b16 v[72:73], v109 offset:0\n"
      "v_mfma_f32_16x16x32_bf16 v[196:199], v[48:51], v[0:3], v[196:199]\nds_read_b64_tr_b16 v[74:75], v109 offset:1024\nv_mfma_f32_16x16x32_bf16 v[196:199], v[52:55], v[4:7], v[196:199]\n"
      "ds_read_b64_tr_b16 v[76:77], v109 offset:8192\nv_mfma_f32_16x16x32_bf16 v[200:203], v[60:63], v[4:7], v[200:203]\nds_read_b64_tr_b16 v[78:79], v109 offset:9216\n"
      "v_mfma_f32_16x16x32_bf16 v[200:203], v[56:59], v[0:3], v[200:203]\nds_read_b64_tr_b16 v[80:81], v110 offset:0\nv_mfma_f32_16x16x32_bf16 v[216:219], v[56:59], v[8:11], v[216:219]\n"
      "ds_read_b128 v[0:3], v102 offset:0\nv_mfma_f32_16x16x32_bf16 v[216:219], v[60:63], v[12:15], v[216:219]\nds_read_b128 v[4:7], v103 offset:0\n"
      "v_mfma_f32_16x16x32_bf16 v[212:215], v[52:55], v[12:15], v[212:215]\nds_read_b64_tr_b16 v[82:83], v110 offset:1024\nv_mfma_f32_16x16x32_bf16 v[212:215], v[48:51], v[8:11], v[212:215]\n"
      "ds_read_b64_tr_b16 v[84:85], v110 offset:8192\nv_mfma_f32_16x16x32_bf16 v[208:211], v[40:43], v[8:11], v[208:211]\nds_read_b64_tr_b16 v[86:87], v110 offset:9216\n"
      "v_mfma_f32_16x16x32_bf16 v[208:211], v[44:47], v[12:15], v[208:211]\nds_read_b64_tr_b16 v[88:89], v111 offset:0\nv_mfma_f32_16x16x32_bf16 v[204:207], v[36:39], v[12:15], v[204:207]\n"
      "ds_read_b64_tr_b16 v[90:91], v111 offset:1024\nv_mfma_f32_16x16x32_bf16 v[204:207], v[32:35], v[8:11], v[204:207]\nds_read_b64_tr_b16 v[92:93], v111 offset:8192\n"
      "v_mfma_f32_16x16x32_bf16 v[220:223], v[32:35], v[16:19], v[220:223]\nds_read_b128 v[8:11], v122 offset:0\nv_mfma_f32_16x16x32_bf16 v[220:223], v[36:39], v[20:23], v[220:223]\n"
      "ds_read_b128 v[12:15], v123 offset:0\nv_mfma_f32_16x16x32_bf16 v[224:227], v[44:47], v[20:23], v[224:227]\nds_read_b64_tr_b16 v[94:95], v111 offset:9216\n"
      "v_mfma_f32_16x16x32_bf16 v[224:227], v[40:43], v[16:19], v[224:227]\nv_mfma_f32_16x16x32_bf16 v[228:231], v[48:51], v[16:19], v[228:231]\n"
      "v_mfma_f32_16x16x32_bf16 v[228:231], v[52:55], v[20:23], v[228:231]\nv_mfma_f32_16x16x32_bf16 v[232:235], v[60:63], v[20:23], v[232:235]\ns_waitcnt lgkmcnt(0)\n"
      "v_mfma_f32_16x16x32_bf16 v[232:235], v[56:59], v[16:19], v[232:235]\nds_read_b128 v[16:19], v98 offset:0\nds_read_b128 v[20:23], v99 offset:0\n"
      "v_mfma_f32_16x16x32_bf16 v[124:127], v[64:67], v[0:3], v[124:127]\nds_read_b128 v[24:27], v98 offset:2048\nv_mfma_f32_16x16x32_bf16 v[124:127], v[68:71], v[4:7], v[124:127]\n"
      "ds_read_b128 v[28:31], v99 offset:2048\nv_mfma_f32_16x16x32_bf16 v[128:131], v[76:79], v[4:7], v[128:131]\nv_mfma_f32_16x16x32_bf16 v[128:131], v[72:75], v[0:3], v[128:131]\n"
      "v_mfma_f32_16x16x32_bf16 v[132:135], v[80:83], v[0:3], v[132:135]\nv_mfma_f32_16x16x32_bf16 v[132:135], v[84:87], v[4:7], v[132:135]\n"
      "v_mfma_f32_16x16x32_bf16 v[136:139], v[92:95], v[4:7], v[136:139]\nv_mfma_f32_16x16x32_bf16 v[136:139], v[88:91], v[0:3], v[136:139]\n"
      "v_mfma_f32_16x16x32_bf16 v[152:155], v[88:91], v[8:11], v[152:155]\nds_read_b128 v[0:3], v98 offset:4096\nv_mfma_f32_16x16x32_bf16 v[152:155], v[92:95], v[12:15], v[152:155]\n"
      "ds_read_b128 v[4:7], v99 offset:4096\nv_mfma_f32_16x16x32_bf16 v[148:151], v[84:87], v[12:15], v[148:151]\nv_mfma_f32_16x16x32_bf16 v[148:151], v[80:83], v[8:11], v[148:151]\n"
      "v_mfma_f32_16x16x32_bf16 v[144:147], v[72:75], v[8:11], v[144:147]\nv_mfma_f32_16x16x32_bf16 v[144:147], v[76:79], v[12:15], v[144:147]\n"
      "v_mfma_f32_16x16x32_bf16 v[140:143], v[68:71], v[12:15], v[140:143]\ns_waitcnt lgkmcnt(4)\nv_mfma_f32_16x16x32_bf16 v[140:143], v[64:67], v[8:11], v[140:143]\n"
      "v_mfma_f32_16x16x32_bf16 v[156:159], v[64:67], v[16:19], v[156:159]\nds_read_b128 v[8:11], v98 offset:6144\nv_mfma_f32_16x16x32_bf16 v[156:159], v[68:71], v[20:23], v[156:159]\n"
      "ds_read_b128 v[12:15], v99 offset:6144\nv_mfma_f32_16x16x32_bf16 v[160:163], v[76:79], v[20:23], v[160:163]\nv_mfma_f32_16x16x32_bf16 v[160:163], v[72:75], v[16:19], v[160:163]\n"
      "v_mfma_f32_16x16x32_bf16 v[164:167], v[80:83], v[16:19], v[164:167]\nv_mfma_f32_16x16x32_bf16 v[164:167], v[84:87], v[20:23], v[164:167]\n"
      "v_mfma_f32_16x16x32_bf16 v[168:171], v[92:95], v[20:23], v[168:171]\ns_waitcnt lgkmcnt(2)\nv_mfma_f32_16x16x32_bf16 v[168:171], v[88:91], v[16:19], v[168:171]\n"
      "v_mfma_f32_16x16x32_bf16 v[184:187], v[88:91], v[24:27], v[184:187]\nds_read_b128 v[16:19], v98 offset:8192\nv_mfma_f32_16x16x32_bf16 v[184:187], v[92:95], v[28:31], v[184:187]\n"
      "ds_read_b128 v[20:23], v99 offset:8192\nv_mfma_f32_16x16x32_bf16 v[180:183], v[84:87], v[28:31], v[180:183]\nv_mfma_f32_16x16x32_bf16 v[180:183], v[80:83], v[24:27], v[180:183]\n"
      "v_mfma_f32_16x16x32_bf16 v[176:179], v[72:75], v[24:27], v[176:179]\nv_mfma_f32_16x16x32_bf16 v[176:179], v[76:79], v[28:31], v[176:179]\n"
      "v_mfma_f32_16x16x32_bf16 v[172:175], v[68:71], v[28:31], v[172:175]\ns_waitcnt vmcnt(0) lgkmcnt(0)\ns_barrier\nv_mfma_f32_16x16x32_bf16 v[172:175], v[64:67], v[24:27], v[172:175]\n"
      "ds_read_b64_tr_b16 v[32:33], v104 offset:0\nv_mfma_f32_16x16x32_bf16 v[188:191], v[64:67], v[0:3], v[188:191]\nds_read_b64_tr_b16 v[34:35], v104 offset:1024\n"
      "v_mfma_f32_16x16x32_bf16 v[188:191], v[68:71], v[4:7], v[188:191]\nds_read_b64_tr_b16 v[36:37], v104 offset:8192\nv_mfma_f32_16x16x32_bf16 v[192:195], v[76:79], v[4:7], v[192:195]\n"
      "ds_read_b64_tr_b16 v[38:39], v104 offset:9216\nv_mfma_f32_16x16x32_bf16 v[192:195], v[72:75], v[0:3], v[192:195]\nds_read_b64_tr_b16 v[40:41], v105 offset:0\n"
      "v_mfma_f32_16x16x32_bf16 v[196:199], v[80:83], v[0:3], v[196:199]\nds_read_b64_tr_b16 v[42:43], v105 offset:1024\nv_mfma_f32_16x16x32_bf16 v[196:199], v[84:87], v[4:7], v[196:199]\n"
      "ds_read_b64_tr_b16 v[44:45], v105 offset:8192\nv_mfma_f32_16x16x32_bf16 v[200:203], v[92:95], v[4:7], v[200:203]\nds_read_b64_tr_b16 v[46:47], v105 offset:9216\n"
      "v_mfma_f32_16x16x32_bf16 v[200:203], v[88:91], v[0:3], v[200:203]\nds_read_b64_tr_b16 v[48:49], v106 offset:0\nv_mfma_f32_16x16x32_bf16 v[216:219], v[88:91], v[8:11], v[216:219]\n"
      "ds_read_b128 v[0:3], v100 offset:0\nv_mfma_f32_16x16x32_bf16 v[216:219], v[92:95], v[12:15], v[216:219]\nds_read_b128 v[4:7], v101 offset:0\n"
      "v_mfma_f32_16x16x32_bf16 v[212:215], v[84:87], v[12:15], v[212:215]\nds_read_b64_tr_b16 v[50:51], v106 offset:1024\nv_mfma_f32_16x16x32_bf16 v[212:215], v[80:83], v[8:11], v[212:215]\n"
      "ds_read_b64_tr_b16 v[52:53], v106 offset:8192\nv_mfma_f32_16x16x32_bf16 v[208:211], v[72:75], v[8:11], v[208:211]\nds_read_b64_tr_b16 v[54:55], v106 offset:9216\n"
      "v_mfma_f32_16x16x32_bf16 v[208:211], v[76:79], v[12:15], v[208:211]\nds_read_b64_tr_b16 v[56:57], v107 offset:0\nv_mfma_f32_16x16x32_bf16 v[204:207], v[68:71], v[12:15], v[204:207]\n"
      "ds_read_b64_tr_b16 v[58:59], v107 offset:1024\nv_mfma_f32_16x16x32_bf16 v[204:207], v[64:67], v[8:11], v[204:207]\nds_read_b64_tr_b16 v[60:61], v107 offset:8192\n"
      "v_mfma_f32_16x16x32_bf16 v[220:223], v[64:67], v[16:19], v[220:223]\nds_read_b128 v[8:11], v120 offset:0\nv_mfma_f32_16x16x32_bf16 v[220:223], v[68:71], v[20:23], v[220:223]\n"
      "ds_read_b128 v[12:15], v121 offset:0\nv_mfma_f32_16x16x32_bf16 v[224:227], v[76:79], v[20:23], v[224:227]\nds_read_b64_tr_b16 v[62:63], v107 offset:9216\n"
      "v_mfma_f32_16x16x32_bf16 v[224:227], v[72:75], v[16:19], v[224:227]\nv_mfma_f32_16x16x32_bf16 v[228:231], v[80:83], v[16:19], v[228:231]\n"
      "v_mfma_f32_16x16x32_bf16 v[228:231], v[84:87], v[20:23], v[228:231]\nv_mfma_f32_16x16x32_bf16 v[232:235], v[92:95], v[20:23], v[232:235]\ns_waitcnt lgkmcnt(0)\n"
      "v_mfma_f32_16x16x32_bf16 v[232:235], v[88:91], v[16:19], v[232:235]\nds_read_b128 v[16:19], v96 offset:0\nds_read_b128 v[20:23], v97 offset:0\n"
      "v_mfma_f32_16x16x32_bf16 v[124:127], v[32:35], v[0:3], v[124:127]\nds_read_b128 v[24:27], v96 offset:2048\nv_mfma_f32_16x16x32_bf16 v[124:127], v[36:39], v[4:7], v[124:127]\n"
      "ds_read_b128 v[28:31], v97 offset:2048\nv_mfma_f32_16x16x32_bf16 v[128:131], v[44:47], v[4:7], v[128:131]\nv_mfma_f32_16x16x32_bf16 v[128:131], v[40:43], v[0:3], v[128:131]\n"
      "v_mfma_f32_16x16x32_bf16 v[132:135], v[48:51], v[0:3], v[132:135]\nv_mfma_f32_16x16x32_bf16 v[132:135], v[52:55], v[4:7], v[132:135]\n"
      "v_mfma_f32_16x16x32_bf16 v[136:139], v[60:63], v[4:7], v[136:139]\nv_mfma_f32_16x16x32_bf16 v[136:139], v[56:59], v[0:3], v[136:139]\n"
      "v_mfma_f32_16x16x32_bf16 v[152:155], v[56:59], v[8:11], v[152:155]\nds_read_b128 v[0:3], v96 offset:4096\nv_mfma_f32_16x16x32_bf16 v[152:155], v[60:63], v[12:15], v[152:155]\n"
      "ds_read_b128 v[4:7], v97 offset:4096\nv_mfma_f32_16x16x32_bf16 v[148:151], v[52:55], v[12:15], v[148:151]\nv_mfma_f32_16x16x32_bf16 v[148:151], v[48:51], v[8:11], v[148:151]\n"
      "v_mfma_f32_16x16x32_bf16 v[144:147], v[40:43], v[8:11], v[144:147]\nv_mfma_f32_16x16x32_bf16 v[144:147], v[44:47], v[12:15], v[144:147]\n"
      "v_mfma_f32_16x16x32_bf16 v[140:143], v[36:39], v[12:15], v[140:143]\ns_waitcnt lgkmcnt(4)\nv_mfma_f32_16x16x32_bf16 v[140:143], v[32:35], v[8:11], v[140:143]\n"
      "v_mfma_f32_16x16x32_bf16 v[156:159], v[32:35], v[16:19], v[156:159]\nds_read_b128 v[8:11], v96 offset:6144\nv_mfma_f32_16x16x32_bf16 v[156:159], v[36:39], v[20:23], v[156:159]\n"
      "ds_read_b128 v[12:15], v97 offset:6144\nv_mfma_f32_16x16x32_bf16 v[160:163], v[44:47], v[20:23], v[160:163]\nv_mfma_f32_16x16x32_bf16 v[160:163], v[40:43], v[16:19], v[160:163]\n"
      "v_mfma_f32_16x16x32_bf16 v[164:167], v[48:51], v[16:19], v[164:167]\nv_mfma_f32_16x16x32_bf16 v[164:167], v[52:55], v[20:23], v[164:167]\n"
      "v_mfma_f32_16x16x32_bf16 v[168:171], v[60:63], v[20:23], v[168:171]\ns_waitcnt lgkmcnt(2)\nv_mfma_f32_16x16x32_bf16 v[168:171], v[56:59], v[16:19], v[168:171]\n"
      "v_mfma_f32_16x16x32_bf16 v[184:187], v[56:59], v[24:27], v[184:187]\nds_read_b128 v[16:19], v96 offset:8192\nv_mfma_f32_16x16x32_bf16 v[184:187], v[60:63], v[28:31], v[184:187]\n"
      "ds_read_b128 v[20:23], v97 offset:8192\nv_mfma_f32_16x16x32_bf16 v[180:183], v[52:55], v[28:31], v[180:183]\nv_mfma_f32_16x16x32_bf16 v[180:183], v[48:51], v[24:27], v[180:183]\n"
      "v_mfma_f32_16x16x32_bf16 v[176:179], v[40:43], v[24:27], v[176:179]\nv_mfma_f32_16x16x32_bf16 v[176:179], v[44:47], v[28:31], v[176:179]\n"
      "v_mfma_f32_16x16x32_bf16 v[172:175], v[36:39], v[28:31], v[172:175]\ns_waitcnt vmcnt(0) lgkmcnt(0)\ns_barrier\nv_mfma_f32_16x16x32_bf16 v[172:175], v[32:35], v[24:27], v[172:175]\n"
      "ds_read_b64_tr_b16 v[64:65], v104 offset:45056\nv_mfma_f32_16x16x32_bf16 v[188:191], v[32:35], v[0:3], v[188:191]\nds_read_b64_tr_b16 v[66:67], v104 offset:46080\n"
      "v_mfma_f32_16x16x32_bf16 v[188:191], v[36:39], v[4:7], v[188:191]\nds_read_b64_tr_b16 v[68:69], v104 offset:53248\nv_mfma_f32_16x16x32_bf16 v[192:195], v[44:47], v[4:7], v[192:195]\n"
      "ds_read_b64_tr_b16 v[70:71], v104 offset:54272\nv_mfma_f32_16x16x32_bf16 v[192:195], v[40:43], v[0:3], v[192:195]\nds_read_b64_tr_b16 v[72:73], v105 offset:45056\n"
      "v_mfma_f32_16x16x32_bf16 v[196:199], v[48:51], v[0:3], v[196:199]\nds_read_b64_tr_b16 v[74:75], v105 offset:46080\nv_mfma_f32_16x16x32_bf16 v[196:199], v[52:55], v[4:7], v[196:199]\n"
      "ds_read_b64_tr_b16 v[76:77], v105 offset:53248\nv_mfma_f32_16x16x32_bf16 v[200:203], v[60:63], v[4:7], v[200:203]\nds_read_b64_tr_b16 v[78:79], v105 offset:54272\n"
      "v_mfma_f32_16x16x32_bf16 v[200:203], v[56:59], v[0:3], v[200:203]\nds_read_b64_tr_b16 v[80:81], v106 offset:45056\nv_mfma_f32_16x16x32_bf16 v[216:219], v[56:59], v[8:11], v[216:219]\n"
      "ds_read_b128 v[0:3], v100 offset:45056\nv_mfma_f32_16x16x32_bf16 v[216:219], v[60:63], v[12:15], v[216:219]\nds_read_b128 v[4:7], v101 offset:45056\n"
      "v_mfma_f32_16x16x32_bf16 v[212:215], v[52:55], v[12:15], v[212:215]\nds_read_b64_tr_b16 v[82:83], v106 offset:46080\nv_mfma_f32_16x16x32_bf16 v[212:215], v[48:51], v[8:11], v[212:215]\n"
      "ds_read_b64_tr_b16 v[84:85], v106 offset:53248\nv_mfma_f32_16x16x32_bf16 v[208:211], v[40:43], v[8:11], v[208:211]\nds_read_b64_tr_b16 v[86:87], v106 offset:54272\n"
      "v_mfma_f32_16x16x32_bf16 v[208:211], v[44:47], v[12:15], v[208:211]\nds_read_b64_tr_b16 v[88:89], v107 offset:45056\nv_mfma_f32_16x16x32_bf16 v[204:207], v[36:39], v[12:15], v[204:207]\n"
      "ds_read_b64_tr_b16 v[90:91], v107 offset:46080\nv_mfma_f32_16x16x32_bf16 v[204:207], v[32:35], v[8:11], v[204:207]\nds_read_b64_tr_b16 v[92:93], v107 offset:53248\n"
      "v_mfma_f32_16x16x32_bf16 v[220:223], v[32:35], v[16:19], v[220:223]\nds_read_b128 v[8:11], v120 offset:45056\nv_mfma_f32_16x16x32_bf16 v[220:223], v[36:39], v[20:23], v[220:223]\n"
      "ds_read_b128 v[12:15], v121 offset:45056\nv_mfma_f32_16x16x32_bf16 v[224:227], v[44:47], v[20:23], v[224:227]\nds_read_b64_tr_b16 v[94:95], v107 offset:54272\n"
      "v_mfma_f32_16x16x32_bf16 v[224:227], v[40:43], v[16:19], v[224:227]\nv_mfma_f32_16x16x32_bf16 v[228:231], v[48:51], v[16:19], v[228:231]\ns_sub_u32 s58, s58, 1\ns_cmp_lg_u32 s58, 0\n"
      "v_mfma_f32_16x16x32_bf16 v[228:231], v[52:55], v[20:23], v[228:231]\nv_mfma_f32_16x16x32_bf16 v[232:235], v[60:63], v[20:23], v[232:235]\ns_waitcnt lgkmcnt(0)\n"
      "v_mfma_f32_16x16x32_bf16 v[232:235], v[56:59], v[16:19], v[232:235]\nds_read_b128 v[16:19], v96 offset:45056\nds_read_b128 v[20:23], v97 offset:45056\ns_cbranch_scc1 .Lgl14\n"
      "v_mfma_f32_16x16x32_bf16 v[124:127], v[64:67], v[0:3], v[124:127]\nds_read_b128 v[24:27], v96 offset:47104\nv_cmp_eq_u32 s[62:63], 0, v240\n"
      "v_mfma_f32_16x16x32_bf16 v[124:127], v[68:71], v[4:7], v[124:127]\nds_read_b128 v[28:31], v97 offset:47104\nv_mov_b32 v253, 262144\n"
      "v_mfma_f32_16x16x32_bf16 v[128:131], v[76:79], v[4:7], v[128:131]\nv_mov_b32 v248, 162048\nv_mov_b32 v241, 1\nv_mfma_f32_16x16x32_bf16 v[128:131], v[72:75], v[0:3], v[128:131]\n"
      "v_cndmask_b32 v253, v253, v248, s[62:63]\nv_lshrrev_b32 v112, 4, v240\nv_mfma_f32_16x16x32_bf16 v[132:135], v[80:83], v[0:3], v[132:135]\nv_bfe_u32 v113, v240, 2, 2\n"
      "v_and_b32 v114, 3, v240\nv_mfma_f32_16x16x32_bf16 v[132:135], v[84:87], v[4:7], v[132:135]\nv_mul_u32_u24 v114, 0x80008, v114\nv_add_u32 v244, v112, v113\n"
      "v_mfma_f32_16x16x32_bf16 v[136:139], v[92:95], v[4:7], v[136:139]\nv_and_b32 v244, 3, v244\nv_add_u32 v116, 22, v113\nv_mfma_f32_16x16x32_bf16 v[136:139], v[88:91], v[0:3], v[136:139]\n"
      "v_cmp_gt_u32 s[62:63], 2, v113\nv_and_b32 v113, 60, v240\nv_mfma_f32_16x16x32_bf16 v[152:155], v[88:91], v[8:11], v[152:155]\nds_read_b128 v[0:3], v96 offset:49152\ns_mov_b32 s42, 0\n"
      "v_mfma_f32_16x16x32_bf16 v[152:155], v[92:95], v[12:15], v[152:155]\nds_read_b128 v[4:7], v97 offset:49152\nv_cndmask_b32 v116, v116, v244, s[62:63]\n"
      "v_mfma_f32_16x16x32_bf16 v[148:151], v[84:87], v[12:15], v[148:151]\nv_lshl_or_b32 v244, v244, 16, v116\nv_lshl_add_u32 v244, v244, 5, v114\n"
      "v_mfma_f32_16x16x32_bf16 v[148:151], v[80:83], v[8:11], v[148:151]\nv_lshl_add_u32 v252, v240, 4, s42\ns_mov_b32 s42, 0x76325410\n"
      "v_mfma_f32_16x16x32_bf16 v[144:147], v[72:75], v[8:11], v[144:147]\ns_mov_b32 s43, 0x21005410\nv_lshrrev_b64 v[116:117], v113, s[42:43]\n"
      "v_mfma_f32_16x16x32_bf16 v[144:147], v[76:79], v[12:15], v[144:147]\nv_and_b32 v116, 7, v116\nv_lshl_add_u32 v116, v112, 3, v116\n"
      "v_mfma_f32_16x16x32_bf16 v[140:143], v[68:71], v[12:15], v[140:143]\ns_waitcnt lgkmcnt(4)\ns_mov_b32 s42, 0x54107632\nv_mfma_f32_16x16x32_bf16 v[140:143], v[64:67], v[8:11], v[140:143]\n"
      "s_mov_b32 s43, 0x21107632\nv_lshrrev_b64 v[118:119], v113, s[42:43]\nv_mfma_f32_16x16x32_bf16 v[156:159], v[64:67], v[16:19], v[156:159]\nds_read_b128 v[8:11], v96 offset:51200\n"
      "v_and_b32 v118, 7, v118\nv_mfma_f32_16x16x32_bf16 v[156:159], v[68:71], v[20:23], v[156:159]\nds_read_b128 v[12:15], v97 offset:51200\nv_lshl_add_u32 v118, v112, 3, v118\n"
      "v_mfma_f32_16x16x32_bf16 v[160:163], v[76:79], v[20:23], v[160:163]\nv_lshl_or_b32 v245, v118, 16, v116\nv_lshl_add_u32 v245, v245, 5, v114\n"
      "v_mfma_f32_16x16x32_bf16 v[160:163], v[72:75], v[16:19], v[160:163]\nv_and_b32 v113, 1, v240\nv_mul_u32_u24 v113, 0xaaaa, v113\n"
      "v_mfma_f32_16x16x32_bf16 v[164:167], v[80:83], v[16:19], v[164:167]\nv_add_u32 v242, 0x4444, v113\ns_lshl_b32 s42, s36, 4\n"
      "v_mfma_f32_16x16x32_bf16 v[164:167], v[84:87], v[20:23], v[164:167]\ns_add_u32 s42, s42, 160640\nv_lshl_add_u32 v243, v112, 2, s42\n"
      "v_mfma_f32_16x16x32_bf16 v[168:171], v[92:95], v[20:23], v[168:171]\ns_waitcnt lgkmcnt(2)\nv_and_b32 v112, 1, v112\nv_mfma_f32_16x16x32_bf16 v[168:171], v[88:91], v[16:19], v[168:171]\n"
      "v_lshl_add_u32 v112, v112, 3, s24\ns_sub_u32 s42, s64, 1\nv_mfma_f32_16x16x32_bf16 v[184:187], v[88:91], v[24:27], v[184:187]\nds_read_b128 v[16:19], v96 offset:53248\n"
      "v_min_u32 v112, s42, v112\nv_mfma_f32_16x16x32_bf16 v[184:187], v[92:95], v[28:31], v[184:187]\nds_read_b128 v[20:23], v97 offset:53248\nv_lshlrev_b32 v112, 3, v112\n"
      "v_mfma_f32_16x16x32_bf16 v[180:183], v[84:87], v[28:31], v[180:183]\nv_add_u32 v250, 160256, v112\nv_and_b32 v113, 15, v240\n"
      "v_mfma_f32_16x16x32_bf16 v[180:183], v[80:83], v[24:27], v[180:183]\nv_min_u32 v113, 12, v113\ns_lshl_b32 s42, s30, 7\nv_mfma_f32_16x16x32_bf16 v[176:179], v[72:75], v[24:27], v[176:179]\n"
      "v_lshl_add_u32 v113, v113, 15, s42\nv_add_u32 v249, v113, v112\nv_mfma_f32_16x16x32_bf16 v[176:179], v[76:79], v[28:31], v[176:179]\ns_mul_i32 s65, s36, 22272\ns_add_u32 s65, s65, 45056\n"
      "v_mfma_f32_16x16x32_bf16 v[172:175], v[68:71], v[28:31], v[172:175]\ns_waitcnt vmcnt(0) lgkmcnt(0)\ns_barrier\nv_mfma_f32_16x16x32_bf16 v[172:175], v[64:67], v[24:27], v[172:175]\n"
      "ds_read_b64_tr_b16 v[32:33], v108 offset:0\ns_add_u32 s68, s64, 8\nv_mfma_f32_16x16x32_bf16 v[188:191], v[64:67], v[0:3], v[188:191]\nds_read_b64_tr_b16 v[34:35], v108 offset:1024\n"
      "s_min_u32 s68, s68, 21\nv_mfma_f32_16x16x32_bf16 v[188:191], v[68:71], v[4:7], v[188:191]\nds_read_b64_tr_b16 v[36:37], v108 offset:8192\ns_lshl_b32 s38, s38, 16\n"
      "v_mfma_f32_16x16x32_bf16 v[192:195], v[76:79], v[4:7], v[192:195]\nds_read_b64_tr_b16 v[38:39], v108 offset:9216\ns_lshr_b32 s42, s24, 2\n"
      "v_mfma_f32_16x16x32_bf16 v[192:195], v[72:75], v[0:3], v[192:195]\nds_read_b64_tr_b16 v[40:41], v109 offset:0\ns_mul_i32 s43, s42, 3\n"
      "v_mfma_f32_16x16x32_bf16 v[196:199], v[80:83], v[0:3], v[196:199]\nds_read_b64_tr_b16 v[42:43], v109 offset:1024\ns_xor_b32 s43, s43, s36\n"
      "v_mfma_f32_16x16x32_bf16 v[196:199], v[84:87], v[4:7], v[196:199]\nds_read_b64_tr_b16 v[44:45], v109 offset:8192\ns_add_u32 s42, s42, s64\n"
      "v_mfma_f32_16x16x32_bf16 v[200:203], v[92:95], v[4:7], v[200:203]\nds_read_b64_tr_b16 v[46:47], v109 offset:9216\ns_add_u32 s42, s42, 9\n"
      "v_mfma_f32_16x16x32_bf16 v[200:203], v[88:91], v[0:3], v[200:203]\nds_read_b64_tr_b16 v[48:49], v110 offset:0\ns_and_b32 s42, s42, 1\n"
      "v_mfma_f32_16x16x32_bf16 v[216:219], v[88:91], v[8:11], v[216:219]\nds_read_b128 v[0:3], v102 offset:0\ns_lshl1_add_u32 s67, s43, s42\n"
      "v_mfma_f32_16x16x32_bf16 v[216:219], v[92:95], v[12:15], v[216:219]\nds_read_b128 v[4:7], v103 offset:0\ns_add_u32 s66, s64, s67\n"
      "v_mfma_f32_16x16x32_bf16 v[212:215], v[84:87], v[12:15], v[212:215]\nds_read_b64_tr_b16 v[50:51], v110 offset:1024\ns_cmp_lt_u32 s66, s68\n"
      "v_mfma_f32_16x16x32_bf16 v[212:215], v[80:83], v[8:11], v[212:215]\nds_read_b64_tr_b16 v[52:53], v110 offset:8192\ns_cselect_b32 s66, s66, 127\n"
      "v_mfma_f32_16x16x32_bf16 v[208:211], v[72:75], v[8:11], v[208:211]\nds_read_b64_tr_b16 v[54:55], v110 offset:9216\ns_sub_u32 s66, s66, 8\n"
      "v_mfma_f32_16x16x32_bf16 v[208:211], v[76:79], v[12:15], v[208:211]\nds_read_b64_tr_b16 v[56:57], v111 offset:0\ns_max_i32 s66, s66, 0\n"
      "v_mfma_f32_16x16x32_bf16 v[204:207], v[68:71], v[12:15], v[204:207]\nds_read_b64_tr_b16 v[58:59], v111 offset:1024\ns_getpc_b64 s[34:35]\n"
      ".Lk1064:\nv_mfma_f32_16x16x32_bf16 v[204:207], v[64:67], v[8:11], v[204:207]\nds_read_b64_tr_b16 v[60:61], v111 offset:8192\ns_add_u32 s34, s34, .Lt_g-.Lk1064\n"
      "v_mfma_f32_16x16x32_bf16 v[220:223], v[64:67], v[16:19], v[220:223]\nds_read_b128 v[8:11], v122 offset:0\ns_addc_u32 s35, s35, 0\n"
      "v_mfma_f32_16x16x32_bf16 v[220:223], v[68:71], v[20:23], v[220:223]\nds_read_b128 v[12:15], v123 offset:0\ns_lshr_b32 s42, s2, 3\n"
      "v_mfma_f32_16x16x32_bf16 v[224:227], v[76:79], v[20:23], v[224:227]\nds_read_b64_tr_b16 v[62:63], v111 offset:9216\ns_lshl1_add_u32 s42, s2, s42\n"
      "v_mfma_f32_16x16x32_bf16 v[224:227], v[72:75], v[16:19], v[224:227]\ns_and_b32 s69, s42, 15\ns_lshl_b32 s43, s69, 7\nv_mfma_f32_16x16x32_bf16 v[228:231], v[80:83], v[16:19], v[228:231]\n"
      "s_add_u32 s43, s43, 32768\ns_add_u32 s8, s20, s43\nv_mfma_f32_16x16x32_bf16 v[228:231], v[84:87], v[20:23], v[228:231]\ns_addc_u32 s9, s21, 0\ns_add_u32 s12, s20, 65536\n"
      "v_mfma_f32_16x16x32_bf16 v[232:235], v[92:95], v[20:23], v[232:235]\ns_waitcnt lgkmcnt(0)\ns_addc_u32 s13, s21, 0\nv_mfma_f32_16x16x32_bf16 v[232:235], v[88:91], v[16:19], v[232:235]\n"
      "ds_read_b128 v[16:19], v98 offset:0\nds_read_b128 v[20:23], v99 offset:0\nv_mfma_f32_16x16x32_bf16 v[124:127], v[32:35], v[0:3], v[124:127]\nds_read_b128 v[24:27], v98 offset:2048\n"
      "s_add_u32 s16, s20, 2097152\nv_mfma_f32_16x16x32_bf16 v[124:127], v[36:39], v[4:7], v[124:127]\nds_read_b128 v[28:31], v99 offset:2048\ns_addc_u32 s17, s21, 0\n"
      "v_mfma_f32_16x16x32_bf16 v[128:131], v[44:47], v[4:7], v[128:131]\ns_lshl_b32 s42, s69, 4\ns_max_u32 s42, s42, 24\nv_mfma_f32_16x16x32_bf16 v[128:131], v[40:43], v[0:3], v[128:131]\n"
      "s_sub_u32 s42, s42, 24\ns_min_u32 s69, s42, 192\nv_mfma_f32_16x16x32_bf16 v[132:135], v[48:51], v[0:3], v[132:135]\ns_lshl_b32 s42, s69, 3\ns_add_u32 s42, s42, 24576\n"
      "v_mfma_f32_16x16x32_bf16 v[132:135], v[52:55], v[4:7], v[132:135]\ns_add_u32 s14, s20, s42\ns_addc_u32 s15, s21, 0\nv_mfma_f32_16x16x32_bf16 v[136:139], v[60:63], v[4:7], v[136:139]\n"
      "v_mfma_f32_16x16x32_bf16 v[136:139], v[56:59], v[0:3], v[136:139]\nv_mfma_f32_16x16x32_bf16 v[152:155], v[56:59], v[8:11], v[152:155]\nds_read_b128 v[0:3], v98 offset:4096\n"
      "v_mfma_f32_16x16x32_bf16 v[152:155], v[60:63], v[12:15], v[152:155]\nds_read_b128 v[4:7], v99 offset:4096\nv_mfma_f32_16x16x32_bf16 v[148:151], v[52:55], v[12:15], v[148:151]\n"
      "v_mfma_f32_16x16x32_bf16 v[148:151], v[48:51], v[8:11], v[148:151]\nv_mfma_f32_16x16x32_bf16 v[144:147], v[40:43], v[8:11], v[144:147]\n"
      "v_mfma_f32_16x16x32_bf16 v[144:147], v[44:47], v[12:15], v[144:147]\nv_mfma_f32_16x16x32_bf16 v[140:143], v[36:39], v[12:15], v[140:143]\ns_waitcnt lgkmcnt(4)\n"
      "v_mfma_f32_16x16x32_bf16 v[140:143], v[32:35], v[8:11], v[140:143]\nv_mfma_f32_16x16x32_bf16 v[156:159], v[32:35], v[16:19], v[156:159]\nds_read_b128 v[8:11], v98 offset:6144\n"
      "v_mfma_f32_16x16x32_bf16 v[156:159], v[36:39], v[20:23], v[156:159]\nds_read_b128 v[12:15], v99 offset:6144\nv_mfma_f32_16x16x32_bf16 v[160:163], v[44:47], v[20:23], v[160:163]\n"
      "v_mfma_f32_16x16x32_bf16 v[160:163], v[40:43], v[16:19], v[160:163]\nv_mfma_f32_16x16x32_bf16 v[164:167], v[48:51], v[16:19], v[164:167]\n"
      "v_mfma_f32_16x16x32_bf16 v[164:167], v[52:55], v[20:23], v[164:167]\nv_mfma_f32_16x16x32_bf16 v[168:171], v[60:63], v[20:23], v[168:171]\ns_waitcnt lgkmcnt(2)\n"
      "v_mfma_f32_16x16x32_bf16 v[168:171], v[56:59], v[16:19], v[168:171]\nv_mfma_f32_16x16x32_bf16 v[184:187], v[56:59], v[24:27], v[184:187]\nds_read_b128 v[16:19], v98 offset:8192\n"
      "v_mfma_f32_16x16x32_bf16 v[184:187], v[60:63], v[28:31], v[184:187]\nds_read_b128 v[20:23], v99 offset:8192\nv_mfma_f32_16x16x32_bf16 v[180:183], v[52:55], v[28:31], v[180:183]\n"
      "v_mfma_f32_16x16x32_bf16 v[180:183], v[48:51], v[24:27], v[180:183]\nv_mfma_f32_16x16x32_bf16 v[176:179], v[40:43], v[24:27], v[176:179]\n"
      "v_mfma_f32_16x16x32_bf16 v[176:179], v[44:47], v[28:31], v[176:179]\nv_mfma_f32_16x16x32_bf16 v[172:175], v[36:39], v[28:31], v[172:175]\ns_waitcnt vmcnt(0) lgkmcnt(0)\ns_barrier\n"
      "v_mfma_f32_16x16x32_bf16 v[172:175], v[32:35], v[24:27], v[172:175]\nds_read_b128 v[72:75], v252 offset:0\nv_lshl_add_u32 v246, v240, 1, v248\n"
      "v_mfma_f32_16x16x32_bf16 v[188:191], v[32:35], v[0:3], v[188:191]\nds_read_b128 v[24:27], v236 offset:512\nds_read_u16 v251, v246 offset:64\n"
      "v_mfma_f32_16x16x32_bf16 v[188:191], v[36:39], v[4:7], v[188:191]\nds_read_b128 v[28:31], v236 offset:576\nds_read_b32 v247, v248 offset:24\n"
      "v_mfma_f32_16x16x32_bf16 v[192:195], v[44:47], v[4:7], v[192:195]\nds_read_b128 v[76:79], v252 offset:1024\nv_and_b32 v246, 48, v240\n"
      "v_mfma_f32_16x16x32_bf16 v[192:195], v[40:43], v[0:3], v[192:195]\nds_read_b128 v[80:83], v252 offset:2048\ns_mov_b32 s42, 42240\n"
      "v_mfma_f32_16x16x32_bf16 v[196:199], v[48:51], v[0:3], v[196:199]\nds_read_b128 v[84:87], v252 offset:3072\nv_lshl_add_u32 v246, v246, 2, s42\n"
      "v_mfma_f32_16x16x32_bf16 v[196:199], v[52:55], v[4:7], v[196:199]\nds_read_b128 v[88:91], v252 offset:4096\ns_mov_b32 s58, 0xbdd2d3e7\n"
      "v_mfma_f32_16x16x32_bf16 v[200:203], v[60:63], v[4:7], v[200:203]\nds_read_b128 v[92:95], v252 offset:5120\ns_mov_b32 s59, 0xc0135761\n"
      "v_mfma_f32_16x16x32_bf16 v[200:203], v[56:59], v[0:3], v[200:203]\nds_read_b128 v[96:99], v252 offset:6144\ns_mov_b32 s60, 0xc1e00000\n"
      "v_mfma_f32_16x16x32_bf16 v[216:219], v[56:59], v[8:11], v[216:219]\nds_read_b128 v[0:3], v236 offset:0\ns_add_u32 s49, s65, 1856\n"
      "v_mfma_f32_16x16x32_bf16 v[216:219], v[60:63], v[12:15], v[216:219]\nds_read_b128 v[4:7], v236 offset:64\ns_mov_b32 s45, 2\n"
      "v_mfma_f32_16x16x32_bf16 v[212:215], v[52:55], v[12:15], v[212:215]\nds_read_b128 v[100:103], v252 offset:7168\ns_mov_b32 s46, 0\n"
      "v_mfma_f32_16x16x32_bf16 v[212:215], v[48:51], v[8:11], v[212:215]\nds_read_b128 v[104:107], v252 offset:8192\ns_waitcnt lgkmcnt(8)\n"
      "v_mfma_f32_16x16x32_bf16 v[208:211], v[40:43], v[8:11], v[208:211]\nds_read_b128 v[108:111], v252 offset:9216\nv_readfirstlane_b32 s39, v247\n"
      "v_mfma_f32_16x16x32_bf16 v[208:211], v[44:47], v[12:15], v[208:211]\nds_read_b128 v[64:67], v237 offset:512\nv_readlane_b32 s55, v251, 0\n"
      "v_mfma_f32_16x16x32_bf16 v[204:207], v[36:39], v[12:15], v[204:207]\nds_read_b128 v[68:71], v237 offset:576\ns_setprio 3\nv_mfma_f32_16x16x32_bf16 v[204:207], v[32:35], v[8:11], v[204:207]\n"
      "ds_read_b128 v[112:115], v252 offset:10240\ns_xor_b32 s40, s39, 0xa5c30f96\nv_mfma_f32_16x16x32_bf16 v[220:223], v[32:35], v[16:19], v[220:223]\nds_read_b128 v[8:11], v237 offset:0\n"
      "s_mul_i32 s41, s39, 0x9E3779B1\nv_mfma_f32_16x16x32_bf16 v[220:223], v[36:39], v[20:23], v[220:223]\nds_read_b128 v[12:15], v237 offset:64\ns_lshr_b32 s41, s41, 24\n"
      "v_mfma_f32_16x16x32_bf16 v[224:227], v[44:47], v[20:23], v[224:227]\nds_read_b128 v[116:119], v252 offset:11264\nv_mfma_f32_16x16x32_bf16 v[224:227], v[40:43], v[16:19], v[224:227]\n"
      "ds_read_b128 v[120:123], v252 offset:12288\nv_add_u32_sdwa v239, s49, v245 dst_sel:DWORD dst_unused:UNUSED_PAD src0_sel:DWORD src1_sel:WORD_1\n"
      "v_mfma_f32_16x16x32_bf16 v[228:231], v[48:51], v[16:19], v[228:231]\nv_add_u32_sdwa v236, s49, v244 dst_sel:DWORD dst_unused:UNUSED_PAD src0_sel:DWORD src1_sel:WORD_0\n"
      "v_mfma_f32_16x16x32_bf16 v[228:231], v[52:55], v[20:23], v[228:231]\nv_add_u32_sdwa v237, s49, v244 dst_sel:DWORD dst_unused:UNUSED_PAD src0_sel:DWORD src1_sel:WORD_1\n"
      "v_mfma_f32_16x16x32_bf16 v[232:235], v[60:63], v[20:23], v[232:235]\nv_add_u32_sdwa v238, s49, v245 dst_sel:DWORD dst_unused:UNUSED_PAD src0_sel:DWORD src1_sel:WORD_0\ns_waitcnt lgkmcnt(0)\n"
      "v_mfma_f32_16x16x32_bf16 v[232:235], v[56:59], v[16:19], v[232:235]\nv_pk_mul_f32 v[44:45], v[124:125], v[124:125]\nv_pk_mul_f32 v[46:47], v[126:127], v[126:127]\n"
      "v_pk_mul_f32 v[48:49], v[128:129], v[128:129]\nv_pk_mul_f32 v[50:51], v[130:131], v[130:131]\nv_pk_fma_f32 v[44:45], v[44:45], s[58:59], s[58:59] op_sel:[0,0,1] op_sel_hi:[1,0,1]\n"
      "v_pk_fma_f32 v[46:47], v[46:47], s[58:59], s[58:59] op_sel:[0,0,1] op_sel_hi:[1,0,1]\nv_pk_fma_f32 v[48:49], v[48:49], s[58:59], s[58:59] op_sel:[0,0,1] op_sel_hi:[1,0,1]\n"
      "v_pk_fma_f32 v[50:51], v[50:51], s[58:59], s[58:59] op_sel:[0,0,1] op_sel_hi:[1,0,1]\nv_pk_fma_f32 v[44:45], v[44:45], v[124:125], s[60:61] op_sel_hi:[1,1,0]\n"
      "v_pk_fma_f32 v[46:47], v[46:47], v[126:127], s[60:61] op_sel_hi:[1,1,0]\nv_pk_fma_f32 v[48:49], v[48:49], v[128:129], s[60:61] op_sel_hi:[1,1,0]\n"
      "v_pk_fma_f32 v[50:51], v[50:51], v[130:131], s[60:61] op_sel_hi:[1,1,0]\nv_exp_f32 v44, v44 clamp\nv_pk_mul_f32 v[52:53], v[132:133], v[132:133]\nv_exp_f32 v45, v45 clamp\n"
      "v_pk_mul_f32 v[54:55], v[134:135], v[134:135]\nv_pk_mul_f32 v[56:57], v[136:137], v[136:137]\nv_exp_f32 v46, v46 clamp\nv_pk_mul_f32 v[58:59], v[138:139], v[138:139]\n"
      "v_exp_f32 v47, v47 clamp\nv_pk_fma_f32 v[52:53], v[52:53], s[58:59], s[58:59] op_sel:[0,0,1] op_sel_hi:[1,0,1]\n"
      "v_pk_fma_f32 v[54:55], v[54:55], s[58:59], s[58:59] op_sel:[0,0,1] op_sel_hi:[1,0,1]\nv_exp_f32 v48, v48 clamp\n"
      "v_pk_fma_f32 v[56:57], v[56:57], s[58:59], s[58:59] op_sel:[0,0,1] op_sel_hi:[1,0,1]\nv_exp_f32 v49, v49 clamp\n"
      "v_pk_fma_f32 v[58:59], v[58:59], s[58:59], s[58:59] op_sel:[0,0,1] op_sel_hi:[1,0,1]\nv_pk_fma_f32 v[52:53], v[52:53], v[132:133], s[60:61] op_sel_hi:[1,1,0]\nv_exp_f32 v50, v50 clamp\n"
      "v_pk_fma_f32 v[54:55], v[54:55], v[134:135], s[60:61] op_sel_hi:[1,1,0]\nv_exp_f32 v51, v51 clamp\nv_pk_fma_f32 v[56:57], v[56:57], v[136:137], s[60:61] op_sel_hi:[1,1,0]\n"
      "v_pk_fma_f32 v[58:59], v[58:59], v[138:139], s[60:61] op_sel_hi:[1,1,0]\nv_exp_f32 v52, v52 clamp\nv_pk_fma_f32 v[46:47], v[46:47], v[2:3], v[26:27]\n"
      "v_pk_fma_f32 v[44:45], v[44:45], v[0:1], v[24:25]\nv_pk_fma_f32 v[48:49], v[48:49], v[4:5], v[28:29]\nv_exp_f32 v53, v53 clamp\nv_pk_fma_f32 v[50:51], v[50:51], v[6:7], v[30:31]\n"
      "v_pk_mul_f32 v[124:125], v[124:125], v[46:47]\nv_pk_mul_f32 v[128:129], v[128:129], v[50:51]\nv_exp_f32 v54, v54 clamp\nv_pk_fma_f32 v[124:125], v[126:127], v[44:45], v[124:125]\n"
      "v_pk_fma_f32 v[128:129], v[130:131], v[48:49], v[128:129]\nv_pk_mul_f32 v[44:45], v[44:45], v[46:47]\nv_exp_f32 v55, v55 clamp\nv_pk_mul_f32 v[48:49], v[48:49], v[50:51]\n"
      "v_pk_mul_f32 v[46:47], v[44:45], v[48:49]\nv_rcp_f32 v46, v46\nv_pk_mul_f32 v[124:125], v[124:125], v[48:49]\nv_rcp_f32 v47, v47\nv_pk_fma_f32 v[124:125], v[128:129], v[44:45], v[124:125]\n"
      "v_pk_mul_f32 v[32:33], v[124:125], v[46:47]\nv_pk_mul_f32 v[16:17], v[140:141], v[140:141]\nv_pk_mul_f32 v[18:19], v[142:143], v[142:143]\nv_exp_f32 v56, v56 clamp\n"
      "v_pk_mul_f32 v[20:21], v[144:145], v[144:145]\nv_pk_mul_f32 v[22:23], v[146:147], v[146:147]\nv_exp_f32 v57, v57 clamp\n"
      "v_pk_fma_f32 v[16:17], v[16:17], s[58:59], s[58:59] op_sel:[0,0,1] op_sel_hi:[1,0,1]\nv_pk_fma_f32 v[18:19], v[18:19], s[58:59], s[58:59] op_sel:[0,0,1] op_sel_hi:[1,0,1]\n"
      "v_pk_fma_f32 v[20:21], v[20:21], s[58:59], s[58:59] op_sel:[0,0,1] op_sel_hi:[1,0,1]\nv_exp_f32 v58, v58 clamp\n"
      "v_pk_fma_f32 v[22:23], v[22:23], s[58:59], s[58:59] op_sel:[0,0,1] op_sel_hi:[1,0,1]\nv_pk_fma_f32 v[16:17], v[16:17], v[140:141], s[60:61] op_sel_hi:[1,1,0]\n"
      "v_pk_fma_f32 v[18:19], v[18:19], v[142:143], s[60:61] op_sel_hi:[1,1,0]\nv_exp_f32 v59, v59 clamp\nv_pk_fma_f32 v[20:21], v[20:21], v[144:145], s[60:61] op_sel_hi:[1,1,0]\n"
      "v_pk_fma_f32 v[22:23], v[22:23], v[146:147], s[60:61] op_sel_hi:[1,1,0]\nv_exp_f32 v16, v16 clamp\nv_pk_fma_f32 v[54:55], v[54:55], v[10:11], v[66:67]\n"
      "v_pk_fma_f32 v[52:53], v[52:53], v[8:9], v[64:65]\nv_pk_fma_f32 v[56:57], v[56:57], v[12:13], v[68:69]\nv_exp_f32 v17, v17 clamp\nv_pk_fma_f32 v[58:59], v[58:59], v[14:15], v[70:71]\n"
      "v_pk_mul_f32 v[132:133], v[132:133], v[54:55]\nv_pk_mul_f32 v[136:137], v[136:137], v[58:59]\nv_exp_f32 v18, v18 clamp\nv_pk_fma_f32 v[132:133], v[134:135], v[52:53], v[132:133]\n"
      "v_pk_fma_f32 v[136:137], v[138:139], v[56:57], v[136:137]\nv_pk_mul_f32 v[52:53], v[52:53], v[54:55]\nv_exp_f32 v19, v19 clamp\nv_pk_mul_f32 v[56:57], v[56:57], v[58:59]\n"
      "v_pk_mul_f32 v[54:55], v[52:53], v[56:57]\nv_rcp_f32 v54, v54\nv_pk_mul_f32 v[132:133], v[132:133], v[56:57]\nv_rcp_f32 v55, v55\nv_pk_fma_f32 v[132:133], v[136:137], v[52:53], v[132:133]\n"
      "v_pk_fma_f32 v[32:33], v[132:133], v[54:55], v[32:33]\nv_add_f32 v42, v32, v33\nv_pk_mul_f32 v[44:45], v[148:149], v[148:149]\nv_pk_mul_f32 v[46:47], v[150:151], v[150:151]\n"
      "v_exp_f32 v20, v20 clamp\nv_pk_mul_f32 v[48:49], v[152:153], v[152:153]\nv_pk_mul_f32 v[50:51], v[154:155], v[154:155]\nv_exp_f32 v21, v21 clamp\n"
      "v_pk_fma_f32 v[44:45], v[44:45], s[58:59], s[58:59] op_sel:[0,0,1] op_sel_hi:[1,0,1]\nv_pk_fma_f32 v[46:47], v[46:47], s[58:59], s[58:59] op_sel:[0,0,1] op_sel_hi:[1,0,1]\n"
      "v_pk_fma_f32 v[48:49], v[48:49], s[58:59], s[58:59] op_sel:[0,0,1] op_sel_hi:[1,0,1]\nv_exp_f32 v22, v22 clamp\n"
      "v_pk_fma_f32 v[50:51], v[50:51], s[58:59], s[58:59] op_sel:[0,0,1] op_sel_hi:[1,0,1]\nv_pk_fma_f32 v[44:45], v[44:45], v[148:149], s[60:61] op_sel_hi:[1,1,0]\n"
      "v_pk_fma_f32 v[46:47], v[46:47], v[150:151], s[60:61] op_sel_hi:[1,1,0]\nv_exp_f32 v23, v23 clamp\nv_pk_fma_f32 v[48:49], v[48:49], v[152:153], s[60:61] op_sel_hi:[1,1,0]\n"
      "v_pk_fma_f32 v[50:51], v[50:51], v[154:155], s[60:61] op_sel_hi:[1,1,0]\nv_exp_f32 v44, v44 clamp\nv_pk_fma_f32 v[18:19], v[18:19], v[2:3], v[26:27]\n"
      "v_pk_fma_f32 v[16:17], v[16:17], v[0:1], v[24:25]\nv_pk_fma_f32 v[20:21], v[20:21], v[4:5], v[28:29]\nv_exp_f32 v45, v45 clamp\nv_pk_fma_f32 v[22:23], v[22:23], v[6:7], v[30:31]\n"
      "v_pk_mul_f32 v[140:141], v[140:141], v[18:19]\nv_pk_mul_f32 v[144:145], v[144:145], v[22:23]\nv_exp_f32 v46, v46 clamp\nv_pk_fma_f32 v[140:141], v[142:143], v[16:17], v[140:141]\n"
      "v_pk_fma_f32 v[144:145], v[146:147], v[20:21], v[144:145]\nv_pk_mul_f32 v[16:17], v[16:17], v[18:19]\nv_exp_f32 v47, v47 clamp\nv_pk_mul_f32 v[20:21], v[20:21], v[22:23]\n"
      "v_pk_mul_f32 v[18:19], v[16:17], v[20:21]\nv_rcp_f32 v18, v18\nv_pk_mul_f32 v[140:141], v[140:141], v[20:21]\nv_rcp_f32 v19, v19\nv_pk_fma_f32 v[140:141], v[144:145], v[16:17], v[140:141]\n"
      "v_pk_mul_f32 v[32:33], v[140:141], v[18:19]\nv_pk_mul_f32 v[52:53], v[156:157], v[156:157]\nv_pk_mul_f32 v[54:55], v[158:159], v[158:159]\nv_exp_f32 v48, v48 clamp\n"
      "v_pk_mul_f32 v[56:57], v[160:161], v[160:161]\nv_pk_mul_f32 v[58:59], v[162:163], v[162:163]\nv_exp_f32 v49, v49 clamp\n"
      "v_pk_fma_f32 v[52:53], v[52:53], s[58:59], s[58:59] op_sel:[0,0,1] op_sel_hi:[1,0,1]\nv_pk_fma_f32 v[54:55], v[54:55], s[58:59], s[58:59] op_sel:[0,0,1] op_sel_hi:[1,0,1]\n"
      "v_pk_fma_f32 v[56:57], v[56:57], s[58:59], s[58:59] op_sel:[0,0,1] op_sel_hi:[1,0,1]\nv_exp_f32 v50, v50 clamp\n"
      "v_pk_fma_f32 v[58:59], v[58:59], s[58:59], s[58:59] op_sel:[0,0,1] op_sel_hi:[1,0,1]\nv_pk_fma_f32 v[52:53], v[52:53], v[156:157], s[60:61] op_sel_hi:[1,1,0]\n"
      "v_pk_fma_f32 v[54:55], v[54:55], v[158:159], s[60:61] op_sel_hi:[1,1,0]\nv_exp_f32 v51, v51 clamp\nv_pk_fma_f32 v[56:57], v[56:57], v[160:161], s[60:61] op_sel_hi:[1,1,0]\n"
      "v_pk_fma_f32 v[58:59], v[58:59], v[162:163], s[60:61] op_sel_hi:[1,1,0]\nv_exp_f32 v52, v52 clamp\nv_pk_fma_f32 v[46:47], v[46:47], v[10:11], v[66:67]\n"
      "v_pk_fma_f32 v[44:45], v[44:45], v[8:9], v[64:65]\nv_pk_fma_f32 v[48:49], v[48:49], v[12:13], v[68:69]\nv_exp_f32 v53, v53 clamp\nv_pk_fma_f32 v[50:51], v[50:51], v[14:15], v[70:71]\n"
      "v_pk_mul_f32 v[148:149], v[148:149], v[46:47]\nv_pk_mul_f32 v[152:153], v[152:153], v[50:51]\nv_exp_f32 v54, v54 clamp\nv_pk_fma_f32 v[148:149], v[150:151], v[44:45], v[148:149]\n"
      "v_pk_fma_f32 v[152:153], v[154:155], v[48:49], v[152:153]\nv_pk_mul_f32 v[44:45], v[44:45], v[46:47]\nv_exp_f32 v55, v55 clamp\nv_pk_mul_f32 v[48:49], v[48:49], v[50:51]\n"
      "v_pk_mul_f32 v[46:47], v[44:45], v[48:49]\nv_rcp_f32 v46, v46\nv_pk_mul_f32 v[148:149], v[148:149], v[48:49]\nv_rcp_f32 v47, v47\nv_pk_fma_f32 v[148:149], v[152:153], v[44:45], v[148:149]\n"
      "v_pk_fma_f32 v[32:33], v[148:149], v[46:47], v[32:33]\nv_add_f32 v41, v32, v33\nv_pk_mul_f32 v[16:17], v[164:165], v[164:165]\nv_pk_mul_f32 v[18:19], v[166:167], v[166:167]\n"
      "v_exp_f32 v56, v56 clamp\nv_pk_mul_f32 v[20:21], v[168:169], v[168:169]\nv_pk_mul_f32 v[22:23], v[170:171], v[170:171]\nv_exp_f32 v57, v57 clamp\n"
      "v_pk_fma_f32 v[16:17], v[16:17], s[58:59], s[58:59] op_sel:[0,0,1] op_sel_hi:[1,0,1]\nv_pk_fma_f32 v[18:19], v[18:19], s[58:59], s[58:59] op_sel:[0,0,1] op_sel_hi:[1,0,1]\n"
      "v_pk_fma_f32 v[20:21], v[20:21], s[58:59], s[58:59] op_sel:[0,0,1] op_sel_hi:[1,0,1]\nv_exp_f32 v58, v58 clamp\n"
      "v_pk_fma_f32 v[22:23], v[22:23], s[58:59], s[58:59] op_sel:[0,0,1] op_sel_hi:[1,0,1]\nv_pk_fma_f32 v[16:17], v[16:17], v[164:165], s[60:61] op_sel_hi:[1,1,0]\n"
      "v_pk_fma_f32 v[18:19], v[18:19], v[166:167], s[60:61] op_sel_hi:[1,1,0]\nv_exp_f32 v59, v59 clamp\nv_pk_fma_f32 v[20:21], v[20:21], v[168:169], s[60:61] op_sel_hi:[1,1,0]\n"
      "v_pk_fma_f32 v[22:23], v[22:23], v[170:171], s[60:61] op_sel_hi:[1,1,0]\nv_exp_f32 v16, v16 clamp\nv_pk_fma_f32 v[54:55], v[54:55], v[2:3], v[26:27]\n"
      "v_pk_fma_f32 v[52:53], v[52:53], v[0:1], v[24:25]\nv_pk_fma_f32 v[56:57], v[56:57], v[4:5], v[28:29]\nv_exp_f32 v17, v17 clamp\nv_pk_fma_f32 v[58:59], v[58:59], v[6:7], v[30:31]\n"
      "v_pk_mul_f32 v[156:157], v[156:157], v[54:55]\nv_pk_mul_f32 v[160:161], v[160:161], v[58:59]\nv_exp_f32 v18, v18 clamp\nv_pk_fma_f32 v[156:157], v[158:159], v[52:53], v[156:157]\n"
      "v_pk_fma_f32 v[160:161], v[162:163], v[56:57], v[160:161]\nv_pk_mul_f32 v[52:53], v[52:53], v[54:55]\nv_exp_f32 v19, v19 clamp\nv_pk_mul_f32 v[56:57], v[56:57], v[58:59]\n"
      "v_pk_mul_f32 v[54:55], v[52:53], v[56:57]\nv_rcp_f32 v54, v54\nv_pk_mul_f32 v[156:157], v[156:157], v[56:57]\nv_rcp_f32 v55, v55\nv_pk_fma_f32 v[156:157], v[160:161], v[52:53], v[156:157]\n"
      "v_pk_mul_f32 v[32:33], v[156:157], v[54:55]\nv_pk_mul_f32 v[44:45], v[172:173], v[172:173]\nv_pk_mul_f32 v[46:47], v[174:175], v[174:175]\nv_exp_f32 v20, v20 clamp\n"
      "v_pk_mul_f32 v[48:49], v[176:177], v[176:177]\nv_pk_mul_f32 v[50:51], v[178:179], v[178:179]\nv_exp_f32 v21, v21 clamp\n"
      "v_pk_fma_f32 v[44:45], v[44:45], s[58:59], s[58:59] op_sel:[0,0,1] op_sel_hi:[1,0,1]\nv_pk_fma_f32 v[46:47], v[46:47], s[58:59], s[58:59] op_sel:[0,0,1] op_sel_hi:[1,0,1]\n"
      "v_pk_fma_f32 v[48:49], v[48:49], s[58:59], s[58:59] op_sel:[0,0,1] op_sel_hi:[1,0,1]\nv_exp_f32 v22, v22 clamp\n"
      "v_pk_fma_f32 v[50:51], v[50:51], s[58:59], s[58:59] op_sel:[0,0,1] op_sel_hi:[1,0,1]\nv_pk_fma_f32 v[44:45], v[44:45], v[172:173], s[60:61] op_sel_hi:[1,1,0]\n"
      "v_pk_fma_f32 v[46:47], v[46:47], v[174:175], s[60:61] op_sel_hi:[1,1,0]\nv_exp_f32 v23, v23 clamp\nv_pk_fma_f32 v[48:49], v[48:49], v[176:177], s[60:61] op_sel_hi:[1,1,0]\n"
      "v_pk_fma_f32 v[50:51], v[50:51], v[178:179], s[60:61] op_sel_hi:[1,1,0]\nv_exp_f32 v44, v44 clamp\nv_pk_fma_f32 v[18:19], v[18:19], v[10:11], v[66:67]\n"
      "v_pk_fma_f32 v[16:17], v[16:17], v[8:9], v[64:65]\nv_pk_fma_f32 v[20:21], v[20:21], v[12:13], v[68:69]\nv_exp_f32 v45, v45 clamp\nv_pk_fma_f32 v[22:23], v[22:23], v[14:15], v[70:71]\n"
      "v_pk_mul_f32 v[164:165], v[164:165], v[18:19]\nv_pk_mul_f32 v[168:169], v[168:169], v[22:23]\nv_exp_f32 v46, v46 clamp\nv_pk_fma_f32 v[164:165], v[166:167], v[16:17], v[164:165]\n"
      "v_pk_fma_f32 v[168:169], v[170:171], v[20:21], v[168:169]\nv_pk_mul_f32 v[16:17], v[16:17], v[18:19]\nv_exp_f32 v47, v47 clamp\nv_pk_mul_f32 v[20:21], v[20:21], v[22:23]\n"
      "v_pk_mul_f32 v[18:19], v[16:17], v[20:21]\nv_rcp_f32 v18, v18\nv_pk_mul_f32 v[164:165], v[164:165], v[20:21]\nv_rcp_f32 v19, v19\nv_pk_fma_f32 v[164:165], v[168:169], v[16:17], v[164:165]\n"
      "v_pk_fma_f32 v[32:33], v[164:165], v[18:19], v[32:33]\nv_add_f32 v36, v32, v33\nv_pk_mul_f32 v[52:53], v[180:181], v[180:181]\nv_pk_mul_f32 v[54:55], v[182:183], v[182:183]\n"
      "v_exp_f32 v48, v48 clamp\nv_pk_mul_f32 v[56:57], v[184:185], v[184:185]\nv_pk_mul_f32 v[58:59], v[186:187], v[186:187]\nv_exp_f32 v49, v49 clamp\n"
      "v_pk_fma_f32 v[52:53], v[52:53], s[58:59], s[58:59] op_sel:[0,0,1] op_sel_hi:[1,0,1]\nv_pk_fma_f32 v[54:55], v[54:55], s[58:59], s[58:59] op_sel:[0,0,1] op_sel_hi:[1,0,1]\n"
      "v_pk_fma_f32 v[56:57], v[56:57], s[58:59], s[58:59] op_sel:[0,0,1] op_sel_hi:[1,0,1]\nv_exp_f32 v50, v50 clamp\n"
      "v_pk_fma_f32 v[58:59], v[58:59], s[58:59], s[58:59] op_sel:[0,0,1] op_sel_hi:[1,0,1]\nv_pk_fma_f32 v[52:53], v[52:53], v[180:181], s[60:61] op_sel_hi:[1,1,0]\n"
      "v_pk_fma_f32 v[54:55], v[54:55], v[182:183], s[60:61] op_sel_hi:[1,1,0]\nv_exp_f32 v51, v51 clamp\nv_pk_fma_f32 v[56:57], v[56:57], v[184:185], s[60:61] op_sel_hi:[1,1,0]\n"
      "v_pk_fma_f32 v[58:59], v[58:59], v[186:187], s[60:61] op_sel_hi:[1,1,0]\nv_exp_f32 v52, v52 clamp\nv_pk_fma_f32 v[46:47], v[46:47], v[2:3], v[26:27]\n"
      "v_pk_fma_f32 v[44:45], v[44:45], v[0:1], v[24:25]\nv_pk_fma_f32 v[48:49], v[48:49], v[4:5], v[28:29]\nv_exp_f32 v53, v53 clamp\nv_pk_fma_f32 v[50:51], v[50:51], v[6:7], v[30:31]\n"
      "v_pk_mul_f32 v[172:173], v[172:173], v[46:47]\nv_pk_mul_f32 v[176:177], v[176:177], v[50:51]\nv_exp_f32 v54, v54 clamp\nv_pk_fma_f32 v[172:173], v[174:175], v[44:45], v[172:173]\n"
      "v_pk_fma_f32 v[176:177], v[178:179], v[48:49], v[176:177]\nv_pk_mul_f32 v[44:45], v[44:45], v[46:47]\nv_exp_f32 v55, v55 clamp\nv_pk_mul_f32 v[48:49], v[48:49], v[50:51]\n"
      "v_pk_mul_f32 v[46:47], v[44:45], v[48:49]\nv_rcp_f32 v46, v46\nv_pk_mul_f32 v[172:173], v[172:173], v[48:49]\nv_rcp_f32 v47, v47\nv_pk_fma_f32 v[172:173], v[176:177], v[44:45], v[172:173]\n"
      "v_pk_mul_f32 v[32:33], v[172:173], v[46:47]\nv_pk_mul_f32 v[16:17], v[188:189], v[188:189]\nv_pk_mul_f32 v[18:19], v[190:191], v[190:191]\nv_exp_f32 v56, v56 clamp\n"
      "v_pk_mul_f32 v[20:21], v[192:193], v[192:193]\nv_pk_mul_f32 v[22:23], v[194:195], v[194:195]\nv_exp_f32 v57, v57 clamp\n"
      "v_pk_fma_f32 v[16:17], v[16:17], s[58:59], s[58:59] op_sel:[0,0,1] op_sel_hi:[1,0,1]\nv_pk_fma_f32 v[18:19], v[18:19], s[58:59], s[58:59] op_sel:[0,0,1] op_sel_hi:[1,0,1]\n"
      "v_pk_fma_f32 v[20:21], v[20:21], s[58:59], s[58:59] op_sel:[0,0,1] op_sel_hi:[1,0,1]\nv_exp_f32 v58, v58 clamp\n"
      "v_pk_fma_f32 v[22:23], v[22:23], s[58:59], s[58:59] op_sel:[0,0,1] op_sel_hi:[1,0,1]\nv_pk_fma_f32 v[16:17], v[16:17], v[188:189], s[60:61] op_sel_hi:[1,1,0]\n"
      "v_pk_fma_f32 v[18:19], v[18:19], v[190:191], s[60:61] op_sel_hi:[1,1,0]\nv_exp_f32 v59, v59 clamp\nv_pk_fma_f32 v[20:21], v[20:21], v[192:193], s[60:61] op_sel_hi:[1,1,0]\n"
      "v_pk_fma_f32 v[22:23], v[22:23], v[194:195], s[60:61] op_sel_hi:[1,1,0]\nv_exp_f32 v16, v16 clamp\nv_pk_fma_f32 v[54:55], v[54:55], v[10:11], v[66:67]\n"
      "v_pk_fma_f32 v[52:53], v[52:53], v[8:9], v[64:65]\nv_pk_fma_f32 v[56:57], v[56:57], v[12:13], v[68:69]\nv_exp_f32 v17, v17 clamp\nv_pk_fma_f32 v[58:59], v[58:59], v[14:15], v[70:71]\n"
      "v_pk_mul_f32 v[180:181], v[180:181], v[54:55]\nv_pk_mul_f32 v[184:185], v[184:185], v[58:59]\nv_exp_f32 v18, v18 clamp\nv_pk_fma_f32 v[180:181], v[182:183], v[52:53], v[180:181]\n"
      "v_pk_fma_f32 v[184:185], v[186:187], v[56:57], v[184:185]\nv_pk_mul_f32 v[52:53], v[52:53], v[54:55]\nv_exp_f32 v19, v19 clamp\nv_pk_mul_f32 v[56:57], v[56:57], v[58:59]\n"
      "v_pk_mul_f32 v[54:55], v[52:53], v[56:57]\nv_rcp_f32 v54, v54\nv_pk_mul_f32 v[180:181], v[180:181], v[56:57]\nv_rcp_f32 v55, v55\nv_pk_fma_f32 v[180:181], v[184:185], v[52:53], v[180:181]\n"
      "v_pk_fma_f32 v[32:33], v[180:181], v[54:55], v[32:33]\nv_add_f32 v37, v32, v33\nv_pk_mul_f32 v[44:45], v[196:197], v[196:197]\nv_pk_mul_f32 v[46:47], v[198:199], v[198:199]\n"
      "v_exp_f32 v20, v20 clamp\nv_pk_mul_f32 v[48:49], v[200:201], v[200:201]\nv_pk_mul_f32 v[50:51], v[202:203], v[202:203]\nv_exp_f32 v21, v21 clamp\n"
      "v_pk_fma_f32 v[44:45], v[44:45], s[58:59], s[58:59] op_sel:[0,0,1] op_sel_hi:[1,0,1]\nv_pk_fma_f32 v[46:47], v[46:47], s[58:59], s[58:59] op_sel:[0,0,1] op_sel_hi:[1,0,1]\n"
      "v_pk_fma_f32 v[48:49], v[48:49], s[58:59], s[58:59] op_sel:[0,0,1] op_sel_hi:[1,0,1]\nv_exp_f32 v22, v22 clamp\n"
      "v_pk_fma_f32 v[50:51], v[50:51], s[58:59], s[58:59] op_sel:[0,0,1] op_sel_hi:[1,0,1]\nv_pk_fma_f32 v[44:45], v[44:45], v[196:197], s[60:61] op_sel_hi:[1,1,0]\n"
      "v_pk_fma_f32 v[46:47], v[46:47], v[198:199], s[60:61] op_sel_hi:[1,1,0]\nv_exp_f32 v23, v23 clamp\nv_pk_fma_f32 v[48:49], v[48:49], v[200:201], s[60:61] op_sel_hi:[1,1,0]\n"
      "v_pk_fma_f32 v[50:51], v[50:51], v[202:203], s[60:61] op_sel_hi:[1,1,0]\nv_exp_f32 v44, v44 clamp\nv_pk_fma_f32 v[18:19], v[18:19], v[2:3], v[26:27]\n"
      "v_pk_fma_f32 v[16:17], v[16:17], v[0:1], v[24:25]\nv_pk_fma_f32 v[20:21], v[20:21], v[4:5], v[28:29]\nv_exp_f32 v45, v45 clamp\nv_pk_fma_f32 v[22:23], v[22:23], v[6:7], v[30:31]\n"
      "v_pk_mul_f32 v[188:189], v[188:189], v[18:19]\nv_pk_mul_f32 v[192:193], v[192:193], v[22:23]\nv_exp_f32 v46, v46 clamp\nv_pk_fma_f32 v[188:189], v[190:191], v[16:17], v[188:189]\n"
      "v_pk_fma_f32 v[192:193], v[194:195], v[20:21], v[192:193]\nv_pk_mul_f32 v[16:17], v[16:17], v[18:19]\nv_exp_f32 v47, v47 clamp\nv_pk_mul_f32 v[20:21], v[20:21], v[22:23]\n"
      "v_pk_mul_f32 v[18:19], v[16:17], v[20:21]\nv_rcp_f32 v18, v18\nv_pk_mul_f32 v[188:189], v[188:189], v[20:21]\nv_rcp_f32 v19, v19\nv_pk_fma_f32 v[188:189], v[192:193], v[16:17], v[188:189]\n"
      "v_pk_mul_f32 v[32:33], v[188:189], v[18:19]\nv_pk_mul_f32 v[52:53], v[204:205], v[204:205]\nv_pk_mul_f32 v[54:55], v[206:207], v[206:207]\nv_exp_f32 v48, v48 clamp\n"
      "v_pk_mul_f32 v[56:57], v[208:209], v[208:209]\nv_pk_mul_f32 v[58:59], v[210:211], v[210:211]\nv_exp_f32 v49, v49 clamp\n"
      "v_pk_fma_f32 v[52:53], v[52:53], s[58:59], s[58:59] op_sel:[0,0,1] op_sel_hi:[1,0,1]\nv_pk_fma_f32 v[54:55], v[54:55], s[58:59], s[58:59] op_sel:[0,0,1] op_sel_hi:[1,0,1]\n"
      "v_pk_fma_f32 v[56:57], v[56:57], s[58:59], s[58:59] op_sel:[0,0,1] op_sel_hi:[1,0,1]\nv_exp_f32 v50, v50 clamp\n"
      "v_pk_fma_f32 v[58:59], v[58:59], s[58:59], s[58:59] op_sel:[0,0,1] op_sel_hi:[1,0,1]\nv_pk_fma_f32 v[52:53], v[52:53], v[204:205], s[60:61] op_sel_hi:[1,1,0]\n"
      "v_pk_fma_f32 v[54:55], v[54:55], v[206:207], s[60:61] op_sel_hi:[1,1,0]\nv_exp_f32 v51, v51 clamp\nv_pk_fma_f32 v[56:57], v[56:57], v[208:209], s[60:61] op_sel_hi:[1,1,0]\n"
      "v_pk_fma_f32 v[58:59], v[58:59], v[210:211], s[60:61] op_sel_hi:[1,1,0]\nv_exp_f32 v52, v52 clamp\nv_pk_fma_f32 v[46:47], v[46:47], v[10:11], v[66:67]\n"
      "v_pk_fma_f32 v[44:45], v[44:45], v[8:9], v[64:65]\nv_pk_fma_f32 v[48:49], v[48:49], v[12:13], v[68:69]\nv_exp_f32 v53, v53 clamp\nv_pk_fma_f32 v[50:51], v[50:51], v[14:15], v[70:71]\n"
      "v_pk_mul_f32 v[196:197], v[196:197], v[46:47]\nv_pk_mul_f32 v[200:201], v[200:201], v[50:51]\nv_exp_f32 v54, v54 clamp\nv_pk_fma_f32 v[196:197], v[198:199], v[44:45], v[196:197]\n"
      "v_pk_fma_f32 v[200:201], v[202:203], v[48:49], v[200:201]\nv_pk_mul_f32 v[44:45], v[44:45], v[46:47]\nv_exp_f32 v55, v55 clamp\nv_pk_mul_f32 v[48:49], v[48:49], v[50:51]\n"
      "v_pk_mul_f32 v[46:47], v[44:45], v[48:49]\nv_rcp_f32 v46, v46\nv_pk_mul_f32 v[196:197], v[196:197], v[48:49]\nv_rcp_f32 v47, v47\nv_pk_fma_f32 v[196:197], v[200:201], v[44:45], v[196:197]\n"
      "v_pk_fma_f32 v[32:33], v[196:197], v[46:47], v[32:33]\nv_add_f32 v38, v32, v33\nv_pk_mul_f32 v[16:17], v[212:213], v[212:213]\nv_pk_mul_f32 v[18:19], v[214:215], v[214:215]\n"
      "v_exp_f32 v56, v56 clamp\nv_pk_mul_f32 v[20:21], v[216:217], v[216:217]\nv_pk_mul_f32 v[22:23], v[218:219], v[218:219]\nv_exp_f32 v57, v57 clamp\n"
      "v_pk_fma_f32 v[16:17], v[16:17], s[58:59], s[58:59] op_sel:[0,0,1] op_sel_hi:[1,0,1]\nv_pk_fma_f32 v[18:19], v[18:19], s[58:59], s[58:59] op_sel:[0,0,1] op_sel_hi:[1,0,1]\n"
      "v_pk_fma_f32 v[20:21], v[20:21], s[58:59], s[58:59] op_sel:[0,0,1] op_sel_hi:[1,0,1]\nv_exp_f32 v58, v58 clamp\n"
      "v_pk_fma_f32 v[22:23], v[22:23], s[58:59], s[58:59] op_sel:[0,0,1] op_sel_hi:[1,0,1]\nv_pk_fma_f32 v[16:17], v[16:17], v[212:213], s[60:61] op_sel_hi:[1,1,0]\n"
      "v_pk_fma_f32 v[18:19], v[18:19], v[214:215], s[60:61] op_sel_hi:[1,1,0]\nv_exp_f32 v59, v59 clamp\nv_pk_fma_f32 v[20:21], v[20:21], v[216:217], s[60:61] op_sel_hi:[1,1,0]\n"
      "v_pk_fma_f32 v[22:23], v[22:23], v[218:219], s[60:61] op_sel_hi:[1,1,0]\nv_exp_f32 v16, v16 clamp\nv_pk_fma_f32 v[54:55], v[54:55], v[2:3], v[26:27]\n"
      "v_pk_fma_f32 v[52:53], v[52:53], v[0:1], v[24:25]\nv_pk_fma_f32 v[56:57], v[56:57], v[4:5], v[28:29]\nv_exp_f32 v17, v17 clamp\nv_pk_fma_f32 v[58:59], v[58:59], v[6:7], v[30:31]\n"
      "v_pk_mul_f32 v[204:205], v[204:205], v[54:55]\nv_pk_mul_f32 v[208:209], v[208:209], v[58:59]\nv_exp_f32 v18, v18 clamp\nv_pk_fma_f32 v[204:205], v[206:207], v[52:53], v[204:205]\n"
      "v_pk_fma_f32 v[208:209], v[210:211], v[56:57], v[208:209]\nv_pk_mul_f32 v[52:53], v[52:53], v[54:55]\nv_exp_f32 v19, v19 clamp\nv_pk_mul_f32 v[56:57], v[56:57], v[58:59]\n"
      "v_pk_mul_f32 v[54:55], v[52:53], v[56:57]\nv_rcp_f32 v54, v54\nv_pk_mul_f32 v[204:205], v[204:205], v[56:57]\nv_rcp_f32 v55, v55\nv_pk_fma_f32 v[204:205], v[208:209], v[52:53], v[204:205]\n"
      "v_pk_mul_f32 v[32:33], v[204:205], v[54:55]\nv_pk_mul_f32 v[44:45], v[220:221], v[220:221]\nv_pk_mul_f32 v[46:47], v[222:223], v[222:223]\nv_exp_f32 v20, v20 clamp\n"
      "v_pk_mul_f32 v[48:49], v[224:225], v[224:225]\nv_pk_mul_f32 v[50:51], v[226:227], v[226:227]\nv_exp_f32 v21, v21 clamp\n"
      "v_pk_fma_f32 v[44:45], v[44:45], s[58:59], s[58:59] op_sel:[0,0,1] op_sel_hi:[1,0,1]\nv_pk_fma_f32 v[46:47], v[46:47], s[58:59], s[58:59] op_sel:[0,0,1] op_sel_hi:[1,0,1]\n"
      "v_pk_fma_f32 v[48:49], v[48:49], s[58:59], s[58:59] op_sel:[0,0,1] op_sel_hi:[1,0,1]\nv_exp_f32 v22, v22 clamp\n"
      "v_pk_fma_f32 v[50:51], v[50:51], s[58:59], s[58:59] op_sel:[0,0,1] op_sel_hi:[1,0,1]\nv_pk_fma_f32 v[44:45], v[44:45], v[220:221], s[60:61] op_sel_hi:[1,1,0]\n"
      "v_pk_fma_f32 v[46:47], v[46:47], v[222:223], s[60:61] op_sel_hi:[1,1,0]\nv_exp_f32 v23, v23 clamp\nv_pk_fma_f32 v[48:49], v[48:49], v[224:225], s[60:61] op_sel_hi:[1,1,0]\n"
      "v_pk_fma_f32 v[50:51], v[50:51], v[226:227], s[60:61] op_sel_hi:[1,1,0]\nv_exp_f32 v44, v44 clamp\nv_pk_fma_f32 v[18:19], v[18:19], v[10:11], v[66:67]\n"
      "v_pk_fma_f32 v[16:17], v[16:17], v[8:9], v[64:65]\nv_pk_fma_f32 v[20:21], v[20:21], v[12:13], v[68:69]\nv_exp_f32 v45, v45 clamp\nv_pk_fma_f32 v[22:23], v[22:23], v[14:15], v[70:71]\n"
      "v_pk_mul_f32 v[212:213], v[212:213], v[18:19]\nv_pk_mul_f32 v[216:217], v[216:217], v[22:23]\nv_exp_f32 v46, v46 clamp\nv_pk_fma_f32 v[212:213], v[214:215], v[16:17], v[212:213]\n"
      "v_pk_fma_f32 v[216:217], v[218:219], v[20:21], v[216:217]\nv_pk_mul_f32 v[16:17], v[16:17], v[18:19]\nv_exp_f32 v47, v47 clamp\nv_pk_mul_f32 v[20:21], v[20:21], v[22:23]\n"
      "v_pk_mul_f32 v[18:19], v[16:17], v[20:21]\nv_rcp_f32 v18, v18\nv_pk_mul_f32 v[212:213], v[212:213], v[20:21]\nv_rcp_f32 v19, v19\nv_pk_fma_f32 v[212:213], v[216:217], v[16:17], v[212:213]\n"
      "v_pk_fma_f32 v[32:33], v[212:213], v[18:19], v[32:33]\nv_add_f32 v39, v32, v33\nv_pk_mul_f32 v[52:53], v[228:229], v[228:229]\nv_pk_mul_f32 v[54:55], v[230:231], v[230:231]\n"
      "v_exp_f32 v48, v48 clamp\nv_pk_mul_f32 v[56:57], v[232:233], v[232:233]\nv_pk_mul_f32 v[58:59], v[234:235], v[234:235]\nv_exp_f32 v49, v49 clamp\n"
      "v_pk_fma_f32 v[52:53], v[52:53], s[58:59], s[58:59] op_sel:[0,0,1] op_sel_hi:[1,0,1]\nv_pk_fma_f32 v[54:55], v[54:55], s[58:59], s[58:59] op_sel:[0,0,1] op_sel_hi:[1,0,1]\n"
      "v_pk_fma_f32 v[56:57], v[56:57], s[58:59], s[58:59] op_sel:[0,0,1] op_sel_hi:[1,0,1]\nv_exp_f32 v50, v50 clamp\n"
      "v_pk_fma_f32 v[58:59], v[58:59], s[58:59], s[58:59] op_sel:[0,0,1] op_sel_hi:[1,0,1]\nv_pk_fma_f32 v[52:53], v[52:53], v[228:229], s[60:61] op_sel_hi:[1,1,0]\n"
      "v_pk_fma_f32 v[54:55], v[54:55], v[230:231], s[60:61] op_sel_hi:[1,1,0]\nv_exp_f32 v51, v51 clamp\nv_pk_fma_f32 v[56:57], v[56:57], v[232:233], s[60:61] op_sel_hi:[1,1,0]\n"
      "v_pk_fma_f32 v[58:59], v[58:59], v[234:235], s[60:61] op_sel_hi:[1,1,0]\nv_exp_f32 v52, v52 clamp\nv_pk_fma_f32 v[46:47], v[46:47], v[2:3], v[26:27]\nv_exp_f32 v53, v53 clamp\n"
      "v_pk_fma_f32 v[44:45], v[44:45], v[0:1], v[24:25]\nv_pk_fma_f32 v[48:49], v[48:49], v[4:5], v[28:29]\nv_exp_f32 v54, v54 clamp\nv_pk_fma_f32 v[50:51], v[50:51], v[6:7], v[30:31]\n"
      "v_pk_mul_f32 v[220:221], v[220:221], v[46:47]\nv_exp_f32 v55, v55 clamp\nv_pk_mul_f32 v[224:225], v[224:225], v[50:51]\nv_pk_fma_f32 v[220:221], v[222:223], v[44:45], v[220:221]\n"
      "v_exp_f32 v56, v56 clamp\nv_pk_fma_f32 v[224:225], v[226:227], v[48:49], v[224:225]\nv_exp_f32 v57, v57 clamp\nv_pk_mul_f32 v[44:45], v[44:45], v[46:47]\n"
      "v_pk_mul_f32 v[48:49], v[48:49], v[50:51]\nv_exp_f32 v58, v58 clamp\nv_pk_mul_f32 v[46:47], v[44:45], v[48:49]\nv_rcp_f32 v46, v46\nv_pk_mul_f32 v[220:221], v[220:221], v[48:49]\n"
      "v_exp_f32 v59, v59 clamp\nv_rcp_f32 v47, v47\nv_pk_fma_f32 v[220:221], v[224:225], v[44:45], v[220:221]\nv_pk_mul_f32 v[32:33], v[220:221], v[46:47]\n"
      "v_pk_fma_f32 v[54:55], v[54:55], v[10:11], v[66:67]\nv_pk_fma_f32 v[52:53], v[52:53], v[8:9], v[64:65]\nv_pk_fma_f32 v[56:57], v[56:57], v[12:13], v[68:69]\n"
      "v_pk_fma_f32 v[58:59], v[58:59], v[14:15], v[70:71]\nv_pk_mul_f32 v[228:229], v[228:229], v[54:55]\nv_pk_mul_f32 v[232:233], v[232:233], v[58:59]\n"
      "v_pk_fma_f32 v[228:229], v[230:231], v[52:53], v[228:229]\nv_pk_fma_f32 v[232:233], v[234:235], v[56:57], v[232:233]\nv_pk_mul_f32 v[52:53], v[52:53], v[54:55]\n"
      "v_pk_mul_f32 v[56:57], v[56:57], v[58:59]\nv_pk_mul_f32 v[54:55], v[52:53], v[56:57]\nv_rcp_f32 v54, v54\nv_pk_mul_f32 v[228:229], v[228:229], v[56:57]\nv_rcp_f32 v55, v55\n"
      "v_pk_fma_f32 v[228:229], v[232:233], v[52:53], v[228:229]\nv_pk_fma_f32 v[32:33], v[228:229], v[54:55], v[32:33]\nv_add_f32 v40, v32, v33\ns_barrier\ns_branch .Lge\n"
      ".Lg12:\ns_waitcnt vmcnt(4)\ns_barrier\nds_read_b64_tr_b16 v[64:65], v104 offset:45056\nds_read_b64_tr_b16 v[66:67], v104 offset:46080\nds_read_b64_tr_b16 v[68:69], v104 offset:53248\n"
      "ds_read_b64_tr_b16 v[70:71], v104 offset:54272\nds_read_b64_tr_b16 v[72:73], v105 offset:45056\nds_read_b64_tr_b16 v[74:75], v105 offset:46080\n"
      "ds_read_b64_tr_b16 v[76:77], v105 offset:53248\nds_read_b64_tr_b16 v[78:79], v105 offset:54272\nds_read_b64_tr_b16 v[80:81], v106 offset:45056\n"
      "ds_read_b64_tr_b16 v[82:83], v106 offset:46080\nds_read_b64_tr_b16 v[84:85], v106 offset:53248\nds_read_b64_tr_b16 v[86:87], v106 offset:54272\n"
      "ds_read_b64_tr_b16 v[88:89], v107 offset:45056\nds_read_b64_tr_b16 v[90:91], v107 offset:46080\nds_read_b64_tr_b16 v[92:93], v107 offset:53248\n"
      "ds_read_b64_tr_b16 v[94:95], v107 offset:54272\nds_read_b128 v[8:11], v120 offset:45056\nds_read_b128 v[12:15], v121 offset:45056\nds_read_b128 v[16:19], v96 offset:45056\n"
      "ds_read_b128 v[20:23], v97 offset:45056\ns_waitcnt lgkmcnt(2)\n.p2align 3\n"
      ".Lgl12:\nv_mfma_f32_16x16x32_bf16 v[152:155], v[88:91], v[8:11], v[152:155]\nds_read_b128 v[24:27], v96 offset:47104\nv_mfma_f32_16x16x32_bf16 v[152:155], v[92:95], v[12:15], v[152:155]\n"
      "ds_read_b128 v[28:31], v97 offset:47104\nv_mfma_f32_16x16x32_bf16 v[148:151], v[84:87], v[12:15], v[148:151]\nds_read_b128 v[0:3], v96 offset:49152\n"
      "v_mfma_f32_16x16x32_bf16 v[148:151], v[80:83], v[8:11], v[148:151]\nds_read_b128 v[4:7], v97 offset:49152\nv_mfma_f32_16x16x32_bf16 v[144:147], v[72:75], v[8:11], v[144:147]\n"
      "v_mfma_f32_16x16x32_bf16 v[144:147], v[76:79], v[12:15], v[144:147]\nv_mfma_f32_16x16x32_bf16 v[140:143], v[68:71], v[12:15], v[140:143]\ns_waitcnt lgkmcnt(4)\n"
      "v_mfma_f32_16x16x32_bf16 v[140:143], v[64:67], v[8:11], v[140:143]\nv_mfma_f32_16x16x32_bf16 v[156:159], v[64:67], v[16:19], v[156:159]\nds_read_b128 v[8:11], v96 offset:51200\n"
      "v_mfma_f32_16x16x32_bf16 v[156:159], v[68:71], v[20:23], v[156:159]\nds_read_b128 v[12:15], v97 offset:51200\nv_mfma_f32_16x16x32_bf16 v[160:163], v[76:79], v[20:23], v[160:163]\n"
      "v_mfma_f32_16x16x32_bf16 v[160:163], v[72:75], v[16:19], v[160:163]\nv_mfma_f32_16x16x32_bf16 v[164:167], v[80:83], v[16:19], v[164:167]\n"
      "v_mfma_f32_16x16x32_bf16 v[164:167], v[84:87], v[20:23], v[164:167]\nv_mfma_f32_16x16x32_bf16 v[168:171], v[92:95], v[20:23], v[168:171]\ns_waitcnt lgkmcnt(2)\n"
      "v_mfma_f32_16x16x32_bf16 v[168:171], v[88:91], v[16:19], v[168:171]\nv_mfma_f32_16x16x32_bf16 v[184:187], v[88:91], v[24:27], v[184:187]\nds_read_b128 v[16:19], v96 offset:53248\n"
      "v_mfma_f32_16x16x32_bf16 v[184:187], v[92:95], v[28:31], v[184:187]\nds_read_b128 v[20:23], v97 offset:53248\nv_mfma_f32_16x16x32_bf16 v[180:183], v[84:87], v[28:31], v[180:183]\n"
      "v_mfma_f32_16x16x32_bf16 v[180:183], v[80:83], v[24:27], v[180:183]\nv_mfma_f32_16x16x32_bf16 v[176:179], v[72:75], v[24:27], v[176:179]\n"
      "v_mfma_f32_16x16x32_bf16 v[176:179], v[76:79], v[28:31], v[176:179]\nv_mfma_f32_16x16x32_bf16 v[172:175], v[68:71], v[28:31], v[172:175]\ns_waitcnt vmcnt(0) lgkmcnt(0)\ns_barrier\n"
      "v_mfma_f32_16x16x32_bf16 v[172:175], v[64:67], v[24:27], v[172:175]\nds_read_b64_tr_b16 v[32:33], v108 offset:0\nv_mfma_f32_16x16x32_bf16 v[188:191], v[64:67], v[0:3], v[188:191]\n"
      "ds_read_b64_tr_b16 v[34:35], v108 offset:1024\nv_mfma_f32_16x16x32_bf16 v[188:191], v[68:71], v[4:7], v[188:191]\nds_read_b64_tr_b16 v[36:37], v108 offset:8192\n"
      "v_mfma_f32_16x16x32_bf16 v[192:195], v[76:79], v[4:7], v[192:195]\nds_read_b64_tr_b16 v[38:39], v108 offset:9216\nv_mfma_f32_16x16x32_bf16 v[192:195], v[72:75], v[0:3], v[192:195]\n"
      "ds_read_b64_tr_b16 v[40:41], v109 offset:0\nv_mfma_f32_16x16x32_bf16 v[196:199], v[80:83], v[0:3], v[196:199]\nds_read_b64_tr_b16 v[42:43], v109 offset:1024\n"
      "v_mfma_f32_16x16x32_bf16 v[196:199], v[84:87], v[4:7], v[196:199]\nds_read_b64_tr_b16 v[44:45], v109 offset:8192\nv_mfma_f32_16x16x32_bf16 v[200:203], v[92:95], v[4:7], v[200:203]\n"
      "ds_read_b64_tr_b16 v[46:47], v109 offset:9216\nv_mfma_f32_16x16x32_bf16 v[200:203], v[88:91], v[0:3], v[200:203]\nds_read_b64_tr_b16 v[48:49], v110 offset:0\n"
      "v_mfma_f32_16x16x32_bf16 v[216:219], v[88:91], v[8:11], v[216:219]\nv_mfma_f32_16x16x32_bf16 v[216:219], v[92:95], v[12:15], v[216:219]\n"
      "v_mfma_f32_16x16x32_bf16 v[212:215], v[84:87], v[12:15], v[212:215]\nds_read_b64_tr_b16 v[50:51], v110 offset:1024\nv_mfma_f32_16x16x32_bf16 v[212:215], v[80:83], v[8:11], v[212:215]\n"
      "ds_read_b64_tr_b16 v[52:53], v110 offset:8192\nv_mfma_f32_16x16x32_bf16 v[208:211], v[72:75], v[8:11], v[208:211]\nds_read_b64_tr_b16 v[54:55], v110 offset:9216\n"
      "v_mfma_f32_16x16x32_bf16 v[208:211], v[76:79], v[12:15], v[208:211]\nds_read_b64_tr_b16 v[56:57], v111 offset:0\nv_mfma_f32_16x16x32_bf16 v[204:207], v[68:71], v[12:15], v[204:207]\n"
      "ds_read_b64_tr_b16 v[58:59], v111 offset:1024\nv_mfma_f32_16x16x32_bf16 v[204:207], v[64:67], v[8:11], v[204:207]\nds_read_b64_tr_b16 v[60:61], v111 offset:8192\n"
      "v_mfma_f32_16x16x32_bf16 v[220:223], v[64:67], v[16:19], v[220:223]\nds_read_b128 v[8:11], v122 offset:0\nv_mfma_f32_16x16x32_bf16 v[220:223], v[68:71], v[20:23], v[220:223]\n"
      "ds_read_b128 v[12:15], v123 offset:0\nv_mfma_f32_16x16x32_bf16 v[224:227], v[76:79], v[20:23], v[224:227]\nds_read_b64_tr_b16 v[62:63], v111 offset:9216\n"
      "v_mfma_f32_16x16x32_bf16 v[224:227], v[72:75], v[16:19], v[224:227]\nv_mfma_f32_16x16x32_bf16 v[228:231], v[80:83], v[16:19], v[228:231]\n"
      "v_mfma_f32_16x16x32_bf16 v[228:231], v[84:87], v[20:23], v[228:231]\nv_mfma_f32_16x16x32_bf16 v[232:235], v[92:95], v[20:23], v[232:235]\ns_waitcnt lgkmcnt(0)\n"
      "v_mfma_f32_16x16x32_bf16 v[232:235], v[88:91], v[16:19], v[232:235]\nds_read_b128 v[16:19], v98 offset:0\nds_read_b128 v[20:23], v99 offset:0\n"
      "v_mfma_f32_16x16x32_bf16 v[152:155], v[56:59], v[8:11], v[152:155]\nds_read_b128 v[24:27], v98 offset:2048\nv_mfma_f32_16x16x32_bf16 v[152:155], v[60:63], v[12:15], v[152:155]\n"
      "ds_read_b128 v[28:31], v99 offset:2048\nv_mfma_f32_16x16x32_bf16 v[148:151], v[52:55], v[12:15], v[148:151]\nds_read_b128 v[0:3], v98 offset:4096\n"
      "v_mfma_f32_16x16x32_bf16 v[148:151], v[48:51], v[8:11], v[148:151]\nds_read_b128 v[4:7], v99 offset:4096\nv_mfma_f32_16x16x32_bf16 v[144:147], v[40:43], v[8:11], v[144:147]\n"
      "v_mfma_f32_16x16x32_bf16 v[144:147], v[44:47], v[12:15], v[144:147]\nv_mfma_f32_16x16x32_bf16 v[140:143], v[36:39], v[12:15], v[140:143]\ns_waitcnt lgkmcnt(4)\n"
      "v_mfma_f32_16x16x32_bf16 v[140:143], v[32:35], v[8:11], v[140:143]\nv_mfma_f32_16x16x32_bf16 v[156:159], v[32:35], v[16:19], v[156:159]\nds_read_b128 v[8:11], v98 offset:6144\n"
      "v_mfma_f32_16x16x32_bf16 v[156:159], v[36:39], v[20:23], v[156:159]\nds_read_b128 v[12:15], v99 offset:6144\nv_mfma_f32_16x16x32_bf16 v[160:163], v[44:47], v[20:23], v[160:163]\n"
      "v_mfma_f32_16x16x32_bf16 v[160:163], v[40:43], v[16:19], v[160:163]\nv_mfma_f32_16x16x32_bf16 v[164:167], v[48:51], v[16:19], v[164:167]\n"
      "v_mfma_f32_16x16x32_bf16 v[164:167], v[52:55], v[20:23], v[164:167]\nv_mfma_f32_16x16x32_bf16 v[168:171], v[60:63], v[20:23], v[168:171]\ns_waitcnt lgkmcnt(2)\n"
      "v_mfma_f32_16x16x32_bf16 v[168:171], v[56:59], v[16:19], v[168:171]\nv_mfma_f32_16x16x32_bf16 v[184:187], v[56:59], v[24:27], v[184:187]\nds_read_b128 v[16:19], v98 offset:8192\n"
      "v_mfma_f32_16x16x32_bf16 v[184:187], v[60:63], v[28:31], v[184:187]\nds_read_b128 v[20:23], v99 offset:8192\nv_mfma_f32_16x16x32_bf16 v[180:183], v[52:55], v[28:31], v[180:183]\n"
      "v_mfma_f32_16x16x32_bf16 v[180:183], v[48:51], v[24:27], v[180:183]\nv_mfma_f32_16x16x32_bf16 v[176:179], v[40:43], v[24:27], v[176:179]\n"
      "v_mfma_f32_16x16x32_bf16 v[176:179], v[44:47], v[28:31], v[176:179]\nv_mfma_f32_16x16x32_bf16 v[172:175], v[36:39], v[28:31], v[172:175]\ns_waitcnt vmcnt(0) lgkmcnt(0)\ns_barrier\n"
      "v_mfma_f32_16x16x32_bf16 v[172:175], v[32:35], v[24:27], v[172:175]\nds_read_b64_tr_b16 v[64:65], v104 offset:0\nv_mfma_f32_16x16x32_bf16 v[188:191], v[32:35], v[0:3], v[188:191]\n"
      "ds_read_b64_tr_b16 v[66:67], v104 offset:1024\nv_mfma_f32_16x16x32_bf16 v[188:191], v[36:39], v[4:7], v[188:191]\nds_read_b64_tr_b16 v[68:69], v104 offset:8192\n"
      "v_mfma_f32_16x16x32_bf16 v[192:195], v[44:47], v[4:7], v[192:195]\nds_read_b64_tr_b16 v[70:71], v104 offset:9216\nv_mfma_f32_16x16x32_bf16 v[192:195], v[40:43], v[0:3], v[192:195]\n"
      "ds_read_b64_tr_b16 v[72:73], v105 offset:0\nv_mfma_f32_16x16x32_bf16 v[196:199], v[48:51], v[0:3], v[196:199]\nds_read_b64_tr_b16 v[74:75], v105 offset:1024\n"
      "v_mfma_f32_16x16x32_bf16 v[196:199], v[52:55], v[4:7], v[196:199]\nds_read_b64_tr_b16 v[76:77], v105 offset:8192\nv_mfma_f32_16x16x32_bf16 v[200:203], v[60:63], v[4:7], v[200:203]\n"
      "ds_read_b64_tr_b16 v[78:79], v105 offset:9216\nv_mfma_f32_16x16x32_bf16 v[200:203], v[56:59], v[0:3], v[200:203]\nds_read_b64_tr_b16 v[80:81], v106 offset:0\n"
      "v_mfma_f32_16x16x32_bf16 v[216:219], v[56:59], v[8:11], v[216:219]\nv_mfma_f32_16x16x32_bf16 v[216:219], v[60:63], v[12:15], v[216:219]\n"
      "v_mfma_f32_16x16x32_bf16 v[212:215], v[52:55], v[12:15], v[212:215]\nds_read_b64_tr_b16 v[82:83], v106 offset:1024\nv_mfma_f32_16x16x32_bf16 v[212:215], v[48:51], v[8:11], v[212:215]\n"
      "ds_read_b64_tr_b16 v[84:85], v106 offset:8192\nv_mfma_f32_16x16x32_bf16 v[208:211], v[40:43], v[8:11], v[208:211]\nds_read_b64_tr_b16 v[86:87], v106 offset:9216\n"
      "v_mfma_f32_16x16x32_bf16 v[208:211], v[44:47], v[12:15], v[208:211]\nds_read_b64_tr_b16 v[88:89], v107 offset:0\nv_mfma_f32_16x16x32_bf16 v[204:207], v[36:39], v[12:15], v[204:207]\n"
      "ds_read_b64_tr_b16 v[90:91], v107 offset:1024\nv_mfma_f32_16x16x32_bf16 v[204:207], v[32:35], v[8:11], v[204:207]\nds_read_b64_tr_b16 v[92:93], v107 offset:8192\n"
      "v_mfma_f32_16x16x32_bf16 v[220:223], v[32:35], v[16:19], v[220:223]\nds_read_b128 v[8:11], v120 offset:0\nv_mfma_f32_16x16x32_bf16 v[220:223], v[36:39], v[20:23], v[220:223]\n"
      "ds_read_b128 v[12:15], v121 offset:0\nv_mfma_f32_16x16x32_bf16 v[224:227], v[44:47], v[20:23], v[224:227]\nds_read_b64_tr_b16 v[94:95], v107 offset:9216\n"
      "v_mfma_f32_16x16x32_bf16 v[224:227], v[40:43], v[16:19], v[224:227]\nv_mfma_f32_16x16x32_bf16 v[228:231], v[48:51], v[16:19], v[228:231]\n"
      "v_mfma_f32_16x16x32_bf16 v[228:231], v[52:55], v[20:23], v[228:231]\nv_mfma_f32_16x16x32_bf16 v[232:235], v[60:63], v[20:23], v[232:235]\ns_waitcnt lgkmcnt(0)\n"
      "v_mfma_f32_16x16x32_bf16 v[232:235], v[56:59], v[16:19], v[232:235]\nds_read_b128 v[16:19], v96 offset:0\nds_read_b128 v[20:23], v97 offset:0\n"
      "v_mfma_f32_16x16x32_bf16 v[152:155], v[88:91], v[8:11], v[152:155]\nds_read_b128 v[24:27], v96 offset:2048\nv_mfma_f32_16x16x32_bf16 v[152:155], v[92:95], v[12:15], v[152:155]\n"
      "ds_read_b128 v[28:31], v97 offset:2048\nv_mfma_f32_16x16x32_bf16 v[148:151], v[84:87], v[12:15], v[148:151]\nds_read_b128 v[0:3], v96 offset:4096\n"
      "v_mfma_f32_16x16x32_bf16 v[148:151], v[80:83], v[8:11], v[148:151]\nds_read_b128 v[4:7], v97 offset:4096\nv_mfma_f32_16x16x32_bf16 v[144:147], v[72:75], v[8:11], v[144:147]\n"
      "v_mfma_f32_16x16x32_bf16 v[144:147], v[76:79], v[12:15], v[144:147]\nv_mfma_f32_16x16x32_bf16 v[140:143], v[68:71], v[12:15], v[140:143]\ns_waitcnt lgkmcnt(4)\n"
      "v_mfma_f32_16x16x32_bf16 v[140:143], v[64:67], v[8:11], v[140:143]\nv_mfma_f32_16x16x32_bf16 v[156:159], v[64:67], v[16:19], v[156:159]\nds_read_b128 v[8:11], v96 offset:6144\n"
      "v_mfma_f32_16x16x32_bf16 v[156:159], v[68:71], v[20:23], v[156:159]\nds_read_b128 v[12:15], v97 offset:6144\nv_mfma_f32_16x16x32_bf16 v[160:163], v[76:79], v[20:23], v[160:163]\n"
      "v_mfma_f32_16x16x32_bf16 v[160:163], v[72:75], v[16:19], v[160:163]\nv_mfma_f32_16x16x32_bf16 v[164:167], v[80:83], v[16:19], v[164:167]\n"
      "v_mfma_f32_16x16x32_bf16 v[164:167], v[84:87], v[20:23], v[164:167]\nv_mfma_f32_16x16x32_bf16 v[168:171], v[92:95], v[20:23], v[168:171]\ns_waitcnt lgkmcnt(2)\n"
      "v_mfma_f32_16x16x32_bf16 v[168:171], v[88:91], v[16:19], v[168:171]\nv_mfma_f32_16x16x32_bf16 v[184:187], v[88:91], v[24:27], v[184:187]\nds_read_b128 v[16:19], v96 offset:8192\n"
      "v_mfma_f32_16x16x32_bf16 v[184:187], v[92:95], v[28:31], v[184:187]\nds_read_b128 v[20:23], v97 offset:8192\nv_mfma_f32_16x16x32_bf16 v[180:183], v[84:87], v[28:31], v[180:183]\n"
      "v_mfma_f32_16x16x32_bf16 v[180:183], v[80:83], v[24:27], v[180:183]\nv_mfma_f32_16x16x32_bf16 v[176:179], v[72:75], v[24:27], v[176:179]\n"
      "v_mfma_f32_16x16x32_bf16 v[176:179], v[76:79], v[28:31], v[176:179]\nv_mfma_f32_16x16x32_bf16 v[172:175], v[68:71], v[28:31], v[172:175]\ns_waitcnt vmcnt(0) lgkmcnt(0)\ns_barrier\n"
      "v_mfma_f32_16x16x32_bf16 v[172:175], v[64:67], v[24:27], v[172:175]\nds_read_b64_tr_b16 v[32:33], v104 offset:45056\nv_mfma_f32_16x16x32_bf16 v[188:191], v[64:67], v[0:3], v[188:191]\n"
      "ds_read_b64_tr_b16 v[34:35], v104 offset:46080\nv_mfma_f32_16x16x32_bf16 v[188:191], v[68:71], v[4:7], v[188:191]\nds_read_b64_tr_b16 v[36:37], v104 offset:53248\n"
      "v_mfma_f32_16x16x32_bf16 v[192:195], v[76:79], v[4:7], v[192:195]\nds_read_b64_tr_b16 v[38:39], v104 offset:54272\nv_mfma_f32_16x16x32_bf16 v[192:195], v[72:75], v[0:3], v[192:195]\n"
      "ds_read_b64_tr_b16 v[40:41], v105 offset:45056\nv_mfma_f32_16x16x32_bf16 v[196:199], v[80:83], v[0:3], v[196:199]\nds_read_b64_tr_b16 v[42:43], v105 offset:46080\n"
      "v_mfma_f32_16x16x32_bf16 v[196:199], v[84:87], v[4:7], v[196:199]\nds_read_b64_tr_b16 v[44:45], v105 offset:53248\nv_mfma_f32_16x16x32_bf16 v[200:203], v[92:95], v[4:7], v[200:203]\n"
      "ds_read_b64_tr_b16 v[46:47], v105 offset:54272\nv_mfma_f32_16x16x32_bf16 v[200:203], v[88:91], v[0:3], v[200:203]\nds_read_b64_tr_b16 v[48:49], v106 offset:45056\n"
      "v_mfma_f32_16x16x32_bf16 v[216:219], v[88:91], v[8:11], v[216:219]\nv_mfma_f32_16x16x32_bf16 v[216:219], v[92:95], v[12:15], v[216:219]\n"
      "v_mfma_f32_16x16x32_bf16 v[212:215], v[84:87], v[12:15], v[212:215]\nds_read_b64_tr_b16 v[50:51], v106 offset:46080\nv_mfma_f32_16x16x32_bf16 v[212:215], v[80:83], v[8:11], v[212:215]\n"
      "ds_read_b64_tr_b16 v[52:53], v106 offset:53248\nv_mfma_f32_16x16x32_bf16 v[208:211], v[72:75], v[8:11], v[208:211]\nds_read_b64_tr_b16 v[54:55], v106 offset:54272\n"
      "v_mfma_f32_16x16x32_bf16 v[208:211], v[76:79], v[12:15], v[208:211]\nds_read_b64_tr_b16 v[56:57], v107 offset:45056\nv_mfma_f32_16x16x32_bf16 v[204:207], v[68:71], v[12:15], v[204:207]\n"
      "ds_read_b64_tr_b16 v[58:59], v107 offset:46080\nv_mfma_f32_16x16x32_bf16 v[204:207], v[64:67], v[8:11], v[204:207]\nds_read_b64_tr_b16 v[60:61], v107 offset:53248\n"
      "v_mfma_f32_16x16x32_bf16 v[220:223], v[64:67], v[16:19], v[220:223]\nds_read_b128 v[8:11], v120 offset:45056\nv_mfma_f32_16x16x32_bf16 v[220:223], v[68:71], v[20:23], v[220:223]\n"
      "ds_read_b128 v[12:15], v121 offset:45056\nv_mfma_f32_16x16x32_bf16 v[224:227], v[76:79], v[20:23], v[224:227]\nds_read_b64_tr_b16 v[62:63], v107 offset:54272\n"
      "v_mfma_f32_16x16x32_bf16 v[224:227], v[72:75], v[16:19], v[224:227]\nv_mfma_f32_16x16x32_bf16 v[228:231], v[80:83], v[16:19], v[228:231]\n"
      "v_mfma_f32_16x16x32_bf16 v[228:231], v[84:87], v[20:23], v[228:231]\nv_mfma_f32_16x16x32_bf16 v[232:235], v[92:95], v[20:23], v[232:235]\ns_waitcnt lgkmcnt(0)\n"
      "v_mfma_f32_16x16x32_bf16 v[232:235], v[88:91], v[16:19], v[232:235]\nds_read_b128 v[16:19], v96 offset:45056\nds_read_b128 v[20:23], v97 offset:45056\n"
      "v_mfma_f32_16x16x32_bf16 v[152:155], v[56:59], v[8:11], v[152:155]\nds_read_b128 v[24:27], v96 offset:47104\nv_mfma_f32_16x16x32_bf16 v[152:155], v[60:63], v[12:15], v[152:155]\n"
      "ds_read_b128 v[28:31], v97 offset:47104\nv_mfma_f32_16x16x32_bf16 v[148:151], v[52:55], v[12:15], v[148:151]\nds_read_b128 v[0:3], v96 offset:49152\n"
      "v_mfma_f32_16x16x32_bf16 v[148:151], v[48:51], v[8:11], v[148:151]\nds_read_b128 v[4:7], v97 offset:49152\nv_mfma_f32_16x16x32_bf16 v[144:147], v[40:43], v[8:11], v[144:147]\n"
      "v_mfma_f32_16x16x32_bf16 v[144:147], v[44:47], v[12:15], v[144:147]\nv_mfma_f32_16x16x32_bf16 v[140:143], v[36:39], v[12:15], v[140:143]\ns_waitcnt lgkmcnt(4)\n"
      "v_mfma_f32_16x16x32_bf16 v[140:143], v[32:35], v[8:11], v[140:143]\nv_mfma_f32_16x16x32_bf16 v[156:159], v[32:35], v[16:19], v[156:159]\nds_read_b128 v[8:11], v96 offset:51200\n"
      "v_mfma_f32_16x16x32_bf16 v[156:159], v[36:39], v[20:23], v[156:159]\nds_read_b128 v[12:15], v97 offset:51200\nv_mfma_f32_16x16x32_bf16 v[160:163], v[44:47], v[20:23], v[160:163]\n"
      "v_mfma_f32_16x16x32_bf16 v[160:163], v[40:43], v[16:19], v[160:163]\nv_mfma_f32_16x16x32_bf16 v[164:167], v[48:51], v[16:19], v[164:167]\n"
      "v_mfma_f32_16x16x32_bf16 v[164:167], v[52:55], v[20:23], v[164:167]\nv_mfma_f32_16x16x32_bf16 v[168:171], v[60:63], v[20:23], v[168:171]\ns_waitcnt lgkmcnt(2)\n"
      "v_mfma_f32_16x16x32_bf16 v[168:171], v[56:59], v[16:19], v[168:171]\nv_mfma_f32_16x16x32_bf16 v[184:187], v[56:59], v[24:27], v[184:187]\nds_read_b128 v[16:19], v96 offset:53248\n"
      "v_mfma_f32_16x16x32_bf16 v[184:187], v[60:63], v[28:31], v[184:187]\nds_read_b128 v[20:23], v97 offset:53248\nv_mfma_f32_16x16x32_bf16 v[180:183], v[52:55], v[28:31], v[180:183]\n"
      "v_mfma_f32_16x16x32_bf16 v[180:183], v[48:51], v[24:27], v[180:183]\nv_mfma_f32_16x16x32_bf16 v[176:179], v[40:43], v[24:27], v[176:179]\n"
      "v_mfma_f32_16x16x32_bf16 v[176:179], v[44:47], v[28:31], v[176:179]\nv_mfma_f32_16x16x32_bf16 v[172:175], v[36:39], v[28:31], v[172:175]\ns_waitcnt vmcnt(0) lgkmcnt(0)\ns_barrier\n"
      "v_mfma_f32_16x16x32_bf16 v[172:175], v[32:35], v[24:27], v[172:175]\nds_read_b64_tr_b16 v[64:65], v108 offset:0\nv_mfma_f32_16x16x32_bf16 v[188:191], v[32:35], v[0:3], v[188:191]\n"
      "ds_read_b64_tr_b16 v[66:67], v108 offset:1024\nv_mfma_f32_16x16x32_bf16 v[188:191], v[36:39], v[4:7], v[188:191]\nds_read_b64_tr_b16 v[68:69], v108 offset:8192\n"
      "v_mfma_f32_16x16x32_bf16 v[192:195], v[44:47], v[4:7], v[192:195]\nds_read_b64_tr_b16 v[70:71], v108 offset:9216\nv_mfma_f32_16x16x32_bf16 v[192:195], v[40:43], v[0:3], v[192:195]\n"
      "ds_read_b64_tr_b16 v[72:73], v109 offset:0\nv_mfma_f32_16x16x32_bf16 v[196:199], v[48:51], v[0:3], v[196:199]\nds_read_b64_tr_b16 v[74:75], v109 offset:1024\n"
      "v_mfma_f32_16x16x32_bf16 v[196:199], v[52:55], v[4:7], v[196:199]\nds_read_b64_tr_b16 v[76:77], v109 offset:8192\nv_mfma_f32_16x16x32_bf16 v[200:203], v[60:63], v[4:7], v[200:203]\n"
      "ds_read_b64_tr_b16 v[78:79], v109 offset:9216\nv_mfma_f32_16x16x32_bf16 v[200:203], v[56:59], v[0:3], v[200:203]\nds_read_b64_tr_b16 v[80:81], v110 offset:0\n"
      "v_mfma_f32_16x16x32_bf16 v[216:219], v[56:59], v[8:11], v[216:219]\nv_mfma_f32_16x16x32_bf16 v[216:219], v[60:63], v[12:15], v[216:219]\n"
      "v_mfma_f32_16x16x32_bf16 v[212:215], v[52:55], v[12:15], v[212:215]\nds_read_b64_tr_b16 v[82:83], v110 offset:1024\nv_mfma_f32_16x16x32_bf16 v[212:215], v[48:51], v[8:11], v[212:215]\n"
      "ds_read_b64_tr_b16 v[84:85], v110 offset:8192\nv_mfma_f32_16x16x32_bf16 v[208:211], v[40:43], v[8:11], v[208:211]\nds_read_b64_tr_b16 v[86:87], v110 offset:9216\n"
      "v_mfma_f32_16x16x32_bf16 v[208:211], v[44:47], v[12:15], v[208:211]\nds_read_b64_tr_b16 v[88:89], v111 offset:0\nv_mfma_f32_16x16x32_bf16 v[204:207], v[36:39], v[12:15], v[204:207]\n"
      "ds_read_b64_tr_b16 v[90:91], v111 offset:1024\nv_mfma_f32_16x16x32_bf16 v[204:207], v[32:35], v[8:11], v[204:207]\nds_read_b64_tr_b16 v[92:93], v111 offset:8192\n"
      "v_mfma_f32_16x16x32_bf16 v[220:223], v[32:35], v[16:19], v[220:223]\nds_read_b128 v[8:11], v122 offset:0\nv_mfma_f32_16x16x32_bf16 v[220:223], v[36:39], v[20:23], v[220:223]\n"
      "ds_read_b128 v[12:15], v123 offset:0\nv_mfma_f32_16x16x32_bf16 v[224:227], v[44:47], v[20:23], v[224:227]\nds_read_b64_tr_b16 v[94:95], v111 offset:9216\n"
      "v_mfma_f32_16x16x32_bf16 v[224:227], v[40:43], v[16:19], v[224:227]\nv_mfma_f32_16x16x32_bf16 v[228:231], v[48:51], v[16:19], v[228:231]\n"
      "v_mfma_f32_16x16x32_bf16 v[228:231], v[52:55], v[20:23], v[228:231]\nv_mfma_f32_16x16x32_bf16 v[232:235], v[60:63], v[20:23], v[232:235]\ns_waitcnt lgkmcnt(0)\n"
      "v_mfma_f32_16x16x32_bf16 v[232:235], v[56:59], v[16:19], v[232:235]\nds_read_b128 v[16:19], v98 offset:0\nds_read_b128 v[20:23], v99 offset:0\n"
      "v_mfma_f32_16x16x32_bf16 v[152:155], v[88:91], v[8:11], v[152:155]\nds_read_b128 v[24:27], v98 offset:2048\nv_mfma_f32_16x16x32_bf16 v[152:155], v[92:95], v[12:15], v[152:155]\n"
      "ds_read_b128 v[28:31], v99 offset:2048\nv_mfma_f32_16x16x32_bf16 v[148:151], v[84:87], v[12:15], v[148:151]\nds_read_b128 v[0:3], v98 offset:4096\n"
      "v_mfma_f32_16x16x32_bf16 v[148:151], v[80:83], v[8:11], v[148:151]\nds_read_b128 v[4:7], v99 offset:4096\nv_mfma_f32_16x16x32_bf16 v[144:147], v[72:75], v[8:11], v[144:147]\n"
      "v_mfma_f32_16x16x32_bf16 v[144:147], v[76:79], v[12:15], v[144:147]\nv_mfma_f32_16x16x32_bf16 v[140:143], v[68:71], v[12:15], v[140:143]\ns_waitcnt lgkmcnt(4)\n"
      "v_mfma_f32_16x16x32_bf16 v[140:143], v[64:67], v[8:11], v[140:143]\nv_mfma_f32_16x16x32_bf16 v[156:159], v[64:67], v[16:19], v[156:159]\nds_read_b128 v[8:11], v98 offset:6144\n"
      "v_mfma_f32_16x16x32_bf16 v[156:159], v[68:71], v[20:23], v[156:159]\nds_read_b128 v[12:15], v99 offset:6144\nv_mfma_f32_16x16x32_bf16 v[160:163], v[76:79], v[20:23], v[160:163]\n"
      "v_mfma_f32_16x16x32_bf16 v[160:163], v[72:75], v[16:19], v[160:163]\nv_mfma_f32_16x16x32_bf16 v[164:167], v[80:83], v[16:19], v[164:167]\n"
      "v_mfma_f32_16x16x32_bf16 v[164:167], v[84:87], v[20:23], v[164:167]\nv_mfma_f32_16x16x32_bf16 v[168:171], v[92:95], v[20:23], v[168:171]\ns_waitcnt lgkmcnt(2)\n"
      "v_mfma_f32_16x16x32_bf16 v[168:171], v[88:91], v[16:19], v[168:171]\nv_mfma_f32_16x16x32_bf16 v[184:187], v[88:91], v[24:27], v[184:187]\nds_read_b128 v[16:19], v98 offset:8192\n"
      "v_mfma_f32_16x16x32_bf16 v[184:187], v[92:95], v[28:31], v[184:187]\nds_read_b128 v[20:23], v99 offset:8192\nv_mfma_f32_16x16x32_bf16 v[180:183], v[84:87], v[28:31], v[180:183]\n"
      "v_mfma_f32_16x16x32_bf16 v[180:183], v[80:83], v[24:27], v[180:183]\nv_mfma_f32_16x16x32_bf16 v[176:179], v[72:75], v[24:27], v[176:179]\n"
      "v_mfma_f32_16x16x32_bf16 v[176:179], v[76:79], v[28:31], v[176:179]\nv_mfma_f32_16x16x32_bf16 v[172:175], v[68:71], v[28:31], v[172:175]\ns_waitcnt vmcnt(0) lgkmcnt(0)\ns_barrier\n"
      "v_mfma_f32_16x16x32_bf16 v[172:175], v[64:67], v[24:27], v[172:175]\nds_read_b64_tr_b16 v[32:33], v104 offset:0\nv_mfma_f32_16x16x32_bf16 v[188:191], v[64:67], v[0:3], v[188:191]\n"
      "ds_read_b64_tr_b16 v[34:35], v104 offset:1024\nv_mfma_f32_16x16x32_bf16 v[188:191], v[68:71], v[4:7], v[188:191]\nds_read_b64_tr_b16 v[36:37], v104 offset:8192\n"
      "v_mfma_f32_16x16x32_bf16 v[192:195], v[76:79], v[4:7], v[192:195]\nds_read_b64_tr_b16 v[38:39], v104 offset:9216\nv_mfma_f32_16x16x32_bf16 v[192:195], v[72:75], v[0:3], v[192:195]\n"
      "ds_read_b64_tr_b16 v[40:41], v105 offset:0\nv_mfma_f32_16x16x32_bf16 v[196:199], v[80:83], v[0:3], v[196:199]\nds_read_b64_tr_b16 v[42:43], v105 offset:1024\n"
      "v_mfma_f32_16x16x32_bf16 v[196:199], v[84:87], v[4:7], v[196:199]\nds_read_b64_tr_b16 v[44:45], v105 offset:8192\nv_mfma_f32_16x16x32_bf16 v[200:203], v[92:95], v[4:7], v[200:203]\n"
      "ds_read_b64_tr_b16 v[46:47], v105 offset:9216\nv_mfma_f32_16x16x32_bf16 v[200:203], v[88:91], v[0:3], v[200:203]\nds_read_b64_tr_b16 v[48:49], v106 offset:0\n"
      "v_mfma_f32_16x16x32_bf16 v[216:219], v[88:91], v[8:11], v[216:219]\nv_mfma_f32_16x16x32_bf16 v[216:219], v[92:95], v[12:15], v[216:219]\n"
      "v_mfma_f32_16x16x32_bf16 v[212:215], v[84:87], v[12:15], v[212:215]\nds_read_b64_tr_b16 v[50:51], v106 offset:1024\nv_mfma_f32_16x16x32_bf16 v[212:215], v[80:83], v[8:11], v[212:215]\n"
      "ds_read_b64_tr_b16 v[52:53], v106 offset:8192\nv_mfma_f32_16x16x32_bf16 v[208:211], v[72:75], v[8:11], v[208:211]\nds_read_b64_tr_b16 v[54:55], v106 offset:9216\n"
      "v_mfma_f32_16x16x32_bf16 v[208:211], v[76:79], v[12:15], v[208:211]\nds_read_b64_tr_b16 v[56:57], v107 offset:0\nv_mfma_f32_16x16x32_bf16 v[204:207], v[68:71], v[12:15], v[204:207]\n"
      "ds_read_b64_tr_b16 v[58:59], v107 offset:1024\nv_mfma_f32_16x16x32_bf16 v[204:207], v[64:67], v[8:11], v[204:207]\nds_read_b64_tr_b16 v[60:61], v107 offset:8192\n"
      "v_mfma_f32_16x16x32_bf16 v[220:223], v[64:67], v[16:19], v[220:223]\nds_read_b128 v[8:11], v120 offset:0\nv_mfma_f32_16x16x32_bf16 v[220:223], v[68:71], v[20:23], v[220:223]\n"
      "ds_read_b128 v[12:15], v121 offset:0\nv_mfma_f32_16x16x32_bf16 v[224:227], v[76:79], v[20:23], v[224:227]\nds_read_b64_tr_b16 v[62:63], v107 offset:9216\n"
      "v_mfma_f32_16x16x32_bf16 v[224:227], v[72:75], v[16:19], v[224:227]\nv_mfma_f32_16x16x32_bf16 v[228:231], v[80:83], v[16:19], v[228:231]\n"
      "v_mfma_f32_16x16x32_bf16 v[228:231], v[84:87], v[20:23], v[228:231]\nv_mfma_f32_16x16x32_bf16 v[232:235], v[92:95], v[20:23], v[232:235]\ns_waitcnt lgkmcnt(0)\n"
      "v_mfma_f32_16x16x32_bf16 v[232:235], v[88:91], v[16:19], v[232:235]\nds_read_b128 v[16:19], v96 offset:0\nds_read_b128 v[20:23], v97 offset:0\n"
      "v_mfma_f32_16x16x32_bf16 v[152:155], v[56:59], v[8:11], v[152:155]\nds_read_b128 v[24:27], v96 offset:2048\nv_mfma_f32_16x16x32_bf16 v[152:155], v[60:63], v[12:15], v[152:155]\n"
      "ds_read_b128 v[28:31], v97 offset:2048\nv_mfma_f32_16x16x32_bf16 v[148:151], v[52:55], v[12:15], v[148:151]\nds_read_b128 v[0:3], v96 offset:4096\n"
      "v_mfma_f32_16x16x32_bf16 v[148:151], v[48:51], v[8:11], v[148:151]\nds_read_b128 v[4:7], v97 offset:4096\nv_mfma_f32_16x16x32_bf16 v[144:147], v[40:43], v[8:11], v[144:147]\n"
      "v_mfma_f32_16x16x32_bf16 v[144:147], v[44:47], v[12:15], v[144:147]\nv_mfma_f32_16x16x32_bf16 v[140:143], v[36:39], v[12:15], v[140:143]\ns_waitcnt lgkmcnt(4)\n"
      "v_mfma_f32_16x16x32_bf16 v[140:143], v[32:35], v[8:11], v[140:143]\nv_mfma_f32_16x16x32_bf16 v[156:159], v[32:35], v[16:19], v[156:159]\nds_read_b128 v[8:11], v96 offset:6144\n"
      "v_mfma_f32_16x16x32_bf16 v[156:159], v[36:39], v[20:23], v[156:159]\nds_read_b128 v[12:15], v97 offset:6144\nv_mfma_f32_16x16x32_bf16 v[160:163], v[44:47], v[20:23], v[160:163]\n"
      "v_mfma_f32_16x16x32_bf16 v[160:163], v[40:43], v[16:19], v[160:163]\nv_mfma_f32_16x16x32_bf16 v[164:167], v[48:51], v[16:19], v[164:167]\n"
      "v_mfma_f32_16x16x32_bf16 v[164:167], v[52:55], v[20:23], v[164:167]\nv_mfma_f32_16x16x32_bf16 v[168:171], v[60:63], v[20:23], v[168:171]\ns_waitcnt lgkmcnt(2)\n"
      "v_mfma_f32_16x16x32_bf16 v[168:171], v[56:59], v[16:19], v[168:171]\nv_mfma_f32_16x16x32_bf16 v[184:187], v[56:59], v[24:27], v[184:187]\nds_read_b128 v[16:19], v96 offset:8192\n"
      "v_mfma_f32_16x16x32_bf16 v[184:187], v[60:63], v[28:31], v[184:187]\nds_read_b128 v[20:23], v97 offset:8192\nv_mfma_f32_16x16x32_bf16 v[180:183], v[52:55], v[28:31], v[180:183]\n"
      "v_mfma_f32_16x16x32_bf16 v[180:183], v[48:51], v[24:27], v[180:183]\nv_mfma_f32_16x16x32_bf16 v[176:179], v[40:43], v[24:27], v[176:179]\n"
      "v_mfma_f32_16x16x32_bf16 v[176:179], v[44:47], v[28:31], v[176:179]\nv_mfma_f32_16x16x32_bf16 v[172:175], v[36:39], v[28:31], v[172:175]\ns_waitcnt vmcnt(0) lgkmcnt(0)\ns_barrier\n"
      "v_mfma_f32_16x16x32_bf16 v[172:175], v[32:35], v[24:27], v[172:175]\nds_read_b64_tr_b16 v[64:65], v104 offset:45056\nv_mfma_f32_16x16x32_bf16 v[188:191], v[32:35], v[0:3], v[188:191]\n"
      "ds_read_b64_tr_b16 v[66:67], v104 offset:46080\nv_mfma_f32_16x16x32_bf16 v[188:191], v[36:39], v[4:7], v[188:191]\nds_read_b64_tr_b16 v[68:69], v104 offset:53248\n"
      "v_mfma_f32_16x16x32_bf16 v[192:195], v[44:47], v[4:7], v[192:195]\nds_read_b64_tr_b16 v[70:71], v104 offset:54272\nv_mfma_f32_16x16x32_bf16 v[192:195], v[40:43], v[0:3], v[192:195]\n"
      "ds_read_b64_tr_b16 v[72:73], v105 offset:45056\nv_mfma_f32_16x16x32_bf16 v[196:199], v[48:51], v[0:3], v[196:199]\nds_read_b64_tr_b16 v[74:75], v105 offset:46080\n"
      "v_mfma_f32_16x16x32_bf16 v[196:199], v[52:55], v[4:7], v[196:199]\nds_read_b64_tr_b16 v[76:77], v105 offset:53248\nv_mfma_f32_16x16x32_bf16 v[200:203], v[60:63], v[4:7], v[200:203]\n"
      "ds_read_b64_tr_b16 v[78:79], v105 offset:54272\nv_mfma_f32_16x16x32_bf16 v[200:203], v[56:59], v[0:3], v[200:203]\nds_read_b64_tr_b16 v[80:81], v106 offset:45056\n"
      "v_mfma_f32_16x16x32_bf16 v[216:219], v[56:59], v[8:11], v[216:219]\nv_mfma_f32_16x16x32_bf16 v[216:219], v[60:63], v[12:15], v[216:219]\n"
      "v_mfma_f32_16x16x32_bf16 v[212:215], v[52:55], v[12:15], v[212:215]\nds_read_b64_tr_b16 v[82:83], v106 offset:46080\nv_mfma_f32_16x16x32_bf16 v[212:215], v[48:51], v[8:11], v[212:215]\n"
      "ds_read_b64_tr_b16 v[84:85], v106 offset:53248\nv_mfma_f32_16x16x32_bf16 v[208:211], v[40:43], v[8:11], v[208:211]\nds_read_b64_tr_b16 v[86:87], v106 offset:54272\n"
      "v_mfma_f32_16x16x32_bf16 v[208:211], v[44:47], v[12:15], v[208:211]\nds_read_b64_tr_b16 v[88:89], v107 offset:45056\nv_mfma_f32_16x16x32_bf16 v[204:207], v[36:39], v[12:15], v[204:207]\n"
      "ds_read_b64_tr_b16 v[90:91], v107 offset:46080\nv_mfma_f32_16x16x32_bf16 v[204:207], v[32:35], v[8:11], v[204:207]\nds_read_b64_tr_b16 v[92:93], v107 offset:53248\n"
      "v_mfma_f32_16x16x32_bf16 v[220:223], v[32:35], v[16:19], v[220:223]\nds_read_b128 v[8:11], v120 offset:45056\nv_mfma_f32_16x16x32_bf16 v[220:223], v[36:39], v[20:23], v[220:223]\n"
      "ds_read_b128 v[12:15], v121 offset:45056\nv_mfma_f32_16x16x32_bf16 v[224:227], v[44:47], v[20:23], v[224:227]\nds_read_b64_tr_b16 v[94:95], v107 offset:54272\n"
      "v_mfma_f32_16x16x32_bf16 v[224:227], v[40:43], v[16:19], v[224:227]\nv_mfma_f32_16x16x32_bf16 v[228:231], v[48:51], v[16:19], v[228:231]\ns_sub_u32 s58, s58, 1\ns_cmp_lg_u32 s58, 0\n"
      "v_mfma_f32_16x16x32_bf16 v[228:231], v[52:55], v[20:23], v[228:231]\nv_mfma_f32_16x16x32_bf16 v[232:235], v[60:63], v[20:23], v[232:235]\ns_waitcnt lgkmcnt(0)\n"
      "v_mfma_f32_16x16x32_bf16 v[232:235], v[56:59], v[16:19], v[232:235]\nds_read_b128 v[16:19], v96 offset:45056\nds_read_b128 v[20:23], v97 offset:45056\ns_cbranch_scc1 .Lgl12\n"
      "v_mfma_f32_16x16x32_bf16 v[152:155], v[88:91], v[8:11], v[152:155]\nds_read_b128 v[24:27], v96 offset:47104\nv_cmp_eq_u32 s[62:63], 0, v240\n"
      "v_mfma_f32_16x16x32_bf16 v[152:155], v[92:95], v[12:15], v[152:155]\nds_read_b128 v[28:31], v97 offset:47104\nv_mov_b32 v253, 262144\n"
      "v_mfma_f32_16x16x32_bf16 v[148:151], v[84:87], v[12:15], v[148:151]\nds_read_b128 v[0:3], v96 offset:49152\nv_mov_b32 v248, 162048\n"
      "v_mfma_f32_16x16x32_bf16 v[148:151], v[80:83], v[8:11], v[148:151]\nds_read_b128 v[4:7], v97 offset:49152\nv_mov_b32 v241, 1\n"
      "v_mfma_f32_16x16x32_bf16 v[144:147], v[72:75], v[8:11], v[144:147]\nv_cndmask_b32 v253, v253, v248, s[62:63]\nv_lshrrev_b32 v112, 4, v240\n"
      "v_mfma_f32_16x16x32_bf16 v[144:147], v[76:79], v[12:15], v[144:147]\nv_bfe_u32 v113, v240, 2, 2\nv_and_b32 v114, 3, v240\n"
      "v_mfma_f32_16x16x32_bf16 v[140:143], v[68:71], v[12:15], v[140:143]\ns_waitcnt lgkmcnt(4)\nv_mul_u32_u24 v114, 0x80008, v114\n"
      "v_mfma_f32_16x16x32_bf16 v[140:143], v[64:67], v[8:11], v[140:143]\nv_add_u32 v244, v112, v113\nv_and_b32 v244, 3, v244\nv_mfma_f32_16x16x32_bf16 v[156:159], v[64:67], v[16:19], v[156:159]\n"
      "ds_read_b128 v[8:11], v96 offset:51200\nv_add_u32 v116, 22, v113\nv_mfma_f32_16x16x32_bf16 v[156:159], v[68:71], v[20:23], v[156:159]\nds_read_b128 v[12:15], v97 offset:51200\n"
      "v_cmp_gt_u32 s[62:63], 2, v113\nv_mfma_f32_16x16x32_bf16 v[160:163], v[76:79], v[20:23], v[160:163]\nv_and_b32 v113, 60, v240\ns_mov_b32 s42, 0\n"
      "v_mfma_f32_16x16x32_bf16 v[160:163], v[72:75], v[16:19], v[160:163]\nv_cndmask_b32 v116, v116, v244, s[62:63]\nv_lshl_or_b32 v244, v244, 16, v116\n"
      "v_mfma_f32_16x16x32_bf16 v[164:167], v[80:83], v[16:19], v[164:167]\nv_lshl_add_u32 v244, v244, 5, v114\nv_lshl_add_u32 v252, v240, 4, s42\n"
      "v_mfma_f32_16x16x32_bf16 v[164:167], v[84:87], v[20:23], v[164:167]\ns_mov_b32 s42, 0x76325410\ns_mov_b32 s43, 0x21005410\n"
      "v_mfma_f32_16x16x32_bf16 v[168:171], v[92:95], v[20:23], v[168:171]\ns_waitcnt lgkmcnt(2)\nv_lshrrev_b64 v[116:117], v113, s[42:43]\n"
      "v_mfma_f32_16x16x32_bf16 v[168:171], v[88:91], v[16:19], v[168:171]\nv_and_b32 v116, 7, v116\nv_lshl_add_u32 v116, v112, 3, v116\n"
      "v_mfma_f32_16x16x32_bf16 v[184:187], v[88:91], v[24:27], v[184:187]\nds_read_b128 v[16:19], v96 offset:53248\ns_mov_b32 s42, 0x54107632\n"
      "v_mfma_f32_16x16x32_bf16 v[184:187], v[92:95], v[28:31], v[184:187]\nds_read_b128 v[20:23], v97 offset:53248\ns_mov_b32 s43, 0x21107632\n"
      "v_mfma_f32_16x16x32_bf16 v[180:183], v[84:87], v[28:31], v[180:183]\nv_lshrrev_b64 v[118:119], v113, s[42:43]\nv_and_b32 v118, 7, v118\n"
      "v_mfma_f32_16x16x32_bf16 v[180:183], v[80:83], v[24:27], v[180:183]\nv_lshl_add_u32 v118, v112, 3, v118\nv_lshl_or_b32 v245, v118, 16, v116\n"
      "v_mfma_f32_16x16x32_bf16 v[176:179], v[72:75], v[24:27], v[176:179]\nv_lshl_add_u32 v245, v245, 5, v114\nv_and_b32 v113, 1, v240\n"
      "v_mfma_f32_16x16x32_bf16 v[176:179], v[76:79], v[28:31], v[176:179]\nv_mul_u32_u24 v113, 0xaaaa, v113\nv_add_u32 v242, 0x4444, v113\n"
      "v_mfma_f32_16x16x32_bf16 v[172:175], v[68:71], v[28:31], v[172:175]\ns_waitcnt vmcnt(0) lgkmcnt(0)\ns_barrier\nv_mfma_f32_16x16x32_bf16 v[172:175], v[64:67], v[24:27], v[172:175]\n"
      "ds_read_b64_tr_b16 v[32:33], v108 offset:0\ns_lshl_b32 s42, s36, 4\nv_mfma_f32_16x16x32_bf16 v[188:191], v[64:67], v[0:3], v[188:191]\nds_read_b64_tr_b16 v[34:35], v108 offset:1024\n"
      "s_add_u32 s42, s42, 160640\nv_mfma_f32_16x16x32_bf16 v[188:191], v[68:71], v[4:7], v[188:191]\nds_read_b64_tr_b16 v[36:37], v108 offset:8192\nv_lshl_add_u32 v243, v112, 2, s42\n"
      "v_mfma_f32_16x16x32_bf16 v[192:195], v[76:79], v[4:7], v[192:195]\nds_read_b64_tr_b16 v[38:39], v108 offset:9216\nv_and_b32 v112, 1, v112\n"
      "v_mfma_f32_16x16x32_bf16 v[192:195], v[72:75], v[0:3], v[192:195]\nds_read_b64_tr_b16 v[40:41], v109 offset:0\nv_lshl_add_u32 v112, v112, 3, s24\n"
      "v_mfma_f32_16x16x32_bf16 v[196:199], v[80:83], v[0:3], v[196:199]\nds_read_b64_tr_b16 v[42:43], v109 offset:1024\ns_sub_u32 s42, s64, 1\n"
      "v_mfma_f32_16x16x32_bf16 v[196:199], v[84:87], v[4:7], v[196:199]\nds_read_b64_tr_b16 v[44:45], v109 offset:8192\nv_min_u32 v112, s42, v112\n"
      "v_mfma_f32_16x16x32_bf16 v[200:203], v[92:95], v[4:7], v[200:203]\nds_read_b64_tr_b16 v[46:47], v109 offset:9216\nv_lshlrev_b32 v112, 3, v112\n"
      "v_mfma_f32_16x16x32_bf16 v[200:203], v[88:91], v[0:3], v[200:203]\nds_read_b64_tr_b16 v[48:49], v110 offset:0\nv_add_u32 v250, 160256, v112\n"
      "v_mfma_f32_16x16x32_bf16 v[216:219], v[88:91], v[8:11], v[216:219]\nv_and_b32 v113, 15, v240\nv_min_u32 v113, 12, v113\nv_mfma_f32_16x16x32_bf16 v[216:219], v[92:95], v[12:15], v[216:219]\n"
      "s_lshl_b32 s42, s30, 7\nv_lshl_add_u32 v113, v113, 15, s42\nv_mfma_f32_16x16x32_bf16 v[212:215], v[84:87], v[12:15], v[212:215]\nds_read_b64_tr_b16 v[50:51], v110 offset:1024\n"
      "v_add_u32 v249, v113, v112\nv_mfma_f32_16x16x32_bf16 v[212:215], v[80:83], v[8:11], v[212:215]\nds_read_b64_tr_b16 v[52:53], v110 offset:8192\ns_mul_i32 s65, s36, 22272\n"
      "v_mfma_f32_16x16x32_bf16 v[208:211], v[72:75], v[8:11], v[208:211]\nds_read_b64_tr_b16 v[54:55], v110 offset:9216\ns_add_u32 s65, s65, 45056\n"
      "v_mfma_f32_16x16x32_bf16 v[208:211], v[76:79], v[12:15], v[208:211]\nds_read_b64_tr_b16 v[56:57], v111 offset:0\ns_add_u32 s68, s64, 8\n"
      "v_mfma_f32_16x16x32_bf16 v[204:207], v[68:71], v[12:15], v[204:207]\nds_read_b64_tr_b16 v[58:59], v111 offset:1024\ns_min_u32 s68, s68, 21\n"
      "v_mfma_f32_16x16x32_bf16 v[204:207], v[64:67], v[8:11], v[204:207]\nds_read_b64_tr_b16 v[60:61], v111 offset:8192\ns_lshl_b32 s38, s38, 16\n"
      "v_mfma_f32_16x16x32_bf16 v[220:223], v[64:67], v[16:19], v[220:223]\nds_read_b128 v[8:11], v122 offset:0\ns_lshr_b32 s42, s24, 2\n"
      "v_mfma_f32_16x16x32_bf16 v[220:223], v[68:71], v[20:23], v[220:223]\nds_read_b128 v[12:15], v123 offset:0\ns_mul_i32 s43, s42, 3\n"
      "v_mfma_f32_16x16x32_bf16 v[224:227], v[76:79], v[20:23], v[224:227]\nds_read_b64_tr_b16 v[62:63], v111 offset:9216\ns_xor_b32 s43, s43, s36\n"
      "v_mfma_f32_16x16x32_bf16 v[224:227], v[72:75], v[16:19], v[224:227]\ns_add_u32 s42, s42, s64\ns_add_u32 s42, s42, 9\nv_mfma_f32_16x16x32_bf16 v[228:231], v[80:83], v[16:19], v[228:231]\n"
      "s_and_b32 s42, s42, 1\ns_lshl1_add_u32 s67, s43, s42\nv_mfma_f32_16x16x32_bf16 v[228:231], v[84:87], v[20:23], v[228:231]\ns_add_u32 s66, s64, s67\ns_cmp_lt_u32 s66, s68\n"
      "v_mfma_f32_16x16x32_bf16 v[232:235], v[92:95], v[20:23], v[232:235]\ns_waitcnt lgkmcnt(0)\ns_cselect_b32 s66, s66, 127\nv_mfma_f32_16x16x32_bf16 v[232:235], v[88:91], v[16:19], v[232:235]\n"
      "ds_read_b128 v[16:19], v98 offset:0\nds_read_b128 v[20:23], v99 offset:0\nv_mfma_f32_16x16x32_bf16 v[152:155], v[56:59], v[8:11], v[152:155]\nds_read_b128 v[24:27], v98 offset:2048\n"
      "s_sub_u32 s66, s66, 8\nv_mfma_f32_16x16x32_bf16 v[152:155], v[60:63], v[12:15], v[152:155]\nds_read_b128 v[28:31], v99 offset:2048\ns_max_i32 s66, s66, 0\n"
      "v_mfma_f32_16x16x32_bf16 v[148:151], v[52:55], v[12:15], v[148:151]\nds_read_b128 v[0:3], v98 offset:4096\ns_getpc_b64 s[34:35]\n"
      ".Lk1065:\nv_mfma_f32_16x16x32_bf16 v[148:151], v[48:51], v[8:11], v[148:151]\nds_read_b128 v[4:7], v99 offset:4096\ns_add_u32 s34, s34, .Lt_g-.Lk1065\n"
      "v_mfma_f32_16x16x32_bf16 v[144:147], v[40:43], v[8:11], v[144:147]\ns_addc_u32 s35, s35, 0\ns_lshr_b32 s42, s2, 3\nv_mfma_f32_16x16x32_bf16 v[144:147], v[44:47], v[12:15], v[144:147]\n"
      "s_lshl1_add_u32 s42, s2, s42\ns_and_b32 s69, s42, 15\nv_mfma_f32_16x16x32_bf16 v[140:143], v[36:39], v[12:15], v[140:143]\ns_waitcnt lgkmcnt(4)\ns_lshl_b32 s43, s69, 7\n"
      "v_mfma_f32_16x16x32_bf16 v[140:143], v[32:35], v[8:11], v[140:143]\ns_add_u32 s43, s43, 32768\ns_add_u32 s8, s20, s43\nv_mfma_f32_16x16x32_bf16 v[156:159], v[32:35], v[16:19], v[156:159]\n"
      "ds_read_b128 v[8:11], v98 offset:6144\ns_addc_u32 s9, s21, 0\nv_mfma_f32_16x16x32_bf16 v[156:159], v[36:39], v[20:23], v[156:159]\nds_read_b128 v[12:15], v99 offset:6144\n"
      "s_add_u32 s12, s20, 65536\nv_mfma_f32_16x16x32_bf16 v[160:163], v[44:47], v[20:23], v[160:163]\ns_addc_u32 s13, s21, 0\ns_add_u32 s16, s20, 2097152\n"
      "v_mfma_f32_16x16x32_bf16 v[160:163], v[40:43], v[16:19], v[160:163]\ns_addc_u32 s17, s21, 0\ns_lshl_b32 s42, s69, 4\nv_mfma_f32_16x16x32_bf16 v[164:167], v[48:51], v[16:19], v[164:167]\n"
      "s_max_u32 s42, s42, 24\ns_sub_u32 s42, s42, 24\nv_mfma_f32_16x16x32_bf16 v[164:167], v[52:55], v[20:23], v[164:167]\ns_min_u32 s69, s42, 192\ns_lshl_b32 s42, s69, 3\n"
      "v_mfma_f32_16x16x32_bf16 v[168:171], v[60:63], v[20:23], v[168:171]\ns_waitcnt lgkmcnt(2)\ns_add_u32 s42, s42, 24576\nv_mfma_f32_16x16x32_bf16 v[168:171], v[56:59], v[16:19], v[168:171]\n"
      "s_add_u32 s14, s20, s42\ns_addc_u32 s15, s21, 0\nv_mfma_f32_16x16x32_bf16 v[184:187], v[56:59], v[24:27], v[184:187]\nds_read_b128 v[16:19], v98 offset:8192\n"
      "v_mfma_f32_16x16x32_bf16 v[184:187], v[60:63], v[28:31], v[184:187]\nds_read_b128 v[20:23], v99 offset:8192\nv_mfma_f32_16x16x32_bf16 v[180:183], v[52:55], v[28:31], v[180:183]\n"
      "v_mfma_f32_16x16x32_bf16 v[180:183], v[48:51], v[24:27], v[180:183]\nv_mfma_f32_16x16x32_bf16 v[176:179], v[40:43], v[24:27], v[176:179]\n"
      "v_mfma_f32_16x16x32_bf16 v[176:179], v[44:47], v[28:31], v[176:179]\nv_mfma_f32_16x16x32_bf16 v[172:175], v[36:39], v[28:31], v[172:175]\ns_waitcnt vmcnt(0) lgkmcnt(0)\ns_barrier\n"
      "v_mfma_f32_16x16x32_bf16 v[172:175], v[32:35], v[24:27], v[172:175]\nds_read_b128 v[72:75], v252 offset:0\nv_lshl_add_u32 v246, v240, 1, v248\n"
      "v_mfma_f32_16x16x32_bf16 v[188:191], v[32:35], v[0:3], v[188:191]\nds_read_b128 v[24:27], v236 offset:512\nds_read_u16 v251, v246 offset:64\n"
      "v_mfma_f32_16x16x32_bf16 v[188:191], v[36:39], v[4:7], v[188:191]\nds_read_b128 v[28:31], v236 offset:576\nds_read_b32 v247, v248 offset:24\n"
      "v_mfma_f32_16x16x32_bf16 v[192:195], v[44:47], v[4:7], v[192:195]\nds_read_b128 v[76:79], v252 offset:1024\nv_and_b32 v246, 48, v240\n"
      "v_mfma_f32_16x16x32_bf16 v[192:195], v[40:43], v[0:3], v[192:195]\nds_read_b128 v[80:83], v252 offset:2048\ns_mov_b32 s42, 42240\n"
      "v_mfma_f32_16x16x32_bf16 v[196:199], v[48:51], v[0:3], v[196:199]\nds_read_b128 v[84:87], v252 offset:3072\nv_lshl_add_u32 v246, v246, 2, s42\n"
      "v_mfma_f32_16x16x32_bf16 v[196:199], v[52:55], v[4:7], v[196:199]\nds_read_b128 v[88:91], v252 offset:4096\ns_mov_b32 s58, 0xbdd2d3e7\n"
      "v_mfma_f32_16x16x32_bf16 v[200:203], v[60:63], v[4:7], v[200:203]\nds_read_b128 v[92:95], v252 offset:5120\ns_mov_b32 s59, 0xc0135761\n"
      "v_mfma_f32_16x16x32_bf16 v[200:203], v[56:59], v[0:3], v[200:203]\nds_read_b128 v[96:99], v252 offset:6144\ns_mov_b32 s60, 0xc1e00000\n"
      "v_mfma_f32_16x16x32_bf16 v[216:219], v[56:59], v[8:11], v[216:219]\nds_read_b128 v[0:3], v236 offset:0\ns_add_u32 s49, s65, 1856\n"
      "v_mfma_f32_16x16x32_bf16 v[216:219], v[60:63], v[12:15], v[216:219]\nds_read_b128 v[4:7], v236 offset:64\ns_mov_b32 s45, 2\n"
      "v_mfma_f32_16x16x32_bf16 v[212:215], v[52:55], v[12:15], v[212:215]\nds_read_b128 v[100:103], v252 offset:7168\ns_mov_b32 s46, 0\n"
      "v_mfma_f32_16x16x32_bf16 v[212:215], v[48:51], v[8:11], v[212:215]\nds_read_b128 v[104:107], v252 offset:8192\ns_waitcnt lgkmcnt(8)\n"
      "v_mfma_f32_16x16x32_bf16 v[208:211], v[40:43], v[8:11], v[208:211]\nds_read_b128 v[108:111], v252 offset:9216\nv_readfirstlane_b32 s39, v247\n"
      "v_mfma_f32_16x16x32_bf16 v[208:211], v[44:47], v[12:15], v[208:211]\nds_read_b128 v[64:67], v237 offset:512\nv_readlane_b32 s55, v251, 0\n"
      "v_mfma_f32_16x16x32_bf16 v[204:207], v[36:39], v[12:15], v[204:207]\nds_read_b128 v[68:71], v237 offset:576\ns_setprio 3\nv_mfma_f32_16x16x32_bf16 v[204:207], v[32:35], v[8:11], v[204:207]\n"
      "ds_read_b128 v[112:115], v252 offset:10240\ns_xor_b32 s40, s39, 0xa5c30f96\nv_mfma_f32_16x16x32_bf16 v[220:223], v[32:35], v[16:19], v[220:223]\nds_read_b128 v[8:11], v237 offset:0\n"
      "s_mul_i32 s41, s39, 0x9E3779B1\nv_mfma_f32_16x16x32_bf16 v[220:223], v[36:39], v[20:23], v[220:223]\nds_read_b128 v[12:15], v237 offset:64\ns_lshr_b32 s41, s41, 24\n"
      "v_mfma_f32_16x16x32_bf16 v[224:227], v[44:47], v[20:23], v[224:227]\nds_read_b128 v[116:119], v252 offset:11264\nv_mfma_f32_16x16x32_bf16 v[224:227], v[40:43], v[16:19], v[224:227]\n"
      "ds_read_b128 v[120:123], v252 offset:12288\nv_add_u32_sdwa v239, s49, v245 dst_sel:DWORD dst_unused:UNUSED_PAD src0_sel:DWORD src1_sel:WORD_1\n"
      "v_mfma_f32_16x16x32_bf16 v[228:231], v[48:51], v[16:19], v[228:231]\nds_read_b128 v[124:127], v252 offset:13312\n"
      "v_add_u32_sdwa v236, s49, v244 dst_sel:DWORD dst_unused:UNUSED_PAD src0_sel:DWORD src1_sel:WORD_0\nv_mfma_f32_16x16x32_bf16 v[228:231], v[52:55], v[20:23], v[228:231]\n"
      "ds_read_b128 v[128:131], v252 offset:14336\nv_add_u32_sdwa v237, s49, v244 dst_sel:DWORD dst_unused:UNUSED_PAD src0_sel:DWORD src1_sel:WORD_1\n"
      "v_mfma_f32_16x16x32_bf16 v[232:235], v[60:63], v[20:23], v[232:235]\nds_read_b128 v[132:135], v252 offset:15360\n"
      "v_add_u32_sdwa v238, s49, v245 dst_sel:DWORD dst_unused:UNUSED_PAD src0_sel:DWORD src1_sel:WORD_0\ns_waitcnt lgkmcnt(0)\nv_mfma_f32_16x16x32_bf16 v[232:235], v[56:59], v[16:19], v[232:235]\n"
      "ds_read_b128 v[136:139], v252 offset:16384\nv_mov_b32 v42, 0\nv_pk_mul_f32 v[44:45], v[140:141], v[140:141]\nv_pk_mul_f32 v[46:47], v[142:143], v[142:143]\n"
      "v_pk_mul_f32 v[48:49], v[144:145], v[144:145]\nv_pk_mul_f32 v[50:51], v[146:147], v[146:147]\nv_pk_fma_f32 v[44:45], v[44:45], s[58:59], s[58:59] op_sel:[0,0,1] op_sel_hi:[1,0,1]\n"
      "v_pk_fma_f32 v[46:47], v[46:47], s[58:59], s[58:59] op_sel:[0,0,1] op_sel_hi:[1,0,1]\nv_pk_fma_f32 v[48:49], v[48:49], s[58:59], s[58:59] op_sel:[0,0,1] op_sel_hi:[1,0,1]\n"
      "v_pk_fma_f32 v[50:51], v[50:51], s[58:59], s[58:59] op_sel:[0,0,1] op_sel_hi:[1,0,1]\nv_pk_fma_f32 v[44:45], v[44:45], v[140:141], s[60:61] op_sel_hi:[1,1,0]\n"
      "v_pk_fma_f32 v[46:47], v[46:47], v[142:143], s[60:61] op_sel_hi:[1,1,0]\nv_pk_fma_f32 v[48:49], v[48:49], v[144:145], s[60:61] op_sel_hi:[1,1,0]\n"
      "v_pk_fma_f32 v[50:51], v[50:51], v[146:147], s[60:61] op_sel_hi:[1,1,0]\nv_exp_f32 v44, v44 clamp\nv_pk_mul_f32 v[52:53], v[148:149], v[148:149]\nv_exp_f32 v45, v45 clamp\n"
      "v_pk_mul_f32 v[54:55], v[150:151], v[150:151]\nv_pk_mul_f32 v[56:57], v[152:153], v[152:153]\nv_exp_f32 v46, v46 clamp\nv_pk_mul_f32 v[58:59], v[154:155], v[154:155]\n"
      "v_exp_f32 v47, v47 clamp\nv_pk_fma_f32 v[52:53], v[52:53], s[58:59], s[58:59] op_sel:[0,0,1] op_sel_hi:[1,0,1]\n"
      "v_pk_fma_f32 v[54:55], v[54:55], s[58:59], s[58:59] op_sel:[0,0,1] op_sel_hi:[1,0,1]\nv_exp_f32 v48, v48 clamp\n"
      "v_pk_fma_f32 v[56:57], v[56:57], s[58:59], s[58:59] op_sel:[0,0,1] op_sel_hi:[1,0,1]\nv_exp_f32 v49, v49 clamp\n"
      "v_pk_fma_f32 v[58:59], v[58:59], s[58:59], s[58:59] op_sel:[0,0,1] op_sel_hi:[1,0,1]\nv_pk_fma_f32 v[52:53], v[52:53], v[148:149], s[60:61] op_sel_hi:[1,1,0]\nv_exp_f32 v50, v50 clamp\n"
      "v_pk_fma_f32 v[54:55], v[54:55], v[150:151], s[60:61] op_sel_hi:[1,1,0]\nv_exp_f32 v51, v51 clamp\nv_pk_fma_f32 v[56:57], v[56:57], v[152:153], s[60:61] op_sel_hi:[1,1,0]\n"
      "v_pk_fma_f32 v[58:59], v[58:59], v[154:155], s[60:61] op_sel_hi:[1,1,0]\nv_exp_f32 v52, v52 clamp\nv_pk_fma_f32 v[46:47], v[46:47], v[2:3], v[26:27]\n"
      "v_pk_fma_f32 v[44:45], v[44:45], v[0:1], v[24:25]\nv_pk_fma_f32 v[48:49], v[48:49], v[4:5], v[28:29]\nv_exp_f32 v53, v53 clamp\nv_pk_fma_f32 v[50:51], v[50:51], v[6:7], v[30:31]\n"
      "v_pk_mul_f32 v[140:141], v[140:141], v[46:47]\nv_pk_mul_f32 v[144:145], v[144:145], v[50:51]\nv_exp_f32 v54, v54 clamp\nv_pk_fma_f32 v[140:141], v[142:143], v[44:45], v[140:141]\n"
      "v_pk_fma_f32 v[144:145], v[146:147], v[48:49], v[144:145]\nv_pk_mul_f32 v[44:45], v[44:45], v[46:47]\nv_exp_f32 v55, v55 clamp\nv_pk_mul_f32 v[48:49], v[48:49], v[50:51]\n"
      "v_pk_mul_f32 v[46:47], v[44:45], v[48:49]\nv_rcp_f32 v46, v46\nv_pk_mul_f32 v[140:141], v[140:141], v[48:49]\nv_rcp_f32 v47, v47\nv_pk_fma_f32 v[140:141], v[144:145], v[44:45], v[140:141]\n"
      "v_pk_mul_f32 v[32:33], v[140:141], v[46:47]\nv_pk_mul_f32 v[16:17], v[156:157], v[156:157]\nv_pk_mul_f32 v[18:19], v[158:159], v[158:159]\nv_exp_f32 v56, v56 clamp\n"
      "v_pk_mul_f32 v[20:21], v[160:161], v[160:161]\nv_pk_mul_f32 v[22:23], v[162:163], v[162:163]\nv_exp_f32 v57, v57 clamp\n"
      "v_pk_fma_f32 v[16:17], v[16:17], s[58:59], s[58:59] op_sel:[0,0,1] op_sel_hi:[1,0,1]\nv_pk_fma_f32 v[18:19], v[18:19], s[58:59], s[58:59] op_sel:[0,0,1] op_sel_hi:[1,0,1]\n"
      "v_pk_fma_f32 v[20:21], v[20:21], s[58:59], s[58:59] op_sel:[0,0,1] op_sel_hi:[1,0,1]\nv_exp_f32 v58, v58 clamp\n"
      "v_pk_fma_f32 v[22:23], v[22:23], s[58:59], s[58:59] op_sel:[0,0,1] op_sel_hi:[1,0,1]\nv_pk_fma_f32 v[16:17], v[16:17], v[156:157], s[60:61] op_sel_hi:[1,1,0]\n"
      "v_pk_fma_f32 v[18:19], v[18:19], v[158:159], s[60:61] op_sel_hi:[1,1,0]\nv_exp_f32 v59, v59 clamp\nv_pk_fma_f32 v[20:21], v[20:21], v[160:161], s[60:61] op_sel_hi:[1,1,0]\n"
      "v_pk_fma_f32 v[22:23], v[22:23], v[162:163], s[60:61] op_sel_hi:[1,1,0]\nv_exp_f32 v16, v16 clamp\nv_pk_fma_f32 v[54:55], v[54:55], v[10:11], v[66:67]\n"
      "v_pk_fma_f32 v[52:53], v[52:53], v[8:9], v[64:65]\nv_pk_fma_f32 v[56:57], v[56:57], v[12:13], v[68:69]\nv_exp_f32 v17, v17 clamp\nv_pk_fma_f32 v[58:59], v[58:59], v[14:15], v[70:71]\n"
      "v_pk_mul_f32 v[148:149], v[148:149], v[54:55]\nv_pk_mul_f32 v[152:153], v[152:153], v[58:59]\nv_exp_f32 v18, v18 clamp\nv_pk_fma_f32 v[148:149], v[150:151], v[52:53], v[148:149]\n"
      "v_pk_fma_f32 v[152:153], v[154:155], v[56:57], v[152:153]\nv_pk_mul_f32 v[52:53], v[52:53], v[54:55]\nv_exp_f32 v19, v19 clamp\nv_pk_mul_f32 v[56:57], v[56:57], v[58:59]\n"
      "v_pk_mul_f32 v[54:55], v[52:53], v[56:57]\nv_rcp_f32 v54, v54\nv_pk_mul_f32 v[148:149], v[148:149], v[56:57]\nv_rcp_f32 v55, v55\nv_pk_fma_f32 v[148:149], v[152:153], v[52:53], v[148:149]\n"
      "v_pk_fma_f32 v[32:33], v[148:149], v[54:55], v[32:33]\nv_add_f32 v41, v32, v33\nv_pk_mul_f32 v[44:45], v[164:165], v[164:165]\nv_pk_mul_f32 v[46:47], v[166:167], v[166:167]\n"
      "v_exp_f32 v20, v20 clamp\nv_pk_mul_f32 v[48:49], v[168:169], v[168:169]\nv_pk_mul_f32 v[50:51], v[170:171], v[170:171]\nv_exp_f32 v21, v21 clamp\n"
      "v_pk_fma_f32 v[44:45], v[44:45], s[58:59], s[58:59] op_sel:[0,0,1] op_sel_hi:[1,0,1]\nv_pk_fma_f32 v[46:47], v[46:47], s[58:59], s[58:59] op_sel:[0,0,1] op_sel_hi:[1,0,1]\n"
      "v_pk_fma_f32 v[48:49], v[48:49], s[58:59], s[58:59] op_sel:[0,0,1] op_sel_hi:[1,0,1]\nv_exp_f32 v22, v22 clamp\n"
      "v_pk_fma_f32 v[50:51], v[50:51], s[58:59], s[58:59] op_sel:[0,0,1] op_sel_hi:[1,0,1]\nv_pk_fma_f32 v[44:45], v[44:45], v[164:165], s[60:61] op_sel_hi:[1,1,0]\n"
      "v_pk_fma_f32 v[46:47], v[46:47], v[166:167], s[60:61] op_sel_hi:[1,1,0]\nv_exp_f32 v23, v23 clamp\nv_pk_fma_f32 v[48:49], v[48:49], v[168:169], s[60:61] op_sel_hi:[1,1,0]\n"
      "v_pk_fma_f32 v[50:51], v[50:51], v[170:171], s[60:61] op_sel_hi:[1,1,0]\nv_exp_f32 v44, v44 clamp\nv_pk_fma_f32 v[18:19], v[18:19], v[2:3], v[26:27]\n"
      "v_pk_fma_f32 v[16:17], v[16:17], v[0:1], v[24:25]\nv_pk_fma_f32 v[20:21], v[20:21], v[4:5], v[28:29]\nv_exp_f32 v45, v45 clamp\nv_pk_fma_f32 v[22:23], v[22:23], v[6:7], v[30:31]\n"
      "v_pk_mul_f32 v[156:157], v[156:157], v[18:19]\nv_pk_mul_f32 v[160:161], v[160:161], v[22:23]\nv_exp_f32 v46, v46 clamp\nv_pk_fma_f32 v[156:157], v[158:159], v[16:17], v[156:157]\n"
      "v_pk_fma_f32 v[160:161], v[162:163], v[20:21], v[160:161]\nv_pk_mul_f32 v[16:17], v[16:17], v[18:19]\nv_exp_f32 v47, v47 clamp\nv_pk_mul_f32 v[20:21], v[20:21], v[22:23]\n"
      "v_pk_mul_f32 v[18:19], v[16:17], v[20:21]\nv_rcp_f32 v18, v18\nv_pk_mul_f32 v[156:157], v[156:157], v[20:21]\nv_rcp_f32 v19, v19\nv_pk_fma_f32 v[156:157], v[160:161], v[16:17], v[156:157]\n"
      "v_pk_mul_f32 v[32:33], v[156:157], v[18:19]\nv_pk_mul_f32 v[52:53], v[172:173], v[172:173]\nv_pk_mul_f32 v[54:55], v[174:175], v[174:175]\nv_exp_f32 v48, v48 clamp\n"
      "v_pk_mul_f32 v[56:57], v[176:177], v[176:177]\nv_pk_mul_f32 v[58:59], v[178:179], v[178:179]\nv_exp_f32 v49, v49 clamp\n"
      "v_pk_fma_f32 v[52:53], v[52:53], s[58:59], s[58:59] op_sel:[0,0,1] op_sel_hi:[1,0,1]\nv_pk_fma_f32 v[54:55], v[54:55], s[58:59], s[58:59] op_sel:[0,0,1] op_sel_hi:[1,0,1]\n"
      "v_pk_fma_f32 v[56:57], v[56:57], s[58:59], s[58:59] op_sel:[0,0,1] op_sel_hi:[1,0,1]\nv_exp_f32 v50, v50 clamp\n"
      "v_pk_fma_f32 v[58:59], v[58:59], s[58:59], s[58:59] op_sel:[0,0,1] op_sel_hi:[1,0,1]\nv_pk_fma_f32 v[52:53], v[52:53], v[172:173], s[60:61] op_sel_hi:[1,1,0]\n"
      "v_pk_fma_f32 v[54:55], v[54:55], v[174:175], s[60:61] op_sel_hi:[1,1,0]\nv_exp_f32 v51, v51 clamp\nv_pk_fma_f32 v[56:57], v[56:57], v[176:177], s[60:61] op_sel_hi:[1,1,0]\n"
      "v_pk_fma_f32 v[58:59], v[58:59], v[178:179], s[60:61] op_sel_hi:[1,1,0]\nv_exp_f32 v52, v52 clamp\nv_pk_fma_f32 v[46:47], v[46:47], v[10:11], v[66:67]\n"
      "v_pk_fma_f32 v[44:45], v[44:45], v[8:9], v[64:65]\nv_pk_fma_f32 v[48:49], v[48:49], v[12:13], v[68:69]\nv_exp_f32 v53, v53 clamp\nv_pk_fma_f32 v[50:51], v[50:51], v[14:15], v[70:71]\n"
      "v_pk_mul_f32 v[164:165], v[164:165], v[46:47]\nv_pk_mul_f32 v[168:169], v[168:169], v[50:51]\nv_exp_f32 v54, v54 clamp\nv_pk_fma_f32 v[164:165], v[166:167], v[44:45], v[164:165]\n"
      "v_pk_fma_f32 v[168:169], v[170:171], v[48:49], v[168:169]\nv_pk_mul_f32 v[44:45], v[44:45], v[46:47]\nv_exp_f32 v55, v55 clamp\nv_pk_mul_f32 v[48:49], v[48:49], v[50:51]\n"
      "v_pk_mul_f32 v[46:47], v[44:45], v[48:49]\nv_rcp_f32 v46, v46\nv_pk_mul_f32 v[164:165], v[164:165], v[48:49]\nv_rcp_f32 v47, v47\nv_pk_fma_f32 v[164:165], v[168:169], v[44:45], v[164:165]\n"
      "v_pk_fma_f32 v[32:33], v[164:165], v[46:47], v[32:33]\nv_add_f32 v36, v32, v33\nv_pk_mul_f32 v[16:17], v[180:181], v[180:181]\nv_pk_mul_f32 v[18:19], v[182:183], v[182:183]\n"
      "v_exp_f32 v56, v56 clamp\nv_pk_mul_f32 v[20:21], v[184:185], v[184:185]\nv_pk_mul_f32 v[22:23], v[186:187], v[186:187]\nv_exp_f32 v57, v57 clamp\n"
      "v_pk_fma_f32 v[16:17], v[16:17], s[58:59], s[58:59] op_sel:[0,0,1] op_sel_hi:[1,0,1]\nv_pk_fma_f32 v[18:19], v[18:19], s[58:59], s[58:59] op_sel:[0,0,1] op_sel_hi:[1,0,1]\n"
      "v_pk_fma_f32 v[20:21], v[20:21], s[58:59], s[58:59] op_sel:[0,0,1] op_sel_hi:[1,0,1]\nv_exp_f32 v58, v58 clamp\n"
      "v_pk_fma_f32 v[22:23], v[22:23], s[58:59], s[58:59] op_sel:[0,0,1] op_sel_hi:[1,0,1]\nv_pk_fma_f32 v[16:17], v[16:17], v[180:181], s[60:61] op_sel_hi:[1,1,0]\n"
      "v_pk_fma_f32 v[18:19], v[18:19], v[182:183], s[60:61] op_sel_hi:[1,1,0]\nv_exp_f32 v59, v59 clamp\nv_pk_fma_f32 v[20:21], v[20:21], v[184:185], s[60:61] op_sel_hi:[1,1,0]\n"
      "v_pk_fma_f32 v[22:23], v[22:23], v[186:187], s[60:61] op_sel_hi:[1,1,0]\nv_exp_f32 v16, v16 clamp\nv_pk_fma_f32 v[54:55], v[54:55], v[2:3], v[26:27]\n"
      "v_pk_fma_f32 v[52:53], v[52:53], v[0:1], v[24:25]\nv_pk_fma_f32 v[56:57], v[56:57], v[4:5], v[28:29]\nv_exp_f32 v17, v17 clamp\nv_pk_fma_f32 v[58:59], v[58:59], v[6:7], v[30:31]\n"
      "v_pk_mul_f32 v[172:173], v[172:173], v[54:55]\nv_pk_mul_f32 v[176:177], v[176:177], v[58:59]\nv_exp_f32 v18, v18 clamp\nv_pk_fma_f32 v[172:173], v[174:175], v[52:53], v[172:173]\n"
      "v_pk_fma_f32 v[176:177], v[178:179], v[56:57], v[176:177]\nv_pk_mul_f32 v[52:53], v[52:53], v[54:55]\nv_exp_f32 v19, v19 clamp\nv_pk_mul_f32 v[56:57], v[56:57], v[58:59]\n"
      "v_pk_mul_f32 v[54:55], v[52:53], v[56:57]\nv_rcp_f32 v54, v54\nv_pk_mul_f32 v[172:173], v[172:173], v[56:57]\nv_rcp_f32 v55, v55\nv_pk_fma_f32 v[172:173], v[176:177], v[52:53], v[172:173]\n"
      "v_pk_mul_f32 v[32:33], v[172:173], v[54:55]\nv_pk_mul_f32 v[44:45], v[188:189], v[188:189]\nv_pk_mul_f32 v[46:47], v[190:191], v[190:191]\nv_exp_f32 v20, v20 clamp\n"
      "v_pk_mul_f32 v[48:49], v[192:193], v[192:193]\nv_pk_mul_f32 v[50:51], v[194:195], v[194:195]\nv_exp_f32 v21, v21 clamp\n"
      "v_pk_fma_f32 v[44:45], v[44:45], s[58:59], s[58:59] op_sel:[0,0,1] op_sel_hi:[1,0,1]\nv_pk_fma_f32 v[46:47], v[46:47], s[58:59], s[58:59] op_sel:[0,0,1] op_sel_hi:[1,0,1]\n"
      "v_pk_fma_f32 v[48:49], v[48:49], s[58:59], s[58:59] op_sel:[0,0,1] op_sel_hi:[1,0,1]\nv_exp_f32 v22, v22 clamp\n"
      "v_pk_fma_f32 v[50:51], v[50:51], s[58:59], s[58:59] op_sel:[0,0,1] op_sel_hi:[1,0,1]\nv_pk_fma_f32 v[44:45], v[44:45], v[188:189], s[60:61] op_sel_hi:[1,1,0]\n"
      "v_pk_fma_f32 v[46:47], v[46:47], v[190:191], s[60:61] op_sel_hi:[1,1,0]\nv_exp_f32 v23, v23 clamp\nv_pk_fma_f32 v[48:49], v[48:49], v[192:193], s[60:61] op_sel_hi:[1,1,0]\n"
      "v_pk_fma_f32 v[50:51], v[50:51], v[194:195], s[60:61] op_sel_hi:[1,1,0]\nv_exp_f32 v44, v44 clamp\nv_pk_fma_f32 v[18:19], v[18:19], v[10:11], v[66:67]\n"
      "v_pk_fma_f32 v[16:17], v[16:17], v[8:9], v[64:65]\nv_pk_fma_f32 v[20:21], v[20:21], v[12:13], v[68:69]\nv_exp_f32 v45, v45 clamp\nv_pk_fma_f32 v[22:23], v[22:23], v[14:15], v[70:71]\n"
      "v_pk_mul_f32 v[180:181], v[180:181], v[18:19]\nv_pk_mul_f32 v[184:185], v[184:185], v[22:23]\nv_exp_f32 v46, v46 clamp\nv_pk_fma_f32 v[180:181], v[182:183], v[16:17], v[180:181]\n"
      "v_pk_fma_f32 v[184:185], v[186:187], v[20:21], v[184:185]\nv_pk_mul_f32 v[16:17], v[16:17], v[18:19]\nv_exp_f32 v47, v47 clamp\nv_pk_mul_f32 v[20:21], v[20:21], v[22:23]\n"
      "v_pk_mul_f32 v[18:19], v[16:17], v[20:21]\nv_rcp_f32 v18, v18\nv_pk_mul_f32 v[180:181], v[180:181], v[20:21]\nv_rcp_f32 v19, v19\nv_pk_fma_f32 v[180:181], v[184:185], v[16:17], v[180:181]\n"
      "v_pk_fma_f32 v[32:33], v[180:181], v[18:19], v[32:33]\nv_add_f32 v37, v32, v33\nv_pk_mul_f32 v[52:53], v[196:197], v[196:197]\nv_pk_mul_f32 v[54:55], v[198:199], v[198:199]\n"
      "v_exp_f32 v48, v48 clamp\nv_pk_mul_f32 v[56:57], v[200:201], v[200:201]\nv_pk_mul_f32 v[58:59], v[202:203], v[202:203]\nv_exp_f32 v49, v49 clamp\n"
      "v_pk_fma_f32 v[52:53], v[52:53], s[58:59], s[58:59] op_sel:[0,0,1] op_sel_hi:[1,0,1]\nv_pk_fma_f32 v[54:55], v[54:55], s[58:59], s[58:59] op_sel:[0,0,1] op_sel_hi:[1,0,1]\n"
      "v_pk_fma_f32 v[56:57], v[56:57], s[58:59], s[58:59] op_sel:[0,0,1] op_sel_hi:[1,0,1]\nv_exp_f32 v50, v50 clamp\n"
      "v_pk_fma_f32 v[58:59], v[58:59], s[58:59], s[58:59] op_sel:[0,0,1] op_sel_hi:[1,0,1]\nv_pk_fma_f32 v[52:53], v[52:53], v[196:197], s[60:61] op_sel_hi:[1,1,0]\n"
      "v_pk_fma_f32 v[54:55], v[54:55], v[198:199], s[60:61] op_sel_hi:[1,1,0]\nv_exp_f32 v51, v51 clamp\nv_pk_fma_f32 v[56:57], v[56:57], v[200:201], s[60:61] op_sel_hi:[1,1,0]\n"
      "v_pk_fma_f32 v[58:59], v[58:59], v[202:203], s[60:61] op_sel_hi:[1,1,0]\nv_exp_f32 v52, v52 clamp\nv_pk_fma_f32 v[46:47], v[46:47], v[2:3], v[26:27]\n"
      "v_pk_fma_f32 v[44:45], v[44:45], v[0:1], v[24:25]\nv_pk_fma_f32 v[48:49], v[48:49], v[4:5], v[28:29]\nv_exp_f32 v53, v53 clamp\nv_pk_fma_f32 v[50:51], v[50:51], v[6:7], v[30:31]\n"
      "v_pk_mul_f32 v[188:189], v[188:189], v[46:47]\nv_pk_mul_f32 v[192:193], v[192:193], v[50:51]\nv_exp_f32 v54, v54 clamp\nv_pk_fma_f32 v[188:189], v[190:191], v[44:45], v[188:189]\n"
      "v_pk_fma_f32 v[192:193], v[194:195], v[48:49], v[192:193]\nv_pk_mul_f32 v[44:45], v[44:45], v[46:47]\nv_exp_f32 v55, v55 clamp\nv_pk_mul_f32 v[48:49], v[48:49], v[50:51]\n"
      "v_pk_mul_f32 v[46:47], v[44:45], v[48:49]\nv_rcp_f32 v46, v46\nv_pk_mul_f32 v[188:189], v[188:189], v[48:49]\nv_rcp_f32 v47, v47\nv_pk_fma_f32 v[188:189], v[192:193], v[44:45], v[188:189]\n"
      "v_pk_mul_f32 v[32:33], v[188:189], v[46:47]\nv_pk_mul_f32 v[16:17], v[204:205], v[204:205]\nv_pk_mul_f32 v[18:19], v[206:207], v[206:207]\nv_exp_f32 v56, v56 clamp\n"
      "v_pk_mul_f32 v[20:21], v[208:209], v[208:209]\nv_pk_mul_f32 v[22:23], v[210:211], v[210:211]\nv_exp_f32 v57, v57 clamp\n"
      "v_pk_fma_f32 v[16:17], v[16:17], s[58:59], s[58:59] op_sel:[0,0,1] op_sel_hi:[1,0,1]\nv_pk_fma_f32 v[18:19], v[18:19], s[58:59], s[58:59] op_sel:[0,0,1] op_sel_hi:[1,0,1]\n"
      "v_pk_fma_f32 v[20:21], v[20:21], s[58:59], s[58:59] op_sel:[0,0,1] op_sel_hi:[1,0,1]\nv_exp_f32 v58, v58 clamp\n"
      "v_pk_fma_f32 v[22:23], v[22:23], s[58:59], s[58:59] op_sel:[0,0,1] op_sel_hi:[1,0,1]\nv_pk_fma_f32 v[16:17], v[16:17], v[204:205], s[60:61] op_sel_hi:[1,1,0]\n"
      "v_pk_fma_f32 v[18:19], v[18:19], v[206:207], s[60:61] op_sel_hi:[1,1,0]\nv_exp_f32 v59, v59 clamp\nv_pk_fma_f32 v[20:21], v[20:21], v[208:209], s[60:61] op_sel_hi:[1,1,0]\n"
      "v_pk_fma_f32 v[22:23], v[22:23], v[210:211], s[60:61] op_sel_hi:[1,1,0]\nv_exp_f32 v16, v16 clamp\nv_pk_fma_f32 v[54:55], v[54:55], v[10:11], v[66:67]\n"
      "v_pk_fma_f32 v[52:53], v[52:53], v[8:9], v[64:65]\nv_pk_fma_f32 v[56:57], v[56:57], v[12:13], v[68:69]\nv_exp_f32 v17, v17 clamp\nv_pk_fma_f32 v[58:59], v[58:59], v[14:15], v[70:71]\n"
      "v_pk_mul_f32 v[196:197], v[196:197], v[54:55]\nv_pk_mul_f32 v[200:201], v[200:201], v[58:59]\nv_exp_f32 v18, v18 clamp\nv_pk_fma_f32 v[196:197], v[198:199], v[52:53], v[196:197]\n"
      "v_pk_fma_f32 v[200:201], v[202:203], v[56:57], v[200:201]\nv_pk_mul_f32 v[52:53], v[52:53], v[54:55]\nv_exp_f32 v19, v19 clamp\nv_pk_mul_f32 v[56:57], v[56:57], v[58:59]\n"
      "v_pk_mul_f32 v[54:55], v[52:53], v[56:57]\nv_rcp_f32 v54, v54\nv_pk_mul_f32 v[196:197], v[196:197], v[56:57]\nv_rcp_f32 v55, v55\nv_pk_fma_f32 v[196:197], v[200:201], v[52:53], v[196:197]\n"
      "v_pk_fma_f32 v[32:33], v[196:197], v[54:55], v[32:33]\nv_add_f32 v38, v32, v33\nv_pk_mul_f32 v[44:45], v[212:213], v[212:213]\nv_pk_mul_f32 v[46:47], v[214:215], v[214:215]\n"
      "v_exp_f32 v20, v20 clamp\nv_pk_mul_f32 v[48:49], v[216:217], v[216:217]\nv_pk_mul_f32 v[50:51], v[218:219], v[218:219]\nv_exp_f32 v21, v21 clamp\n"
      "v_pk_fma_f32 v[44:45], v[44:45], s[58:59], s[58:59] op_sel:[0,0,1] op_sel_hi:[1,0,1]\nv_pk_fma_f32 v[46:47], v[46:47], s[58:59], s[58:59] op_sel:[0,0,1] op_sel_hi:[1,0,1]\n"
      "v_pk_fma_f32 v[48:49], v[48:49], s[58:59], s[58:59] op_sel:[0,0,1] op_sel_hi:[1,0,1]\nv_exp_f32 v22, v22 clamp\n"
      "v_pk_fma_f32 v[50:51], v[50:51], s[58:59], s[58:59] op_sel:[0,0,1] op_sel_hi:[1,0,1]\nv_pk_fma_f32 v[44:45], v[44:45], v[212:213], s[60:61] op_sel_hi:[1,1,0]\n"
      "v_pk_fma_f32 v[46:47], v[46:47], v[214:215], s[60:61] op_sel_hi:[1,1,0]\nv_exp_f32 v23, v23 clamp\nv_pk_fma_f32 v[48:49], v[48:49], v[216:217], s[60:61] op_sel_hi:[1,1,0]\n"
      "v_pk_fma_f32 v[50:51], v[50:51], v[218:219], s[60:61] op_sel_hi:[1,1,0]\nv_exp_f32 v44, v44 clamp\nv_pk_fma_f32 v[18:19], v[18:19], v[2:3], v[26:27]\n"
      "v_pk_fma_f32 v[16:17], v[16:17], v[0:1], v[24:25]\nv_pk_fma_f32 v[20:21], v[20:21], v[4:5], v[28:29]\nv_exp_f32 v45, v45 clamp\nv_pk_fma_f32 v[22:23], v[22:23], v[6:7], v[30:31]\n"
      "v_pk_mul_f32 v[204:205], v[204:205], v[18:19]\nv_pk_mul_f32 v[208:209], v[208:209], v[22:23]\nv_exp_f32 v46, v46 clamp\nv_pk_fma_f32 v[204:205], v[206:207], v[16:17], v[204:205]\n"
      "v_pk_fma_f32 v[208:209], v[210:211], v[20:21], v[208:209]\nv_pk_mul_f32 v[16:17], v[16:17], v[18:19]\nv_exp_f32 v47, v47 clamp\nv_pk_mul_f32 v[20:21], v[20:21], v[22:23]\n"
      "v_pk_mul_f32 v[18:19], v[16:17], v[20:21]\nv_rcp_f32 v18, v18\nv_pk_mul_f32 v[204:205], v[204:205], v[20:21]\nv_rcp_f32 v19, v19\nv_pk_fma_f32 v[204:205], v[208:209], v[16:17], v[204:205]\n"
      "v_pk_mul_f32 v[32:33], v[204:205], v[18:19]\nv_pk_mul_f32 v[52:53], v[220:221], v[220:221]\nv_pk_mul_f32 v[54:55], v[222:223], v[222:223]\nv_exp_f32 v48, v48 clamp\n"
      "v_pk_mul_f32 v[56:57], v[224:225], v[224:225]\nv_pk_mul_f32 v[58:59], v[226:227], v[226:227]\nv_exp_f32 v49, v49 clamp\n"
      "v_pk_fma_f32 v[52:53], v[52:53], s[58:59], s[58:59] op_sel:[0,0,1] op_sel_hi:[1,0,1]\nv_pk_fma_f32 v[54:55], v[54:55], s[58:59], s[58:59] op_sel:[0,0,1] op_sel_hi:[1,0,1]\n"
      "v_pk_fma_f32 v[56:57], v[56:57], s[58:59], s[58:59] op_sel:[0,0,1] op_sel_hi:[1,0,1]\nv_exp_f32 v50, v50 clamp\n"
      "v_pk_fma_f32 v[58:59], v[58:59], s[58:59], s[58:59] op_sel:[0,0,1] op_sel_hi:[1,0,1]\nv_pk_fma_f32 v[52:53], v[52:53], v[220:221], s[60:61] op_sel_hi:[1,1,0]\n"
      "v_pk_fma_f32 v[54:55], v[54:55], v[222:223], s[60:61] op_sel_hi:[1,1,0]\nv_exp_f32 v51, v51 clamp\nv_pk_fma_f32 v[56:57], v[56:57], v[224:225], s[60:61] op_sel_hi:[1,1,0]\n"
      "v_pk_fma_f32 v[58:59], v[58:59], v[226:227], s[60:61] op_sel_hi:[1,1,0]\nv_exp_f32 v52, v52 clamp\nv_pk_fma_f32 v[46:47], v[46:47], v[10:11], v[66:67]\n"
      "v_pk_fma_f32 v[44:45], v[44:45], v[8:9], v[64:65]\nv_pk_fma_f32 v[48:49], v[48:49], v[12:13], v[68:69]\nv_exp_f32 v53, v53 clamp\nv_pk_fma_f32 v[50:51], v[50:51], v[14:15], v[70:71]\n"
      "v_pk_mul_f32 v[212:213], v[212:213], v[46:47]\nv_pk_mul_f32 v[216:217], v[216:217], v[50:51]\nv_exp_f32 v54, v54 clamp\nv_pk_fma_f32 v[212:213], v[214:215], v[44:45], v[212:213]\n"
      "v_pk_fma_f32 v[216:217], v[218:219], v[48:49], v[216:217]\nv_pk_mul_f32 v[44:45], v[44:45], v[46:47]\nv_exp_f32 v55, v55 clamp\nv_pk_mul_f32 v[48:49], v[48:49], v[50:51]\n"
      "v_pk_mul_f32 v[46:47], v[44:45], v[48:49]\nv_rcp_f32 v46, v46\nv_pk_mul_f32 v[212:213], v[212:213], v[48:49]\nv_rcp_f32 v47, v47\nv_pk_fma_f32 v[212:213], v[216:217], v[44:45], v[212:213]\n"
      "v_pk_fma_f32 v[32:33], v[212:213], v[46:47], v[32:33]\nv_add_f32 v39, v32, v33\nv_pk_mul_f32 v[16:17], v[228:229], v[228:229]\nv_pk_mul_f32 v[18:19], v[230:231], v[230:231]\n"
      "v_exp_f32 v56, v56 clamp\nv_pk_mul_f32 v[20:21], v[232:233], v[232:233]\nv_pk_mul_f32 v[22:23], v[234:235], v[234:235]\nv_exp_f32 v57, v57 clamp\n"
      "v_pk_fma_f32 v[16:17], v[16:17], s[58:59], s[58:59] op_sel:[0,0,1] op_sel_hi:[1,0,1]\nv_pk_fma_f32 v[18:19], v[18:19], s[58:59], s[58:59] op_sel:[0,0,1] op_sel_hi:[1,0,1]\n"
      "v_pk_fma_f32 v[20:21], v[20:21], s[58:59], s[58:59] op_sel:[0,0,1] op_sel_hi:[1,0,1]\nv_exp_f32 v58, v58 clamp\n"
      "v_pk_fma_f32 v[22:23], v[22:23], s[58:59], s[58:59] op_sel:[0,0,1] op_sel_hi:[1,0,1]\nv_pk_fma_f32 v[16:17], v[16:17], v[228:229], s[60:61] op_sel_hi:[1,1,0]\n"
      "v_pk_fma_f32 v[18:19], v[18:19], v[230:231], s[60:61] op_sel_hi:[1,1,0]\nv_exp_f32 v59, v59 clamp\nv_pk_fma_f32 v[20:21], v[20:21], v[232:233], s[60:61] op_sel_hi:[1,1,0]\n"
      "v_pk_fma_f32 v[22:23], v[22:23], v[234:235], s[60:61] op_sel_hi:[1,1,0]\nv_exp_f32 v16, v16 clamp\nv_pk_fma_f32 v[54:55], v[54:55], v[2:3], v[26:27]\nv_exp_f32 v17, v17 clamp\n"
      "v_pk_fma_f32 v[52:53], v[52:53], v[0:1], v[24:25]\nv_pk_fma_f32 v[56:57], v[56:57], v[4:5], v[28:29]\nv_exp_f32 v18, v18 clamp\nv_pk_fma_f32 v[58:59], v[58:59], v[6:7], v[30:31]\n"
      "v_pk_mul_f32 v[220:221], v[220:221], v[54:55]\nv_exp_f32 v19, v19 clamp\nv_pk_mul_f32 v[224:225], v[224:225], v[58:59]\nv_pk_fma_f32 v[220:221], v[222:223], v[52:53], v[220:221]\n"
      "v_exp_f32 v20, v20 clamp\nv_pk_fma_f32 v[224:225], v[226:227], v[56:57], v[224:225]\nv_exp_f32 v21, v21 clamp\nv_pk_mul_f32 v[52:53], v[52:53], v[54:55]\n"
      "v_pk_mul_f32 v[56:57], v[56:57], v[58:59]\nv_exp_f32 v22, v22 clamp\nv_pk_mul_f32 v[54:55], v[52:53], v[56:57]\nv_rcp_f32 v54, v54\nv_pk_mul_f32 v[220:221], v[220:221], v[56:57]\n"
      "v_exp_f32 v23, v23 clamp\nv_rcp_f32 v55, v55\nv_pk_fma_f32 v[220:221], v[224:225], v[52:53], v[220:221]\nv_pk_mul_f32 v[32:33], v[220:221], v[54:55]\n"
      "v_pk_fma_f32 v[18:19], v[18:19], v[10:11], v[66:67]\nv_pk_fma_f32 v[16:17], v[16:17], v[8:9], v[64:65]\nv_pk_fma_f32 v[20:21], v[20:21], v[12:13], v[68:69]\n"
      "v_pk_fma_f32 v[22:23], v[22:23], v[14:15], v[70:71]\nv_pk_mul_f32 v[228:229], v[228:229], v[18:19]\nv_pk_mul_f32 v[232:233], v[232:233], v[22:23]\n"
      "v_pk_fma_f32 v[228:229], v[230:231], v[16:17], v[228:229]\nv_pk_fma_f32 v[232:233], v[234:235], v[20:21], v[232:233]\nv_pk_mul_f32 v[16:17], v[16:17], v[18:19]\n"
      "v_pk_mul_f32 v[20:21], v[20:21], v[22:23]\nv_pk_mul_f32 v[18:19], v[16:17], v[20:21]\nv_rcp_f32 v18, v18\nv_pk_mul_f32 v[228:229], v[228:229], v[20:21]\nv_rcp_f32 v19, v19\n"
      "v_pk_fma_f32 v[228:229], v[232:233], v[16:17], v[228:229]\nv_pk_fma_f32 v[32:33], v[228:229], v[18:19], v[32:33]\nv_add_f32 v40, v32, v33\ns_barrier\ns_branch .Lge\n"
      ".Lg13:\ns_waitcnt vmcnt(4)\ns_barrier\nds_read_b64_tr_b16 v[64:65], v104 offset:45056\nds_read_b64_tr_b16 v[66:67], v104 offset:46080\nds_read_b64_tr_b16 v[68:69], v104 offset:53248\n"
      "ds_read_b64_tr_b16 v[70:71], v104 offset:54272\nds_read_b64_tr_b16 v[72:73], v105 offset:45056\nds_read_b64_tr_b16 v[74:75], v105 offset:46080\n"
      "ds_read_b64_tr_b16 v[76:77], v105 offset:53248\nds_read_b64_tr_b16 v[78:79], v105 offset:54272\nds_read_b64_tr_b16 v[80:81], v106 offset:45056\n"
      "ds_read_b64_tr_b16 v[82:83], v106 offset:46080\nds_read_b64_tr_b16 v[84:85], v106 offset:53248\nds_read_b64_tr_b16 v[86:87], v106 offset:54272\n"
      "ds_read_b64_tr_b16 v[88:89], v107 offset:45056\nds_read_b64_tr_b16 v[90:91], v107 offset:46080\nds_read_b64_tr_b16 v[92:93], v107 offset:53248\n"
      "ds_read_b64_tr_b16 v[94:95], v107 offset:54272\nds_read_b128 v[0:3], v100 offset:45056\nds_read_b128 v[4:7], v101 offset:45056\nds_read_b128 v[8:11], v120 offset:45056\n"
      "ds_read_b128 v[12:15], v121 offset:45056\nds_read_b128 v[16:19], v96 offset:45056\nds_read_b128 v[20:23], v97 offset:45056\ns_waitcnt lgkmcnt(2)\n.p2align 3\n"
      ".Lgl13:\nv_mfma_f32_16x16x32_bf16 v[124:127], v[64:67], v[0:3], v[124:127]\nds_read_b128 v[24:27], v96 offset:47104\nv_mfma_f32_16x16x32_bf16 v[124:127], v[68:71], v[4:7], v[124:127]\n"
      "ds_read_b128 v[28:31], v97 offset:47104\nv_mfma_f32_16x16x32_bf16 v[128:131], v[76:79], v[4:7], v[128:131]\nv_mfma_f32_16x16x32_bf16 v[128:131], v[72:75], v[0:3], v[128:131]\n"
      "v_mfma_f32_16x16x32_bf16 v[152:155], v[88:91], v[8:11], v[152:155]\nds_read_b128 v[0:3], v96 offset:49152\nv_mfma_f32_16x16x32_bf16 v[152:155], v[92:95], v[12:15], v[152:155]\n"
      "ds_read_b128 v[4:7], v97 offset:49152\nv_mfma_f32_16x16x32_bf16 v[148:151], v[84:87], v[12:15], v[148:151]\nv_mfma_f32_16x16x32_bf16 v[148:151], v[80:83], v[8:11], v[148:151]\n"
      "v_mfma_f32_16x16x32_bf16 v[144:147], v[72:75], v[8:11], v[144:147]\nv_mfma_f32_16x16x32_bf16 v[144:147], v[76:79], v[12:15], v[144:147]\n"
      "v_mfma_f32_16x16x32_bf16 v[140:143], v[68:71], v[12:15], v[140:143]\ns_waitcnt lgkmcnt(4)\nv_mfma_f32_16x16x32_bf16 v[140:143], v[64:67], v[8:11], v[140:143]\n"
      "v_mfma_f32_16x16x32_bf16 v[156:159], v[64:67], v[16:19], v[156:159]\nds_read_b128 v[8:11], v96 offset:51200\nv_mfma_f32_16x16x32_bf16 v[156:159], v[68:71], v[20:23], v[156:159]\n"
      "ds_read_b128 v[12:15], v97 offset:51200\nv_mfma_f32_16x16x32_bf16 v[160:163], v[76:79], v[20:23], v[160:163]\nv_mfma_f32_16x16x32_bf16 v[160:163], v[72:75], v[16:19], v[160:163]\n"
      "v_mfma_f32_16x16x32_bf16 v[164:167], v[80:83], v[16:19], v[164:167]\nv_mfma_f32_16x16x32_bf16 v[164:167], v[84:87], v[20:23], v[164:167]\n"
      "v_mfma_f32_16x16x32_bf16 v[168:171], v[92:95], v[20:23], v[168:171]\ns_waitcnt lgkmcnt(2)\nv_mfma_f32_16x16x32_bf16 v[168:171], v[88:91], v[16:19], v[168:171]\n"
      "v_mfma_f32_16x16x32_bf16 v[184:187], v[88:91], v[24:27], v[184:187]\nds_read_b128 v[16:19], v96 offset:53248\nv_mfma_f32_16x16x32_bf16 v[184:187], v[92:95], v[28:31], v[184:187]\n"
      "ds_read_b128 v[20:23], v97 offset:53248\nv_mfma_f32_16x16x32_bf16 v[180:183], v[84:87], v[28:31], v[180:183]\nv_mfma_f32_16x16x32_bf16 v[180:183], v[80:83], v[24:27], v[180:183]\n"
      "v_mfma_f32_16x16x32_bf16 v[176:179], v[72:75], v[24:27], v[176:179]\nv_mfma_f32_16x16x32_bf16 v[176:179], v[76:79], v[28:31], v[176:179]\n"
      "v_mfma_f32_16x16x32_bf16 v[172:175], v[68:71], v[28:31], v[172:175]\ns_waitcnt vmcnt(0) lgkmcnt(0)\ns_barrier\nv_mfma_f32_16x16x32_bf16 v[172:175], v[64:67], v[24:27], v[172:175]\n"
      "ds_read_b64_tr_b16 v[32:33], v108 offset:0\nv_mfma_f32_16x16x32_bf16 v[188:191], v[64:67], v[0:3], v[188:191]\nds_read_b64_tr_b16 v[34:35], v108 offset:1024\n"
      "v_mfma_f32_16x16x32_bf16 v[188:191], v[68:71], v[4:7], v[188:191]\nds_read_b64_tr_b16 v[36:37], v108 offset:8192\nv_mfma_f32_16x16x32_bf16 v[192:195], v[76:79], v[4:7], v[192:195]\n"
      "ds_read_b64_tr_b16 v[38:39], v108 offset:9216\nv_mfma_f32_16x16x32_bf16 v[192:195], v[72:75], v[0:3], v[192:195]\nds_read_b64_tr_b16 v[40:41], v109 offset:0\n"
      "v_mfma_f32_16x16x32_bf16 v[196:199], v[80:83], v[0:3], v[196:199]\nds_read_b64_tr_b16 v[42:43], v109 offset:1024\nv_mfma_f32_16x16x32_bf16 v[196:199], v[84:87], v[4:7], v[196:199]\n"
      "ds_read_b64_tr_b16 v[44:45], v109 offset:8192\nv_mfma_f32_16x16x32_bf16 v[200:203], v[92:95], v[4:7], v[200:203]\nds_read_b64_tr_b16 v[46:47], v109 offset:9216\n"
      "v_mfma_f32_16x16x32_bf16 v[200:203], v[88:91], v[0:3], v[200:203]\nds_read_b64_tr_b16 v[48:49], v110 offset:0\nv_mfma_f32_16x16x32_bf16 v[216:219], v[88:91], v[8:11], v[216:219]\n"
      "ds_read_b128 v[0:3], v102 offset:0\nv_mfma_f32_16x16x32_bf16 v[216:219], v[92:95], v[12:15], v[216:219]\nds_read_b128 v[4:7], v103 offset:0\n"
      "v_mfma_f32_16x16x32_bf16 v[212:215], v[84:87], v[12:15], v[212:215]\nds_read_b64_tr_b16 v[50:51], v110 offset:1024\nv_mfma_f32_16x16x32_bf16 v[212:215], v[80:83], v[8:11], v[212:215]\n"
      "ds_read_b64_tr_b16 v[52:53], v110 offset:8192\nv_mfma_f32_16x16x32_bf16 v[208:211], v[72:75], v[8:11], v[208:211]\nds_read_b64_tr_b16 v[54:55], v110 offset:9216\n"
      "v_mfma_f32_16x16x32_bf16 v[208:211], v[76:79], v[12:15], v[208:211]\nds_read_b64_tr_b16 v[56:57], v111 offset:0\nv_mfma_f32_16x16x32_bf16 v[204:207], v[68:71], v[12:15], v[204:207]\n"
      "ds_read_b64_tr_b16 v[58:59], v111 offset:1024\nv_mfma_f32_16x16x32_bf16 v[204:207], v[64:67], v[8:11], v[204:207]\nds_read_b64_tr_b16 v[60:61], v111 offset:8192\n"
      "v_mfma_f32_16x16x32_bf16 v[220:223], v[64:67], v[16:19], v[220:223]\nds_read_b128 v[8:11], v122 offset:0\nv_mfma_f32_16x16x32_bf16 v[220:223], v[68:71], v[20:23], v[220:223]\n"
      "ds_read_b128 v[12:15], v123 offset:0\nv_mfma_f32_16x16x32_bf16 v[224:227], v[76:79], v[20:23], v[224:227]\nds_read_b64_tr_b16 v[62:63], v111 offset:9216\n"
      "v_mfma_f32_16x16x32_bf16 v[224:227], v[72:75], v[16:19], v[224:227]\nv_mfma_f32_16x16x32_bf16 v[228:231], v[80:83], v[16:19], v[228:231]\n"
      "v_mfma_f32_16x16x32_bf16 v[228:231], v[84:87], v[20:23], v[228:231]\nv_mfma_f32_16x16x32_bf16 v[232:235], v[92:95], v[20:23], v[232:235]\ns_waitcnt lgkmcnt(0)\n"
      "v_mfma_f32_16x16x32_bf16 v[232:235], v[88:91], v[16:19], v[232:235]\nds_read_b128 v[16:19], v98 offset:0\nds_read_b128 v[20:23], v99 offset:0\n"
      "v_mfma_f32_16x16x32_bf16 v[124:127], v[32:35], v[0:3], v[124:127]\nds_read_b128 v[24:27], v98 offset:2048\nv_mfma_f32_16x16x32_bf16 v[124:127], v[36:39], v[4:7], v[124:127]\n"
      "ds_read_b128 v[28:31], v99 offset:2048\nv_mfma_f32_16x16x32_bf16 v[128:131], v[44:47], v[4:7], v[128:131]\nv_mfma_f32_16x16x32_bf16 v[128:131], v[40:43], v[0:3], v[128:131]\n"
      "v_mfma_f32_16x16x32_bf16 v[152:155], v[56:59], v[8:11], v[152:155]\nds_read_b128 v[0:3], v98 offset:4096\nv_mfma_f32_16x16x32_bf16 v[152:155], v[60:63], v[12:15], v[152:155]\n"
      "ds_read_b128 v[4:7], v99 offset:4096\nv_mfma_f32_16x16x32_bf16 v[148:151], v[52:55], v[12:15], v[148:151]\nv_mfma_f32_16x16x32_bf16 v[148:151], v[48:51], v[8:11], v[148:151]\n"
      "v_mfma_f32_16x16x32_bf16 v[144:147], v[40:43], v[8:11], v[144:147]\nv_mfma_f32_16x16x32_bf16 v[144:147], v[44:47], v[12:15], v[144:147]\n"
      "v_mfma_f32_16x16x32_bf16 v[140:143], v[36:39], v[12:15], v[140:143]\ns_waitcnt lgkmcnt(4)\nv_mfma_f32_16x16x32_bf16 v[140:143], v[32:35], v[8:11], v[140:143]\n"
      "v_mfma_f32_16x16x32_bf16 v[156:159], v[32:35], v[16:19], v[156:159]\nds_read_b128 v[8:11], v98 offset:6144\nv_mfma_f32_16x16x32_bf16 v[156:159], v[36:39], v[20:23], v[156:159]\n"
      "ds_read_b128 v[12:15], v99 offset:6144\nv_mfma_f32_16x16x32_bf16 v[160:163], v[44:47], v[20:23], v[160:163]\nv_mfma_f32_16x16x32_bf16 v[160:163], v[40:43], v[16:19], v[160:163]\n"
      "v_mfma_f32_16x16x32_bf16 v[164:167], v[48:51], v[16:19], v[164:167]\nv_mfma_f32_16x16x32_bf16 v[164:167], v[52:55], v[20:23], v[164:167]\n"
      "v_mfma_f32_16x16x32_bf16 v[168:171], v[60:63], v[20:23], v[168:171]\ns_waitcnt lgkmcnt(2)\nv_mfma_f32_16x16x32_bf16 v[168:171], v[56:59], v[16:19], v[168:171]\n"
      "v_mfma_f32_16x16x32_bf16 v[184:187], v[56:59], v[24:27], v[184:187]\nds_read_b128 v[16:19], v98 offset:8192\nv_mfma_f32_16x16x32_bf16 v[184:187], v[60:63], v[28:31], v[184:187]\n"
      "ds_read_b128 v[20:23], v99 offset:8192\nv_mfma_f32_16x16x32_bf16 v[180:183], v[52:55], v[28:31], v[180:183]\nv_mfma_f32_16x16x32_bf16 v[180:183], v[48:51], v[24:27], v[180:183]\n"
      "v_mfma_f32_16x16x32_bf16 v[176:179], v[40:43], v[24:27], v[176:179]\nv_mfma_f32_16x16x32_bf16 v[176:179], v[44:47], v[28:31], v[176:179]\n"
      "v_mfma_f32_16x16x32_bf16 v[172:175], v[36:39], v[28:31], v[172:175]\ns_waitcnt vmcnt(0) lgkmcnt(0)\ns_barrier\nv_mfma_f32_16x16x32_bf16 v[172:175], v[32:35], v[24:27], v[172:175]\n"
      "ds_read_b64_tr_b16 v[64:65], v104 offset:0\nv_mfma_f32_16x16x32_bf16 v[188:191], v[32:35], v[0:3], v[188:191]\nds_read_b64_tr_b16 v[66:67], v104 offset:1024\n"
      "v_mfma_f32_16x16x32_bf16 v[188:191], v[36:39], v[4:7], v[188:191]\nds_read_b64_tr_b16 v[68:69], v104 offset:8192\nv_mfma_f32_16x16x32_bf16 v[192:195], v[44:47], v[4:7], v[192:195]\n"
      "ds_read_b64_tr_b16 v[70:71], v104 offset:9216\nv_mfma_f32_16x16x32_bf16 v[192:195], v[40:43], v[0:3], v[192:195]\nds_read_b64_tr_b16 v[72:73], v105 offset:0\n"
      "v_mfma_f32_16x16x32_bf16 v[196:199], v[48:51], v[0:3], v[196:199]\nds_read_b64_tr_b16 v[74:75], v105 offset:1024\nv_mfma_f32_16x16x32_bf16 v[196:199], v[52:55], v[4:7], v[196:199]\n"
      "ds_read_b64_tr_b16 v[76:77], v105 offset:8192\nv_mfma_f32_16x16x32_bf16 v[200:203], v[60:63], v[4:7], v[200:203]\nds_read_b64_tr_b16 v[78:79], v105 offset:9216\n"
      "v_mfma_f32_16x16x32_bf16 v[200:203], v[56:59], v[0:3], v[200:203]\nds_read_b64_tr_b16 v[80:81], v106 offset:0\nv_mfma_f32_16x16x32_bf16 v[216:219], v[56:59], v[8:11], v[216:219]\n"
      "ds_read_b128 v[0:3], v100 offset:0\nv_mfma_f32_16x16x32_bf16 v[216:219], v[60:63], v[12:15], v[216:219]\nds_read_b128 v[4:7], v101 offset:0\n"
      "v_mfma_f32_16x16x32_bf16 v[212:215], v[52:55], v[12:15], v[212:215]\nds_read_b64_tr_b16 v[82:83], v106 offset:1024\nv_mfma_f32_16x16x32_bf16 v[212:215], v[48:51], v[8:11], v[212:215]\n"
      "ds_read_b64_tr_b16 v[84:85], v106 offset:8192\nv_mfma_f32_16x16x32_bf16 v[208:211], v[40:43], v[8:11], v[208:211]\nds_read_b64_tr_b16 v[86:87], v106 offset:9216\n"
      "v_mfma_f32_16x16x32_bf16 v[208:211], v[44:47], v[12:15], v[208:211]\nds_read_b64_tr_b16 v[88:89], v107 offset:0\nv_mfma_f32_16x16x32_bf16 v[204:207], v[36:39], v[12:15], v[204:207]\n"
      "ds_read_b64_tr_b16 v[90:91], v107 offset:1024\nv_mfma_f32_16x16x32_bf16 v[204:207], v[32:35], v[8:11], v[204:207]\nds_read_b64_tr_b16 v[92:93], v107 offset:8192\n"
      "v_mfma_f32_16x16x32_bf16 v[220:223], v[32:35], v[16:19], v[220:223]\nds_read_b128 v[8:11], v120 offset:0\nv_mfma_f32_16x16x32_bf16 v[220:223], v[36:39], v[20:23], v[220:223]\n"
      "ds_read_b128 v[12:15], v121 offset:0\nv_mfma_f32_16x16x32_bf16 v[224:227], v[44:47], v[20:23], v[224:227]\nds_read_b64_tr_b16 v[94:95], v107 offset:9216\n"
      "v_mfma_f32_16x16x32_bf16 v[224:227], v[40:43], v[16:19], v[224:227]\nv_mfma_f32_16x16x32_bf16 v[228:231], v[48:51], v[16:19], v[228:231]\n"
      "v_mfma_f32_16x16x32_bf16 v[228:231], v[52:55], v[20:23], v[228:231]\nv_mfma_f32_16x16x32_bf16 v[232:235], v[60:63], v[20:23], v[232:235]\ns_waitcnt lgkmcnt(0)\n"
      "v_mfma_f32_16x16x32_bf16 v[232:235], v[56:59], v[16:19], v[232:235]\nds_read_b128 v[16:19], v96 offset:0\nds_read_b128 v[20:23], v97 offset:0\n"
      "v_mfma_f32_16x16x32_bf16 v[124:127], v[64:67], v[0:3], v[124:127]\nds_read_b128 v[24:27], v96 offset:2048\nv_mfma_f32_16x16x32_bf16 v[124:127], v[68:71], v[4:7], v[124:127]\n"
      "ds_read_b128 v[28:31], v97 offset:2048\nv_mfma_f32_16x16x32_bf16 v[128:131], v[76:79], v[4:7], v[128:131]\nv_mfma_f32_16x16x32_bf16 v[128:131], v[72:75], v[0:3], v[128:131]\n"
      "v_mfma_f32_16x16x32_bf16 v[152:155], v[88:91], v[8:11], v[152:155]\nds_read_b128 v[0:3], v96 offset:4096\nv_mfma_f32_16x16x32_bf16 v[152:155], v[92:95], v[12:15], v[152:155]\n"
      "ds_read_b128 v[4:7], v97 offset:4096\nv_mfma_f32_16x16x32_bf16 v[148:151], v[84:87], v[12:15], v[148:151]\nv_mfma_f32_16x16x32_bf16 v[148:151], v[80:83], v[8:11], v[148:151]\n"
      "v_mfma_f32_16x16x32_bf16 v[144:147], v[72:75], v[8:11], v[144:147]\nv_mfma_f32_16x16x32_bf16 v[144:147], v[76:79], v[12:15], v[144:147]\n"
      "v_mfma_f32_16x16x32_bf16 v[140:143], v[68:71], v[12:15], v[140:143]\ns_waitcnt lgkmcnt(4)\nv_mfma_f32_16x16x32_bf16 v[140:143], v[64:67], v[8:11], v[140:143]\n"
      "v_mfma_f32_16x16x32_bf16 v[156:159], v[64:67], v[16:19], v[156:159]\nds_read_b128 v[8:11], v96 offset:6144\nv_mfma_f32_16x16x32_bf16 v[156:159], v[68:71], v[20:23], v[156:159]\n"
      "ds_read_b128 v[12:15], v97 offset:6144\nv_mfma_f32_16x16x32_bf16 v[160:163], v[76:79], v[20:23], v[160:163]\nv_mfma_f32_16x16x32_bf16 v[160:163], v[72:75], v[16:19], v[160:163]\n"
      "v_mfma_f32_16x16x32_bf16 v[164:167], v[80:83], v[16:19], v[164:167]\nv_mfma_f32_16x16x32_bf16 v[164:167], v[84:87], v[20:23], v[164:167]\n"
      "v_mfma_f32_16x16x32_bf16 v[168:171], v[92:95], v[20:23], v[168:171]\ns_waitcnt lgkmcnt(2)\nv_mfma_f32_16x16x32_bf16 v[168:171], v[88:91], v[16:19], v[168:171]\n"
      "v_mfma_f32_16x16x32_bf16 v[184:187], v[88:91], v[24:27], v[184:187]\nds_read_b128 v[16:19], v96 offset:8192\nv_mfma_f32_16x16x32_bf16 v[184:187], v[92:95], v[28:31], v[184:187]\n"
      "ds_read_b128 v[20:23], v97 offset:8192\nv_mfma_f32_16x16x32_bf16 v[180:183], v[84:87], v[28:31], v[180:183]\nv_mfma_f32_16x16x32_bf16 v[180:183], v[80:83], v[24:27], v[180:183]\n"
      "v_mfma_f32_16x16x32_bf16 v[176:179], v[72:75], v[24:27], v[176:179]\nv_mfma_f32_16x16x32_bf16 v[176:179], v[76:79], v[28:31], v[176:179]\n"
      "v_mfma_f32_16x16x32_bf16 v[172:175], v[68:71], v[28:31], v[172:175]\ns_waitcnt vmcnt(0) lgkmcnt(0)\ns_barrier\nv_mfma_f32_16x16x32_bf16 v[172:175], v[64:67], v[24:27], v[172:175]\n"
      "ds_read_b64_tr_b16 v[32:33], v104 offset:45056\nv_mfma_f32_16x16x32_bf16 v[188:191], v[64:67], v[0:3], v[188:191]\nds_read_b64_tr_b16 v[34:35], v104 offset:46080\n"
      "v_mfma_f32_16x16x32_bf16 v[188:191], v[68:71], v[4:7], v[188:191]\nds_read_b64_tr_b16 v[36:37], v104 offset:53248\nv_mfma_f32_16x16x32_bf16 v[192:195], v[76:79], v[4:7], v[192:195]\n"
      "ds_read_b64_tr_b16 v[38:39], v104 offset:54272\nv_mfma_f32_16x16x32_bf16 v[192:195], v[72:75], v[0:3], v[192:195]\nds_read_b64_tr_b16 v[40:41], v105 offset:45056\n"
      "v_mfma_f32_16x16x32_bf16 v[196:199], v[80:83], v[0:3], v[196:199]\nds_read_b64_tr_b16 v[42:43], v105 offset:46080\nv_mfma_f32_16x16x32_bf16 v[196:199], v[84:87], v[4:7], v[196:199]\n"
      "ds_read_b64_tr_b16 v[44:45], v105 offset:53248\nv_mfma_f32_16x16x32_bf16 v[200:203], v[92:95], v[4:7], v[200:203]\nds_read_b64_tr_b16 v[46:47], v105 offset:54272\n"
      "v_mfma_f32_16x16x32_bf16 v[200:203], v[88:91], v[0:3], v[200:203]\nds_read_b64_tr_b16 v[48:49], v106 offset:45056\nv_mfma_f32_16x16x32_bf16 v[216:219], v[88:91], v[8:11], v[216:219]\n"
      "ds_read_b128 v[0:3], v100 offset:45056\nv_mfma_f32_16x16x32_bf16 v[216:219], v[92:95], v[12:15], v[216:219]\nds_read_b128 v[4:7], v101 offset:45056\n"
      "v_mfma_f32_16x16x32_bf16 v[212:215], v[84:87], v[12:15], v[212:215]\nds_read_b64_tr_b16 v[50:51], v106 offset:46080\nv_mfma_f32_16x16x32_bf16 v[212:215], v[80:83], v[8:11], v[212:215]\n"
      "ds_read_b64_tr_b16 v[52:53], v106 offset:53248\nv_mfma_f32_16x16x32_bf16 v[208:211], v[72:75], v[8:11], v[208:211]\nds_read_b64_tr_b16 v[54:55], v106 offset:54272\n"
      "v_mfma_f32_16x16x32_bf16 v[208:211], v[76:79], v[12:15], v[208:211]\nds_read_b64_tr_b16 v[56:57], v107 offset:45056\nv_mfma_f32_16x16x32_bf16 v[204:207], v[68:71], v[12:15], v[204:207]\n"
      "ds_read_b64_tr_b16 v[58:59], v107 offset:46080\nv_mfma_f32_16x16x32_bf16 v[204:207], v[64:67], v[8:11], v[204:207]\nds_read_b64_tr_b16 v[60:61], v107 offset:53248\n"
      "v_mfma_f32_16x16x32_bf16 v[220:223], v[64:67], v[16:19], v[220:223]\nds_read_b128 v[8:11], v120 offset:45056\nv_mfma_f32_16x16x32_bf16 v[220:223], v[68:71], v[20:23], v[220:223]\n"
      "ds_read_b128 v[12:15], v121 offset:45056\nv_mfma_f32_16x16x32_bf16 v[224:227], v[76:79], v[20:23], v[224:227]\nds_read_b64_tr_b16 v[62:63], v107 offset:54272\n"
      "v_mfma_f32_16x16x32_bf16 v[224:227], v[72:75], v[16:19], v[224:227]\nv_mfma_f32_16x16x32_bf16 v[228:231], v[80:83], v[16:19], v[228:231]\n"
      "v_mfma_f32_16x16x32_bf16 v[228:231], v[84:87], v[20:23], v[228:231]\nv_mfma_f32_16x16x32_bf16 v[232:235], v[92:95], v[20:23], v[232:235]\ns_waitcnt lgkmcnt(0)\n"
      "v_mfma_f32_16x16x32_bf16 v[232:235], v[88:91], v[16:19], v[232:235]\nds_read_b128 v[16:19], v96 offset:45056\nds_read_b128 v[20:23], v97 offset:45056\n"
      "v_mfma_f32_16x16x32_bf16 v[124:127], v[32:35], v[0:3], v[124:127]\nds_read_b128 v[24:27], v96 offset:47104\nv_mfma_f32_16x16x32_bf16 v[124:127], v[36:39], v[4:7], v[124:127]\n"
      "ds_read_b128 v[28:31], v97 offset:47104\nv_mfma_f32_16x16x32_bf16 v[128:131], v[44:47], v[4:7], v[128:131]\nv_mfma_f32_16x16x32_bf16 v[128:131], v[40:43], v[0:3], v[128:131]\n"
      "v_mfma_f32_16x16x32_bf16 v[152:155], v[56:59], v[8:11], v[152:155]\nds_read_b128 v[0:3], v96 offset:49152\nv_mfma_f32_16x16x32_bf16 v[152:155], v[60:63], v[12:15], v[152:155]\n"
      "ds_read_b128 v[4:7], v97 offset:49152\nv_mfma_f32_16x16x32_bf16 v[148:151], v[52:55], v[12:15], v[148:151]\nv_mfma_f32_16x16x32_bf16 v[148:151], v[48:51], v[8:11], v[148:151]\n"
      "v_mfma_f32_16x16x32_bf16 v[144:147], v[40:43], v[8:11], v[144:147]\nv_mfma_f32_16x16x32_bf16 v[144:147], v[44:47], v[12:15], v[144:147]\n"
      "v_mfma_f32_16x16x32_bf16 v[140:143], v[36:39], v[12:15], v[140:143]\ns_waitcnt lgkmcnt(4)\nv_mfma_f32_16x16x32_bf16 v[140:143], v[32:35], v[8:11], v[140:143]\n"
      "v_mfma_f32_16x16x32_bf16 v[156:159], v[32:35], v[16:19], v[156:159]\nds_read_b128 v[8:11], v96 offset:51200\nv_mfma_f32_16x16x32_bf16 v[156:159], v[36:39], v[20:23], v[156:159]\n"
      "ds_read_b128 v[12:15], v97 offset:51200\nv_mfma_f32_16x16x32_bf16 v[160:163], v[44:47], v[20:23], v[160:163]\nv_mfma_f32_16x16x32_bf16 v[160:163], v[40:43], v[16:19], v[160:163]\n"
      "v_mfma_f32_16x16x32_bf16 v[164:167], v[48:51], v[16:19], v[164:167]\nv_mfma_f32_16x16x32_bf16 v[164:167], v[52:55], v[20:23], v[164:167]\n"
      "v_mfma_f32_16x16x32_bf16 v[168:171], v[60:63], v[20:23], v[168:171]\ns_waitcnt lgkmcnt(2)\nv_mfma_f32_16x16x32_bf16 v[168:171], v[56:59], v[16:19], v[168:171]\n"
      "v_mfma_f32_16x16x32_bf16 v[184:187], v[56:59], v[24:27], v[184:187]\nds_read_b128 v[16:19], v96 offset:53248\nv_mfma_f32_16x16x32_bf16 v[184:187], v[60:63], v[28:31], v[184:187]\n"
      "ds_read_b128 v[20:23], v97 offset:53248\nv_mfma_f32_16x16x32_bf16 v[180:183], v[52:55], v[28:31], v[180:183]\nv_mfma_f32_16x16x32_bf16 v[180:183], v[48:51], v[24:27], v[180:183]\n"
      "v_mfma_f32_16x16x32_bf16 v[176:179], v[40:43], v[24:27], v[176:179]\nv_mfma_f32_16x16x32_bf16 v[176:179], v[44:47], v[28:31], v[176:179]\n"
      "v_mfma_f32_16x16x32_bf16 v[172:175], v[36:39], v[28:31], v[172:175]\ns_waitcnt vmcnt(0) lgkmcnt(0)\ns_barrier\nv_mfma_f32_16x16x32_bf16 v[172:175], v[32:35], v[24:27], v[172:175]\n"
      "ds_read_b64_tr_b16 v[64:65], v108 offset:0\nv_mfma_f32_16x16x32_bf16 v[188:191], v[32:35], v[0:3], v[188:191]\nds_read_b64_tr_b16 v[66:67], v108 offset:1024\n"
      "v_mfma_f32_16x16x32_bf16 v[188:191], v[36:39], v[4:7], v[188:191]\nds_read_b64_tr_b16 v[68:69], v108 offset:8192\nv_mfma_f32_16x16x32_bf16 v[192:195], v[44:47], v[4:7], v[192:195]\n"
      "ds_read_b64_tr_b16 v[70:71], v108 offset:9216\nv_mfma_f32_16x16x32_bf16 v[192:195], v[40:43], v[0:3], v[192:195]\nds_read_b64_tr_b16 v[72:73], v109 offset:0\n"
      "v_mfma_f32_16x16x32_bf16 v[196:199], v[48:51], v[0:3], v[196:199]\nds_read_b64_tr_b16 v[74:75], v109 offset:1024\nv_mfma_f32_16x16x32_bf16 v[196:199], v[52:55], v[4:7], v[196:199]\n"
      "ds_read_b64_tr_b16 v[76:77], v109 offset:8192\nv_mfma_f32_16x16x32_bf16 v[200:203], v[60:63], v[4:7], v[200:203]\nds_read_b64_tr_b16 v[78:79], v109 offset:9216\n"
      "v_mfma_f32_16x16x32_bf16 v[200:203], v[56:59], v[0:3], v[200:203]\nds_read_b64_tr_b16 v[80:81], v110 offset:0\nv_mfma_f32_16x16x32_bf16 v[216:219], v[56:59], v[8:11], v[216:219]\n"
      "ds_read_b128 v[0:3], v102 offset:0\nv_mfma_f32_16x16x32_bf16 v[216:219], v[60:63], v[12:15], v[216:219]\nds_read_b128 v[4:7], v103 offset:0\n"
      "v_mfma_f32_16x16x32_bf16 v[212:215], v[52:55], v[12:15], v[212:215]\nds_read_b64_tr_b16 v[82:83], v110 offset:1024\nv_mfma_f32_16x16x32_bf16 v[212:215], v[48:51], v[8:11], v[212:215]\n"
      "ds_read_b64_tr_b16 v[84:85], v110 offset:8192\nv_mfma_f32_16x16x32_bf16 v[208:211], v[40:43], v[8:11], v[208:211]\nds_read_b64_tr_b16 v[86:87], v110 offset:9216\n"
      "v_mfma_f32_16x16x32_bf16 v[208:211], v[44:47], v[12:15], v[208:211]\nds_read_b64_tr_b16 v[88:89], v111 offset:0\nv_mfma_f32_16x16x32_bf16 v[204:207], v[36:39], v[12:15], v[204:207]\n"
      "ds_read_b64_tr_b16 v[90:91], v111 offset:1024\nv_mfma_f32_16x16x32_bf16 v[204:207], v[32:35], v[8:11], v[204:207]\nds_read_b64_tr_b16 v[92:93], v111 offset:8192\n"
      "v_mfma_f32_16x16x32_bf16 v[220:223], v[32:35], v[16:19], v[220:223]\nds_read_b128 v[8:11], v122 offset:0\nv_mfma_f32_16x16x32_bf16 v[220:223], v[36:39], v[20:23], v[220:223]\n"
      "ds_read_b128 v[12:15], v123 offset:0\nv_mfma_f32_16x16x32_bf16 v[224:227], v[44:47], v[20:23], v[224:227]\nds_read_b64_tr_b16 v[94:95], v111 offset:9216\n"
      "v_mfma_f32_16x16x32_bf16 v[224:227], v[40:43], v[16:19], v[224:227]\nv_mfma_f32_16x16x32_bf16 v[228:231], v[48:51], v[16:19], v[228:231]\n"
      "v_mfma_f32_16x16x32_bf16 v[228:231], v[52:55], v[20:23], v[228:231]\nv_mfma_f32_16x16x32_bf16 v[232:235], v[60:63], v[20:23], v[232:235]\ns_waitcnt lgkmcnt(0)\n"
      "v_mfma_f32_16x16x32_bf16 v[232:235], v[56:59], v[16:19], v[232:235]\nds_read_b128 v[16:19], v98 offset:0\nds_read_b128 v[20:23], v99 offset:0\n"
      "v_mfma_f32_16x16x32_bf16 v[124:127], v[64:67], v[0:3], v[124:127]\nds_read_b128 v[24:27], v98 offset:2048\nv_mfma_f32_16x16x32_bf16 v[124:127], v[68:71], v[4:7], v[124:127]\n"
      "ds_read_b128 v[28:31], v99 offset:2048\nv_mfma_f32_16x16x32_bf16 v[128:131], v[76:79], v[4:7], v[128:131]\nv_mfma_f32_16x16x32_bf16 v[128:131], v[72:75], v[0:3], v[128:131]\n"
      "v_mfma_f32_16x16x32_bf16 v[152:155], v[88:91], v[8:11], v[152:155]\nds_read_b128 v[0:3], v98 offset:4096\nv_mfma_f32_16x16x32_bf16 v[152:155], v[92:95], v[12:15], v[152:155]\n"
      "ds_read_b128 v[4:7], v99 offset:4096\nv_mfma_f32_16x16x32_bf16 v[148:151], v[84:87], v[12:15], v[148:151]\nv_mfma_f32_16x16x32_bf16 v[148:151], v[80:83], v[8:11], v[148:151]\n"
      "v_mfma_f32_16x16x32_bf16 v[144:147], v[72:75], v[8:11], v[144:147]\nv_mfma_f32_16x16x32_bf16 v[144:147], v[76:79], v[12:15], v[144:147]\n"
      "v_mfma_f32_16x16x32_bf16 v[140:143], v[68:71], v[12:15], v[140:143]\ns_waitcnt lgkmcnt(4)\nv_mfma_f32_16x16x32_bf16 v[140:143], v[64:67], v[8:11], v[140:143]\n"
      "v_mfma_f32_16x16x32_bf16 v[156:159], v[64:67], v[16:19], v[156:159]\nds_read_b128 v[8:11], v98 offset:6144\nv_mfma_f32_16x16x32_bf16 v[156:159], v[68:71], v[20:23], v[156:159]\n"
      "ds_read_b128 v[12:15], v99 offset:6144\nv_mfma_f32_16x16x32_bf16 v[160:163], v[76:79], v[20:23], v[160:163]\nv_mfma_f32_16x16x32_bf16 v[160:163], v[72:75], v[16:19], v[160:163]\n"
      "v_mfma_f32_16x16x32_bf16 v[164:167], v[80:83], v[16:19], v[164:167]\nv_mfma_f32_16x16x32_bf16 v[164:167], v[84:87], v[20:23], v[164:167]\n"
      "v_mfma_f32_16x16x32_bf16 v[168:171], v[92:95], v[20:23], v[168:171]\ns_waitcnt lgkmcnt(2)\nv_mfma_f32_16x16x32_bf16 v[168:171], v[88:91], v[16:19], v[168:171]\n"
      "v_mfma_f32_16x16x32_bf16 v[184:187], v[88:91], v[24:27], v[184:187]\nds_read_b128 v[16:19], v98 offset:8192\nv_mfma_f32_16x16x32_bf16 v[184:187], v[92:95], v[28:31], v[184:187]\n"
      "ds_read_b128 v[20:23], v99 offset:8192\nv_mfma_f32_16x16x32_bf16 v[180:183], v[84:87], v[28:31], v[180:183]\nv_mfma_f32_16x16x32_bf16 v[180:183], v[80:83], v[24:27], v[180:183]\n"
      "v_mfma_f32_16x16x32_bf16 v[176:179], v[72:75], v[24:27], v[176:179]\nv_mfma_f32_16x16x32_bf16 v[176:179], v[76:79], v[28:31], v[176:179]\n"
      "v_mfma_f32_16x16x32_bf16 v[172:175], v[68:71], v[28:31], v[172:175]\ns_waitcnt vmcnt(0) lgkmcnt(0)\ns_barrier\nv_mfma_f32_16x16x32_bf16 v[172:175], v[64:67], v[24:27], v[172:175]\n"
      "ds_read_b64_tr_b16 v[32:33], v104 offset:0\nv_mfma_f32_16x16x32_bf16 v[188:191], v[64:67], v[0:3], v[188:191]\nds_read_b64_tr_b16 v[34:35], v104 offset:1024\n"
      "v_mfma_f32_16x16x32_bf16 v[188:191], v[68:71], v[4:7], v[188:191]\nds_read_b64_tr_b16 v[36:37], v104 offset:8192\nv_mfma_f32_16x16x32_bf16 v[192:195], v[76:79], v[4:7], v[192:195]\n"
      "ds_read_b64_tr_b16 v[38:39], v104 offset:9216\nv_mfma_f32_16x16x32_bf16 v[192:195], v[72:75], v[0:3], v[192:195]\nds_read_b64_tr_b16 v[40:41], v105 offset:0\n"
      "v_mfma_f32_16x16x32_bf16 v[196:199], v[80:83], v[0:3], v[196:199]\nds_read_b64_tr_b16 v[42:43], v105 offset:1024\nv_mfma_f32_16x16x32_bf16 v[196:199], v[84:87], v[4:7], v[196:199]\n"
      "ds_read_b64_tr_b16 v[44:45], v105 offset:8192\nv_mfma_f32_16x16x32_bf16 v[200:203], v[92:95], v[4:7], v[200:203]\nds_read_b64_tr_b16 v[46:47], v105 offset:9216\n"
      "v_mfma_f32_16x16x32_bf16 v[200:203], v[88:91], v[0:3], v[200:203]\nds_read_b64_tr_b16 v[48:49], v106 offset:0\nv_mfma_f32_16x16x32_bf16 v[216:219], v[88:91], v[8:11], v[216:219]\n"
      "ds_read_b128 v[0:3], v100 offset:0\nv_mfma_f32_16x16x32_bf16 v[216:219], v[92:95], v[12:15], v[216:219]\nds_read_b128 v[4:7], v101 offset:0\n"
      "v_mfma_f32_16x16x32_bf16 v[212:215], v[84:87], v[12:15], v[212:215]\nds_read_b64_tr_b16 v[50:51], v106 offset:1024\nv_mfma_f32_16x16x32_bf16 v[212:215], v[80:83], v[8:11], v[212:215]\n"
      "ds_read_b64_tr_b16 v[52:53], v106 offset:8192\nv_mfma_f32_16x16x32_bf16 v[208:211], v[72:75], v[8:11], v[208:211]\nds_read_b64_tr_b16 v[54:55], v106 offset:9216\n"
      "v_mfma_f32_16x16x32_bf16 v[208:211], v[76:79], v[12:15], v[208:211]\nds_read_b64_tr_b16 v[56:57], v107 offset:0\nv_mfma_f32_16x16x32_bf16 v[204:207], v[68:71], v[12:15], v[204:207]\n"
      "ds_read_b64_tr_b16 v[58:59], v107 offset:1024\nv_mfma_f32_16x16x32_bf16 v[204:207], v[64:67], v[8:11], v[204:207]\nds_read_b64_tr_b16 v[60:61], v107 offset:8192\n"
      "v_mfma_f32_16x16x32_bf16 v[220:223], v[64:67], v[16:19], v[220:223]\nds_read_b128 v[8:11], v120 offset:0\nv_mfma_f32_16x16x32_bf16 v[220:223], v[68:71], v[20:23], v[220:223]\n"
      "ds_read_b128 v[12:15], v121 offset:0\nv_mfma_f32_16x16x32_bf16 v[224:227], v[76:79], v[20:23], v[224:227]\nds_read_b64_tr_b16 v[62:63], v107 offset:9216\n"
      "v_mfma_f32_16x16x32_bf16 v[224:227], v[72:75], v[16:19], v[224:227]\nv_mfma_f32_16x16x32_bf16 v[228:231], v[80:83], v[16:19], v[228:231]\n"
      "v_mfma_f32_16x16x32_bf16 v[228:231], v[84:87], v[20:23], v[228:231]\nv_mfma_f32_16x16x32_bf16 v[232:235], v[92:95], v[20:23], v[232:235]\ns_waitcnt lgkmcnt(0)\n"
      "v_mfma_f32_16x16x32_bf16 v[232:235], v[88:91], v[16:19], v[232:235]\nds_read_b128 v[16:19], v96 offset:0\nds_read_b128 v[20:23], v97 offset:0\n"
      "v_mfma_f32_16x16x32_bf16 v[124:127], v[32:35], v[0:3], v[124:127]\nds_read_b128 v[24:27], v96 offset:2048\nv_mfma_f32_16x16x32_bf16 v[124:127], v[36:39], v[4:7], v[124:127]\n"
      "ds_read_b128 v[28:31], v97 offset:2048\nv_mfma_f32_16x16x32_bf16 v[128:131], v[44:47], v[4:7], v[128:131]\nv_mfma_f32_16x16x32_bf16 v[128:131], v[40:43], v[0:3], v[128:131]\n"
      "v_mfma_f32_16x16x32_bf16 v[152:155], v[56:59], v[8:11], v[152:155]\nds_read_b128 v[0:3], v96 offset:4096\nv_mfma_f32_16x16x32_bf16 v[152:155], v[60:63], v[12:15], v[152:155]\n"
      "ds_read_b128 v[4:7], v97 offset:4096\nv_mfma_f32_16x16x32_bf16 v[148:151], v[52:55], v[12:15], v[148:151]\nv_mfma_f32_16x16x32_bf16 v[148:151], v[48:51], v[8:11], v[148:151]\n"
      "v_mfma_f32_16x16x32_bf16 v[144:147], v[40:43], v[8:11], v[144:147]\nv_mfma_f32_16x16x32_bf16 v[144:147], v[44:47], v[12:15], v[144:147]\n"
      "v_mfma_f32_16x16x32_bf16 v[140:143], v[36:39], v[12:15], v[140:143]\ns_waitcnt lgkmcnt(4)\nv_mfma_f32_16x16x32_bf16 v[140:143], v[32:35], v[8:11], v[140:143]\n"
      "v_mfma_f32_16x16x32_bf16 v[156:159], v[32:35], v[16:19], v[156:159]\nds_read_b128 v[8:11], v96 offset:6144\nv_mfma_f32_16x16x32_bf16 v[156:159], v[36:39], v[20:23], v[156:159]\n"
      "ds_read_b128 v[12:15], v97 offset:6144\nv_mfma_f32_16x16x32_bf16 v[160:163], v[44:47], v[20:23], v[160:163]\nv_mfma_f32_16x16x32_bf16 v[160:163], v[40:43], v[16:19], v[160:163]\n"
      "v_mfma_f32_16x16x32_bf16 v[164:167], v[48:51], v[16:19], v[164:167]\nv_mfma_f32_16x16x32_bf16 v[164:167], v[52:55], v[20:23], v[164:167]\n"
      "v_mfma_f32_16x16x32_bf16 v[168:171], v[60:63], v[20:23], v[168:171]\ns_waitcnt lgkmcnt(2)\nv_mfma_f32_16x16x32_bf16 v[168:171], v[56:59], v[16:19], v[168:171]\n"
      "v_mfma_f32_16x16x32_bf16 v[184:187], v[56:59], v[24:27], v[184:187]\nds_read_b128 v[16:19], v96 offset:8192\nv_mfma_f32_16x16x32_bf16 v[184:187], v[60:63], v[28:31], v[184:187]\n"
      "ds_read_b128 v[20:23], v97 offset:8192\nv_mfma_f32_16x16x32_bf16 v[180:183], v[52:55], v[28:31], v[180:183]\nv_mfma_f32_16x16x32_bf16 v[180:183], v[48:51], v[24:27], v[180:183]\n"
      "v_mfma_f32_16x16x32_bf16 v[176:179], v[40:43], v[24:27], v[176:179]\nv_mfma_f32_16x16x32_bf16 v[176:179], v[44:47], v[28:31], v[176:179]\n"
      "v_mfma_f32_16x16x32_bf16 v[172:175], v[36:39], v[28:31], v[172:175]\ns_waitcnt vmcnt(0) lgkmcnt(0)\ns_barrier\nv_mfma_f32_16x16x32_bf16 v[172:175], v[32:35], v[24:27], v[172:175]\n"
      "ds_read_b64_tr_b16 v[64:65], v104 offset:45056\nv_mfma_f32_16x16x32_bf16 v[188:191], v[32:35], v[0:3], v[188:191]\nds_read_b64_tr_b16 v[66:67], v104 offset:46080\n"
      "v_mfma_f32_16x16x32_bf16 v[188:191], v[36:39], v[4:7], v[188:191]\nds_read_b64_tr_b16 v[68:69], v104 offset:53248\nv_mfma_f32_16x16x32_bf16 v[192:195], v[44:47], v[4:7], v[192:195]\n"
      "ds_read_b64_tr_b16 v[70:71], v104 offset:54272\nv_mfma_f32_16x16x32_bf16 v[192:195], v[40:43], v[0:3], v[192:195]\nds_read_b64_tr_b16 v[72:73], v105 offset:45056\n"
      "v_mfma_f32_16x16x32_bf16 v[196:199], v[48:51], v[0:3], v[196:199]\nds_read_b64_tr_b16 v[74:75], v105 offset:46080\nv_mfma_f32_16x16x32_bf16 v[196:199], v[52:55], v[4:7], v[196:199]\n"
      "ds_read_b64_tr_b16 v[76:77], v105 offset:53248\nv_mfma_f32_16x16x32_bf16 v[200:203], v[60:63], v[4:7], v[200:203]\nds_read_b64_tr_b16 v[78:79], v105 offset:54272\n"
      "v_mfma_f32_16x16x32_bf16 v[200:203], v[56:59], v[0:3], v[200:203]\nds_read_b64_tr_b16 v[80:81], v106 offset:45056\nv_mfma_f32_16x16x32_bf16 v[216:219], v[56:59], v[8:11], v[216:219]\n"
      "ds_read_b128 v[0:3], v100 offset:45056\nv_mfma_f32_16x16x32_bf16 v[216:219], v[60:63], v[12:15], v[216:219]\nds_read_b128 v[4:7], v101 offset:45056\n"
      "v_mfma_f32_16x16x32_bf16 v[212:215], v[52:55], v[12:15], v[212:215]\nds_read_b64_tr_b16 v[82:83], v106 offset:46080\nv_mfma_f32_16x16x32_bf16 v[212:215], v[48:51], v[8:11], v[212:215]\n"
      "ds_read_b64_tr_b16 v[84:85], v106 offset:53248\nv_mfma_f32_16x16x32_bf16 v[208:211], v[40:43], v[8:11], v[208:211]\nds_read_b64_tr_b16 v[86:87], v106 offset:54272\n"
      "v_mfma_f32_16x16x32_bf16 v[208:211], v[44:47], v[12:15], v[208:211]\nds_read_b64_tr_b16 v[88:89], v107 offset:45056\nv_mfma_f32_16x16x32_bf16 v[204:207], v[36:39], v[12:15], v[204:207]\n"
      "ds_read_b64_tr_b16 v[90:91], v107 offset:46080\nv_mfma_f32_16x16x32_bf16 v[204:207], v[32:35], v[8:11], v[204:207]\nds_read_b64_tr_b16 v[92:93], v107 offset:53248\n"
      "v_mfma_f32_16x16x32_bf16 v[220:223], v[32:35], v[16:19], v[220:223]\nds_read_b128 v[8:11], v120 offset:45056\nv_mfma_f32_16x16x32_bf16 v[220:223], v[36:39], v[20:23], v[220:223]\n"
      "ds_read_b128 v[12:15], v121 offset:45056\nv_mfma_f32_16x16x32_bf16 v[224:227], v[44:47], v[20:23], v[224:227]\nds_read_b64_tr_b16 v[94:95], v107 offset:54272\n"
      "v_mfma_f32_16x16x32_bf16 v[224:227], v[40:43], v[16:19], v[224:227]\nv_mfma_f32_16x16x32_bf16 v[228:231], v[48:51], v[16:19], v[228:231]\ns_sub_u32 s58, s58, 1\ns_cmp_lg_u32 s58, 0\n"
      "v_mfma_f32_16x16x32_bf16 v[228:231], v[52:55], v[20:23], v[228:231]\nv_mfma_f32_16x16x32_bf16 v[232:235], v[60:63], v[20:23], v[232:235]\ns_waitcnt lgkmcnt(0)\n"
      "v_mfma_f32_16x16x32_bf16 v[232:235], v[56:59], v[16:19], v[232:235]\nds_read_b128 v[16:19], v96 offset:45056\nds_read_b128 v[20:23], v97 offset:45056\ns_cbranch_scc1 .Lgl13\n"
      "v_mfma_f32_16x16x32_bf16 v[124:127], v[64:67], v[0:3], v[124:127]\nds_read_b128 v[24:27], v96 offset:47104\nv_cmp_eq_u32 s[62:63], 0, v240\n"
      "v_mfma_f32_16x16x32_bf16 v[124:127], v[68:71], v[4:7], v[124:127]\nds_read_b128 v[28:31], v97 offset:47104\nv_mov_b32 v253, 262144\n"
      "v_mfma_f32_16x16x32_bf16 v[128:131], v[76:79], v[4:7], v[128:131]\nv_mov_b32 v248, 162048\nv_mov_b32 v241, 1\nv_mfma_f32_16x16x32_bf16 v[128:131], v[72:75], v[0:3], v[128:131]\n"
      "v_cndmask_b32 v253, v253, v248, s[62:63]\nv_lshrrev_b32 v112, 4, v240\nv_mfma_f32_16x16x32_bf16 v[152:155], v[88:91], v[8:11], v[152:155]\nds_read_b128 v[0:3], v96 offset:49152\n"
      "v_bfe_u32 v113, v240, 2, 2\nv_mfma_f32_16x16x32_bf16 v[152:155], v[92:95], v[12:15], v[152:155]\nds_read_b128 v[4:7], v97 offset:49152\nv_and_b32 v114, 3, v240\n"
      "v_mfma_f32_16x16x32_bf16 v[148:151], v[84:87], v[12:15], v[148:151]\nv_mul_u32_u24 v114, 0x80008, v114\nv_add_u32 v244, v112, v113\n"
      "v_mfma_f32_16x16x32_bf16 v[148:151], v[80:83], v[8:11], v[148:151]\nv_and_b32 v244, 3, v244\nv_add_u32 v116, 22, v113\nv_mfma_f32_16x16x32_bf16 v[144:147], v[72:75], v[8:11], v[144:147]\n"
      "v_cmp_gt_u32 s[62:63], 2, v113\nv_and_b32 v113, 60, v240\nv_mfma_f32_16x16x32_bf16 v[144:147], v[76:79], v[12:15], v[144:147]\ns_mov_b32 s42, 0\nv_cndmask_b32 v116, v116, v244, s[62:63]\n"
      "v_mfma_f32_16x16x32_bf16 v[140:143], v[68:71], v[12:15], v[140:143]\ns_waitcnt lgkmcnt(4)\nv_lshl_or_b32 v244, v244, 16, v116\n"
      "v_mfma_f32_16x16x32_bf16 v[140:143], v[64:67], v[8:11], v[140:143]\nv_lshl_add_u32 v244, v244, 5, v114\nv_lshl_add_u32 v252, v240, 4, s42\n"
      "v_mfma_f32_16x16x32_bf16 v[156:159], v[64:67], v[16:19], v[156:159]\nds_read_b128 v[8:11], v96 offset:51200\ns_mov_b32 s42, 0x76325410\n"
      "v_mfma_f32_16x16x32_bf16 v[156:159], v[68:71], v[20:23], v[156:159]\nds_read_b128 v[12:15], v97 offset:51200\ns_mov_b32 s43, 0x21005410\n"
      "v_mfma_f32_16x16x32_bf16 v[160:163], v[76:79], v[20:23], v[160:163]\nv_lshrrev_b64 v[116:117], v113, s[42:43]\nv_and_b32 v116, 7, v116\n"
      "v_mfma_f32_16x16x32_bf16 v[160:163], v[72:75], v[16:19], v[160:163]\nv_lshl_add_u32 v116, v112, 3, v116\ns_mov_b32 s42, 0x54107632\n"
      "v_mfma_f32_16x16x32_bf16 v[164:167], v[80:83], v[16:19], v[164:167]\ns_mov_b32 s43, 0x21107632\nv_lshrrev_b64 v[118:119], v113, s[42:43]\n"
      "v_mfma_f32_16x16x32_bf16 v[164:167], v[84:87], v[20:23], v[164:167]\nv_and_b32 v118, 7, v118\nv_lshl_add_u32 v118, v112, 3, v118\n"
      "v_mfma_f32_16x16x32_bf16 v[168:171], v[92:95], v[20:23], v[168:171]\ns_waitcnt lgkmcnt(2)\nv_lshl_or_b32 v245, v118, 16, v116\n"
      "v_mfma_f32_16x16x32_bf16 v[168:171], v[88:91], v[16:19], v[168:171]\nv_lshl_add_u32 v245, v245, 5, v114\nv_and_b32 v113, 1, v240\n"
      "v_mfma_f32_16x16x32_bf16 v[184:187], v[88:91], v[24:27], v[184:187]\nds_read_b128 v[16:19], v96 offset:53248\nv_mul_u32_u24 v113, 0xaaaa, v113\n"
      "v_mfma_f32_16x16x32_bf16 v[184:187], v[92:95], v[28:31], v[184:187]\nds_read_b128 v[20:23], v97 offset:53248\nv_add_u32 v242, 0x4444, v113\n"
      "v_mfma_f32_16x16x32_bf16 v[180:183], v[84:87], v[28:31], v[180:183]\ns_lshl_b32 s42, s36, 4\ns_add_u32 s42, s42, 160640\nv_mfma_f32_16x16x32_bf16 v[180:183], v[80:83], v[24:27], v[180:183]\n"
      "v_lshl_add_u32 v243, v112, 2, s42\nv_and_b32 v112, 1, v112\nv_mfma_f32_16x16x32_bf16 v[176:179], v[72:75], v[24:27], v[176:179]\nv_lshl_add_u32 v112, v112, 3, s24\ns_sub_u32 s42, s64, 1\n"
      "v_mfma_f32_16x16x32_bf16 v[176:179], v[76:79], v[28:31], v[176:179]\nv_min_u32 v112, s42, v112\nv_lshlrev_b32 v112, 3, v112\n"
      "v_mfma_f32_16x16x32_bf16 v[172:175], v[68:71], v[28:31], v[172:175]\ns_waitcnt vmcnt(0) lgkmcnt(0)\ns_barrier\nv_mfma_f32_16x16x32_bf16 v[172:175], v[64:67], v[24:27], v[172:175]\n"
      "ds_read_b64_tr_b16 v[32:33], v108 offset:0\nv_add_u32 v250, 160256, v112\nv_mfma_f32_16x16x32_bf16 v[188:191], v[64:67], v[0:3], v[188:191]\nds_read_b64_tr_b16 v[34:35], v108 offset:1024\n"
      "v_and_b32 v113, 15, v240\nv_mfma_f32_16x16x32_bf16 v[188:191], v[68:71], v[4:7], v[188:191]\nds_read_b64_tr_b16 v[36:37], v108 offset:8192\nv_min_u32 v113, 12, v113\n"
      "v_mfma_f32_16x16x32_bf16 v[192:195], v[76:79], v[4:7], v[192:195]\nds_read_b64_tr_b16 v[38:39], v108 offset:9216\ns_lshl_b32 s42, s30, 7\n"
      "v_mfma_f32_16x16x32_bf16 v[192:195], v[72:75], v[0:3], v[192:195]\nds_read_b64_tr_b16 v[40:41], v109 offset:0\nv_lshl_add_u32 v113, v113, 15, s42\n"
      "v_mfma_f32_16x16x32_bf16 v[196:199], v[80:83], v[0:3], v[196:199]\nds_read_b64_tr_b16 v[42:43], v109 offset:1024\nv_add_u32 v249, v113, v112\n"
      "v_mfma_f32_16x16x32_bf16 v[196:199], v[84:87], v[4:7], v[196:199]\nds_read_b64_tr_b16 v[44:45], v109 offset:8192\ns_mul_i32 s65, s36, 22272\n"
      "v_mfma_f32_16x16x32_bf16 v[200:203], v[92:95], v[4:7], v[200:203]\nds_read_b64_tr_b16 v[46:47], v109 offset:9216\ns_add_u32 s65, s65, 45056\n"
      "v_mfma_f32_16x16x32_bf16 v[200:203], v[88:91], v[0:3], v[200:203]\nds_read_b64_tr_b16 v[48:49], v110 offset:0\ns_add_u32 s68, s64, 8\n"
      "v_mfma_f32_16x16x32_bf16 v[216:219], v[88:91], v[8:11], v[216:219]\nds_read_b128 v[0:3], v102 offset:0\ns_min_u32 s68, s68, 21\n"
      "v_mfma_f32_16x16x32_bf16 v[216:219], v[92:95], v[12:15], v[216:219]\nds_read_b128 v[4:7], v103 offset:0\ns_lshl_b32 s38, s38, 16\n"
      "v_mfma_f32_16x16x32_bf16 v[212:215], v[84:87], v[12:15], v[212:215]\nds_read_b64_tr_b16 v[50:51], v110 offset:1024\ns_lshr_b32 s42, s24, 2\n"
      "v_mfma_f32_16x16x32_bf16 v[212:215], v[80:83], v[8:11], v[212:215]\nds_read_b64_tr_b16 v[52:53], v110 offset:8192\ns_mul_i32 s43, s42, 3\n"
      "v_mfma_f32_16x16x32_bf16 v[208:211], v[72:75], v[8:11], v[208:211]\nds_read_b64_tr_b16 v[54:55], v110 offset:9216\ns_xor_b32 s43, s43, s36\n"
      "v_mfma_f32_16x16x32_bf16 v[208:211], v[76:79], v[12:15], v[208:211]\nds_read_b64_tr_b16 v[56:57], v111 offset:0\ns_add_u32 s42, s42, s64\n"
      "v_mfma_f32_16x16x32_bf16 v[204:207], v[68:71], v[12:15], v[204:207]\nds_read_b64_tr_b16 v[58:59], v111 offset:1024\ns_add_u32 s42, s42, 9\n"
      "v_mfma_f32_16x16x32_bf16 v[204:207], v[64:67], v[8:11], v[204:207]\nds_read_b64_tr_b16 v[60:61], v111 offset:8192\ns_and_b32 s42, s42, 1\n"
      "v_mfma_f32_16x16x32_bf16 v[220:223], v[64:67], v[16:19], v[220:223]\nds_read_b128 v[8:11], v122 offset:0\ns_lshl1_add_u32 s67, s43, s42\n"
      "v_mfma_f32_16x16x32_bf16 v[220:223], v[68:71], v[20:23], v[220:223]\nds_read_b128 v[12:15], v123 offset:0\ns_add_u32 s66, s64, s67\n"
      "v_mfma_f32_16x16x32_bf16 v[224:227], v[76:79], v[20:23], v[224:227]\nds_read_b64_tr_b16 v[62:63], v111 offset:9216\ns_cmp_lt_u32 s66, s68\n"
      "v_mfma_f32_16x16x32_bf16 v[224:227], v[72:75], v[16:19], v[224:227]\ns_cselect_b32 s66, s66, 127\ns_sub_u32 s66, s66, 8\nv_mfma_f32_16x16x32_bf16 v[228:231], v[80:83], v[16:19], v[228:231]\n"
      "s_max_i32 s66, s66, 0\ns_getpc_b64 s[34:35]\n"
      ".Lk1066:\nv_mfma_f32_16x16x32_bf16 v[228:231], v[84:87], v[20:23], v[228:231]\ns_add_u32 s34, s34, .Lt_g-.Lk1066\ns_addc_u32 s35, s35, 0\n"
      "v_mfma_f32_16x16x32_bf16 v[232:235], v[92:95], v[20:23], v[232:235]\ns_waitcnt lgkmcnt(0)\ns_lshr_b32 s42, s2, 3\nv_mfma_f32_16x16x32_bf16 v[232:235], v[88:91], v[16:19], v[232:235]\n"
      "ds_read_b128 v[16:19], v98 offset:0\nds_read_b128 v[20:23], v99 offset:0\nv_mfma_f32_16x16x32_bf16 v[124:127], v[32:35], v[0:3], v[124:127]\nds_read_b128 v[24:27], v98 offset:2048\n"
      "s_lshl1_add_u32 s42, s2, s42\nv_mfma_f32_16x16x32_bf16 v[124:127], v[36:39], v[4:7], v[124:127]\nds_read_b128 v[28:31], v99 offset:2048\ns_and_b32 s69, s42, 15\n"
      "v_mfma_f32_16x16x32_bf16 v[128:131], v[44:47], v[4:7], v[128:131]\ns_lshl_b32 s43, s69, 7\ns_add_u32 s43, s43, 32768\nv_mfma_f32_16x16x32_bf16 v[128:131], v[40:43], v[0:3], v[128:131]\n"
      "s_add_u32 s8, s20, s43\ns_addc_u32 s9, s21, 0\nv_mfma_f32_16x16x32_bf16 v[152:155], v[56:59], v[8:11], v[152:155]\nds_read_b128 v[0:3], v98 offset:4096\ns_add_u32 s12, s20, 65536\n"
      "v_mfma_f32_16x16x32_bf16 v[152:155], v[60:63], v[12:15], v[152:155]\nds_read_b128 v[4:7], v99 offset:4096\ns_addc_u32 s13, s21, 0\n"
      "v_mfma_f32_16x16x32_bf16 v[148:151], v[52:55], v[12:15], v[148:151]\ns_add_u32 s16, s20, 2097152\ns_addc_u32 s17, s21, 0\nv_mfma_f32_16x16x32_bf16 v[148:151], v[48:51], v[8:11], v[148:151]\n"
      "s_lshl_b32 s42, s69, 4\ns_max_u32 s42, s42, 24\nv_mfma_f32_16x16x32_bf16 v[144:147], v[40:43], v[8:11], v[144:147]\ns_sub_u32 s42, s42, 24\ns_min_u32 s69, s42, 192\n"
      "v_mfma_f32_16x16x32_bf16 v[144:147], v[44:47], v[12:15], v[144:147]\ns_lshl_b32 s42, s69, 3\ns_add_u32 s42, s42, 24576\nv_mfma_f32_16x16x32_bf16 v[140:143], v[36:39], v[12:15], v[140:143]\n"
      "s_waitcnt lgkmcnt(4)\ns_add_u32 s14, s20, s42\nv_mfma_f32_16x16x32_bf16 v[140:143], v[32:35], v[8:11], v[140:143]\ns_addc_u32 s15, s21, 0\n"
      "v_mfma_f32_16x16x32_bf16 v[156:159], v[32:35], v[16:19], v[156:159]\nds_read_b128 v[8:11], v98 offset:6144\nv_mfma_f32_16x16x32_bf16 v[156:159], v[36:39], v[20:23], v[156:159]\n"
      "ds_read_b128 v[12:15], v99 offset:6144\nv_mfma_f32_16x16x32_bf16 v[160:163], v[44:47], v[20:23], v[160:163]\nv_mfma_f32_16x16x32_bf16 v[160:163], v[40:43], v[16:19], v[160:163]\n"
      "v_mfma_f32_16x16x32_bf16 v[164:167], v[48:51], v[16:19], v[164:167]\nv_mfma_f32_16x16x32_bf16 v[164:167], v[52:55], v[20:23], v[164:167]\n"
      "v_mfma_f32_16x16x32_bf16 v[168:171], v[60:63], v[20:23], v[168:171]\ns_waitcnt lgkmcnt(2)\nv_mfma_f32_16x16x32_bf16 v[168:171], v[56:59], v[16:19], v[168:171]\n"
      "v_mfma_f32_16x16x32_bf16 v[184:187], v[56:59], v[24:27], v[184:187]\nds_read_b128 v[16:19], v98 offset:8192\nv_mfma_f32_16x16x32_bf16 v[184:187], v[60:63], v[28:31], v[184:187]\n"
      "ds_read_b128 v[20:23], v99 offset:8192\nv_mfma_f32_16x16x32_bf16 v[180:183], v[52:55], v[28:31], v[180:183]\nv_mfma_f32_16x16x32_bf16 v[180:183], v[48:51], v[24:27], v[180:183]\n"
      "v_mfma_f32_16x16x32_bf16 v[176:179], v[40:43], v[24:27], v[176:179]\nv_mfma_f32_16x16x32_bf16 v[176:179], v[44:47], v[28:31], v[176:179]\n"
      "v_mfma_f32_16x16x32_bf16 v[172:175], v[36:39], v[28:31], v[172:175]\ns_waitcnt vmcnt(0) lgkmcnt(0)\ns_barrier\nv_mfma_f32_16x16x32_bf16 v[172:175], v[32:35], v[24:27], v[172:175]\n"
      "ds_read_b128 v[72:75], v252 offset:0\nv_lshl_add_u32 v246, v240, 1, v248\nv_mfma_f32_16x16x32_bf16 v[188:191], v[32:35], v[0:3], v[188:191]\nds_read_b128 v[24:27], v236 offset:512\n"
      "ds_read_u16 v251, v246 offset:64\nv_mfma_f32_16x16x32_bf16 v[188:191], v[36:39], v[4:7], v[188:191]\nds_read_b128 v[28:31], v236 offset:576\nds_read_b32 v247, v248 offset:24\n"
      "v_mfma_f32_16x16x32_bf16 v[192:195], v[44:47], v[4:7], v[192:195]\nds_read_b128 v[76:79], v252 offset:1024\nv_and_b32 v246, 48, v240\n"
      "v_mfma_f32_16x16x32_bf16 v[192:195], v[40:43], v[0:3], v[192:195]\nds_read_b128 v[80:83], v252 offset:2048\ns_mov_b32 s42, 42240\n"
      "v_mfma_f32_16x16x32_bf16 v[196:199], v[48:51], v[0:3], v[196:199]\nds_read_b128 v[84:87], v252 offset:3072\nv_lshl_add_u32 v246, v246, 2, s42\n"
      "v_mfma_f32_16x16x32_bf16 v[196:199], v[52:55], v[4:7], v[196:199]\nds_read_b128 v[88:91], v252 offset:4096\ns_mov_b32 s58, 0xbdd2d3e7\n"
      "v_mfma_f32_16x16x32_bf16 v[200:203], v[60:63], v[4:7], v[200:203]\nds_read_b128 v[92:95], v252 offset:5120\ns_mov_b32 s59, 0xc0135761\n"
      "v_mfma_f32_16x16x32_bf16 v[200:203], v[56:59], v[0:3], v[200:203]\nds_read_b128 v[96:99], v252 offset:6144\ns_mov_b32 s60, 0xc1e00000\n"
      "v_mfma_f32_16x16x32_bf16 v[216:219], v[56:59], v[8:11], v[216:219]\nds_read_b128 v[0:3], v236 offset:0\ns_add_u32 s49, s65, 1856\n"
      "v_mfma_f32_16x16x32_bf16 v[216:219], v[60:63], v[12:15], v[216:219]\nds_read_b128 v[4:7], v236 offset:64\ns_mov_b32 s45, 2\n"
      "v_mfma_f32_16x16x32_bf16 v[212:215], v[52:55], v[12:15], v[212:215]\nds_read_b128 v[100:103], v252 offset:7168\ns_mov_b32 s46, 0\n"
      "v_mfma_f32_16x16x32_bf16 v[212:215], v[48:51], v[8:11], v[212:215]\nds_read_b128 v[104:107], v252 offset:8192\ns_waitcnt lgkmcnt(8)\n"
      "v_mfma_f32_16x16x32_bf16 v[208:211], v[40:43], v[8:11], v[208:211]\nds_read_b128 v[108:111], v252 offset:9216\nv_readfirstlane_b32 s39, v247\n"
      "v_mfma_f32_16x16x32_bf16 v[208:211], v[44:47], v[12:15], v[208:211]\nds_read_b128 v[64:67], v237 offset:512\nv_readlane_b32 s55, v251, 0\n"
      "v_mfma_f32_16x16x32_bf16 v[204:207], v[36:39], v[12:15], v[204:207]\nds_read_b128 v[68:71], v237 offset:576\ns_setprio 3\nv_mfma_f32_16x16x32_bf16 v[204:207], v[32:35], v[8:11], v[204:207]\n"
      "ds_read_b128 v[112:115], v252 offset:10240\ns_xor_b32 s40, s39, 0xa5c30f96\nv_mfma_f32_16x16x32_bf16 v[220:223], v[32:35], v[16:19], v[220:223]\nds_read_b128 v[8:11], v237 offset:0\n"
      "s_mul_i32 s41, s39, 0x9E3779B1\nv_mfma_f32_16x16x32_bf16 v[220:223], v[36:39], v[20:23], v[220:223]\nds_read_b128 v[12:15], v237 offset:64\ns_lshr_b32 s41, s41, 24\n"
      "v_mfma_f32_16x16x32_bf16 v[224:227], v[44:47], v[20:23], v[224:227]\nds_read_b128 v[116:119], v252 offset:11264\nv_mfma_f32_16x16x32_bf16 v[224:227], v[40:43], v[16:19], v[224:227]\n"
      "ds_read_b128 v[120:123], v252 offset:12288\nv_add_u32_sdwa v239, s49, v245 dst_sel:DWORD dst_unused:UNUSED_PAD src0_sel:DWORD src1_sel:WORD_1\n"
      "v_mfma_f32_16x16x32_bf16 v[228:231], v[48:51], v[16:19], v[228:231]\nds_read_b128 v[132:135], v252 offset:15360\n"
      "v_add_u32_sdwa v236, s49, v244 dst_sel:DWORD dst_unused:UNUSED_PAD src0_sel:DWORD src1_sel:WORD_0\nv_mfma_f32_16x16x32_bf16 v[228:231], v[52:55], v[20:23], v[228:231]\n"
      "ds_read_b128 v[136:139], v252 offset:16384\nv_add_u32_sdwa v237, s49, v244 dst_sel:DWORD dst_unused:UNUSED_PAD src0_sel:DWORD src1_sel:WORD_1\n"
      "v_mfma_f32_16x16x32_bf16 v[232:235], v[60:63], v[20:23], v[232:235]\nv_add_u32_sdwa v238, s49, v245 dst_sel:DWORD dst_unused:UNUSED_PAD src0_sel:DWORD src1_sel:WORD_0\ns_waitcnt lgkmcnt(0)\n"
      "v_mfma_f32_16x16x32_bf16 v[232:235], v[56:59], v[16:19], v[232:235]\nv_pk_mul_f32 v[44:45], v[124:125], v[124:125]\nv_pk_mul_f32 v[46:47], v[126:127], v[126:127]\n"
      "v_pk_mul_f32 v[48:49], v[128:129], v[128:129]\nv_pk_mul_f32 v[50:51], v[130:131], v[130:131]\nv_pk_fma_f32 v[44:45], v[44:45], s[58:59], s[58:59] op_sel:[0,0,1] op_sel_hi:[1,0,1]\n"
      "v_pk_fma_f32 v[46:47], v[46:47], s[58:59], s[58:59] op_sel:[0,0,1] op_sel_hi:[1,0,1]\nv_pk_fma_f32 v[48:49], v[48:49], s[58:59], s[58:59] op_sel:[0,0,1] op_sel_hi:[1,0,1]\n"
      "v_pk_fma_f32 v[50:51], v[50:51], s[58:59], s[58:59] op_sel:[0,0,1] op_sel_hi:[1,0,1]\nv_pk_fma_f32 v[44:45], v[44:45], v[124:125], s[60:61] op_sel_hi:[1,1,0]\n"
      "v_pk_fma_f32 v[46:47], v[46:47], v[126:127], s[60:61] op_sel_hi:[1,1,0]\nv_pk_fma_f32 v[48:49], v[48:49], v[128:129], s[60:61] op_sel_hi:[1,1,0]\n"
      "v_pk_fma_f32 v[50:51], v[50:51], v[130:131], s[60:61] op_sel_hi:[1,1,0]\nv_exp_f32 v44, v44 clamp\nv_pk_mul_f32 v[52:53], v[140:141], v[140:141]\nv_exp_f32 v45, v45 clamp\n"
      "v_pk_mul_f32 v[54:55], v[142:143], v[142:143]\nv_pk_mul_f32 v[56:57], v[144:145], v[144:145]\nv_exp_f32 v46, v46 clamp\nv_pk_mul_f32 v[58:59], v[146:147], v[146:147]\n"
      "v_exp_f32 v47, v47 clamp\nv_pk_fma_f32 v[52:53], v[52:53], s[58:59], s[58:59] op_sel:[0,0,1] op_sel_hi:[1,0,1]\n"
      "v_pk_fma_f32 v[54:55], v[54:55], s[58:59], s[58:59] op_sel:[0,0,1] op_sel_hi:[1,0,1]\nv_exp_f32 v48, v48 clamp\n"
      "v_pk_fma_f32 v[56:57], v[56:57], s[58:59], s[58:59] op_sel:[0,0,1] op_sel_hi:[1,0,1]\nv_exp_f32 v49, v49 clamp\n"
      "v_pk_fma_f32 v[58:59], v[58:59], s[58:59], s[58:59] op_sel:[0,0,1] op_sel_hi:[1,0,1]\nv_pk_fma_f32 v[52:53], v[52:53], v[140:141], s[60:61] op_sel_hi:[1,1,0]\nv_exp_f32 v50, v50 clamp\n"
      "v_pk_fma_f32 v[54:55], v[54:55], v[142:143], s[60:61] op_sel_hi:[1,1,0]\nv_exp_f32 v51, v51 clamp\nv_pk_fma_f32 v[56:57], v[56:57], v[144:145], s[60:61] op_sel_hi:[1,1,0]\n"
      "v_pk_fma_f32 v[58:59], v[58:59], v[146:147], s[60:61] op_sel_hi:[1,1,0]\nv_exp_f32 v52, v52 clamp\nv_pk_fma_f32 v[46:47], v[46:47], v[2:3], v[26:27]\n"
      "v_pk_fma_f32 v[44:45], v[44:45], v[0:1], v[24:25]\nv_pk_fma_f32 v[48:49], v[48:49], v[4:5], v[28:29]\nv_exp_f32 v53, v53 clamp\nv_pk_fma_f32 v[50:51], v[50:51], v[6:7], v[30:31]\n"
      "v_pk_mul_f32 v[124:125], v[124:125], v[46:47]\nv_pk_mul_f32 v[128:129], v[128:129], v[50:51]\nv_exp_f32 v54, v54 clamp\nv_pk_fma_f32 v[124:125], v[126:127], v[44:45], v[124:125]\n"
      "v_pk_fma_f32 v[128:129], v[130:131], v[48:49], v[128:129]\nv_pk_mul_f32 v[44:45], v[44:45], v[46:47]\nv_exp_f32 v55, v55 clamp\nv_pk_mul_f32 v[48:49], v[48:49], v[50:51]\n"
      "v_pk_mul_f32 v[46:47], v[44:45], v[48:49]\nv_rcp_f32 v46, v46\nv_pk_mul_f32 v[124:125], v[124:125], v[48:49]\nv_rcp_f32 v47, v47\nv_pk_fma_f32 v[124:125], v[128:129], v[44:45], v[124:125]\n"
      "v_pk_mul_f32 v[32:33], v[124:125], v[46:47]\nv_add_f32 v42, v32, v33\nv_pk_mul_f32 v[16:17], v[148:149], v[148:149]\nv_pk_mul_f32 v[18:19], v[150:151], v[150:151]\nv_exp_f32 v56, v56 clamp\n"
      "v_pk_mul_f32 v[20:21], v[152:153], v[152:153]\nv_pk_mul_f32 v[22:23], v[154:155], v[154:155]\nv_exp_f32 v57, v57 clamp\n"
      "v_pk_fma_f32 v[16:17], v[16:17], s[58:59], s[58:59] op_sel:[0,0,1] op_sel_hi:[1,0,1]\nv_pk_fma_f32 v[18:19], v[18:19], s[58:59], s[58:59] op_sel:[0,0,1] op_sel_hi:[1,0,1]\n"
      "v_pk_fma_f32 v[20:21], v[20:21], s[58:59], s[58:59] op_sel:[0,0,1] op_sel_hi:[1,0,1]\nv_exp_f32 v58, v58 clamp\n"
      "v_pk_fma_f32 v[22:23], v[22:23], s[58:59], s[58:59] op_sel:[0,0,1] op_sel_hi:[1,0,1]\nv_pk_fma_f32 v[16:17], v[16:17], v[148:149], s[60:61] op_sel_hi:[1,1,0]\n"
      "v_pk_fma_f32 v[18:19], v[18:19], v[150:151], s[60:61] op_sel_hi:[1,1,0]\nv_exp_f32 v59, v59 clamp\nv_pk_fma_f32 v[20:21], v[20:21], v[152:153], s[60:61] op_sel_hi:[1,1,0]\n"
      "v_pk_fma_f32 v[22:23], v[22:23], v[154:155], s[60:61] op_sel_hi:[1,1,0]\nv_exp_f32 v16, v16 clamp\nv_pk_fma_f32 v[54:55], v[54:55], v[2:3], v[26:27]\n"
      "v_pk_fma_f32 v[52:53], v[52:53], v[0:1], v[24:25]\nv_pk_fma_f32 v[56:57], v[56:57], v[4:5], v[28:29]\nv_exp_f32 v17, v17 clamp\nv_pk_fma_f32 v[58:59], v[58:59], v[6:7], v[30:31]\n"
      "v_pk_mul_f32 v[140:141], v[140:141], v[54:55]\nv_pk_mul_f32 v[144:145], v[144:145], v[58:59]\nv_exp_f32 v18, v18 clamp\nv_pk_fma_f32 v[140:141], v[142:143], v[52:53], v[140:141]\n"
      "v_pk_fma_f32 v[144:145], v[146:147], v[56:57], v[144:145]\nv_pk_mul_f32 v[52:53], v[52:53], v[54:55]\nv_exp_f32 v19, v19 clamp\nv_pk_mul_f32 v[56:57], v[56:57], v[58:59]\n"
      "v_pk_mul_f32 v[54:55], v[52:53], v[56:57]\nv_rcp_f32 v54, v54\nv_pk_mul_f32 v[140:141], v[140:141], v[56:57]\nv_rcp_f32 v55, v55\nv_pk_fma_f32 v[140:141], v[144:145], v[52:53], v[140:141]\n"
      "v_pk_mul_f32 v[32:33], v[140:141], v[54:55]\nv_pk_mul_f32 v[44:45], v[156:157], v[156:157]\nv_pk_mul_f32 v[46:47], v[158:159], v[158:159]\nv_exp_f32 v20, v20 clamp\n"
      "v_pk_mul_f32 v[48:49], v[160:161], v[160:161]\nv_pk_mul_f32 v[50:51], v[162:163], v[162:163]\nv_exp_f32 v21, v21 clamp\n"
      "v_pk_fma_f32 v[44:45], v[44:45], s[58:59], s[58:59] op_sel:[0,0,1] op_sel_hi:[1,0,1]\nv_pk_fma_f32 v[46:47], v[46:47], s[58:59], s[58:59] op_sel:[0,0,1] op_sel_hi:[1,0,1]\n"
      "v_pk_fma_f32 v[48:49], v[48:49], s[58:59], s[58:59] op_sel:[0,0,1] op_sel_hi:[1,0,1]\nv_exp_f32 v22, v22 clamp\n"
      "v_pk_fma_f32 v[50:51], v[50:51], s[58:59], s[58:59] op_sel:[0,0,1] op_sel_hi:[1,0,1]\nv_pk_fma_f32 v[44:45], v[44:45], v[156:157], s[60:61] op_sel_hi:[1,1,0]\n"
      "v_pk_fma_f32 v[46:47], v[46:47], v[158:159], s[60:61] op_sel_hi:[1,1,0]\nv_exp_f32 v23, v23 clamp\nv_pk_fma_f32 v[48:49], v[48:49], v[160:161], s[60:61] op_sel_hi:[1,1,0]\n"
      "v_pk_fma_f32 v[50:51], v[50:51], v[162:163], s[60:61] op_sel_hi:[1,1,0]\nv_exp_f32 v44, v44 clamp\nv_pk_fma_f32 v[18:19], v[18:19], v[10:11], v[66:67]\n"
      "v_pk_fma_f32 v[16:17], v[16:17], v[8:9], v[64:65]\nv_pk_fma_f32 v[20:21], v[20:21], v[12:13], v[68:69]\nv_exp_f32 v45, v45 clamp\nv_pk_fma_f32 v[22:23], v[22:23], v[14:15], v[70:71]\n"
      "v_pk_mul_f32 v[148:149], v[148:149], v[18:19]\nv_pk_mul_f32 v[152:153], v[152:153], v[22:23]\nv_exp_f32 v46, v46 clamp\nv_pk_fma_f32 v[148:149], v[150:151], v[16:17], v[148:149]\n"
      "v_pk_fma_f32 v[152:153], v[154:155], v[20:21], v[152:153]\nv_pk_mul_f32 v[16:17], v[16:17], v[18:19]\nv_exp_f32 v47, v47 clamp\nv_pk_mul_f32 v[20:21], v[20:21], v[22:23]\n"
      "v_pk_mul_f32 v[18:19], v[16:17], v[20:21]\nv_rcp_f32 v18, v18\nv_pk_mul_f32 v[148:149], v[148:149], v[20:21]\nv_rcp_f32 v19, v19\nv_pk_fma_f32 v[148:149], v[152:153], v[16:17], v[148:149]\n"
      "v_pk_fma_f32 v[32:33], v[148:149], v[18:19], v[32:33]\nv_add_f32 v41, v32, v33\nv_pk_mul_f32 v[52:53], v[164:165], v[164:165]\nv_pk_mul_f32 v[54:55], v[166:167], v[166:167]\n"
      "v_exp_f32 v48, v48 clamp\nv_pk_mul_f32 v[56:57], v[168:169], v[168:169]\nv_pk_mul_f32 v[58:59], v[170:171], v[170:171]\nv_exp_f32 v49, v49 clamp\n"
      "v_pk_fma_f32 v[52:53], v[52:53], s[58:59], s[58:59] op_sel:[0,0,1] op_sel_hi:[1,0,1]\nv_pk_fma_f32 v[54:55], v[54:55], s[58:59], s[58:59] op_sel:[0,0,1] op_sel_hi:[1,0,1]\n"
      "v_pk_fma_f32 v[56:57], v[56:57], s[58:59], s[58:59] op_sel:[0,0,1] op_sel_hi:[1,0,1]\nv_exp_f32 v50, v50 clamp\n"
      "v_pk_fma_f32 v[58:59], v[58:59], s[58:59], s[58:59] op_sel:[0,0,1] op_sel_hi:[1,0,1]\nv_pk_fma_f32 v[52:53], v[52:53], v[164:165], s[60:61] op_sel_hi:[1,1,0]\n"
      "v_pk_fma_f32 v[54:55], v[54:55], v[166:167], s[60:61] op_sel_hi:[1,1,0]\nv_exp_f32 v51, v51 clamp\nv_pk_fma_f32 v[56:57], v[56:57], v[168:169], s[60:61] op_sel_hi:[1,1,0]\n"
      "v_pk_fma_f32 v[58:59], v[58:59], v[170:171], s[60:61] op_sel_hi:[1,1,0]\nv_exp_f32 v52, v52 clamp\nv_pk_fma_f32 v[46:47], v[46:47], v[2:3], v[26:27]\n"
      "v_pk_fma_f32 v[44:45], v[44:45], v[0:1], v[24:25]\nv_pk_fma_f32 v[48:49], v[48:49], v[4:5], v[28:29]\nv_exp_f32 v53, v53 clamp\nv_pk_fma_f32 v[50:51], v[50:51], v[6:7], v[30:31]\n"
      "v_pk_mul_f32 v[156:157], v[156:157], v[46:47]\nv_pk_mul_f32 v[160:161], v[160:161], v[50:51]\nv_exp_f32 v54, v54 clamp\nv_pk_fma_f32 v[156:157], v[158:159], v[44:45], v[156:157]\n"
      "v_pk_fma_f32 v[160:161], v[162:163], v[48:49], v[160:161]\nv_pk_mul_f32 v[44:45], v[44:45], v[46:47]\nv_exp_f32 v55, v55 clamp\nv_pk_mul_f32 v[48:49], v[48:49], v[50:51]\n"
      "v_pk_mul_f32 v[46:47], v[44:45], v[48:49]\nv_rcp_f32 v46, v46\nv_pk_mul_f32 v[156:157], v[156:157], v[48:49]\nv_rcp_f32 v47, v47\nv_pk_fma_f32 v[156:157], v[160:161], v[44:45], v[156:157]\n"
      "v_pk_mul_f32 v[32:33], v[156:157], v[46:47]\nv_pk_mul_f32 v[16:17], v[172:173], v[172:173]\nv_pk_mul_f32 v[18:19], v[174:175], v[174:175]\nv_exp_f32 v56, v56 clamp\n"
      "v_pk_mul_f32 v[20:21], v[176:177], v[176:177]\nv_pk_mul_f32 v[22:23], v[178:179], v[178:179]\nv_exp_f32 v57, v57 clamp\n"
      "v_pk_fma_f32 v[16:17], v[16:17], s[58:59], s[58:59] op_sel:[0,0,1] op_sel_hi:[1,0,1]\nv_pk_fma_f32 v[18:19], v[18:19], s[58:59], s[58:59] op_sel:[0,0,1] op_sel_hi:[1,0,1]\n"
      "v_pk_fma_f32 v[20:21], v[20:21], s[58:59], s[58:59] op_sel:[0,0,1] op_sel_hi:[1,0,1]\nv_exp_f32 v58, v58 clamp\n"
      "v_pk_fma_f32 v[22:23], v[22:23], s[58:59], s[58:59] op_sel:[0,0,1] op_sel_hi:[1,0,1]\nv_pk_fma_f32 v[16:17], v[16:17], v[172:173], s[60:61] op_sel_hi:[1,1,0]\n"
      "v_pk_fma_f32 v[18:19], v[18:19], v[174:175], s[60:61] op_sel_hi:[1,1,0]\nv_exp_f32 v59, v59 clamp\nv_pk_fma_f32 v[20:21], v[20:21], v[176:177], s[60:61] op_sel_hi:[1,1,0]\n"
      "v_pk_fma_f32 v[22:23], v[22:23], v[178:179], s[60:61] op_sel_hi:[1,1,0]\nv_exp_f32 v16, v16 clamp\nv_pk_fma_f32 v[54:55], v[54:55], v[10:11], v[66:67]\n"
      "v_pk_fma_f32 v[52:53], v[52:53], v[8:9], v[64:65]\nv_pk_fma_f32 v[56:57], v[56:57], v[12:13], v[68:69]\nv_exp_f32 v17, v17 clamp\nv_pk_fma_f32 v[58:59], v[58:59], v[14:15], v[70:71]\n"
      "v_pk_mul_f32 v[164:165], v[164:165], v[54:55]\nv_pk_mul_f32 v[168:169], v[168:169], v[58:59]\nv_exp_f32 v18, v18 clamp\nv_pk_fma_f32 v[164:165], v[166:167], v[52:53], v[164:165]\n"
      "v_pk_fma_f32 v[168:169], v[170:171], v[56:57], v[168:169]\nv_pk_mul_f32 v[52:53], v[52:53], v[54:55]\nv_exp_f32 v19, v19 clamp\nv_pk_mul_f32 v[56:57], v[56:57], v[58:59]\n"
      "v_pk_mul_f32 v[54:55], v[52:53], v[56:57]\nv_rcp_f32 v54, v54\nv_pk_mul_f32 v[164:165], v[164:165], v[56:57]\nv_rcp_f32 v55, v55\nv_pk_fma_f32 v[164:165], v[168:169], v[52:53], v[164:165]\n"
      "v_pk_fma_f32 v[32:33], v[164:165], v[54:55], v[32:33]\nv_add_f32 v36, v32, v33\nv_pk_mul_f32 v[44:45], v[180:181], v[180:181]\nv_pk_mul_f32 v[46:47], v[182:183], v[182:183]\n"
      "v_exp_f32 v20, v20 clamp\nv_pk_mul_f32 v[48:49], v[184:185], v[184:185]\nv_pk_mul_f32 v[50:51], v[186:187], v[186:187]\nv_exp_f32 v21, v21 clamp\n"
      "v_pk_fma_f32 v[44:45], v[44:45], s[58:59], s[58:59] op_sel:[0,0,1] op_sel_hi:[1,0,1]\nv_pk_fma_f32 v[46:47], v[46:47], s[58:59], s[58:59] op_sel:[0,0,1] op_sel_hi:[1,0,1]\n"
      "v_pk_fma_f32 v[48:49], v[48:49], s[58:59], s[58:59] op_sel:[0,0,1] op_sel_hi:[1,0,1]\nv_exp_f32 v22, v22 clamp\n"
      "v_pk_fma_f32 v[50:51], v[50:51], s[58:59], s[58:59] op_sel:[0,0,1] op_sel_hi:[1,0,1]\nv_pk_fma_f32 v[44:45], v[44:45], v[180:181], s[60:61] op_sel_hi:[1,1,0]\n"
      "v_pk_fma_f32 v[46:47], v[46:47], v[182:183], s[60:61] op_sel_hi:[1,1,0]\nv_exp_f32 v23, v23 clamp\nv_pk_fma_f32 v[48:49], v[48:49], v[184:185], s[60:61] op_sel_hi:[1,1,0]\n"
      "v_pk_fma_f32 v[50:51], v[50:51], v[186:187], s[60:61] op_sel_hi:[1,1,0]\nv_exp_f32 v44, v44 clamp\nv_pk_fma_f32 v[18:19], v[18:19], v[2:3], v[26:27]\n"
      "v_pk_fma_f32 v[16:17], v[16:17], v[0:1], v[24:25]\nv_pk_fma_f32 v[20:21], v[20:21], v[4:5], v[28:29]\nv_exp_f32 v45, v45 clamp\nv_pk_fma_f32 v[22:23], v[22:23], v[6:7], v[30:31]\n"
      "v_pk_mul_f32 v[172:173], v[172:173], v[18:19]\nv_pk_mul_f32 v[176:177], v[176:177], v[22:23]\nv_exp_f32 v46, v46 clamp\nv_pk_fma_f32 v[172:173], v[174:175], v[16:17], v[172:173]\n"
      "v_pk_fma_f32 v[176:177], v[178:179], v[20:21], v[176:177]\nv_pk_mul_f32 v[16:17], v[16:17], v[18:19]\nv_exp_f32 v47, v47 clamp\nv_pk_mul_f32 v[20:21], v[20:21], v[22:23]\n"
      "v_pk_mul_f32 v[18:19], v[16:17], v[20:21]\nv_rcp_f32 v18, v18\nv_pk_mul_f32 v[172:173], v[172:173], v[20:21]\nv_rcp_f32 v19, v19\nv_pk_fma_f32 v[172:173], v[176:177], v[16:17], v[172:173]\n"
      "v_pk_mul_f32 v[32:33], v[172:173], v[18:19]\nv_pk_mul_f32 v[52:53], v[188:189], v[188:189]\nv_pk_mul_f32 v[54:55], v[190:191], v[190:191]\nv_exp_f32 v48, v48 clamp\n"
      "v_pk_mul_f32 v[56:57], v[192:193], v[192:193]\nv_pk_mul_f32 v[58:59], v[194:195], v[194:195]\nv_exp_f32 v49, v49 clamp\n"
      "v_pk_fma_f32 v[52:53], v[52:53], s[58:59], s[58:59] op_sel:[0,0,1] op_sel_hi:[1,0,1]\nv_pk_fma_f32 v[54:55], v[54:55], s[58:59], s[58:59] op_sel:[0,0,1] op_sel_hi:[1,0,1]\n"
      "v_pk_fma_f32 v[56:57], v[56:57], s[58:59], s[58:59] op_sel:[0,0,1] op_sel_hi:[1,0,1]\nv_exp_f32 v50, v50 clamp\n"
      "v_pk_fma_f32 v[58:59], v[58:59], s[58:59], s[58:59] op_sel:[0,0,1] op_sel_hi:[1,0,1]\nv_pk_fma_f32 v[52:53], v[52:53], v[188:189], s[60:61] op_sel_hi:[1,1,0]\n"
      "v_pk_fma_f32 v[54:55], v[54:55], v[190:191], s[60:61] op_sel_hi:[1,1,0]\nv_exp_f32 v51, v51 clamp\nv_pk_fma_f32 v[56:57], v[56:57], v[192:193], s[60:61] op_sel_hi:[1,1,0]\n"
      "v_pk_fma_f32 v[58:59], v[58:59], v[194:195], s[60:61] op_sel_hi:[1,1,0]\nv_exp_f32 v52, v52 clamp\nv_pk_fma_f32 v[46:47], v[46:47], v[10:11], v[66:67]\n"
      "v_pk_fma_f32 v[44:45], v[44:45], v[8:9], v[64:65]\nv_pk_fma_f32 v[48:49], v[48:49], v[12:13], v[68:69]\nv_exp_f32 v53, v53 clamp\nv_pk_fma_f32 v[50:51], v[50:51], v[14:15], v[70:71]\n"
      "v_pk_mul_f32 v[180:181], v[180:181], v[46:47]\nv_pk_mul_f32 v[184:185], v[184:185], v[50:51]\nv_exp_f32 v54, v54 clamp\nv_pk_fma_f32 v[180:181], v[182:183], v[44:45], v[180:181]\n"
      "v_pk_fma_f32 v[184:185], v[186:187], v[48:49], v[184:185]\nv_pk_mul_f32 v[44:45], v[44:45], v[46:47]\nv_exp_f32 v55, v55 clamp\nv_pk_mul_f32 v[48:49], v[48:49], v[50:51]\n"
      "v_pk_mul_f32 v[46:47], v[44:45], v[48:49]\nv_rcp_f32 v46, v46\nv_pk_mul_f32 v[180:181], v[180:181], v[48:49]\nv_rcp_f32 v47, v47\nv_pk_fma_f32 v[180:181], v[184:185], v[44:45], v[180:181]\n"
      "v_pk_fma_f32 v[32:33], v[180:181], v[46:47], v[32:33]\nv_add_f32 v37, v32, v33\nv_pk_mul_f32 v[16:17], v[196:197], v[196:197]\nv_pk_mul_f32 v[18:19], v[198:199], v[198:199]\n"
      "v_exp_f32 v56, v56 clamp\nv_pk_mul_f32 v[20:21], v[200:201], v[200:201]\nv_pk_mul_f32 v[22:23], v[202:203], v[202:203]\nv_exp_f32 v57, v57 clamp\n"
      "v_pk_fma_f32 v[16:17], v[16:17], s[58:59], s[58:59] op_sel:[0,0,1] op_sel_hi:[1,0,1]\nv_pk_fma_f32 v[18:19], v[18:19], s[58:59], s[58:59] op_sel:[0,0,1] op_sel_hi:[1,0,1]\n"
      "v_pk_fma_f32 v[20:21], v[20:21], s[58:59], s[58:59] op_sel:[0,0,1] op_sel_hi:[1,0,1]\nv_exp_f32 v58, v58 clamp\n"
      "v_pk_fma_f32 v[22:23], v[22:23], s[58:59], s[58:59] op_sel:[0,0,1] op_sel_hi:[1,0,1]\nv_pk_fma_f32 v[16:17], v[16:17], v[196:197], s[60:61] op_sel_hi:[1,1,0]\n"
      "v_pk_fma_f32 v[18:19], v[18:19], v[198:199], s[60:61] op_sel_hi:[1,1,0]\nv_exp_f32 v59, v59 clamp\nv_pk_fma_f32 v[20:21], v[20:21], v[200:201], s[60:61] op_sel_hi:[1,1,0]\n"
      "v_pk_fma_f32 v[22:23], v[22:23], v[202:203], s[60:61] op_sel_hi:[1,1,0]\nv_exp_f32 v16, v16 clamp\nv_pk_fma_f32 v[54:55], v[54:55], v[2:3], v[26:27]\n"
      "v_pk_fma_f32 v[52:53], v[52:53], v[0:1], v[24:25]\nv_pk_fma_f32 v[56:57], v[56:57], v[4:5], v[28:29]\nv_exp_f32 v17, v17 clamp\nv_pk_fma_f32 v[58:59], v[58:59], v[6:7], v[30:31]\n"
      "v_pk_mul_f32 v[188:189], v[188:189], v[54:55]\nv_pk_mul_f32 v[192:193], v[192:193], v[58:59]\nv_exp_f32 v18, v18 clamp\nv_pk_fma_f32 v[188:189], v[190:191], v[52:53], v[188:189]\n"
      "v_pk_fma_f32 v[192:193], v[194:195], v[56:57], v[192:193]\nv_pk_mul_f32 v[52:53], v[52:53], v[54:55]\nv_exp_f32 v19, v19 clamp\nv_pk_mul_f32 v[56:57], v[56:57], v[58:59]\n"
      "v_pk_mul_f32 v[54:55], v[52:53], v[56:57]\nv_rcp_f32 v54, v54\nv_pk_mul_f32 v[188:189], v[188:189], v[56:57]\nv_rcp_f32 v55, v55\nv_pk_fma_f32 v[188:189], v[192:193], v[52:53], v[188:189]\n"
      "v_pk_mul_f32 v[32:33], v[188:189], v[54:55]\nv_pk_mul_f32 v[44:45], v[204:205], v[204:205]\nv_pk_mul_f32 v[46:47], v[206:207], v[206:207]\nv_exp_f32 v20, v20 clamp\n"
      "v_pk_mul_f32 v[48:49], v[208:209], v[208:209]\nv_pk_mul_f32 v[50:51], v[210:211], v[210:211]\nv_exp_f32 v21, v21 clamp\n"
      "v_pk_fma_f32 v[44:45], v[44:45], s[58:59], s[58:59] op_sel:[0,0,1] op_sel_hi:[1,0,1]\nv_pk_fma_f32 v[46:47], v[46:47], s[58:59], s[58:59] op_sel:[0,0,1] op_sel_hi:[1,0,1]\n"
      "v_pk_fma_f32 v[48:49], v[48:49], s[58:59], s[58:59] op_sel:[0,0,1] op_sel_hi:[1,0,1]\nv_exp_f32 v22, v22 clamp\n"
      "v_pk_fma_f32 v[50:51], v[50:51], s[58:59], s[58:59] op_sel:[0,0,1] op_sel_hi:[1,0,1]\nv_pk_fma_f32 v[44:45], v[44:45], v[204:205], s[60:61] op_sel_hi:[1,1,0]\n"
      "v_pk_fma_f32 v[46:47], v[46:47], v[206:207], s[60:61] op_sel_hi:[1,1,0]\nv_exp_f32 v23, v23 clamp\nv_pk_fma_f32 v[48:49], v[48:49], v[208:209], s[60:61] op_sel_hi:[1,1,0]\n"
      "v_pk_fma_f32 v[50:51], v[50:51], v[210:211], s[60:61] op_sel_hi:[1,1,0]\nv_exp_f32 v44, v44 clamp\nv_pk_fma_f32 v[18:19], v[18:19], v[10:11], v[66:67]\n"
      "v_pk_fma_f32 v[16:17], v[16:17], v[8:9], v[64:65]\nv_pk_fma_f32 v[20:21], v[20:21], v[12:13], v[68:69]\nv_exp_f32 v45, v45 clamp\nv_pk_fma_f32 v[22:23], v[22:23], v[14:15], v[70:71]\n"
      "v_pk_mul_f32 v[196:197], v[196:197], v[18:19]\nv_pk_mul_f32 v[200:201], v[200:201], v[22:23]\nv_exp_f32 v46, v46 clamp\nv_pk_fma_f32 v[196:197], v[198:199], v[16:17], v[196:197]\n"
      "v_pk_fma_f32 v[200:201], v[202:203], v[20:21], v[200:201]\nv_pk_mul_f32 v[16:17], v[16:17], v[18:19]\nv_exp_f32 v47, v47 clamp\nv_pk_mul_f32 v[20:21], v[20:21], v[22:23]\n"
      "v_pk_mul_f32 v[18:19], v[16:17], v[20:21]\nv_rcp_f32 v18, v18\nv_pk_mul_f32 v[196:197], v[196:197], v[20:21]\nv_rcp_f32 v19, v19\nv_pk_fma_f32 v[196:197], v[200:201], v[16:17], v[196:197]\n"
      "v_pk_fma_f32 v[32:33], v[196:197], v[18:19], v[32:33]\nv_add_f32 v38, v32, v33\nv_pk_mul_f32 v[52:53], v[212:213], v[212:213]\nv_pk_mul_f32 v[54:55], v[214:215], v[214:215]\n"
      "v_exp_f32 v48, v48 clamp\nv_pk_mul_f32 v[56:57], v[216:217], v[216:217]\nv_pk_mul_f32 v[58:59], v[218:219], v[218:219]\nv_exp_f32 v49, v49 clamp\n"
      "v_pk_fma_f32 v[52:53], v[52:53], s[58:59], s[58:59] op_sel:[0,0,1] op_sel_hi:[1,0,1]\nv_pk_fma_f32 v[54:55], v[54:55], s[58:59], s[58:59] op_sel:[0,0,1] op_sel_hi:[1,0,1]\n"
      "v_pk_fma_f32 v[56:57], v[56:57], s[58:59], s[58:59] op_sel:[0,0,1] op_sel_hi:[1,0,1]\nv_exp_f32 v50, v50 clamp\n"
      "v_pk_fma_f32 v[58:59], v[58:59], s[58:59], s[58:59] op_sel:[0,0,1] op_sel_hi:[1,0,1]\nv_pk_fma_f32 v[52:53], v[52:53], v[212:213], s[60:61] op_sel_hi:[1,1,0]\n"
      "v_pk_fma_f32 v[54:55], v[54:55], v[214:215], s[60:61] op_sel_hi:[1,1,0]\nv_exp_f32 v51, v51 clamp\nv_pk_fma_f32 v[56:57], v[56:57], v[216:217], s[60:61] op_sel_hi:[1,1,0]\n"
      "v_pk_fma_f32 v[58:59], v[58:59], v[218:219], s[60:61] op_sel_hi:[1,1,0]\nv_exp_f32 v52, v52 clamp\nv_pk_fma_f32 v[46:47], v[46:47], v[2:3], v[26:27]\n"
      "v_pk_fma_f32 v[44:45], v[44:45], v[0:1], v[24:25]\nv_pk_fma_f32 v[48:49], v[48:49], v[4:5], v[28:29]\nv_exp_f32 v53, v53 clamp\nv_pk_fma_f32 v[50:51], v[50:51], v[6:7], v[30:31]\n"
      "v_pk_mul_f32 v[204:205], v[204:205], v[46:47]\nv_pk_mul_f32 v[208:209], v[208:209], v[50:51]\nv_exp_f32 v54, v54 clamp\nv_pk_fma_f32 v[204:205], v[206:207], v[44:45], v[204:205]\n"
      "v_pk_fma_f32 v[208:209], v[210:211], v[48:49], v[208:209]\nv_pk_mul_f32 v[44:45], v[44:45], v[46:47]\nv_exp_f32 v55, v55 clamp\nv_pk_mul_f32 v[48:49], v[48:49], v[50:51]\n"
      "v_pk_mul_f32 v[46:47], v[44:45], v[48:49]\nv_rcp_f32 v46, v46\nv_pk_mul_f32 v[204:205], v[204:205], v[48:49]\nv_rcp_f32 v47, v47\nv_pk_fma_f32 v[204:205], v[208:209], v[44:45], v[204:205]\n"
      "v_pk_mul_f32 v[32:33], v[204:205], v[46:47]\nv_pk_mul_f32 v[16:17], v[220:221], v[220:221]\nv_pk_mul_f32 v[18:19], v[222:223], v[222:223]\nv_exp_f32 v56, v56 clamp\n"
      "v_pk_mul_f32 v[20:21], v[224:225], v[224:225]\nv_pk_mul_f32 v[22:23], v[226:227], v[226:227]\nv_exp_f32 v57, v57 clamp\n"
      "v_pk_fma_f32 v[16:17], v[16:17], s[58:59], s[58:59] op_sel:[0,0,1] op_sel_hi:[1,0,1]\nv_pk_fma_f32 v[18:19], v[18:19], s[58:59], s[58:59] op_sel:[0,0,1] op_sel_hi:[1,0,1]\n"
      "v_pk_fma_f32 v[20:21], v[20:21], s[58:59], s[58:59] op_sel:[0,0,1] op_sel_hi:[1,0,1]\nv_exp_f32 v58, v58 clamp\n"
      "v_pk_fma_f32 v[22:23], v[22:23], s[58:59], s[58:59] op_sel:[0,0,1] op_sel_hi:[1,0,1]\nv_pk_fma_f32 v[16:17], v[16:17], v[220:221], s[60:61] op_sel_hi:[1,1,0]\n"
      "v_pk_fma_f32 v[18:19], v[18:19], v[222:223], s[60:61] op_sel_hi:[1,1,0]\nv_exp_f32 v59, v59 clamp\nv_pk_fma_f32 v[20:21], v[20:21], v[224:225], s[60:61] op_sel_hi:[1,1,0]\n"
      "v_pk_fma_f32 v[22:23], v[22:23], v[226:227], s[60:61] op_sel_hi:[1,1,0]\nv_exp_f32 v16, v16 clamp\nv_pk_fma_f32 v[54:55], v[54:55], v[10:11], v[66:67]\n"
      "v_pk_fma_f32 v[52:53], v[52:53], v[8:9], v[64:65]\nv_pk_fma_f32 v[56:57], v[56:57], v[12:13], v[68:69]\nv_exp_f32 v17, v17 clamp\nv_pk_fma_f32 v[58:59], v[58:59], v[14:15], v[70:71]\n"
      "v_pk_mul_f32 v[212:213], v[212:213], v[54:55]\nv_pk_mul_f32 v[216:217], v[216:217], v[58:59]\nv_exp_f32 v18, v18 clamp\nv_pk_fma_f32 v[212:213], v[214:215], v[52:53], v[212:213]\n"
      "v_pk_fma_f32 v[216:217], v[218:219], v[56:57], v[216:217]\nv_pk_mul_f32 v[52:53], v[52:53], v[54:55]\nv_exp_f32 v19, v19 clamp\nv_pk_mul_f32 v[56:57], v[56:57], v[58:59]\n"
      "v_pk_mul_f32 v[54:55], v[52:53], v[56:57]\nv_rcp_f32 v54, v54\nv_pk_mul_f32 v[212:213], v[212:213], v[56:57]\nv_rcp_f32 v55, v55\nv_pk_fma_f32 v[212:213], v[216:217], v[52:53], v[212:213]\n"
      "v_pk_fma_f32 v[32:33], v[212:213], v[54:55], v[32:33]\nv_add_f32 v39, v32, v33\nv_pk_mul_f32 v[44:45], v[228:229], v[228:229]\nv_pk_mul_f32 v[46:47], v[230:231], v[230:231]\n"
      "v_exp_f32 v20, v20 clamp\nv_pk_mul_f32 v[48:49], v[232:233], v[232:233]\nv_pk_mul_f32 v[50:51], v[234:235], v[234:235]\nv_exp_f32 v21, v21 clamp\n"
      "v_pk_fma_f32 v[44:45], v[44:45], s[58:59], s[58:59] op_sel:[0,0,1] op_sel_hi:[1,0,1]\nv_pk_fma_f32 v[46:47], v[46:47], s[58:59], s[58:59] op_sel:[0,0,1] op_sel_hi:[1,0,1]\n"
      "v_pk_fma_f32 v[48:49], v[48:49], s[58:59], s[58:59] op_sel:[0,0,1] op_sel_hi:[1,0,1]\nv_exp_f32 v22, v22 clamp\n"
      "v_pk_fma_f32 v[50:51], v[50:51], s[58:59], s[58:59] op_sel:[0,0,1] op_sel_hi:[1,0,1]\nv_pk_fma_f32 v[44:45], v[44:45], v[228:229], s[60:61] op_sel_hi:[1,1,0]\n"
      "v_pk_fma_f32 v[46:47], v[46:47], v[230:231], s[60:61] op_sel_hi:[1,1,0]\nv_exp_f32 v23, v23 clamp\nv_pk_fma_f32 v[48:49], v[48:49], v[232:233], s[60:61] op_sel_hi:[1,1,0]\n"
      "v_pk_fma_f32 v[50:51], v[50:51], v[234:235], s[60:61] op_sel_hi:[1,1,0]\nv_exp_f32 v44, v44 clamp\nv_pk_fma_f32 v[18:19], v[18:19], v[2:3], v[26:27]\nv_exp_f32 v45, v45 clamp\n"
      "v_pk_fma_f32 v[16:17], v[16:17], v[0:1], v[24:25]\nv_pk_fma_f32 v[20:21], v[20:21], v[4:5], v[28:29]\nv_exp_f32 v46, v46 clamp\nv_pk_fma_f32 v[22:23], v[22:23], v[6:7], v[30:31]\n"
      "v_pk_mul_f32 v[220:221], v[220:221], v[18:19]\nv_exp_f32 v47, v47 clamp\nv_pk_mul_f32 v[224:225], v[224:225], v[22:23]\nv_pk_fma_f32 v[220:221], v[222:223], v[16:17], v[220:221]\n"
      "v_exp_f32 v48, v48 clamp\nv_pk_fma_f32 v[224:225], v[226:227], v[20:21], v[224:225]\nv_exp_f32 v49, v49 clamp\nv_pk_mul_f32 v[16:17], v[16:17], v[18:19]\n"
      "v_pk_mul_f32 v[20:21], v[20:21], v[22:23]\nv_exp_f32 v50, v50 clamp\nv_pk_mul_f32 v[18:19], v[16:17], v[20:21]\nv_rcp_f32 v18, v18\nv_pk_mul_f32 v[220:221], v[220:221], v[20:21]\n"
      "v_exp_f32 v51, v51 clamp\nv_rcp_f32 v19, v19\nv_pk_fma_f32 v[220:221], v[224:225], v[16:17], v[220:221]\nv_pk_mul_f32 v[32:33], v[220:221], v[18:19]\n"
      "v_pk_fma_f32 v[46:47], v[46:47], v[10:11], v[66:67]\nv_pk_fma_f32 v[44:45], v[44:45], v[8:9], v[64:65]\nv_pk_fma_f32 v[48:49], v[48:49], v[12:13], v[68:69]\n"
      "v_pk_fma_f32 v[50:51], v[50:51], v[14:15], v[70:71]\nv_pk_mul_f32 v[228:229], v[228:229], v[46:47]\nv_pk_mul_f32 v[232:233], v[232:233], v[50:51]\n"
      "v_pk_fma_f32 v[228:229], v[230:231], v[44:45], v[228:229]\nv_pk_fma_f32 v[232:233], v[234:235], v[48:49], v[232:233]\nv_pk_mul_f32 v[44:45], v[44:45], v[46:47]\n"
      "v_pk_mul_f32 v[48:49], v[48:49], v[50:51]\nv_pk_mul_f32 v[46:47], v[44:45], v[48:49]\nv_rcp_f32 v46, v46\nv_pk_mul_f32 v[228:229], v[228:229], v[48:49]\nv_rcp_f32 v47, v47\n"
      "v_pk_fma_f32 v[228:229], v[232:233], v[44:45], v[228:229]\nv_pk_fma_f32 v[32:33], v[228:229], v[46:47], v[32:33]\nv_add_f32 v40, v32, v33\ns_barrier\n"
      ".Lge:\nv_mov_b32 v43, v42\nds_read_b128 v[48:51], v246\nds_read_b128 v[52:55], v246 offset:16\nds_read_b64_tr_b16 v[68:69], v238 offset:0\nds_read_b64_tr_b16 v[70:71], v239 offset:0\n"
      "ds_read_b64_tr_b16 v[56:57], v236 offset:0\nds_read_b64_tr_b16 v[58:59], v237 offset:128\nds_read_b64_tr_b16 v[60:61], v237 offset:256\nds_read_b64_tr_b16 v[62:63], v237 offset:384\n"
      "ds_read_b64_tr_b16 v[64:65], v237 offset:512\nds_read_b64_tr_b16 v[66:67], v237 offset:640\nds_read_b128 v[44:47], v248 offset:48\nv_permlane32_swap_b32 v36, v38\n"
      "v_permlane32_swap_b32 v37, v39\nv_permlane32_swap_b32 v40, v42\nv_permlane32_swap_b32 v41, v43\nv_add_f32 v36, v36, v38\nv_add_f32 v37, v37, v39\nv_add_f32 v40, v40, v42\n"
      "v_add_f32 v41, v41, v43\ns_nop 0\nv_permlane16_swap_b32 v36, v37\nv_permlane16_swap_b32 v40, v41\nv_add_f32 v36, v36, v37\nv_add_f32 v40, v40, v41\nds_write_b32 v254, v36\n"
      "ds_write_b32 v255, v40 offset:256\nds_add_rtn_u32 v2, v253, v241 offset:20\ns_waitcnt lgkmcnt(11)\nv_readlane_b32 s48, v69, 48\ns_mov_b32 s56, 0\ns_mov_b32 s44, 0\ns_mov_b32 s33, s45\n"
      "s_waitcnt lgkmcnt(10)\nv_pk_mul_f16 v20, v68, v70\nv_pk_mul_f16 v21, v69, v71\ns_waitcnt lgkmcnt(9)\nv_pk_mul_f16 v22, v56, v56 op_sel_hi:[0,1]\nv_pk_mul_f16 v23, v56, v57 op_sel_hi:[0,1]\n"
      "v_pk_mul_f16 v16, v68, s48 op_sel:[0,1]\nv_pk_mul_f16 v17, v69, s48 op_sel:[0,1]\nv_pk_mul_f16 v18, v70, s48 op_sel:[0,1]\nv_pk_mul_f16 v19, v71, s48 op_sel:[0,1]\ns_nop 1\n"
      "v_mfma_f32_16x16x32_f16 v[40:43], v[76:79], v[16:19], 0\nv_mfma_f32_16x16x32_f16 v[40:43], v[88:91], v[20:23], v[40:43]\ns_add_u32 s45, s45, 2\ns_cmp_ge_u32 s45, s46\ns_cbranch_scc1 .Lc_tg\n"
      ".Lc_bg:\nv_mfma_f32_16x16x32_f16 v[36:39], v[84:87], v[20:23], 0\ns_waitcnt lgkmcnt(4)\nv_pk_mul_f16 v24, v66, v66 op_sel_hi:[0,1]\nv_pk_mul_f16 v25, v66, v57 op_sel_hi:[0,1]\n"
      "ds_read_b128 v[124:127], v252 offset:13312\nv_mfma_f32_16x16x32_f16 v[36:39], v[72:75], v[16:19], v[36:39]\nv_pk_mul_f16 v26, v56, v66 op_sel_hi:[0,1]\n"
      "v_pk_mul_f16 v27, v56, v67 op_sel_hi:[0,1]\nds_read_b128 v[128:131], v252 offset:14336\nv_mfma_f32_16x16x32_f16 v[32:35], v[80:83], v[20:23], 0\nv_pk_mul_f16 v28, v58, v66 op_sel_hi:[0,1]\n"
      "v_pk_mul_f16 v29, v58, v67 op_sel_hi:[0,1]\nds_read_b128 v[132:135], v252 offset:15360\ns_waitcnt lgkmcnt(6)\nv_smfmac_f32_16x16x64_f16 v[44:47], v[92:95], v[16:23], v242\n"
      "v_pk_mul_f16 v30, v60, v66 op_sel_hi:[0,1]\nv_pk_mul_f16 v31, v60, v67 op_sel_hi:[0,1]\nds_read_b128 v[136:139], v252 offset:16384\n"
      "v_mfma_f32_16x16x32_f16 v[32:35], v[96:99], v[24:27], v[32:35]\nv_pk_mul_f16 v16, v62, v66 op_sel_hi:[0,1]\nv_pk_mul_f16 v17, v62, v67 op_sel_hi:[0,1]\n"
      "ds_read_b128 v[140:143], v252 offset:17408\nv_mfma_f32_16x16x32_f16 v[32:35], v[108:111], v[28:31], v[32:35]\nv_pk_mul_f16 v18, v64, v66 op_sel_hi:[0,1]\n"
      "ds_read_b128 v[144:147], v252 offset:18432\nv_mfma_f32_16x16x32_f16 v[36:39], v[112:115], v[28:31], v[36:39]\nv_pk_mul_f16 v19, v64, v67 op_sel_hi:[0,1]\ns_cmp_ge_u32 s45, s46\n"
      "s_cbranch_scc1 .Lc_th\n"
      ".Lc_bh:\nds_read_b128 v[148:151], v252 offset:19456\nv_mfma_f32_16x16x32_f16 v[36:39], v[100:103], v[24:27], v[36:39]\nv_pk_mul_f16 v20, v64, v64 op_sel_hi:[0,1]\n"
      "ds_read_b128 v[152:155], v252 offset:20480\nv_mfma_f32_16x16x32_f16 v[40:43], v[104:107], v[24:27], v[40:43]\nv_pk_mul_f16 v21, v64, v57 op_sel_hi:[0,1]\n"
      "ds_read_b128 v[156:159], v252 offset:21504\nv_mfma_f32_16x16x32_f16 v[40:43], v[116:119], v[28:31], v[40:43]\nv_pk_mul_f16 v22, v56, v64 op_sel_hi:[0,1]\n"
      "ds_read_b128 v[160:163], v252 offset:22528\nv_smfmac_f32_16x16x64_f16 v[44:47], v[120:123], v[24:31], v242\nv_pk_mul_f16 v23, v56, v65 op_sel_hi:[0,1]\n"
      "ds_read_b128 v[164:167], v252 offset:23552\ns_waitcnt lgkmcnt(10)\nv_mfma_f32_16x16x32_f16 v[32:35], v[124:127], v[16:19], v[32:35]\nv_pk_mul_f16 v24, v58, v64 op_sel_hi:[0,1]\n"
      "v_pk_mul_f16 v25, v58, v65 op_sel_hi:[0,1]\nds_read_b64_tr_b16 v[70:71], v239 offset:1856\nds_read_b128 v[168:171], v252 offset:24576\ns_waitcnt lgkmcnt(9)\n"
      "v_mfma_f32_16x16x32_f16 v[32:35], v[136:139], v[20:23], v[32:35]\nv_pk_mul_f16 v26, v60, v64 op_sel_hi:[0,1]\nds_read_b64_tr_b16 v[68:69], v238 offset:1856\n"
      "ds_read_b128 v[172:175], v252 offset:25600\ns_waitcnt lgkmcnt(10)\nv_mfma_f32_16x16x32_f16 v[36:39], v[140:143], v[20:23], v[36:39]\nv_pk_mul_f16 v27, v60, v65 op_sel_hi:[0,1]\n"
      "ds_read_b64_tr_b16 v[66:67], v237 offset:2496\nds_read_b128 v[176:179], v252 offset:26624\nv_mfma_f32_16x16x32_f16 v[36:39], v[128:131], v[16:19], v[36:39]\n"
      "v_pk_mul_f16 v28, v62, v64 op_sel_hi:[0,1]\nds_read_b128 v[180:183], v252 offset:27648\nv_mfma_f32_16x16x32_f16 v[40:43], v[132:135], v[16:19], v[40:43]\n"
      "v_pk_mul_f16 v29, v62, v65 op_sel_hi:[0,1]\nds_read_b128 v[184:187], v252 offset:28672\ns_waitcnt lgkmcnt(13)\nv_mfma_f32_16x16x32_f16 v[40:43], v[144:147], v[20:23], v[40:43]\n"
      "v_pk_mul_f16 v30, v62, v62 op_sel_hi:[0,1]\nds_read_b128 v[188:191], v252 offset:29696\ns_waitcnt lgkmcnt(13)\nv_smfmac_f32_16x16x64_f16 v[44:47], v[148:151], v[16:23], v242\n"
      "v_pk_mul_f16 v31, v62, v57 op_sel_hi:[0,1]\nds_read_b128 v[192:195], v252 offset:30720\ns_waitcnt lgkmcnt(13)\nv_mfma_f32_16x16x32_f16 v[32:35], v[152:155], v[24:27], v[32:35]\n"
      "v_pk_mul_f16 v16, v56, v62 op_sel_hi:[0,1]\nv_pk_mul_f16 v17, v56, v63 op_sel_hi:[0,1]\nds_read_b128 v[196:199], v252 offset:31744\ns_waitcnt lgkmcnt(11)\n"
      "v_mfma_f32_16x16x32_f16 v[32:35], v[164:167], v[28:31], v[32:35]\nv_pk_mul_f16 v18, v58, v62 op_sel_hi:[0,1]\nds_read_b128 v[200:203], v252 offset:32768\n"
      "ds_read_b64_tr_b16 v[64:65], v237 offset:2368\ns_waitcnt lgkmcnt(11)\nv_mfma_f32_16x16x32_f16 v[36:39], v[168:171], v[28:31], v[36:39]\nv_pk_mul_f16 v19, v58, v63 op_sel_hi:[0,1]\n"
      "ds_read_b128 v[204:207], v252 offset:33792\nv_mfma_f32_16x16x32_f16 v[36:39], v[156:159], v[24:27], v[36:39]\nv_pk_mul_f16 v20, v60, v62 op_sel_hi:[0,1]\n"
      "ds_read_b128 v[208:211], v252 offset:34816\nv_mfma_f32_16x16x32_f16 v[40:43], v[160:163], v[24:27], v[40:43]\nv_pk_mul_f16 v21, v60, v63 op_sel_hi:[0,1]\n"
      "ds_read_b128 v[212:215], v252 offset:35840\ns_waitcnt lgkmcnt(13)\nv_readlane_b32 s48, v69, 48\ns_waitcnt lgkmcnt(12)\nv_mfma_f32_16x16x32_f16 v[40:43], v[172:175], v[28:31], v[40:43]\n"
      "v_pk_mul_f16 v22, v60, v60 op_sel_hi:[0,1]\nds_read_b128 v[216:219], v252 offset:36864\ns_waitcnt lgkmcnt(11)\nv_smfmac_f32_16x16x64_f16 v[44:47], v[176:179], v[24:31], v242\n"
      "v_pk_mul_f16 v23, v60, v57 op_sel_hi:[0,1]\nds_read_b128 v[220:223], v252 offset:37888\ns_waitcnt lgkmcnt(11)\nv_mfma_f32_16x16x32_f16 v[32:35], v[180:183], v[16:19], v[32:35]\n"
      "v_pk_mul_f16 v24, v56, v60 op_sel_hi:[0,1]\nv_pk_mul_f16 v25, v56, v61 op_sel_hi:[0,1]\nds_read_b128 v[224:227], v252 offset:38912\ns_waitcnt lgkmcnt(9)\n"
      "v_mfma_f32_16x16x32_f16 v[32:35], v[192:195], v[20:23], v[32:35]\nv_pk_mul_f16 v26, v58, v60 op_sel_hi:[0,1]\nv_pk_mul_f16 v27, v58, v61 op_sel_hi:[0,1]\n"
      "ds_read_b128 v[228:231], v252 offset:39936\ns_waitcnt lgkmcnt(9)\nv_mfma_f32_16x16x32_f16 v[36:39], v[196:199], v[20:23], v[36:39]\nv_pk_mul_f16 v28, v58, v58 op_sel_hi:[0,1]\n"
      "ds_read_b128 v[232:235], v252 offset:40960\nds_read_b64_tr_b16 v[62:63], v237 offset:2240\nv_mfma_f32_16x16x32_f16 v[36:39], v[184:187], v[16:19], v[36:39]\n"
      "v_pk_mul_f16 v29, v58, v57 op_sel_hi:[0,1]\nds_read_b64_tr_b16 v[60:61], v237 offset:2112\nv_mfma_f32_16x16x32_f16 v[40:43], v[188:191], v[16:19], v[40:43]\n"
      "v_pk_mul_f16 v30, v56, v58 op_sel_hi:[0,1]\ns_waitcnt lgkmcnt(11)\nv_mfma_f32_16x16x32_f16 v[40:43], v[200:203], v[20:23], v[40:43]\nv_pk_mul_f16 v31, v56, v59 op_sel_hi:[0,1]\n"
      "s_waitcnt lgkmcnt(9)\nv_smfmac_f32_16x16x64_f16 v[44:47], v[204:207], v[16:23], v242\nds_read_b64_tr_b16 v[56:57], v236 offset:1856\nds_read_b64_tr_b16 v[58:59], v237 offset:1984\n"
      "s_waitcnt lgkmcnt(4)\nv_smfmac_f32_16x16x64_f16 v[44:47], v[232:235], v[24:31], v242\nv_pk_mul_f16 v16, v68, s48 op_sel:[0,1]\nv_pk_mul_f16 v17, v69, s48 op_sel:[0,1]\n"
      "v_mfma_f32_16x16x32_f16 v[32:35], v[208:211], v[24:27], v[32:35]\nv_pk_mul_f16 v18, v70, s48 op_sel:[0,1]\nv_pk_mul_f16 v19, v71, s48 op_sel:[0,1]\n"
      "v_mfma_f32_16x16x32_f16 v[32:35], v[220:223], v[28:31], v[32:35]\nv_pk_mul_f16 v20, v68, v70\nv_pk_mul_f16 v21, v69, v71\nv_mfma_f32_16x16x32_f16 v[36:39], v[224:227], v[28:31], v[36:39]\n"
      "v_add_f32 v44, v44, v45\nv_add_f32 v46, v46, v47\nv_mfma_f32_16x16x32_f16 v[36:39], v[212:215], v[24:27], v[36:39]\nv_mul_f32 v55, v44, v54\nv_mul_f32 v55, v55, v46\n"
      "ds_read_b128 v[44:47], v248 offset:48\nv_mfma_f32_16x16x32_f16 v[40:43], v[216:219], v[24:27], v[40:43]\ns_waitcnt lgkmcnt(2)\nv_pk_mul_f16 v22, v56, v56 op_sel_hi:[0,1]\n"
      "v_pk_mul_f16 v23, v56, v57 op_sel_hi:[0,1]\nv_mfma_f32_16x16x32_f16 v[40:43], v[228:231], v[28:31], v[40:43]\nv_mul_f32 v32, v32, v48\nv_mul_f32 v33, v33, v49\nv_mul_f32 v34, v34, v50\n"
      "s_waitcnt lgkmcnt(1)\ns_branch .Lc_u1\n"
      ".Lcin:\nv_readlane_b32 s48, v69, 48\ns_mov_b32 s56, 0\ns_mov_b32 s44, 0\ns_mov_b32 s33, s45\nv_pk_mul_f16 v20, v68, v70\nv_pk_mul_f16 v21, v69, v71\n"
      "v_pk_mul_f16 v22, v56, v56 op_sel_hi:[0,1]\nv_pk_mul_f16 v23, v56, v57 op_sel_hi:[0,1]\nv_pk_mul_f16 v16, v68, s48 op_sel:[0,1]\nv_pk_mul_f16 v17, v69, s48 op_sel:[0,1]\n"
      "v_pk_mul_f16 v18, v70, s48 op_sel:[0,1]\nv_pk_mul_f16 v19, v71, s48 op_sel:[0,1]\n.p2align 3\ns_swappc_b64 s[4:5], s[34:35]\ns_barrier\n"
      "v_mfma_f32_16x16x32_f16 v[40:43], v[76:79], v[16:19], 0\ns_setprio 0\nv_mfma_f32_16x16x32_f16 v[40:43], v[88:91], v[20:23], v[40:43]\ns_add_u32 s45, s45, 2\ns_cmp_ge_u32 s45, s46\n"
      "s_cbranch_scc1 .Lc_t0\n"
      ".Lc_b0:\nv_mfma_f32_16x16x32_f16 v[36:39], v[84:87], v[20:23], 0\nv_pk_mul_f16 v24, v66, v66 op_sel_hi:[0,1]\nv_pk_mul_f16 v25, v66, v57 op_sel_hi:[0,1]\n"
      "v_mfma_f32_16x16x32_f16 v[36:39], v[72:75], v[16:19], v[36:39]\nv_pk_mul_f16 v26, v56, v66 op_sel_hi:[0,1]\nv_pk_mul_f16 v27, v56, v67 op_sel_hi:[0,1]\n"
      "v_mfma_f32_16x16x32_f16 v[32:35], v[80:83], v[20:23], 0\nv_pk_mul_f16 v28, v58, v66 op_sel_hi:[0,1]\nv_pk_mul_f16 v29, v58, v67 op_sel_hi:[0,1]\ns_setprio 1\ns_waitcnt lgkmcnt(0)\n"
      "v_smfmac_f32_16x16x64_f16 v[44:47], v[92:95], v[16:23], v242\nv_pk_mul_f16 v30, v60, v66 op_sel_hi:[0,1]\nv_pk_mul_f16 v31, v60, v67 op_sel_hi:[0,1]\n"
      "v_mfma_f32_16x16x32_f16 v[32:35], v[96:99], v[24:27], v[32:35]\nv_pk_mul_f16 v16, v62, v66 op_sel_hi:[0,1]\nv_pk_mul_f16 v17, v62, v67 op_sel_hi:[0,1]\n"
      "v_mfma_f32_16x16x32_f16 v[32:35], v[108:111], v[28:31], v[32:35]\nv_pk_mul_f16 v18, v64, v66 op_sel_hi:[0,1]\nv_mfma_f32_16x16x32_f16 v[36:39], v[112:115], v[28:31], v[36:39]\n"
      "v_pk_mul_f16 v19, v64, v67 op_sel_hi:[0,1]\nv_mfma_f32_16x16x32_f16 v[36:39], v[100:103], v[24:27], v[36:39]\nv_pk_mul_f16 v20, v64, v64 op_sel_hi:[0,1]\n"
      "v_mfma_f32_16x16x32_f16 v[40:43], v[104:107], v[24:27], v[40:43]\nv_pk_mul_f16 v21, v64, v57 op_sel_hi:[0,1]\nv_mfma_f32_16x16x32_f16 v[40:43], v[116:119], v[28:31], v[40:43]\n"
      "v_pk_mul_f16 v22, v56, v64 op_sel_hi:[0,1]\nv_smfmac_f32_16x16x64_f16 v[44:47], v[120:123], v[24:31], v242\nv_pk_mul_f16 v23, v56, v65 op_sel_hi:[0,1]\ns_setprio 2\n"
      "v_mfma_f32_16x16x32_f16 v[32:35], v[124:127], v[16:19], v[32:35]\nv_pk_mul_f16 v24, v58, v64 op_sel_hi:[0,1]\nv_pk_mul_f16 v25, v58, v65 op_sel_hi:[0,1]\n"
      "ds_read_b64_tr_b16 v[70:71], v239 offset:1856\nv_mfma_f32_16x16x32_f16 v[32:35], v[136:139], v[20:23], v[32:35]\nv_pk_mul_f16 v26, v60, v64 op_sel_hi:[0,1]\n"
      "ds_read_b64_tr_b16 v[68:69], v238 offset:1856\nv_mfma_f32_16x16x32_f16 v[36:39], v[140:143], v[20:23], v[36:39]\nv_pk_mul_f16 v27, v60, v65 op_sel_hi:[0,1]\n"
      "ds_read_b64_tr_b16 v[66:67], v237 offset:2496\nv_mfma_f32_16x16x32_f16 v[36:39], v[128:131], v[16:19], v[36:39]\nv_pk_mul_f16 v28, v62, v64 op_sel_hi:[0,1]\n"
      "v_mfma_f32_16x16x32_f16 v[40:43], v[132:135], v[16:19], v[40:43]\nv_pk_mul_f16 v29, v62, v65 op_sel_hi:[0,1]\nv_mfma_f32_16x16x32_f16 v[40:43], v[144:147], v[20:23], v[40:43]\n"
      "v_pk_mul_f16 v30, v62, v62 op_sel_hi:[0,1]\nv_smfmac_f32_16x16x64_f16 v[44:47], v[148:151], v[16:23], v242\nv_pk_mul_f16 v31, v62, v57 op_sel_hi:[0,1]\n"
      "v_mfma_f32_16x16x32_f16 v[32:35], v[152:155], v[24:27], v[32:35]\nv_pk_mul_f16 v16, v56, v62 op_sel_hi:[0,1]\nv_pk_mul_f16 v17, v56, v63 op_sel_hi:[0,1]\n"
      "v_mfma_f32_16x16x32_f16 v[32:35], v[164:167], v[28:31], v[32:35]\nv_pk_mul_f16 v18, v58, v62 op_sel_hi:[0,1]\nds_read_b64_tr_b16 v[64:65], v237 offset:2368\n"
      "v_mfma_f32_16x16x32_f16 v[36:39], v[168:171], v[28:31], v[36:39]\nv_pk_mul_f16 v19, v58, v63 op_sel_hi:[0,1]\ns_setprio 3\nv_mfma_f32_16x16x32_f16 v[36:39], v[156:159], v[24:27], v[36:39]\n"
      "v_pk_mul_f16 v20, v60, v62 op_sel_hi:[0,1]\nv_mfma_f32_16x16x32_f16 v[40:43], v[160:163], v[24:27], v[40:43]\nv_pk_mul_f16 v21, v60, v63 op_sel_hi:[0,1]\ns_waitcnt lgkmcnt(1)\n"
      "v_readlane_b32 s48, v69, 48\nv_mfma_f32_16x16x32_f16 v[40:43], v[172:175], v[28:31], v[40:43]\nv_pk_mul_f16 v22, v60, v60 op_sel_hi:[0,1]\n"
      "v_smfmac_f32_16x16x64_f16 v[44:47], v[176:179], v[24:31], v242\nv_pk_mul_f16 v23, v60, v57 op_sel_hi:[0,1]\nv_mfma_f32_16x16x32_f16 v[32:35], v[180:183], v[16:19], v[32:35]\n"
      "v_pk_mul_f16 v24, v56, v60 op_sel_hi:[0,1]\nv_pk_mul_f16 v25, v56, v61 op_sel_hi:[0,1]\nv_mfma_f32_16x16x32_f16 v[32:35], v[192:195], v[20:23], v[32:35]\n"
      "v_pk_mul_f16 v26, v58, v60 op_sel_hi:[0,1]\nv_pk_mul_f16 v27, v58, v61 op_sel_hi:[0,1]\nv_mfma_f32_16x16x32_f16 v[36:39], v[196:199], v[20:23], v[36:39]\n"
      "v_pk_mul_f16 v28, v58, v58 op_sel_hi:[0,1]\nds_read_b64_tr_b16 v[62:63], v237 offset:2240\nv_mfma_f32_16x16x32_f16 v[36:39], v[184:187], v[16:19], v[36:39]\n"
      "v_pk_mul_f16 v29, v58, v57 op_sel_hi:[0,1]\nds_read_b64_tr_b16 v[60:61], v237 offset:2112\nv_mfma_f32_16x16x32_f16 v[40:43], v[188:191], v[16:19], v[40:43]\n"
      "v_pk_mul_f16 v30, v56, v58 op_sel_hi:[0,1]\nv_mfma_f32_16x16x32_f16 v[40:43], v[200:203], v[20:23], v[40:43]\nv_pk_mul_f16 v31, v56, v59 op_sel_hi:[0,1]\n"
      "v_smfmac_f32_16x16x64_f16 v[44:47], v[204:207], v[16:23], v242\nds_read_b64_tr_b16 v[56:57], v236 offset:1856\nds_read_b64_tr_b16 v[58:59], v237 offset:1984\n"
      "v_smfmac_f32_16x16x64_f16 v[44:47], v[232:235], v[24:31], v242\nv_pk_mul_f16 v16, v68, s48 op_sel:[0,1]\nv_pk_mul_f16 v17, v69, s48 op_sel:[0,1]\n"
      "v_mfma_f32_16x16x32_f16 v[32:35], v[208:211], v[24:27], v[32:35]\nv_pk_mul_f16 v18, v70, s48 op_sel:[0,1]\nv_pk_mul_f16 v19, v71, s48 op_sel:[0,1]\n"
      "v_mfma_f32_16x16x32_f16 v[32:35], v[220:223], v[28:31], v[32:35]\nv_pk_mul_f16 v20, v68, v70\nv_pk_mul_f16 v21, v69, v71\nv_mfma_f32_16x16x32_f16 v[36:39], v[224:227], v[28:31], v[36:39]\n"
      "v_add_f32 v44, v44, v45\nv_add_f32 v46, v46, v47\nv_mfma_f32_16x16x32_f16 v[36:39], v[212:215], v[24:27], v[36:39]\nv_mul_f32 v55, v44, v54\nv_mul_f32 v55, v55, v46\n"
      "ds_read_b128 v[44:47], v248 offset:48\nv_mfma_f32_16x16x32_f16 v[40:43], v[216:219], v[24:27], v[40:43]\ns_waitcnt lgkmcnt(1)\nv_pk_mul_f16 v22, v56, v56 op_sel_hi:[0,1]\n"
      "v_pk_mul_f16 v23, v56, v57 op_sel_hi:[0,1]\nv_mfma_f32_16x16x32_f16 v[40:43], v[228:231], v[28:31], v[40:43]\nv_mul_f32 v32, v32, v48\nv_mul_f32 v33, v33, v49\nv_mul_f32 v34, v34, v50\n"
      ".Lc_u1:\nv_mfma_f32_16x16x32_f16 v[244:247], v[76:79], v[16:19], 0\nv_fmac_f32 v55, v32, v36\nv_mul_f32 v35, v35, v51\nv_fmac_f32 v55, v33, v37\ns_setprio 0\n"
      "v_mfma_f32_16x16x32_f16 v[244:247], v[88:91], v[20:23], v[244:247]\nv_fmac_f32 v55, v34, v38\nv_fmac_f32 v55, v35, v39\ns_add_u32 s45, s45, 2\ns_cmp_ge_u32 s45, s46\ns_cbranch_scc1 .Lc_t1\n"
      ".Lc_b1:\nv_mfma_f32_16x16x32_f16 v[36:39], v[84:87], v[20:23], 0\nv_pk_mul_f16 v24, v66, v66 op_sel_hi:[0,1]\nv_pk_mul_f16 v25, v66, v57 op_sel_hi:[0,1]\n"
      "v_mfma_f32_16x16x32_f16 v[36:39], v[72:75], v[16:19], v[36:39]\nv_pk_mul_f16 v26, v56, v66 op_sel_hi:[0,1]\nv_pk_mul_f16 v27, v56, v67 op_sel_hi:[0,1]\n"
      "v_mfma_f32_16x16x32_f16 v[32:35], v[80:83], v[20:23], 0\nv_pk_mul_f16 v28, v58, v66 op_sel_hi:[0,1]\nv_pk_mul_f16 v29, v58, v67 op_sel_hi:[0,1]\ns_setprio 1\ns_waitcnt lgkmcnt(0)\n"
      "v_smfmac_f32_16x16x64_f16 v[44:47], v[92:95], v[16:23], v242\nv_pk_mul_f16 v30, v60, v66 op_sel_hi:[0,1]\nv_pk_mul_f16 v31, v60, v67 op_sel_hi:[0,1]\n"
      "v_mfma_f32_16x16x32_f16 v[32:35], v[96:99], v[24:27], v[32:35]\nv_pk_mul_f16 v16, v62, v66 op_sel_hi:[0,1]\nv_pk_mul_f16 v17, v62, v67 op_sel_hi:[0,1]\n"
      "v_mfma_f32_16x16x32_f16 v[32:35], v[108:111], v[28:31], v[32:35]\nv_pk_mul_f16 v18, v64, v66 op_sel_hi:[0,1]\nv_mul_f32 v40, v40, v52\n"
      "v_mfma_f32_16x16x32_f16 v[36:39], v[112:115], v[28:31], v[36:39]\nv_pk_mul_f16 v19, v64, v67 op_sel_hi:[0,1]\nv_mul_f32 v41, v41, v53\n"
      "v_mfma_f32_16x16x32_f16 v[36:39], v[100:103], v[24:27], v[36:39]\nv_pk_mul_f16 v20, v64, v64 op_sel_hi:[0,1]\nv_fmac_f32 v55, v40, v42\n"
      "v_mfma_f32_16x16x32_f16 v[244:247], v[104:107], v[24:27], v[244:247]\nv_pk_mul_f16 v21, v64, v57 op_sel_hi:[0,1]\nv_fmac_f32 v55, v41, v43\n"
      "v_mfma_f32_16x16x32_f16 v[244:247], v[116:119], v[28:31], v[244:247]\nv_pk_mul_f16 v22, v56, v64 op_sel_hi:[0,1]\nv_smfmac_f32_16x16x64_f16 v[44:47], v[120:123], v[24:31], v242\n"
      "v_pk_mul_f16 v23, v56, v65 op_sel_hi:[0,1]\ns_setprio 2\nv_mfma_f32_16x16x32_f16 v[32:35], v[124:127], v[16:19], v[32:35]\nv_pk_mul_f16 v24, v58, v64 op_sel_hi:[0,1]\n"
      "v_pk_mul_f16 v25, v58, v65 op_sel_hi:[0,1]\nds_read_b64_tr_b16 v[70:71], v239 offset:3712\nv_mfma_f32_16x16x32_f16 v[32:35], v[136:139], v[20:23], v[32:35]\n"
      "v_pk_mul_f16 v26, v60, v64 op_sel_hi:[0,1]\nds_read_b64_tr_b16 v[68:69], v238 offset:3712\nv_mfma_f32_16x16x32_f16 v[36:39], v[140:143], v[20:23], v[36:39]\n"
      "v_pk_mul_f16 v27, v60, v65 op_sel_hi:[0,1]\nds_read_b64_tr_b16 v[66:67], v237 offset:4352\nv_mfma_f32_16x16x32_f16 v[36:39], v[128:131], v[16:19], v[36:39]\n"
      "v_pk_mul_f16 v28, v62, v64 op_sel_hi:[0,1]\nv_add_f32_dpp v55, v55, v55 quad_perm:[1,0,3,2] row_mask:0xf bank_mask:0xf\nv_mfma_f32_16x16x32_f16 v[244:247], v[132:135], v[16:19], v[244:247]\n"
      "v_pk_mul_f16 v29, v62, v65 op_sel_hi:[0,1]\nv_add_f32_dpp v55, v55, v55 quad_perm:[2,3,0,1] row_mask:0xf bank_mask:0xf\nv_mfma_f32_16x16x32_f16 v[244:247], v[144:147], v[20:23], v[244:247]\n"
      "v_pk_mul_f16 v30, v62, v62 op_sel_hi:[0,1]\nv_add_f32_dpp v55, v55, v55 row_half_mirror row_mask:0xf bank_mask:0xf\nv_smfmac_f32_16x16x64_f16 v[44:47], v[148:151], v[16:23], v242\n"
      "v_pk_mul_f16 v31, v62, v57 op_sel_hi:[0,1]\nv_add_f32_dpp v55, v55, v55 row_mirror row_mask:0xf bank_mask:0xf\nv_mfma_f32_16x16x32_f16 v[32:35], v[152:155], v[24:27], v[32:35]\n"
      "v_pk_mul_f16 v16, v56, v62 op_sel_hi:[0,1]\nv_pk_mul_f16 v17, v56, v63 op_sel_hi:[0,1]\nds_write_b32 v243, v55 offset:0\nv_mfma_f32_16x16x32_f16 v[32:35], v[164:167], v[28:31], v[32:35]\n"
      "v_pk_mul_f16 v18, v58, v62 op_sel_hi:[0,1]\nds_read_b64_tr_b16 v[64:65], v237 offset:4224\nv_mfma_f32_16x16x32_f16 v[36:39], v[168:171], v[28:31], v[36:39]\n"
      "v_pk_mul_f16 v19, v58, v63 op_sel_hi:[0,1]\ns_setprio 3\nv_mfma_f32_16x16x32_f16 v[36:39], v[156:159], v[24:27], v[36:39]\nv_pk_mul_f16 v20, v60, v62 op_sel_hi:[0,1]\n"
      "v_mfma_f32_16x16x32_f16 v[244:247], v[160:163], v[24:27], v[244:247]\nv_pk_mul_f16 v21, v60, v63 op_sel_hi:[0,1]\ns_waitcnt lgkmcnt(1)\nv_readlane_b32 s48, v69, 48\n"
      "v_mfma_f32_16x16x32_f16 v[244:247], v[172:175], v[28:31], v[244:247]\nv_pk_mul_f16 v22, v60, v60 op_sel_hi:[0,1]\nv_smfmac_f32_16x16x64_f16 v[44:47], v[176:179], v[24:31], v242\n"
      "v_pk_mul_f16 v23, v60, v57 op_sel_hi:[0,1]\nv_mfma_f32_16x16x32_f16 v[32:35], v[180:183], v[16:19], v[32:35]\nv_pk_mul_f16 v24, v56, v60 op_sel_hi:[0,1]\n"
      "v_pk_mul_f16 v25, v56, v61 op_sel_hi:[0,1]\nv_mfma_f32_16x16x32_f16 v[32:35], v[192:195], v[20:23], v[32:35]\nv_pk_mul_f16 v26, v58, v60 op_sel_hi:[0,1]\n"
      "v_pk_mul_f16 v27, v58, v61 op_sel_hi:[0,1]\nv_mfma_f32_16x16x32_f16 v[36:39], v[196:199], v[20:23], v[36:39]\nv_pk_mul_f16 v28, v58, v58 op_sel_hi:[0,1]\n"
      "ds_read_b64_tr_b16 v[62:63], v237 offset:4096\nv_mfma_f32_16x16x32_f16 v[36:39], v[184:187], v[16:19], v[36:39]\nv_pk_mul_f16 v29, v58, v57 op_sel_hi:[0,1]\n"
      "ds_read_b64_tr_b16 v[60:61], v237 offset:3968\nv_mfma_f32_16x16x32_f16 v[244:247], v[188:191], v[16:19], v[244:247]\nv_pk_mul_f16 v30, v56, v58 op_sel_hi:[0,1]\n"
      "v_mfma_f32_16x16x32_f16 v[244:247], v[200:203], v[20:23], v[244:247]\nv_pk_mul_f16 v31, v56, v59 op_sel_hi:[0,1]\nv_smfmac_f32_16x16x64_f16 v[44:47], v[204:207], v[16:23], v242\n"
      "ds_read_b64_tr_b16 v[56:57], v236 offset:3712\nds_read_b64_tr_b16 v[58:59], v237 offset:3840\nv_smfmac_f32_16x16x64_f16 v[44:47], v[232:235], v[24:31], v242\n"
      "v_pk_mul_f16 v16, v68, s48 op_sel:[0,1]\nv_pk_mul_f16 v17, v69, s48 op_sel:[0,1]\nv_mfma_f32_16x16x32_f16 v[32:35], v[208:211], v[24:27], v[32:35]\nv_pk_mul_f16 v18, v70, s48 op_sel:[0,1]\n"
      "v_pk_mul_f16 v19, v71, s48 op_sel:[0,1]\nv_mfma_f32_16x16x32_f16 v[32:35], v[220:223], v[28:31], v[32:35]\nv_pk_mul_f16 v20, v68, v70\nv_pk_mul_f16 v21, v69, v71\n"
      "v_mfma_f32_16x16x32_f16 v[36:39], v[224:227], v[28:31], v[36:39]\nv_add_f32 v44, v44, v45\nv_add_f32 v46, v46, v47\nv_mfma_f32_16x16x32_f16 v[36:39], v[212:215], v[24:27], v[36:39]\n"
      "v_mul_f32 v55, v44, v54\nv_mul_f32 v55, v55, v46\nds_read_b128 v[44:47], v248 offset:48\nv_mfma_f32_16x16x32_f16 v[244:247], v[216:219], v[24:27], v[244:247]\ns_waitcnt lgkmcnt(1)\n"
      "v_pk_mul_f16 v22, v56, v56 op_sel_hi:[0,1]\nv_pk_mul_f16 v23, v56, v57 op_sel_hi:[0,1]\nv_mfma_f32_16x16x32_f16 v[244:247], v[228:231], v[28:31], v[244:247]\nv_mul_f32 v32, v32, v48\n"
      "v_mul_f32 v33, v33, v49\nv_mul_f32 v34, v34, v50\nv_mfma_f32_16x16x32_f16 v[40:43], v[76:79], v[16:19], 0\nds_read_b32 v4, v248\nv_fmac_f32 v55, v32, v36\nv_mul_f32 v35, v35, v51\n"
      "v_fmac_f32 v55, v33, v37\ns_setprio 0\nv_mfma_f32_16x16x32_f16 v[40:43], v[88:91], v[20:23], v[40:43]\nv_fmac_f32 v55, v34, v38\nv_fmac_f32 v55, v35, v39\ns_add_u32 s45, s45, 2\n"
      "s_cmp_ge_u32 s45, s46\ns_cbranch_scc1 .Lc_t2\n"
      ".Lc_b2:\nv_mfma_f32_16x16x32_f16 v[36:39], v[84:87], v[20:23], 0\nv_pk_mul_f16 v24, v66, v66 op_sel_hi:[0,1]\nv_pk_mul_f16 v25, v66, v57 op_sel_hi:[0,1]\n"
      "v_mfma_f32_16x16x32_f16 v[36:39], v[72:75], v[16:19], v[36:39]\nv_pk_mul_f16 v26, v56, v66 op_sel_hi:[0,1]\nv_pk_mul_f16 v27, v56, v67 op_sel_hi:[0,1]\n"
      "v_mfma_f32_16x16x32_f16 v[32:35], v[80:83], v[20:23], 0\nv_pk_mul_f16 v28, v58, v66 op_sel_hi:[0,1]\nv_pk_mul_f16 v29, v58, v67 op_sel_hi:[0,1]\ns_setprio 1\ns_waitcnt lgkmcnt(0)\n"
      "v_smfmac_f32_16x16x64_f16 v[44:47], v[92:95], v[16:23], v242\nv_pk_mul_f16 v30, v60, v66 op_sel_hi:[0,1]\nv_pk_mul_f16 v31, v60, v67 op_sel_hi:[0,1]\n"
      "v_mfma_f32_16x16x32_f16 v[32:35], v[96:99], v[24:27], v[32:35]\nv_pk_mul_f16 v16, v62, v66 op_sel_hi:[0,1]\nv_pk_mul_f16 v17, v62, v67 op_sel_hi:[0,1]\n"
      "v_mfma_f32_16x16x32_f16 v[32:35], v[108:111], v[28:31], v[32:35]\nv_pk_mul_f16 v18, v64, v66 op_sel_hi:[0,1]\nv_mul_f32 v244, v244, v52\n"
      "v_mfma_f32_16x16x32_f16 v[36:39], v[112:115], v[28:31], v[36:39]\nv_pk_mul_f16 v19, v64, v67 op_sel_hi:[0,1]\nv_mul_f32 v245, v245, v53\n"
      "v_mfma_f32_16x16x32_f16 v[36:39], v[100:103], v[24:27], v[36:39]\nv_pk_mul_f16 v20, v64, v64 op_sel_hi:[0,1]\nv_fmac_f32 v55, v244, v246\n"
      "v_mfma_f32_16x16x32_f16 v[40:43], v[104:107], v[24:27], v[40:43]\nv_pk_mul_f16 v21, v64, v57 op_sel_hi:[0,1]\nv_fmac_f32 v55, v245, v247\n"
      "v_mfma_f32_16x16x32_f16 v[40:43], v[116:119], v[28:31], v[40:43]\nv_pk_mul_f16 v22, v56, v64 op_sel_hi:[0,1]\nv_readfirstlane_b32 s44, v4\n"
      "v_smfmac_f32_16x16x64_f16 v[44:47], v[120:123], v[24:31], v242\nv_pk_mul_f16 v23, v56, v65 op_sel_hi:[0,1]\ns_setprio 2\nv_mfma_f32_16x16x32_f16 v[32:35], v[124:127], v[16:19], v[32:35]\n"
      "v_pk_mul_f16 v24, v58, v64 op_sel_hi:[0,1]\nv_pk_mul_f16 v25, v58, v65 op_sel_hi:[0,1]\nds_read_b64_tr_b16 v[70:71], v239 offset:5568\n"
      "v_mfma_f32_16x16x32_f16 v[32:35], v[136:139], v[20:23], v[32:35]\nv_pk_mul_f16 v26, v60, v64 op_sel_hi:[0,1]\nds_read_b64_tr_b16 v[68:69], v238 offset:5568\n"
      "v_mfma_f32_16x16x32_f16 v[36:39], v[140:143], v[20:23], v[36:39]\nv_pk_mul_f16 v27, v60, v65 op_sel_hi:[0,1]\nds_read_b64_tr_b16 v[66:67], v237 offset:6208\n"
      "v_mfma_f32_16x16x32_f16 v[36:39], v[128:131], v[16:19], v[36:39]\nv_pk_mul_f16 v28, v62, v64 op_sel_hi:[0,1]\nv_add_f32_dpp v55, v55, v55 quad_perm:[1,0,3,2] row_mask:0xf bank_mask:0xf\n"
      "v_mfma_f32_16x16x32_f16 v[40:43], v[132:135], v[16:19], v[40:43]\nv_pk_mul_f16 v29, v62, v65 op_sel_hi:[0,1]\nv_add_f32_dpp v55, v55, v55 quad_perm:[2,3,0,1] row_mask:0xf bank_mask:0xf\n"
      "v_mfma_f32_16x16x32_f16 v[40:43], v[144:147], v[20:23], v[40:43]\nv_pk_mul_f16 v30, v62, v62 op_sel_hi:[0,1]\nv_add_f32_dpp v55, v55, v55 row_half_mirror row_mask:0xf bank_mask:0xf\n"
      "v_smfmac_f32_16x16x64_f16 v[44:47], v[148:151], v[16:23], v242\nv_pk_mul_f16 v31, v62, v57 op_sel_hi:[0,1]\nv_add_f32_dpp v55, v55, v55 row_mirror row_mask:0xf bank_mask:0xf\n"
      "v_mfma_f32_16x16x32_f16 v[32:35], v[152:155], v[24:27], v[32:35]\nv_pk_mul_f16 v16, v56, v62 op_sel_hi:[0,1]\nv_pk_mul_f16 v17, v56, v63 op_sel_hi:[0,1]\nds_write_b32 v243, v55 offset:128\n"
      "v_mfma_f32_16x16x32_f16 v[32:35], v[164:167], v[28:31], v[32:35]\nv_pk_mul_f16 v18, v58, v62 op_sel_hi:[0,1]\nds_read_b64_tr_b16 v[64:65], v237 offset:6080\n"
      "v_mfma_f32_16x16x32_f16 v[36:39], v[168:171], v[28:31], v[36:39]\nv_pk_mul_f16 v19, v58, v63 op_sel_hi:[0,1]\ns_setprio 3\nv_mfma_f32_16x16x32_f16 v[36:39], v[156:159], v[24:27], v[36:39]\n"
      "v_pk_mul_f16 v20, v60, v62 op_sel_hi:[0,1]\ns_ff0_i32_b32 s53, s44\nv_mfma_f32_16x16x32_f16 v[40:43], v[160:163], v[24:27], v[40:43]\nv_pk_mul_f16 v21, v60, v63 op_sel_hi:[0,1]\n"
      "s_waitcnt lgkmcnt(1)\nv_readlane_b32 s48, v69, 48\nv_mfma_f32_16x16x32_f16 v[40:43], v[172:175], v[28:31], v[40:43]\nv_pk_mul_f16 v22, v60, v60 op_sel_hi:[0,1]\ns_add_u32 s53, s53, s64\n"
      "v_smfmac_f32_16x16x64_f16 v[44:47], v[176:179], v[24:31], v242\nv_pk_mul_f16 v23, v60, v57 op_sel_hi:[0,1]\nv_mfma_f32_16x16x32_f16 v[32:35], v[180:183], v[16:19], v[32:35]\n"
      "v_pk_mul_f16 v24, v56, v60 op_sel_hi:[0,1]\nv_pk_mul_f16 v25, v56, v61 op_sel_hi:[0,1]\nv_mfma_f32_16x16x32_f16 v[32:35], v[192:195], v[20:23], v[32:35]\n"
      "v_pk_mul_f16 v26, v58, v60 op_sel_hi:[0,1]\nv_pk_mul_f16 v27, v58, v61 op_sel_hi:[0,1]\nv_mfma_f32_16x16x32_f16 v[36:39], v[196:199], v[20:23], v[36:39]\n"
      "v_pk_mul_f16 v28, v58, v58 op_sel_hi:[0,1]\nds_read_b64_tr_b16 v[62:63], v237 offset:5952\nv_mfma_f32_16x16x32_f16 v[36:39], v[184:187], v[16:19], v[36:39]\n"
      "v_pk_mul_f16 v29, v58, v57 op_sel_hi:[0,1]\nds_read_b64_tr_b16 v[60:61], v237 offset:5824\nv_mfma_f32_16x16x32_f16 v[40:43], v[188:191], v[16:19], v[40:43]\n"
      "v_pk_mul_f16 v30, v56, v58 op_sel_hi:[0,1]\nv_mfma_f32_16x16x32_f16 v[40:43], v[200:203], v[20:23], v[40:43]\nv_pk_mul_f16 v31, v56, v59 op_sel_hi:[0,1]\n"
      "v_smfmac_f32_16x16x64_f16 v[44:47], v[204:207], v[16:23], v242\nds_read_b64_tr_b16 v[56:57], v236 offset:5568\nds_read_b64_tr_b16 v[58:59], v237 offset:5696\n"
      "v_smfmac_f32_16x16x64_f16 v[44:47], v[232:235], v[24:31], v242\nv_pk_mul_f16 v16, v68, s48 op_sel:[0,1]\nv_pk_mul_f16 v17, v69, s48 op_sel:[0,1]\n"
      "v_mfma_f32_16x16x32_f16 v[32:35], v[208:211], v[24:27], v[32:35]\nv_pk_mul_f16 v18, v70, s48 op_sel:[0,1]\nv_pk_mul_f16 v19, v71, s48 op_sel:[0,1]\n"
      "v_mfma_f32_16x16x32_f16 v[32:35], v[220:223], v[28:31], v[32:35]\nv_pk_mul_f16 v20, v68, v70\nv_pk_mul_f16 v21, v69, v71\nv_mfma_f32_16x16x32_f16 v[36:39], v[224:227], v[28:31], v[36:39]\n"
      "v_add_f32 v44, v44, v45\nv_add_f32 v46, v46, v47\nv_mfma_f32_16x16x32_f16 v[36:39], v[212:215], v[24:27], v[36:39]\nv_mul_f32 v55, v44, v54\nv_mul_f32 v55, v55, v46\n"
      "ds_read_b128 v[44:47], v248 offset:48\nv_mfma_f32_16x16x32_f16 v[40:43], v[216:219], v[24:27], v[40:43]\ns_waitcnt lgkmcnt(1)\nv_pk_mul_f16 v22, v56, v56 op_sel_hi:[0,1]\n"
      "v_pk_mul_f16 v23, v56, v57 op_sel_hi:[0,1]\nv_mfma_f32_16x16x32_f16 v[40:43], v[228:231], v[28:31], v[40:43]\ns_cmp_lt_u32 s45, s53\nv_mul_f32 v32, v32, v48\nv_mul_f32 v33, v33, v49\n"
      "v_mul_f32 v34, v34, v50\ns_cbranch_scc0 .Lc_s2\n"
      ".Lc_u3:\nv_mfma_f32_16x16x32_f16 v[244:247], v[76:79], v[16:19], 0\nds_read_b32 v4, v248\nv_fmac_f32 v55, v32, v36\nv_mul_f32 v35, v35, v51\nv_fmac_f32 v55, v33, v37\ns_setprio 0\n"
      "v_mfma_f32_16x16x32_f16 v[244:247], v[88:91], v[20:23], v[244:247]\nv_fmac_f32 v55, v34, v38\nv_fmac_f32 v55, v35, v39\ns_add_u32 s45, s45, 2\ns_cmp_ge_u32 s45, s46\ns_cbranch_scc1 .Lc_t3\n"
      ".Lc_b3:\nv_mfma_f32_16x16x32_f16 v[36:39], v[84:87], v[20:23], 0\nv_pk_mul_f16 v24, v66, v66 op_sel_hi:[0,1]\nv_pk_mul_f16 v25, v66, v57 op_sel_hi:[0,1]\n"
      "v_mfma_f32_16x16x32_f16 v[36:39], v[72:75], v[16:19], v[36:39]\nv_pk_mul_f16 v26, v56, v66 op_sel_hi:[0,1]\nv_pk_mul_f16 v27, v56, v67 op_sel_hi:[0,1]\n"
      "v_mfma_f32_16x16x32_f16 v[32:35], v[80:83], v[20:23], 0\nv_pk_mul_f16 v28, v58, v66 op_sel_hi:[0,1]\nv_pk_mul_f16 v29, v58, v67 op_sel_hi:[0,1]\ns_setprio 1\ns_waitcnt lgkmcnt(0)\n"
      "v_smfmac_f32_16x16x64_f16 v[44:47], v[92:95], v[16:23], v242\nv_pk_mul_f16 v30, v60, v66 op_sel_hi:[0,1]\nv_pk_mul_f16 v31, v60, v67 op_sel_hi:[0,1]\n"
      "v_mfma_f32_16x16x32_f16 v[32:35], v[96:99], v[24:27], v[32:35]\nv_pk_mul_f16 v16, v62, v66 op_sel_hi:[0,1]\nv_pk_mul_f16 v17, v62, v67 op_sel_hi:[0,1]\n"
      "v_mfma_f32_16x16x32_f16 v[32:35], v[108:111], v[28:31], v[32:35]\nv_pk_mul_f16 v18, v64, v66 op_sel_hi:[0,1]\nv_mul_f32 v40, v40, v52\n"
      "v_mfma_f32_16x16x32_f16 v[36:39], v[112:115], v[28:31], v[36:39]\nv_pk_mul_f16 v19, v64, v67 op_sel_hi:[0,1]\nv_mul_f32 v41, v41, v53\n"
      "v_mfma_f32_16x16x32_f16 v[36:39], v[100:103], v[24:27], v[36:39]\nv_pk_mul_f16 v20, v64, v64 op_sel_hi:[0,1]\nv_fmac_f32 v55, v40, v42\n"
      "v_mfma_f32_16x16x32_f16 v[244:247], v[104:107], v[24:27], v[244:247]\nv_pk_mul_f16 v21, v64, v57 op_sel_hi:[0,1]\nv_fmac_f32 v55, v41, v43\n"
      "v_mfma_f32_16x16x32_f16 v[244:247], v[116:119], v[28:31], v[244:247]\nv_pk_mul_f16 v22, v56, v64 op_sel_hi:[0,1]\nv_readfirstlane_b32 s44, v4\n"
      "v_smfmac_f32_16x16x64_f16 v[44:47], v[120:123], v[24:31], v242\nv_pk_mul_f16 v23, v56, v65 op_sel_hi:[0,1]\ns_setprio 2\nv_mfma_f32_16x16x32_f16 v[32:35], v[124:127], v[16:19], v[32:35]\n"
      "v_pk_mul_f16 v24, v58, v64 op_sel_hi:[0,1]\nv_pk_mul_f16 v25, v58, v65 op_sel_hi:[0,1]\nds_read_b64_tr_b16 v[70:71], v239 offset:7424\n"
      "v_mfma_f32_16x16x32_f16 v[32:35], v[136:139], v[20:23], v[32:35]\nv_pk_mul_f16 v26, v60, v64 op_sel_hi:[0,1]\nds_read_b64_tr_b16 v[68:69], v238 offset:7424\n"
      "v_mfma_f32_16x16x32_f16 v[36:39], v[140:143], v[20:23], v[36:39]\nv_pk_mul_f16 v27, v60, v65 op_sel_hi:[0,1]\nds_read_b64_tr_b16 v[66:67], v237 offset:8064\n"
      "v_mfma_f32_16x16x32_f16 v[36:39], v[128:131], v[16:19], v[36:39]\nv_pk_mul_f16 v28, v62, v64 op_sel_hi:[0,1]\nv_add_f32_dpp v55, v55, v55 quad_perm:[1,0,3,2] row_mask:0xf bank_mask:0xf\n"
      "v_mfma_f32_16x16x32_f16 v[244:247], v[132:135], v[16:19], v[244:247]\nv_pk_mul_f16 v29, v62, v65 op_sel_hi:[0,1]\nv_add_f32_dpp v55, v55, v55 quad_perm:[2,3,0,1] row_mask:0xf bank_mask:0xf\n"
      "v_mfma_f32_16x16x32_f16 v[244:247], v[144:147], v[20:23], v[244:247]\nv_pk_mul_f16 v30, v62, v62 op_sel_hi:[0,1]\nv_add_f32_dpp v55, v55, v55 row_half_mirror row_mask:0xf bank_mask:0xf\n"
      "v_smfmac_f32_16x16x64_f16 v[44:47], v[148:151], v[16:23], v242\nv_pk_mul_f16 v31, v62, v57 op_sel_hi:[0,1]\nv_add_f32_dpp v55, v55, v55 row_mirror row_mask:0xf bank_mask:0xf\n"
      "v_mfma_f32_16x16x32_f16 v[32:35], v[152:155], v[24:27], v[32:35]\nv_pk_mul_f16 v16, v56, v62 op_sel_hi:[0,1]\nv_pk_mul_f16 v17, v56, v63 op_sel_hi:[0,1]\nds_write_b32 v243, v55 offset:256\n"
      "v_mfma_f32_16x16x32_f16 v[32:35], v[164:167], v[28:31], v[32:35]\nv_pk_mul_f16 v18, v58, v62 op_sel_hi:[0,1]\nds_read_b64_tr_b16 v[64:65], v237 offset:7936\n"
      "v_mfma_f32_16x16x32_f16 v[36:39], v[168:171], v[28:31], v[36:39]\nv_pk_mul_f16 v19, v58, v63 op_sel_hi:[0,1]\ns_setprio 3\nv_mfma_f32_16x16x32_f16 v[36:39], v[156:159], v[24:27], v[36:39]\n"
      "v_pk_mul_f16 v20, v60, v62 op_sel_hi:[0,1]\ns_ff0_i32_b32 s53, s44\nv_mfma_f32_16x16x32_f16 v[244:247], v[160:163], v[24:27], v[244:247]\nv_pk_mul_f16 v21, v60, v63 op_sel_hi:[0,1]\n"
      "s_waitcnt lgkmcnt(1)\nv_readlane_b32 s48, v69, 48\nv_mfma_f32_16x16x32_f16 v[244:247], v[172:175], v[28:31], v[244:247]\nv_pk_mul_f16 v22, v60, v60 op_sel_hi:[0,1]\ns_add_u32 s53, s53, s64\n"
      "v_smfmac_f32_16x16x64_f16 v[44:47], v[176:179], v[24:31], v242\nv_pk_mul_f16 v23, v60, v57 op_sel_hi:[0,1]\nv_mfma_f32_16x16x32_f16 v[32:35], v[180:183], v[16:19], v[32:35]\n"
      "v_pk_mul_f16 v24, v56, v60 op_sel_hi:[0,1]\nv_pk_mul_f16 v25, v56, v61 op_sel_hi:[0,1]\nv_mfma_f32_16x16x32_f16 v[32:35], v[192:195], v[20:23], v[32:35]\n"
      "v_pk_mul_f16 v26, v58, v60 op_sel_hi:[0,1]\nv_pk_mul_f16 v27, v58, v61 op_sel_hi:[0,1]\nv_mfma_f32_16x16x32_f16 v[36:39], v[196:199], v[20:23], v[36:39]\n"
      "v_pk_mul_f16 v28, v58, v58 op_sel_hi:[0,1]\nds_read_b64_tr_b16 v[62:63], v237 offset:7808\nv_mfma_f32_16x16x32_f16 v[36:39], v[184:187], v[16:19], v[36:39]\n"
      "v_pk_mul_f16 v29, v58, v57 op_sel_hi:[0,1]\nds_read_b64_tr_b16 v[60:61], v237 offset:7680\nv_mfma_f32_16x16x32_f16 v[244:247], v[188:191], v[16:19], v[244:247]\n"
      "v_pk_mul_f16 v30, v56, v58 op_sel_hi:[0,1]\nv_mfma_f32_16x16x32_f16 v[244:247], v[200:203], v[20:23], v[244:247]\nv_pk_mul_f16 v31, v56, v59 op_sel_hi:[0,1]\n"
      "v_smfmac_f32_16x16x64_f16 v[44:47], v[204:207], v[16:23], v242\nds_read_b64_tr_b16 v[56:57], v236 offset:7424\nds_read_b64_tr_b16 v[58:59], v237 offset:7552\n"
      "v_smfmac_f32_16x16x64_f16 v[44:47], v[232:235], v[24:31], v242\nv_pk_mul_f16 v16, v68, s48 op_sel:[0,1]\nv_pk_mul_f16 v17, v69, s48 op_sel:[0,1]\n"
      "v_mfma_f32_16x16x32_f16 v[32:35], v[208:211], v[24:27], v[32:35]\nv_pk_mul_f16 v18, v70, s48 op_sel:[0,1]\nv_pk_mul_f16 v19, v71, s48 op_sel:[0,1]\n"
      "v_mfma_f32_16x16x32_f16 v[32:35], v[220:223], v[28:31], v[32:35]\nv_pk_mul_f16 v20, v68, v70\nv_pk_mul_f16 v21, v69, v71\nv_mfma_f32_16x16x32_f16 v[36:39], v[224:227], v[28:31], v[36:39]\n"
      "v_add_f32 v44, v44, v45\nv_add_f32 v46, v46, v47\nv_mfma_f32_16x16x32_f16 v[36:39], v[212:215], v[24:27], v[36:39]\nv_mul_f32 v55, v44, v54\nv_mul_f32 v55, v55, v46\n"
      "ds_read_b128 v[44:47], v248 offset:48\nv_mfma_f32_16x16x32_f16 v[244:247], v[216:219], v[24:27], v[244:247]\ns_waitcnt lgkmcnt(1)\nv_pk_mul_f16 v22, v56, v56 op_sel_hi:[0,1]\n"
      "v_pk_mul_f16 v23, v56, v57 op_sel_hi:[0,1]\nv_mfma_f32_16x16x32_f16 v[244:247], v[228:231], v[28:31], v[244:247]\ns_cmp_lt_u32 s45, s53\nv_mul_f32 v32, v32, v48\nv_mul_f32 v33, v33, v49\n"
      "v_mul_f32 v34, v34, v50\ns_cbranch_scc0 .Lc_s3\n"
      ".Lc_u4:\nv_mfma_f32_16x16x32_f16 v[40:43], v[76:79], v[16:19], 0\nds_read_b32 v4, v248\nv_fmac_f32 v55, v32, v36\nv_mul_f32 v35, v35, v51\nv_fmac_f32 v55, v33, v37\ns_setprio 0\n"
      "v_mfma_f32_16x16x32_f16 v[40:43], v[88:91], v[20:23], v[40:43]\nv_fmac_f32 v55, v34, v38\nv_fmac_f32 v55, v35, v39\ns_add_u32 s45, s45, 2\ns_cmp_ge_u32 s45, s46\ns_cbranch_scc1 .Lc_t4\n"
      ".Lc_b4:\nv_mfma_f32_16x16x32_f16 v[36:39], v[84:87], v[20:23], 0\nv_pk_mul_f16 v24, v66, v66 op_sel_hi:[0,1]\nv_pk_mul_f16 v25, v66, v57 op_sel_hi:[0,1]\n"
      "v_mfma_f32_16x16x32_f16 v[36:39], v[72:75], v[16:19], v[36:39]\nv_pk_mul_f16 v26, v56, v66 op_sel_hi:[0,1]\nv_pk_mul_f16 v27, v56, v67 op_sel_hi:[0,1]\n"
      "v_mfma_f32_16x16x32_f16 v[32:35], v[80:83], v[20:23], 0\nv_pk_mul_f16 v28, v58, v66 op_sel_hi:[0,1]\nv_pk_mul_f16 v29, v58, v67 op_sel_hi:[0,1]\ns_setprio 1\ns_waitcnt lgkmcnt(0)\n"
      "v_smfmac_f32_16x16x64_f16 v[44:47], v[92:95], v[16:23], v242\nv_pk_mul_f16 v30, v60, v66 op_sel_hi:[0,1]\nv_pk_mul_f16 v31, v60, v67 op_sel_hi:[0,1]\n"
      "v_mfma_f32_16x16x32_f16 v[32:35], v[96:99], v[24:27], v[32:35]\nv_pk_mul_f16 v16, v62, v66 op_sel_hi:[0,1]\nv_pk_mul_f16 v17, v62, v67 op_sel_hi:[0,1]\n"
      "v_mfma_f32_16x16x32_f16 v[32:35], v[108:111], v[28:31], v[32:35]\nv_pk_mul_f16 v18, v64, v66 op_sel_hi:[0,1]\nv_mul_f32 v244, v244, v52\n"
      "v_mfma_f32_16x16x32_f16 v[36:39], v[112:115], v[28:31], v[36:39]\nv_pk_mul_f16 v19, v64, v67 op_sel_hi:[0,1]\nv_mul_f32 v245, v245, v53\n"
      "v_mfma_f32_16x16x32_f16 v[36:39], v[100:103], v[24:27], v[36:39]\nv_pk_mul_f16 v20, v64, v64 op_sel_hi:[0,1]\nv_fmac_f32 v55, v244, v246\n"
      "v_mfma_f32_16x16x32_f16 v[40:43], v[104:107], v[24:27], v[40:43]\nv_pk_mul_f16 v21, v64, v57 op_sel_hi:[0,1]\nv_fmac_f32 v55, v245, v247\n"
      "v_mfma_f32_16x16x32_f16 v[40:43], v[116:119], v[28:31], v[40:43]\nv_pk_mul_f16 v22, v56, v64 op_sel_hi:[0,1]\nv_readfirstlane_b32 s44, v4\n"
      "v_smfmac_f32_16x16x64_f16 v[44:47], v[120:123], v[24:31], v242\nv_pk_mul_f16 v23, v56, v65 op_sel_hi:[0,1]\ns_setprio 2\nv_mfma_f32_16x16x32_f16 v[32:35], v[124:127], v[16:19], v[32:35]\n"
      "v_pk_mul_f16 v24, v58, v64 op_sel_hi:[0,1]\nv_pk_mul_f16 v25, v58, v65 op_sel_hi:[0,1]\nds_read_b64_tr_b16 v[70:71], v239 offset:9280\n"
      "v_mfma_f32_16x16x32_f16 v[32:35], v[136:139], v[20:23], v[32:35]\nv_pk_mul_f16 v26, v60, v64 op_sel_hi:[0,1]\nds_read_b64_tr_b16 v[68:69], v238 offset:9280\n"
      "v_mfma_f32_16x16x32_f16 v[36:39], v[140:143], v[20:23], v[36:39]\nv_pk_mul_f16 v27, v60, v65 op_sel_hi:[0,1]\nds_read_b64_tr_b16 v[66:67], v237 offset:9920\n"
      "v_mfma_f32_16x16x32_f16 v[36:39], v[128:131], v[16:19], v[36:39]\nv_pk_mul_f16 v28, v62, v64 op_sel_hi:[0,1]\nv_add_f32_dpp v55, v55, v55 quad_perm:[1,0,3,2] row_mask:0xf bank_mask:0xf\n"
      "v_mfma_f32_16x16x32_f16 v[40:43], v[132:135], v[16:19], v[40:43]\nv_pk_mul_f16 v29, v62, v65 op_sel_hi:[0,1]\nv_add_f32_dpp v55, v55, v55 quad_perm:[2,3,0,1] row_mask:0xf bank_mask:0xf\n"
      "v_mfma_f32_16x16x32_f16 v[40:43], v[144:147], v[20:23], v[40:43]\nv_pk_mul_f16 v30, v62, v62 op_sel_hi:[0,1]\nv_add_f32_dpp v55, v55, v55 row_half_mirror row_mask:0xf bank_mask:0xf\n"
      "v_smfmac_f32_16x16x64_f16 v[44:47], v[148:151], v[16:23], v242\nv_pk_mul_f16 v31, v62, v57 op_sel_hi:[0,1]\nv_add_f32_dpp v55, v55, v55 row_mirror row_mask:0xf bank_mask:0xf\n"
      "v_mfma_f32_16x16x32_f16 v[32:35], v[152:155], v[24:27], v[32:35]\nv_pk_mul_f16 v16, v56, v62 op_sel_hi:[0,1]\nv_pk_mul_f16 v17, v56, v63 op_sel_hi:[0,1]\nds_write_b32 v243, v55 offset:384\n"
      "v_mfma_f32_16x16x32_f16 v[32:35], v[164:167], v[28:31], v[32:35]\nv_pk_mul_f16 v18, v58, v62 op_sel_hi:[0,1]\nds_read_b64_tr_b16 v[64:65], v237 offset:9792\n"
      "v_mfma_f32_16x16x32_f16 v[36:39], v[168:171], v[28:31], v[36:39]\nv_pk_mul_f16 v19, v58, v63 op_sel_hi:[0,1]\ns_setprio 3\nv_mfma_f32_16x16x32_f16 v[36:39], v[156:159], v[24:27], v[36:39]\n"
      "v_pk_mul_f16 v20, v60, v62 op_sel_hi:[0,1]\ns_ff0_i32_b32 s53, s44\nv_mfma_f32_16x16x32_f16 v[40:43], v[160:163], v[24:27], v[40:43]\nv_pk_mul_f16 v21, v60, v63 op_sel_hi:[0,1]\n"
      "s_waitcnt lgkmcnt(1)\nv_readlane_b32 s48, v69, 48\nv_mfma_f32_16x16x32_f16 v[40:43], v[172:175], v[28:31], v[40:43]\nv_pk_mul_f16 v22, v60, v60 op_sel_hi:[0,1]\ns_add_u32 s53, s53, s64\n"
      "v_smfmac_f32_16x16x64_f16 v[44:47], v[176:179], v[24:31], v242\nv_pk_mul_f16 v23, v60, v57 op_sel_hi:[0,1]\nv_mfma_f32_16x16x32_f16 v[32:35], v[180:183], v[16:19], v[32:35]\n"
      "v_pk_mul_f16 v24, v56, v60 op_sel_hi:[0,1]\nv_pk_mul_f16 v25, v56, v61 op_sel_hi:[0,1]\nv_mfma_f32_16x16x32_f16 v[32:35], v[192:195], v[20:23], v[32:35]\n"
      "v_pk_mul_f16 v26, v58, v60 op_sel_hi:[0,1]\nv_pk_mul_f16 v27, v58, v61 op_sel_hi:[0,1]\nv_mfma_f32_16x16x32_f16 v[36:39], v[196:199], v[20:23], v[36:39]\n"
      "v_pk_mul_f16 v28, v58, v58 op_sel_hi:[0,1]\nds_read_b64_tr_b16 v[62:63], v237 offset:9664\nv_mfma_f32_16x16x32_f16 v[36:39], v[184:187], v[16:19], v[36:39]\n"
      "v_pk_mul_f16 v29, v58, v57 op_sel_hi:[0,1]\nds_read_b64_tr_b16 v[60:61], v237 offset:9536\nv_mfma_f32_16x16x32_f16 v[40:43], v[188:191], v[16:19], v[40:43]\n"
      "v_pk_mul_f16 v30, v56, v58 op_sel_hi:[0,1]\nv_mfma_f32_16x16x32_f16 v[40:43], v[200:203], v[20:23], v[40:43]\nv_pk_mul_f16 v31, v56, v59 op_sel_hi:[0,1]\n"
      "v_smfmac_f32_16x16x64_f16 v[44:47], v[204:207], v[16:23], v242\nds_read_b64_tr_b16 v[56:57], v236 offset:9280\nds_read_b64_tr_b16 v[58:59], v237 offset:9408\n"
      "v_smfmac_f32_16x16x64_f16 v[44:47], v[232:235], v[24:31], v242\nv_pk_mul_f16 v16, v68, s48 op_sel:[0,1]\nv_pk_mul_f16 v17, v69, s48 op_sel:[0,1]\n"
      "v_mfma_f32_16x16x32_f16 v[32:35], v[208:211], v[24:27], v[32:35]\nv_pk_mul_f16 v18, v70, s48 op_sel:[0,1]\nv_pk_mul_f16 v19, v71, s48 op_sel:[0,1]\n"
      "v_mfma_f32_16x16x32_f16 v[32:35], v[220:223], v[28:31], v[32:35]\nv_pk_mul_f16 v20, v68, v70\nv_pk_mul_f16 v21, v69, v71\nv_mfma_f32_16x16x32_f16 v[36:39], v[224:227], v[28:31], v[36:39]\n"
      "v_add_f32 v44, v44, v45\nv_add_f32 v46, v46, v47\nv_mfma_f32_16x16x32_f16 v[36:39], v[212:215], v[24:27], v[36:39]\nv_mul_f32 v55, v44, v54\nv_mul_f32 v55, v55, v46\n"
      "ds_read_b128 v[44:47], v248 offset:48\nv_mfma_f32_16x16x32_f16 v[40:43], v[216:219], v[24:27], v[40:43]\ns_waitcnt lgkmcnt(1)\nv_pk_mul_f16 v22, v56, v56 op_sel_hi:[0,1]\n"
      "v_pk_mul_f16 v23, v56, v57 op_sel_hi:[0,1]\nv_mfma_f32_16x16x32_f16 v[40:43], v[228:231], v[28:31], v[40:43]\ns_cmp_lt_u32 s45, s53\nv_mul_f32 v32, v32, v48\nv_mul_f32 v33, v33, v49\n"
      "v_mul_f32 v34, v34, v50\ns_cbranch_scc0 .Lc_s4\n"
      ".Lc_u5:\nv_mfma_f32_16x16x32_f16 v[244:247], v[76:79], v[16:19], 0\nds_read_b32 v4, v248\nv_fmac_f32 v55, v32, v36\nv_mul_f32 v35, v35, v51\nv_fmac_f32 v55, v33, v37\ns_setprio 0\n"
      "v_mfma_f32_16x16x32_f16 v[244:247], v[88:91], v[20:23], v[244:247]\nv_fmac_f32 v55, v34, v38\nv_fmac_f32 v55, v35, v39\ns_add_u32 s45, s45, 2\ns_cmp_ge_u32 s45, s46\ns_cbranch_scc1 .Lc_t5\n"
      ".Lc_b5:\nv_mfma_f32_16x16x32_f16 v[36:39], v[84:87], v[20:23], 0\nv_pk_mul_f16 v24, v66, v66 op_sel_hi:[0,1]\nv_pk_mul_f16 v25, v66, v57 op_sel_hi:[0,1]\n"
      "v_mfma_f32_16x16x32_f16 v[36:39], v[72:75], v[16:19], v[36:39]\nv_pk_mul_f16 v26, v56, v66 op_sel_hi:[0,1]\nv_pk_mul_f16 v27, v56, v67 op_sel_hi:[0,1]\n"
      "v_mfma_f32_16x16x32_f16 v[32:35], v[80:83], v[20:23], 0\nv_pk_mul_f16 v28, v58, v66 op_sel_hi:[0,1]\nv_pk_mul_f16 v29, v58, v67 op_sel_hi:[0,1]\ns_setprio 1\ns_waitcnt lgkmcnt(0)\n"
      "v_smfmac_f32_16x16x64_f16 v[44:47], v[92:95], v[16:23], v242\nv_pk_mul_f16 v30, v60, v66 op_sel_hi:[0,1]\nv_pk_mul_f16 v31, v60, v67 op_sel_hi:[0,1]\n"
      "v_mfma_f32_16x16x32_f16 v[32:35], v[96:99], v[24:27], v[32:35]\nv_pk_mul_f16 v16, v62, v66 op_sel_hi:[0,1]\nv_pk_mul_f16 v17, v62, v67 op_sel_hi:[0,1]\n"
      "v_mfma_f32_16x16x32_f16 v[32:35], v[108:111], v[28:31], v[32:35]\nv_pk_mul_f16 v18, v64, v66 op_sel_hi:[0,1]\nv_mul_f32 v40, v40, v52\n"
      "v_mfma_f32_16x16x32_f16 v[36:39], v[112:115], v[28:31], v[36:39]\nv_pk_mul_f16 v19, v64, v67 op_sel_hi:[0,1]\nv_mul_f32 v41, v41, v53\n"
      "v_mfma_f32_16x16x32_f16 v[36:39], v[100:103], v[24:27], v[36:39]\nv_pk_mul_f16 v20, v64, v64 op_sel_hi:[0,1]\nv_fmac_f32 v55, v40, v42\n"
      "v_mfma_f32_16x16x32_f16 v[244:247], v[104:107], v[24:27], v[244:247]\nv_pk_mul_f16 v21, v64, v57 op_sel_hi:[0,1]\nv_fmac_f32 v55, v41, v43\n"
      "v_mfma_f32_16x16x32_f16 v[244:247], v[116:119], v[28:31], v[244:247]\nv_pk_mul_f16 v22, v56, v64 op_sel_hi:[0,1]\nv_readfirstlane_b32 s44, v4\n"
      "v_smfmac_f32_16x16x64_f16 v[44:47], v[120:123], v[24:31], v242\nv_pk_mul_f16 v23, v56, v65 op_sel_hi:[0,1]\ns_setprio 2\nv_mfma_f32_16x16x32_f16 v[32:35], v[124:127], v[16:19], v[32:35]\n"
      "v_pk_mul_f16 v24, v58, v64 op_sel_hi:[0,1]\nv_pk_mul_f16 v25, v58, v65 op_sel_hi:[0,1]\nds_read_b64_tr_b16 v[70:71], v239 offset:11136\n"
      "v_mfma_f32_16x16x32_f16 v[32:35], v[136:139], v[20:23], v[32:35]\nv_pk_mul_f16 v26, v60, v64 op_sel_hi:[0,1]\nds_read_b64_tr_b16 v[68:69], v238 offset:11136\n"
      "v_mfma_f32_16x16x32_f16 v[36:39], v[140:143], v[20:23], v[36:39]\nv_pk_mul_f16 v27, v60, v65 op_sel_hi:[0,1]\nds_read_b64_tr_b16 v[66:67], v237 offset:11776\n"
      "v_mfma_f32_16x16x32_f16 v[36:39], v[128:131], v[16:19], v[36:39]\nv_pk_mul_f16 v28, v62, v64 op_sel_hi:[0,1]\nv_add_f32_dpp v55, v55, v55 quad_perm:[1,0,3,2] row_mask:0xf bank_mask:0xf\n"
      "v_mfma_f32_16x16x32_f16 v[244:247], v[132:135], v[16:19], v[244:247]\nv_pk_mul_f16 v29, v62, v65 op_sel_hi:[0,1]\nv_add_f32_dpp v55, v55, v55 quad_perm:[2,3,0,1] row_mask:0xf bank_mask:0xf\n"
      "v_mfma_f32_16x16x32_f16 v[244:247], v[144:147], v[20:23], v[244:247]\nv_pk_mul_f16 v30, v62, v62 op_sel_hi:[0,1]\nv_add_f32_dpp v55, v55, v55 row_half_mirror row_mask:0xf bank_mask:0xf\n"
      "v_smfmac_f32_16x16x64_f16 v[44:47], v[148:151], v[16:23], v242\nv_pk_mul_f16 v31, v62, v57 op_sel_hi:[0,1]\nv_add_f32_dpp v55, v55, v55 row_mirror row_mask:0xf bank_mask:0xf\n"
      "v_mfma_f32_16x16x32_f16 v[32:35], v[152:155], v[24:27], v[32:35]\nv_pk_mul_f16 v16, v56, v62 op_sel_hi:[0,1]\nv_pk_mul_f16 v17, v56, v63 op_sel_hi:[0,1]\nds_write_b32 v243, v55 offset:512\n"
      "v_mfma_f32_16x16x32_f16 v[32:35], v[164:167], v[28:31], v[32:35]\nv_pk_mul_f16 v18, v58, v62 op_sel_hi:[0,1]\nds_read_b64_tr_b16 v[64:65], v237 offset:11648\n"
      "v_mfma_f32_16x16x32_f16 v[36:39], v[168:171], v[28:31], v[36:39]\nv_pk_mul_f16 v19, v58, v63 op_sel_hi:[0,1]\ns_setprio 3\nv_mfma_f32_16x16x32_f16 v[36:39], v[156:159], v[24:27], v[36:39]\n"
      "v_pk_mul_f16 v20, v60, v62 op_sel_hi:[0,1]\ns_ff0_i32_b32 s53, s44\nv_mfma_f32_16x16x32_f16 v[244:247], v[160:163], v[24:27], v[244:247]\nv_pk_mul_f16 v21, v60, v63 op_sel_hi:[0,1]\n"
      "s_waitcnt lgkmcnt(1)\nv_readlane_b32 s48, v69, 48\nv_mfma_f32_16x16x32_f16 v[244:247], v[172:175], v[28:31], v[244:247]\nv_pk_mul_f16 v22, v60, v60 op_sel_hi:[0,1]\ns_add_u32 s53, s53, s64\n"
      "v_smfmac_f32_16x16x64_f16 v[44:47], v[176:179], v[24:31], v242\nv_pk_mul_f16 v23, v60, v57 op_sel_hi:[0,1]\nv_mfma_f32_16x16x32_f16 v[32:35], v[180:183], v[16:19], v[32:35]\n"
      "v_pk_mul_f16 v24, v56, v60 op_sel_hi:[0,1]\nv_pk_mul_f16 v25, v56, v61 op_sel_hi:[0,1]\nv_mfma_f32_16x16x32_f16 v[32:35], v[192:195], v[20:23], v[32:35]\n"
      "v_pk_mul_f16 v26, v58, v60 op_sel_hi:[0,1]\nv_pk_mul_f16 v27, v58, v61 op_sel_hi:[0,1]\nv_mfma_f32_16x16x32_f16 v[36:39], v[196:199], v[20:23], v[36:39]\n"
      "v_pk_mul_f16 v28, v58, v58 op_sel_hi:[0,1]\nds_read_b64_tr_b16 v[62:63], v237 offset:11520\nv_mfma_f32_16x16x32_f16 v[36:39], v[184:187], v[16:19], v[36:39]\n"
      "v_pk_mul_f16 v29, v58, v57 op_sel_hi:[0,1]\nds_read_b64_tr_b16 v[60:61], v237 offset:11392\nv_mfma_f32_16x16x32_f16 v[244:247], v[188:191], v[16:19], v[244:247]\n"
      "v_pk_mul_f16 v30, v56, v58 op_sel_hi:[0,1]\nv_mfma_f32_16x16x32_f16 v[244:247], v[200:203], v[20:23], v[244:247]\nv_pk_mul_f16 v31, v56, v59 op_sel_hi:[0,1]\n"
      "v_smfmac_f32_16x16x64_f16 v[44:47], v[204:207], v[16:23], v242\nds_read_b64_tr_b16 v[56:57], v236 offset:11136\nds_read_b64_tr_b16 v[58:59], v237 offset:11264\n"
      "v_smfmac_f32_16x16x64_f16 v[44:47], v[232:235], v[24:31], v242\nv_pk_mul_f16 v16, v68, s48 op_sel:[0,1]\nv_pk_mul_f16 v17, v69, s48 op_sel:[0,1]\n"
      "v_mfma_f32_16x16x32_f16 v[32:35], v[208:211], v[24:27], v[32:35]\nv_pk_mul_f16 v18, v70, s48 op_sel:[0,1]\nv_pk_mul_f16 v19, v71, s48 op_sel:[0,1]\n"
      "v_mfma_f32_16x16x32_f16 v[32:35], v[220:223], v[28:31], v[32:35]\nv_pk_mul_f16 v20, v68, v70\nv_pk_mul_f16 v21, v69, v71\nv_mfma_f32_16x16x32_f16 v[36:39], v[224:227], v[28:31], v[36:39]\n"
      "v_add_f32 v44, v44, v45\nv_add_f32 v46, v46, v47\nv_mfma_f32_16x16x32_f16 v[36:39], v[212:215], v[24:27], v[36:39]\nv_mul_f32 v55, v44, v54\nv_mul_f32 v55, v55, v46\n"
      "ds_read_b128 v[44:47], v248 offset:48\nv_mfma_f32_16x16x32_f16 v[244:247], v[216:219], v[24:27], v[244:247]\ns_waitcnt lgkmcnt(1)\nv_pk_mul_f16 v22, v56, v56 op_sel_hi:[0,1]\n"
      "v_pk_mul_f16 v23, v56, v57 op_sel_hi:[0,1]\nv_mfma_f32_16x16x32_f16 v[244:247], v[228:231], v[28:31], v[244:247]\ns_cmp_lt_u32 s45, s53\nv_mul_f32 v32, v32, v48\nv_mul_f32 v33, v33, v49\n"
      "v_mul_f32 v34, v34, v50\ns_cbranch_scc0 .Lc_s5\n"
      ".Lc_u6:\nv_mfma_f32_16x16x32_f16 v[40:43], v[76:79], v[16:19], 0\nds_read_b32 v4, v248\nv_fmac_f32 v55, v32, v36\nv_mul_f32 v35, v35, v51\nv_fmac_f32 v55, v33, v37\ns_setprio 0\n"
      "v_mfma_f32_16x16x32_f16 v[40:43], v[88:91], v[20:23], v[40:43]\nv_fmac_f32 v55, v34, v38\nv_fmac_f32 v55, v35, v39\ns_add_u32 s45, s45, 2\ns_cmp_ge_u32 s45, s46\ns_cbranch_scc1 .Lc_t6\n"
      ".Lc_b6:\nv_mfma_f32_16x16x32_f16 v[36:39], v[84:87], v[20:23], 0\nv_pk_mul_f16 v24, v66, v66 op_sel_hi:[0,1]\nv_pk_mul_f16 v25, v66, v57 op_sel_hi:[0,1]\n"
      "v_mfma_f32_16x16x32_f16 v[36:39], v[72:75], v[16:19], v[36:39]\nv_pk_mul_f16 v26, v56, v66 op_sel_hi:[0,1]\nv_pk_mul_f16 v27, v56, v67 op_sel_hi:[0,1]\n"
      "v_mfma_f32_16x16x32_f16 v[32:35], v[80:83], v[20:23], 0\nv_pk_mul_f16 v28, v58, v66 op_sel_hi:[0,1]\nv_pk_mul_f16 v29, v58, v67 op_sel_hi:[0,1]\ns_setprio 1\ns_waitcnt lgkmcnt(0)\n"
      "v_smfmac_f32_16x16x64_f16 v[44:47], v[92:95], v[16:23], v242\nv_pk_mul_f16 v30, v60, v66 op_sel_hi:[0,1]\nv_pk_mul_f16 v31, v60, v67 op_sel_hi:[0,1]\n"
      "v_mfma_f32_16x16x32_f16 v[32:35], v[96:99], v[24:27], v[32:35]\nv_pk_mul_f16 v16, v62, v66 op_sel_hi:[0,1]\nv_pk_mul_f16 v17, v62, v67 op_sel_hi:[0,1]\n"
      "v_mfma_f32_16x16x32_f16 v[32:35], v[108:111], v[28:31], v[32:35]\nv_pk_mul_f16 v18, v64, v66 op_sel_hi:[0,1]\nv_mul_f32 v244, v244, v52\n"
      "v_mfma_f32_16x16x32_f16 v[36:39], v[112:115], v[28:31], v[36:39]\nv_pk_mul_f16 v19, v64, v67 op_sel_hi:[0,1]\nv_mul_f32 v245, v245, v53\n"
      "v_mfma_f32_16x16x32_f16 v[36:39], v[100:103], v[24:27], v[36:39]\nv_pk_mul_f16 v20, v64, v64 op_sel_hi:[0,1]\nv_fmac_f32 v55, v244, v246\n"
      "v_mfma_f32_16x16x32_f16 v[40:43], v[104:107], v[24:27], v[40:43]\nv_pk_mul_f16 v21, v64, v57 op_sel_hi:[0,1]\nv_fmac_f32 v55, v245, v247\n"
      "v_mfma_f32_16x16x32_f16 v[40:43], v[116:119], v[28:31], v[40:43]\nv_pk_mul_f16 v22, v56, v64 op_sel_hi:[0,1]\nv_readfirstlane_b32 s44, v4\n"
      "v_smfmac_f32_16x16x64_f16 v[44:47], v[120:123], v[24:31], v242\nv_pk_mul_f16 v23, v56, v65 op_sel_hi:[0,1]\ns_setprio 2\nv_mfma_f32_16x16x32_f16 v[32:35], v[124:127], v[16:19], v[32:35]\n"
      "v_pk_mul_f16 v24, v58, v64 op_sel_hi:[0,1]\nv_pk_mul_f16 v25, v58, v65 op_sel_hi:[0,1]\nds_read_b64_tr_b16 v[70:71], v239 offset:12992\n"
      "v_mfma_f32_16x16x32_f16 v[32:35], v[136:139], v[20:23], v[32:35]\nv_pk_mul_f16 v26, v60, v64 op_sel_hi:[0,1]\nds_read_b64_tr_b16 v[68:69], v238 offset:12992\n"
      "v_mfma_f32_16x16x32_f16 v[36:39], v[140:143], v[20:23], v[36:39]\nv_pk_mul_f16 v27, v60, v65 op_sel_hi:[0,1]\nds_read_b64_tr_b16 v[66:67], v237 offset:13632\n"
      "v_mfma_f32_16x16x32_f16 v[36:39], v[128:131], v[16:19], v[36:39]\nv_pk_mul_f16 v28, v62, v64 op_sel_hi:[0,1]\nv_add_f32_dpp v55, v55, v55 quad_perm:[1,0,3,2] row_mask:0xf bank_mask:0xf\n"
      "v_mfma_f32_16x16x32_f16 v[40:43], v[132:135], v[16:19], v[40:43]\nv_pk_mul_f16 v29, v62, v65 op_sel_hi:[0,1]\nv_add_f32_dpp v55, v55, v55 quad_perm:[2,3,0,1] row_mask:0xf bank_mask:0xf\n"
      "v_mfma_f32_16x16x32_f16 v[40:43], v[144:147], v[20:23], v[40:43]\nv_pk_mul_f16 v30, v62, v62 op_sel_hi:[0,1]\nv_add_f32_dpp v55, v55, v55 row_half_mirror row_mask:0xf bank_mask:0xf\n"
      "v_smfmac_f32_16x16x64_f16 v[44:47], v[148:151], v[16:23], v242\nv_pk_mul_f16 v31, v62, v57 op_sel_hi:[0,1]\nv_add_f32_dpp v55, v55, v55 row_mirror row_mask:0xf bank_mask:0xf\n"
      "v_mfma_f32_16x16x32_f16 v[32:35], v[152:155], v[24:27], v[32:35]\nv_pk_mul_f16 v16, v56, v62 op_sel_hi:[0,1]\nv_pk_mul_f16 v17, v56, v63 op_sel_hi:[0,1]\nds_write_b32 v243, v55 offset:640\n"
      "v_mfma_f32_16x16x32_f16 v[32:35], v[164:167], v[28:31], v[32:35]\nv_pk_mul_f16 v18, v58, v62 op_sel_hi:[0,1]\nds_read_b64_tr_b16 v[64:65], v237 offset:13504\n"
      "v_mfma_f32_16x16x32_f16 v[36:39], v[168:171], v[28:31], v[36:39]\nv_pk_mul_f16 v19, v58, v63 op_sel_hi:[0,1]\ns_setprio 3\nv_mfma_f32_16x16x32_f16 v[36:39], v[156:159], v[24:27], v[36:39]\n"
      "v_pk_mul_f16 v20, v60, v62 op_sel_hi:[0,1]\ns_ff0_i32_b32 s53, s44\nv_mfma_f32_16x16x32_f16 v[40:43], v[160:163], v[24:27], v[40:43]\nv_pk_mul_f16 v21, v60, v63 op_sel_hi:[0,1]\n"
      "s_waitcnt lgkmcnt(1)\nv_readlane_b32 s48, v69, 48\nv_mfma_f32_16x16x32_f16 v[40:43], v[172:175], v[28:31], v[40:43]\nv_pk_mul_f16 v22, v60, v60 op_sel_hi:[0,1]\ns_add_u32 s53, s53, s64\n"
      "v_smfmac_f32_16x16x64_f16 v[44:47], v[176:179], v[24:31], v242\nv_pk_mul_f16 v23, v60, v57 op_sel_hi:[0,1]\nv_mfma_f32_16x16x32_f16 v[32:35], v[180:183], v[16:19], v[32:35]\n"
      "v_pk_mul_f16 v24, v56, v60 op_sel_hi:[0,1]\nv_pk_mul_f16 v25, v56, v61 op_sel_hi:[0,1]\nv_mfma_f32_16x16x32_f16 v[32:35], v[192:195], v[20:23], v[32:35]\n"
      "v_pk_mul_f16 v26, v58, v60 op_sel_hi:[0,1]\nv_pk_mul_f16 v27, v58, v61 op_sel_hi:[0,1]\nv_mfma_f32_16x16x32_f16 v[36:39], v[196:199], v[20:23], v[36:39]\n"
      "v_pk_mul_f16 v28, v58, v58 op_sel_hi:[0,1]\nds_read_b64_tr_b16 v[62:63], v237 offset:13376\nv_mfma_f32_16x16x32_f16 v[36:39], v[184:187], v[16:19], v[36:39]\n"
      "v_pk_mul_f16 v29, v58, v57 op_sel_hi:[0,1]\nds_read_b64_tr_b16 v[60:61], v237 offset:13248\nv_mfma_f32_16x16x32_f16 v[40:43], v[188:191], v[16:19], v[40:43]\n"
      "v_pk_mul_f16 v30, v56, v58 op_sel_hi:[0,1]\nv_mfma_f32_16x16x32_f16 v[40:43], v[200:203], v[20:23], v[40:43]\nv_pk_mul_f16 v31, v56, v59 op_sel_hi:[0,1]\n"
      "v_smfmac_f32_16x16x64_f16 v[44:47], v[204:207], v[16:23], v242\nds_read_b64_tr_b16 v[56:57], v236 offset:12992\nds_read_b64_tr_b16 v[58:59], v237 offset:13120\n"
      "v_smfmac_f32_16x16x64_f16 v[44:47], v[232:235], v[24:31], v242\nv_pk_mul_f16 v16, v68, s48 op_sel:[0,1]\nv_pk_mul_f16 v17, v69, s48 op_sel:[0,1]\n"
      "v_mfma_f32_16x16x32_f16 v[32:35], v[208:211], v[24:27], v[32:35]\nv_pk_mul_f16 v18, v70, s48 op_sel:[0,1]\nv_pk_mul_f16 v19, v71, s48 op_sel:[0,1]\n"
      "v_mfma_f32_16x16x32_f16 v[32:35], v[220:223], v[28:31], v[32:35]\nv_pk_mul_f16 v20, v68, v70\nv_pk_mul_f16 v21, v69, v71\nv_mfma_f32_16x16x32_f16 v[36:39], v[224:227], v[28:31], v[36:39]\n"
      "v_add_f32 v44, v44, v45\nv_add_f32 v46, v46, v47\nv_mfma_f32_16x16x32_f16 v[36:39], v[212:215], v[24:27], v[36:39]\nv_mul_f32 v55, v44, v54\nv_mul_f32 v55, v55, v46\n"
      "ds_read_b128 v[44:47], v248 offset:48\nv_mfma_f32_16x16x32_f16 v[40:43], v[216:219], v[24:27], v[40:43]\ns_waitcnt lgkmcnt(1)\nv_pk_mul_f16 v22, v56, v56 op_sel_hi:[0,1]\n"
      "v_pk_mul_f16 v23, v56, v57 op_sel_hi:[0,1]\nv_mfma_f32_16x16x32_f16 v[40:43], v[228:231], v[28:31], v[40:43]\ns_cmp_lt_u32 s45, s53\nv_mul_f32 v32, v32, v48\nv_mul_f32 v33, v33, v49\n"
      "v_mul_f32 v34, v34, v50\ns_cbranch_scc0 .Lc_s6\n"
      ".Lc_u7:\nv_mfma_f32_16x16x32_f16 v[244:247], v[76:79], v[16:19], 0\nds_read_b32 v4, v248\nv_fmac_f32 v55, v32, v36\nv_mul_f32 v35, v35, v51\nv_fmac_f32 v55, v33, v37\ns_setprio 0\n"
      "v_mfma_f32_16x16x32_f16 v[244:247], v[88:91], v[20:23], v[244:247]\nv_fmac_f32 v55, v34, v38\nv_fmac_f32 v55, v35, v39\ns_add_u32 s45, s45, 2\ns_cmp_ge_u32 s45, s46\ns_cbranch_scc1 .Lc_t7\n"
      ".Lc_b7:\nv_mfma_f32_16x16x32_f16 v[36:39], v[84:87], v[20:23], 0\nv_pk_mul_f16 v24, v66, v66 op_sel_hi:[0,1]\nv_pk_mul_f16 v25, v66, v57 op_sel_hi:[0,1]\n"
      "v_mfma_f32_16x16x32_f16 v[36:39], v[72:75], v[16:19], v[36:39]\nv_pk_mul_f16 v26, v56, v66 op_sel_hi:[0,1]\nv_pk_mul_f16 v27, v56, v67 op_sel_hi:[0,1]\n"
      "v_mfma_f32_16x16x32_f16 v[32:35], v[80:83], v[20:23], 0\nv_pk_mul_f16 v28, v58, v66 op_sel_hi:[0,1]\nv_pk_mul_f16 v29, v58, v67 op_sel_hi:[0,1]\ns_setprio 1\ns_waitcnt lgkmcnt(0)\n"
      "v_smfmac_f32_16x16x64_f16 v[44:47], v[92:95], v[16:23], v242\nv_pk_mul_f16 v30, v60, v66 op_sel_hi:[0,1]\nv_pk_mul_f16 v31, v60, v67 op_sel_hi:[0,1]\n"
      "v_mfma_f32_16x16x32_f16 v[32:35], v[96:99], v[24:27], v[32:35]\nv_pk_mul_f16 v16, v62, v66 op_sel_hi:[0,1]\nv_pk_mul_f16 v17, v62, v67 op_sel_hi:[0,1]\n"
      "v_mfma_f32_16x16x32_f16 v[32:35], v[108:111], v[28:31], v[32:35]\nv_pk_mul_f16 v18, v64, v66 op_sel_hi:[0,1]\nv_mul_f32 v40, v40, v52\n"
      "v_mfma_f32_16x16x32_f16 v[36:39], v[112:115], v[28:31], v[36:39]\nv_pk_mul_f16 v19, v64, v67 op_sel_hi:[0,1]\nv_mul_f32 v41, v41, v53\n"
      "v_mfma_f32_16x16x32_f16 v[36:39], v[100:103], v[24:27], v[36:39]\nv_pk_mul_f16 v20, v64, v64 op_sel_hi:[0,1]\nv_fmac_f32 v55, v40, v42\n"
      "v_mfma_f32_16x16x32_f16 v[244:247], v[104:107], v[24:27], v[244:247]\nv_pk_mul_f16 v21, v64, v57 op_sel_hi:[0,1]\nv_fmac_f32 v55, v41, v43\n"
      "v_mfma_f32_16x16x32_f16 v[244:247], v[116:119], v[28:31], v[244:247]\nv_pk_mul_f16 v22, v56, v64 op_sel_hi:[0,1]\nv_readfirstlane_b32 s44, v4\n"
      "v_smfmac_f32_16x16x64_f16 v[44:47], v[120:123], v[24:31], v242\nv_pk_mul_f16 v23, v56, v65 op_sel_hi:[0,1]\ns_setprio 2\nv_mfma_f32_16x16x32_f16 v[32:35], v[124:127], v[16:19], v[32:35]\n"
      "v_pk_mul_f16 v24, v58, v64 op_sel_hi:[0,1]\nv_pk_mul_f16 v25, v58, v65 op_sel_hi:[0,1]\nds_read_b64_tr_b16 v[70:71], v239 offset:14848\n"
      "v_mfma_f32_16x16x32_f16 v[32:35], v[136:139], v[20:23], v[32:35]\nv_pk_mul_f16 v26, v60, v64 op_sel_hi:[0,1]\nds_read_b64_tr_b16 v[68:69], v238 offset:14848\n"
      "v_mfma_f32_16x16x32_f16 v[36:39], v[140:143], v[20:23], v[36:39]\nv_pk_mul_f16 v27, v60, v65 op_sel_hi:[0,1]\nds_read_b64_tr_b16 v[66:67], v237 offset:15488\n"
      "v_mfma_f32_16x16x32_f16 v[36:39], v[128:131], v[16:19], v[36:39]\nv_pk_mul_f16 v28, v62, v64 op_sel_hi:[0,1]\nv_add_f32_dpp v55, v55, v55 quad_perm:[1,0,3,2] row_mask:0xf bank_mask:0xf\n"
      "v_mfma_f32_16x16x32_f16 v[244:247], v[132:135], v[16:19], v[244:247]\nv_pk_mul_f16 v29, v62, v65 op_sel_hi:[0,1]\nv_add_f32_dpp v55, v55, v55 quad_perm:[2,3,0,1] row_mask:0xf bank_mask:0xf\n"
      "v_mfma_f32_16x16x32_f16 v[244:247], v[144:147], v[20:23], v[244:247]\nv_pk_mul_f16 v30, v62, v62 op_sel_hi:[0,1]\nv_add_f32_dpp v55, v55, v55 row_half_mirror row_mask:0xf bank_mask:0xf\n"
      "v_smfmac_f32_16x16x64_f16 v[44:47], v[148:151], v[16:23], v242\nv_pk_mul_f16 v31, v62, v57 op_sel_hi:[0,1]\nv_add_f32_dpp v55, v55, v55 row_mirror row_mask:0xf bank_mask:0xf\n"
      "v_mfma_f32_16x16x32_f16 v[32:35], v[152:155], v[24:27], v[32:35]\nv_pk_mul_f16 v16, v56, v62 op_sel_hi:[0,1]\nv_pk_mul_f16 v17, v56, v63 op_sel_hi:[0,1]\nds_write_b32 v243, v55 offset:768\n"
      "v_mfma_f32_16x16x32_f16 v[32:35], v[164:167], v[28:31], v[32:35]\nv_pk_mul_f16 v18, v58, v62 op_sel_hi:[0,1]\nds_read_b64_tr_b16 v[64:65], v237 offset:15360\n"
      "v_mfma_f32_16x16x32_f16 v[36:39], v[168:171], v[28:31], v[36:39]\nv_pk_mul_f16 v19, v58, v63 op_sel_hi:[0,1]\ns_setprio 3\nv_mfma_f32_16x16x32_f16 v[36:39], v[156:159], v[24:27], v[36:39]\n"
      "v_pk_mul_f16 v20, v60, v62 op_sel_hi:[0,1]\ns_ff0_i32_b32 s53, s44\nv_mfma_f32_16x16x32_f16 v[244:247], v[160:163], v[24:27], v[244:247]\nv_pk_mul_f16 v21, v60, v63 op_sel_hi:[0,1]\n"
      "s_waitcnt lgkmcnt(1)\nv_readlane_b32 s48, v69, 48\nv_mfma_f32_16x16x32_f16 v[244:247], v[172:175], v[28:31], v[244:247]\nv_pk_mul_f16 v22, v60, v60 op_sel_hi:[0,1]\ns_add_u32 s53, s53, s64\n"
      "v_smfmac_f32_16x16x64_f16 v[44:47], v[176:179], v[24:31], v242\nv_pk_mul_f16 v23, v60, v57 op_sel_hi:[0,1]\nv_mfma_f32_16x16x32_f16 v[32:35], v[180:183], v[16:19], v[32:35]\n"
      "v_pk_mul_f16 v24, v56, v60 op_sel_hi:[0,1]\nv_pk_mul_f16 v25, v56, v61 op_sel_hi:[0,1]\nv_mfma_f32_16x16x32_f16 v[32:35], v[192:195], v[20:23], v[32:35]\n"
      "v_pk_mul_f16 v26, v58, v60 op_sel_hi:[0,1]\nv_pk_mul_f16 v27, v58, v61 op_sel_hi:[0,1]\nv_mfma_f32_16x16x32_f16 v[36:39], v[196:199], v[20:23], v[36:39]\n"
      "v_pk_mul_f16 v28, v58, v58 op_sel_hi:[0,1]\nds_read_b64_tr_b16 v[62:63], v237 offset:15232\nv_mfma_f32_16x16x32_f16 v[36:39], v[184:187], v[16:19], v[36:39]\n"
      "v_pk_mul_f16 v29, v58, v57 op_sel_hi:[0,1]\nds_read_b64_tr_b16 v[60:61], v237 offset:15104\nv_mfma_f32_16x16x32_f16 v[244:247], v[188:191], v[16:19], v[244:247]\n"
      "v_pk_mul_f16 v30, v56, v58 op_sel_hi:[0,1]\nv_mfma_f32_16x16x32_f16 v[244:247], v[200:203], v[20:23], v[244:247]\nv_pk_mul_f16 v31, v56, v59 op_sel_hi:[0,1]\n"
      "v_smfmac_f32_16x16x64_f16 v[44:47], v[204:207], v[16:23], v242\nds_read_b64_tr_b16 v[56:57], v236 offset:14848\nds_read_b64_tr_b16 v[58:59], v237 offset:14976\n"
      "v_smfmac_f32_16x16x64_f16 v[44:47], v[232:235], v[24:31], v242\nv_pk_mul_f16 v16, v68, s48 op_sel:[0,1]\nv_pk_mul_f16 v17, v69, s48 op_sel:[0,1]\n"
      "v_mfma_f32_16x16x32_f16 v[32:35], v[208:211], v[24:27], v[32:35]\nv_pk_mul_f16 v18, v70, s48 op_sel:[0,1]\nv_pk_mul_f16 v19, v71, s48 op_sel:[0,1]\n"
      "v_mfma_f32_16x16x32_f16 v[32:35], v[220:223], v[28:31], v[32:35]\nv_pk_mul_f16 v20, v68, v70\nv_pk_mul_f16 v21, v69, v71\nv_mfma_f32_16x16x32_f16 v[36:39], v[224:227], v[28:31], v[36:39]\n"
      "v_add_f32 v44, v44, v45\nv_add_f32 v46, v46, v47\nv_mfma_f32_16x16x32_f16 v[36:39], v[212:215], v[24:27], v[36:39]\nv_mul_f32 v55, v44, v54\nv_mul_f32 v55, v55, v46\n"
      "ds_read_b128 v[44:47], v248 offset:48\nv_mfma_f32_16x16x32_f16 v[244:247], v[216:219], v[24:27], v[244:247]\ns_waitcnt lgkmcnt(1)\nv_pk_mul_f16 v22, v56, v56 op_sel_hi:[0,1]\n"
      "v_pk_mul_f16 v23, v56, v57 op_sel_hi:[0,1]\nv_mfma_f32_16x16x32_f16 v[244:247], v[228:231], v[28:31], v[244:247]\ns_cmp_lt_u32 s45, s53\nv_mul_f32 v32, v32, v48\nv_mul_f32 v33, v33, v49\n"
      "v_mul_f32 v34, v34, v50\ns_cbranch_scc0 .Lc_s7\n"
      ".Lc_u8:\nv_mfma_f32_16x16x32_f16 v[40:43], v[76:79], v[16:19], 0\nds_read_b32 v4, v248\nv_fmac_f32 v55, v32, v36\nv_mul_f32 v35, v35, v51\nv_fmac_f32 v55, v33, v37\ns_setprio 0\n"
      "v_mfma_f32_16x16x32_f16 v[40:43], v[88:91], v[20:23], v[40:43]\nv_fmac_f32 v55, v34, v38\nv_fmac_f32 v55, v35, v39\ns_add_u32 s45, s45, 2\ns_cmp_ge_u32 s45, s46\ns_cbranch_scc1 .Lc_t8\n"
      ".Lc_b8:\nv_mfma_f32_16x16x32_f16 v[36:39], v[84:87], v[20:23], 0\nv_pk_mul_f16 v24, v66, v66 op_sel_hi:[0,1]\nv_pk_mul_f16 v25, v66, v57 op_sel_hi:[0,1]\n"
      "v_mfma_f32_16x16x32_f16 v[36:39], v[72:75], v[16:19], v[36:39]\nv_pk_mul_f16 v26, v56, v66 op_sel_hi:[0,1]\nv_pk_mul_f16 v27, v56, v67 op_sel_hi:[0,1]\n"
      "v_mfma_f32_16x16x32_f16 v[32:35], v[80:83], v[20:23], 0\nv_pk_mul_f16 v28, v58, v66 op_sel_hi:[0,1]\nv_pk_mul_f16 v29, v58, v67 op_sel_hi:[0,1]\ns_setprio 1\ns_waitcnt lgkmcnt(0)\n"
      "v_smfmac_f32_16x16x64_f16 v[44:47], v[92:95], v[16:23], v242\nv_pk_mul_f16 v30, v60, v66 op_sel_hi:[0,1]\nv_pk_mul_f16 v31, v60, v67 op_sel_hi:[0,1]\n"
      "v_mfma_f32_16x16x32_f16 v[32:35], v[96:99], v[24:27], v[32:35]\nv_pk_mul_f16 v16, v62, v66 op_sel_hi:[0,1]\nv_pk_mul_f16 v17, v62, v67 op_sel_hi:[0,1]\n"
      "v_mfma_f32_16x16x32_f16 v[32:35], v[108:111], v[28:31], v[32:35]\nv_pk_mul_f16 v18, v64, v66 op_sel_hi:[0,1]\nv_mul_f32 v244, v244, v52\n"
      "v_mfma_f32_16x16x32_f16 v[36:39], v[112:115], v[28:31], v[36:39]\nv_pk_mul_f16 v19, v64, v67 op_sel_hi:[0,1]\nv_mul_f32 v245, v245, v53\n"
      "v_mfma_f32_16x16x32_f16 v[36:39], v[100:103], v[24:27], v[36:39]\nv_pk_mul_f16 v20, v64, v64 op_sel_hi:[0,1]\nv_fmac_f32 v55, v244, v246\n"
      "v_mfma_f32_16x16x32_f16 v[40:43], v[104:107], v[24:27], v[40:43]\nv_pk_mul_f16 v21, v64, v57 op_sel_hi:[0,1]\nv_fmac_f32 v55, v245, v247\n"
      "v_mfma_f32_16x16x32_f16 v[40:43], v[116:119], v[28:31], v[40:43]\nv_pk_mul_f16 v22, v56, v64 op_sel_hi:[0,1]\nv_readfirstlane_b32 s44, v4\n"
      "v_smfmac_f32_16x16x64_f16 v[44:47], v[120:123], v[24:31], v242\nv_pk_mul_f16 v23, v56, v65 op_sel_hi:[0,1]\ns_setprio 2\nv_mfma_f32_16x16x32_f16 v[32:35], v[124:127], v[16:19], v[32:35]\n"
      "v_pk_mul_f16 v24, v58, v64 op_sel_hi:[0,1]\nv_pk_mul_f16 v25, v58, v65 op_sel_hi:[0,1]\nds_read_b64_tr_b16 v[70:71], v239 offset:16704\n"
      "v_mfma_f32_16x16x32_f16 v[32:35], v[136:139], v[20:23], v[32:35]\nv_pk_mul_f16 v26, v60, v64 op_sel_hi:[0,1]\nds_read_b64_tr_b16 v[68:69], v238 offset:16704\n"
      "v_mfma_f32_16x16x32_f16 v[36:39], v[140:143], v[20:23], v[36:39]\nv_pk_mul_f16 v27, v60, v65 op_sel_hi:[0,1]\nds_read_b64_tr_b16 v[66:67], v237 offset:17344\n"
      "v_mfma_f32_16x16x32_f16 v[36:39], v[128:131], v[16:19], v[36:39]\nv_pk_mul_f16 v28, v62, v64 op_sel_hi:[0,1]\nv_add_f32_dpp v55, v55, v55 quad_perm:[1,0,3,2] row_mask:0xf bank_mask:0xf\n"
      "v_mfma_f32_16x16x32_f16 v[40:43], v[132:135], v[16:19], v[40:43]\nv_pk_mul_f16 v29, v62, v65 op_sel_hi:[0,1]\nv_add_f32_dpp v55, v55, v55 quad_perm:[2,3,0,1] row_mask:0xf bank_mask:0xf\n"
      "v_mfma_f32_16x16x32_f16 v[40:43], v[144:147], v[20:23], v[40:43]\nv_pk_mul_f16 v30, v62, v62 op_sel_hi:[0,1]\nv_add_f32_dpp v55, v55, v55 row_half_mirror row_mask:0xf bank_mask:0xf\n"
      "v_smfmac_f32_16x16x64_f16 v[44:47], v[148:151], v[16:23], v242\nv_pk_mul_f16 v31, v62, v57 op_sel_hi:[0,1]\nv_add_f32_dpp v55, v55, v55 row_mirror row_mask:0xf bank_mask:0xf\n"
      "v_mfma_f32_16x16x32_f16 v[32:35], v[152:155], v[24:27], v[32:35]\nv_pk_mul_f16 v16, v56, v62 op_sel_hi:[0,1]\nv_pk_mul_f16 v17, v56, v63 op_sel_hi:[0,1]\nds_write_b32 v243, v55 offset:896\n"
      "v_mfma_f32_16x16x32_f16 v[32:35], v[164:167], v[28:31], v[32:35]\nv_pk_mul_f16 v18, v58, v62 op_sel_hi:[0,1]\nds_read_b64_tr_b16 v[64:65], v237 offset:17216\n"
      "v_mfma_f32_16x16x32_f16 v[36:39], v[168:171], v[28:31], v[36:39]\nv_pk_mul_f16 v19, v58, v63 op_sel_hi:[0,1]\ns_setprio 3\nv_mfma_f32_16x16x32_f16 v[36:39], v[156:159], v[24:27], v[36:39]\n"
      "v_pk_mul_f16 v20, v60, v62 op_sel_hi:[0,1]\ns_ff0_i32_b32 s53, s44\nv_mfma_f32_16x16x32_f16 v[40:43], v[160:163], v[24:27], v[40:43]\nv_pk_mul_f16 v21, v60, v63 op_sel_hi:[0,1]\n"
      "s_waitcnt lgkmcnt(1)\nv_readlane_b32 s48, v69, 48\nv_mfma_f32_16x16x32_f16 v[40:43], v[172:175], v[28:31], v[40:43]\nv_pk_mul_f16 v22, v60, v60 op_sel_hi:[0,1]\ns_add_u32 s53, s53, s64\n"
      "v_smfmac_f32_16x16x64_f16 v[44:47], v[176:179], v[24:31], v242\nv_pk_mul_f16 v23, v60, v57 op_sel_hi:[0,1]\nv_mfma_f32_16x16x32_f16 v[32:35], v[180:183], v[16:19], v[32:35]\n"
      "v_pk_mul_f16 v24, v56, v60 op_sel_hi:[0,1]\nv_pk_mul_f16 v25, v56, v61 op_sel_hi:[0,1]\nv_mfma_f32_16x16x32_f16 v[32:35], v[192:195], v[20:23], v[32:35]\n"
      "v_pk_mul_f16 v26, v58, v60 op_sel_hi:[0,1]\nv_pk_mul_f16 v27, v58, v61 op_sel_hi:[0,1]\nv_mfma_f32_16x16x32_f16 v[36:39], v[196:199], v[20:23], v[36:39]\n"
      "v_pk_mul_f16 v28, v58, v58 op_sel_hi:[0,1]\nds_read_b64_tr_b16 v[62:63], v237 offset:17088\nv_mfma_f32_16x16x32_f16 v[36:39], v[184:187], v[16:19], v[36:39]\n"
      "v_pk_mul_f16 v29, v58, v57 op_sel_hi:[0,1]\nds_read_b64_tr_b16 v[60:61], v237 offset:16960\nv_mfma_f32_16x16x32_f16 v[40:43], v[188:191], v[16:19], v[40:43]\n"
      "v_pk_mul_f16 v30, v56, v58 op_sel_hi:[0,1]\nv_mfma_f32_16x16x32_f16 v[40:43], v[200:203], v[20:23], v[40:43]\nv_pk_mul_f16 v31, v56, v59 op_sel_hi:[0,1]\n"
      "v_smfmac_f32_16x16x64_f16 v[44:47], v[204:207], v[16:23], v242\nds_read_b64_tr_b16 v[56:57], v236 offset:16704\nds_read_b64_tr_b16 v[58:59], v237 offset:16832\n"
      "v_smfmac_f32_16x16x64_f16 v[44:47], v[232:235], v[24:31], v242\nv_pk_mul_f16 v16, v68, s48 op_sel:[0,1]\nv_pk_mul_f16 v17, v69, s48 op_sel:[0,1]\n"
      "v_mfma_f32_16x16x32_f16 v[32:35], v[208:211], v[24:27], v[32:35]\nv_pk_mul_f16 v18, v70, s48 op_sel:[0,1]\nv_pk_mul_f16 v19, v71, s48 op_sel:[0,1]\n"
      "v_mfma_f32_16x16x32_f16 v[32:35], v[220:223], v[28:31], v[32:35]\nv_pk_mul_f16 v20, v68, v70\nv_pk_mul_f16 v21, v69, v71\nv_mfma_f32_16x16x32_f16 v[36:39], v[224:227], v[28:31], v[36:39]\n"
      "v_add_f32 v44, v44, v45\nv_add_f32 v46, v46, v47\nv_mfma_f32_16x16x32_f16 v[36:39], v[212:215], v[24:27], v[36:39]\nv_mul_f32 v55, v44, v54\nv_mul_f32 v55, v55, v46\n"
      "ds_read_b128 v[44:47], v248 offset:48\nv_mfma_f32_16x16x32_f16 v[40:43], v[216:219], v[24:27], v[40:43]\ns_waitcnt lgkmcnt(1)\nv_pk_mul_f16 v22, v56, v56 op_sel_hi:[0,1]\n"
      "v_pk_mul_f16 v23, v56, v57 op_sel_hi:[0,1]\nv_mfma_f32_16x16x32_f16 v[40:43], v[228:231], v[28:31], v[40:43]\ns_cmp_lt_u32 s45, s53\nv_mul_f32 v32, v32, v48\nv_mul_f32 v33, v33, v49\n"
      "v_mul_f32 v34, v34, v50\ns_cbranch_scc0 .Lc_s8\n"
      ".Lc_u9:\nv_mfma_f32_16x16x32_f16 v[244:247], v[76:79], v[16:19], 0\nds_read_b32 v4, v248\nv_fmac_f32 v55, v32, v36\nv_mul_f32 v35, v35, v51\nv_fmac_f32 v55, v33, v37\ns_setprio 0\n"
      "v_mfma_f32_16x16x32_f16 v[244:247], v[88:91], v[20:23], v[244:247]\nv_fmac_f32 v55, v34, v38\nv_fmac_f32 v55, v35, v39\ns_add_u32 s45, s45, 2\ns_cmp_ge_u32 s45, s46\ns_cbranch_scc1 .Lc_t9\n"
      ".Lc_b9:\nv_mfma_f32_16x16x32_f16 v[36:39], v[84:87], v[20:23], 0\nv_pk_mul_f16 v24, v66, v66 op_sel_hi:[0,1]\nv_pk_mul_f16 v25, v66, v57 op_sel_hi:[0,1]\n"
      "v_mfma_f32_16x16x32_f16 v[36:39], v[72:75], v[16:19], v[36:39]\nv_pk_mul_f16 v26, v56, v66 op_sel_hi:[0,1]\nv_pk_mul_f16 v27, v56, v67 op_sel_hi:[0,1]\n"
      "v_mfma_f32_16x16x32_f16 v[32:35], v[80:83], v[20:23], 0\nv_pk_mul_f16 v28, v58, v66 op_sel_hi:[0,1]\nv_pk_mul_f16 v29, v58, v67 op_sel_hi:[0,1]\ns_setprio 1\ns_waitcnt lgkmcnt(0)\n"
      "v_smfmac_f32_16x16x64_f16 v[44:47], v[92:95], v[16:23], v242\nv_pk_mul_f16 v30, v60, v66 op_sel_hi:[0,1]\nv_pk_mul_f16 v31, v60, v67 op_sel_hi:[0,1]\n"
      "v_mfma_f32_16x16x32_f16 v[32:35], v[96:99], v[24:27], v[32:35]\nv_pk_mul_f16 v16, v62, v66 op_sel_hi:[0,1]\nv_pk_mul_f16 v17, v62, v67 op_sel_hi:[0,1]\n"
      "v_mfma_f32_16x16x32_f16 v[32:35], v[108:111], v[28:31], v[32:35]\nv_pk_mul_f16 v18, v64, v66 op_sel_hi:[0,1]\nv_mul_f32 v40, v40, v52\n"
      "v_mfma_f32_16x16x32_f16 v[36:39], v[112:115], v[28:31], v[36:39]\nv_pk_mul_f16 v19, v64, v67 op_sel_hi:[0,1]\nv_mul_f32 v41, v41, v53\n"
      "v_mfma_f32_16x16x32_f16 v[36:39], v[100:103], v[24:27], v[36:39]\nv_pk_mul_f16 v20, v64, v64 op_sel_hi:[0,1]\nv_fmac_f32 v55, v40, v42\n"
      "v_mfma_f32_16x16x32_f16 v[244:247], v[104:107], v[24:27], v[244:247]\nv_pk_mul_f16 v21, v64, v57 op_sel_hi:[0,1]\nv_fmac_f32 v55, v41, v43\n"
      "v_mfma_f32_16x16x32_f16 v[244:247], v[116:119], v[28:31], v[244:247]\nv_pk_mul_f16 v22, v56, v64 op_sel_hi:[0,1]\nv_readfirstlane_b32 s44, v4\n"
      "v_smfmac_f32_16x16x64_f16 v[44:47], v[120:123], v[24:31], v242\nv_pk_mul_f16 v23, v56, v65 op_sel_hi:[0,1]\ns_setprio 2\nv_mfma_f32_16x16x32_f16 v[32:35], v[124:127], v[16:19], v[32:35]\n"
      "v_pk_mul_f16 v24, v58, v64 op_sel_hi:[0,1]\nv_pk_mul_f16 v25, v58, v65 op_sel_hi:[0,1]\nds_read_b64_tr_b16 v[70:71], v239 offset:18560\n"
      "v_mfma_f32_16x16x32_f16 v[32:35], v[136:139], v[20:23], v[32:35]\nv_pk_mul_f16 v26, v60, v64 op_sel_hi:[0,1]\nds_read_b64_tr_b16 v[68:69], v238 offset:18560\n"
      "v_mfma_f32_16x16x32_f16 v[36:39], v[140:143], v[20:23], v[36:39]\nv_pk_mul_f16 v27, v60, v65 op_sel_hi:[0,1]\nds_read_b64_tr_b16 v[66:67], v237 offset:19200\n"
      "v_mfma_f32_16x16x32_f16 v[36:39], v[128:131], v[16:19], v[36:39]\nv_pk_mul_f16 v28, v62, v64 op_sel_hi:[0,1]\nv_add_f32_dpp v55, v55, v55 quad_perm:[1,0,3,2] row_mask:0xf bank_mask:0xf\n"
      "v_mfma_f32_16x16x32_f16 v[244:247], v[132:135], v[16:19], v[244:247]\nv_pk_mul_f16 v29, v62, v65 op_sel_hi:[0,1]\nv_add_f32_dpp v55, v55, v55 quad_perm:[2,3,0,1] row_mask:0xf bank_mask:0xf\n"
      "v_mfma_f32_16x16x32_f16 v[244:247], v[144:147], v[20:23], v[244:247]\nv_pk_mul_f16 v30, v62, v62 op_sel_hi:[0,1]\nv_add_f32_dpp v55, v55, v55 row_half_mirror row_mask:0xf bank_mask:0xf\n"
      "v_smfmac_f32_16x16x64_f16 v[44:47], v[148:151], v[16:23], v242\nv_pk_mul_f16 v31, v62, v57 op_sel_hi:[0,1]\nv_add_f32_dpp v55, v55, v55 row_mirror row_mask:0xf bank_mask:0xf\n"
      "v_mfma_f32_16x16x32_f16 v[32:35], v[152:155], v[24:27], v[32:35]\nv_pk_mul_f16 v16, v56, v62 op_sel_hi:[0,1]\nv_pk_mul_f16 v17, v56, v63 op_sel_hi:[0,1]\nds_write_b32 v243, v55 offset:1024\n"
      "v_mfma_f32_16x16x32_f16 v[32:35], v[164:167], v[28:31], v[32:35]\nv_pk_mul_f16 v18, v58, v62 op_sel_hi:[0,1]\nds_read_b64_tr_b16 v[64:65], v237 offset:19072\n"
      "v_mfma_f32_16x16x32_f16 v[36:39], v[168:171], v[28:31], v[36:39]\nv_pk_mul_f16 v19, v58, v63 op_sel_hi:[0,1]\ns_setprio 3\nv_mfma_f32_16x16x32_f16 v[36:39], v[156:159], v[24:27], v[36:39]\n"
      "v_pk_mul_f16 v20, v60, v62 op_sel_hi:[0,1]\ns_ff0_i32_b32 s53, s44\nv_mfma_f32_16x16x32_f16 v[244:247], v[160:163], v[24:27], v[244:247]\nv_pk_mul_f16 v21, v60, v63 op_sel_hi:[0,1]\n"
      "s_waitcnt lgkmcnt(1)\nv_readlane_b32 s48, v69, 48\nv_mfma_f32_16x16x32_f16 v[244:247], v[172:175], v[28:31], v[244:247]\nv_pk_mul_f16 v22, v60, v60 op_sel_hi:[0,1]\ns_add_u32 s53, s53, s64\n"
      "v_smfmac_f32_16x16x64_f16 v[44:47], v[176:179], v[24:31], v242\nv_pk_mul_f16 v23, v60, v57 op_sel_hi:[0,1]\nv_mfma_f32_16x16x32_f16 v[32:35], v[180:183], v[16:19], v[32:35]\n"
      "v_pk_mul_f16 v24, v56, v60 op_sel_hi:[0,1]\nv_pk_mul_f16 v25, v56, v61 op_sel_hi:[0,1]\nv_mfma_f32_16x16x32_f16 v[32:35], v[192:195], v[20:23], v[32:35]\n"
      "v_pk_mul_f16 v26, v58, v60 op_sel_hi:[0,1]\nv_pk_mul_f16 v27, v58, v61 op_sel_hi:[0,1]\nv_mfma_f32_16x16x32_f16 v[36:39], v[196:199], v[20:23], v[36:39]\n"
      "v_pk_mul_f16 v28, v58, v58 op_sel_hi:[0,1]\nds_read_b64_tr_b16 v[62:63], v237 offset:18944\nv_mfma_f32_16x16x32_f16 v[36:39], v[184:187], v[16:19], v[36:39]\n"
      "v_pk_mul_f16 v29, v58, v57 op_sel_hi:[0,1]\nds_read_b64_tr_b16 v[60:61], v237 offset:18816\nv_mfma_f32_16x16x32_f16 v[244:247], v[188:191], v[16:19], v[244:247]\n"
      "v_pk_mul_f16 v30, v56, v58 op_sel_hi:[0,1]\nv_mfma_f32_16x16x32_f16 v[244:247], v[200:203], v[20:23], v[244:247]\nv_pk_mul_f16 v31, v56, v59 op_sel_hi:[0,1]\n"
      "v_smfmac_f32_16x16x64_f16 v[44:47], v[204:207], v[16:23], v242\nds_read_b64_tr_b16 v[56:57], v236 offset:18560\nds_read_b64_tr_b16 v[58:59], v237 offset:18688\n"
      "v_smfmac_f32_16x16x64_f16 v[44:47], v[232:235], v[24:31], v242\nv_pk_mul_f16 v16, v68, s48 op_sel:[0,1]\nv_pk_mul_f16 v17, v69, s48 op_sel:[0,1]\n"
      "v_mfma_f32_16x16x32_f16 v[32:35], v[208:211], v[24:27], v[32:35]\nv_pk_mul_f16 v18, v70, s48 op_sel:[0,1]\nv_pk_mul_f16 v19, v71, s48 op_sel:[0,1]\n"
      "v_mfma_f32_16x16x32_f16 v[32:35], v[220:223], v[28:31], v[32:35]\nv_pk_mul_f16 v20, v68, v70\nv_pk_mul_f16 v21, v69, v71\nv_mfma_f32_16x16x32_f16 v[36:39], v[224:227], v[28:31], v[36:39]\n"
      "v_add_f32 v44, v44, v45\nv_add_f32 v46, v46, v47\nv_mfma_f32_16x16x32_f16 v[36:39], v[212:215], v[24:27], v[36:39]\nv_mul_f32 v55, v44, v54\nv_mul_f32 v55, v55, v46\n"
      "ds_read_b128 v[44:47], v248 offset:48\nv_mfma_f32_16x16x32_f16 v[244:247], v[216:219], v[24:27], v[244:247]\ns_waitcnt lgkmcnt(1)\nv_pk_mul_f16 v22, v56, v56 op_sel_hi:[0,1]\n"
      "v_pk_mul_f16 v23, v56, v57 op_sel_hi:[0,1]\nv_mfma_f32_16x16x32_f16 v[244:247], v[228:231], v[28:31], v[244:247]\ns_cmp_lt_u32 s45, s53\nv_mul_f32 v32, v32, v48\nv_mul_f32 v33, v33, v49\n"
      "v_mul_f32 v34, v34, v50\ns_cbranch_scc0 .Lc_s9\n"
      ".Lc_u10:\nv_mfma_f32_16x16x32_f16 v[40:43], v[76:79], v[16:19], 0\nds_read_b32 v4, v248\nv_fmac_f32 v55, v32, v36\nv_mul_f32 v35, v35, v51\nv_fmac_f32 v55, v33, v37\ns_setprio 0\n"
      "v_mfma_f32_16x16x32_f16 v[40:43], v[88:91], v[20:23], v[40:43]\nv_fmac_f32 v55, v34, v38\nv_fmac_f32 v55, v35, v39\ns_add_u32 s45, s45, 2\ns_cmp_ge_u32 s45, s46\ns_cbranch_scc1 .Lc_t10\n"
      ".Lc_b10:\nv_mfma_f32_16x16x32_f16 v[36:39], v[84:87], v[20:23], 0\nv_pk_mul_f16 v24, v66, v66 op_sel_hi:[0,1]\nv_pk_mul_f16 v25, v66, v57 op_sel_hi:[0,1]\n"
      "v_mfma_f32_16x16x32_f16 v[36:39], v[72:75], v[16:19], v[36:39]\nv_pk_mul_f16 v26, v56, v66 op_sel_hi:[0,1]\nv_pk_mul_f16 v27, v56, v67 op_sel_hi:[0,1]\n"
      "v_mfma_f32_16x16x32_f16 v[32:35], v[80:83], v[20:23], 0\nv_pk_mul_f16 v28, v58, v66 op_sel_hi:[0,1]\nv_pk_mul_f16 v29, v58, v67 op_sel_hi:[0,1]\ns_setprio 1\ns_waitcnt lgkmcnt(0)\n"
      "v_smfmac_f32_16x16x64_f16 v[44:47], v[92:95], v[16:23], v242\nv_pk_mul_f16 v30, v60, v66 op_sel_hi:[0,1]\nv_pk_mul_f16 v31, v60, v67 op_sel_hi:[0,1]\n"
      "v_mfma_f32_16x16x32_f16 v[32:35], v[96:99], v[24:27], v[32:35]\nv_pk_mul_f16 v16, v62, v66 op_sel_hi:[0,1]\nv_pk_mul_f16 v17, v62, v67 op_sel_hi:[0,1]\n"
      "v_mfma_f32_16x16x32_f16 v[32:35], v[108:111], v[28:31], v[32:35]\nv_pk_mul_f16 v18, v64, v66 op_sel_hi:[0,1]\nv_mul_f32 v244, v244, v52\n"
      "v_mfma_f32_16x16x32_f16 v[36:39], v[112:115], v[28:31], v[36:39]\nv_pk_mul_f16 v19, v64, v67 op_sel_hi:[0,1]\nv_mul_f32 v245, v245, v53\n"
      "v_mfma_f32_16x16x32_f16 v[36:39], v[100:103], v[24:27], v[36:39]\nv_pk_mul_f16 v20, v64, v64 op_sel_hi:[0,1]\nv_fmac_f32 v55, v244, v246\n"
      "v_mfma_f32_16x16x32_f16 v[40:43], v[104:107], v[24:27], v[40:43]\nv_pk_mul_f16 v21, v64, v57 op_sel_hi:[0,1]\nv_fmac_f32 v55, v245, v247\n"
      "v_mfma_f32_16x16x32_f16 v[40:43], v[116:119], v[28:31], v[40:43]\nv_pk_mul_f16 v22, v56, v64 op_sel_hi:[0,1]\nv_readfirstlane_b32 s44, v4\n"
      "v_smfmac_f32_16x16x64_f16 v[44:47], v[120:123], v[24:31], v242\nv_pk_mul_f16 v23, v56, v65 op_sel_hi:[0,1]\ns_setprio 2\nv_mfma_f32_16x16x32_f16 v[32:35], v[124:127], v[16:19], v[32:35]\n"
      "v_pk_mul_f16 v24, v58, v64 op_sel_hi:[0,1]\nv_pk_mul_f16 v25, v58, v65 op_sel_hi:[0,1]\nv_mfma_f32_16x16x32_f16 v[32:35], v[136:139], v[20:23], v[32:35]\n"
      "v_pk_mul_f16 v26, v60, v64 op_sel_hi:[0,1]\nv_mfma_f32_16x16x32_f16 v[36:39], v[140:143], v[20:23], v[36:39]\nv_pk_mul_f16 v27, v60, v65 op_sel_hi:[0,1]\n"
      "v_mfma_f32_16x16x32_f16 v[36:39], v[128:131], v[16:19], v[36:39]\nv_pk_mul_f16 v28, v62, v64 op_sel_hi:[0,1]\nv_add_f32_dpp v55, v55, v55 quad_perm:[1,0,3,2] row_mask:0xf bank_mask:0xf\n"
      "v_mfma_f32_16x16x32_f16 v[40:43], v[132:135], v[16:19], v[40:43]\nv_pk_mul_f16 v29, v62, v65 op_sel_hi:[0,1]\nv_add_f32_dpp v55, v55, v55 quad_perm:[2,3,0,1] row_mask:0xf bank_mask:0xf\n"
      "v_mfma_f32_16x16x32_f16 v[40:43], v[144:147], v[20:23], v[40:43]\nv_pk_mul_f16 v30, v62, v62 op_sel_hi:[0,1]\nv_add_f32_dpp v55, v55, v55 row_half_mirror row_mask:0xf bank_mask:0xf\n"
      "v_smfmac_f32_16x16x64_f16 v[44:47], v[148:151], v[16:23], v242\nv_pk_mul_f16 v31, v62, v57 op_sel_hi:[0,1]\nv_add_f32_dpp v55, v55, v55 row_mirror row_mask:0xf bank_mask:0xf\n"
      "v_mfma_f32_16x16x32_f16 v[32:35], v[152:155], v[24:27], v[32:35]\nv_pk_mul_f16 v16, v56, v62 op_sel_hi:[0,1]\nv_pk_mul_f16 v17, v56, v63 op_sel_hi:[0,1]\nds_write_b32 v243, v55 offset:1152\n"
      "v_mfma_f32_16x16x32_f16 v[32:35], v[164:167], v[28:31], v[32:35]\nv_pk_mul_f16 v18, v58, v62 op_sel_hi:[0,1]\nv_mfma_f32_16x16x32_f16 v[36:39], v[168:171], v[28:31], v[36:39]\n"
      "v_pk_mul_f16 v19, v58, v63 op_sel_hi:[0,1]\ns_setprio 3\nv_mfma_f32_16x16x32_f16 v[36:39], v[156:159], v[24:27], v[36:39]\nv_pk_mul_f16 v20, v60, v62 op_sel_hi:[0,1]\n"
      "s_ff0_i32_b32 s53, s44\nv_mfma_f32_16x16x32_f16 v[40:43], v[160:163], v[24:27], v[40:43]\nv_pk_mul_f16 v21, v60, v63 op_sel_hi:[0,1]\n"
      "v_mfma_f32_16x16x32_f16 v[40:43], v[172:175], v[28:31], v[40:43]\nv_pk_mul_f16 v22, v60, v60 op_sel_hi:[0,1]\ns_add_u32 s53, s53, s64\n"
      "v_smfmac_f32_16x16x64_f16 v[44:47], v[176:179], v[24:31], v242\nv_pk_mul_f16 v23, v60, v57 op_sel_hi:[0,1]\nv_mfma_f32_16x16x32_f16 v[32:35], v[180:183], v[16:19], v[32:35]\n"
      "v_pk_mul_f16 v24, v56, v60 op_sel_hi:[0,1]\nv_pk_mul_f16 v25, v56, v61 op_sel_hi:[0,1]\nv_mfma_f32_16x16x32_f16 v[32:35], v[192:195], v[20:23], v[32:35]\n"
      "v_pk_mul_f16 v26, v58, v60 op_sel_hi:[0,1]\nv_pk_mul_f16 v27, v58, v61 op_sel_hi:[0,1]\nv_mfma_f32_16x16x32_f16 v[36:39], v[196:199], v[20:23], v[36:39]\n"
      "v_pk_mul_f16 v28, v58, v58 op_sel_hi:[0,1]\nv_mfma_f32_16x16x32_f16 v[36:39], v[184:187], v[16:19], v[36:39]\nv_pk_mul_f16 v29, v58, v57 op_sel_hi:[0,1]\n"
      "v_mfma_f32_16x16x32_f16 v[40:43], v[188:191], v[16:19], v[40:43]\nv_pk_mul_f16 v30, v56, v58 op_sel_hi:[0,1]\nv_mfma_f32_16x16x32_f16 v[40:43], v[200:203], v[20:23], v[40:43]\n"
      "v_pk_mul_f16 v31, v56, v59 op_sel_hi:[0,1]\nv_smfmac_f32_16x16x64_f16 v[44:47], v[204:207], v[16:23], v242\ns_nop 0\nv_smfmac_f32_16x16x64_f16 v[44:47], v[232:235], v[24:31], v242\ns_nop 1\n"
      "v_mfma_f32_16x16x32_f16 v[32:35], v[208:211], v[24:27], v[32:35]\ns_nop 1\nv_mfma_f32_16x16x32_f16 v[32:35], v[220:223], v[28:31], v[32:35]\ns_nop 1\n"
      "v_mfma_f32_16x16x32_f16 v[36:39], v[224:227], v[28:31], v[36:39]\nv_add_f32 v44, v44, v45\nv_add_f32 v46, v46, v47\nv_mfma_f32_16x16x32_f16 v[36:39], v[212:215], v[24:27], v[36:39]\n"
      "v_mul_f32 v55, v44, v54\nv_mul_f32 v55, v55, v46\nds_read_b128 v[44:47], v248 offset:48\nv_mfma_f32_16x16x32_f16 v[40:43], v[216:219], v[24:27], v[40:43]\ns_nop 2\n"
      "v_mfma_f32_16x16x32_f16 v[40:43], v[228:231], v[28:31], v[40:43]\nv_mul_f32 v32, v32, v48\nv_mul_f32 v33, v33, v49\nv_mul_f32 v34, v34, v50\n"
      ".Lc_slow:\ns_sub_u32 s49, s45, s64\ns_lshr_b32 s50, s44, 16\ns_or_b32 s50, s50, s56\ns_ashr_i32 s50, s50, s49\ns_cmp_lg_u32 s50, -1\ns_cbranch_scc1 .Lc_more\n"
      ".Lc_fin:\ns_sub_u32 s50, s45, s33\ns_bitcmp1_b32 s50, 1\ns_cselect_b64 vcc, -1, 0\ns_lshl_b32 s50, s50, 6\ns_sub_u32 s50, s50, 128\nv_fmac_f32 v55, v32, v36\nv_mul_f32 v35, v35, v51\n"
      "v_fmac_f32 v55, v33, v37\nv_fmac_f32 v55, v34, v38\nv_fmac_f32 v55, v35, v39\nv_cndmask_b32 v40, v244, v40, vcc\nv_cndmask_b32 v41, v245, v41, vcc\nv_cndmask_b32 v42, v246, v42, vcc\n"
      "v_cndmask_b32 v43, v247, v43, vcc\nv_mul_f32 v40, v40, v52\nv_mul_f32 v41, v41, v53\nv_fmac_f32 v55, v40, v42\nv_fmac_f32 v55, v41, v43\nv_add_u32 v243, s50, v243\ns_nop 0\n"
      "v_add_f32_dpp v55, v55, v55 quad_perm:[1,0,3,2] row_mask:0xf bank_mask:0xf\ns_nop 1\nv_add_f32_dpp v55, v55, v55 quad_perm:[2,3,0,1] row_mask:0xf bank_mask:0xf\ns_nop 1\n"
      "v_add_f32_dpp v55, v55, v55 row_half_mirror row_mask:0xf bank_mask:0xf\ns_nop 1\nv_add_f32_dpp v55, v55, v55 row_mirror row_mask:0xf bank_mask:0xf\nds_write_b32 v243, v55\n"
      "s_cmp_gt_u32 s24, 1\ns_waitcnt lgkmcnt(0)\ns_barrier\ns_cbranch_scc1 .Lc_end\nds_read_b128 v[4:7], v10\nds_read_b32 v3, v11\nds_read_b32 v2, v248\nds_read_b64 v[8:9], v13\n"
      "s_mov_b32 s42, 0x11111111\ns_cmp_eq_u32 s24, 0\ns_cselect_b32 s43, s42, 0\ns_cselect_b32 s42, s42, 0x11111\ns_waitcnt lgkmcnt(2)\nv_add_f32 v4, v4, v5\nv_add_f32 v6, v6, v7\n"
      "v_add_f32 v4, v4, v6\nv_ldexp_f32 v4, v4, v3\ns_waitcnt lgkmcnt(1)\nv_and_b32 v2, 0xff, v2\nv_add_f32_dpp v4, v4, v4 quad_perm:[1,0,3,2] row_mask:0xf bank_mask:0xf\n"
      "v_lshrrev_b32 v2, v14, v2\nv_or_b32 v2, v2, v15\nv_add_f32_dpp v4, v4, v4 quad_perm:[2,3,0,1] row_mask:0xf bank_mask:0xf\nv_and_b32 v2, 1, v2\nv_cmp_eq_u32 vcc, 1, v2\ns_waitcnt lgkmcnt(0)\n"
      "v_add_f32 v4, v4, v9\nv_and_b32 v8, 0x1ffe, v8\nv_cvt_pk_bf16_f32 v4, v4, v4\ns_and_b64 exec, vcc, s[42:43]\ns_cbranch_execz .Lc_end\nglobal_store_short v8, v4, s[22:23]\ns_branch .Lc_end\n"
      ".Lc_s2:\ns_getpc_b64 s[60:61]\ns_branch .Lc_slow\ns_branch .Lc_u3\n"
      ".Lc_s3:\ns_getpc_b64 s[60:61]\ns_branch .Lc_slow\ns_branch .Lc_u4\n"
      ".Lc_s4:\ns_getpc_b64 s[60:61]\ns_branch .Lc_slow\ns_branch .Lc_u5\n"
      ".Lc_s5:\ns_getpc_b64 s[60:61]\ns_branch .Lc_slow\ns_branch .Lc_u6\n"
      ".Lc_s6:\ns_getpc_b64 s[60:61]\ns_branch .Lc_slow\ns_branch .Lc_u7\n"
      ".Lc_s7:\ns_getpc_b64 s[60:61]\ns_branch .Lc_slow\ns_branch .Lc_u8\n"
      ".Lc_s8:\ns_getpc_b64 s[60:61]\ns_branch .Lc_slow\ns_branch .Lc_u9\n"
      ".Lc_s9:\ns_getpc_b64 s[60:61]\ns_branch .Lc_slow\ns_branch .Lc_u10\n"
      ".Lc_t0:\ns_swappc_b64 s[4:5], s[34:35]\ns_branch .Lc_b0\n"
      ".Lc_t1:\ns_swappc_b64 s[4:5], s[34:35]\ns_branch .Lc_b1\n"
      ".Lc_t2:\ns_swappc_b64 s[4:5], s[34:35]\ns_branch .Lc_b2\n"
      ".Lc_t3:\ns_swappc_b64 s[4:5], s[34:35]\ns_branch .Lc_b3\n"
      ".Lc_t4:\ns_swappc_b64 s[4:5], s[34:35]\ns_branch .Lc_b4\n"
      ".Lc_t5:\ns_swappc_b64 s[4:5], s[34:35]\ns_branch .Lc_b5\n"
      ".Lc_t6:\ns_swappc_b64 s[4:5], s[34:35]\ns_branch .Lc_b6\n"
      ".Lc_t7:\ns_swappc_b64 s[4:5], s[34:35]\ns_branch .Lc_b7\n"
      ".Lc_t8:\ns_swappc_b64 s[4:5], s[34:35]\ns_branch .Lc_b8\n"
      ".Lc_t9:\ns_swappc_b64 s[4:5], s[34:35]\ns_branch .Lc_b9\n"
      ".Lc_t10:\ns_swappc_b64 s[4:5], s[34:35]\ns_branch .Lc_b10\n"
      ".Lc_tg:\ns_swappc_b64 s[4:5], s[34:35]\ns_branch .Lc_bg\n"
      ".Lc_th:\ns_swappc_b64 s[4:5], s[34:35]\ns_branch .Lc_bh\n"
      ".Lc_more:\ns_mov_b32 s51, 0\ns_add_u32 s60, s60, 4\ns_addc_u32 s61, s61, 0\n"
      ".Lc_chk:\ns_sub_u32 s49, s45, s64\ns_sub_u32 s53, s68, s64\ns_lshl_b32 s53, -1, s53\ns_lshr_b32 s50, s44, 16\ns_or_b32 s50, s50, s53\ns_ashr_i32 s50, s50, s49\ns_cmp_eq_u32 s50, -1\n"
      "s_cbranch_scc0 .Lc_c1\ns_cmp_lg_u32 s56, 0\ns_cbranch_scc1 .Lc_fin\ns_branch .Lc_c2\n"
      ".Lc_c1:\ns_lshr_b32 s50, s44, s49\ns_and_b32 s50, s50, 0x10001\ns_cbranch_scc1 .Lc_have\n"
      ".Lc_c2:\ns_add_u32 s51, s51, 1\ns_bitcmp1_b32 s51, 22\ns_cbranch_scc1 .Lc_fin\ns_cmp_lg_u32 s56, 0\ns_cbranch_scc1 .Lc_rd\ns_swappc_b64 s[4:5], s[34:35]\n"
      ".Lc_rd:\nds_read_b32 v4, v248\ns_waitcnt lgkmcnt(0)\nv_readfirstlane_b32 s44, v4\ns_branch .Lc_chk\n"
      ".Lc_have:\ns_cmp_eq_u32 s51, 0\ns_cbranch_scc1 .Lc_go\ns_sub_u32 s49, s45, s33\ns_mulk_i32 s49, 928\nv_add_u32 v24, s49, v236\nv_add_u32 v25, s49, v237\nv_add_u32 v26, s49, v238\n"
      "v_add_u32 v27, s49, v239\nds_read_b64_tr_b16 v[68:69], v26 offset:0\nds_read_b64_tr_b16 v[70:71], v27 offset:0\nds_read_b64_tr_b16 v[56:57], v24 offset:0\n"
      "ds_read_b64_tr_b16 v[58:59], v25 offset:128\nds_read_b64_tr_b16 v[60:61], v25 offset:256\nds_read_b64_tr_b16 v[62:63], v25 offset:384\nds_read_b64_tr_b16 v[64:65], v25 offset:512\n"
      "ds_read_b64_tr_b16 v[66:67], v25 offset:640\ns_waitcnt lgkmcnt(0)\nv_readlane_b32 s48, v69, 48\nv_pk_mul_f16 v20, v68, v70\nv_pk_mul_f16 v21, v69, v71\n"
      "v_pk_mul_f16 v22, v56, v56 op_sel_hi:[0,1]\nv_pk_mul_f16 v23, v56, v57 op_sel_hi:[0,1]\nv_pk_mul_f16 v16, v68, s48 op_sel:[0,1]\nv_pk_mul_f16 v17, v69, s48 op_sel:[0,1]\n"
      "v_pk_mul_f16 v18, v70, s48 op_sel:[0,1]\nv_pk_mul_f16 v19, v71, s48 op_sel:[0,1]\n"
      ".Lc_go:\ns_setpc_b64 s[60:61]\n"
      ".Lt_g:\ns_waitcnt lgkmcnt(0)\nv_readfirstlane_b32 s42, v2\ns_mov_b32 s43, 43008\nv_lshl_add_u32 v1, v240, 2, s43\ns_cmp_lg_u32 s42, 3\ns_cbranch_scc1 .Lt_p\nds_read_b32 v3, v248 offset:764\n"
      "ds_read2st64_b32 v[6:7], v1 offset0:0 offset1:4\nds_read2st64_b32 v[8:9], v1 offset0:1 offset1:5\nds_read2st64_b32 v[10:11], v1 offset0:2 offset1:6\n"
      "ds_read2st64_b32 v[12:13], v1 offset0:3 offset1:7\ns_lshl_b32 s42, s28, 8\ns_add_u32 s42, s42, s29\ns_lshl_b32 s42, s42, 7\ns_add_u32 s58, s12, s42\ns_addc_u32 s59, s13, 0\n"
      "v_lshlrev_b32 v14, 3, v240\ns_cmp_eq_u32 s25, 2\ns_cselect_b64 vcc, -1, 0\ns_movk_i32 s42, 0xff00\nv_mov_b32 v1, s41\ns_lshl_b32 s43, s25, 3\ns_waitcnt lgkmcnt(0)\nv_add_f32 v12, v12, v13\n"
      "v_add_f32 v6, v6, v7\nv_add_f32 v8, v8, v9\nv_add_f32 v10, v10, v11\nv_mov_b32 v13, v12\nv_mov_b32 v15, v12\ns_nop 1\nv_permlane16_swap_b32 v13, v15\nv_add_f32 v13, v13, v15\n"
      "v_cndmask_b32 v12, v12, v13, vcc\nv_mul_f32 v6, v3, v6\nv_mul_f32 v8, v3, v8\nv_mul_f32 v10, v3, v10\nv_mul_f32 v12, v3, v12\nv_add_u32 v6, 0x80, v6\nv_and_or_b32 v6, v6, s42, v1\n"
      "v_mov_b32 v7, s40\nv_add_u32 v8, 0x80, v8\nv_and_or_b32 v8, v8, s42, v1\nv_mov_b32 v9, s40\nv_add_u32 v10, 0x80, v10\nv_and_or_b32 v10, v10, s42, v1\nv_mov_b32 v11, s40\n"
      "v_add_u32 v12, 0x80, v12\nv_and_or_b32 v12, v12, s42, v1\nv_mov_b32 v13, s40\nglobal_store_dwordx2 v14, v[6:7], s[58:59] offset:0 sc1\n"
      "global_store_dwordx2 v14, v[8:9], s[58:59] offset:512 sc1\nglobal_store_dwordx2 v14, v[10:11], s[58:59] offset:1024 sc1\ns_cmp_eq_u32 s43, 0\ns_cbranch_scc1 .Lt_p\ns_bfm_b64 exec, s43, 0\n"
      "global_store_dwordx2 v14, v[12:13], s[58:59] offset:1536 sc1\ns_mov_b64 exec, -1\n"
      ".Lt_p:\ns_cmp_gt_u32 s66, 100\ns_cbranch_scc1 .Lt_d\ns_mov_b32 s46, s66\ns_swappc_b64 s[34:35], s[4:5]\ns_lshl_b32 s43, 0x10000, s67\ns_cmp_gt_u32 s44, 0xffff\ns_cbranch_scc1 .Lt_a\n"
      "v_lshlrev_b32 v1, 3, v240\ns_mov_b64 exec, 1\nglobal_atomic_add v5, v1, v241, s[8:9] sc0 sc1\ns_mov_b64 exec, -1\nglobal_load_dwordx2 v[254:255], v249, s[12:13] sc1\n"
      ".Lt_f:\nglobal_load_dwordx2 v[2:3], v1, s[14:15] sc1\ns_swappc_b64 s[34:35], s[4:5]\ns_waitcnt vmcnt(0)\nv_readfirstlane_b32 s50, v5\nv_cmp_eq_u32 vcc, s41, v2\n"
      "v_cmp_eq_u32 s[62:63], s40, v3\nv_mov_b32 v7, s43\ns_cmp_lt_u32 s50, s55\ns_cbranch_scc1 .Lt_r\nds_add_u32 v253, v7\ns_branch .Lt_c\n"
      ".Lt_r:\ns_add_u32 s50, s50, 1\nv_readlane_b32 s54, v251, s50\ns_and_b64 s[62:63], s[62:63], vcc\ns_bfe_u32 s50, s54, 0x50004\ns_lshr_b32 s49, s54, 9\ns_lshl3_add_u32 s50, s50, s49\n"
      "s_sub_u32 s50, s50, s69\ns_bitcmp0_b64 s[62:63], s50\ns_cbranch_scc1 .Lt_f\ns_mul_i32 s50, s54, 3584\ns_add_u32 s62, s16, s50\ns_addc_u32 s63, s17, 0\ns_add_u32 s50, s64, s67\n"
      "s_mul_i32 s49, s50, 928\ns_add_u32 m0, s49, 45056\ns_mov_b32 exec_hi, 0x7fffff\nglobal_load_lds_dwordx4 v252, s[62:63] offset:0 sc1\ns_add_u32 m0, m0, 21376\ns_lshl_b32 s54, s54, 3\n"
      "global_load_lds_dwordx4 v252, s[62:63] offset:896 sc1\ns_add_u32 m0, m0, 21376\ns_lshl3_add_u32 s50, s50, 160256\nglobal_load_lds_dwordx4 v252, s[62:63] offset:1792 sc1\n"
      "s_add_u32 m0, m0, 21376\ns_lshl_b32 s43, 1, s67\nglobal_load_lds_dwordx4 v252, s[62:63] offset:2688 sc1\ns_mov_b32 exec_hi, -1\ns_mov_b32 exec_lo, 0\nv_and_b32 v249, 0xffff8000, v249\n"
      "v_or_b32 v249, s54, v249\nv_mov_b32 v250, s50\ns_mov_b32 exec_lo, -1\ns_swappc_b64 s[34:35], s[4:5]\ns_waitcnt vmcnt(0)\n"
      ".Lt_a:\nv_mov_b32 v0, s43\nds_add_u32 v253, v0\n"
      ".Lt_d:\ns_sub_u32 s46, s64, 4\ns_cmp_ge_i32 s45, s46\ns_cbranch_scc1 .Lt_l\ns_swappc_b64 s[34:35], s[4:5]\n"
      ".Lt_l:\nglobal_load_dwordx2 v[254:255], v249, s[12:13] sc1\ns_swappc_b64 s[34:35], s[4:5]\ns_waitcnt vmcnt(0)\n"
      ".Lt_c:\nv_and_b32 v6, 0xff, v254\nv_cmp_ne_u32 vcc, s40, v255\nv_cmp_ne_u32 s[62:63], s41, v6\nv_and_b32 v9, 0xffffff00, v254\ns_or_b64 vcc, vcc, s[62:63]\ns_cbranch_vccnz .Lt_d\n"
      "s_mov_b32 vcc_lo, 0x1fff1fff\ns_mov_b32 vcc_hi, vcc_lo\nv_bfe_u32 v8, v249, 2, 13\nv_cndmask_b32 v9, 0, v9, vcc\ns_sub_u32 s56, s68, s64\ns_lshl_b32 s56, -1, s56\n"
      "v_add_f32_dpp v9, v9, v9 quad_perm:[1,0,3,2] row_mask:0xf bank_mask:0xf\ns_movk_i32 s46, 99\ns_nop 0\nv_add_f32_dpp v9, v9, v9 quad_perm:[2,3,0,1] row_mask:0xf bank_mask:0xf\ns_nop 1\n"
      "v_add_f32_dpp v9, v9, v9 row_half_mirror row_mask:0xf bank_mask:0xf\ns_nop 1\nv_add_f32_dpp v9, v9, v9 row_mirror row_mask:0xf bank_mask:0xf\nv_add_f32 v9, s38, v9\n"
      "ds_write_b64 v250, v[8:9]\ns_setpc_b64 s[4:5]\n"
      ".Lc_end:\n"
      ".Lxd_end:"
      :: "{v240}"(lane), "{v253}"(tch), "{v[248:251]}"(mrow), "{v252}"(msc), "{v[254:255]}"(rsp), "{s[4:19]}"(ka0), "{s[20:23]}"(ka1), "{s[24:31]}"(gs), "{s2}"(blk)
      : "memory", "vcc", "scc", "s33", "s34", "s35", "s36", "s37", "s38", "s39", "s40", "s41", "s42", "s43", "s44", "s45", "s46", "s47", "s48", "s49", "s50", "s51", "s52", "s53", "s54", "s55", "s56", "s57", "s58", "s59", "s60", "s61", "s62", "s63", "s64", "s65", "s66", "s67", "s68", "s69",
        "v0", "v1", "v2", "v3", "v4", "v5", "v6", "v7", "v8", "v9", "v10", "v11", "v12", "v13", "v14", "v15", "v16", "v17", "v18", "v19", "v20", "v21", "v22", "v23", "v24", "v25", "v26", "v27",
        "v28", "v29", "v30", "v31", "v32", "v33", "v34", "v35", "v36", "v37", "v38", "v39", "v40", "v41", "v42", "v43", "v44", "v45", "v46", "v47", "v48", "v49", "v50", "v51", "v52", "v53", "v54",
        "v55", "v56", "v57", "v58", "v59", "v60", "v61", "v62", "v63", "v64", "v65", "v66", "v67", "v68", "v69", "v70", "v71", "v72", "v73", "v74", "v75", "v76", "v77", "v78", "v79", "v80", "v81",
        "v82", "v83", "v84", "v85", "v86", "v87", "v88", "v89", "v90", "v91", "v92", "v93", "v94", "v95", "v96", "v97", "v98", "v99", "v100", "v101", "v102", "v103", "v104", "v105", "v106", "v107",
        "v108", "v109", "v110", "v111", "v112", "v113", "v114", "v115", "v116", "v117", "v118", "v119", "v120", "v121", "v122", "v123", "v124", "v125", "v126", "v127", "v128", "v129", "v130",
        "v131", "v132", "v133", "v134", "v135", "v136", "v137", "v138", "v139", "v140", "v141", "v142", "v143", "v144", "v145", "v146", "v147", "v148", "v149", "v150", "v151", "v152", "v153",
        "v154", "v155", "v156", "v157", "v158", "v159", "v160", "v161", "v162", "v163", "v164", "v165", "v166", "v167", "v168", "v169", "v170", "v171", "v172", "v173", "v174", "v175", "v176",
        "v177", "v178", "v179", "v180", "v181", "v182", "v183", "v184", "v185", "v186", "v187", "v188", "v189", "v190", "v191", "v192", "v193", "v194", "v195", "v196", "v197", "v198", "v199",
        "v200", "v201", "v202", "v203", "v204", "v205", "v206", "v207", "v208", "v209", "v210", "v211", "v212", "v213", "v214", "v215", "v216", "v217", "v218", "v219", "v220", "v221", "v222",
        "v223", "v224", "v225", "v226", "v227", "v228", "v229", "v230", "v231", "v232", "v233", "v234", "v235", "v236", "v237", "v238", "v239", "v241", "v242", "v243", "v244", "v245", "v246", "v247");
}

extern "C" void kernel_launch(void* const* d_in, const int* in_sizes, int n_in, void* d_out, int out_size,
                              void* d_ws, size_t ws_size, hipStream_t stream) {
  const int* idx = (const int*)d_in[0];
  const u16* table = (const u16*)d_in[1];
  const u16* ws_cin = (const u16*)d_in[2];
  const u16* wout = (const u16*)d_in[3];
  const u16* w1 = (const u16*)d_in[4];
  const u16* b1 = (const u16*)d_in[5];
  const u16* w2 = (const u16*)d_in[6];
  const u16* b_out = (const u16*)d_in[7];
  dnn_kernel<<<256, 512, 0, stream>>>(idx, table, ws_cin, w1, b1, w2, wout, b_out, (char*)d_ws, (u16*)d_out);
}
```

```cpp
#include <hip/hip_runtime.h>
#include <hip/hip_bf16.h>
#include <stdint.h>
#include <utility>

typedef __attribute__((ext_vector_type(8))) short bf16x8;
typedef __attribute__((ext_vector_type(4))) short s16x4;
typedef __attribute__((ext_vector_type(4))) float f32x4;
typedef __attribute__((ext_vector_type(2))) float f32x2;
typedef __attribute__((ext_vector_type(2))) __bf16 bf16x2;
typedef __attribute__((ext_vector_type(16))) float f32x16;
typedef unsigned short u16;
typedef __attribute__((ext_vector_type(2))) unsigned u32x2;
typedef __attribute__((ext_vector_type(4))) unsigned u32x4;

constexpr int NB = 4096, NF = 26, ND = 64, DIN = 1664;

#define STAMP(k,i)
#define WSTAMP(i)
template <class F, int... I>
__device__ __forceinline__ void static_for(F& f, std::integer_sequence<int, I...>) { (f(std::integral_constant<int, I>{}), ...); }
template <int CTRL, int RM = 0xf>
__device__ __forceinline__ float dpp(float v) { return __builtin_bit_cast(float, __builtin_amdgcn_update_dpp(0, __builtin_bit_cast(int, v), CTRL, RM, 0xf, false)); }
__device__ __forceinline__ unsigned pk2(float a, float b) { return __builtin_bit_cast(unsigned, __builtin_convertvector((f32x2){a, b}, bf16x2)); }

template <int V> using IC = std::integral_constant<int, V>;
typedef __attribute__((ext_vector_type(8))) unsigned u32x8;
typedef __attribute__((ext_vector_type(16))) unsigned u32x16;
constexpr int STAGE = 224 * 128 + 64 * 256, G_LDS = 163840;
constexpr int MC = 41 * 1024;
constexpr int W3S = 0, U_OFF = W3S + 35840, NC_OFF = U_OFF + 2048, GT_OFF = NC_OFF + 1024, WO_OFF = GT_OFF + 2048, DP = 4352, W2S = 2 * STAGE;
static_assert(WO_OFF + 256 <= STAGE, "lds");
constexpr int WS_RESP = 4096, WS_POOL = 32768, WS_M = 1 << 20;
#define NCLV 8
constexpr int NSLOT = 21, NCL = NCLV;

struct Geo { unsigned nt, s0, ns; };
constexpr __host__ __device__ Geo geo(unsigned e) {
  const unsigned i = (e * 79) >> 10, nt = e - 13 * i, c = nt < 9 ? 13108 : 13798, t = i * c + ((nt * 247) & 1023);
  return Geo{nt, t >> 10, ((t + c) >> 10) - (t >> 10)};
}
constexpr unsigned long long builders() {
  unsigned long long k = 0;
  for (unsigned x = 0; x < 8; ++x)
    for (unsigned s = 32; s-- > 0;)
      if (geo(32 * x + s).ns == 12) k = (k & ~(31ull << (5 * x))) | (unsigned long long)s << (5 * x);
  return k;
}
constexpr unsigned long long BK = builders();
#define KEEP 10, 13, 15, 11, 8
constexpr int KP[5] = {KEEP};
constexpr int WANT[5] = {138, 163, 177, 135, 113};
constexpr int kind(unsigned b) { return ((BK >> (5 * (b & 7))) & 31) == b >> 3 ? 3 + ((b & 7) >> 2) : 14 - geo((b & 7) * 32 + (b >> 3)).ns; }
constexpr __host__ __device__ unsigned group(unsigned b) { return ((b >> 3) + 2 * b) & 15; }
constexpr bool geo_ok() {
  for (unsigned nt = 0; nt < 13; ++nt) {
    unsigned s = 0;
    for (unsigned e = nt; e < 256; e += 13) {
      const Geo g = geo(e);
      if (g.nt != nt || g.s0 != s || g.ns < 12 || g.ns > 14 || e < g.s0 || e >= g.s0 + g.ns) return false;
      s += g.ns;
    }
    if (s != 256) return false;
  }
  return true;
}
static_assert(geo_ok(), "tiles");

struct PI { int a, b; };
constexpr PI prod(int n) {
  int m = n - 2;
  for (int jj = 0; jj < 6; ++jj) {
    const int j = jj ? 6 - jj : 0;
    if (m < 2 + 2 * j) return m == 0 ? PI{2 * j, 2 * j} : m == 1 ? PI{2 * j, 1} : PI{(m - 2) & ~1, 2 * j + (m & 1)};
    m -= 2 + 2 * j;
  }
  return PI{0, 0};
}
constexpr int xfield(int k, int h, int g) { return k == 1 ? 24 + h : 4 * (k >> 1) + ((g + 2 * (k & 1) + h) & 3); }
constexpr int pqfield(int q, int sl, int g) { return g < 3 ? 8 * g + 4 * (sl >> 1) + (sl & 1) + (((g ^ q) & 1) << 1) : sl == 3 ? 26 : 24 + ((sl + q) >> 1); }
constexpr unsigned short slot_code(int f, int t, int g, int h) {
  if (f == 0) {
    const int sl = 2 * (t & 1) + h, q = t >> 1;
    return g == 3 && (q || (sl & 1)) ? 0x8000 : 0x4000 | 6 << 10 | (26 * 33 + pqfield(q, sl, g));
  }
  const int n = 4 * (f - 1) + t;
  const int p = n < 2 ? pqfield(0, 2 * n + h, g) : xfield(prod(n).a, 0, g), q = n < 2 ? pqfield(1, 2 * n + h, g) : xfield(prod(n).b, h, g);
  return (p == 26 ? 0x4000 | 10 << 10 : p == q ? 1 << 10 : 2 << 10) | (p * 33 + q);
}
struct GT { unsigned short t[2][64][8], pool[16][64]; };
constexpr GT make_gt() {
  GT r{};
  for (int l = 0; l < 64; ++l)
    for (int e = 0; e < 8; ++e) {
      r.t[0][l][e] = l < 48 ? slot_code(l >> 2, e >> 1, l & 3, e & 1) : 0x8000;
      r.t[1][l][e] = l < 48 ? slot_code(2 * (l >> 3) + ((l >> 1) & 1), 2 * ((e >> 1) & 1) + ((l >> 2) & 1), 2 * (l & 1) + (e >> 2), e & 1) : 0x8000;
    }
  int tot = 0, dem = 0, acc = 0, k = 15, b = 0;
  for (int i = 0; i < 256; ++i) { tot += 16 - KP[kind(i)]; dem += WANT[kind(i)] - 10 * KP[kind(i)]; }
  for (int q = 0, n = 0; q < 16; ++q) {
    for (int i = 0; i < 256; ++i) acc += group(i) == q ? WANT[kind(i)] - 10 * KP[kind(i)] : 0;
    const int len = (acc * tot + dem / 2) / dem - n;
    n += len;
    r.pool[q][0] = len;
    for (int i = 1; i <= len; ++i) {
      while (k < KP[kind(b)]) { ++b; k = 15; }
      r.pool[q][i] = ((b & 7) * 32 + (b >> 3)) * 16 + k--;
    }
  }
  return r;
}
constexpr bool pool_ok() {
  const GT t = make_gt();
  for (int q = 0; q < 16; ++q) {
    int n = 0;
    for (int i = 0; i < 256; ++i) n += group(i) != q ? 0 : NSLOT - KP[kind(i)] < NCL ? NSLOT - KP[kind(i)] : NCL;
    if (t.pool[q][0] > 62 || t.pool[q][0] > n) return false;
    const int fb = 16 * q < 24 ? 0 : 16 * q - 24 > 192 ? 192 : 16 * q - 24;
    for (int i = 1; i <= t.pool[q][0]; ++i) {
      const int b = ((t.pool[q][i] >> 4) & 31) * 8 + (t.pool[q][i] >> 9);
      if (b < fb || b >= fb + 64) return false;
    }
  }
  for (int k = 0; k < 5; ++k) if (KP[k] < 8 || KP[k] > 16) return false;
  return true;
}
static_assert(pool_ok(), "pools");
static __device__ const __attribute__((aligned(16))) GT g_tab = make_gt();

__global__ __launch_bounds__(512, 2) void dnn_kernel(const int* __restrict__ idx, const u16* __restrict__ table,
                                                     const u16* __restrict__ ws_cin, const u16* __restrict__ w1,
                                                     const u16* __restrict__ b1, const u16* __restrict__ w2,
                                                     const u16* __restrict__ wout, const u16* __restrict__ b_out,
                                                     char* __restrict__ ws, u16* __restrict__ out) {
  __shared__ __attribute__((aligned(1024))) char shm[G_LDS];
  u32x4 ka1;
  u32x8 gs;
  u32x16 ka0;
  unsigned tch, blk = blockIdx.x;
  int lane;
  asm volatile(
      "s_load_dwordx16 s[4:19], %[ka], 0\ns_load_dwordx4 s[20:23], %[ka], 64\nv_readfirstlane_b32 s24, %[tid]\ns_lshr_b32 s37, s2, 3\ns_cmp_lt_u32 s24, 448\ns_cbranch_scc1 .Lf3\n"
      "s_getpc_b64 s[42:43]\n"
      ".Lxd_pc0:\nv_and_b32 v245, 15, %[tid]\nv_lshl_add_u32 v245, s37, 4, v245\nv_lshlrev_b32 v245, 7, v245\nv_min_u32 v245, .Lxd_end-.Lxd_pc0-4, v245\nglobal_load_dword v253, v245, s[42:43]\n"
      ".Lf3:\ns_and_b32 s36, s2, 7\ns_lshl_b32 s30, s36, 5\ns_add_u32 s30, s30, s37\ns_lshr_b32 s24, s24, 6\ns_mul_i32 s38, s30, 79\ns_lshr_b32 s38, s38, 10\ns_mul_i32 s28, s38, 13\n"
      "s_sub_u32 s28, s30, s28\nv_and_b32 v240, 63, %[tid]\ns_lshr_b32 s27, s36, 1\ns_add_u32 s27, s27, s36\ns_and_b32 s39, s28, 3\ns_mul_i32 s39, s39, 3\ns_add_u32 s27, s27, s39\n"
      "s_sub_u32 s39, s27, 12\ns_min_u32 s27, s27, s39\ns_lshl_b32 s27, s27, 1\ns_cmp_ge_u32 s24, 4\ns_cbranch_scc1 .Lf1\ns_movk_i32 s39, 13798\nv_mul_u32_u24 v245, 9363, v240\n"
      "s_cmp_lt_u32 s28, 9\ns_cselect_b32 s39, 13108, s39\ns_or_b32 s41, s27, 2\ns_mul_i32 s40, s28, 247\nv_lshrrev_b32 v245, 16, v245\ns_and_b32 s40, s40, 1023\n"
      "v_mad_i32_i24 v246, v245, -7, v240\ns_mul_i32 s38, s38, s39\nv_lshl_add_u32 v246, v246, 2, s41\ns_add_u32 s38, s38, s40\nv_subrev_u32 v247, 26, v246\ns_lshr_b32 s29, s38, 10\n"
      "v_mul_u32_u24 v245, 26, v245\ns_add_u32 s38, s38, s39\nv_min_u32 v246, v246, v247\ns_lshr_b32 s38, s38, 10\nv_add_lshl_u32 v241, v246, v245, 2\ns_sub_u32 s38, s38, s29\n"
      "v_add_u32 v244, 192, v241\ns_lshl_b32 s38, s38, 1\nv_add_u32 v243, 128, v241\ns_sub_u32 s25, s38, 24\nv_add_u32 v242, 64, v241\ns_mul_i32 s38, s24, 6272\ns_add_u32 m0, s38, 139648\n"
      "s_lshl1_add_u32 s38, s29, s24\ns_mul_i32 s38, s38, 832\ns_mul_i32 s40, s24, 3328\ns_add_u32 s38, s38, 16640\ns_sub_u32 s40, 16896, s40\ns_mov_b32 exec_hi, 0xffffff\n"
      "v_add_u32 v245, 256, v241\nv_add_u32 v246, 2432, v241\ns_waitcnt lgkmcnt(0)\ns_add_u32 s38, s4, s38\ns_addc_u32 s39, s5, 0\ns_cmp_ge_u32 s24, s25\nglobal_load_lds_dwordx4 v241, s[38:39]\n"
      "s_cbranch_scc1 .Li0\nglobal_load_lds_dwordx4 v246, s[38:39] offset:896\n"
      ".Li0:\ns_sub_u32 m0, m0, 4480\ns_sub_u32 s38, s38, s40\ns_subb_u32 s39, s39, 0\nglobal_load_lds_dwordx4 v245, s[38:39] offset:0\nglobal_load_lds_dwordx4 v244, s[38:39] offset:896\n"
      "global_load_lds_dwordx4 v243, s[38:39] offset:1792\nglobal_load_lds_dwordx4 v242, s[38:39] offset:2688\nglobal_load_lds_dwordx4 v241, s[38:39] offset:3584\ns_mov_b32 exec_hi, -1\n"
      "s_add_u32 s27, s27, 2\ns_mul_i32 s36, s36, 5\ns_mov_b32 s38, %[bkl]\ns_mov_b32 s39, %[bkh]\ns_lshr_b64 s[38:39], s[38:39], s36\ns_and_b32 s38, s38, 31\ns_xor_b32 s31, s38, s37\n"
      "s_cbranch_scc0 .Lbld\n"
      ".Lbp:\ns_mov_b32 s36, s24\ns_mul_i32 s42, s36, 6272\ns_cmp_ge_u32 s36, s25\ns_cselect_b64 vcc, 0, -1\ns_and_b32 s44, s27, 2\ns_xor_b32 s44, s44, 2\ns_lshl_b32 s44, s44, 2\n"
      "s_add_u32 s42, s42, s44\ns_add_u32 s42, s42, 135168\nv_lshrrev_b32 v22, 3, v240\nv_mul_u32_u24 v6, 112, v22\nv_add_u32 v6, s42, v6\nv_lshrrev_b32 v22, 4, v240\ns_lshl_b32 s42, s36, 2\n"
      "v_xor_b32 v22, v22, v240\nv_xor_b32 v22, s42, v22\nv_and_b32 v22, 7, v22\nv_lshlrev_b32 v4, 4, v22\nv_xor_b32 v5, 64, v4\ns_sub_u32 s44, s6, 1024\ns_subb_u32 s45, s7, 0\n"
      "s_sub_u32 s46, s6, 2048\ns_subb_u32 s47, s7, 0\ns_sub_u32 s48, s6, 3072\ns_subb_u32 s49, s7, 0\ns_mul_i32 s53, s36, 5120\ns_lshl_b32 s26, s36, 10\ns_lshl_b32 s54, s36, 12\n"
      "s_add_u32 s54, s54, 28672\nv_add_u32 v7, 1792, v6\nv_add_u32 v8, 3584, v6\nv_add_u32 v9, 5376, v6\ns_waitcnt vmcnt(0)\nds_read2_b64 v[196:199], v8 offset0:112 offset1:113\n"
      "ds_read2_b64 v[200:203], v9 offset0:0 offset1:1\nds_read2_b64 v[176:179], v6 offset0:0 offset1:1\nds_read2_b64 v[180:183], v6 offset0:112 offset1:113\n"
      "ds_read2_b64 v[184:187], v7 offset0:0 offset1:1\nds_read2_b64 v[188:191], v7 offset0:112 offset1:113\nds_read2_b64 v[192:195], v8 offset0:0 offset1:1\ns_waitcnt lgkmcnt(0)\n"
      "v_lshl_add_u32 v22, v176, 7, v4\nv_lshl_add_u32 v23, v180, 7, v5\nv_lshl_add_u32 v24, v184, 7, v4\nv_lshl_add_u32 v25, v188, 7, v5\nv_lshl_add_u32 v26, v192, 7, v4\n"
      "v_lshl_add_u32 v27, v196, 7, v4\nv_lshl_add_u32 v28, v200, 7, v4\ns_add_u32 m0, s26, 65536\ns_nop 0\nglobal_load_lds_dwordx4 v27, s[6:7] offset:0\nv_lshl_add_u32 v27, v197, 7, v4\n"
      "ds_read2_b64 v[196:199], v8 offset0:113 offset1:114\ns_cbranch_vccz .La1001\ns_add_u32 m0, s26, 69632\ns_nop 0\nglobal_load_lds_dwordx4 v28, s[6:7] offset:0\n"
      ".La1001:\nv_lshl_add_u32 v28, v201, 7, v4\ns_add_u32 m0, s53, 45056\ns_nop 0\nglobal_load_lds_dwordx4 v22, s[6:7] offset:0\nv_lshl_add_u32 v22, v177, 7, v4\n"
      "global_load_lds_dwordx4 v23, s[44:45] offset:1024\nv_lshl_add_u32 v23, v181, 7, v5\nds_read2_b64 v[180:183], v6 offset0:113 offset1:114\nglobal_load_lds_dwordx4 v24, s[46:47] offset:2048\n"
      "v_lshl_add_u32 v24, v185, 7, v4\nglobal_load_lds_dwordx4 v25, s[48:49] offset:3072\nv_lshl_add_u32 v25, v189, 7, v5\nds_read2_b64 v[188:191], v7 offset0:113 offset1:114\n"
      "s_add_u32 m0, s53, 49152\ns_nop 0\nglobal_load_lds_dwordx4 v26, s[6:7] offset:0\nv_lshl_add_u32 v26, v193, 7, v4\ns_waitcnt lgkmcnt(0)\ns_setprio 3\ns_barrier\ns_add_u32 m0, s26, 110592\n"
      "s_nop 0\nglobal_load_lds_dwordx4 v27, s[6:7] offset:0\nv_lshl_add_u32 v27, v196, 7, v4\ns_cbranch_vccz .La1002\ns_add_u32 m0, s26, 114688\ns_nop 0\n"
      "global_load_lds_dwordx4 v28, s[6:7] offset:0\n"
      ".La1002:\nv_lshl_add_u32 v28, v202, 7, v4\ns_add_u32 m0, s53, 90112\ns_nop 0\nglobal_load_lds_dwordx4 v22, s[6:7] offset:0\nv_lshl_add_u32 v22, v178, 7, v4\n"
      "global_load_lds_dwordx4 v23, s[44:45] offset:1024\nv_lshl_add_u32 v23, v180, 7, v5\nglobal_load_lds_dwordx4 v24, s[46:47] offset:2048\nv_lshl_add_u32 v24, v186, 7, v4\n"
      "global_load_lds_dwordx4 v25, s[48:49] offset:3072\nv_lshl_add_u32 v25, v188, 7, v5\ns_add_u32 m0, s53, 94208\ns_nop 0\nglobal_load_lds_dwordx4 v26, s[6:7] offset:0\n"
      "v_lshl_add_u32 v26, v194, 7, v4\nv_lshrrev_b32 v30, 2, v240\nv_bfe_u32 v31, v240, 1, 1\ns_lshl_b32 s42, s36, 1\nv_lshrrev_b32 v32, 3, v240\nv_or_b32 v31, s42, v31\ns_sub_u32 s33, s30, s29\n"
      "s_lshl_b32 s42, s33, 11\nv_xor_b32 v31, v31, v32\nv_lshl_add_u32 v30, v30, 7, s42\nv_and_b32 v32, 1, v240\nv_lshl_add_u32 v30, v31, 4, v30\nv_lshl_add_u32 v30, v32, 3, v30\n"
      "v_add_u32 v31, 90112, v30\nv_mov_b32 v32, 0\nv_mov_b32 v33, 0x3d800000\ns_mov_b32 s55, 0x80008000\ns_and_b32 s41, s24, 3\nv_lshrrev_b32 v245, 4, v240\nv_and_b32 v246, 15, v240\n"
      "s_mul_i32 s40, s41, 53248\nv_lshlrev_b32 v238, 1, v245\nv_mul_u32_u24 v245, 3328, v245\nv_xor_b32 v247, v246, v238\nv_add_u32 v245, s40, v245\nv_lshl_add_u32 v236, v247, 4, v245\n"
      "v_xor_b32 v238, 128, v236\nv_add_u32 v237, 12288, v236\nv_add_u32 v239, 36864, v238\nv_add_u32 v238, 24576, v238\ns_lshl_b32 s40, s28, 8\ns_add_u32 s10, s10, s40\ns_addc_u32 s11, s11, 0\n"
      "s_add_u32 s52, s27, 1\ns_mul_i32 s52, s52, 212992\ns_add_u32 s50, s10, s52\ns_addc_u32 s51, s11, 0\ns_add_u32 s50, s50, 212992\ns_addc_u32 s51, s51, 0\ns_cmp_eq_u32 s27, 24\n"
      "s_cselect_b64 s[50:51], s[10:11], s[50:51]\ns_lshl_b32 s42, s2, 2\ns_load_dword s37, s[20:21], s42\ns_load_dword s38, s[18:19], 0\ns_bfe_u32 s42, s2, 0x10002\ns_add_u32 s42, s42, 3\n"
      "s_lshr_b32 s43, s25, 1\ns_sub_u32 s43, 2, s43\ns_cmp_eq_u32 s31, 0\ns_cselect_b32 s42, s42, s43\ns_mul_i32 s42, s42, 5\ns_lshr_b32 s64, %[kp], s42\ns_and_b32 s64, s64, 31\ns_branch .Lfeed\n"
      ".Lf1:\ns_and_b32 s41, s24, 3\nv_lshrrev_b32 v245, 4, v240\nv_and_b32 v246, 15, v240\ns_mul_i32 s40, s41, 53248\nv_lshlrev_b32 v238, 1, v245\nv_mul_u32_u24 v245, 3328, v245\n"
      "v_xor_b32 v247, v246, v238\nv_add_u32 v245, s40, v245\nv_lshl_add_u32 v236, v247, 4, v245\nv_xor_b32 v238, 128, v236\nv_add_u32 v237, 12288, v236\nv_add_u32 v239, 36864, v238\n"
      "v_add_u32 v238, 24576, v238\ns_lshl_b32 s41, s41, 12\ns_mov_b32 m0, 160512\ns_lshl_b32 s40, s28, 8\ns_add_u32 s39, s27, 2\ns_mul_i32 s39, s39, 212992\ns_add_u32 s39, s39, s40\n"
      "v_lshl_add_u32 v244, v240, 4, s40\nv_lshl_add_u32 v243, v240, 2, s40\ns_mov_b64 exec, 0xffff\ns_cmp_lg_u32 s24, 5\ns_waitcnt lgkmcnt(0)\nglobal_load_lds_dwordx4 v244, s[12:13]\n"
      "s_mov_b64 exec, -1\ns_cbranch_scc1 .Lf4\nglobal_load_dword v253, v243, s[14:15]\n"
      ".Lf4:\ns_add_u32 m0, s41, 73728\ns_add_u32 s42, s10, s39\ns_addc_u32 s43, s11, 0\nglobal_load_lds_dwordx4 v236, s[42:43] offset:0\nglobal_load_lds_dwordx4 v237, s[42:43] offset:1024\n"
      "global_load_lds_dwordx4 v238, s[42:43] offset:2048\nglobal_load_lds_dwordx4 v239, s[42:43] offset:3072\ns_add_u32 m0, s41, 118784\ns_add_u32 s42, s42, 212992\ns_addc_u32 s43, s43, 0\n"
      "global_load_lds_dwordx4 v236, s[42:43] offset:0\nglobal_load_lds_dwordx4 v237, s[42:43] offset:1024\nglobal_load_lds_dwordx4 v238, s[42:43] offset:2048\n"
      "global_load_lds_dwordx4 v239, s[42:43] offset:3072\ns_movk_i32 s39, 13798\ns_cmp_lt_u32 s28, 9\ns_cselect_b32 s39, 13108, s39\ns_mul_i32 s40, s28, 247\ns_and_b32 s40, s40, 1023\n"
      "s_mul_i32 s38, s38, s39\ns_add_u32 s38, s38, s40\ns_lshr_b32 s29, s38, 10\ns_add_u32 s38, s38, s39\ns_lshr_b32 s38, s38, 10\ns_sub_u32 s38, s38, s29\ns_lshl_b32 s38, s38, 1\n"
      "s_sub_u32 s25, s38, 24\ns_add_u32 s27, s27, 2\ns_mul_i32 s36, s36, 5\ns_mov_b32 s38, %[bkl]\ns_mov_b32 s39, %[bkh]\ns_lshr_b64 s[38:39], s[38:39], s36\ns_and_b32 s38, s38, 31\n"
      "s_xor_b32 s31, s38, s37\ns_cbranch_scc0 .Lbld\n"
      ".Lbg:\ns_and_b32 s36, s24, 3\ns_lshl_b32 s42, s2, 2\ns_load_dword s37, s[20:21], s42\ns_load_dword s38, s[18:19], 0\ns_bfe_u32 s42, s2, 0x10002\ns_add_u32 s42, s42, 3\n"
      "s_lshr_b32 s43, s25, 1\ns_sub_u32 s43, 2, s43\ns_cmp_eq_u32 s31, 0\ns_cselect_b32 s42, s42, s43\ns_mul_i32 s42, s42, 5\ns_lshr_b32 s64, %[kp], s42\ns_and_b32 s64, s64, 31\ns_setprio 2\n"
      "s_and_b32 s56, s36, 1\ns_lshr_b32 s57, s36, 1\nv_lshrrev_b32 v1, 4, v240\nv_and_b32 v2, 15, v240\nv_lshlrev_b32 v3, 2, v2\nv_add_u32 v3, 162048, v3\nv_mov_b32 v4, 0\nds_write_b32 v3, v4\n"
      "s_mul_i32 s42, s56, 10240\nv_lshrrev_b32 v3, 1, v2\nv_xor_b32 v3, v3, v1\nv_lshl_add_u32 v4, v2, 7, s42\nv_lshl_add_u32 v96, v3, 4, v4\nv_xor_b32 v97, 64, v96\nv_add_u32 v98, 90112, v96\n"
      "v_add_u32 v99, 90112, v97\ns_cmp_eq_u32 s25, 4\ns_cselect_b32 s42, 4, 5\ns_mul_i32 s42, s42, s56\ns_sub_u32 s42, 12, s42\ns_lshl_b32 s42, s42, 11\ns_lshl_b32 s43, s56, 13\n"
      "s_sub_u32 s43, 20480, s43\nv_add_u32 v100, s42, v96\nv_add_u32 v120, s43, v96\nv_add_u32 v101, s42, v97\nv_add_u32 v121, s43, v97\nv_add_u32 v102, s42, v98\nv_add_u32 v122, s43, v98\n"
      "v_add_u32 v103, s42, v99\nv_add_u32 v123, s43, v99\nv_lshrrev_b32 v3, 2, v2\nv_lshl_add_u32 v4, v1, 3, v3\nv_and_b32 v5, 1, v1\nv_lshl_add_u32 v3, v5, 2, v3\ns_lshl_b32 s42, s36, 1\n"
      "v_xor_b32 v3, s42, v3\nv_and_b32 v5, 3, v2\nv_lshlrev_b32 v5, 3, v5\nv_lshl_add_u32 v4, v4, 8, v5\nv_lshl_add_u32 v4, v3, 5, v4\nv_add_u32 v104, 28672, v4\nv_xor_b32 v105, 32, v104\n"
      "v_xor_b32 v106, 64, v104\nv_xor_b32 v107, 96, v104\nv_add_u32 v108, 90112, v104\nv_add_u32 v109, 90112, v105\nv_add_u32 v110, 90112, v106\nv_add_u32 v111, 90112, v107\n"
      "s_lshl_b32 s42, s57, 10\ns_add_u32 s42, s42, 43008\ns_mul_i32 s43, s56, 320\ns_add_u32 s43, s43, s42\nv_lshl_add_u32 v254, v240, 2, s43\nv_cmp_gt_u32 vcc, 16, v240\ns_lshl_b32 s43, s56, 6\n"
      "s_add_u32 s42, s42, s43\ns_add_u32 s42, s42, 256\nv_min_u32 v5, 2, v1\nv_lshl_add_u32 v255, v2, 2, s42\nv_lshl_add_u32 v255, v5, 7, v255\nv_cndmask_b32 v255, v255, v254, vcc\n"
      "s_lshl_b32 s42, s36, 6\ns_add_u32 s42, s42, 160512\nv_lshl_add_u32 v3, v1, 3, s42\nv_xor_b32 v4, 64, v3\ns_lshl_b32 s42, s36, 7\ns_add_u32 s42, s42, 162816\nv_lshl_add_u32 v236, v1, 4, s42\n"
      "v_xor_b32 v237, 128, v236\ns_cmp_lg_u32 s24, 5\ns_cbranch_scc1 .Lj1063\ns_waitcnt vmcnt(8)\nv_lshlrev_b32 v16, 16, v253\nv_and_b32 v17, 0xffff0000, v253\ns_mov_b32 s42, 0xf000000\n"
      "v_max3_f32 v18, |v16|, |v17|, s42\ns_nop 1\nv_max_u32_dpp v18, v18, v18 quad_perm:[1,0,3,2] row_mask:0xf bank_mask:0xf\ns_nop 1\n"
      "v_max_u32_dpp v18, v18, v18 quad_perm:[2,3,0,1] row_mask:0xf bank_mask:0xf\ns_nop 1\nv_max_u32_dpp v18, v18, v18 row_half_mirror row_mask:0xf bank_mask:0xf\ns_nop 1\n"
      "v_max_u32_dpp v18, v18, v18 row_mirror row_mask:0xf bank_mask:0xf\nv_mov_b32 v19, v18\ns_mov_b32 s42, 162816\nv_lshl_add_u32 v20, v240, 3, s42\nv_permlane16_swap_b32 v18, v19\n"
      "v_max_u32 v18, v18, v19\nv_mov_b32 v19, v18\nv_mov_b32 v21, 162812\ns_nop 0\nv_permlane32_swap_b32 v18, v19\nv_max_u32 v18, v18, v19\nv_and_b32 v18, 0x7f800000, v18\n"
      "v_subrev_u32 v19, 0xc000000, v18\nds_write_b32 v21, v19\nv_lshrrev_b32 v18, 23, v18\nv_sub_u32 v18, 123, v18\nv_ldexp_f32 v16, v16, v18\nv_ldexp_f32 v17, v17, v18\n"
      "s_mov_b32 s42, 0x32000000\nv_max_f32 v18, |v16|, s42\nv_max_f32 v19, |v17|, s42\ns_brev_b32 s42, -2\nv_bfi_b32 v16, s42, v18, v16\nv_bfi_b32 v17, s42, v19, v17\nv_rcp_f32 v16, v16\n"
      "v_rcp_f32 v17, v17\ns_nop 0\nv_mul_f32 v18, 0x31800000, v16\nv_mul_f32 v19, 0x31800000, v17\nds_write2st64_b64 v20, v[16:17], v[18:19] offset1:1\n"
      ".Lj1063:\ns_barrier\ns_waitcnt vmcnt(8)\nds_read_b64 v[8:9], v3 offset:0\nds_read_b64 v[10:11], v3 offset:32\nds_read_b64 v[12:13], v4 offset:0\nds_read_b64 v[14:15], v4 offset:32\n"
      "s_waitcnt lgkmcnt(0)\nv_lshlrev_b32 v124, 16, v8\nv_and_b32 v125, 0xffff0000, v8\nv_lshlrev_b32 v126, 16, v9\nv_and_b32 v127, 0xffff0000, v9\nv_lshlrev_b32 v128, 16, v10\n"
      "v_and_b32 v129, 0xffff0000, v10\nv_lshlrev_b32 v130, 16, v11\nv_and_b32 v131, 0xffff0000, v11\nv_lshlrev_b32 v132, 16, v12\nv_and_b32 v133, 0xffff0000, v12\nv_lshlrev_b32 v134, 16, v13\n"
      "v_and_b32 v135, 0xffff0000, v13\nv_lshlrev_b32 v136, 16, v14\nv_and_b32 v137, 0xffff0000, v14\nv_lshlrev_b32 v138, 16, v15\nv_and_b32 v139, 0xffff0000, v15\n"
      "v_mov_b64 v[140:141], v[124:125]\nv_mov_b64 v[142:143], v[126:127]\nv_mov_b64 v[144:145], v[128:129]\nv_mov_b64 v[146:147], v[130:131]\nv_mov_b64 v[148:149], v[132:133]\n"
      "v_mov_b64 v[150:151], v[134:135]\nv_mov_b64 v[152:153], v[136:137]\nv_mov_b64 v[154:155], v[138:139]\nv_mov_b64 v[156:157], v[124:125]\nv_mov_b64 v[158:159], v[126:127]\n"
      "v_mov_b64 v[160:161], v[128:129]\nv_mov_b64 v[162:163], v[130:131]\nv_mov_b64 v[164:165], v[132:133]\nv_mov_b64 v[166:167], v[134:135]\nv_mov_b64 v[168:169], v[136:137]\n"
      "v_mov_b64 v[170:171], v[138:139]\nv_mov_b64 v[172:173], v[124:125]\nv_mov_b64 v[174:175], v[126:127]\nv_mov_b64 v[176:177], v[128:129]\nv_mov_b64 v[178:179], v[130:131]\n"
      "v_mov_b64 v[180:181], v[132:133]\nv_mov_b64 v[182:183], v[134:135]\nv_mov_b64 v[184:185], v[136:137]\nv_mov_b64 v[186:187], v[138:139]\nv_mov_b64 v[188:189], v[124:125]\n"
      "v_mov_b64 v[190:191], v[126:127]\nv_mov_b64 v[192:193], v[128:129]\nv_mov_b64 v[194:195], v[130:131]\nv_mov_b64 v[196:197], v[132:133]\nv_mov_b64 v[198:199], v[134:135]\n"
      "v_mov_b64 v[200:201], v[136:137]\nv_mov_b64 v[202:203], v[138:139]\nv_mov_b64 v[204:205], v[124:125]\nv_mov_b64 v[206:207], v[126:127]\nv_mov_b64 v[208:209], v[128:129]\n"
      "v_mov_b64 v[210:211], v[130:131]\nv_mov_b64 v[212:213], v[132:133]\nv_mov_b64 v[214:215], v[134:135]\nv_mov_b64 v[216:217], v[136:137]\nv_mov_b64 v[218:219], v[138:139]\n"
      "v_mov_b64 v[220:221], v[124:125]\nv_mov_b64 v[222:223], v[126:127]\nv_mov_b64 v[224:225], v[128:129]\nv_mov_b64 v[226:227], v[130:131]\nv_mov_b64 v[228:229], v[132:133]\n"
      "v_mov_b64 v[230:231], v[134:135]\nv_mov_b64 v[232:233], v[136:137]\nv_mov_b64 v[234:235], v[138:139]\ns_mov_b32 s58, 4\ns_cmp_eq_u32 s25, 2\ns_cbranch_scc1 .Lg13\ns_cmp_eq_u32 s25, 0\n"
      "s_cbranch_scc1 .Lg12\ns_branch .Lg14\n"
      ".Lbld:\n"
      : "=&{v240}"(lane), "=&{v253}"(tch), "=&{s[4:19]}"(ka0), "=&{s[20:23]}"(ka1), "=&{s[24:31]}"(gs), "+{s2}"(blk)
      : [ka] "s"(__builtin_amdgcn_kernarg_segment_ptr()), [tid] "v"(threadIdx.x), [bkl] "i"((unsigned)BK), [bkh] "i"((unsigned)(BK >> 32)), [kp] "i"(KP[0] | KP[1] << 5 | KP[2] << 10 | KP[3] << 15 | KP[4] << 20)
      : "memory", "scc", "s36", "s37", "s38", "s39", "s40", "s41", "s42", "s43", "v236", "v237", "v238", "v239", "v241", "v242", "v243", "v244", "v245", "v246", "v247");
#define SB __builtin_amdgcn_sched_barrier(0)
#define PIN(x) asm volatile("" : "+v"(x))
  const int w = gs[0], tid = w * 64 + lane;
  ws_cin = (const u16*)((unsigned long long)ka0[5] << 32 | ka0[4]);
  wout = (const u16*)((unsigned long long)ka0[13] << 32 | ka0[12]);
  ws = (char*)((unsigned long long)ka1[1] << 32 | ka1[0]);
  const unsigned sb = (unsigned)(size_t)(__attribute__((address_space(3))) char*)shm;
  const int g = lane >> 4, fr = lane & 15, hf = lane >> 5, l31 = lane & 31;
  constexpr unsigned TX = 0xA5C30F96u;
  auto chk = [](unsigned t) -> unsigned { return (t * 0x9E3779B1u) >> 24; };
  const int bid = blk & 7;
  const bool rblk = bid >= 4;
  const int fi = (bid & 3) * 8 + w, mi_ = min(fi, NF - 1);
  const unsigned tc = sb + (g & 1) * 32 + (fr & 3) * 8, h0 = 8 * (g >> 1) + (fr >> 2);
  u32x4 mrow;
  u32x2 rsp;
  unsigned msc;
  {
    unsigned ma_, ur[2], uw[6], fa[2][2];
    if (rblk) {
      const unsigned o3 = min(w * 4480, NF * NF * NF * 2 - 4480);
      asm volatile("s_mov_b32 m0, %2\n\ts_nop 0\n\tglobal_load_lds_dwordx4 %0, %1\n\tglobal_load_lds_dwordx4 %0, %1 offset:1024\n\tglobal_load_lds_dwordx4 %0, %1 offset:2048\n\t"
                   "global_load_lds_dwordx4 %0, %1 offset:3072\n\tglobal_load_lds_dwordx4 %0, %1 offset:3456"
                   :: "v"(lane * 16), "s"((const char*)(ws_cin + 2 * NF * NF * NF) + o3), "s"(sb + W3S + o3) : "memory");
      const unsigned rr = (bid & 3) * 8 + ((lane >> 2) & 7);
      const unsigned vo = (hf * NF * NF + min(rr, 25u) * NF) * 2 + (lane & 3) * 16;
#pragma unroll
      for (int m = 0; m < 2; ++m) {
        const unsigned p = min(w + 8 * m, 12);
        asm volatile("s_mov_b32 m0, %2\n\ts_nop 0\n\tglobal_load_lds_dwordx4 %0, %1" :: "v"(vo), "s"(ws_cin + (NF + 2 * p) * NF * NF), "s"(sb + W2S + p * 1024) : "memory");
      }
      asm volatile("ds_write_b32 %0, %1" :: "v"(sb + U_OFF + tid * 4), "v"(0) : "memory");
    } else {
#pragma unroll
      for (int s = 0; s < 2; ++s)
        asm volatile("s_mov_b32 m0, %2\n\ts_nop 0\n\tglobal_load_lds_dwordx4 %0, %1" :: "v"(min(l31, NF - 1) * NF * 2 + hf * 16), "s"(ws_cin + mi_ * NF * NF + s * 16), "s"(sb + W3S + w * DP + s * 1024) : "memory");
    }
    if (w < 3) asm volatile("s_mov_b32 m0, %2\n\ts_nop 0\n\tglobal_load_lds_dwordx4 %0, %1 sc1" :: "v"(lane * 16), "s"(w ? (const char*)&g_tab + (w - 1) * 1024 : ws), "s"(sb + NC_OFF + w * 1024) : "memory");
    if (w == 3) asm volatile("s_mov_b32 m0, %2\n\ts_nop 0\n\tglobal_load_lds_dword %0, %1" :: "v"(min(lane, 38) * 4), "s"(wout), "s"(sb + WO_OFF) : "memory");
    {
      const int ty = bid >> 2, f = lane >> 2;
      const bool sp = fi >= 24;
      int c0 = sp ? 5 : fi < 16 ? ty - 1 : 1, c1 = sp ? 16 : 3072, rho = sp ? 4 * (fi - 24) + 2 * ty : fi < 16 ? fi : 2 * (fi & 6) + (fi & 1) + 2 * ty;
      asm volatile("" : "+s"(c0), "+s"(c1), "+s"(rho));
      int t = (7 * (f >> 1) + c0) * 1024 + (f & 1) * c1 + ((lane & 3) * 16 + rho) * 16;
      PIN(t);
      ma_ = min((unsigned)t | (fi < 28 ? 0u : ~0u), 41u * 1024 + 1024);
      ma_ = lane < 48 ? ma_ : 43008;
    }
#pragma unroll
    for (int p = 0; p < 2; ++p) {
      ur[p] = sb + W3S + min(8 * g + 4 * p + (fr >> 2), NF - 1) * (NF * NF * 2) + w * 32 + (fr & 3) * 8;
      fa[0][p] = tc + W2S + ((h0 + 4 * p) * 8 + w) * 64; fa[1][p] = tc + W2S + (min(h0 + 16 + 4 * p, 25u) * 8 + w) * 64;
      PIN(ur[p]); PIN(fa[0][p]); PIN(fa[1][p]);
    }
#pragma unroll
    for (int m = 0; m < 6; ++m) {
      const unsigned e = (w + 8 * m) * 16 + fr;
      uw[m] = sb + U_OFF + e * 2 + ((e * 2521u) >> 16) * 12;
      PIN(uw[m]);
      if (m == 5) uw[m] = e < NF * NF ? uw[m] : 0x40000u;
    }
    PIN(uw[5]); PIN(ma_);
    asm volatile("s_waitcnt vmcnt(0) lgkmcnt(0)\n\ts_barrier" :: "{v253}"(tch) : "memory");
    const unsigned wov = *(const unsigned*)(shm + WO_OFF + lane * 4);
    const u32x4 tb = *(const u32x4*)(shm + GT_OFF + (fi >= 24) * 1024 + lane * 16);
    bf16x8 P[2], Q[2];
    if (rblk) {
      s16x4 wl[6][2];
      u32x4 wa;
      unsigned wo2;
      asm volatile("ds_read2_b64 %0, %1 offset0:13 offset1:14" : "=v"(wa) : "v"(sb + WO_OFF + g * 16));
      asm volatile("ds_read_u16 %0, %1 offset:52" : "=v"(wo2) : "v"(sb + WO_OFF + lane * 2));
#pragma unroll
      for (int m = 0; m < 6; ++m)
#pragma unroll
        for (int p = 0; p < 2; ++p) asm volatile("ds_read_b64_tr_b16 %0, %1 offset:%2" : "=v"(wl[m][p]) : "v"(ur[p]), "i"(m * 256));
      u32x2 fl[2][2];
#pragma unroll
      for (int s = 0; s < 2; ++s)
#pragma unroll
        for (int p = 0; p < 2; ++p) asm volatile("ds_read_b64_tr_b16 %0, %1" : "=v"(fl[s][p]) : "v"(fa[s][p]));
      asm volatile("s_waitcnt lgkmcnt(0)" : "+v"(wa), "+v"(wo2), "+v"(wl[0][0]), "+v"(wl[0][1]), "+v"(wl[1][0]), "+v"(wl[1][1]), "+v"(wl[2][0]), "+v"(wl[2][1]), "+v"(wl[3][0]), "+v"(wl[3][1]),
                   "+v"(wl[4][0]), "+v"(wl[4][1]), "+v"(wl[5][0]), "+v"(wl[5][1]), "+v"(fl[0][0]), "+v"(fl[0][1]), "+v"(fl[1][0]), "+v"(fl[1][1]));
      const unsigned km = g < 3 ? ~0u : 0u;
      const u32x4 wm = {wa[0], wa[1] & km, wa[2] & km, wa[3] & km};
      f32x4 du[6];
#pragma unroll
      for (int m = 0; m < 6; ++m)
        du[m] = __builtin_amdgcn_mfma_f32_16x16x32_bf16(__builtin_bit_cast(bf16x8, wm), __builtin_shufflevector(wl[m][0], wl[m][1], 0, 1, 2, 3, 4, 5, 6, 7), (f32x4){0.f, 0.f, 0.f, 0.f}, 0, 0, 0);
      asm volatile("ds_write_b16 %0, %1" :: "v"(lane < NF ? sb + U_OFF + lane * 64 + 52 : 0x40000u), "v"(wo2) : "memory");
#pragma unroll
      for (int m = 0; m < 6; ++m) {
        asm volatile("ds_write_b16 %0, %1" :: "v"(uw[m]), "v"(pk2(du[m][0], du[m][0])) : "memory");
        asm volatile("" :: "v"(du[m]));
      }
      asm volatile("s_waitcnt lgkmcnt(0)\n\ts_barrier" ::: "memory");
      s16x4 ul[2][2];
#pragma unroll
      for (int s = 0; s < 2; ++s)
#pragma unroll
        for (int p = 0; p < 2; ++p)
          asm volatile("ds_read_b64_tr_b16 %0, %1 offset:%2" : "=v"(ul[s][p]) : "v"(tc + U_OFF + h0 * 64), "i"(s * 1024 + p * 256));
      const unsigned fmask = l31 < NF ? ~0u : 0u;
#pragma unroll
      for (int s = 0; s < 2; ++s) {
        u32x4 q4 = {fl[s][0][0] & fmask, fl[s][0][1] & fmask, fl[s][1][0] & fmask, fl[s][1][1] & fmask};
        Q[s] = __builtin_bit_cast(bf16x8, q4);
      }
      asm volatile("s_waitcnt lgkmcnt(0)" : "+v"(ul[0][0]), "+v"(ul[0][1]), "+v"(ul[1][0]), "+v"(ul[1][1]));
#pragma unroll
      for (int s = 0; s < 2; ++s) P[s] = __builtin_shufflevector(ul[s][0], ul[s][1], 0, 1, 2, 3, 4, 5, 6, 7);
    } else {
#pragma unroll
      for (int s = 0; s < 2; ++s) {
        u32x4 xf = *(const u32x4*)(shm + W3S + w * DP + s * 1024 + lane * 16);
        if (s == 1) { const unsigned m = hf ? 0u : ~0u; xf[1] &= m; xf[2] &= m; xf[3] &= m; }
        Q[s] = __builtin_bit_cast(bf16x8, xf);
        const unsigned e = (l31 >> 1) - 8 * s - 4 * hf, v = 0x3f80u << ((lane & 1) * 16);
        u32x4 q4 = {e == 0 ? v : 0u, e == 1 ? v : 0u, e == 2 ? v : 0u, e == 3 ? v : 0u};
        P[s] = __builtin_bit_cast(bf16x8, q4);
      }
    }
    f32x16 D = {};
#pragma unroll
    for (int s = 0; s < 2; ++s) {
      D = __builtin_amdgcn_mfma_f32_32x32x16_bf16(P[s], Q[s], D, 0, 0, 0);
      D = __builtin_amdgcn_mfma_f32_32x32x16_bf16(Q[s], P[s], D, 0, 0, 0);
    }
    float* dl = (float*)(shm + W3S + w * DP);
    D[14] = lane == NF ? __uint_as_float(((unsigned)__builtin_amdgcn_readlane(wov, mi_ >> 1) >> ((mi_ & 1) * 16)) << 16) : D[14];
#pragma unroll
    for (int r = 0; r < 15; ++r) dl[((r & 3) + 8 * (r >> 2) + 4 * hf) * 33 + l31] = D[r];
    __builtin_amdgcn_wave_barrier();
    float v[8], mx = 0.f;
    const unsigned zm = fi >= NF ? 0xffff : rblk ? 0x8000 : 0xc000;
#pragma unroll
    for (int e = 0; e < 8; ++e) v[e] = dl[(tb[e >> 1] >> (16 * (e & 1))) & 0x3ff];
#pragma unroll
    for (int e = 0; e < 8; ++e) {
      const unsigned c = tb[e >> 1] >> (16 * (e & 1));
      float x = __builtin_amdgcn_ldexpf(v[e], ((c >> 10) & 15) - 2);
      PIN(x);
      v[e] = c & zm ? 0.f : x;
      mx = fmaxf(mx, fabsf(v[e]));
    }
    mx = fmaxf(mx, dpp<0xB1>(mx)); mx = fmaxf(mx, dpp<0x4E>(mx)); mx = fmaxf(mx, dpp<0x141>(mx)); mx = fmaxf(mx, dpp<0x140>(mx));
    const unsigned mxu = max(max((unsigned)__builtin_amdgcn_readlane(__float_as_int(mx), 0), (unsigned)__builtin_amdgcn_readlane(__float_as_int(mx), 16)), (unsigned)__builtin_amdgcn_readlane(__float_as_int(mx), 32));
    const unsigned ex = min(max(mxu >> 23, 40u), 250u);
    const float al = __uint_as_float((268 - ex) << 23);
#pragma unroll
    for (int e = 0; e < 4; ++e) {
      const unsigned q = pk2(v[2 * e] * al, v[2 * e + 1] * al);
      mrow[e] = __builtin_bit_cast(unsigned, __builtin_amdgcn_cvt_pkrtz(__uint_as_float(q << 16), __uint_as_float(q & 0xffff0000u)));
    }
    msc = fi < NF ? (ex - 14) << 23 : 0u;
    PIN(msc);
    asm volatile("global_store_dwordx4 %0, %1, %2 sc1" :: "v"(ma_), "{v[248:251]}"(mrow), "s"(ws + WS_M) : "memory");
    asm volatile("global_store_dword %0, %1, %2 sc1" :: "v"((fi < 16 ? (fi >> 2) * 64 + (fi & 3) * 4 : fi < 24 ? ((fi & 6) << 5) + (fi & 1) * 4 + 16 : (fi & 3) * 64 + (fi & 4) + 24) + (bid & 4) * 8), "{v252}"(msc), "s"(ws + WS_M + MC) : "memory");
    if (fi == 31) asm volatile("global_store_dword %0, %1, %2 sc1\n\ts_waitcnt vmcnt(0)" :: "v"((lane & 15) * 128), "{v252}"(msc), "s"(ws + WS_POOL) : "memory");
    const unsigned mval = *(const unsigned*)(shm + NC_OFF + (tid & 255) * 4), qval = *(const unsigned*)(shm + NC_OFF);
    rsp = (u32x2){qval << 8 | chk(mval), mval ^ TX};
    PIN(rsp);
    asm volatile("s_waitcnt lgkmcnt(0)\n\ts_barrier" ::: "memory");
  }
  static_assert(STAGE == 45056 && MC == 41984 && NSLOT == 21 && WS_RESP == 4096 && WS_POOL == 32768 && WS_M == 1048576 && NCL == 8 && sizeof(g_tab.t) == 2048, "constants of the code below");
  asm volatile(
      "s_cmp_lt_u32 s24, 4\ns_cbranch_scc1 .Lbp\ns_branch .Lbg\n"
      ".Lg14:\ns_waitcnt vmcnt(4)\ns_barrier\nds_read_b64_tr_b16 v[64:65], v104 offset:45056\nds_read_b64_tr_b16 v[66:67], v104 offset:46080\nds_read_b64_tr_b16 v[68:69], v104 offset:53248\n"
      "ds_read_b64_tr_b16 v[70:71], v104 offset:54272\nds_read_b64_tr_b16 v[72:73], v105 offset:45056\nds_read_b64_tr_b16 v[74:75], v105 offset:46080\n"
      "ds_read_b64_tr_b16 v[76:77], v105 offset:53248\nds_read_b64_tr_b16 v[78:79], v105 offset:54272\nds_read_b64_tr_b16 v[80:81], v106 offset:45056\n"
      "ds_read_b64_tr_b16 v[82:83], v106 offset:46080\nds_read_b64_tr_b16 v[84:85], v106 offset:53248\nds_read_b64_tr_b16 v[86:87], v106 offset:54272\n"
      "ds_read_b64_tr_b16 v[88:89], v107 offset:45056\nds_read_b64_tr_b16 v[90:91], v107 offset:46080\nds_read_b64_tr_b16 v[92:93], v107 offset:53248\n"
      "ds_read_b64_tr_b16 v[94:95], v107 offset:54272\nds_read_b128 v[0:3], v100 offset:45056\nds_read_b128 v[4:7], v101 offset:45056\nds_read_b128 v[8:11], v120 offset:45056\n"
      "ds_read_b128 v[12:15], v121 offset:45056\nds_read_b128 v[16:19], v96 offset:45056\nds_read_b128 v[20:23], v97 offset:45056\ns_waitcnt lgkmcnt(2)\n.p2align 3\n"
      ".Lgl14:\nv_mfma_f32_16x16x32_bf16 v[124:127], v[64:67], v[0:3], v[124:127]\nds_read_b128 v[24:27], v96 offset:47104\nv_mfma_f32_16x16x32_bf16 v[124:127], v[68:71], v[4:7], v[124:127]\n"
      "ds_read_b128 v[28:31], v97 offset:47104\nv_mfma_f32_16x16x32_bf16 v[128:131], v[76:79], v[4:7], v[128:131]\nv_mfma_f32_16x16x32_bf16 v[128:131], v[72:75], v[0:3], v[128:131]\n"
      "v_mfma_f32_16x16x32_bf16 v[132:135], v[80:83], v[0:3], v[132:135]\nv_mfma_f32_16x16x32_bf16 v[132:135], v[84:87], v[4:7], v[132:135]\n"
      "v_mfma_f32_16x16x32_bf16 v[136:139], v[92:95], v[4:7], v[136:139]\nv_mfma_f32_16x16x32_bf16 v[136:139], v[88:91], v[0:3], v[136:139]\n"
      "v_mfma_f32_16x16x32_bf16 v[152:155], v[88:91], v[8:11], v[152:155]\nds_read_b128 v[0:3], v96 offset:49152\nv_mfma_f32_16x16x32_bf16 v[152:155], v[92:95], v[12:15], v[152:155]\n"
      "ds_read_b128 v[4:7], v97 offset:49152\nv_mfma_f32_16x16x32_bf16 v[148:151], v[84:87], v[12:15], v[148:151]\nv_mfma_f32_16x16x32_bf16 v[148:151], v[80:83], v[8:11], v[148:151]\n"
      "v_mfma_f32_16x16x32_bf16 v[144:147], v[72:75], v[8:11], v[144:147]\nv_mfma_f32_16x16x32_bf16 v[144:147], v[76:79], v[12:15], v[144:147]\n"
      "v_mfma_f32_16x16x32_bf16 v[140:143], v[68:71], v[12:15], v[140:143]\ns_waitcnt lgkmcnt(4)\nv_mfma_f32_16x16x32_bf16 v[140:143], v[64:67], v[8:11], v[140:143]\n"
      "v_mfma_f32_16x16x32_bf16 v[156:159], v[64:67], v[16:19], v[156:159]\nds_read_b128 v[8:11], v96 offset:51200\nv_mfma_f32_16x16x32_bf16 v[156:159], v[68:71], v[20:23], v[156:159]\n"
      "ds_read_b128 v[12:15], v97 offset:51200\nv_mfma_f32_16x16x32_bf16 v[160:163], v[76:79], v[20:23], v[160:163]\nv_mfma_f32_16x16x32_bf16 v[160:163], v[72:75], v[16:19], v[160:163]\n"
      "v_mfma_f32_16x16x32_bf16 v[164:167], v[80:83], v[16:19], v[164:167]\nv_mfma_f32_16x16x32_bf16 v[164:167], v[84:87], v[20:23], v[164:167]\n"
      "v_mfma_f32_16x16x32_bf16 v[168:171], v[92:95], v[20:23], v[168:171]\ns_waitcnt lgkmcnt(2)\nv_mfma_f32_16x16x32_bf16 v[168:171], v[88:91], v[16:19], v[168:171]\n"
      "v_mfma_f32_16x16x32_bf16 v[184:187], v[88:91], v[24:27], v[184:187]\nds_read_b128 v[16:19], v96 offset:53248\nv_mfma_f32_16x16x32_bf16 v[184:187], v[92:95], v[28:31], v[184:187]\n"
      "ds_read_b128 v[20:23], v97 offset:53248\nv_mfma_f32_16x16x32_bf16 v[180:183], v[84:87], v[28:31], v[180:183]\nv_mfma_f32_16x16x32_bf16 v[180:183], v[80:83], v[24:27], v[180:183]\n"
      "v_mfma_f32_16x16x32_bf16 v[176:179], v[72:75], v[24:27], v[176:179]\nv_mfma_f32_16x16x32_bf16 v[176:179], v[76:79], v[28:31], v[176:179]\n"
      "v_mfma_f32_16x16x32_bf16 v[172:175], v[68:71], v[28:31], v[172:175]\ns_waitcnt vmcnt(0) lgkmcnt(0)\ns_barrier\nv_mfma_f32_16x16x32_bf16 v[172:175], v[64:67], v[24:27], v[172:175]\n"
      "ds_read_b64_tr_b16 v[32:33], v108 offset:0\nv_mfma_f32_16x16x32_bf16 v[188:191], v[64:67], v[0:3], v[188:191]\nds_read_b64_tr_b16 v[34:35], v108 offset:1024\n"
      "v_mfma_f32_16x16x32_bf16 v[188:191], v[68:71], v[4:7], v[188:191]\nds_read_b64_tr_b16 v[36:37], v108 offset:8192\nv_mfma_f32_16x16x32_bf16 v[192:195], v[76:79], v[4:7], v[192:195]\n"
      "ds_read_b64_tr_b16 v[38:39], v108 offset:9216\nv_mfma_f32_16x16x32_bf16 v[192:195], v[72:75], v[0:3], v[192:195]\nds_read_b64_tr_b16 v[40:41], v109 offset:0\n"
      "v_mfma_f32_16x16x32_bf16 v[196:199], v[80:83], v[0:3], v[196:199]\nds_read_b64_tr_b16 v[42:43], v109 offset:1024\nv_mfma_f32_16x16x32_bf16 v[196:199], v[84:87], v[4:7], v[196:199]\n"
      "ds_read_b64_tr_b16 v[44:45], v109 offset:8192\nv_mfma_f32_16x16x32_bf16 v[200:203], v[92:95], v[4:7], v[200:203]\nds_read_b64_tr_b16 v[46:47], v109 offset:9216\n"
      "v_mfma_f32_16x16x32_bf16 v[200:203], v[88:91], v[0:3], v[200:203]\nds_read_b64_tr_b16 v[48:49], v110 offset:0\nv_mfma_f32_16x16x32_bf16 v[216:219], v[88:91], v[8:11], v[216:219]\n"
      "ds_read_b128 v[0:3], v102 offset:0\nv_mfma_f32_16x16x32_bf16 v[216:219], v[92:95], v[12:15], v[216:219]\nds_read_b128 v[4:7], v103 offset:0\n"
      "v_mfma_f32_16x16x32_bf16 v[212:215], v[84:87], v[12:15], v[212:215]\nds_read_b64_tr_b16 v[50:51], v110 offset:1024\nv_mfma_f32_16x16x32_bf16 v[212:215], v[80:83], v[8:11], v[212:215]\n"
      "ds_read_b64_tr_b16 v[52:53], v110 offset:8192\nv_mfma_f32_16x16x32_bf16 v[208:211], v[72:75], v[8:11], v[208:211]\nds_read_b64_tr_b16 v[54:55], v110 offset:9216\n"
      "v_mfma_f32_16x16x32_bf16 v[208:211], v[76:79], v[12:15], v[208:211]\nds_read_b64_tr_b16 v[56:57], v111 offset:0\nv_mfma_f32_16x16x32_bf16 v[204:207], v[68:71], v[12:15], v[204:207]\n"
      "ds_read_b64_tr_b16 v[58:59], v111 offset:1024\nv_mfma_f32_16x16x32_bf16 v[204:207], v[64:67], v[8:11], v[204:207]\nds_read_b64_tr_b16 v[60:61], v111 offset:8192\n"
      "v_mfma_f32_16x16x32_bf16 v[220:223], v[64:67], v[16:19], v[220:223]\nds_read_b128 v[8:11], v122 offset:0\nv_mfma_f32_16x16x32_bf16 v[220:223], v[68:71], v[20:23], v[220:223]\n"
      "ds_read_b128 v[12:15], v123 offset:0\nv_mfma_f32_16x16x32_bf16 v[224:227], v[76:79], v[20:23], v[224:227]\nds_read_b64_tr_b16 v[62:63], v111 offset:9216\n"
      "v_mfma_f32_16x16x32_bf16 v[224:227], v[72:75], v[16:19], v[224:227]\nv_mfma_f32_16x16x32_bf16 v[228:231], v[80:83], v[16:19], v[228:231]\n"
      "v_mfma_f32_16x16x32_bf16 v[228:231], v[84:87], v[20:23], v[228:231]\nv_mfma_f32_16x16x32_bf16 v[232:235], v[92:95], v[20:23], v[232:235]\ns_waitcnt lgkmcnt(0)\n"
      "v_mfma_f32_16x16x32_bf16 v[232:235], v[88:91], v[16:19], v[232:235]\nds_read_b128 v[16:19], v98 offset:0\nds_read_b128 v[20:23], v99 offset:0\n"
      "v_mfma_f32_16x16x32_bf16 v[124:127], v[32:35], v[0:3], v[124:127]\nds_read_b128 v[24:27], v98 offset:2048\nv_mfma_f32_16x16x32_bf16 v[124:127], v[36:39], v[4:7], v[124:127]\n"
      "ds_read_b128 v[28:31], v99 offset:2048\nv_mfma_f32_16x16x32_bf16 v[128:131], v[44:47], v[4:7], v[128:131]\nv_mfma_f32_16x16x32_bf16 v[128:131], v[40:43], v[0:3], v[128:131]\n"
      "v_mfma_f32_16x16x32_bf16 v[132:135], v[48:51], v[0:3], v[132:135]\nv_mfma_f32_16x16x32_bf16 v[132:135], v[52:55], v[4:7], v[132:135]\n"
      "v_mfma_f32_16x16x32_bf16 v[136:139], v[60:63], v[4:7], v[136:139]\nv_mfma_f32_16x16x32_bf16 v[136:139], v[56:59], v[0:3], v[136:139]\n"
      "v_mfma_f32_16x16x32_bf16 v[152:155], v[56:59], v[8:11], v[152:155]\nds_read_b128 v[0:3], v98 offset:4096\nv_mfma_f32_16x16x32_bf16 v[152:155], v[60:63], v[12:15], v[152:155]\n"
      "ds_read_b128 v[4:7], v99 offset:4096\nv_mfma_f32_16x16x32_bf16 v[148:151], v[52:55], v[12:15], v[148:151]\nv_mfma_f32_16x16x32_bf16 v[148:151], v[48:51], v[8:11], v[148:151]\n"
      "v_mfma_f32_16x16x32_bf16 v[144:147], v[40:43], v[8:11], v[144:147]\nv_mfma_f32_16x16x32_bf16 v[144:147], v[44:47], v[12:15], v[144:147]\n"
      "v_mfma_f32_16x16x32_bf16 v[140:143], v[36:39], v[12:15], v[140:143]\ns_waitcnt lgkmcnt(4)\nv_mfma_f32_16x16x32_bf16 v[140:143], v[32:35], v[8:11], v[140:143]\n"
      "v_mfma_f32_16x16x32_bf16 v[156:159], v[32:35], v[16:19], v[156:159]\nds_read_b128 v[8:11], v98 offset:6144\nv_mfma_f32_16x16x32_bf16 v[156:159], v[36:39], v[20:23], v[156:159]\n"
      "ds_read_b128 v[12:15], v99 offset:6144\nv_mfma_f32_16x16x32_bf16 v[160:163], v[44:47], v[20:23], v[160:163]\nv_mfma_f32_16x16x32_bf16 v[160:163], v[40:43], v[16:19], v[160:163]\n"
      "v_mfma_f32_16x16x32_bf16 v[164:167], v[48:51], v[16:19], v[164:167]\nv_mfma_f32_16x16x32_bf16 v[164:167], v[52:55], v[20:23], v[164:167]\n"
      "v_mfma_f32_16x16x32_bf16 v[168:171], v[60:63], v[20:23], v[168:171]\ns_waitcnt lgkmcnt(2)\nv_mfma_f32_16x16x32_bf16 v[168:171], v[56:59], v[16:19], v[168:171]\n"
      "v_mfma_f32_16x16x32_bf16 v[184:187], v[56:59], v[24:27], v[184:187]\nds_read_b128 v[16:19], v98 offset:8192\nv_mfma_f32_16x16x32_bf16 v[184:187], v[60:63], v[28:31], v[184:187]\n"
      "ds_read_b128 v[20:23], v99 offset:8192\nv_mfma_f32_16x16x32_bf16 v[180:183], v[52:55], v[28:31], v[180:183]\nv_mfma_f32_16x16x32_bf16 v[180:183], v[48:51], v[24:27], v[180:183]\n"
      "v_mfma_f32_16x16x32_bf16 v[176:179], v[40:43], v[24:27], v[176:179]\nv_mfma_f32_16x16x32_bf16 v[176:179], v[44:47], v[28:31], v[176:179]\n"
      "v_mfma_f32_16x16x32_bf16 v[172:175], v[36:39], v[28:31], v[172:175]\ns_waitcnt vmcnt(0) lgkmcnt(0)\ns_barrier\nv_mfma_f32_16x16x32_bf16 v[172:175], v[32:35], v[24:27], v[172:175]\n"
      "ds_read_b64_tr_b16 v[64:65], v104 offset:0\nv_mfma_f32_16x16x32_bf16 v[188:191], v[32:35], v[0:3], v[188:191]\nds_read_b64_tr_b16 v[66:67], v104 offset:1024\n"
      "v_mfma_f32_16x16x32_bf16 v[188:191], v[36:39], v[4:7], v[188:191]\nds_read_b64_tr_b16 v[68:69], v104 offset:8192\nv_mfma_f32_16x16x32_bf16 v[192:195], v[44:47], v[4:7], v[192:195]\n"
      "ds_read_b64_tr_b16 v[70:71], v104 offset:9216\nv_mfma_f32_16x16x32_bf16 v[192:195], v[40:43], v[0:3], v[192:195]\nds_read_b64_tr_b16 v[72:73], v105 offset:0\n"
      "v_mfma_f32_16x16x32_bf16 v[196:199], v[48:51], v[0:3], v[196:199]\nds_read_b64_tr_b16 v[74:75], v105 offset:1024\nv_mfma_f32_16x16x32_bf16 v[196:199], v[52:55], v[4:7], v[196:199]\n"
      "ds_read_b64_tr_b16 v[76:77], v105 offset:8192\nv_mfma_f32_16x16x32_bf16 v[200:203], v[60:63], v[4:7], v[200:203]\nds_read_b64_tr_b16 v[78:79], v105 offset:9216\n"
      "v_mfma_f32_16x16x32_bf16 v[200:203], v[56:59], v[0:3], v[200:203]\nds_read_b64_tr_b16 v[80:81], v106 offset:0\nv_mfma_f32_16x16x32_bf16 v[216:219], v[56:59], v[8:11], v[216:219]\n"
      "ds_read_b128 v[0:3], v100 offset:0\nv_mfma_f32_16x16x32_bf16 v[216:219], v[60:63], v[12:15], v[216:219]\nds_read_b128 v[4:7], v101 offset:0\n"
      "v_mfma_f32_16x16x32_bf16 v[212:215], v[52:55], v[12:15], v[212:215]\nds_read_b64_tr_b16 v[82:83], v106 offset:1024\nv_mfma_f32_16x16x32_bf16 v[212:215], v[48:51], v[8:11], v[212:215]\n"
      "ds_read_b64_tr_b16 v[84:85], v106 offset:8192\nv_mfma_f32_16x16x32_bf16 v[208:211], v[40:43], v[8:11], v[208:211]\nds_read_b64_tr_b16 v[86:87], v106 offset:9216\n"
      "v_mfma_f32_16x16x32_bf16 v[208:211], v[44:47], v[12:15], v[208:211]\nds_read_b64_tr_b16 v[88:89], v107 offset:0\nv_mfma_f32_16x16x32_bf16 v[204:207], v[36:39], v[12:15], v[204:207]\n"
      "ds_read_b64_tr_b16 v[90:91], v107 offset:1024\nv_mfma_f32_16x16x32_bf16 v[204:207], v[32:35], v[8:11], v[204:207]\nds_read_b64_tr_b16 v[92:93], v107 offset:8192\n"
      "v_mfma_f32_16x16x32_bf16 v[220:223], v[32:35], v[16:19], v[220:223]\nds_read_b128 v[8:11], v120 offset:0\nv_mfma_f32_16x16x32_bf16 v[220:223], v[36:39], v[20:23], v[220:223]\n"
      "ds_read_b128 v[12:15], v121 offset:0\nv_mfma_f32_16x16x32_bf16 v[224:227], v[44:47], v[20:23], v[224:227]\nds_read_b64_tr_b16 v[94:95], v107 offset:9216\n"
      "v_mfma_f32_16x16x32_bf16 v[224:227], v[40:43], v[16:19], v[224:227]\nv_mfma_f32_16x16x32_bf16 v[228:231], v[48:51], v[16:19], v[228:231]\n"
      "v_mfma_f32_16x16x32_bf16 v[228:231], v[52:55], v[20:23], v[228:231]\nv_mfma_f32_16x16x32_bf16 v[232:235], v[60:63], v[20:23], v[232:235]\ns_waitcnt lgkmcnt(0)\n"
      "v_mfma_f32_16x16x32_bf16 v[232:235], v[56:59], v[16:19], v[232:235]\nds_read_b128 v[16:19], v96 offset:0\nds_read_b128 v[20:23], v97 offset:0\n"
      "v_mfma_f32_16x16x32_bf16 v[124:127], v[64:67], v[0:3], v[124:127]\nds_read_b128 v[24:27], v96 offset:2048\nv_mfma_f32_16x16x32_bf16 v[124:127], v[68:71], v[4:7], v[124:127]\n"
      "ds_read_b128 v[28:31], v97 offset:2048\nv_mfma_f32_16x16x32_bf16 v[128:131], v[76:79], v[4:7], v[128:131]\nv_mfma_f32_16x16x32_bf16 v[128:131], v[72:75], v[0:3], v[128:131]\n"
      "v_mfma_f32_16x16x32_bf16 v[132:135], v[80:83], v[0:3], v[132:135]\nv_mfma_f32_16x16x32_bf16 v[132:135], v[84:87], v[4:7], v[132:135]\n"
      "v_mfma_f32_16x16x32_bf16 v[136:139], v[92:95], v[4:7], v[136:139]\nv_mfma_f32_16x16x32_bf16 v[136:139], v[88:91], v[0:3], v[136:139]\n"
      "v_mfma_f32_16x16x32_bf16 v[152:155], v[88:91], v[8:11], v[152:155]\nds_read_b128 v[0:3], v96 offset:4096\nv_mfma_f32_16x16x32_bf16 v[152:155], v[92:95], v[12:15], v[152:155]\n"
      "ds_read_b128 v[4:7], v97 offset:4096\nv_mfma_f32_16x16x32_bf16 v[148:151], v[84:87], v[12:15], v[148:151]\nv_mfma_f32_16x16x32_bf16 v[148:151], v[80:83], v[8:11], v[148:151]\n"
      "v_mfma_f32_16x16x32_bf16 v[144:147], v[72:75], v[8:11], v[144:147]\nv_mfma_f32_16x16x32_bf16 v[144:147], v[76:79], v[12:15], v[144:147]\n"
      "v_mfma_f32_16x16x32_bf16 v[140:143], v[68:71], v[12:15], v[140:143]\ns_waitcnt lgkmcnt(4)\nv_mfma_f32_16x16x32_bf16 v[140:143], v[64:67], v[8:11], v[140:143]\n"
      "v_mfma_f32_16x16x32_bf16 v[156:159], v[64:67], v[16:19], v[156:159]\nds_read_b128 v[8:11], v96 offset:6144\nv_mfma_f32_16x16x32_bf16 v[156:159], v[68:71], v[20:23], v[156:159]\n"
      "ds_read_b128 v[12:15], v97 offset:6144\nv_mfma_f32_16x16x32_bf16 v[160:163], v[76:79], v[20:23], v[160:163]\nv_mfma_f32_16x16x32_bf16 v[160:163], v[72:75], v[16:19], v[160:163]\n"
      "v_mfma_f32_16x16x32_bf16 v[164:167], v[80:83], v[16:19], v[164:167]\nv_mfma_f32_16x16x32_bf16 v[164:167], v[84:87], v[20:23], v[164:167]\n"
      "v_mfma_f32_16x16x32_bf16 v[168:171], v[92:95], v[20:23], v[168:171]\ns_waitcnt lgkmcnt(2)\nv_mfma_f32_16x16x32_bf16 v[168:171], v[88:91], v[16:19], v[168:171]\n"
      "v_mfma_f32_16x16x32_bf16 v[184:187], v[88:91], v[24:27], v[184:187]\nds_read_b128 v[16:19], v96 offset:8192\nv_mfma_f32_16x16x32_bf16 v[184:187], v[92:95], v[28:31], v[184:187]\n"
      "ds_read_b128 v[20:23], v97 offset:8192\nv_mfma_f32_16x16x32_bf16 v[180:183], v[84:87], v[28:31], v[180:183]\nv_mfma_f32_16x16x32_bf16 v[180:183], v[80:83], v[24:27], v[180:183]\n"
      "v_mfma_f32_16x16x32_bf16 v[176:179], v[72:75], v[24:27], v[176:179]\nv_mfma_f32_16x16x32_bf16 v[176:179], v[76:79], v[28:31], v[176:179]\n"
      "v_mfma_f32_16x16x32_bf16 v[172:175], v[68:71], v[28:31], v[172:175]\ns_waitcnt vmcnt(0) lgkmcnt(0)\ns_barrier\nv_mfma_f32_16x16x32_bf16 v[172:175], v[64:67], v[24:27], v[172:175]\n"
      "ds_read_b64_tr_b16 v[32:33], v104 offset:45056\nv_mfma_f32_16x16x32_bf16 v[188:191], v[64:67], v[0:3], v[188:191]\nds_read_b64_tr_b16 v[34:35], v104 offset:46080\n"
      "v_mfma_f32_16x16x32_bf16 v[188:191], v[68:71], v[4:7], v[188:191]\nds_read_b64_tr_b16 v[36:37], v104 offset:53248\nv_mfma_f32_16x16x32_bf16 v[192:195], v[76:79], v[4:7], v[192:195]\n"
      "ds_read_b64_tr_b16 v[38:39], v104 offset:54272\nv_mfma_f32_16x16x32_bf16 v[192:195], v[72:75], v[0:3], v[192:195]\nds_read_b64_tr_b16 v[40:41], v105 offset:45056\n"
      "v_mfma_f32_16x16x32_bf16 v[196:199], v[80:83], v[0:3], v[196:199]\nds_read_b64_tr_b16 v[42:43], v105 offset:46080\nv_mfma_f32_16x16x32_bf16 v[196:199], v[84:87], v[4:7], v[196:199]\n"
      "ds_read_b64_tr_b16 v[44:45], v105 offset:53248\nv_mfma_f32_16x16x32_bf16 v[200:203], v[92:95], v[4:7], v[200:203]\nds_read_b64_tr_b16 v[46:47], v105 offset:54272\n"
      "v_mfma_f32_16x16x32_bf16 v[200:203], v[88:91], v[0:3], v[200:203]\nds_read_b64_tr_b16 v[48:49], v106 offset:45056\nv_mfma_f32_16x16x32_bf16 v[216:219], v[88:91], v[8:11], v[216:219]\n"
      "ds_read_b128 v[0:3], v100 offset:45056\nv_mfma_f32_16x16x32_bf16 v[216:219], v[92:95], v[12:15], v[216:219]\nds_read_b128 v[4:7], v101 offset:45056\n"
      "v_mfma_f32_16x16x32_bf16 v[212:215], v[84:87], v[12:15], v[212:215]\nds_read_b64_tr_b16 v[50:51], v106 offset:46080\nv_mfma_f32_16x16x32_bf16 v[212:215], v[80:83], v[8:11], v[212:215]\n"
      "ds_read_b64_tr_b16 v[52:53], v106 offset:53248\nv_mfma_f32_16x16x32_bf16 v[208:211], v[72:75], v[8:11], v[208:211]\nds_read_b64_tr_b16 v[54:55], v106 offset:54272\n"
      "v_mfma_f32_16x16x32_bf16 v[208:211], v[76:79], v[12:15], v[208:211]\nds_read_b64_tr_b16 v[56:57], v107 offset:45056\nv_mfma_f32_16x16x32_bf16 v[204:207], v[68:71], v[12:15], v[204:207]\n"
      "ds_read_b64_tr_b16 v[58:59], v107 offset:46080\nv_mfma_f32_16x16x32_bf16 v[204:207], v[64:67], v[8:11], v[204:207]\nds_read_b64_tr_b16 v[60:61], v107 offset:53248\n"
      "v_mfma_f32_16x16x32_bf16 v[220:223], v[64:67], v[16:19], v[220:223]\nds_read_b128 v[8:11], v120 offset:45056\nv_mfma_f32_16x16x32_bf16 v[220:223], v[68:71], v[20:23], v[220:223]\n"
      "ds_read_b128 v[12:15], v121 offset:45056\nv_mfma_f32_16x16x32_bf16 v[224:227], v[76:79], v[20:23], v[224:227]\nds_read_b64_tr_b16 v[62:63], v107 offset:54272\n"
      "v_mfma_f32_16x16x32_bf16 v[224:227], v[72:75], v[16:19], v[224:227]\nv_mfma_f32_16x16x32_bf16 v[228:231], v[80:83], v[16:19], v[228:231]\n"
      "v_mfma_f32_16x16x32_bf16 v[228:231], v[84:87], v[20:23], v[228:231]\nv_mfma_f32_16x16x32_bf16 v[232:235], v[92:95], v[20:23], v[232:235]\ns_waitcnt lgkmcnt(0)\n"
      "v_mfma_f32_16x16x32_bf16 v[232:235], v[88:91], v[16:19], v[232:235]\nds_read_b128 v[16:19], v96 offset:45056\nds_read_b128 v[20:23], v97 offset:45056\n"
      "v_mfma_f32_16x16x32_bf16 v[124:127], v[32:35], v[0:3], v[124:127]\nds_read_b128 v[24:27], v96 offset:47104\nv_mfma_f32_16x16x32_bf16 v[124:127], v[36:39], v[4:7], v[124:127]\n"
      "ds_read_b128 v[28:31], v97 offset:47104\nv_mfma_f32_16x16x32_bf16 v[128:131], v[44:47], v[4:7], v[128:131]\nv_mfma_f32_16x16x32_bf16 v[128:131], v[40:43], v[0:3], v[128:131]\n"
      "v_mfma_f32_16x16x32_bf16 v[132:135], v[48:51], v[0:3], v[132:135]\nv_mfma_f32_16x16x32_bf16 v[132:135], v[52:55], v[4:7], v[132:135]\n"
      "v_mfma_f32_16x16x32_bf16 v[136:139], v[60:63], v[4:7], v[136:139]\nv_mfma_f32_16x16x32_bf16 v[136:139], v[56:59], v[0:3], v[136:139]\n"
      "v_mfma_f32_16x16x32_bf16 v[152:155], v[56:59], v[8:11], v[152:155]\nds_read_b128 v[0:3], v96 offset:49152\nv_mfma_f32_16x16x32_bf16 v[152:155], v[60:63], v[12:15], v[152:155]\n"
      "ds_read_b128 v[4:7], v97 offset:49152\nv_mfma_f32_16x16x32_bf16 v[148:151], v[52:55], v[12:15], v[148:151]\nv_mfma_f32_16x16x32_bf16 v[148:151], v[48:51], v[8:11], v[148:151]\n"
      "v_mfma_f32_16x16x32_bf16 v[144:147], v[40:43], v[8:11], v[144:147]\nv_mfma_f32_16x16x32_bf16 v[144:147], v[44:47], v[12:15], v[144:147]\n"
      "v_mfma_f32_16x16x32_bf16 v[140:143], v[36:39], v[12:15], v[140:143]\ns_waitcnt lgkmcnt(4)\nv_mfma_f32_16x16x32_bf16 v[140:143], v[32:35], v[8:11], v[140:143]\n"
      "v_mfma_f32_16x16x32_bf16 v[156:159], v[32:35], v[16:19], v[156:159]\nds_read_b128 v[8:11], v96 offset:51200\nv_mfma_f32_16x16x32_bf16 v[156:159], v[36:39], v[20:23], v[156:159]\n"
      "ds_read_b128 v[12:15], v97 offset:51200\nv_mfma_f32_16x16x32_bf16 v[160:163], v[44:47], v[20:23], v[160:163]\nv_mfma_f32_16x16x32_bf16 v[160:163], v[40:43], v[16:19], v[160:163]\n"
      "v_mfma_f32_16x16x32_bf16 v[164:167], v[48:51], v[16:19], v[164:167]\nv_mfma_f32_16x16x32_bf16 v[164:167], v[52:55], v[20:23], v[164:167]\n"
      "v_mfma_f32_16x16x32_bf16 v[168:171], v[60:63], v[20:23], v[168:171]\ns_waitcnt lgkmcnt(2)\nv_mfma_f32_16x16x32_bf16 v[168:171], v[56:59], v[16:19], v[168:171]\n"
      "v_mfma_f32_16x16x32_bf16 v[184:187], v[56:59], v[24:27], v[184:187]\nds_read_b128 v[16:19], v96 offset:53248\nv_mfma_f32_16x16x32_bf16 v[184:187], v[60:63], v[28:31], v[184:187]\n"
      "ds_read_b128 v[20:23], v97 offset:53248\nv_mfma_f32_16x16x32_bf16 v[180:183], v[52:55], v[28:31], v[180:183]\nv_mfma_f32_16x16x32_bf16 v[180:183], v[48:51], v[24:27], v[180:183]\n"
      "v_mfma_f32_16x16x32_bf16 v[176:179], v[40:43], v[24:27], v[176:179]\nv_mfma_f32_16x16x32_bf16 v[176:179], v[44:47], v[28:31], v[176:179]\n"
      "v_mfma_f32_16x16x32_bf16 v[172:175], v[36:39], v[28:31], v[172:175]\ns_waitcnt vmcnt(0) lgkmcnt(0)\ns_barrier\nv_mfma_f32_16x16x32_bf16 v[172:175], v[32:35], v[24:27], v[172:175]\n"
      "ds_read_b64_tr_b16 v[64:65], v108 offset:0\nv_mfma_f32_16x16x32_bf16 v[188:191], v[32:35], v[0:3], v[188:191]\nds_read_b64_tr_b16 v[66:67], v108 offset:1024\n"
      "v_mfma_f32_16x16x32_bf16 v[188:191], v[36:39], v[4:7], v[188:191]\nds_read_b64_tr_b16 v[68:69], v108 offset:8192\nv_mfma_f32_16x16x32_bf16 v[192:195], v[44:47], v[4:7], v[192:195]\n"
      "ds_read_b64_tr_b16 v[70:71], v108 offset:9216\nv_mfma_f32_16x16x32_bf16 v[192:195], v[40:43], v[0:3], v[192:195]\nds_read_b64_tr_b16 v[72:73], v109 offset:0\n"
      "v_mfma_f32_16x16x32_bf16 v[196:199], v[48:51], v[0:3], v[196:199]\nds_read_b64_tr_b16 v[74:75], v109 offset:1024\nv_mfma_f32_16x16x32_bf16 v[196:199], v[52:55], v[4:7], v[196:199]\n"
      "ds_read_b64_tr_b16 v[76:77], v109 offset:8192\nv_mfma_f32_16x16x32_bf16 v[200:203], v[60:63], v[4:7], v[200:203]\nds_read_b64_tr_b16 v[78:79], v109 offset:9216\n"
      "v_mfma_f32_16x16x32_bf16 v[200:203], v[56:59], v[0:3], v[200:203]\nds_read_b64_tr_b16 v[80:81], v110 offset:0\nv_mfma_f32_16x16x32_bf16 v[216:219], v[56:59], v[8:11], v[216:219]\n"
      "ds_read_b128 v[0:3], v102 offset:0\nv_mfma_f32_16x16x32_bf16 v[216:219], v[60:63], v[12:15], v[216:219]\nds_read_b128 v[4:7], v103 offset:0\n"
      "v_mfma_f32_16x16x32_bf16 v[212:215], v[52:55], v[12:15], v[212:215]\nds_read_b64_tr_b16 v[82:83], v110 offset:1024\nv_mfma_f32_16x16x32_bf16 v[212:215], v[48:51], v[8:11], v[212:215]\n"
      "ds_read_b64_tr_b16 v[84:85], v110 offset:8192\nv_mfma_f32_16x16x32_bf16 v[208:211], v[40:43], v[8:11], v[208:211]\nds_read_b64_tr_b16 v[86:87], v110 offset:9216\n"
      "v_mfma_f32_16x16x32_bf16 v[208:211], v[44:47], v[12:15], v[208:211]\nds_read_b64_tr_b16 v[88:89], v111 offset:0\nv_mfma_f32_16x16x32_bf16 v[204:207], v[36:39], v[12:15], v[204:207]\n"
      "ds_read_b64_tr_b16 v[90:91], v111 offset:1024\nv_mfma_f32_16x16x32_bf16 v[204:207], v[32:35], v[8:11], v[204:207]\nds_read_b64_tr_b16 v[92:93], v111 offset:8192\n"
      "v_mfma_f32_16x16x32_bf16 v[220:223], v[32:35], v[16:19], v[220:223]\nds_read_b128 v[8:11], v122 offset:0\nv_mfma_f32_16x16x32_bf16 v[220:223], v[36:39], v[20:23], v[220:223]\n"
      "ds_read_b128 v[12:15], v123 offset:0\nv_mfma_f32_16x16x32_bf16 v[224:227], v[44:47], v[20:23], v[224:227]\nds_read_b64_tr_b16 v[94:95], v111 offset:9216\n"
      "v_mfma_f32_16x16x32_bf16 v[224:227], v[40:43], v[16:19], v[224:227]\nv_mfma_f32_16x16x32_bf16 v[228:231], v[48:51], v[16:19], v[228:231]\n"
      "v_mfma_f32_16x16x32_bf16 v[228:231], v[52:55], v[20:23], v[228:231]\nv_mfma_f32_16x16x32_bf16 v[232:235], v[60:63], v[20:23], v[232:235]\ns_waitcnt lgkmcnt(0)\n"
      "v_mfma_f32_16x16x32_bf16 v[232:235], v[56:59], v[16:19], v[232:235]\nds_read_b128 v[16:19], v98 offset:0\nds_read_b128 v[20:23], v99 offset:0\n"
      "v_mfma_f32_16x16x32_bf16 v[124:127], v[64:67], v[0:3], v[124:127]\nds_read_b128 v[24:27], v98 offset:2048\nv_mfma_f32_16x16x32_bf16 v[124:127], v[68:71], v[4:7], v[124:127]\n"
      "ds_read_b128 v[28:31], v99 offset:2048\nv_mfma_f32_16x16x32_bf16 v[128:131], v[76:79], v[4:7], v[128:131]\nv_mfma_f32_16x16x32_bf16 v[128:131], v[72:75], v[0:3], v[128:131]\n"
      "v_mfma_f32_16x16x32_bf16 v[132:135], v[80:83], v[0:3], v[132:135]\nv_mfma_f32_16x16x32_bf16 v[132:135], v[84:87], v[4:7], v[132:135]\n"
      "v_mfma_f32_16x16x32_bf16 v[136:139], v[92:95], v[4:7], v[136:139]\nv_mfma_f32_16x16x32_bf16 v[136:139], v[88:91], v[0:3], v[136:139]\n"
      "v_mfma_f32_16x16x32_bf16 v[152:155], v[88:91], v[8:11], v[152:155]\nds_read_b128 v[0:3], v98 offset:4096\nv_mfma_f32_16x16x32_bf16 v[152:155], v[92:95], v[12:15], v[152:155]\n"
      "ds_read_b128 v[4:7], v99 offset:4096\nv_mfma_f32_16x16x32_bf16 v[148:151], v[84:87], v[12:15], v[148:151]\nv_mfma_f32_16x16x32_bf16 v[148:151], v[80:83], v[8:11], v[148:151]\n"
      "v_mfma_f32_16x16x32_bf16 v[144:147], v[72:75], v[8:11], v[144:147]\nv_mfma_f32_16x16x32_bf16 v[144:147], v[76:79], v[12:15], v[144:147]\n"
      "v_mfma_f32_16x16x32_bf16 v[140:143], v[68:71], v[12:15], v[140:143]\ns_waitcnt lgkmcnt(4)\nv_mfma_f32_16x16x32_bf16 v[140:143], v[64:67], v[8:11], v[140:143]\n"
      "v_mfma_f32_16x16x32_bf16 v[156:159], v[64:67], v[16:19], v[156:159]\nds_read_b128 v[8:11], v98 offset:6144\nv_mfma_f32_16x16x32_bf16 v[156:159], v[68:71], v[20:23], v[156:159]\n"
      "ds_read_b128 v[12:15], v99 offset:6144\nv_mfma_f32_16x16x32_bf16 v[160:163], v[76:79], v[20:23], v[160:163]\nv_mfma_f32_16x16x32_bf16 v[160:163], v[72:75], v[16:19], v[160:163]\n"
      "v_mfma_f32_16x16x32_bf16 v[164:167], v[80:83], v[16:19], v[164:167]\nv_mfma_f32_16x16x32_bf16 v[164:167], v[84:87], v[20:23], v[164:167]\n"
      "v_mfma_f32_16x16x32_bf16 v[168:171], v[92:95], v[20:23], v[168:171]\ns_waitcnt lgkmcnt(2)\nv_mfma_f32_16x16x32_bf16 v[168:171], v[88:91], v[16:19], v[168:171]\n"
      "v_mfma_f32_16x16x32_bf16 v[184:187], v[88:91], v[24:27], v[184:187]\nds_read_b128 v[16:19], v98 offset:8192\nv_mfma_f32_16x16x32_bf16 v[184:187], v[92:95], v[28:31], v[184:187]\n"
      "ds_read_b128 v[20:23], v99 offset:8192\nv_mfma_f32_16x16x32_bf16 v[180:183], v[84:87], v[28:31], v[180:183]\nv_mfma_f32_16x16x32_bf16 v[180:183], v[80:83], v[24:27], v[180:183]\n"
      "v_mfma_f32_16x16x32_bf16 v[176:179], v[72:75], v[24:27], v[176:179]\nv_mfma_f32_16x16x32_bf16 v[176:179], v[76:79], v[28:31], v[176:179]\n"
      "v_mfma_f32_16x16x32_bf16 v[172:175], v[68:71], v[28:31], v[172:175]\ns_waitcnt vmcnt(0) lgkmcnt(0)\ns_barrier\nv_mfma_f32_16x16x32_bf16 v[172:175], v[64:67], v[24:27], v[172:175]\n"
      "ds_read_b64_tr_b16 v[32:33], v104 offset:0\nv_mfma_f32_16x16x32_bf16 v[188:191], v[64:67], v[0:3], v[188:191]\nds_read_b64_tr_b16 v[34:35], v104 offset:1024\n"
      "v_mfma_f32_16x16x32_bf16 v[188:191], v[68:71], v[4:7], v[188:191]\nds_read_b64_tr_b16 v[36:37], v104 offset:8192\nv_mfma_f32_16x16x32_bf16 v[192:195], v[76:79], v[4:7], v[192:195]\n"
      "ds_read_b64_tr_b16 v[38:39], v104 offset:9216\nv_mfma_f32_16x16x32_bf16 v[192:195], v[72:75], v[0:3], v[192:195]\nds_read_b64_tr_b16 v[40:41], v105 offset:0\n"
      "v_mfma_f32_16x16x32_bf16 v[196:199], v[80:83], v[0:3], v[196:199]\nds_read_b64_tr_b16 v[42:43], v105 offset:1024\nv_mfma_f32_16x16x32_bf16 v[196:199], v[84:87], v[4:7], v[196:199]\n"
      "ds_read_b64_tr_b16 v[44:45], v105 offset:8192\nv_mfma_f32_16x16x32_bf16 v[200:203], v[92:95], v[4:7], v[200:203]\nds_read_b64_tr_b16 v[46:47], v105 offset:9216\n"
      "v_mfma_f32_16x16x32_bf16 v[200:203], v[88:91], v[0:3], v[200:203]\nds_read_b64_tr_b16 v[48:49], v106 offset:0\nv_mfma_f32_16x16x32_bf16 v[216:219], v[88:91], v[8:11], v[216:219]\n"
      "ds_read_b128 v[0:3], v100 offset:0\nv_mfma_f32_16x16x32_bf16 v[216:219], v[92:95], v[12:15], v[216:219]\nds_read_b128 v[4:7], v101 offset:0\n"
      "v_mfma_f32_16x16x32_bf16 v[212:215], v[84:87], v[12:15], v[212:215]\nds_read_b64_tr_b16 v[50:51], v106 offset:1024\nv_mfma_f32_16x16x32_bf16 v[212:215], v[80:83], v[8:11], v[212:215]\n"
      "ds_read_b64_tr_b16 v[52:53], v106 offset:8192\nv_mfma_f32_16x16x32_bf16 v[208:211], v[72:75], v[8:11], v[208:211]\nds_read_b64_tr_b16 v[54:55], v106 offset:9216\n"
      "v_mfma_f32_16x16x32_bf16 v[208:211], v[76:79], v[12:15], v[208:211]\nds_read_b64_tr_b16 v[56:57], v107 offset:0\nv_mfma_f32_16x16x32_bf16 v[204:207], v[68:71], v[12:15], v[204:207]\n"
      "ds_read_b64_tr_b16 v[58:59], v107 offset:1024\nv_mfma_f32_16x16x32_bf16 v[204:207], v[64:67], v[8:11], v[204:207]\nds_read_b64_tr_b16 v[60:61], v107 offset:8192\n"
      "v_mfma_f32_16x16x32_bf16 v[220:223], v[64:67], v[16:19], v[220:223]\nds_read_b128 v[8:11], v120 offset:0\nv_mfma_f32_16x16x32_bf16 v[220:223], v[68:71], v[20:23], v[220:223]\n"
      "ds_read_b128 v[12:15], v121 offset:0\nv_mfma_f32_16x16x32_bf16 v[224:227], v[76:79], v[20:23], v[224:227]\nds_read_b64_tr_b16 v[62:63], v107 offset:9216\n"
      "v_mfma_f32_16x16x32_bf16 v[224:227], v[72:75], v[16:19], v[224:227]\nv_mfma_f32_16x16x32_bf16 v[228:231], v[80:83], v[16:19], v[228:231]\n"
      "v_mfma_f32_16x16x32_bf16 v[228:231], v[84:87], v[20:23], v[228:231]\nv_mfma_f32_16x16x32_bf16 v[232:235], v[92:95], v[20:23], v[232:235]\ns_waitcnt lgkmcnt(0)\n"
      "v_mfma_f32_16x16x32_bf16 v[232:235], v[88:91], v[16:19], v[232:235]\nds_read_b128 v[16:19], v96 offset:0\nds_read_b128 v[20:23], v97 offset:0\n"
      "v_mfma_f32_16x16x32_bf16 v[124:127], v[32:35], v[0:3], v[124:127]\nds_read_b128 v[24:27], v96 offset:2048\nv_mfma_f32_16x16x32_bf16 v[124:127], v[36:39], v[4:7], v[124:127]\n"
      "ds_read_b128 v[28:31], v97 offset:2048\nv_mfma_f32_16x16x32_bf16 v[128:131], v[44:47], v[4:7], v[128:131]\nv_mfma_f32_16x16x32_bf16 v[128:131], v[40:43], v[0:3], v[128:131]\n"
      "v_mfma_f32_16x16x32_bf16 v[132:135], v[48:51], v[0:3], v[132:135]\nv_mfma_f32_16x16x32_bf16 v[132:135], v[52:55], v[4:7], v[132:135]\n"
      "v_mfma_f32_16x16x32_bf16 v[136:139], v[60:63], v[4:7], v[136:139]\nv_mfma_f32_16x16x32_bf16 v[136:139], v[56:59], v[0:3], v[136:139]\n"
      "v_mfma_f32_16x16x32_bf16 v[152:155], v[56:59], v[8:11], v[152:155]\nds_read_b128 v[0:3], v96 offset:4096\nv_mfma_f32_16x16x32_bf16 v[152:155], v[60:63], v[12:15], v[152:155]\n"
      "ds_read_b128 v[4:7], v97 offset:4096\nv_mfma_f32_16x16x32_bf16 v[148:151], v[52:55], v[12:15], v[148:151]\nv_mfma_f32_16x16x32_bf16 v[148:151], v[48:51], v[8:11], v[148:151]\n"
      "v_mfma_f32_16x16x32_bf16 v[144:147], v[40:43], v[8:11], v[144:147]\nv_mfma_f32_16x16x32_bf16 v[144:147], v[44:47], v[12:15], v[144:147]\n"
      "v_mfma_f32_16x16x32_bf16 v[140:143], v[36:39], v[12:15], v[140:143]\ns_waitcnt lgkmcnt(4)\nv_mfma_f32_16x16x32_bf16 v[140:143], v[32:35], v[8:11], v[140:143]\n"
      "v_mfma_f32_16x16x32_bf16 v[156:159], v[32:35], v[16:19], v[156:159]\nds_read_b128 v[8:11], v96 offset:6144\nv_mfma_f32_16x16x32_bf16 v[156:159], v[36:39], v[20:23], v[156:159]\n"
      "ds_read_b128 v[12:15], v97 offset:6144\nv_mfma_f32_16x16x32_bf16 v[160:163], v[44:47], v[20:23], v[160:163]\nv_mfma_f32_16x16x32_bf16 v[160:163], v[40:43], v[16:19], v[160:163]\n"
      "v_mfma_f32_16x16x32_bf16 v[164:167], v[48:51], v[16:19], v[164:167]\nv_mfma_f32_16x16x32_bf16 v[164:167], v[52:55], v[20:23], v[164:167]\n"
      "v_mfma_f32_16x16x32_bf16 v[168:171], v[60:63], v[20:23], v[168:171]\ns_waitcnt lgkmcnt(2)\nv_mfma_f32_16x16x32_bf16 v[168:171], v[56:59], v[16:19], v[168:171]\n"
      "v_mfma_f32_16x16x32_bf16 v[184:187], v[56:59], v[24:27], v[184:187]\nds_read_b128 v[16:19], v96 offset:8192\nv_mfma_f32_16x16x32_bf16 v[184:187], v[60:63], v[28:31], v[184:187]\n"
      "ds_read_b128 v[20:23], v97 offset:8192\nv_mfma_f32_16x16x32_bf16 v[180:183], v[52:55], v[28:31], v[180:183]\nv_mfma_f32_16x16x32_bf16 v[180:183], v[48:51], v[24:27], v[180:183]\n"
      "v_mfma_f32_16x16x32_bf16 v[176:179], v[40:43], v[24:27], v[176:179]\nv_mfma_f32_16x16x32_bf16 v[176:179], v[44:47], v[28:31], v[176:179]\n"
      "v_mfma_f32_16x16x32_bf16 v[172:175], v[36:39], v[28:31], v[172:175]\ns_waitcnt vmcnt(0) lgkmcnt(0)\ns_barrier\nv_mfma_f32_16x16x32_bf16 v[172:175], v[32:35], v[24:27], v[172:175]\n"
      "ds_read_b64_tr_b16 v[64:65], v104 offset:45056\nv_mfma_f32_16x16x32_bf16 v[188:191], v[32:35], v[0:3], v[188:191]\nds_read_b64_tr_b16 v[66:67], v104 offset:46080\n"
      "v_mfma_f32_16x16x32_bf16 v[188:191], v[36:39], v[4:7], v[188:191]\nds_read_b64_tr_b16 v[68:69], v104 offset:53248\nv_mfma_f32_16x16x32_bf16 v[192:195], v[44:47], v[4:7], v[192:195]\n"
      "ds_read_b64_tr_b16 v[70:71], v104 offset:54272\nv_mfma_f32_16x16x32_bf16 v[192:195], v[40:43], v[0:3], v[192:195]\nds_read_b64_tr_b16 v[72:73], v105 offset:45056\n"
      "v_mfma_f32_16x16x32_bf16 v[196:199], v[48:51], v[0:3], v[196:199]\nds_read_b64_tr_b16 v[74:75], v105 offset:46080\nv_mfma_f32_16x16x32_bf16 v[196:199], v[52:55], v[4:7], v[196:199]\n"
      "ds_read_b64_tr_b16 v[76:77], v105 offset:53248\nv_mfma_f32_16x16x32_bf16 v[200:203], v[60:63], v[4:7], v[200:203]\nds_read_b64_tr_b16 v[78:79], v105 offset:54272\n"
      "v_mfma_f32_16x16x32_bf16 v[200:203], v[56:59], v[0:3], v[200:203]\nds_read_b64_tr_b16 v[80:81], v106 offset:45056\nv_mfma_f32_16x16x32_bf16 v[216:219], v[56:59], v[8:11], v[216:219]\n"
      "ds_read_b128 v[0:3], v100 offset:45056\nv_mfma_f32_16x16x32_bf16 v[216:219], v[60:63], v[12:15], v[216:219]\nds_read_b128 v[4:7], v101 offset:45056\n"
      "v_mfma_f32_16x16x32_bf16 v[212:215], v[52:55], v[12:15], v[212:215]\nds_read_b64_tr_b16 v[82:83], v106 offset:46080\nv_mfma_f32_16x16x32_bf16 v[212:215], v[48:51], v[8:11], v[212:215]\n"
      "ds_read_b64_tr_b16 v[84:85], v106 offset:53248\nv_mfma_f32_16x16x32_bf16 v[208:211], v[40:43], v[8:11], v[208:211]\nds_read_b64_tr_b16 v[86:87], v106 offset:54272\n"
      "v_mfma_f32_16x16x32_bf16 v[208:211], v[44:47], v[12:15], v[208:211]\nds_read_b64_tr_b16 v[88:89], v107 offset:45056\nv_mfma_f32_16x16x32_bf16 v[204:207], v[36:39], v[12:15], v[204:207]\n"
      "ds_read_b64_tr_b16 v[90:91], v107 offset:46080\nv_mfma_f32_16x16x32_bf16 v[204:207], v[32:35], v[8:11], v[204:207]\nds_read_b64_tr_b16 v[92:93], v107 offset:53248\n"
      "v_mfma_f32_16x16x32_bf16 v[220:223], v[32:35], v[16:19], v[220:223]\nds_read_b128 v[8:11], v120 offset:45056\nv_mfma_f32_16x16x32_bf16 v[220:223], v[36:39], v[20:23], v[220:223]\n"
      "ds_read_b128 v[12:15], v121 offset:45056\nv_mfma_f32_16x16x32_bf16 v[224:227], v[44:47], v[20:23], v[224:227]\nds_read_b64_tr_b16 v[94:95], v107 offset:54272\n"
      "v_mfma_f32_16x16x32_bf16 v[224:227], v[40:43], v[16:19], v[224:227]\nv_mfma_f32_16x16x32_bf16 v[228:231], v[48:51], v[16:19], v[228:231]\ns_sub_u32 s58, s58, 1\ns_cmp_lg_u32 s58, 0\n"
      "v_mfma_f32_16x16x32_bf16 v[228:231], v[52:55], v[20:23], v[228:231]\nv_mfma_f32_16x16x32_bf16 v[232:235], v[60:63], v[20:23], v[232:235]\ns_waitcnt lgkmcnt(0)\n"
      "v_mfma_f32_16x16x32_bf16 v[232:235], v[56:59], v[16:19], v[232:235]\nds_read_b128 v[16:19], v96 offset:45056\nds_read_b128 v[20:23], v97 offset:45056\ns_cbranch_scc1 .Lgl14\n"
      "v_mfma_f32_16x16x32_bf16 v[124:127], v[64:67], v[0:3], v[124:127]\nds_read_b128 v[24:27], v96 offset:47104\nv_cmp_eq_u32 s[62:63], 0, v240\n"
      "v_mfma_f32_16x16x32_bf16 v[124:127], v[68:71], v[4:7], v[124:127]\nds_read_b128 v[28:31], v97 offset:47104\nv_mov_b32 v253, 262144\n"
      "v_mfma_f32_16x16x32_bf16 v[128:131], v[76:79], v[4:7], v[128:131]\nv_mov_b32 v248, 162048\nv_mov_b32 v241, 1\nv_mfma_f32_16x16x32_bf16 v[128:131], v[72:75], v[0:3], v[128:131]\n"
      "v_cndmask_b32 v253, v253, v248, s[62:63]\nv_lshrrev_b32 v112, 4, v240\nv_mfma_f32_16x16x32_bf16 v[132:135], v[80:83], v[0:3], v[132:135]\nv_bfe_u32 v113, v240, 2, 2\n"
      "v_and_b32 v114, 3, v240\nv_mfma_f32_16x16x32_bf16 v[132:135], v[84:87], v[4:7], v[132:135]\nv_mul_u32_u24 v114, 0x80008, v114\nv_add_u32 v244, v112, v113\n"
      "v_mfma_f32_16x16x32_bf16 v[136:139], v[92:95], v[4:7], v[136:139]\nv_and_b32 v244, 3, v244\nv_add_u32 v116, 22, v113\nv_mfma_f32_16x16x32_bf16 v[136:139], v[88:91], v[0:3], v[136:139]\n"
      "v_cmp_gt_u32 s[62:63], 2, v113\nv_and_b32 v113, 60, v240\nv_mfma_f32_16x16x32_bf16 v[152:155], v[88:91], v[8:11], v[152:155]\nds_read_b128 v[0:3], v96 offset:49152\ns_mov_b32 s42, 0\n"
      "v_mfma_f32_16x16x32_bf16 v[152:155], v[92:95], v[12:15], v[152:155]\nds_read_b128 v[4:7], v97 offset:49152\nv_cndmask_b32 v116, v116, v244, s[62:63]\n"
      "v_mfma_f32_16x16x32_bf16 v[148:151], v[84:87], v[12:15], v[148:151]\nv_lshl_or_b32 v244, v244, 16, v116\nv_lshl_add_u32 v244, v244, 5, v114\n"
      "v_mfma_f32_16x16x32_bf16 v[148:151], v[80:83], v[8:11], v[148:151]\nv_lshl_add_u32 v252, v240, 4, s42\ns_mov_b32 s42, 0x76325410\n"
      "v_mfma_f32_16x16x32_bf16 v[144:147], v[72:75], v[8:11], v[144:147]\ns_mov_b32 s43, 0x21005410\nv_lshrrev_b64 v[116:117], v113, s[42:43]\n"
      "v_mfma_f32_16x16x32_bf16 v[144:147], v[76:79], v[12:15], v[144:147]\nv_and_b32 v116, 7, v116\nv_lshl_add_u32 v116, v112, 3, v116\n"
      "v_mfma_f32_16x16x32_bf16 v[140:143], v[68:71], v[12:15], v[140:143]\ns_waitcnt lgkmcnt(4)\ns_mov_b32 s42, 0x54107632\nv_mfma_f32_16x16x32_bf16 v[140:143], v[64:67], v[8:11], v[140:143]\n"
      "s_mov_b32 s43, 0x21107632\nv_lshrrev_b64 v[118:119], v113, s[42:43]\nv_mfma_f32_16x16x32_bf16 v[156:159], v[64:67], v[16:19], v[156:159]\nds_read_b128 v[8:11], v96 offset:51200\n"
      "v_and_b32 v118, 7, v118\nv_mfma_f32_16x16x32_bf16 v[156:159], v[68:71], v[20:23], v[156:159]\nds_read_b128 v[12:15], v97 offset:51200\nv_lshl_add_u32 v118, v112, 3, v118\n"
      "v_mfma_f32_16x16x32_bf16 v[160:163], v[76:79], v[20:23], v[160:163]\nv_lshl_or_b32 v245, v118, 16, v116\nv_lshl_add_u32 v245, v245, 5, v114\n"
      "v_mfma_f32_16x16x32_bf16 v[160:163], v[72:75], v[16:19], v[160:163]\nv_and_b32 v113, 1, v240\nv_mul_u32_u24 v113, 0xaaaa, v113\n"
      "v_mfma_f32_16x16x32_bf16 v[164:167], v[80:83], v[16:19], v[164:167]\nv_add_u32 v242, 0x4444, v113\ns_lshl_b32 s42, s36, 4\n"
      "v_mfma_f32_16x16x32_bf16 v[164:167], v[84:87], v[20:23], v[164:167]\ns_add_u32 s42, s42, 160640\nv_lshl_add_u32 v243, v112, 2, s42\n"
      "v_mfma_f32_16x16x32_bf16 v[168:171], v[92:95], v[20:23], v[168:171]\ns_waitcnt lgkmcnt(2)\nv_and_b32 v112, 1, v112\nv_mfma_f32_16x16x32_bf16 v[168:171], v[88:91], v[16:19], v[168:171]\n"
      "v_lshl_add_u32 v112, v112, 3, s24\ns_sub_u32 s42, s64, 1\nv_mfma_f32_16x16x32_bf16 v[184:187], v[88:91], v[24:27], v[184:187]\nds_read_b128 v[16:19], v96 offset:53248\n"
      "v_min_u32 v112, s42, v112\nv_mfma_f32_16x16x32_bf16 v[184:187], v[92:95], v[28:31], v[184:187]\nds_read_b128 v[20:23], v97 offset:53248\nv_lshlrev_b32 v112, 3, v112\n"
      "v_mfma_f32_16x16x32_bf16 v[180:183], v[84:87], v[28:31], v[180:183]\nv_add_u32 v250, 160256, v112\nv_and_b32 v113, 15, v240\n"
      "v_mfma_f32_16x16x32_bf16 v[180:183], v[80:83], v[24:27], v[180:183]\nv_min_u32 v113, 12, v113\ns_lshl_b32 s42, s30, 7\nv_mfma_f32_16x16x32_bf16 v[176:179], v[72:75], v[24:27], v[176:179]\n"
      "v_lshl_add_u32 v113, v113, 15, s42\nv_add_u32 v249, v113, v112\nv_mfma_f32_16x16x32_bf16 v[176:179], v[76:79], v[28:31], v[176:179]\ns_mul_i32 s65, s36, 22272\ns_add_u32 s65, s65, 45056\n"
      "v_mfma_f32_16x16x32_bf16 v[172:175], v[68:71], v[28:31], v[172:175]\ns_waitcnt vmcnt(0) lgkmcnt(0)\ns_barrier\nv_mfma_f32_16x16x32_bf16 v[172:175], v[64:67], v[24:27], v[172:175]\n"
      "ds_read_b64_tr_b16 v[32:33], v108 offset:0\ns_add_u32 s68, s64, 8\nv_mfma_f32_16x16x32_bf16 v[188:191], v[64:67], v[0:3], v[188:191]\nds_read_b64_tr_b16 v[34:35], v108 offset:1024\n"
      "s_min_u32 s68, s68, 21\nv_mfma_f32_16x16x32_bf16 v[188:191], v[68:71], v[4:7], v[188:191]\nds_read_b64_tr_b16 v[36:37], v108 offset:8192\ns_lshl_b32 s38, s38, 16\n"
      "v_mfma_f32_16x16x32_bf16 v[192:195], v[76:79], v[4:7], v[192:195]\nds_read_b64_tr_b16 v[38:39], v108 offset:9216\ns_lshr_b32 s42, s24, 2\n"
      "v_mfma_f32_16x16x32_bf16 v[192:195], v[72:75], v[0:3], v[192:195]\nds_read_b64_tr_b16 v[40:41], v109 offset:0\ns_mul_i32 s43, s42, 3\n"
      "v_mfma_f32_16x16x32_bf16 v[196:199], v[80:83], v[0:3], v[196:199]\nds_read_b64_tr_b16 v[42:43], v109 offset:1024\ns_xor_b32 s43, s43, s36\n"
      "v_mfma_f32_16x16x32_bf16 v[196:199], v[84:87], v[4:7], v[196:199]\nds_read_b64_tr_b16 v[44:45], v109 offset:8192\ns_add_u32 s42, s42, s64\n"
      "v_mfma_f32_16x16x32_bf16 v[200:203], v[92:95], v[4:7], v[200:203]\nds_read_b64_tr_b16 v[46:47], v109 offset:9216\ns_add_u32 s42, s42, 9\n"
      "v_mfma_f32_16x16x32_bf16 v[200:203], v[88:91], v[0:3], v[200:203]\nds_read_b64_tr_b16 v[48:49], v110 offset:0\ns_and_b32 s42, s42, 1\n"
      "v_mfma_f32_16x16x32_bf16 v[216:219], v[88:91], v[8:11], v[216:219]\nds_read_b128 v[0:3], v102 offset:0\ns_lshl1_add_u32 s67, s43, s42\n"
      "v_mfma_f32_16x16x32_bf16 v[216:219], v[92:95], v[12:15], v[216:219]\nds_read_b128 v[4:7], v103 offset:0\ns_add_u32 s66, s64, s67\n"
      "v_mfma_f32_16x16x32_bf16 v[212:215], v[84:87], v[12:15], v[212:215]\nds_read_b64_tr_b16 v[50:51], v110 offset:1024\ns_cmp_lt_u32 s66, s68\n"
      "v_mfma_f32_16x16x32_bf16 v[212:215], v[80:83], v[8:11], v[212:215]\nds_read_b64_tr_b16 v[52:53], v110 offset:8192\ns_cselect_b32 s66, s66, 127\n"
      "v_mfma_f32_16x16x32_bf16 v[208:211], v[72:75], v[8:11], v[208:211]\nds_read_b64_tr_b16 v[54:55], v110 offset:9216\ns_sub_u32 s66, s66, 8\n"
      "v_mfma_f32_16x16x32_bf16 v[208:211], v[76:79], v[12:15], v[208:211]\nds_read_b64_tr_b16 v[56:57], v111 offset:0\ns_max_i32 s66, s66, 0\n"
      "v_mfma_f32_16x16x32_bf16 v[204:207], v[68:71], v[12:15], v[204:207]\nds_read_b64_tr_b16 v[58:59], v111 offset:1024\ns_getpc_b64 s[34:35]\n"
      ".Lk1064:\nv_mfma_f32_16x16x32_bf16 v[204:207], v[64:67], v[8:11], v[204:207]\nds_read_b64_tr_b16 v[60:61], v111 offset:8192\ns_add_u32 s34, s34, .Lt_g-.Lk1064\n"
      "v_mfma_f32_16x16x32_bf16 v[220:223], v[64:67], v[16:19], v[220:223]\nds_read_b128 v[8:11], v122 offset:0\ns_addc_u32 s35, s35, 0\n"
      "v_mfma_f32_16x16x32_bf16 v[220:223], v[68:71], v[20:23], v[220:223]\nds_read_b128 v[12:15], v123 offset:0\ns_lshr_b32 s42, s2, 3\n"
      "v_mfma_f32_16x16x32_bf16 v[224:227], v[76:79], v[20:23], v[224:227]\nds_read_b64_tr_b16 v[62:63], v111 offset:9216\ns_lshl1_add_u32 s42, s2, s42\n"
      "v_mfma_f32_16x16x32_bf16 v[224:227], v[72:75], v[16:19], v[224:227]\ns_and_b32 s69, s42, 15\ns_lshl_b32 s43, s69, 7\nv_mfma_f32_16x16x32_bf16 v[228:231], v[80:83], v[16:19], v[228:231]\n"
      "s_add_u32 s43, s43, 32768\ns_add_u32 s8, s20, s43\nv_mfma_f32_16x16x32_bf16 v[228:231], v[84:87], v[20:23], v[228:231]\ns_addc_u32 s9, s21, 0\ns_add_u32 s12, s20, 65536\n"
      "v_mfma_f32_16x16x32_bf16 v[232:235], v[92:95], v[20:23], v[232:235]\ns_waitcnt lgkmcnt(0)\ns_addc_u32 s13, s21, 0\nv_mfma_f32_16x16x32_bf16 v[232:235], v[88:91], v[16:19], v[232:235]\n"
      "ds_read_b128 v[16:19], v98 offset:0\nds_read_b128 v[20:23], v99 offset:0\nv_mfma_f32_16x16x32_bf16 v[124:127], v[32:35], v[0:3], v[124:127]\nds_read_b128 v[24:27], v98 offset:2048\n"
      "s_add_u32 s16, s20, 2097152\nv_mfma_f32_16x16x32_bf16 v[124:127], v[36:39], v[4:7], v[124:127]\nds_read_b128 v[28:31], v99 offset:2048\ns_addc_u32 s17, s21, 0\n"
      "v_mfma_f32_16x16x32_bf16 v[128:131], v[44:47], v[4:7], v[128:131]\ns_lshl_b32 s42, s69, 4\ns_max_u32 s42, s42, 24\nv_mfma_f32_16x16x32_bf16 v[128:131], v[40:43], v[0:3], v[128:131]\n"
      "s_sub_u32 s42, s42, 24\ns_min_u32 s69, s42, 192\nv_mfma_f32_16x16x32_bf16 v[132:135], v[48:51], v[0:3], v[132:135]\ns_lshl_b32 s42, s69, 3\ns_add_u32 s42, s42, 24576\n"
      "v_mfma_f32_16x16x32_bf16 v[132:135], v[52:55], v[4:7], v[132:135]\ns_add_u32 s14, s20, s42\ns_addc_u32 s15, s21, 0\nv_mfma_f32_16x16x32_bf16 v[136:139], v[60:63], v[4:7], v[136:139]\n"
      "v_mfma_f32_16x16x32_bf16 v[136:139], v[56:59], v[0:3], v[136:139]\nv_mfma_f32_16x16x32_bf16 v[152:155], v[56:59], v[8:11], v[152:155]\nds_read_b128 v[0:3], v98 offset:4096\n"
      "v_mfma_f32_16x16x32_bf16 v[152:155], v[60:63], v[12:15], v[152:155]\nds_read_b128 v[4:7], v99 offset:4096\nv_mfma_f32_16x16x32_bf16 v[148:151], v[52:55], v[12:15], v[148:151]\n"
      "v_mfma_f32_16x16x32_bf16 v[148:151], v[48:51], v[8:11], v[148:151]\nv_mfma_f32_16x16x32_bf16 v[144:147], v[40:43], v[8:11], v[144:147]\n"
      "v_mfma_f32_16x16x32_bf16 v[144:147], v[44:47], v[12:15], v[144:147]\nv_mfma_f32_16x16x32_bf16 v[140:143], v[36:39], v[12:15], v[140:143]\ns_waitcnt lgkmcnt(4)\n"
      "v_mfma_f32_16x16x32_bf16 v[140:143], v[32:35], v[8:11], v[140:143]\nv_mfma_f32_16x16x32_bf16 v[156:159], v[32:35], v[16:19], v[156:159]\nds_read_b128 v[8:11], v98 offset:6144\n"
      "v_mfma_f32_16x16x32_bf16 v[156:159], v[36:39], v[20:23], v[156:159]\nds_read_b128 v[12:15], v99 offset:6144\nv_mfma_f32_16x16x32_bf16 v[160:163], v[44:47], v[20:23], v[160:163]\n"
      "v_mfma_f32_16x16x32_bf16 v[160:163], v[40:43], v[16:19], v[160:163]\nv_mfma_f32_16x16x32_bf16 v[164:167], v[48:51], v[16:19], v[164:167]\n"
      "v_mfma_f32_16x16x32_bf16 v[164:167], v[52:55], v[20:23], v[164:167]\nv_mfma_f32_16x16x32_bf16 v[168:171], v[60:63], v[20:23], v[168:171]\ns_waitcnt lgkmcnt(2)\n"
      "v_mfma_f32_16x16x32_bf16 v[168:171], v[56:59], v[16:19], v[168:171]\nv_mfma_f32_16x16x32_bf16 v[184:187], v[56:59], v[24:27], v[184:187]\nds_read_b128 v[16:19], v98 offset:8192\n"
      "v_mfma_f32_16x16x32_bf16 v[184:187], v[60:63], v[28:31], v[184:187]\nds_read_b128 v[20:23], v99 offset:8192\nv_mfma_f32_16x16x32_bf16 v[180:183], v[52:55], v[28:31], v[180:183]\n"
      "v_mfma_f32_16x16x32_bf16 v[180:183], v[48:51], v[24:27], v[180:183]\nv_mfma_f32_16x16x32_bf16 v[176:179], v[40:43], v[24:27], v[176:179]\n"
      "v_mfma_f32_16x16x32_bf16 v[176:179], v[44:47], v[28:31], v[176:179]\nv_mfma_f32_16x16x32_bf16 v[172:175], v[36:39], v[28:31], v[172:175]\ns_waitcnt vmcnt(0) lgkmcnt(0)\ns_barrier\n"
      "v_mfma_f32_16x16x32_bf16 v[172:175], v[32:35], v[24:27], v[172:175]\nds_read_b128 v[72:75], v252 offset:0\nv_lshl_add_u32 v246, v240, 1, v248\n"
      "v_mfma_f32_16x16x32_bf16 v[188:191], v[32:35], v[0:3], v[188:191]\nds_read_b128 v[24:27], v236 offset:512\nds_read_u16 v251, v246 offset:64\n"
      "v_mfma_f32_16x16x32_bf16 v[188:191], v[36:39], v[4:7], v[188:191]\nds_read_b128 v[28:31], v236 offset:576\nds_read_b32 v247, v248 offset:24\n"
      "v_mfma_f32_16x16x32_bf16 v[192:195], v[44:47], v[4:7], v[192:195]\nds_read_b128 v[76:79], v252 offset:1024\nv_and_b32 v246, 48, v240\n"
      "v_mfma_f32_16x16x32_bf16 v[192:195], v[40:43], v[0:3], v[192:195]\nds_read_b128 v[80:83], v252 offset:2048\ns_mov_b32 s42, 42240\n"
      "v_mfma_f32_16x16x32_bf16 v[196:199], v[48:51], v[0:3], v[196:199]\nds_read_b128 v[84:87], v252 offset:3072\nv_lshl_add_u32 v246, v246, 2, s42\n"
      "v_mfma_f32_16x16x32_bf16 v[196:199], v[52:55], v[4:7], v[196:199]\nds_read_b128 v[88:91], v252 offset:4096\ns_mov_b32 s58, 0xbdd2d3e7\n"
      "v_mfma_f32_16x16x32_bf16 v[200:203], v[60:63], v[4:7], v[200:203]\nds_read_b128 v[92:95], v252 offset:5120\ns_mov_b32 s59, 0xc0135761\n"
      "v_mfma_f32_16x16x32_bf16 v[200:203], v[56:59], v[0:3], v[200:203]\nds_read_b128 v[96:99], v252 offset:6144\ns_mov_b32 s60, 0xc1e00000\n"
      "v_mfma_f32_16x16x32_bf16 v[216:219], v[56:59], v[8:11], v[216:219]\nds_read_b128 v[0:3], v236 offset:0\ns_add_u32 s49, s65, 1856\n"
      "v_mfma_f32_16x16x32_bf16 v[216:219], v[60:63], v[12:15], v[216:219]\nds_read_b128 v[4:7], v236 offset:64\ns_mov_b32 s45, 2\n"
      "v_mfma_f32_16x16x32_bf16 v[212:215], v[52:55], v[12:15], v[212:215]\nds_read_b128 v[100:103], v252 offset:7168\ns_mov_b32 s46, 0\n"
      "v_mfma_f32_16x16x32_bf16 v[212:215], v[48:51], v[8:11], v[212:215]\nds_read_b128 v[104:107], v252 offset:8192\ns_waitcnt lgkmcnt(8)\n"
      "v_mfma_f32_16x16x32_bf16 v[208:211], v[40:43], v[8:11], v[208:211]\nds_read_b128 v[108:111], v252 offset:9216\nv_readfirstlane_b32 s39, v247\n"
      "v_mfma_f32_16x16x32_bf16 v[208:211], v[44:47], v[12:15], v[208:211]\nds_read_b128 v[64:67], v237 offset:512\nv_readlane_b32 s55, v251, 0\n"
      "v_mfma_f32_16x16x32_bf16 v[204:207], v[36:39], v[12:15], v[204:207]\nds_read_b128 v[68:71], v237 offset:576\ns_setprio 3\nv_mfma_f32_16x16x32_bf16 v[204:207], v[32:35], v[8:11], v[204:207]\n"
      "ds_read_b128 v[112:115], v252 offset:10240\ns_xor_b32 s40, s39, 0xa5c30f96\nv_mfma_f32_16x16x32_bf16 v[220:223], v[32:35], v[16:19], v[220:223]\nds_read_b128 v[8:11], v237 offset:0\n"
      "s_mul_i32 s41, s39, 0x9E3779B1\nv_mfma_f32_16x16x32_bf16 v[220:223], v[36:39], v[20:23], v[220:223]\nds_read_b128 v[12:15], v237 offset:64\ns_lshr_b32 s41, s41, 24\n"
      "v_mfma_f32_16x16x32_bf16 v[224:227], v[44:47], v[20:23], v[224:227]\nds_read_b128 v[116:119], v252 offset:11264\nv_mfma_f32_16x16x32_bf16 v[224:227], v[40:43], v[16:19], v[224:227]\n"
      "ds_read_b128 v[120:123], v252 offset:12288\nv_add_u32_sdwa v239, s49, v245 dst_sel:DWORD dst_unused:UNUSED_PAD src0_sel:DWORD src1_sel:WORD_1\n"
      "v_mfma_f32_16x16x32_bf16 v[228:231], v[48:51], v[16:19], v[228:231]\nv_add_u32_sdwa v236, s49, v244 dst_sel:DWORD dst_unused:UNUSED_PAD src0_sel:DWORD src1_sel:WORD_0\n"
      "v_mfma_f32_16x16x32_bf16 v[228:231], v[52:55], v[20:23], v[228:231]\nv_add_u32_sdwa v237, s49, v244 dst_sel:DWORD dst_unused:UNUSED_PAD src0_sel:DWORD src1_sel:WORD_1\n"
      "v_mfma_f32_16x16x32_bf16 v[232:235], v[60:63], v[20:23], v[232:235]\nv_add_u32_sdwa v238, s49, v245 dst_sel:DWORD dst_unused:UNUSED_PAD src0_sel:DWORD src1_sel:WORD_0\ns_waitcnt lgkmcnt(0)\n"
      "v_mfma_f32_16x16x32_bf16 v[232:235], v[56:59], v[16:19], v[232:235]\nv_pk_mul_f32 v[44:45], v[124:125], v[124:125]\nv_pk_mul_f32 v[46:47], v[126:127], v[126:127]\n"
      "v_pk_mul_f32 v[48:49], v[128:129], v[128:129]\nv_pk_mul_f32 v[50:51], v[130:131], v[130:131]\nv_pk_fma_f32 v[44:45], v[44:45], s[58:59], s[58:59] op_sel:[0,0,1] op_sel_hi:[1,0,1]\n"
      "v_pk_fma_f32 v[46:47], v[46:47], s[58:59], s[58:59] op_sel:[0,0,1] op_sel_hi:[1,0,1]\nv_pk_fma_f32 v[48:49], v[48:49], s[58:59], s[58:59] op_sel:[0,0,1] op_sel_hi:[1,0,1]\n"
      "v_pk_fma_f32 v[50:51], v[50:51], s[58:59], s[58:59] op_sel:[0,0,1] op_sel_hi:[1,0,1]\nv_pk_fma_f32 v[44:45], v[44:45], v[124:125], s[60:61] op_sel_hi:[1,1,0]\n"
      "v_pk_fma_f32 v[46:47], v[46:47], v[126:127], s[60:61] op_sel_hi:[1,1,0]\nv_pk_fma_f32 v[48:49], v[48:49], v[128:129], s[60:61] op_sel_hi:[1,1,0]\n"
      "v_pk_fma_f32 v[50:51], v[50:51], v[130:131], s[60:61] op_sel_hi:[1,1,0]\nv_exp_f32 v44, v44 clamp\nv_pk_mul_f32 v[52:53], v[132:133], v[132:133]\nv_exp_f32 v45, v45 clamp\n"
      "v_pk_mul_f32 v[54:55], v[134:135], v[134:135]\nv_pk_mul_f32 v[56:57], v[136:137], v[136:137]\nv_exp_f32 v46, v46 clamp\nv_pk_mul_f32 v[58:59], v[138:139], v[138:139]\n"
      "v_exp_f32 v47, v47 clamp\nv_pk_fma_f32 v[52:53], v[52:53], s[58:59], s[58:59] op_sel:[0,0,1] op_sel_hi:[1,0,1]\n"
      "v_pk_fma_f32 v[54:55], v[54:55], s[58:59], s[58:59] op_sel:[0,0,1] op_sel_hi:[1,0,1]\nv_exp_f32 v48, v48 clamp\n"
      "v_pk_fma_f32 v[56:57], v[56:57], s[58:59], s[58:59] op_sel:[0,0,1] op_sel_hi:[1,0,1]\nv_exp_f32 v49, v49 clamp\n"
      "v_pk_fma_f32 v[58:59], v[58:59], s[58:59], s[58:59] op_sel:[0,0,1] op_sel_hi:[1,0,1]\nv_pk_fma_f32 v[52:53], v[52:53], v[132:133], s[60:61] op_sel_hi:[1,1,0]\nv_exp_f32 v50, v50 clamp\n"
      "v_pk_fma_f32 v[54:55], v[54:55], v[134:135], s[60:61] op_sel_hi:[1,1,0]\nv_exp_f32 v51, v51 clamp\nv_pk_fma_f32 v[56:57], v[56:57], v[136:137], s[60:61] op_sel_hi:[1,1,0]\n"
      "v_pk_fma_f32 v[58:59], v[58:59], v[138:139], s[60:61] op_sel_hi:[1,1,0]\nv_exp_f32 v52, v52 clamp\nv_pk_fma_f32 v[46:47], v[46:47], v[2:3], v[26:27]\n"
      "v_pk_fma_f32 v[44:45], v[44:45], v[0:1], v[24:25]\nv_pk_fma_f32 v[48:49], v[48:49], v[4:5], v[28:29]\nv_exp_f32 v53, v53 clamp\nv_pk_fma_f32 v[50:51], v[50:51], v[6:7], v[30:31]\n"
      "v_pk_mul_f32 v[124:125], v[124:125], v[46:47]\nv_pk_mul_f32 v[128:129], v[128:129], v[50:51]\nv_exp_f32 v54, v54 clamp\nv_pk_fma_f32 v[124:125], v[126:127], v[44:45], v[124:125]\n"
      "v_pk_fma_f32 v[128:129], v[130:131], v[48:49], v[128:129]\nv_pk_mul_f32 v[44:45], v[44:45], v[46:47]\nv_exp_f32 v55, v55 clamp\nv_pk_mul_f32 v[48:49], v[48:49], v[50:51]\n"
      "v_pk_mul_f32 v[46:47], v[44:45], v[48:49]\nv_rcp_f32 v46, v46\nv_pk_mul_f32 v[124:125], v[124:125], v[48:49]\nv_rcp_f32 v47, v47\nv_pk_fma_f32 v[124:125], v[128:129], v[44:45], v[124:125]\n"
      "v_pk_mul_f32 v[32:33], v[124:125], v[46:47]\nv_pk_mul_f32 v[16:17], v[140:141], v[140:141]\nv_pk_mul_f32 v[18:19], v[142:143], v[142:143]\nv_exp_f32 v56, v56 clamp\n"
      "v_pk_mul_f32 v[20:21], v[144:145], v[144:145]\nv_pk_mul_f32 v[22:23], v[146:147], v[146:147]\nv_exp_f32 v57, v57 clamp\n"
      "v_pk_fma_f32 v[16:17], v[16:17], s[58:59], s[58:59] op_sel:[0,0,1] op_sel_hi:[1,0,1]\nv_pk_fma_f32 v[18:19], v[18:19], s[58:59], s[58:59] op_sel:[0,0,1] op_sel_hi:[1,0,1]\n"
      "v_pk_fma_f32 v[20:21], v[20:21], s[58:59], s[58:59] op_sel:[0,0,1] op_sel_hi:[1,0,1]\nv_exp_f32 v58, v58 clamp\n"
      "v_pk_fma_f32 v[22:23], v[22:23], s[58:59], s[58:59] op_sel:[0,0,1] op_sel_hi:[1,0,1]\nv_pk_fma_f32 v[16:17], v[16:17], v[140:141], s[60:61] op_sel_hi:[1,1,0]\n"
      "v_pk_fma_f32 v[18:19], v[18:19], v[142:143], s[60:61] op_sel_hi:[1,1,0]\nv_exp_f32 v59, v59 clamp\nv_pk_fma_f32 v[20:21], v[20:21], v[144:145], s[60:61] op_sel_hi:[1,1,0]\n"
      "v_pk_fma_f32 v[22:23], v[22:23], v[146:147], s[60:61] op_sel_hi:[1,1,0]\nv_exp_f32 v16, v16 clamp\nv_pk_fma_f32 v[54:55], v[54:55], v[10:11], v[66:67]\n"
      "v_pk_fma_f32 v[52:53], v[52:53], v[8:9], v[64:65]\nv_pk_fma_f32 v[56:57], v[56:57], v[12:13], v[68:69]\nv_exp_f32 v17, v17 clamp\nv_pk_fma_f32 v[58:59], v[58:59], v[14:15], v[70:71]\n"
      "v_pk_mul_f32 v[132:133], v[132:133], v[54:55]\nv_pk_mul_f32 v[136:137], v[136:137], v[58:59]\nv_exp_f32 v18, v18 clamp\nv_pk_fma_f32 v[132:133], v[134:135], v[52:53], v[132:133]\n"
      "v_pk_fma_f32 v[136:137], v[138:139], v[56:57], v[136:137]\nv_pk_mul_f32 v[52:53], v[52:53], v[54:55]\nv_exp_f32 v19, v19 clamp\nv_pk_mul_f32 v[56:57], v[56:57], v[58:59]\n"
      "v_pk_mul_f32 v[54:55], v[52:53], v[56:57]\nv_rcp_f32 v54, v54\nv_pk_mul_f32 v[132:133], v[132:133], v[56:57]\nv_rcp_f32 v55, v55\nv_pk_fma_f32 v[132:133], v[136:137], v[52:53], v[132:133]\n"
      "v_pk_fma_f32 v[32:33], v[132:133], v[54:55], v[32:33]\nv_add_f32 v42, v32, v33\nv_pk_mul_f32 v[44:45], v[148:149], v[148:149]\nv_pk_mul_f32 v[46:47], v[150:151], v[150:151]\n"
      "v_exp_f32 v20, v20 clamp\nv_pk_mul_f32 v[48:49], v[152:153], v[152:153]\nv_pk_mul_f32 v[50:51], v[154:155], v[154:155]\nv_exp_f32 v21, v21 clamp\n"
      "v_pk_fma_f32 v[44:45], v[44:45], s[58:59], s[58:59] op_sel:[0,0,1] op_sel_hi:[1,0,1]\nv_pk_fma_f32 v[46:47], v[46:47], s[58:59], s[58:59] op_sel:[0,0,1] op_sel_hi:[1,0,1]\n"
      "v_pk_fma_f32 v[48:49], v[48:49], s[58:59], s[58:59] op_sel:[0,0,1] op_sel_hi:[1,0,1]\nv_exp_f32 v22, v22 clamp\n"
      "v_pk_fma_f32 v[50:51], v[50:51], s[58:59], s[58:59] op_sel:[0,0,1] op_sel_hi:[1,0,1]\nv_pk_fma_f32 v[44:45], v[44:45], v[148:149], s[60:61] op_sel_hi:[1,1,0]\n"
      "v_pk_fma_f32 v[46:47], v[46:47], v[150:151], s[60:61] op_sel_hi:[1,1,0]\nv_exp_f32 v23, v23 clamp\nv_pk_fma_f32 v[48:49], v[48:49], v[152:153], s[60:61] op_sel_hi:[1,1,0]\n"
      "v_pk_fma_f32 v[50:51], v[50:51], v[154:155], s[60:61] op_sel_hi:[1,1,0]\nv_exp_f32 v44, v44 clamp\nv_pk_fma_f32 v[18:19], v[18:19], v[2:3], v[26:27]\n"
      "v_pk_fma_f32 v[16:17], v[16:17], v[0:1], v[24:25]\nv_pk_fma_f32 v[20:21], v[20:21], v[4:5], v[28:29]\nv_exp_f32 v45, v45 clamp\nv_pk_fma_f32 v[22:23], v[22:23], v[6:7], v[30:31]\n"
      "v_pk_mul_f32 v[140:141], v[140:141], v[18:19]\nv_pk_mul_f32 v[144:145], v[144:145], v[22:23]\nv_exp_f32 v46, v46 clamp\nv_pk_fma_f32 v[140:141], v[142:143], v[16:17], v[140:141]\n"
      "v_pk_fma_f32 v[144:145], v[146:147], v[20:21], v[144:145]\nv_pk_mul_f32 v[16:17], v[16:17], v[18:19]\nv_exp_f32 v47, v47 clamp\nv_pk_mul_f32 v[20:21], v[20:21], v[22:23]\n"
      "v_pk_mul_f32 v[18:19], v[16:17], v[20:21]\nv_rcp_f32 v18, v18\nv_pk_mul_f32 v[140:141], v[140:141], v[20:21]\nv_rcp_f32 v19, v19\nv_pk_fma_f32 v[140:141], v[144:145], v[16:17], v[140:141]\n"
      "v_pk_mul_f32 v[32:33], v[140:141], v[18:19]\nv_pk_mul_f32 v[52:53], v[156:157], v[156:157]\nv_pk_mul_f32 v[54:55], v[158:159], v[158:159]\nv_exp_f32 v48, v48 clamp\n"
      "v_pk_mul_f32 v[56:57], v[160:161], v[160:161]\nv_pk_mul_f32 v[58:59], v[162:163], v[162:163]\nv_exp_f32 v49, v49 clamp\n"
      "v_pk_fma_f32 v[52:53], v[52:53], s[58:59], s[58:59] op_sel:[0,0,1] op_sel_hi:[1,0,1]\nv_pk_fma_f32 v[54:55], v[54:55], s[58:59], s[58:59] op_sel:[0,0,1] op_sel_hi:[1,0,1]\n"
      "v_pk_fma_f32 v[56:57], v[56:57], s[58:59], s[58:59] op_sel:[0,0,1] op_sel_hi:[1,0,1]\nv_exp_f32 v50, v50 clamp\n"
      "v_pk_fma_f32 v[58:59], v[58:59], s[58:59], s[58:59] op_sel:[0,0,1] op_sel_hi:[1,0,1]\nv_pk_fma_f32 v[52:53], v[52:53], v[156:157], s[60:61] op_sel_hi:[1,1,0]\n"
      "v_pk_fma_f32 v[54:55], v[54:55], v[158:159], s[60:61] op_sel_hi:[1,1,0]\nv_exp_f32 v51, v51 clamp\nv_pk_fma_f32 v[56:57], v[56:57], v[160:161], s[60:61] op_sel_hi:[1,1,0]\n"
      "v_pk_fma_f32 v[58:59], v[58:59], v[162:163], s[60:61] op_sel_hi:[1,1,0]\nv_exp_f32 v52, v52 clamp\nv_pk_fma_f32 v[46:47], v[46:47], v[10:11], v[66:67]\n"
      "v_pk_fma_f32 v[44:45], v[44:45], v[8:9], v[64:65]\nv_pk_fma_f32 v[48:49], v[48:49], v[12:13], v[68:69]\nv_exp_f32 v53, v53 clamp\nv_pk_fma_f32 v[50:51], v[50:51], v[14:15], v[70:71]\n"
      "v_pk_mul_f32 v[148:149], v[148:149], v[46:47]\nv_pk_mul_f32 v[152:153], v[152:153], v[50:51]\nv_exp_f32 v54, v54 clamp\nv_pk_fma_f32 v[148:149], v[150:151], v[44:45], v[148:149]\n"
      "v_pk_fma_f32 v[152:153], v[154:155], v[48:49], v[152:153]\nv_pk_mul_f32 v[44:45], v[44:45], v[46:47]\nv_exp_f32 v55, v55 clamp\nv_pk_mul_f32 v[48:49], v[48:49], v[50:51]\n"
      "v_pk_mul_f32 v[46:47], v[44:45], v[48:49]\nv_rcp_f32 v46, v46\nv_pk_mul_f32 v[148:149], v[148:149], v[48:49]\nv_rcp_f32 v47, v47\nv_pk_fma_f32 v[148:149], v[152:153], v[44:45], v[148:149]\n"
      "v_pk_fma_f32 v[32:33], v[148:149], v[46:47], v[32:33]\nv_add_f32 v41, v32, v33\nv_pk_mul_f32 v[16:17], v[164:165], v[164:165]\nv_pk_mul_f32 v[18:19], v[166:167], v[166:167]\n"
      "v_exp_f32 v56, v56 clamp\nv_pk_mul_f32 v[20:21], v[168:169], v[168:169]\nv_pk_mul_f32 v[22:23], v[170:171], v[170:171]\nv_exp_f32 v57, v57 clamp\n"
      "v_pk_fma_f32 v[16:17], v[16:17], s[58:59], s[58:59] op_sel:[0,0,1] op_sel_hi:[1,0,1]\nv_pk_fma_f32 v[18:19], v[18:19], s[58:59], s[58:59] op_sel:[0,0,1] op_sel_hi:[1,0,1]\n"
      "v_pk_fma_f32 v[20:21], v[20:21], s[58:59], s[58:59] op_sel:[0,0,1] op_sel_hi:[1,0,1]\nv_exp_f32 v58, v58 clamp\n"
      "v_pk_fma_f32 v[22:23], v[22:23], s[58:59], s[58:59] op_sel:[0,0,1] op_sel_hi:[1,0,1]\nv_pk_fma_f32 v[16:17], v[16:17], v[164:165], s[60:61] op_sel_hi:[1,1,0]\n"
      "v_pk_fma_f32 v[18:19], v[18:19], v[166:167], s[60:61] op_sel_hi:[1,1,0]\nv_exp_f32 v59, v59 clamp\nv_pk_fma_f32 v[20:21], v[20:21], v[168:169], s[60:61] op_sel_hi:[1,1,0]\n"
      "v_pk_fma_f32 v[22:23], v[22:23], v[170:171], s[60:61] op_sel_hi:[1,1,0]\nv_exp_f32 v16, v16 clamp\nv_pk_fma_f32 v[54:55], v[54:55], v[2:3], v[26:27]\n"
      "v_pk_fma_f32 v[52:53], v[52:53], v[0:1], v[24:25]\nv_pk_fma_f32 v[56:57], v[56:57], v[4:5], v[28:29]\nv_exp_f32 v17, v17 clamp\nv_pk_fma_f32 v[58:59], v[58:59], v[6:7], v[30:31]\n"
      "v_pk_mul_f32 v[156:157], v[156:157], v[54:55]\nv_pk_mul_f32 v[160:161], v[160:161], v[58:59]\nv_exp_f32 v18, v18 clamp\nv_pk_fma_f32 v[156:157], v[158:159], v[52:53], v[156:157]\n"
      "v_pk_fma_f32 v[160:161], v[162:163], v[56:57], v[160:161]\nv_pk_mul_f32 v[52:53], v[52:53], v[54:55]\nv_exp_f32 v19, v19 clamp\nv_pk_mul_f32 v[56:57], v[56:57], v[58:59]\n"
      "v_pk_mul_f32 v[54:55], v[52:53], v[56:57]\nv_rcp_f32 v54, v54\nv_pk_mul_f32 v[156:157], v[156:157], v[56:57]\nv_rcp_f32 v55, v55\nv_pk_fma_f32 v[156:157], v[160:161], v[52:53], v[156:157]\n"
      "v_pk_mul_f32 v[32:33], v[156:157], v[54:55]\nv_pk_mul_f32 v[44:45], v[172:173], v[172:173]\nv_pk_mul_f32 v[46:47], v[174:175], v[174:175]\nv_exp_f32 v20, v20 clamp\n"
      "v_pk_mul_f32 v[48:49], v[176:177], v[176:177]\nv_pk_mul_f32 v[50:51], v[178:179], v[178:179]\nv_exp_f32 v21, v21 clamp\n"
      "v_pk_fma_f32 v[44:45], v[44:45], s[58:59], s[58:59] op_sel:[0,0,1] op_sel_hi:[1,0,1]\nv_pk_fma_f32 v[46:47], v[46:47], s[58:59], s[58:59] op_sel:[0,0,1] op_sel_hi:[1,0,1]\n"
      "v_pk_fma_f32 v[48:49], v[48:49], s[58:59], s[58:59] op_sel:[0,0,1] op_sel_hi:[1,0,1]\nv_exp_f32 v22, v22 clamp\n"
      "v_pk_fma_f32 v[50:51], v[50:51], s[58:59], s[58:59] op_sel:[0,0,1] op_sel_hi:[1,0,1]\nv_pk_fma_f32 v[44:45], v[44:45], v[172:173], s[60:61] op_sel_hi:[1,1,0]\n"
      "v_pk_fma_f32 v[46:47], v[46:47], v[174:175], s[60:61] op_sel_hi:[1,1,0]\nv_exp_f32 v23, v23 clamp\nv_pk_fma_f32 v[48:49], v[48:49], v[176:177], s[60:61] op_sel_hi:[1,1,0]\n"
      "v_pk_fma_f32 v[50:51], v[50:51], v[178:179], s[60:61] op_sel_hi:[1,1,0]\nv_exp_f32 v44, v44 clamp\nv_pk_fma_f32 v[18:19], v[18:19], v[10:11], v[66:67]\n"
      "v_pk_fma_f32 v[16:17], v[16:17], v[8:9], v[64:65]\nv_pk_fma_f32 v[20:21], v[20:21], v[12:13], v[68:69]\nv_exp_f32 v45, v45 clamp\nv_pk_fma_f32 v[22:23], v[22:23], v[14:15], v[70:71]\n"
      "v_pk_mul_f32 v[164:165], v[164:165], v[18:19]\nv_pk_mul_f32 v[168:169], v[168:169], v[22:23]\nv_exp_f32 v46, v46 clamp\nv_pk_fma_f32 v[164:165], v[166:167], v[16:17], v[164:165]\n"
      "v_pk_fma_f32 v[168:169], v[170:171], v[20:21], v[168:169]\nv_pk_mul_f32 v[16:17], v[16:17], v[18:19]\nv_exp_f32 v47, v47 clamp\nv_pk_mul_f32 v[20:21], v[20:21], v[22:23]\n"
      "v_pk_mul_f32 v[18:19], v[16:17], v[20:21]\nv_rcp_f32 v18, v18\nv_pk_mul_f32 v[164:165], v[164:165], v[20:21]\nv_rcp_f32 v19, v19\nv_pk_fma_f32 v[164:165], v[168:169], v[16:17], v[164:165]\n"
      "v_pk_fma_f32 v[32:33], v[164:165], v[18:19], v[32:33]\nv_add_f32 v36, v32, v33\nv_pk_mul_f32 v[52:53], v[180:181], v[180:181]\nv_pk_mul_f32 v[54:55], v[182:183], v[182:183]\n"
      "v_exp_f32 v48, v48 clamp\nv_pk_mul_f32 v[56:57], v[184:185], v[184:185]\nv_pk_mul_f32 v[58:59], v[186:187], v[186:187]\nv_exp_f32 v49, v49 clamp\n"
      "v_pk_fma_f32 v[52:53], v[52:53], s[58:59], s[58:59] op_sel:[0,0,1] op_sel_hi:[1,0,1]\nv_pk_fma_f32 v[54:55], v[54:55], s[58:59], s[58:59] op_sel:[0,0,1] op_sel_hi:[1,0,1]\n"
      "v_pk_fma_f32 v[56:57], v[56:57], s[58:59], s[58:59] op_sel:[0,0,1] op_sel_hi:[1,0,1]\nv_exp_f32 v50, v50 clamp\n"
      "v_pk_fma_f32 v[58:59], v[58:59], s[58:59], s[58:59] op_sel:[0,0,1] op_sel_hi:[1,0,1]\nv_pk_fma_f32 v[52:53], v[52:53], v[180:181], s[60:61] op_sel_hi:[1,1,0]\n"
      "v_pk_fma_f32 v[54:55], v[54:55], v[182:183], s[60:61] op_sel_hi:[1,1,0]\nv_exp_f32 v51, v51 clamp\nv_pk_fma_f32 v[56:57], v[56:57], v[184:185], s[60:61] op_sel_hi:[1,1,0]\n"
      "v_pk_fma_f32 v[58:59], v[58:59], v[186:187], s[60:61] op_sel_hi:[1,1,0]\nv_exp_f32 v52, v52 clamp\nv_pk_fma_f32 v[46:47], v[46:47], v[2:3], v[26:27]\n"
      "v_pk_fma_f32 v[44:45], v[44:45], v[0:1], v[24:25]\nv_pk_fma_f32 v[48:49], v[48:49], v[4:5], v[28:29]\nv_exp_f32 v53, v53 clamp\nv_pk_fma_f32 v[50:51], v[50:51], v[6:7], v[30:31]\n"
      "v_pk_mul_f32 v[172:173], v[172:173], v[46:47]\nv_pk_mul_f32 v[176:177], v[176:177], v[50:51]\nv_exp_f32 v54, v54 clamp\nv_pk_fma_f32 v[172:173], v[174:175], v[44:45], v[172:173]\n"
      "v_pk_fma_f32 v[176:177], v[178:179], v[48:49], v[176:177]\nv_pk_mul_f32 v[44:45], v[44:45], v[46:47]\nv_exp_f32 v55, v55 clamp\nv_pk_mul_f32 v[48:49], v[48:49], v[50:51]\n"
      "v_pk_mul_f32 v[46:47], v[44:45], v[48:49]\nv_rcp_f32 v46, v46\nv_pk_mul_f32 v[172:173], v[172:173], v[48:49]\nv_rcp_f32 v47, v47\nv_pk_fma_f32 v[172:173], v[176:177], v[44:45], v[172:173]\n"
      "v_pk_mul_f32 v[32:33], v[172:173], v[46:47]\nv_pk_mul_f32 v[16:17], v[188:189], v[188:189]\nv_pk_mul_f32 v[18:19], v[190:191], v[190:191]\nv_exp_f32 v56, v56 clamp\n"
      "v_pk_mul_f32 v[20:21], v[192:193], v[192:193]\nv_pk_mul_f32 v[22:23], v[194:195], v[194:195]\nv_exp_f32 v57, v57 clamp\n"
      "v_pk_fma_f32 v[16:17], v[16:17], s[58:59], s[58:59] op_sel:[0,0,1] op_sel_hi:[1,0,1]\nv_pk_fma_f32 v[18:19], v[18:19], s[58:59], s[58:59] op_sel:[0,0,1] op_sel_hi:[1,0,1]\n"
      "v_pk_fma_f32 v[20:21], v[20:21], s[58:59], s[58:59] op_sel:[0,0,1] op_sel_hi:[1,0,1]\nv_exp_f32 v58, v58 clamp\n"
      "v_pk_fma_f32 v[22:23], v[22:23], s[58:59], s[58:59] op_sel:[0,0,1] op_sel_hi:[1,0,1]\nv_pk_fma_f32 v[16:17], v[16:17], v[188:189], s[60:61] op_sel_hi:[1,1,0]\n"
      "v_pk_fma_f32 v[18:19], v[18:19], v[190:191], s[60:61] op_sel_hi:[1,1,0]\nv_exp_f32 v59, v59 clamp\nv_pk_fma_f32 v[20:21], v[20:21], v[192:193], s[60:61] op_sel_hi:[1,1,0]\n"
      "v_pk_fma_f32 v[22:23], v[22:23], v[194:195], s[60:61] op_sel_hi:[1,1,0]\nv_exp_f32 v16, v16 clamp\nv_pk_fma_f32 v[54:55], v[54:55], v[10:11], v[66:67]\n"
      "v_pk_fma_f32 v[52:53], v[52:53], v[8:9], v[64:65]\nv_pk_fma_f32 v[56:57], v[56:57], v[12:13], v[68:69]\nv_exp_f32 v17, v17 clamp\nv_pk_fma_f32 v[58:59], v[58:59], v[14:15], v[70:71]\n"
      "v_pk_mul_f32 v[180:181], v[180:181], v[54:55]\nv_pk_mul_f32 v[184:185], v[184:185], v[58:59]\nv_exp_f32 v18, v18 clamp\nv_pk_fma_f32 v[180:181], v[182:183], v[52:53], v[180:181]\n"
      "v_pk_fma_f32 v[184:185], v[186:187], v[56:57], v[184:185]\nv_pk_mul_f32 v[52:53], v[52:53], v[54:55]\nv_exp_f32 v19, v19 clamp\nv_pk_mul_f32 v[56:57], v[56:57], v[58:59]\n"
      "v_pk_mul_f32 v[54:55], v[52:53], v[56:57]\nv_rcp_f32 v54, v54\nv_pk_mul_f32 v[180:181], v[180:181], v[56:57]\nv_rcp_f32 v55, v55\nv_pk_fma_f32 v[180:181], v[184:185], v[52:53], v[180:181]\n"
      "v_pk_fma_f32 v[32:33], v[180:181], v[54:55], v[32:33]\nv_add_f32 v37, v32, v33\nv_pk_mul_f32 v[44:45], v[196:197], v[196:197]\nv_pk_mul_f32 v[46:47], v[198:199], v[198:199]\n"
      "v_exp_f32 v20, v20 clamp\nv_pk_mul_f32 v[48:49], v[200:201], v[200:201]\nv_pk_mul_f32 v[50:51], v[202:203], v[202:203]\nv_exp_f32 v21, v21 clamp\n"
      "v_pk_fma_f32 v[44:45], v[44:45], s[58:59], s[58:59] op_sel:[0,0,1] op_sel_hi:[1,0,1]\nv_pk_fma_f32 v[46:47], v[46:47], s[58:59], s[58:59] op_sel:[0,0,1] op_sel_hi:[1,0,1]\n"
      "v_pk_fma_f32 v[48:49], v[48:49], s[58:59], s[58:59] op_sel:[0,0,1] op_sel_hi:[1,0,1]\nv_exp_f32 v22, v22 clamp\n"
      "v_pk_fma_f32 v[50:51], v[50:51], s[58:59], s[58:59] op_sel:[0,0,1] op_sel_hi:[1,0,1]\nv_pk_fma_f32 v[44:45], v[44:45], v[196:197], s[60:61] op_sel_hi:[1,1,0]\n"
      "v_pk_fma_f32 v[46:47], v[46:47], v[198:199], s[60:61] op_sel_hi:[1,1,0]\nv_exp_f32 v23, v23 clamp\nv_pk_fma_f32 v[48:49], v[48:49], v[200:201], s[60:61] op_sel_hi:[1,1,0]\n"
      "v_pk_fma_f32 v[50:51], v[50:51], v[202:203], s[60:61] op_sel_hi:[1,1,0]\nv_exp_f32 v44, v44 clamp\nv_pk_fma_f32 v[18:19], v[18:19], v[2:3], v[26:27]\n"
      "v_pk_fma_f32 v[16:17], v[16:17], v[0:1], v[24:25]\nv_pk_fma_f32 v[20:21], v[20:21], v[4:5], v[28:29]\nv_exp_f32 v45, v45 clamp\nv_pk_fma_f32 v[22:23], v[22:23], v[6:7], v[30:31]\n"
      "v_pk_mul_f32 v[188:189], v[188:189], v[18:19]\nv_pk_mul_f32 v[192:193], v[192:193], v[22:23]\nv_exp_f32 v46, v46 clamp\nv_pk_fma_f32 v[188:189], v[190:191], v[16:17], v[188:189]\n"
      "v_pk_fma_f32 v[192:193], v[194:195], v[20:21], v[192:193]\nv_pk_mul_f32 v[16:17], v[16:17], v[18:19]\nv_exp_f32 v47, v47 clamp\nv_pk_mul_f32 v[20:21], v[20:21], v[22:23]\n"
      "v_pk_mul_f32 v[18:19], v[16:17], v[20:21]\nv_rcp_f32 v18, v18\nv_pk_mul_f32 v[188:189], v[188:189], v[20:21]\nv_rcp_f32 v19, v19\nv_pk_fma_f32 v[188:189], v[192:193], v[16:17], v[188:189]\n"
      "v_pk_mul_f32 v[32:33], v[188:189], v[18:19]\nv_pk_mul_f32 v[52:53], v[204:205], v[204:205]\nv_pk_mul_f32 v[54:55], v[206:207], v[206:207]\nv_exp_f32 v48, v48 clamp\n"
      "v_pk_mul_f32 v[56:57], v[208:209], v[208:209]\nv_pk_mul_f32 v[58:59], v[210:211], v[210:211]\nv_exp_f32 v49, v49 clamp\n"
      "v_pk_fma_f32 v[52:53], v[52:53], s[58:59], s[58:59] op_sel:[0,0,1] op_sel_hi:[1,0,1]\nv_pk_fma_f32 v[54:55], v[54:55], s[58:59], s[58:59] op_sel:[0,0,1] op_sel_hi:[1,0,1]\n"
      "v_pk_fma_f32 v[56:57], v[56:57], s[58:59], s[58:59] op_sel:[0,0,1] op_sel_hi:[1,0,1]\nv_exp_f32 v50, v50 clamp\n"
      "v_pk_fma_f32 v[58:59], v[58:59], s[58:59], s[58:59] op_sel:[0,0,1] op_sel_hi:[1,0,1]\nv_pk_fma_f32 v[52:53], v[52:53], v[204:205], s[60:61] op_sel_hi:[1,1,0]\n"
      "v_pk_fma_f32 v[54:55], v[54:55], v[206:207], s[60:61] op_sel_hi:[1,1,0]\nv_exp_f32 v51, v51 clamp\nv_pk_fma_f32 v[56:57], v[56:57], v[208:209], s[60:61] op_sel_hi:[1,1,0]\n"
      "v_pk_fma_f32 v[58:59], v[58:59], v[210:211], s[60:61] op_sel_hi:[1,1,0]\nv_exp_f32 v52, v52 clamp\nv_pk_fma_f32 v[46:47], v[46:47], v[10:11], v[66:67]\n"
      "v_pk_fma_f32 v[44:45], v[44:45], v[8:9], v[64:65]\nv_pk_fma_f32 v[48:49], v[48:49], v[12:13], v[68:69]\nv_exp_f32 v53, v53 clamp\nv_pk_fma_f32 v[50:51], v[50:51], v[14:15], v[70:71]\n"
      "v_pk_mul_f32 v[196:197], v[196:197], v[46:47]\nv_pk_mul_f32 v[200:201], v[200:201], v[50:51]\nv_exp_f32 v54, v54 clamp\nv_pk_fma_f32 v[196:197], v[198:199], v[44:45], v[196:197]\n"
      "v_pk_fma_f32 v[200:201], v[202:203], v[48:49], v[200:201]\nv_pk_mul_f32 v[44:45], v[44:45], v[46:47]\nv_exp_f32 v55, v55 clamp\nv_pk_mul_f32 v[48:49], v[48:49], v[50:51]\n"
      "v_pk_mul_f32 v[46:47], v[44:45], v[48:49]\nv_rcp_f32 v46, v46\nv_pk_mul_f32 v[196:197], v[196:197], v[48:49]\nv_rcp_f32 v47, v47\nv_pk_fma_f32 v[196:197], v[200:201], v[44:45], v[196:197]\n"
      "v_pk_fma_f32 v[32:33], v[196:197], v[46:47], v[32:33]\nv_add_f32 v38, v32, v33\nv_pk_mul_f32 v[16:17], v[212:213], v[212:213]\nv_pk_mul_f32 v[18:19], v[214:215], v[214:215]\n"
      "v_exp_f32 v56, v56 clamp\nv_pk_mul_f32 v[20:21], v[216:217], v[216:217]\nv_pk_mul_f32 v[22:23], v[218:219], v[218:219]\nv_exp_f32 v57, v57 clamp\n"
      "v_pk_fma_f32 v[16:17], v[16:17], s[58:59], s[58:59] op_sel:[0,0,1] op_sel_hi:[1,0,1]\nv_pk_fma_f32 v[18:19], v[18:19], s[58:59], s[58:59] op_sel:[0,0,1] op_sel_hi:[1,0,1]\n"
      "v_pk_fma_f32 v[20:21], v[20:21], s[58:59], s[58:59] op_sel:[0,0,1] op_sel_hi:[1,0,1]\nv_exp_f32 v58, v58 clamp\n"
      "v_pk_fma_f32 v[22:23], v[22:23], s[58:59], s[58:59] op_sel:[0,0,1] op_sel_hi:[1,0,1]\nv_pk_fma_f32 v[16:17], v[16:17], v[212:213], s[60:61] op_sel_hi:[1,1,0]\n"
      "v_pk_fma_f32 v[18:19], v[18:19], v[214:215], s[60:61] op_sel_hi:[1,1,0]\nv_exp_f32 v59, v59 clamp\nv_pk_fma_f32 v[20:21], v[20:21], v[216:217], s[60:61] op_sel_hi:[1,1,0]\n"
      "v_pk_fma_f32 v[22:23], v[22:23], v[218:219], s[60:61] op_sel_hi:[1,1,0]\nv_exp_f32 v16, v16 clamp\nv_pk_fma_f32 v[54:55], v[54:55], v[2:3], v[26:27]\n"
      "v_pk_fma_f32 v[52:53], v[52:53], v[0:1], v[24:25]\nv_pk_fma_f32 v[56:57], v[56:57], v[4:5], v[28:29]\nv_exp_f32 v17, v17 clamp\nv_pk_fma_f32 v[58:59], v[58:59], v[6:7], v[30:31]\n"
      "v_pk_mul_f32 v[204:205], v[204:205], v[54:55]\nv_pk_mul_f32 v[208:209], v[208:209], v[58:59]\nv_exp_f32 v18, v18 clamp\nv_pk_fma_f32 v[204:205], v[206:207], v[52:53], v[204:205]\n"
      "v_pk_fma_f32 v[208:209], v[210:211], v[56:57], v[208:209]\nv_pk_mul_f32 v[52:53], v[52:53], v[54:55]\nv_exp_f32 v19, v19 clamp\nv_pk_mul_f32 v[56:57], v[56:57], v[58:59]\n"
      "v_pk_mul_f32 v[54:55], v[52:53], v[56:57]\nv_rcp_f32 v54, v54\nv_pk_mul_f32 v[204:205], v[204:205], v[56:57]\nv_rcp_f32 v55, v55\nv_pk_fma_f32 v[204:205], v[208:209], v[52:53], v[204:205]\n"
      "v_pk_mul_f32 v[32:33], v[204:205], v[54:55]\nv_pk_mul_f32 v[44:45], v[220:221], v[220:221]\nv_pk_mul_f32 v[46:47], v[222:223], v[222:223]\nv_exp_f32 v20, v20 clamp\n"
      "v_pk_mul_f32 v[48:49], v[224:225], v[224:225]\nv_pk_mul_f32 v[50:51], v[226:227], v[226:227]\nv_exp_f32 v21, v21 clamp\n"
      "v_pk_fma_f32 v[44:45], v[44:45], s[58:59], s[58:59] op_sel:[0,0,1] op_sel_hi:[1,0,1]\nv_pk_fma_f32 v[46:47], v[46:47], s[58:59], s[58:59] op_sel:[0,0,1] op_sel_hi:[1,0,1]\n"
      "v_pk_fma_f32 v[48:49], v[48:49], s[58:59], s[58:59] op_sel:[0,0,1] op_sel_hi:[1,0,1]\nv_exp_f32 v22, v22 clamp\n"
      "v_pk_fma_f32 v[50:51], v[50:51], s[58:59], s[58:59] op_sel:[0,0,1] op_sel_hi:[1,0,1]\nv_pk_fma_f32 v[44:45], v[44:45], v[220:221], s[60:61] op_sel_hi:[1,1,0]\n"
      "v_pk_fma_f32 v[46:47], v[46:47], v[222:223], s[60:61] op_sel_hi:[1,1,0]\nv_exp_f32 v23, v23 clamp\nv_pk_fma_f32 v[48:49], v[48:49], v[224:225], s[60:61] op_sel_hi:[1,1,0]\n"
      "v_pk_fma_f32 v[50:51], v[50:51], v[226:227], s[60:61] op_sel_hi:[1,1,0]\nv_exp_f32 v44, v44 clamp\nv_pk_fma_f32 v[18:19], v[18:19], v[10:11], v[66:67]\n"
      "v_pk_fma_f32 v[16:17], v[16:17], v[8:9], v[64:65]\nv_pk_fma_f32 v[20:21], v[20:21], v[12:13], v[68:69]\nv_exp_f32 v45, v45 clamp\nv_pk_fma_f32 v[22:23], v[22:23], v[14:15], v[70:71]\n"
      "v_pk_mul_f32 v[212:213], v[212:213], v[18:19]\nv_pk_mul_f32 v[216:217], v[216:217], v[22:23]\nv_exp_f32 v46, v46 clamp\nv_pk_fma_f32 v[212:213], v[214:215], v[16:17], v[212:213]\n"
      "v_pk_fma_f32 v[216:217], v[218:219], v[20:21], v[216:217]\nv_pk_mul_f32 v[16:17], v[16:17], v[18:19]\nv_exp_f32 v47, v47 clamp\nv_pk_mul_f32 v[20:21], v[20:21], v[22:23]\n"
      "v_pk_mul_f32 v[18:19], v[16:17], v[20:21]\nv_rcp_f32 v18, v18\nv_pk_mul_f32 v[212:213], v[212:213], v[20:21]\nv_rcp_f32 v19, v19\nv_pk_fma_f32 v[212:213], v[216:217], v[16:17], v[212:213]\n"
      "v_pk_fma_f32 v[32:33], v[212:213], v[18:19], v[32:33]\nv_add_f32 v39, v32, v33\nv_pk_mul_f32 v[52:53], v[228:229], v[228:229]\nv_pk_mul_f32 v[54:55], v[230:231], v[230:231]\n"
      "v_exp_f32 v48, v48 clamp\nv_pk_mul_f32 v[56:57], v[232:233], v[232:233]\nv_pk_mul_f32 v[58:59], v[234:235], v[234:235]\nv_exp_f32 v49, v49 clamp\n"
      "v_pk_fma_f32 v[52:53], v[52:53], s[58:59], s[58:59] op_sel:[0,0,1] op_sel_hi:[1,0,1]\nv_pk_fma_f32 v[54:55], v[54:55], s[58:59], s[58:59] op_sel:[0,0,1] op_sel_hi:[1,0,1]\n"
      "v_pk_fma_f32 v[56:57], v[56:57], s[58:59], s[58:59] op_sel:[0,0,1] op_sel_hi:[1,0,1]\nv_exp_f32 v50, v50 clamp\n"
      "v_pk_fma_f32 v[58:59], v[58:59], s[58:59], s[58:59] op_sel:[0,0,1] op_sel_hi:[1,0,1]\nv_pk_fma_f32 v[52:53], v[52:53], v[228:229], s[60:61] op_sel_hi:[1,1,0]\n"
      "v_pk_fma_f32 v[54:55], v[54:55], v[230:231], s[60:61] op_sel_hi:[1,1,0]\nv_exp_f32 v51, v51 clamp\nv_pk_fma_f32 v[56:57], v[56:57], v[232:233], s[60:61] op_sel_hi:[1,1,0]\n"
      "v_pk_fma_f32 v[58:59], v[58:59], v[234:235], s[60:61] op_sel_hi:[1,1,0]\nv_exp_f32 v52, v52 clamp\nv_pk_fma_f32 v[46:47], v[46:47], v[2:3], v[26:27]\nv_exp_f32 v53, v53 clamp\n"
      "v_pk_fma_f32 v[44:45], v[44:45], v[0:1], v[24:25]\nv_pk_fma_f32 v[48:49], v[48:49], v[4:5], v[28:29]\nv_exp_f32 v54, v54 clamp\nv_pk_fma_f32 v[50:51], v[50:51], v[6:7], v[30:31]\n"
      "v_pk_mul_f32 v[220:221], v[220:221], v[46:47]\nv_exp_f32 v55, v55 clamp\nv_pk_mul_f32 v[224:225], v[224:225], v[50:51]\nv_pk_fma_f32 v[220:221], v[222:223], v[44:45], v[220:221]\n"
      "v_exp_f32 v56, v56 clamp\nv_pk_fma_f32 v[224:225], v[226:227], v[48:49], v[224:225]\nv_exp_f32 v57, v57 clamp\nv_pk_mul_f32 v[44:45], v[44:45], v[46:47]\n"
      "v_pk_mul_f32 v[48:49], v[48:49], v[50:51]\nv_exp_f32 v58, v58 clamp\nv_pk_mul_f32 v[46:47], v[44:45], v[48:49]\nv_rcp_f32 v46, v46\nv_pk_mul_f32 v[220:221], v[220:221], v[48:49]\n"
      "v_exp_f32 v59, v59 clamp\nv_rcp_f32 v47, v47\nv_pk_fma_f32 v[220:221], v[224:225], v[44:45], v[220:221]\nv_pk_mul_f32 v[32:33], v[220:221], v[46:47]\n"
      "v_pk_fma_f32 v[54:55], v[54:55], v[10:11], v[66:67]\nv_pk_fma_f32 v[52:53], v[52:53], v[8:9], v[64:65]\nv_pk_fma_f32 v[56:57], v[56:57], v[12:13], v[68:69]\n"
      "v_pk_fma_f32 v[58:59], v[58:59], v[14:15], v[70:71]\nv_pk_mul_f32 v[228:229], v[228:229], v[54:55]\nv_pk_mul_f32 v[232:233], v[232:233], v[58:59]\n"
      "v_pk_fma_f32 v[228:229], v[230:231], v[52:53], v[228:229]\nv_pk_fma_f32 v[232:233], v[234:235], v[56:57], v[232:233]\nv_pk_mul_f32 v[52:53], v[52:53], v[54:55]\n"
      "v_pk_mul_f32 v[56:57], v[56:57], v[58:59]\nv_pk_mul_f32 v[54:55], v[52:53], v[56:57]\nv_rcp_f32 v54, v54\nv_pk_mul_f32 v[228:229], v[228:229], v[56:57]\nv_rcp_f32 v55, v55\n"
      "v_pk_fma_f32 v[228:229], v[232:233], v[52:53], v[228:229]\nv_pk_fma_f32 v[32:33], v[228:229], v[54:55], v[32:33]\nv_add_f32 v40, v32, v33\ns_barrier\ns_branch .Lge\n"
      ".Lg12:\ns_waitcnt vmcnt(4)\ns_barrier\nds_read_b64_tr_b16 v[64:65], v104 offset:45056\nds_read_b64_tr_b16 v[66:67], v104 offset:46080\nds_read_b64_tr_b16 v[68:69], v104 offset:53248\n"
      "ds_read_b64_tr_b16 v[70:71], v104 offset:54272\nds_read_b64_tr_b16 v[72:73], v105 offset:45056\nds_read_b64_tr_b16 v[74:75], v105 offset:46080\n"
      "ds_read_b64_tr_b16 v[76:77], v105 offset:53248\nds_read_b64_tr_b16 v[78:79], v105 offset:54272\nds_read_b64_tr_b16 v[80:81], v106 offset:45056\n"
      "ds_read_b64_tr_b16 v[82:83], v106 offset:46080\nds_read_b64_tr_b16 v[84:85], v106 offset:53248\nds_read_b64_tr_b16 v[86:87], v106 offset:54272\n"
      "ds_read_b64_tr_b16 v[88:89], v107 offset:45056\nds_read_b64_tr_b16 v[90:91], v107 offset:46080\nds_read_b64_tr_b16 v[92:93], v107 offset:53248\n"
      "ds_read_b64_tr_b16 v[94:95], v107 offset:54272\nds_read_b128 v[8:11], v120 offset:45056\nds_read_b128 v[12:15], v121 offset:45056\nds_read_b128 v[16:19], v96 offset:45056\n"
      "ds_read_b128 v[20:23], v97 offset:45056\ns_waitcnt lgkmcnt(2)\n.p2align 3\n"
      ".Lgl12:\nv_mfma_f32_16x16x32_bf16 v[152:155], v[88:91], v[8:11], v[152:155]\nds_read_b128 v[24:27], v96 offset:47104\nv_mfma_f32_16x16x32_bf16 v[152:155], v[92:95], v[12:15], v[152:155]\n"
      "ds_read_b128 v[28:31], v97 offset:47104\nv_mfma_f32_16x16x32_bf16 v[148:151], v[84:87], v[12:15], v[148:151]\nds_read_b128 v[0:3], v96 offset:49152\n"
      "v_mfma_f32_16x16x32_bf16 v[148:151], v[80:83], v[8:11], v[148:151]\nds_read_b128 v[4:7], v97 offset:49152\nv_mfma_f32_16x16x32_bf16 v[144:147], v[72:75], v[8:11], v[144:147]\n"
      "v_mfma_f32_16x16x32_bf16 v[144:147], v[76:79], v[12:15], v[144:147]\nv_mfma_f32_16x16x32_bf16 v[140:143], v[68:71], v[12:15], v[140:143]\ns_waitcnt lgkmcnt(4)\n"
      "v_mfma_f32_16x16x32_bf16 v[140:143], v[64:67], v[8:11], v[140:143]\nv_mfma_f32_16x16x32_bf16 v[156:159], v[64:67], v[16:19], v[156:159]\nds_read_b128 v[8:11], v96 offset:51200\n"
      "v_mfma_f32_16x16x32_bf16 v[156:159], v[68:71], v[20:23], v[156:159]\nds_read_b128 v[12:15], v97 offset:51200\nv_mfma_f32_16x16x32_bf16 v[160:163], v[76:79], v[20:23], v[160:163]\n"
      "v_mfma_f32_16x16x32_bf16 v[160:163], v[72:75], v[16:19], v[160:163]\nv_mfma_f32_16x16x32_bf16 v[164:167], v[80:83], v[16:19], v[164:167]\n"
      "v_mfma_f32_16x16x32_bf16 v[164:167], v[84:87], v[20:23], v[164:167]\nv_mfma_f32_16x16x32_bf16 v[168:171], v[92:95], v[20:23], v[168:171]\ns_waitcnt lgkmcnt(2)\n"
      "v_mfma_f32_16x16x32_bf16 v[168:171], v[88:91], v[16:19], v[168:171]\nv_mfma_f32_16x16x32_bf16 v[184:187], v[88:91], v[24:27], v[184:187]\nds_read_b128 v[16:19], v96 offset:53248\n"
      "v_mfma_f32_16x16x32_bf16 v[184:187], v[92:95], v[28:31], v[184:187]\nds_read_b128 v[20:23], v97 offset:53248\nv_mfma_f32_16x16x32_bf16 v[180:183], v[84:87], v[28:31], v[180:183]\n"
      "v_mfma_f32_16x16x32_bf16 v[180:183], v[80:83], v[24:27], v[180:183]\nv_mfma_f32_16x16x32_bf16 v[176:179], v[72:75], v[24:27], v[176:179]\n"
      "v_mfma_f32_16x16x32_bf16 v[176:179], v[76:79], v[28:31], v[176:179]\nv_mfma_f32_16x16x32_bf16 v[172:175], v[68:71], v[28:31], v[172:175]\ns_waitcnt vmcnt(0) lgkmcnt(0)\ns_barrier\n"
      "v_mfma_f32_16x16x32_bf16 v[172:175], v[64:67], v[24:27], v[172:175]\nds_read_b64_tr_b16 v[32:33], v108 offset:0\nv_mfma_f32_16x16x32_bf16 v[188:191], v[64:67], v[0:3], v[188:191]\n"
      "ds_read_b64_tr_b16 v[34:35], v108 offset:1024\nv_mfma_f32_16x16x32_bf16 v[188:191], v[68:71], v[4:7], v[188:191]\nds_read_b64_tr_b16 v[36:37], v108 offset:8192\n"
      "v_mfma_f32_16x16x32_bf16 v[192:195], v[76:79], v[4:7], v[192:195]\nds_read_b64_tr_b16 v[38:39], v108 offset:9216\nv_mfma_f32_16x16x32_bf16 v[192:195], v[72:75], v[0:3], v[192:195]\n"
      "ds_read_b64_tr_b16 v[40:41], v109 offset:0\nv_mfma_f32_16x16x32_bf16 v[196:199], v[80:83], v[0:3], v[196:199]\nds_read_b64_tr_b16 v[42:43], v109 offset:1024\n"
      "v_mfma_f32_16x16x32_bf16 v[196:199], v[84:87], v[4:7], v[196:199]\nds_read_b64_tr_b16 v[44:45], v109 offset:8192\nv_mfma_f32_16x16x32_bf16 v[200:203], v[92:95], v[4:7], v[200:203]\n"
      "ds_read_b64_tr_b16 v[46:47], v109 offset:9216\nv_mfma_f32_16x16x32_bf16 v[200:203], v[88:91], v[0:3], v[200:203]\nds_read_b64_tr_b16 v[48:49], v110 offset:0\n"
      "v_mfma_f32_16x16x32_bf16 v[216:219], v[88:91], v[8:11], v[216:219]\nv_mfma_f32_16x16x32_bf16 v[216:219], v[92:95], v[12:15], v[216:219]\n"
      "v_mfma_f32_16x16x32_bf16 v[212:215], v[84:87], v[12:15], v[212:215]\nds_read_b64_tr_b16 v[50:51], v110 offset:1024\nv_mfma_f32_16x16x32_bf16 v[212:215], v[80:83], v[8:11], v[212:215]\n"
      "ds_read_b64_tr_b16 v[52:53], v110 offset:8192\nv_mfma_f32_16x16x32_bf16 v[208:211], v[72:75], v[8:11], v[208:211]\nds_read_b64_tr_b16 v[54:55], v110 offset:9216\n"
      "v_mfma_f32_16x16x32_bf16 v[208:211], v[76:79], v[12:15], v[208:211]\nds_read_b64_tr_b16 v[56:57], v111 offset:0\nv_mfma_f32_16x16x32_bf16 v[204:207], v[68:71], v[12:15], v[204:207]\n"
      "ds_read_b64_tr_b16 v[58:59], v111 offset:1024\nv_mfma_f32_16x16x32_bf16 v[204:207], v[64:67], v[8:11], v[204:207]\nds_read_b64_tr_b16 v[60:61], v111 offset:8192\n"
      "v_mfma_f32_16x16x32_bf16 v[220:223], v[64:67], v[16:19], v[220:223]\nds_read_b128 v[8:11], v122 offset:0\nv_mfma_f32_16x16x32_bf16 v[220:223], v[68:71], v[20:23], v[220:223]\n"
      "ds_read_b128 v[12:15], v123 offset:0\nv_mfma_f32_16x16x32_bf16 v[224:227], v[76:79], v[20:23], v[224:227]\nds_read_b64_tr_b16 v[62:63], v111 offset:9216\n"
      "v_mfma_f32_16x16x32_bf16 v[224:227], v[72:75], v[16:19], v[224:227]\nv_mfma_f32_16x16x32_bf16 v[228:231], v[80:83], v[16:19], v[228:231]\n"
      "v_mfma_f32_16x16x32_bf16 v[228:231], v[84:87], v[20:23], v[228:231]\nv_mfma_f32_16x16x32_bf16 v[232:235], v[92:95], v[20:23], v[232:235]\ns_waitcnt lgkmcnt(0)\n"
      "v_mfma_f32_16x16x32_bf16 v[232:235], v[88:91], v[16:19], v[232:235]\nds_read_b128 v[16:19], v98 offset:0\nds_read_b128 v[20:23], v99 offset:0\n"
      "v_mfma_f32_16x16x32_bf16 v[152:155], v[56:59], v[8:11], v[152:155]\nds_read_b128 v[24:27], v98 offset:2048\nv_mfma_f32_16x16x32_bf16 v[152:155], v[60:63], v[12:15], v[152:155]\n"
      "ds_read_b128 v[28:31], v99 offset:2048\nv_mfma_f32_16x16x32_bf16 v[148:151], v[52:55], v[12:15], v[148:151]\nds_read_b128 v[0:3], v98 offset:4096\n"
      "v_mfma_f32_16x16x32_bf16 v[148:151], v[48:51], v[8:11], v[148:151]\nds_read_b128 v[4:7], v99 offset:4096\nv_mfma_f32_16x16x32_bf16 v[144:147], v[40:43], v[8:11], v[144:147]\n"
      "v_mfma_f32_16x16x32_bf16 v[144:147], v[44:47], v[12:15], v[144:147]\nv_mfma_f32_16x16x32_bf16 v[140:143], v[36:39], v[12:15], v[140:143]\ns_waitcnt lgkmcnt(4)\n"
      "v_mfma_f32_16x16x32_bf16 v[140:143], v[32:35], v[8:11], v[140:143]\nv_mfma_f32_16x16x32_bf16 v[156:159], v[32:35], v[16:19], v[156:159]\nds_read_b128 v[8:11], v98 offset:6144\n"
      "v_mfma_f32_16x16x32_bf16 v[156:159], v[36:39], v[20:23], v[156:159]\nds_read_b128 v[12:15], v99 offset:6144\nv_mfma_f32_16x16x32_bf16 v[160:163], v[44:47], v[20:23], v[160:163]\n"
      "v_mfma_f32_16x16x32_bf16 v[160:163], v[40:43], v[16:19], v[160:163]\nv_mfma_f32_16x16x32_bf16 v[164:167], v[48:51], v[16:19], v[164:167]\n"
      "v_mfma_f32_16x16x32_bf16 v[164:167], v[52:55], v[20:23], v[164:167]\nv_mfma_f32_16x16x32_bf16 v[168:171], v[60:63], v[20:23], v[168:171]\ns_waitcnt lgkmcnt(2)\n"
      "v_mfma_f32_16x16x32_bf16 v[168:171], v[56:59], v[16:19], v[168:171]\nv_mfma_f32_16x16x32_bf16 v[184:187], v[56:59], v[24:27], v[184:187]\nds_read_b128 v[16:19], v98 offset:8192\n"
      "v_mfma_f32_16x16x32_bf16 v[184:187], v[60:63], v[28:31], v[184:187]\nds_read_b128 v[20:23], v99 offset:8192\nv_mfma_f32_16x16x32_bf16 v[180:183], v[52:55], v[28:31], v[180:183]\n"
      "v_mfma_f32_16x16x32_bf16 v[180:183], v[48:51], v[24:27], v[180:183]\nv_mfma_f32_16x16x32_bf16 v[176:179], v[40:43], v[24:27], v[176:179]\n"
      "v_mfma_f32_16x16x32_bf16 v[176:179], v[44:47], v[28:31], v[176:179]\nv_mfma_f32_16x16x32_bf16 v[172:175], v[36:39], v[28:31], v[172:175]\ns_waitcnt vmcnt(0) lgkmcnt(0)\ns_barrier\n"
      "v_mfma_f32_16x16x32_bf16 v[172:175], v[32:35], v[24:27], v[172:175]\nds_read_b64_tr_b16 v[64:65], v104 offset:0\nv_mfma_f32_16x16x32_bf16 v[188:191], v[32:35], v[0:3], v[188:191]\n"
      "ds_read_b64_tr_b16 v[66:67], v104 offset:1024\nv_mfma_f32_16x16x32_bf16 v[188:191], v[36:39], v[4:7], v[188:191]\nds_read_b64_tr_b16 v[68:69], v104 offset:8192\n"
      "v_mfma_f32_16x16x32_bf16 v[192:195], v[44:47], v[4:7], v[192:195]\nds_read_b64_tr_b16 v[70:71], v104 offset:9216\nv_mfma_f32_16x16x32_bf16 v[192:195], v[40:43], v[0:3], v[192:195]\n"
      "ds_read_b64_tr_b16 v[72:73], v105 offset:0\nv_mfma_f32_16x16x32_bf16 v[196:199], v[48:51], v[0:3], v[196:199]\nds_read_b64_tr_b16 v[74:75], v105 offset:1024\n"
      "v_mfma_f32_16x16x32_bf16 v[196:199], v[52:55], v[4:7], v[196:199]\nds_read_b64_tr_b16 v[76:77], v105 offset:8192\nv_mfma_f32_16x16x32_bf16 v[200:203], v[60:63], v[4:7], v[200:203]\n"
      "ds_read_b64_tr_b16 v[78:79], v105 offset:9216\nv_mfma_f32_16x16x32_bf16 v[200:203], v[56:59], v[0:3], v[200:203]\nds_read_b64_tr_b16 v[80:81], v106 offset:0\n"
      "v_mfma_f32_16x16x32_bf16 v[216:219], v[56:59], v[8:11], v[216:219]\nv_mfma_f32_16x16x32_bf16 v[216:219], v[60:63], v[12:15], v[216:219]\n"
      "v_mfma_f32_16x16x32_bf16 v[212:215], v[52:55], v[12:15], v[212:215]\nds_read_b64_tr_b16 v[82:83], v106 offset:1024\nv_mfma_f32_16x16x32_bf16 v[212:215], v[48:51], v[8:11], v[212:215]\n"
      "ds_read_b64_tr_b16 v[84:85], v106 offset:8192\nv_mfma_f32_16x16x32_bf16 v[208:211], v[40:43], v[8:11], v[208:211]\nds_read_b64_tr_b16 v[86:87], v106 offset:9216\n"
      "v_mfma_f32_16x16x32_bf16 v[208:211], v[44:47], v[12:15], v[208:211]\nds_read_b64_tr_b16 v[88:89], v107 offset:0\nv_mfma_f32_16x16x32_bf16 v[204:207], v[36:39], v[12:15], v[204:207]\n"
      "ds_read_b64_tr_b16 v[90:91], v107 offset:1024\nv_mfma_f32_16x16x32_bf16 v[204:207], v[32:35], v[8:11], v[204:207]\nds_read_b64_tr_b16 v[92:93], v107 offset:8192\n"
      "v_mfma_f32_16x16x32_bf16 v[220:223], v[32:35], v[16:19], v[220:223]\nds_read_b128 v[8:11], v120 offset:0\nv_mfma_f32_16x16x32_bf16 v[220:223], v[36:39], v[20:23], v[220:223]\n"
      "ds_read_b128 v[12:15], v121 offset:0\nv_mfma_f32_16x16x32_bf16 v[224:227], v[44:47], v[20:23], v[224:227]\nds_read_b64_tr_b16 v[94:95], v107 offset:9216\n"
      "v_mfma_f32_16x16x32_bf16 v[224:227], v[40:43], v[16:19], v[224:227]\nv_mfma_f32_16x16x32_bf16 v[228:231], v[48:51], v[16:19], v[228:231]\n"
      "v_mfma_f32_16x16x32_bf16 v[228:231], v[52:55], v[20:23], v[228:231]\nv_mfma_f32_16x16x32_bf16 v[232:235], v[60:63], v[20:23], v[232:235]\ns_waitcnt lgkmcnt(0)\n"
      "v_mfma_f32_16x16x32_bf16 v[232:235], v[56:59], v[16:19], v[232:235]\nds_read_b128 v[16:19], v96 offset:0\nds_read_b128 v[20:23], v97 offset:0\n"
      "v_mfma_f32_16x16x32_bf16 v[152:155], v[88:91], v[8:11], v[152:155]\nds_read_b128 v[24:27], v96 offset:2048\nv_mfma_f32_16x16x32_bf16 v[152:155], v[92:95], v[12:15], v[152:155]\n"
      "ds_read_b128 v[28:31], v97 offset:2048\nv_mfma_f32_16x16x32_bf16 v[148:151], v[84:87], v[12:15], v[148:151]\nds_read_b128 v[0:3], v96 offset:4096\n"
      "v_mfma_f32_16x16x32_bf16 v[148:151], v[80:83], v[8:11], v[148:151]\nds_read_b128 v[4:7], v97 offset:4096\nv_mfma_f32_16x16x32_bf16 v[144:147], v[72:75], v[8:11], v[144:147]\n"
      "v_mfma_f32_16x16x32_bf16 v[144:147], v[76:79], v[12:15], v[144:147]\nv_mfma_f32_16x16x32_bf16 v[140:143], v[68:71], v[12:15], v[140:143]\ns_waitcnt lgkmcnt(4)\n"
      "v_mfma_f32_16x16x32_bf16 v[140:143], v[64:67], v[8:11], v[140:143]\nv_mfma_f32_16x16x32_bf16 v[156:159], v[64:67], v[16:19], v[156:159]\nds_read_b128 v[8:11], v96 offset:6144\n"
      "v_mfma_f32_16x16x32_bf16 v[156:159], v[68:71], v[20:23], v[156:159]\nds_read_b128 v[12:15], v97 offset:6144\nv_mfma_f32_16x16x32_bf16 v[160:163], v[76:79], v[20:23], v[160:163]\n"
      "v_mfma_f32_16x16x32_bf16 v[160:163], v[72:75], v[16:19], v[160:163]\nv_mfma_f32_16x16x32_bf16 v[164:167], v[80:83], v[16:19], v[164:167]\n"
      "v_mfma_f32_16x16x32_bf16 v[164:167], v[84:87], v[20:23], v[164:167]\nv_mfma_f32_16x16x32_bf16 v[168:171], v[92:95], v[20:23], v[168:171]\ns_waitcnt lgkmcnt(2)\n"
      "v_mfma_f32_16x16x32_bf16 v[168:171], v[88:91], v[16:19], v[168:171]\nv_mfma_f32_16x16x32_bf16 v[184:187], v[88:91], v[24:27], v[184:187]\nds_read_b128 v[16:19], v96 offset:8192\n"
      "v_mfma_f32_16x16x32_bf16 v[184:187], v[92:95], v[28:31], v[184:187]\nds_read_b128 v[20:23], v97 offset:8192\nv_mfma_f32_16x16x32_bf16 v[180:183], v[84:87], v[28:31], v[180:183]\n"
      "v_mfma_f32_16x16x32_bf16 v[180:183], v[80:83], v[24:27], v[180:183]\nv_mfma_f32_16x16x32_bf16 v[176:179], v[72:75], v[24:27], v[176:179]\n"
      "v_mfma_f32_16x16x32_bf16 v[176:179], v[76:79], v[28:31], v[176:179]\nv_mfma_f32_16x16x32_bf16 v[172:175], v[68:71], v[28:31], v[172:175]\ns_waitcnt vmcnt(0) lgkmcnt(0)\ns_barrier\n"
      "v_mfma_f32_16x16x32_bf16 v[172:175], v[64:67], v[24:27], v[172:175]\nds_read_b64_tr_b16 v[32:33], v104 offset:45056\nv_mfma_f32_16x16x32_bf16 v[188:191], v[64:67], v[0:3], v[188:191]\n"
      "ds_read_b64_tr_b16 v[34:35], v104 offset:46080\nv_mfma_f32_16x16x32_bf16 v[188:191], v[68:71], v[4:7], v[188:191]\nds_read_b64_tr_b16 v[36:37], v104 offset:53248\n"
      "v_mfma_f32_16x16x32_bf16 v[192:195], v[76:79], v[4:7], v[192:195]\nds_read_b64_tr_b16 v[38:39], v104 offset:54272\nv_mfma_f32_16x16x32_bf16 v[192:195], v[72:75], v[0:3], v[192:195]\n"
      "ds_read_b64_tr_b16 v[40:41], v105 offset:45056\nv_mfma_f32_16x16x32_bf16 v[196:199], v[80:83], v[0:3], v[196:199]\nds_read_b64_tr_b16 v[42:43], v105 offset:46080\n"
      "v_mfma_f32_16x16x32_bf16 v[196:199], v[84:87], v[4:7], v[196:199]\nds_read_b64_tr_b16 v[44:45], v105 offset:53248\nv_mfma_f32_16x16x32_bf16 v[200:203], v[92:95], v[4:7], v[200:203]\n"
      "ds_read_b64_tr_b16 v[46:47], v105 offset:54272\nv_mfma_f32_16x16x32_bf16 v[200:203], v[88:91], v[0:3], v[200:203]\nds_read_b64_tr_b16 v[48:49], v106 offset:45056\n"
      "v_mfma_f32_16x16x32_bf16 v[216:219], v[88:91], v[8:11], v[216:219]\nv_mfma_f32_16x16x32_bf16 v[216:219], v[92:95], v[12:15], v[216:219]\n"
      "v_mfma_f32_16x16x32_bf16 v[212:215], v[84:87], v[12:15], v[212:215]\nds_read_b64_tr_b16 v[50:51], v106 offset:46080\nv_mfma_f32_16x16x32_bf16 v[212:215], v[80:83], v[8:11], v[212:215]\n"
      "ds_read_b64_tr_b16 v[52:53], v106 offset:53248\nv_mfma_f32_16x16x32_bf16 v[208:211], v[72:75], v[8:11], v[208:211]\nds_read_b64_tr_b16 v[54:55], v106 offset:54272\n"
      "v_mfma_f32_16x16x32_bf16 v[208:211], v[76:79], v[12:15], v[208:211]\nds_read_b64_tr_b16 v[56:57], v107 offset:45056\nv_mfma_f32_16x16x32_bf16 v[204:207], v[68:71], v[12:15], v[204:207]\n"
      "ds_read_b64_tr_b16 v[58:59], v107 offset:46080\nv_mfma_f32_16x16x32_bf16 v[204:207], v[64:67], v[8:11], v[204:207]\nds_read_b64_tr_b16 v[60:61], v107 offset:53248\n"
      "v_mfma_f32_16x16x32_bf16 v[220:223], v[64:67], v[16:19], v[220:223]\nds_read_b128 v[8:11], v120 offset:45056\nv_mfma_f32_16x16x32_bf16 v[220:223], v[68:71], v[20:23], v[220:223]\n"
      "ds_read_b128 v[12:15], v121 offset:45056\nv_mfma_f32_16x16x32_bf16 v[224:227], v[76:79], v[20:23], v[224:227]\nds_read_b64_tr_b16 v[62:63], v107 offset:54272\n"
      "v_mfma_f32_16x16x32_bf16 v[224:227], v[72:75], v[16:19], v[224:227]\nv_mfma_f32_16x16x32_bf16 v[228:231], v[80:83], v[16:19], v[228:231]\n"
      "v_mfma_f32_16x16x32_bf16 v[228:231], v[84:87], v[20:23], v[228:231]\nv_mfma_f32_16x16x32_bf16 v[232:235], v[92:95], v[20:23], v[232:235]\ns_waitcnt lgkmcnt(0)\n"
      "v_mfma_f32_16x16x32_bf16 v[232:235], v[88:91], v[16:19], v[232:235]\nds_read_b128 v[16:19], v96 offset:45056\nds_read_b128 v[20:23], v97 offset:45056\n"
      "v_mfma_f32_16x16x32_bf16 v[152:155], v[56:59], v[8:11], v[152:155]\nds_read_b128 v[24:27], v96 offset:47104\nv_mfma_f32_16x16x32_bf16 v[152:155], v[60:63], v[12:15], v[152:155]\n"
      "ds_read_b128 v[28:31], v97 offset:47104\nv_mfma_f32_16x16x32_bf16 v[148:151], v[52:55], v[12:15], v[148:151]\nds_read_b128 v[0:3], v96 offset:49152\n"
      "v_mfma_f32_16x16x32_bf16 v[148:151], v[48:51], v[8:11], v[148:151]\nds_read_b128 v[4:7], v97 offset:49152\nv_mfma_f32_16x16x32_bf16 v[144:147], v[40:43], v[8:11], v[144:147]\n"
      "v_mfma_f32_16x16x32_bf16 v[144:147], v[44:47], v[12:15], v[144:147]\nv_mfma_f32_16x16x32_bf16 v[140:143], v[36:39], v[12:15], v[140:143]\ns_waitcnt lgkmcnt(4)\n"
      "v_mfma_f32_16x16x32_bf16 v[140:143], v[32:35], v[8:11], v[140:143]\nv_mfma_f32_16x16x32_bf16 v[156:159], v[32:35], v[16:19], v[156:159]\nds_read_b128 v[8:11], v96 offset:51200\n"
      "v_mfma_f32_16x16x32_bf16 v[156:159], v[36:39], v[20:23], v[156:159]\nds_read_b128 v[12:15], v97 offset:51200\nv_mfma_f32_16x16x32_bf16 v[160:163], v[44:47], v[20:23], v[160:163]\n"
      "v_mfma_f32_16x16x32_bf16 v[160:163], v[40:43], v[16:19], v[160:163]\nv_mfma_f32_16x16x32_bf16 v[164:167], v[48:51], v[16:19], v[164:167]\n"
      "v_mfma_f32_16x16x32_bf16 v[164:167], v[52:55], v[20:23], v[164:167]\nv_mfma_f32_16x16x32_bf16 v[168:171], v[60:63], v[20:23], v[168:171]\ns_waitcnt lgkmcnt(2)\n"
      "v_mfma_f32_16x16x32_bf16 v[168:171], v[56:59], v[16:19], v[168:171]\nv_mfma_f32_16x16x32_bf16 v[184:187], v[56:59], v[24:27], v[184:187]\nds_read_b128 v[16:19], v96 offset:53248\n"
      "v_mfma_f32_16x16x32_bf16 v[184:187], v[60:63], v[28:31], v[184:187]\nds_read_b128 v[20:23], v97 offset:53248\nv_mfma_f32_16x16x32_bf16 v[180:183], v[52:55], v[28:31], v[180:183]\n"
      "v_mfma_f32_16x16x32_bf16 v[180:183], v[48:51], v[24:27], v[180:183]\nv_mfma_f32_16x16x32_bf16 v[176:179], v[40:43], v[24:27], v[176:179]\n"
      "v_mfma_f32_16x16x32_bf16 v[176:179], v[44:47], v[28:31], v[176:179]\nv_mfma_f32_16x16x32_bf16 v[172:175], v[36:39], v[28:31], v[172:175]\ns_waitcnt vmcnt(0) lgkmcnt(0)\ns_barrier\n"
      "v_mfma_f32_16x16x32_bf16 v[172:175], v[32:35], v[24:27], v[172:175]\nds_read_b64_tr_b16 v[64:65], v108 offset:0\nv_mfma_f32_16x16x32_bf16 v[188:191], v[32:35], v[0:3], v[188:191]\n"
      "ds_read_b64_tr_b16 v[66:67], v108 offset:1024\nv_mfma_f32_16x16x32_bf16 v[188:191], v[36:39], v[4:7], v[188:191]\nds_read_b64_tr_b16 v[68:69], v108 offset:8192\n"
      "v_mfma_f32_16x16x32_bf16 v[192:195], v[44:47], v[4:7], v[192:195]\nds_read_b64_tr_b16 v[70:71], v108 offset:9216\nv_mfma_f32_16x16x32_bf16 v[192:195], v[40:43], v[0:3], v[192:195]\n"
      "ds_read_b64_tr_b16 v[72:73], v109 offset:0\nv_mfma_f32_16x16x32_bf16 v[196:199], v[48:51], v[0:3], v[196:199]\nds_read_b64_tr_b16 v[74:75], v109 offset:1024\n"
      "v_mfma_f32_16x16x32_bf16 v[196:199], v[52:55], v[4:7], v[196:199]\nds_read_b64_tr_b16 v[76:77], v109 offset:8192\nv_mfma_f32_16x16x32_bf16 v[200:203], v[60:63], v[4:7], v[200:203]\n"
      "ds_read_b64_tr_b16 v[78:79], v109 offset:9216\nv_mfma_f32_16x16x32_bf16 v[200:203], v[56:59], v[0:3], v[200:203]\nds_read_b64_tr_b16 v[80:81], v110 offset:0\n"
      "v_mfma_f32_16x16x32_bf16 v[216:219], v[56:59], v[8:11], v[216:219]\nv_mfma_f32_16x16x32_bf16 v[216:219], v[60:63], v[12:15], v[216:219]\n"
      "v_mfma_f32_16x16x32_bf16 v[212:215], v[52:55], v[12:15], v[212:215]\nds_read_b64_tr_b16 v[82:83], v110 offset:1024\nv_mfma_f32_16x16x32_bf16 v[212:215], v[48:51], v[8:11], v[212:215]\n"
      "ds_read_b64_tr_b16 v[84:85], v110 offset:8192\nv_mfma_f32_16x16x32_bf16 v[208:211], v[40:43], v[8:11], v[208:211]\nds_read_b64_tr_b16 v[86:87], v110 offset:9216\n"
      "v_mfma_f32_16x16x32_bf16 v[208:211], v[44:47], v[12:15], v[208:211]\nds_read_b64_tr_b16 v[88:89], v111 offset:0\nv_mfma_f32_16x16x32_bf16 v[204:207], v[36:39], v[12:15], v[204:207]\n"
      "ds_read_b64_tr_b16 v[90:91], v111 offset:1024\nv_mfma_f32_16x16x32_bf16 v[204:207], v[32:35], v[8:11], v[204:207]\nds_read_b64_tr_b16 v[92:93], v111 offset:8192\n"
      "v_mfma_f32_16x16x32_bf16 v[220:223], v[32:35], v[16:19], v[220:223]\nds_read_b128 v[8:11], v122 offset:0\nv_mfma_f32_16x16x32_bf16 v[220:223], v[36:39], v[20:23], v[220:223]\n"
      "ds_read_b128 v[12:15], v123 offset:0\nv_mfma_f32_16x16x32_bf16 v[224:227], v[44:47], v[20:23], v[224:227]\nds_read_b64_tr_b16 v[94:95], v111 offset:9216\n"
      "v_mfma_f32_16x16x32_bf16 v[224:227], v[40:43], v[16:19], v[224:227]\nv_mfma_f32_16x16x32_bf16 v[228:231], v[48:51], v[16:19], v[228:231]\n"
      "v_mfma_f32_16x16x32_bf16 v[228:231], v[52:55], v[20:23], v[228:231]\nv_mfma_f32_16x16x32_bf16 v[232:235], v[60:63], v[20:23], v[232:235]\ns_waitcnt lgkmcnt(0)\n"
      "v_mfma_f32_16x16x32_bf16 v[232:235], v[56:59], v[16:19], v[232:235]\nds_read_b128 v[16:19], v98 offset:0\nds_read_b128 v[20:23], v99 offset:0\n"
      "v_mfma_f32_16x16x32_bf16 v[152:155], v[88:91], v[8:11], v[152:155]\nds_read_b128 v[24:27], v98 offset:2048\nv_mfma_f32_16x16x32_bf16 v[152:155], v[92:95], v[12:15], v[152:155]\n"
      "ds_read_b128 v[28:31], v99 offset:2048\nv_mfma_f32_16x16x32_bf16 v[148:151], v[84:87], v[12:15], v[148:151]\nds_read_b128 v[0:3], v98 offset:4096\n"
      "v_mfma_f32_16x16x32_bf16 v[148:151], v[80:83], v[8:11], v[148:151]\nds_read_b128 v[4:7], v99 offset:4096\nv_mfma_f32_16x16x32_bf16 v[144:147], v[72:75], v[8:11], v[144:147]\n"
      "v_mfma_f32_16x16x32_bf16 v[144:147], v[76:79], v[12:15], v[144:147]\nv_mfma_f32_16x16x32_bf16 v[140:143], v[68:71], v[12:15], v[140:143]\ns_waitcnt lgkmcnt(4)\n"
      "v_mfma_f32_16x16x32_bf16 v[140:143], v[64:67], v[8:11], v[140:143]\nv_mfma_f32_16x16x32_bf16 v[156:159], v[64:67], v[16:19], v[156:159]\nds_read_b128 v[8:11], v98 offset:6144\n"
      "v_mfma_f32_16x16x32_bf16 v[156:159], v[68:71], v[20:23], v[156:159]\nds_read_b128 v[12:15], v99 offset:6144\nv_mfma_f32_16x16x32_bf16 v[160:163], v[76:79], v[20:23], v[160:163]\n"
      "v_mfma_f32_16x16x32_bf16 v[160:163], v[72:75], v[16:19], v[160:163]\nv_mfma_f32_16x16x32_bf16 v[164:167], v[80:83], v[16:19], v[164:167]\n"
      "v_mfma_f32_16x16x32_bf16 v[164:167], v[84:87], v[20:23], v[164:167]\nv_mfma_f32_16x16x32_bf16 v[168:171], v[92:95], v[20:23], v[168:171]\ns_waitcnt lgkmcnt(2)\n"
      "v_mfma_f32_16x16x32_bf16 v[168:171], v[88:91], v[16:19], v[168:171]\nv_mfma_f32_16x16x32_bf16 v[184:187], v[88:91], v[24:27], v[184:187]\nds_read_b128 v[16:19], v98 offset:8192\n"
      "v_mfma_f32_16x16x32_bf16 v[184:187], v[92:95], v[28:31], v[184:187]\nds_read_b128 v[20:23], v99 offset:8192\nv_mfma_f32_16x16x32_bf16 v[180:183], v[84:87], v[28:31], v[180:183]\n"
      "v_mfma_f32_16x16x32_bf16 v[180:183], v[80:83], v[24:27], v[180:183]\nv_mfma_f32_16x16x32_bf16 v[176:179], v[72:75], v[24:27], v[176:179]\n"
      "v_mfma_f32_16x16x32_bf16 v[176:179], v[76:79], v[28:31], v[176:179]\nv_mfma_f32_16x16x32_bf16 v[172:175], v[68:71], v[28:31], v[172:175]\ns_waitcnt vmcnt(0) lgkmcnt(0)\ns_barrier\n"
      "v_mfma_f32_16x16x32_bf16 v[172:175], v[64:67], v[24:27], v[172:175]\nds_read_b64_tr_b16 v[32:33], v104 offset:0\nv_mfma_f32_16x16x32_bf16 v[188:191], v[64:67], v[0:3], v[188:191]\n"
      "ds_read_b64_tr_b16 v[34:35], v104 offset:1024\nv_mfma_f32_16x16x32_bf16 v[188:191], v[68:71], v[4:7], v[188:191]\nds_read_b64_tr_b16 v[36:37], v104 offset:8192\n"
      "v_mfma_f32_16x16x32_bf16 v[192:195], v[76:79], v[4:7], v[192:195]\nds_read_b64_tr_b16 v[38:39], v104 offset:9216\nv_mfma_f32_16x16x32_bf16 v[192:195], v[72:75], v[0:3], v[192:195]\n"
      "ds_read_b64_tr_b16 v[40:41], v105 offset:0\nv_mfma_f32_16x16x32_bf16 v[196:199], v[80:83], v[0:3], v[196:199]\nds_read_b64_tr_b16 v[42:43], v105 offset:1024\n"
      "v_mfma_f32_16x16x32_bf16 v[196:199], v[84:87], v[4:7], v[196:199]\nds_read_b64_tr_b16 v[44:45], v105 offset:8192\nv_mfma_f32_16x16x32_bf16 v[200:203], v[92:95], v[4:7], v[200:203]\n"
      "ds_read_b64_tr_b16 v[46:47], v105 offset:9216\nv_mfma_f32_16x16x32_bf16 v[200:203], v[88:91], v[0:3], v[200:203]\nds_read_b64_tr_b16 v[48:49], v106 offset:0\n"
      "v_mfma_f32_16x16x32_bf16 v[216:219], v[88:91], v[8:11], v[216:219]\nv_mfma_f32_16x16x32_bf16 v[216:219], v[92:95], v[12:15], v[216:219]\n"
      "v_mfma_f32_16x16x32_bf16 v[212:215], v[84:87], v[12:15], v[212:215]\nds_read_b64_tr_b16 v[50:51], v106 offset:1024\nv_mfma_f32_16x16x32_bf16 v[212:215], v[80:83], v[8:11], v[212:215]\n"
      "ds_read_b64_tr_b16 v[52:53], v106 offset:8192\nv_mfma_f32_16x16x32_bf16 v[208:211], v[72:75], v[8:11], v[208:211]\nds_read_b64_tr_b16 v[54:55], v106 offset:9216\n"
      "v_mfma_f32_16x16x32_bf16 v[208:211], v[76:79], v[12:15], v[208:211]\nds_read_b64_tr_b16 v[56:57], v107 offset:0\nv_mfma_f32_16x16x32_bf16 v[204:207], v[68:71], v[12:15], v[204:207]\n"
      "ds_read_b64_tr_b16 v[58:59], v107 offset:1024\nv_mfma_f32_16x16x32_bf16 v[204:207], v[64:67], v[8:11], v[204:207]\nds_read_b64_tr_b16 v[60:61], v107 offset:8192\n"
      "v_mfma_f32_16x16x32_bf16 v[220:223], v[64:67], v[16:19], v[220:223]\nds_read_b128 v[8:11], v120 offset:0\nv_mfma_f32_16x16x32_bf16 v[220:223], v[68:71], v[20:23], v[220:223]\n"
      "ds_read_b128 v[12:15], v121 offset:0\nv_mfma_f32_16x16x32_bf16 v[224:227], v[76:79], v[20:23], v[224:227]\nds_read_b64_tr_b16 v[62:63], v107 offset:9216\n"
      "v_mfma_f32_16x16x32_bf16 v[224:227], v[72:75], v[16:19], v[224:227]\nv_mfma_f32_16x16x32_bf16 v[228:231], v[80:83], v[16:19], v[228:231]\n"
      "v_mfma_f32_16x16x32_bf16 v[228:231], v[84:87], v[20:23], v[228:231]\nv_mfma_f32_16x16x32_bf16 v[232:235], v[92:95], v[20:23], v[232:235]\ns_waitcnt lgkmcnt(0)\n"
      "v_mfma_f32_16x16x32_bf16 v[232:235], v[88:91], v[16:19], v[232:235]\nds_read_b128 v[16:19], v96 offset:0\nds_read_b128 v[20:23], v97 offset:0\n"
      "v_mfma_f32_16x16x32_bf16 v[152:155], v[56:59], v[8:11], v[152:155]\nds_read_b128 v[24:27], v96 offset:2048\nv_mfma_f32_16x16x32_bf16 v[152:155], v[60:63], v[12:15], v[152:155]\n"
      "ds_read_b128 v[28:31], v97 offset:2048\nv_mfma_f32_16x16x32_bf16 v[148:151], v[52:55], v[12:15], v[148:151]\nds_read_b128 v[0:3], v96 offset:4096\n"
      "v_mfma_f32_16x16x32_bf16 v[148:151], v[48:51], v[8:11], v[148:151]\nds_read_b128 v[4:7], v97 offset:4096\nv_mfma_f32_16x16x32_bf16 v[144:147], v[40:43], v[8:11], v[144:147]\n"
      "v_mfma_f32_16x16x32_bf16 v[144:147], v[44:47], v[12:15], v[144:147]\nv_mfma_f32_16x16x32_bf16 v[140:143], v[36:39], v[12:15], v[140:143]\ns_waitcnt lgkmcnt(4)\n"
      "v_mfma_f32_16x16x32_bf16 v[140:143], v[32:35], v[8:11], v[140:143]\nv_mfma_f32_16x16x32_bf16 v[156:159], v[32:35], v[16:19], v[156:159]\nds_read_b128 v[8:11], v96 offset:6144\n"
      "v_mfma_f32_16x16x32_bf16 v[156:159], v[36:39], v[20:23], v[156:159]\nds_read_b128 v[12:15], v97 offset:6144\nv_mfma_f32_16x16x32_bf16 v[160:163], v[44:47], v[20:23], v[160:163]\n"
      "v_mfma_f32_16x16x32_bf16 v[160:163], v[40:43], v[16:19], v[160:163]\nv_mfma_f32_16x16x32_bf16 v[164:167], v[48:51], v[16:19], v[164:167]\n"
      "v_mfma_f32_16x16x32_bf16 v[164:167], v[52:55], v[20:23], v[164:167]\nv_mfma_f32_16x16x32_bf16 v[168:171], v[60:63], v[20:23], v[168:171]\ns_waitcnt lgkmcnt(2)\n"
      "v_mfma_f32_16x16x32_bf16 v[168:171], v[56:59], v[16:19], v[168:171]\nv_mfma_f32_16x16x32_bf16 v[184:187], v[56:59], v[24:27], v[184:187]\nds_read_b128 v[16:19], v96 offset:8192\n"
      "v_mfma_f32_16x16x32_bf16 v[184:187], v[60:63], v[28:31], v[184:187]\nds_read_b128 v[20:23], v97 offset:8192\nv_mfma_f32_16x16x32_bf16 v[180:183], v[52:55], v[28:31], v[180:183]\n"
      "v_mfma_f32_16x16x32_bf16 v[180:183], v[48:51], v[24:27], v[180:183]\nv_mfma_f32_16x16x32_bf16 v[176:179], v[40:43], v[24:27], v[176:179]\n"
      "v_mfma_f32_16x16x32_bf16 v[176:179], v[44:47], v[28:31], v[176:179]\nv_mfma_f32_16x16x32_bf16 v[172:175], v[36:39], v[28:31], v[172:175]\ns_waitcnt vmcnt(0) lgkmcnt(0)\ns_barrier\n"
      "v_mfma_f32_16x16x32_bf16 v[172:175], v[32:35], v[24:27], v[172:175]\nds_read_b64_tr_b16 v[64:65], v104 offset:45056\nv_mfma_f32_16x16x32_bf16 v[188:191], v[32:35], v[0:3], v[188:191]\n"
      "ds_read_b64_tr_b16 v[66:67], v104 offset:46080\nv_mfma_f32_16x16x32_bf16 v[188:191], v[36:39], v[4:7], v[188:191]\nds_read_b64_tr_b16 v[68:69], v104 offset:53248\n"
      "v_mfma_f32_16x16x32_bf16 v[192:195], v[44:47], v[4:7], v[192:195]\nds_read_b64_tr_b16 v[70:71], v104 offset:54272\nv_mfma_f32_16x16x32_bf16 v[192:195], v[40:43], v[0:3], v[192:195]\n"
      "ds_read_b64_tr_b16 v[72:73], v105 offset:45056\nv_mfma_f32_16x16x32_bf16 v[196:199], v[48:51], v[0:3], v[196:199]\nds_read_b64_tr_b16 v[74:75], v105 offset:46080\n"
      "v_mfma_f32_16x16x32_bf16 v[196:199], v[52:55], v[4:7], v[196:199]\nds_read_b64_tr_b16 v[76:77], v105 offset:53248\nv_mfma_f32_16x16x32_bf16 v[200:203], v[60:63], v[4:7], v[200:203]\n"
      "ds_read_b64_tr_b16 v[78:79], v105 offset:54272\nv_mfma_f32_16x16x32_bf16 v[200:203], v[56:59], v[0:3], v[200:203]\nds_read_b64_tr_b16 v[80:81], v106 offset:45056\n"
      "v_mfma_f32_16x16x32_bf16 v[216:219], v[56:59], v[8:11], v[216:219]\nv_mfma_f32_16x16x32_bf16 v[216:219], v[60:63], v[12:15], v[216:219]\n"
      "v_mfma_f32_16x16x32_bf16 v[212:215], v[52:55], v[12:15], v[212:215]\nds_read_b64_tr_b16 v[82:83], v106 offset:46080\nv_mfma_f32_16x16x32_bf16 v[212:215], v[48:51], v[8:11], v[212:215]\n"
      "ds_read_b64_tr_b16 v[84:85], v106 offset:53248\nv_mfma_f32_16x16x32_bf16 v[208:211], v[40:43], v[8:11], v[208:211]\nds_read_b64_tr_b16 v[86:87], v106 offset:54272\n"
      "v_mfma_f32_16x16x32_bf16 v[208:211], v[44:47], v[12:15], v[208:211]\nds_read_b64_tr_b16 v[88:89], v107 offset:45056\nv_mfma_f32_16x16x32_bf16 v[204:207], v[36:39], v[12:15], v[204:207]\n"
      "ds_read_b64_tr_b16 v[90:91], v107 offset:46080\nv_mfma_f32_16x16x32_bf16 v[204:207], v[32:35], v[8:11], v[204:207]\nds_read_b64_tr_b16 v[92:93], v107 offset:53248\n"
      "v_mfma_f32_16x16x32_bf16 v[220:223], v[32:35], v[16:19], v[220:223]\nds_read_b128 v[8:11], v120 offset:45056\nv_mfma_f32_16x16x32_bf16 v[220:223], v[36:39], v[20:23], v[220:223]\n"
      "ds_read_b128 v[12:15], v121 offset:45056\nv_mfma_f32_16x16x32_bf16 v[224:227], v[44:47], v[20:23], v[224:227]\nds_read_b64_tr_b16 v[94:95], v107 offset:54272\n"
      "v_mfma_f32_16x16x32_bf16 v[224:227], v[40:43], v[16:19], v[224:227]\nv_mfma_f32_16x16x32_bf16 v[228:231], v[48:51], v[16:19], v[228:231]\ns_sub_u32 s58, s58, 1\ns_cmp_lg_u32 s58, 0\n"
      "v_mfma_f32_16x16x32_bf16 v[228:231], v[52:55], v[20:23], v[228:231]\nv_mfma_f32_16x16x32_bf16 v[232:235], v[60:63], v[20:23], v[232:235]\ns_waitcnt lgkmcnt(0)\n"
      "v_mfma_f32_16x16x32_bf16 v[232:235], v[56:59], v[16:19], v[232:235]\nds_read_b128 v[16:19], v96 offset:45056\nds_read_b128 v[20:23], v97 offset:45056\ns_cbranch_scc1 .Lgl12\n"
      "v_mfma_f32_16x16x32_bf16 v[152:155], v[88:91], v[8:11], v[152:155]\nds_read_b128 v[24:27], v96 offset:47104\nv_cmp_eq_u32 s[62:63], 0, v240\n"
      "v_mfma_f32_16x16x32_bf16 v[152:155], v[92:95], v[12:15], v[152:155]\nds_read_b128 v[28:31], v97 offset:47104\nv_mov_b32 v253, 262144\n"
      "v_mfma_f32_16x16x32_bf16 v[148:151], v[84:87], v[12:15], v[148:151]\nds_read_b128 v[0:3], v96 offset:49152\nv_mov_b32 v248, 162048\n"
      "v_mfma_f32_16x16x32_bf16 v[148:151], v[80:83], v[8:11], v[148:151]\nds_read_b128 v[4:7], v97 offset:49152\nv_mov_b32 v241, 1\n"
      "v_mfma_f32_16x16x32_bf16 v[144:147], v[72:75], v[8:11], v[144:147]\nv_cndmask_b32 v253, v253, v248, s[62:63]\nv_lshrrev_b32 v112, 4, v240\n"
      "v_mfma_f32_16x16x32_bf16 v[144:147], v[76:79], v[12:15], v[144:147]\nv_bfe_u32 v113, v240, 2, 2\nv_and_b32 v114, 3, v240\n"
      "v_mfma_f32_16x16x32_bf16 v[140:143], v[68:71], v[12:15], v[140:143]\ns_waitcnt lgkmcnt(4)\nv_mul_u32_u24 v114, 0x80008, v114\n"
      "v_mfma_f32_16x16x32_bf16 v[140:143], v[64:67], v[8:11], v[140:143]\nv_add_u32 v244, v112, v113\nv_and_b32 v244, 3, v244\nv_mfma_f32_16x16x32_bf16 v[156:159], v[64:67], v[16:19], v[156:159]\n"
      "ds_read_b128 v[8:11], v96 offset:51200\nv_add_u32 v116, 22, v113\nv_mfma_f32_16x16x32_bf16 v[156:159], v[68:71], v[20:23], v[156:159]\nds_read_b128 v[12:15], v97 offset:51200\n"
      "v_cmp_gt_u32 s[62:63], 2, v113\nv_mfma_f32_16x16x32_bf16 v[160:163], v[76:79], v[20:23], v[160:163]\nv_and_b32 v113, 60, v240\ns_mov_b32 s42, 0\n"
      "v_mfma_f32_16x16x32_bf16 v[160:163], v[72:75], v[16:19], v[160:163]\nv_cndmask_b32 v116, v116, v244, s[62:63]\nv_lshl_or_b32 v244, v244, 16, v116\n"
      "v_mfma_f32_16x16x32_bf16 v[164:167], v[80:83], v[16:19], v[164:167]\nv_lshl_add_u32 v244, v244, 5, v114\nv_lshl_add_u32 v252, v240, 4, s42\n"
      "v_mfma_f32_16x16x32_bf16 v[164:167], v[84:87], v[20:23], v[164:167]\ns_mov_b32 s42, 0x76325410\ns_mov_b32 s43, 0x21005410\n"
      "v_mfma_f32_16x16x32_bf16 v[168:171], v[92:95], v[20:23], v[168:171]\ns_waitcnt lgkmcnt(2)\nv_lshrrev_b64 v[116:117], v113, s[42:43]\n"
      "v_mfma_f32_16x16x32_bf16 v[168:171], v[88:91], v[16:19], v[168:171]\nv_and_b32 v116, 7, v116\nv_lshl_add_u32 v116, v112, 3, v116\n"
      "v_mfma_f32_16x16x32_bf16 v[184:187], v[88:91], v[24:27], v[184:187]\nds_read_b128 v[16:19], v96 offset:53248\ns_mov_b32 s42, 0x54107632\n"
      "v_mfma_f32_16x16x32_bf16 v[184:187], v[92:95], v[28:31], v[184:187]\nds_read_b128 v[20:23], v97 offset:53248\ns_mov_b32 s43, 0x21107632\n"
      "v_mfma_f32_16x16x32_bf16 v[180:183], v[84:87], v[28:31], v[180:183]\nv_lshrrev_b64 v[118:119], v113, s[42:43]\nv_and_b32 v118, 7, v118\n"
      "v_mfma_f32_16x16x32_bf16 v[180:183], v[80:83], v[24:27], v[180:183]\nv_lshl_add_u32 v118, v112, 3, v118\nv_lshl_or_b32 v245, v118, 16, v116\n"
      "v_mfma_f32_16x16x32_bf16 v[176:179], v[72:75], v[24:27], v[176:179]\nv_lshl_add_u32 v245, v245, 5, v114\nv_and_b32 v113, 1, v240\n"
      "v_mfma_f32_16x16x32_bf16 v[176:179], v[76:79], v[28:31], v[176:179]\nv_mul_u32_u24 v113, 0xaaaa, v113\nv_add_u32 v242, 0x4444, v113\n"
      "v_mfma_f32_16x16x32_bf16 v[172:175], v[68:71], v[28:31], v[172:175]\ns_waitcnt vmcnt(0) lgkmcnt(0)\ns_barrier\nv_mfma_f32_16x16x32_bf16 v[172:175], v[64:67], v[24:27], v[172:175]\n"
      "ds_read_b64_tr_b16 v[32:33], v108 offset:0\ns_lshl_b32 s42, s36, 4\nv_mfma_f32_16x16x32_bf16 v[188:191], v[64:67], v[0:3], v[188:191]\nds_read_b64_tr_b16 v[34:35], v108 offset:1024\n"
      "s_add_u32 s42, s42, 160640\nv_mfma_f32_16x16x32_bf16 v[188:191], v[68:71], v[4:7], v[188:191]\nds_read_b64_tr_b16 v[36:37], v108 offset:8192\nv_lshl_add_u32 v243, v112, 2, s42\n"
      "v_mfma_f32_16x16x32_bf16 v[192:195], v[76:79], v[4:7], v[192:195]\nds_read_b64_tr_b16 v[38:39], v108 offset:9216\nv_and_b32 v112, 1, v112\n"
      "v_mfma_f32_16x16x32_bf16 v[192:195], v[72:75], v[0:3], v[192:195]\nds_read_b64_tr_b16 v[40:41], v109 offset:0\nv_lshl_add_u32 v112, v112, 3, s24\n"
      "v_mfma_f32_16x16x32_bf16 v[196:199], v[80:83], v[0:3], v[196:199]\nds_read_b64_tr_b16 v[42:43], v109 offset:1024\ns_sub_u32 s42, s64, 1\n"
      "v_mfma_f32_16x16x32_bf16 v[196:199], v[84:87], v[4:7], v[196:199]\nds_read_b64_tr_b16 v[44:45], v109 offset:8192\nv_min_u32 v112, s42, v112\n"
      "v_mfma_f32_16x16x32_bf16 v[200:203], v[92:95], v[4:7], v[200:203]\nds_read_b64_tr_b16 v[46:47], v109 offset:9216\nv_lshlrev_b32 v112, 3, v112\n"
      "v_mfma_f32_16x16x32_bf16 v[200:203], v[88:91], v[0:3], v[200:203]\nds_read_b64_tr_b16 v[48:49], v110 offset:0\nv_add_u32 v250, 160256, v112\n"
      "v_mfma_f32_16x16x32_bf16 v[216:219], v[88:91], v[8:11], v[216:219]\nv_and_b32 v113, 15, v240\nv_min_u32 v113, 12, v113\nv_mfma_f32_16x16x32_bf16 v[216:219], v[92:95], v[12:15], v[216:219]\n"
      "s_lshl_b32 s42, s30, 7\nv_lshl_add_u32 v113, v113, 15, s42\nv_mfma_f32_16x16x32_bf16 v[212:215], v[84:87], v[12:15], v[212:215]\nds_read_b64_tr_b16 v[50:51], v110 offset:1024\n"
      "v_add_u32 v249, v113, v112\nv_mfma_f32_16x16x32_bf16 v[212:215], v[80:83], v[8:11], v[212:215]\nds_read_b64_tr_b16 v[52:53], v110 offset:8192\ns_mul_i32 s65, s36, 22272\n"
      "v_mfma_f32_16x16x32_bf16 v[208:211], v[72:75], v[8:11], v[208:211]\nds_read_b64_tr_b16 v[54:55], v110 offset:9216\ns_add_u32 s65, s65, 45056\n"
      "v_mfma_f32_16x16x32_bf16 v[208:211], v[76:79], v[12:15], v[208:211]\nds_read_b64_tr_b16 v[56:57], v111 offset:0\ns_add_u32 s68, s64, 8\n"
      "v_mfma_f32_16x16x32_bf16 v[204:207], v[68:71], v[12:15], v[204:207]\nds_read_b64_tr_b16 v[58:59], v111 offset:1024\ns_min_u32 s68, s68, 21\n"
      "v_mfma_f32_16x16x32_bf16 v[204:207], v[64:67], v[8:11], v[204:207]\nds_read_b64_tr_b16 v[60:61], v111 offset:8192\ns_lshl_b32 s38, s38, 16\n"
      "v_mfma_f32_16x16x32_bf16 v[220:223], v[64:67], v[16:19], v[220:223]\nds_read_b128 v[8:11], v122 offset:0\ns_lshr_b32 s42, s24, 2\n"
      "v_mfma_f32_16x16x32_bf16 v[220:223], v[68:71], v[20:23], v[220:223]\nds_read_b128 v[12:15], v123 offset:0\ns_mul_i32 s43, s42, 3\n"
      "v_mfma_f32_16x16x32_bf16 v[224:227], v[76:79], v[20:23], v[224:227]\nds_read_b64_tr_b16 v[62:63], v111 offset:9216\ns_xor_b32 s43, s43, s36\n"
      "v_mfma_f32_16x16x32_bf16 v[224:227], v[72:75], v[16:19], v[224:227]\ns_add_u32 s42, s42, s64\ns_add_u32 s42, s42, 9\nv_mfma_f32_16x16x32_bf16 v[228:231], v[80:83], v[16:19], v[228:231]\n"
      "s_and_b32 s42, s42, 1\ns_lshl1_add_u32 s67, s43, s42\nv_mfma_f32_16x16x32_bf16 v[228:231], v[84:87], v[20:23], v[228:231]\ns_add_u32 s66, s64, s67\ns_cmp_lt_u32 s66, s68\n"
      "v_mfma_f32_16x16x32_bf16 v[232:235], v[92:95], v[20:23], v[232:235]\ns_waitcnt lgkmcnt(0)\ns_cselect_b32 s66, s66, 127\nv_mfma_f32_16x16x32_bf16 v[232:235], v[88:91], v[16:19], v[232:235]\n"
      "ds_read_b128 v[16:19], v98 offset:0\nds_read_b128 v[20:23], v99 offset:0\nv_mfma_f32_16x16x32_bf16 v[152:155], v[56:59], v[8:11], v[152:155]\nds_read_b128 v[24:27], v98 offset:2048\n"
      "s_sub_u32 s66, s66, 8\nv_mfma_f32_16x16x32_bf16 v[152:155], v[60:63], v[12:15], v[152:155]\nds_read_b128 v[28:31], v99 offset:2048\ns_max_i32 s66, s66, 0\n"
      "v_mfma_f32_16x16x32_bf16 v[148:151], v[52:55], v[12:15], v[148:151]\nds_read_b128 v[0:3], v98 offset:4096\ns_getpc_b64 s[34:35]\n"
      ".Lk1065:\nv_mfma_f32_16x16x32_bf16 v[148:151], v[48:51], v[8:11], v[148:151]\nds_read_b128 v[4:7], v99 offset:4096\ns_add_u32 s34, s34, .Lt_g-.Lk1065\n"
      "v_mfma_f32_16x16x32_bf16 v[144:147], v[40:43], v[8:11], v[144:147]\ns_addc_u32 s35, s35, 0\ns_lshr_b32 s42, s2, 3\nv_mfma_f32_16x16x32_bf16 v[144:147], v[44:47], v[12:15], v[144:147]\n"
      "s_lshl1_add_u32 s42, s2, s42\ns_and_b32 s69, s42, 15\nv_mfma_f32_16x16x32_bf16 v[140:143], v[36:39], v[12:15], v[140:143]\ns_waitcnt lgkmcnt(4)\ns_lshl_b32 s43, s69, 7\n"
      "v_mfma_f32_16x16x32_bf16 v[140:143], v[32:35], v[8:11], v[140:143]\ns_add_u32 s43, s43, 32768\ns_add_u32 s8, s20, s43\nv_mfma_f32_16x16x32_bf16 v[156:159], v[32:35], v[16:19], v[156:159]\n"
      "ds_read_b128 v[8:11], v98 offset:6144\ns_addc_u32 s9, s21, 0\nv_mfma_f32_16x16x32_bf16 v[156:159], v[36:39], v[20:23], v[156:159]\nds_read_b128 v[12:15], v99 offset:6144\n"
      "s_add_u32 s12, s20, 65536\nv_mfma_f32_16x16x32_bf16 v[160:163], v[44:47], v[20:23], v[160:163]\ns_addc_u32 s13, s21, 0\ns_add_u32 s16, s20, 2097152\n"
      "v_mfma_f32_16x16x32_bf16 v[160:163], v[40:43], v[16:19], v[160:163]\ns_addc_u32 s17, s21, 0\ns_lshl_b32 s42, s69, 4\nv_mfma_f32_16x16x32_bf16 v[164:167], v[48:51], v[16:19], v[164:167]\n"
      "s_max_u32 s42, s42, 24\ns_sub_u32 s42, s42, 24\nv_mfma_f32_16x16x32_bf16 v[164:167], v[52:55], v[20:23], v[164:167]\ns_min_u32 s69, s42, 192\ns_lshl_b32 s42, s69, 3\n"
      "v_mfma_f32_16x16x32_bf16 v[168:171], v[60:63], v[20:23], v[168:171]\ns_waitcnt lgkmcnt(2)\ns_add_u32 s42, s42, 24576\nv_mfma_f32_16x16x32_bf16 v[168:171], v[56:59], v[16:19], v[168:171]\n"
      "s_add_u32 s14, s20, s42\ns_addc_u32 s15, s21, 0\nv_mfma_f32_16x16x32_bf16 v[184:187], v[56:59], v[24:27], v[184:187]\nds_read_b128 v[16:19], v98 offset:8192\n"
      "v_mfma_f32_16x16x32_bf16 v[184:187], v[60:63], v[28:31], v[184:187]\nds_read_b128 v[20:23], v99 offset:8192\nv_mfma_f32_16x16x32_bf16 v[180:183], v[52:55], v[28:31], v[180:183]\n"
      "v_mfma_f32_16x16x32_bf16 v[180:183], v[48:51], v[24:27], v[180:183]\nv_mfma_f32_16x16x32_bf16 v[176:179], v[40:43], v[24:27], v[176:179]\n"
      "v_mfma_f32_16x16x32_bf16 v[176:179], v[44:47], v[28:31], v[176:179]\nv_mfma_f32_16x16x32_bf16 v[172:175], v[36:39], v[28:31], v[172:175]\ns_waitcnt vmcnt(0) lgkmcnt(0)\ns_barrier\n"
      "v_mfma_f32_16x16x32_bf16 v[172:175], v[32:35], v[24:27], v[172:175]\nds_read_b128 v[72:75], v252 offset:0\nv_lshl_add_u32 v246, v240, 1, v248\n"
      "v_mfma_f32_16x16x32_bf16 v[188:191], v[32:35], v[0:3], v[188:191]\nds_read_b128 v[24:27], v236 offset:512\nds_read_u16 v251, v246 offset:64\n"
      "v_mfma_f32_16x16x32_bf16 v[188:191], v[36:39], v[4:7], v[188:191]\nds_read_b128 v[28:31], v236 offset:576\nds_read_b32 v247, v248 offset:24\n"
      "v_mfma_f32_16x16x32_bf16 v[192:195], v[44:47], v[4:7], v[192:195]\nds_read_b128 v[76:79], v252 offset:1024\nv_and_b32 v246, 48, v240\n"
      "v_mfma_f32_16x16x32_bf16 v[192:195], v[40:43], v[0:3], v[192:195]\nds_read_b128 v[80:83], v252 offset:2048\ns_mov_b32 s42, 42240\n"
      "v_mfma_f32_16x16x32_bf16 v[196:199], v[48:51], v[0:3], v[196:199]\nds_read_b128 v[84:87], v252 offset:3072\nv_lshl_add_u32 v246, v246, 2, s42\n"
      "v_mfma_f32_16x16x32_bf16 v[196:199], v[52:55], v[4:7], v[196:199]\nds_read_b128 v[88:91], v252 offset:4096\ns_mov_b32 s58, 0xbdd2d3e7\n"
      "v_mfma_f32_16x16x32_bf16 v[200:203], v[60:63], v[4:7], v[200:203]\nds_read_b128 v[92:95], v252 offset:5120\ns_mov_b32 s59, 0xc0135761\n"
      "v_mfma_f32_16x16x32_bf16 v[200:203], v[56:59], v[0:3], v[200:203]\nds_read_b128 v[96:99], v252 offset:6144\ns_mov_b32 s60, 0xc1e00000\n"
      "v_mfma_f32_16x16x32_bf16 v[216:219], v[56:59], v[8:11], v[216:219]\nds_read_b128 v[0:3], v236 offset:0\ns_add_u32 s49, s65, 1856\n"
      "v_mfma_f32_16x16x32_bf16 v[216:219], v[60:63], v[12:15], v[216:219]\nds_read_b128 v[4:7], v236 offset:64\ns_mov_b32 s45, 2\n"
      "v_mfma_f32_16x16x32_bf16 v[212:215], v[52:55], v[12:15], v[212:215]\nds_read_b128 v[100:103], v252 offset:7168\ns_mov_b32 s46, 0\n"
      "v_mfma_f32_16x16x32_bf16 v[212:215], v[48:51], v[8:11], v[212:215]\nds_read_b128 v[104:107], v252 offset:8192\ns_waitcnt lgkmcnt(8)\n"
      "v_mfma_f32_16x16x32_bf16 v[208:211], v[40:43], v[8:11], v[208:211]\nds_read_b128 v[108:111], v252 offset:9216\nv_readfirstlane_b32 s39, v247\n"
      "v_mfma_f32_16x16x32_bf16 v[208:211], v[44:47], v[12:15], v[208:211]\nds_read_b128 v[64:67], v237 offset:512\nv_readlane_b32 s55, v251, 0\n"
      "v_mfma_f32_16x16x32_bf16 v[204:207], v[36:39], v[12:15], v[204:207]\nds_read_b128 v[68:71], v237 offset:576\ns_setprio 3\nv_mfma_f32_16x16x32_bf16 v[204:207], v[32:35], v[8:11], v[204:207]\n"
      "ds_read_b128 v[112:115], v252 offset:10240\ns_xor_b32 s40, s39, 0xa5c30f96\nv_mfma_f32_16x16x32_bf16 v[220:223], v[32:35], v[16:19], v[220:223]\nds_read_b128 v[8:11], v237 offset:0\n"
      "s_mul_i32 s41, s39, 0x9E3779B1\nv_mfma_f32_16x16x32_bf16 v[220:223], v[36:39], v[20:23], v[220:223]\nds_read_b128 v[12:15], v237 offset:64\ns_lshr_b32 s41, s41, 24\n"
      "v_mfma_f32_16x16x32_bf16 v[224:227], v[44:47], v[20:23], v[224:227]\nds_read_b128 v[116:119], v252 offset:11264\nv_mfma_f32_16x16x32_bf16 v[224:227], v[40:43], v[16:19], v[224:227]\n"
      "ds_read_b128 v[120:123], v252 offset:12288\nv_add_u32_sdwa v239, s49, v245 dst_sel:DWORD dst_unused:UNUSED_PAD src0_sel:DWORD src1_sel:WORD_1\n"
      "v_mfma_f32_16x16x32_bf16 v[228:231], v[48:51], v[16:19], v[228:231]\nds_read_b128 v[124:127], v252 offset:13312\n"
      "v_add_u32_sdwa v236, s49, v244 dst_sel:DWORD dst_unused:UNUSED_PAD src0_sel:DWORD src1_sel:WORD_0\nv_mfma_f32_16x16x32_bf16 v[228:231], v[52:55], v[20:23], v[228:231]\n"
      "ds_read_b128 v[128:131], v252 offset:14336\nv_add_u32_sdwa v237, s49, v244 dst_sel:DWORD dst_unused:UNUSED_PAD src0_sel:DWORD src1_sel:WORD_1\n"
      "v_mfma_f32_16x16x32_bf16 v[232:235], v[60:63], v[20:23], v[232:235]\nds_read_b128 v[132:135], v252 offset:15360\n"
      "v_add_u32_sdwa v238, s49, v245 dst_sel:DWORD dst_unused:UNUSED_PAD src0_sel:DWORD src1_sel:WORD_0\ns_waitcnt lgkmcnt(0)\nv_mfma_f32_16x16x32_bf16 v[232:235], v[56:59], v[16:19], v[232:235]\n"
      "ds_read_b128 v[136:139], v252 offset:16384\nv_mov_b32 v42, 0\nv_pk_mul_f32 v[44:45], v[140:141], v[140:141]\nv_pk_mul_f32 v[46:47], v[142:143], v[142:143]\n"
      "v_pk_mul_f32 v[48:49], v[144:145], v[144:145]\nv_pk_mul_f32 v[50:51], v[146:147], v[146:147]\nv_pk_fma_f32 v[44:45], v[44:45], s[58:59], s[58:59] op_sel:[0,0,1] op_sel_hi:[1,0,1]\n"
      "v_pk_fma_f32 v[46:47], v[46:47], s[58:59], s[58:59] op_sel:[0,0,1] op_sel_hi:[1,0,1]\nv_pk_fma_f32 v[48:49], v[48:49], s[58:59], s[58:59] op_sel:[0,0,1] op_sel_hi:[1,0,1]\n"
      "v_pk_fma_f32 v[50:51], v[50:51], s[58:59], s[58:59] op_sel:[0,0,1] op_sel_hi:[1,0,1]\nv_pk_fma_f32 v[44:45], v[44:45], v[140:141], s[60:61] op_sel_hi:[1,1,0]\n"
      "v_pk_fma_f32 v[46:47], v[46:47], v[142:143], s[60:61] op_sel_hi:[1,1,0]\nv_pk_fma_f32 v[48:49], v[48:49], v[144:145], s[60:61] op_sel_hi:[1,1,0]\n"
      "v_pk_fma_f32 v[50:51], v[50:51], v[146:147], s[60:61] op_sel_hi:[1,1,0]\nv_exp_f32 v44, v44 clamp\nv_pk_mul_f32 v[52:53], v[148:149], v[148:149]\nv_exp_f32 v45, v45 clamp\n"
      "v_pk_mul_f32 v[54:55], v[150:151], v[150:151]\nv_pk_mul_f32 v[56:57], v[152:153], v[152:153]\nv_exp_f32 v46, v46 clamp\nv_pk_mul_f32 v[58:59], v[154:155], v[154:155]\n"
      "v_exp_f32 v47, v47 clamp\nv_pk_fma_f32 v[52:53], v[52:53], s[58:59], s[58:59] op_sel:[0,0,1] op_sel_hi:[1,0,1]\n"
      "v_pk_fma_f32 v[54:55], v[54:55], s[58:59], s[58:59] op_sel:[0,0,1] op_sel_hi:[1,0,1]\nv_exp_f32 v48, v48 clamp\n"
      "v_pk_fma_f32 v[56:57], v[56:57], s[58:59], s[58:59] op_sel:[0,0,1] op_sel_hi:[1,0,1]\nv_exp_f32 v49, v49 clamp\n"
      "v_pk_fma_f32 v[58:59], v[58:59], s[58:59], s[58:59] op_sel:[0,0,1] op_sel_hi:[1,0,1]\nv_pk_fma_f32 v[52:53], v[52:53], v[148:149], s[60:61] op_sel_hi:[1,1,0]\nv_exp_f32 v50, v50 clamp\n"
      "v_pk_fma_f32 v[54:55], v[54:55], v[150:151], s[60:61] op_sel_hi:[1,1,0]\nv_exp_f32 v51, v51 clamp\nv_pk_fma_f32 v[56:57], v[56:57], v[152:153], s[60:61] op_sel_hi:[1,1,0]\n"
      "v_pk_fma_f32 v[58:59], v[58:59], v[154:155], s[60:61] op_sel_hi:[1,1,0]\nv_exp_f32 v52, v52 clamp\nv_pk_fma_f32 v[46:47], v[46:47], v[2:3], v[26:27]\n"
      "v_pk_fma_f32 v[44:45], v[44:45], v[0:1], v[24:25]\nv_pk_fma_f32 v[48:49], v[48:49], v[4:5], v[28:29]\nv_exp_f32 v53, v53 clamp\nv_pk_fma_f32 v[50:51], v[50:51], v[6:7], v[30:31]\n"
      "v_pk_mul_f32 v[140:141], v[140:141], v[46:47]\nv_pk_mul_f32 v[144:145], v[144:145], v[50:51]\nv_exp_f32 v54, v54 clamp\nv_pk_fma_f32 v[140:141], v[142:143], v[44:45], v[140:141]\n"
      "v_pk_fma_f32 v[144:145], v[146:147], v[48:49], v[144:145]\nv_pk_mul_f32 v[44:45], v[44:45], v[46:47]\nv_exp_f32 v55, v55 clamp\nv_pk_mul_f32 v[48:49], v[48:49], v[50:51]\n"
      "v_pk_mul_f32 v[46:47], v[44:45], v[48:49]\nv_rcp_f32 v46, v46\nv_pk_mul_f32 v[140:141], v[140:141], v[48:49]\nv_rcp_f32 v47, v47\nv_pk_fma_f32 v[140:141], v[144:145], v[44:45], v[140:141]\n"
      "v_pk_mul_f32 v[32:33], v[140:141], v[46:47]\nv_pk_mul_f32 v[16:17], v[156:157], v[156:157]\nv_pk_mul_f32 v[18:19], v[158:159], v[158:159]\nv_exp_f32 v56, v56 clamp\n"
      "v_pk_mul_f32 v[20:21], v[160:161], v[160:161]\nv_pk_mul_f32 v[22:23], v[162:163], v[162:163]\nv_exp_f32 v57, v57 clamp\n"
      "v_pk_fma_f32 v[16:17], v[16:17], s[58:59], s[58:59] op_sel:[0,0,1] op_sel_hi:[1,0,1]\nv_pk_fma_f32 v[18:19], v[18:19], s[58:59], s[58:59] op_sel:[0,0,1] op_sel_hi:[1,0,1]\n"
      "v_pk_fma_f32 v[20:21], v[20:21], s[58:59], s[58:59] op_sel:[0,0,1] op_sel_hi:[1,0,1]\nv_exp_f32 v58, v58 clamp\n"
      "v_pk_fma_f32 v[22:23], v[22:23], s[58:59], s[58:59] op_sel:[0,0,1] op_sel_hi:[1,0,1]\nv_pk_fma_f32 v[16:17], v[16:17], v[156:157], s[60:61] op_sel_hi:[1,1,0]\n"
      "v_pk_fma_f32 v[18:19], v[18:19], v[158:159], s[60:61] op_sel_hi:[1,1,0]\nv_exp_f32 v59, v59 clamp\nv_pk_fma_f32 v[20:21], v[20:21], v[160:161], s[60:61] op_sel_hi:[1,1,0]\n"
      "v_pk_fma_f32 v[22:23], v[22:23], v[162:163], s[60:61] op_sel_hi:[1,1,0]\nv_exp_f32 v16, v16 clamp\nv_pk_fma_f32 v[54:55], v[54:55], v[10:11], v[66:67]\n"
      "v_pk_fma_f32 v[52:53], v[52:53], v[8:9], v[64:65]\nv_pk_fma_f32 v[56:57], v[56:57], v[12:13], v[68:69]\nv_exp_f32 v17, v17 clamp\nv_pk_fma_f32 v[58:59], v[58:59], v[14:15], v[70:71]\n"
      "v_pk_mul_f32 v[148:149], v[148:149], v[54:55]\nv_pk_mul_f32 v[152:153], v[152:153], v[58:59]\nv_exp_f32 v18, v18 clamp\nv_pk_fma_f32 v[148:149], v[150:151], v[52:53], v[148:149]\n"
      "v_pk_fma_f32 v[152:153], v[154:155], v[56:57], v[152:153]\nv_pk_mul_f32 v[52:53], v[52:53], v[54:55]\nv_exp_f32 v19, v19 clamp\nv_pk_mul_f32 v[56:57], v[56:57], v[58:59]\n"
      "v_pk_mul_f32 v[54:55], v[52:53], v[56:57]\nv_rcp_f32 v54, v54\nv_pk_mul_f32 v[148:149], v[148:149], v[56:57]\nv_rcp_f32 v55, v55\nv_pk_fma_f32 v[148:149], v[152:153], v[52:53], v[148:149]\n"
      "v_pk_fma_f32 v[32:33], v[148:149], v[54:55], v[32:33]\nv_add_f32 v41, v32, v33\nv_pk_mul_f32 v[44:45], v[164:165], v[164:165]\nv_pk_mul_f32 v[46:47], v[166:167], v[166:167]\n"
      "v_exp_f32 v20, v20 clamp\nv_pk_mul_f32 v[48:49], v[168:169], v[168:169]\nv_pk_mul_f32 v[50:51], v[170:171], v[170:171]\nv_exp_f32 v21, v21 clamp\n"
      "v_pk_fma_f32 v[44:45], v[44:45], s[58:59], s[58:59] op_sel:[0,0,1] op_sel_hi:[1,0,1]\nv_pk_fma_f32 v[46:47], v[46:47], s[58:59], s[58:59] op_sel:[0,0,1] op_sel_hi:[1,0,1]\n"
      "v_pk_fma_f32 v[48:49], v[48:49], s[58:59], s[58:59] op_sel:[0,0,1] op_sel_hi:[1,0,1]\nv_exp_f32 v22, v22 clamp\n"
      "v_pk_fma_f32 v[50:51], v[50:51], s[58:59], s[58:59] op_sel:[0,0,1] op_sel_hi:[1,0,1]\nv_pk_fma_f32 v[44:45], v[44:45], v[164:165], s[60:61] op_sel_hi:[1,1,0]\n"
      "v_pk_fma_f32 v[46:47], v[46:47], v[166:167], s[60:61] op_sel_hi:[1,1,0]\nv_exp_f32 v23, v23 clamp\nv_pk_fma_f32 v[48:49], v[48:49], v[168:169], s[60:61] op_sel_hi:[1,1,0]\n"
      "v_pk_fma_f32 v[50:51], v[50:51], v[170:171], s[60:61] op_sel_hi:[1,1,0]\nv_exp_f32 v44, v44 clamp\nv_pk_fma_f32 v[18:19], v[18:19], v[2:3], v[26:27]\n"
      "v_pk_fma_f32 v[16:17], v[16:17], v[0:1], v[24:25]\nv_pk_fma_f32 v[20:21], v[20:21], v[4:5], v[28:29]\nv_exp_f32 v45, v45 clamp\nv_pk_fma_f32 v[22:23], v[22:23], v[6:7], v[30:31]\n"
      "v_pk_mul_f32 v[156:157], v[156:157], v[18:19]\nv_pk_mul_f32 v[160:161], v[160:161], v[22:23]\nv_exp_f32 v46, v46 clamp\nv_pk_fma_f32 v[156:157], v[158:159], v[16:17], v[156:157]\n"
      "v_pk_fma_f32 v[160:161], v[162:163], v[20:21], v[160:161]\nv_pk_mul_f32 v[16:17], v[16:17], v[18:19]\nv_exp_f32 v47, v47 clamp\nv_pk_mul_f32 v[20:21], v[20:21], v[22:23]\n"
      "v_pk_mul_f32 v[18:19], v[16:17], v[20:21]\nv_rcp_f32 v18, v18\nv_pk_mul_f32 v[156:157], v[156:157], v[20:21]\nv_rcp_f32 v19, v19\nv_pk_fma_f32 v[156:157], v[160:161], v[16:17], v[156:157]\n"
      "v_pk_mul_f32 v[32:33], v[156:157], v[18:19]\nv_pk_mul_f32 v[52:53], v[172:173], v[172:173]\nv_pk_mul_f32 v[54:55], v[174:175], v[174:175]\nv_exp_f32 v48, v48 clamp\n"
      "v_pk_mul_f32 v[56:57], v[176:177], v[176:177]\nv_pk_mul_f32 v[58:59], v[178:179], v[178:179]\nv_exp_f32 v49, v49 clamp\n"
      "v_pk_fma_f32 v[52:53], v[52:53], s[58:59], s[58:59] op_sel:[0,0,1] op_sel_hi:[1,0,1]\nv_pk_fma_f32 v[54:55], v[54:55], s[58:59], s[58:59] op_sel:[0,0,1] op_sel_hi:[1,0,1]\n"
      "v_pk_fma_f32 v[56:57], v[56:57], s[58:59], s[58:59] op_sel:[0,0,1] op_sel_hi:[1,0,1]\nv_exp_f32 v50, v50 clamp\n"
      "v_pk_fma_f32 v[58:59], v[58:59], s[58:59], s[58:59] op_sel:[0,0,1] op_sel_hi:[1,0,1]\nv_pk_fma_f32 v[52:53], v[52:53], v[172:173], s[60:61] op_sel_hi:[1,1,0]\n"
      "v_pk_fma_f32 v[54:55], v[54:55], v[174:175], s[60:61] op_sel_hi:[1,1,0]\nv_exp_f32 v51, v51 clamp\nv_pk_fma_f32 v[56:57], v[56:57], v[176:177], s[60:61] op_sel_hi:[1,1,0]\n"
      "v_pk_fma_f32 v[58:59], v[58:59], v[178:179], s[60:61] op_sel_hi:[1,1,0]\nv_exp_f32 v52, v52 clamp\nv_pk_fma_f32 v[46:47], v[46:47], v[10:11], v[66:67]\n"
      "v_pk_fma_f32 v[44:45], v[44:45], v[8:9], v[64:65]\nv_pk_fma_f32 v[48:49], v[48:49], v[12:13], v[68:69]\nv_exp_f32 v53, v53 clamp\nv_pk_fma_f32 v[50:51], v[50:51], v[14:15], v[70:71]\n"
      "v_pk_mul_f32 v[164:165], v[164:165], v[46:47]\nv_pk_mul_f32 v[168:169], v[168:169], v[50:51]\nv_exp_f32 v54, v54 clamp\nv_pk_fma_f32 v[164:165], v[166:167], v[44:45], v[164:165]\n"
      "v_pk_fma_f32 v[168:169], v[170:171], v[48:49], v[168:169]\nv_pk_mul_f32 v[44:45], v[44:45], v[46:47]\nv_exp_f32 v55, v55 clamp\nv_pk_mul_f32 v[48:49], v[48:49], v[50:51]\n"
      "v_pk_mul_f32 v[46:47], v[44:45], v[48:49]\nv_rcp_f32 v46, v46\nv_pk_mul_f32 v[164:165], v[164:165], v[48:49]\nv_rcp_f32 v47, v47\nv_pk_fma_f32 v[164:165], v[168:169], v[44:45], v[164:165]\n"
      "v_pk_fma_f32 v[32:33], v[164:165], v[46:47], v[32:33]\nv_add_f32 v36, v32, v33\nv_pk_mul_f32 v[16:17], v[180:181], v[180:181]\nv_pk_mul_f32 v[18:19], v[182:183], v[182:183]\n"
      "v_exp_f32 v56, v56 clamp\nv_pk_mul_f32 v[20:21], v[184:185], v[184:185]\nv_pk_mul_f32 v[22:23], v[186:187], v[186:187]\nv_exp_f32 v57, v57 clamp\n"
      "v_pk_fma_f32 v[16:17], v[16:17], s[58:59], s[58:59] op_sel:[0,0,1] op_sel_hi:[1,0,1]\nv_pk_fma_f32 v[18:19], v[18:19], s[58:59], s[58:59] op_sel:[0,0,1] op_sel_hi:[1,0,1]\n"
      "v_pk_fma_f32 v[20:21], v[20:21], s[58:59], s[58:59] op_sel:[0,0,1] op_sel_hi:[1,0,1]\nv_exp_f32 v58, v58 clamp\n"
      "v_pk_fma_f32 v[22:23], v[22:23], s[58:59], s[58:59] op_sel:[0,0,1] op_sel_hi:[1,0,1]\nv_pk_fma_f32 v[16:17], v[16:17], v[180:181], s[60:61] op_sel_hi:[1,1,0]\n"
      "v_pk_fma_f32 v[18:19], v[18:19], v[182:183], s[60:61] op_sel_hi:[1,1,0]\nv_exp_f32 v59, v59 clamp\nv_pk_fma_f32 v[20:21], v[20:21], v[184:185], s[60:61] op_sel_hi:[1,1,0]\n"
      "v_pk_fma_f32 v[22:23], v[22:23], v[186:187], s[60:61] op_sel_hi:[1,1,0]\nv_exp_f32 v16, v16 clamp\nv_pk_fma_f32 v[54:55], v[54:55], v[2:3], v[26:27]\n"
      "v_pk_fma_f32 v[52:53], v[52:53], v[0:1], v[24:25]\nv_pk_fma_f32 v[56:57], v[56:57], v[4:5], v[28:29]\nv_exp_f32 v17, v17 clamp\nv_pk_fma_f32 v[58:59], v[58:59], v[6:7], v[30:31]\n"
      "v_pk_mul_f32 v[172:173], v[172:173], v[54:55]\nv_pk_mul_f32 v[176:177], v[176:177], v[58:59]\nv_exp_f32 v18, v18 clamp\nv_pk_fma_f32 v[172:173], v[174:175], v[52:53], v[172:173]\n"
      "v_pk_fma_f32 v[176:177], v[178:179], v[56:57], v[176:177]\nv_pk_mul_f32 v[52:53], v[52:53], v[54:55]\nv_exp_f32 v19, v19 clamp\nv_pk_mul_f32 v[56:57], v[56:57], v[58:59]\n"
      "v_pk_mul_f32 v[54:55], v[52:53], v[56:57]\nv_rcp_f32 v54, v54\nv_pk_mul_f32 v[172:173], v[172:173], v[56:57]\nv_rcp_f32 v55, v55\nv_pk_fma_f32 v[172:173], v[176:177], v[52:53], v[172:173]\n"
      "v_pk_mul_f32 v[32:33], v[172:173], v[54:55]\nv_pk_mul_f32 v[44:45], v[188:189], v[188:189]\nv_pk_mul_f32 v[46:47], v[190:191], v[190:191]\nv_exp_f32 v20, v20 clamp\n"
      "v_pk_mul_f32 v[48:49], v[192:193], v[192:193]\nv_pk_mul_f32 v[50:51], v[194:195], v[194:195]\nv_exp_f32 v21, v21 clamp\n"
      "v_pk_fma_f32 v[44:45], v[44:45], s[58:59], s[58:59] op_sel:[0,0,1] op_sel_hi:[1,0,1]\nv_pk_fma_f32 v[46:47], v[46:47], s[58:59], s[58:59] op_sel:[0,0,1] op_sel_hi:[1,0,1]\n"
      "v_pk_fma_f32 v[48:49], v[48:49], s[58:59], s[58:59] op_sel:[0,0,1] op_sel_hi:[1,0,1]\nv_exp_f32 v22, v22 clamp\n"
      "v_pk_fma_f32 v[50:51], v[50:51], s[58:59], s[58:59] op_sel:[0,0,1] op_sel_hi:[1,0,1]\nv_pk_fma_f32 v[44:45], v[44:45], v[188:189], s[60:61] op_sel_hi:[1,1,0]\n"
      "v_pk_fma_f32 v[46:47], v[46:47], v[190:191], s[60:61] op_sel_hi:[1,1,0]\nv_exp_f32 v23, v23 clamp\nv_pk_fma_f32 v[48:49], v[48:49], v[192:193], s[60:61] op_sel_hi:[1,1,0]\n"
      "v_pk_fma_f32 v[50:51], v[50:51], v[194:195], s[60:61] op_sel_hi:[1,1,0]\nv_exp_f32 v44, v44 clamp\nv_pk_fma_f32 v[18:19], v[18:19], v[10:11], v[66:67]\n"
      "v_pk_fma_f32 v[16:17], v[16:17], v[8:9], v[64:65]\nv_pk_fma_f32 v[20:21], v[20:21], v[12:13], v[68:69]\nv_exp_f32 v45, v45 clamp\nv_pk_fma_f32 v[22:23], v[22:23], v[14:15], v[70:71]\n"
      "v_pk_mul_f32 v[180:181], v[180:181], v[18:19]\nv_pk_mul_f32 v[184:185], v[184:185], v[22:23]\nv_exp_f32 v46, v46 clamp\nv_pk_fma_f32 v[180:181], v[182:183], v[16:17], v[180:181]\n"
      "v_pk_fma_f32 v[184:185], v[186:187], v[20:21], v[184:185]\nv_pk_mul_f32 v[16:17], v[16:17], v[18:19]\nv_exp_f32 v47, v47 clamp\nv_pk_mul_f32 v[20:21], v[20:21], v[22:23]\n"
      "v_pk_mul_f32 v[18:19], v[16:17], v[20:21]\nv_rcp_f32 v18, v18\nv_pk_mul_f32 v[180:181], v[180:181], v[20:21]\nv_rcp_f32 v19, v19\nv_pk_fma_f32 v[180:181], v[184:185], v[16:17], v[180:181]\n"
      "v_pk_fma_f32 v[32:33], v[180:181], v[18:19], v[32:33]\nv_add_f32 v37, v32, v33\nv_pk_mul_f32 v[52:53], v[196:197], v[196:197]\nv_pk_mul_f32 v[54:55], v[198:199], v[198:199]\n"
      "v_exp_f32 v48, v48 clamp\nv_pk_mul_f32 v[56:57], v[200:201], v[200:201]\nv_pk_mul_f32 v[58:59], v[202:203], v[202:203]\nv_exp_f32 v49, v49 clamp\n"
      "v_pk_fma_f32 v[52:53], v[52:53], s[58:59], s[58:59] op_sel:[0,0,1] op_sel_hi:[1,0,1]\nv_pk_fma_f32 v[54:55], v[54:55], s[58:59], s[58:59] op_sel:[0,0,1] op_sel_hi:[1,0,1]\n"
      "v_pk_fma_f32 v[56:57], v[56:57], s[58:59], s[58:59] op_sel:[0,0,1] op_sel_hi:[1,0,1]\nv_exp_f32 v50, v50 clamp\n"
      "v_pk_fma_f32 v[58:59], v[58:59], s[58:59], s[58:59] op_sel:[0,0,1] op_sel_hi:[1,0,1]\nv_pk_fma_f32 v[52:53], v[52:53], v[196:197], s[60:61] op_sel_hi:[1,1,0]\n"
      "v_pk_fma_f32 v[54:55], v[54:55], v[198:199], s[60:61] op_sel_hi:[1,1,0]\nv_exp_f32 v51, v51 clamp\nv_pk_fma_f32 v[56:57], v[56:57], v[200:201], s[60:61] op_sel_hi:[1,1,0]\n"
      "v_pk_fma_f32 v[58:59], v[58:59], v[202:203], s[60:61] op_sel_hi:[1,1,0]\nv_exp_f32 v52, v52 clamp\nv_pk_fma_f32 v[46:47], v[46:47], v[2:3], v[26:27]\n"
      "v_pk_fma_f32 v[44:45], v[44:45], v[0:1], v[24:25]\nv_pk_fma_f32 v[48:49], v[48:49], v[4:5], v[28:29]\nv_exp_f32 v53, v53 clamp\nv_pk_fma_f32 v[50:51], v[50:51], v[6:7], v[30:31]\n"
      "v_pk_mul_f32 v[188:189], v[188:189], v[46:47]\nv_pk_mul_f32 v[192:193], v[192:193], v[50:51]\nv_exp_f32 v54, v54 clamp\nv_pk_fma_f32 v[188:189], v[190:191], v[44:45], v[188:189]\n"
      "v_pk_fma_f32 v[192:193], v[194:195], v[48:49], v[192:193]\nv_pk_mul_f32 v[44:45], v[44:45], v[46:47]\nv_exp_f32 v55, v55 clamp\nv_pk_mul_f32 v[48:49], v[48:49], v[50:51]\n"
      "v_pk_mul_f32 v[46:47], v[44:45], v[48:49]\nv_rcp_f32 v46, v46\nv_pk_mul_f32 v[188:189], v[188:189], v[48:49]\nv_rcp_f32 v47, v47\nv_pk_fma_f32 v[188:189], v[192:193], v[44:45], v[188:189]\n"
      "v_pk_mul_f32 v[32:33], v[188:189], v[46:47]\nv_pk_mul_f32 v[16:17], v[204:205], v[204:205]\nv_pk_mul_f32 v[18:19], v[206:207], v[206:207]\nv_exp_f32 v56, v56 clamp\n"
      "v_pk_mul_f32 v[20:21], v[208:209], v[208:209]\nv_pk_mul_f32 v[22:23], v[210:211], v[210:211]\nv_exp_f32 v57, v57 clamp\n"
      "v_pk_fma_f32 v[16:17], v[16:17], s[58:59], s[58:59] op_sel:[0,0,1] op_sel_hi:[1,0,1]\nv_pk_fma_f32 v[18:19], v[18:19], s[58:59], s[58:59] op_sel:[0,0,1] op_sel_hi:[1,0,1]\n"
      "v_pk_fma_f32 v[20:21], v[20:21], s[58:59], s[58:59] op_sel:[0,0,1] op_sel_hi:[1,0,1]\nv_exp_f32 v58, v58 clamp\n"
      "v_pk_fma_f32 v[22:23], v[22:23], s[58:59], s[58:59] op_sel:[0,0,1] op_sel_hi:[1,0,1]\nv_pk_fma_f32 v[16:17], v[16:17], v[204:205], s[60:61] op_sel_hi:[1,1,0]\n"
      "v_pk_fma_f32 v[18:19], v[18:19], v[206:207], s[60:61] op_sel_hi:[1,1,0]\nv_exp_f32 v59, v59 clamp\nv_pk_fma_f32 v[20:21], v[20:21], v[208:209], s[60:61] op_sel_hi:[1,1,0]\n"
      "v_pk_fma_f32 v[22:23], v[22:23], v[210:211], s[60:61] op_sel_hi:[1,1,0]\nv_exp_f32 v16, v16 clamp\nv_pk_fma_f32 v[54:55], v[54:55], v[10:11], v[66:67]\n"
      "v_pk_fma_f32 v[52:53], v[52:53], v[8:9], v[64:65]\nv_pk_fma_f32 v[56:57], v[56:57], v[12:13], v[68:69]\nv_exp_f32 v17, v17 clamp\nv_pk_fma_f32 v[58:59], v[58:59], v[14:15], v[70:71]\n"
      "v_pk_mul_f32 v[196:197], v[196:197], v[54:55]\nv_pk_mul_f32 v[200:201], v[200:201], v[58:59]\nv_exp_f32 v18, v18 clamp\nv_pk_fma_f32 v[196:197], v[198:199], v[52:53], v[196:197]\n"
      "v_pk_fma_f32 v[200:201], v[202:203], v[56:57], v[200:201]\nv_pk_mul_f32 v[52:53], v[52:53], v[54:55]\nv_exp_f32 v19, v19 clamp\nv_pk_mul_f32 v[56:57], v[56:57], v[58:59]\n"
      "v_pk_mul_f32 v[54:55], v[52:53], v[56:57]\nv_rcp_f32 v54, v54\nv_pk_mul_f32 v[196:197], v[196:197], v[56:57]\nv_rcp_f32 v55, v55\nv_pk_fma_f32 v[196:197], v[200:201], v[52:53], v[196:197]\n"
      "v_pk_fma_f32 v[32:33], v[196:197], v[54:55], v[32:33]\nv_add_f32 v38, v32, v33\nv_pk_mul_f32 v[44:45], v[212:213], v[212:213]\nv_pk_mul_f32 v[46:47], v[214:215], v[214:215]\n"
      "v_exp_f32 v20, v20 clamp\nv_pk_mul_f32 v[48:49], v[216:217], v[216:217]\nv_pk_mul_f32 v[50:51], v[218:219], v[218:219]\nv_exp_f32 v21, v21 clamp\n"
      "v_pk_fma_f32 v[44:45], v[44:45], s[58:59], s[58:59] op_sel:[0,0,1] op_sel_hi:[1,0,1]\nv_pk_fma_f32 v[46:47], v[46:47], s[58:59], s[58:59] op_sel:[0,0,1] op_sel_hi:[1,0,1]\n"
      "v_pk_fma_f32 v[48:49], v[48:49], s[58:59], s[58:59] op_sel:[0,0,1] op_sel_hi:[1,0,1]\nv_exp_f32 v22, v22 clamp\n"
      "v_pk_fma_f32 v[50:51], v[50:51], s[58:59], s[58:59] op_sel:[0,0,1] op_sel_hi:[1,0,1]\nv_pk_fma_f32 v[44:45], v[44:45], v[212:213], s[60:61] op_sel_hi:[1,1,0]\n"
      "v_pk_fma_f32 v[46:47], v[46:47], v[214:215], s[60:61] op_sel_hi:[1,1,0]\nv_exp_f32 v23, v23 clamp\nv_pk_fma_f32 v[48:49], v[48:49], v[216:217], s[60:61] op_sel_hi:[1,1,0]\n"
      "v_pk_fma_f32 v[50:51], v[50:51], v[218:219], s[60:61] op_sel_hi:[1,1,0]\nv_exp_f32 v44, v44 clamp\nv_pk_fma_f32 v[18:19], v[18:19], v[2:3], v[26:27]\n"
      "v_pk_fma_f32 v[16:17], v[16:17], v[0:1], v[24:25]\nv_pk_fma_f32 v[20:21], v[20:21], v[4:5], v[28:29]\nv_exp_f32 v45, v45 clamp\nv_pk_fma_f32 v[22:23], v[22:23], v[6:7], v[30:31]\n"
      "v_pk_mul_f32 v[204:205], v[204:205], v[18:19]\nv_pk_mul_f32 v[208:209], v[208:209], v[22:23]\nv_exp_f32 v46, v46 clamp\nv_pk_fma_f32 v[204:205], v[206:207], v[16:17], v[204:205]\n"
      "v_pk_fma_f32 v[208:209], v[210:211], v[20:21], v[208:209]\nv_pk_mul_f32 v[16:17], v[16:17], v[18:19]\nv_exp_f32 v47, v47 clamp\nv_pk_mul_f32 v[20:21], v[20:21], v[22:23]\n"
      "v_pk_mul_f32 v[18:19], v[16:17], v[20:21]\nv_rcp_f32 v18, v18\nv_pk_mul_f32 v[204:205], v[204:205], v[20:21]\nv_rcp_f32 v19, v19\nv_pk_fma_f32 v[204:205], v[208:209], v[16:17], v[204:205]\n"
      "v_pk_mul_f32 v[32:33], v[204:205], v[18:19]\nv_pk_mul_f32 v[52:53], v[220:221], v[220:221]\nv_pk_mul_f32 v[54:55], v[222:223], v[222:223]\nv_exp_f32 v48, v48 clamp\n"
      "v_pk_mul_f32 v[56:57], v[224:225], v[224:225]\nv_pk_mul_f32 v[58:59], v[226:227], v[226:227]\nv_exp_f32 v49, v49 clamp\n"
      "v_pk_fma_f32 v[52:53], v[52:53], s[58:59], s[58:59] op_sel:[0,0,1] op_sel_hi:[1,0,1]\nv_pk_fma_f32 v[54:55], v[54:55], s[58:59], s[58:59] op_sel:[0,0,1] op_sel_hi:[1,0,1]\n"
      "v_pk_fma_f32 v[56:57], v[56:57], s[58:59], s[58:59] op_sel:[0,0,1] op_sel_hi:[1,0,1]\nv_exp_f32 v50, v50 clamp\n"
      "v_pk_fma_f32 v[58:59], v[58:59], s[58:59], s[58:59] op_sel:[0,0,1] op_sel_hi:[1,0,1]\nv_pk_fma_f32 v[52:53], v[52:53], v[220:221], s[60:61] op_sel_hi:[1,1,0]\n"
      "v_pk_fma_f32 v[54:55], v[54:55], v[222:223], s[60:61] op_sel_hi:[1,1,0]\nv_exp_f32 v51, v51 clamp\nv_pk_fma_f32 v[56:57], v[56:57], v[224:225], s[60:61] op_sel_hi:[1,1,0]\n"
      "v_pk_fma_f32 v[58:59], v[58:59], v[226:227], s[60:61] op_sel_hi:[1,1,0]\nv_exp_f32 v52, v52 clamp\nv_pk_fma_f32 v[46:47], v[46:47], v[10:11], v[66:67]\n"
      "v_pk_fma_f32 v[44:45], v[44:45], v[8:9], v[64:65]\nv_pk_fma_f32 v[48:49], v[48:49], v[12:13], v[68:69]\nv_exp_f32 v53, v53 clamp\nv_pk_fma_f32 v[50:51], v[50:51], v[14:15], v[70:71]\n"
      "v_pk_mul_f32 v[212:213], v[212:213], v[46:47]\nv_pk_mul_f32 v[216:217], v[216:217], v[50:51]\nv_exp_f32 v54, v54 clamp\nv_pk_fma_f32 v[212:213], v[214:215], v[44:45], v[212:213]\n"
      "v_pk_fma_f32 v[216:217], v[218:219], v[48:49], v[216:217]\nv_pk_mul_f32 v[44:45], v[44:45], v[46:47]\nv_exp_f32 v55, v55 clamp\nv_pk_mul_f32 v[48:49], v[48:49], v[50:51]\n"
      "v_pk_mul_f32 v[46:47], v[44:45], v[48:49]\nv_rcp_f32 v46, v46\nv_pk_mul_f32 v[212:213], v[212:213], v[48:49]\nv_rcp_f32 v47, v47\nv_pk_fma_f32 v[212:213], v[216:217], v[44:45], v[212:213]\n"
      "v_pk_fma_f32 v[32:33], v[212:213], v[46:47], v[32:33]\nv_add_f32 v39, v32, v33\nv_pk_mul_f32 v[16:17], v[228:229], v[228:229]\nv_pk_mul_f32 v[18:19], v[230:231], v[230:231]\n"
      "v_exp_f32 v56, v56 clamp\nv_pk_mul_f32 v[20:21], v[232:233], v[232:233]\nv_pk_mul_f32 v[22:23], v[234:235], v[234:235]\nv_exp_f32 v57, v57 clamp\n"
      "v_pk_fma_f32 v[16:17], v[16:17], s[58:59], s[58:59] op_sel:[0,0,1] op_sel_hi:[1,0,1]\nv_pk_fma_f32 v[18:19], v[18:19], s[58:59], s[58:59] op_sel:[0,0,1] op_sel_hi:[1,0,1]\n"
      "v_pk_fma_f32 v[20:21], v[20:21], s[58:59], s[58:59] op_sel:[0,0,1] op_sel_hi:[1,0,1]\nv_exp_f32 v58, v58 clamp\n"
      "v_pk_fma_f32 v[22:23], v[22:23], s[58:59], s[58:59] op_sel:[0,0,1] op_sel_hi:[1,0,1]\nv_pk_fma_f32 v[16:17], v[16:17], v[228:229], s[60:61] op_sel_hi:[1,1,0]\n"
      "v_pk_fma_f32 v[18:19], v[18:19], v[230:231], s[60:61] op_sel_hi:[1,1,0]\nv_exp_f32 v59, v59 clamp\nv_pk_fma_f32 v[20:21], v[20:21], v[232:233], s[60:61] op_sel_hi:[1,1,0]\n"
      "v_pk_fma_f32 v[22:23], v[22:23], v[234:235], s[60:61] op_sel_hi:[1,1,0]\nv_exp_f32 v16, v16 clamp\nv_pk_fma_f32 v[54:55], v[54:55], v[2:3], v[26:27]\nv_exp_f32 v17, v17 clamp\n"
      "v_pk_fma_f32 v[52:53], v[52:53], v[0:1], v[24:25]\nv_pk_fma_f32 v[56:57], v[56:57], v[4:5], v[28:29]\nv_exp_f32 v18, v18 clamp\nv_pk_fma_f32 v[58:59], v[58:59], v[6:7], v[30:31]\n"
      "v_pk_mul_f32 v[220:221], v[220:221], v[54:55]\nv_exp_f32 v19, v19 clamp\nv_pk_mul_f32 v[224:225], v[224:225], v[58:59]\nv_pk_fma_f32 v[220:221], v[222:223], v[52:53], v[220:221]\n"
      "v_exp_f32 v20, v20 clamp\nv_pk_fma_f32 v[224:225], v[226:227], v[56:57], v[224:225]\nv_exp_f32 v21, v21 clamp\nv_pk_mul_f32 v[52:53], v[52:53], v[54:55]\n"
      "v_pk_mul_f32 v[56:57], v[56:57], v[58:59]\nv_exp_f32 v22, v22 clamp\nv_pk_mul_f32 v[54:55], v[52:53], v[56:57]\nv_rcp_f32 v54, v54\nv_pk_mul_f32 v[220:221], v[220:221], v[56:57]\n"
      "v_exp_f32 v23, v23 clamp\nv_rcp_f32 v55, v55\nv_pk_fma_f32 v[220:221], v[224:225], v[52:53], v[220:221]\nv_pk_mul_f32 v[32:33], v[220:221], v[54:55]\n"
      "v_pk_fma_f32 v[18:19], v[18:19], v[10:11], v[66:67]\nv_pk_fma_f32 v[16:17], v[16:17], v[8:9], v[64:65]\nv_pk_fma_f32 v[20:21], v[20:21], v[12:13], v[68:69]\n"
      "v_pk_fma_f32 v[22:23], v[22:23], v[14:15], v[70:71]\nv_pk_mul_f32 v[228:229], v[228:229], v[18:19]\nv_pk_mul_f32 v[232:233], v[232:233], v[22:23]\n"
      "v_pk_fma_f32 v[228:229], v[230:231], v[16:17], v[228:229]\nv_pk_fma_f32 v[232:233], v[234:235], v[20:21], v[232:233]\nv_pk_mul_f32 v[16:17], v[16:17], v[18:19]\n"
      "v_pk_mul_f32 v[20:21], v[20:21], v[22:23]\nv_pk_mul_f32 v[18:19], v[16:17], v[20:21]\nv_rcp_f32 v18, v18\nv_pk_mul_f32 v[228:229], v[228:229], v[20:21]\nv_rcp_f32 v19, v19\n"
      "v_pk_fma_f32 v[228:229], v[232:233], v[16:17], v[228:229]\nv_pk_fma_f32 v[32:33], v[228:229], v[18:19], v[32:33]\nv_add_f32 v40, v32, v33\ns_barrier\ns_branch .Lge\n"
      ".Lg13:\ns_waitcnt vmcnt(4)\ns_barrier\nds_read_b64_tr_b16 v[64:65], v104 offset:45056\nds_read_b64_tr_b16 v[66:67], v104 offset:46080\nds_read_b64_tr_b16 v[68:69], v104 offset:53248\n"
      "ds_read_b64_tr_b16 v[70:71], v104 offset:54272\nds_read_b64_tr_b16 v[72:73], v105 offset:45056\nds_read_b64_tr_b16 v[74:75], v105 offset:46080\n"
      "ds_read_b64_tr_b16 v[76:77], v105 offset:53248\nds_read_b64_tr_b16 v[78:79], v105 offset:54272\nds_read_b64_tr_b16 v[80:81], v106 offset:45056\n"
      "ds_read_b64_tr_b16 v[82:83], v106 offset:46080\nds_read_b64_tr_b16 v[84:85], v106 offset:53248\nds_read_b64_tr_b16 v[86:87], v106 offset:54272\n"
      "ds_read_b64_tr_b16 v[88:89], v107 offset:45056\nds_read_b64_tr_b16 v[90:91], v107 offset:46080\nds_read_b64_tr_b16 v[92:93], v107 offset:53248\n"
      "ds_read_b64_tr_b16 v[94:95], v107 offset:54272\nds_read_b128 v[0:3], v100 offset:45056\nds_read_b128 v[4:7], v101 offset:45056\nds_read_b128 v[8:11], v120 offset:45056\n"
      "ds_read_b128 v[12:15], v121 offset:45056\nds_read_b128 v[16:19], v96 offset:45056\nds_read_b128 v[20:23], v97 offset:45056\ns_waitcnt lgkmcnt(2)\n.p2align 3\n"
      ".Lgl13:\nv_mfma_f32_16x16x32_bf16 v[124:127], v[64:67], v[0:3], v[124:127]\nds_read_b128 v[24:27], v96 offset:47104\nv_mfma_f32_16x16x32_bf16 v[124:127], v[68:71], v[4:7], v[124:127]\n"
      "ds_read_b128 v[28:31], v97 offset:47104\nv_mfma_f32_16x16x32_bf16 v[128:131], v[76:79], v[4:7], v[128:131]\nv_mfma_f32_16x16x32_bf16 v[128:131], v[72:75], v[0:3], v[128:131]\n"
      "v_mfma_f32_16x16x32_bf16 v[152:155], v[88:91], v[8:11], v[152:155]\nds_read_b128 v[0:3], v96 offset:49152\nv_mfma_f32_16x16x32_bf16 v[152:155], v[92:95], v[12:15], v[152:155]\n"
      "ds_read_b128 v[4:7], v97 offset:49152\nv_mfma_f32_16x16x32_bf16 v[148:151], v[84:87], v[12:15], v[148:151]\nv_mfma_f32_16x16x32_bf16 v[148:151], v[80:83], v[8:11], v[148:151]\n"
      "v_mfma_f32_16x16x32_bf16 v[144:147], v[72:75], v[8:11], v[144:147]\nv_mfma_f32_16x16x32_bf16 v[144:147], v[76:79], v[12:15], v[144:147]\n"
      "v_mfma_f32_16x16x32_bf16 v[140:143], v[68:71], v[12:15], v[140:143]\ns_waitcnt lgkmcnt(4)\nv_mfma_f32_16x16x32_bf16 v[140:143], v[64:67], v[8:11], v[140:143]\n"
      "v_mfma_f32_16x16x32_bf16 v[156:159], v[64:67], v[16:19], v[156:159]\nds_read_b128 v[8:11], v96 offset:51200\nv_mfma_f32_16x16x32_bf16 v[156:159], v[68:71], v[20:23], v[156:159]\n"
      "ds_read_b128 v[12:15], v97 offset:51200\nv_mfma_f32_16x16x32_bf16 v[160:163], v[76:79], v[20:23], v[160:163]\nv_mfma_f32_16x16x32_bf16 v[160:163], v[72:75], v[16:19], v[160:163]\n"
      "v_mfma_f32_16x16x32_bf16 v[164:167], v[80:83], v[16:19], v[164:167]\nv_mfma_f32_16x16x32_bf16 v[164:167], v[84:87], v[20:23], v[164:167]\n"
      "v_mfma_f32_16x16x32_bf16 v[168:171], v[92:95], v[20:23], v[168:171]\ns_waitcnt lgkmcnt(2)\nv_mfma_f32_16x16x32_bf16 v[168:171], v[88:91], v[16:19], v[168:171]\n"
      "v_mfma_f32_16x16x32_bf16 v[184:187], v[88:91], v[24:27], v[184:187]\nds_read_b128 v[16:19], v96 offset:53248\nv_mfma_f32_16x16x32_bf16 v[184:187], v[92:95], v[28:31], v[184:187]\n"
      "ds_read_b128 v[20:23], v97 offset:53248\nv_mfma_f32_16x16x32_bf16 v[180:183], v[84:87], v[28:31], v[180:183]\nv_mfma_f32_16x16x32_bf16 v[180:183], v[80:83], v[24:27], v[180:183]\n"
      "v_mfma_f32_16x16x32_bf16 v[176:179], v[72:75], v[24:27], v[176:179]\nv_mfma_f32_16x16x32_bf16 v[176:179], v[76:79], v[28:31], v[176:179]\n"
      "v_mfma_f32_16x16x32_bf16 v[172:175], v[68:71], v[28:31], v[172:175]\ns_waitcnt vmcnt(0) lgkmcnt(0)\ns_barrier\nv_mfma_f32_16x16x32_bf16 v[172:175], v[64:67], v[24:27], v[172:175]\n"
      "ds_read_b64_tr_b16 v[32:33], v108 offset:0\nv_mfma_f32_16x16x32_bf16 v[188:191], v[64:67], v[0:3], v[188:191]\nds_read_b64_tr_b16 v[34:35], v108 offset:1024\n"
      "v_mfma_f32_16x16x32_bf16 v[188:191], v[68:71], v[4:7], v[188:191]\nds_read_b64_tr_b16 v[36:37], v108 offset:8192\nv_mfma_f32_16x16x32_bf16 v[192:195], v[76:79], v[4:7], v[192:195]\n"
      "ds_read_b64_tr_b16 v[38:39], v108 offset:9216\nv_mfma_f32_16x16x32_bf16 v[192:195], v[72:75], v[0:3], v[192:195]\nds_read_b64_tr_b16 v[40:41], v109 offset:0\n"
      "v_mfma_f32_16x16x32_bf16 v[196:199], v[80:83], v[0:3], v[196:199]\nds_read_b64_tr_b16 v[42:43], v109 offset:1024\nv_mfma_f32_16x16x32_bf16 v[196:199], v[84:87], v[4:7], v[196:199]\n"
      "ds_read_b64_tr_b16 v[44:45], v109 offset:8192\nv_mfma_f32_16x16x32_bf16 v[200:203], v[92:95], v[4:7], v[200:203]\nds_read_b64_tr_b16 v[46:47], v109 offset:9216\n"
      "v_mfma_f32_16x16x32_bf16 v[200:203], v[88:91], v[0:3], v[200:203]\nds_read_b64_tr_b16 v[48:49], v110 offset:0\nv_mfma_f32_16x16x32_bf16 v[216:219], v[88:91], v[8:11], v[216:219]\n"
      "ds_read_b128 v[0:3], v102 offset:0\nv_mfma_f32_16x16x32_bf16 v[216:219], v[92:95], v[12:15], v[216:219]\nds_read_b128 v[4:7], v103 offset:0\n"
      "v_mfma_f32_16x16x32_bf16 v[212:215], v[84:87], v[12:15], v[212:215]\nds_read_b64_tr_b16 v[50:51], v110 offset:1024\nv_mfma_f32_16x16x32_bf16 v[212:215], v[80:83], v[8:11], v[212:215]\n"
      "ds_read_b64_tr_b16 v[52:53], v110 offset:8192\nv_mfma_f32_16x16x32_bf16 v[208:211], v[72:75], v[8:11], v[208:211]\nds_read_b64_tr_b16 v[54:55], v110 offset:9216\n"
      "v_mfma_f32_16x16x32_bf16 v[208:211], v[76:79], v[12:15], v[208:211]\nds_read_b64_tr_b16 v[56:57], v111 offset:0\nv_mfma_f32_16x16x32_bf16 v[204:207], v[68:71], v[12:15], v[204:207]\n"
      "ds_read_b64_tr_b16 v[58:59], v111 offset:1024\nv_mfma_f32_16x16x32_bf16 v[204:207], v[64:67], v[8:11], v[204:207]\nds_read_b64_tr_b16 v[60:61], v111 offset:8192\n"
      "v_mfma_f32_16x16x32_bf16 v[220:223], v[64:67], v[16:19], v[220:223]\nds_read_b128 v[8:11], v122 offset:0\nv_mfma_f32_16x16x32_bf16 v[220:223], v[68:71], v[20:23], v[220:223]\n"
      "ds_read_b128 v[12:15], v123 offset:0\nv_mfma_f32_16x16x32_bf16 v[224:227], v[76:79], v[20:23], v[224:227]\nds_read_b64_tr_b16 v[62:63], v111 offset:9216\n"
      "v_mfma_f32_16x16x32_bf16 v[224:227], v[72:75], v[16:19], v[224:227]\nv_mfma_f32_16x16x32_bf16 v[228:231], v[80:83], v[16:19], v[228:231]\n"
      "v_mfma_f32_16x16x32_bf16 v[228:231], v[84:87], v[20:23], v[228:231]\nv_mfma_f32_16x16x32_bf16 v[232:235], v[92:95], v[20:23], v[232:235]\ns_waitcnt lgkmcnt(0)\n"
      "v_mfma_f32_16x16x32_bf16 v[232:235], v[88:91], v[16:19], v[232:235]\nds_read_b128 v[16:19], v98 offset:0\nds_read_b128 v[20:23], v99 offset:0\n"
      "v_mfma_f32_16x16x32_bf16 v[124:127], v[32:35], v[0:3], v[124:127]\nds_read_b128 v[24:27], v98 offset:2048\nv_mfma_f32_16x16x32_bf16 v[124:127], v[36:39], v[4:7], v[124:127]\n"
      "ds_read_b128 v[28:31], v99 offset:2048\nv_mfma_f32_16x16x32_bf16 v[128:131], v[44:47], v[4:7], v[128:131]\nv_mfma_f32_16x16x32_bf16 v[128:131], v[40:43], v[0:3], v[128:131]\n"
      "v_mfma_f32_16x16x32_bf16 v[152:155], v[56:59], v[8:11], v[152:155]\nds_read_b128 v[0:3], v98 offset:4096\nv_mfma_f32_16x16x32_bf16 v[152:155], v[60:63], v[12:15], v[152:155]\n"
      "ds_read_b128 v[4:7], v99 offset:4096\nv_mfma_f32_16x16x32_bf16 v[148:151], v[52:55], v[12:15], v[148:151]\nv_mfma_f32_16x16x32_bf16 v[148:151], v[48:51], v[8:11], v[148:151]\n"
      "v_mfma_f32_16x16x32_bf16 v[144:147], v[40:43], v[8:11], v[144:147]\nv_mfma_f32_16x16x32_bf16 v[144:147], v[44:47], v[12:15], v[144:147]\n"
      "v_mfma_f32_16x16x32_bf16 v[140:143], v[36:39], v[12:15], v[140:143]\ns_waitcnt lgkmcnt(4)\nv_mfma_f32_16x16x32_bf16 v[140:143], v[32:35], v[8:11], v[140:143]\n"
      "v_mfma_f32_16x16x32_bf16 v[156:159], v[32:35], v[16:19], v[156:159]\nds_read_b128 v[8:11], v98 offset:6144\nv_mfma_f32_16x16x32_bf16 v[156:159], v[36:39], v[20:23], v[156:159]\n"
      "ds_read_b128 v[12:15], v99 offset:6144\nv_mfma_f32_16x16x32_bf16 v[160:163], v[44:47], v[20:23], v[160:163]\nv_mfma_f32_16x16x32_bf16 v[160:163], v[40:43], v[16:19], v[160:163]\n"
      "v_mfma_f32_16x16x32_bf16 v[164:167], v[48:51], v[16:19], v[164:167]\nv_mfma_f32_16x16x32_bf16 v[164:167], v[52:55], v[20:23], v[164:167]\n"
      "v_mfma_f32_16x16x32_bf16 v[168:171], v[60:63], v[20:23], v[168:171]\ns_waitcnt lgkmcnt(2)\nv_mfma_f32_16x16x32_bf16 v[168:171], v[56:59], v[16:19], v[168:171]\n"
      "v_mfma_f32_16x16x32_bf16 v[184:187], v[56:59], v[24:27], v[184:187]\nds_read_b128 v[16:19], v98 offset:8192\nv_mfma_f32_16x16x32_bf16 v[184:187], v[60:63], v[28:31], v[184:187]\n"
      "ds_read_b128 v[20:23], v99 offset:8192\nv_mfma_f32_16x16x32_bf16 v[180:183], v[52:55], v[28:31], v[180:183]\nv_mfma_f32_16x16x32_bf16 v[180:183], v[48:51], v[24:27], v[180:183]\n"
      "v_mfma_f32_16x16x32_bf16 v[176:179], v[40:43], v[24:27], v[176:179]\nv_mfma_f32_16x16x32_bf16 v[176:179], v[44:47], v[28:31], v[176:179]\n"
      "v_mfma_f32_16x16x32_bf16 v[172:175], v[36:39], v[28:31], v[172:175]\ns_waitcnt vmcnt(0) lgkmcnt(0)\ns_barrier\nv_mfma_f32_16x16x32_bf16 v[172:175], v[32:35], v[24:27], v[172:175]\n"
      "ds_read_b64_tr_b16 v[64:65], v104 offset:0\nv_mfma_f32_16x16x32_bf16 v[188:191], v[32:35], v[0:3], v[188:191]\nds_read_b64_tr_b16 v[66:67], v104 offset:1024\n"
      "v_mfma_f32_16x16x32_bf16 v[188:191], v[36:39], v[4:7], v[188:191]\nds_read_b64_tr_b16 v[68:69], v104 offset:8192\nv_mfma_f32_16x16x32_bf16 v[192:195], v[44:47], v[4:7], v[192:195]\n"
      "ds_read_b64_tr_b16 v[70:71], v104 offset:9216\nv_mfma_f32_16x16x32_bf16 v[192:195], v[40:43], v[0:3], v[192:195]\nds_read_b64_tr_b16 v[72:73], v105 offset:0\n"
      "v_mfma_f32_16x16x32_bf16 v[196:199], v[48:51], v[0:3], v[196:199]\nds_read_b64_tr_b16 v[74:75], v105 offset:1024\nv_mfma_f32_16x16x32_bf16 v[196:199], v[52:55], v[4:7], v[196:199]\n"
      "ds_read_b64_tr_b16 v[76:77], v105 offset:8192\nv_mfma_f32_16x16x32_bf16 v[200:203], v[60:63], v[4:7], v[200:203]\nds_read_b64_tr_b16 v[78:79], v105 offset:9216\n"
      "v_mfma_f32_16x16x32_bf16 v[200:203], v[56:59], v[0:3], v[200:203]\nds_read_b64_tr_b16 v[80:81], v106 offset:0\nv_mfma_f32_16x16x32_bf16 v[216:219], v[56:59], v[8:11], v[216:219]\n"
      "ds_read_b128 v[0:3], v100 offset:0\nv_mfma_f32_16x16x32_bf16 v[216:219], v[60:63], v[12:15], v[216:219]\nds_read_b128 v[4:7], v101 offset:0\n"
      "v_mfma_f32_16x16x32_bf16 v[212:215], v[52:55], v[12:15], v[212:215]\nds_read_b64_tr_b16 v[82:83], v106 offset:1024\nv_mfma_f32_16x16x32_bf16 v[212:215], v[48:51], v[8:11], v[212:215]\n"
      "ds_read_b64_tr_b16 v[84:85], v106 offset:8192\nv_mfma_f32_16x16x32_bf16 v[208:211], v[40:43], v[8:11], v[208:211]\nds_read_b64_tr_b16 v[86:87], v106 offset:9216\n"
      "v_mfma_f32_16x16x32_bf16 v[208:211], v[44:47], v[12:15], v[208:211]\nds_read_b64_tr_b16 v[88:89], v107 offset:0\nv_mfma_f32_16x16x32_bf16 v[204:207], v[36:39], v[12:15], v[204:207]\n"
      "ds_read_b64_tr_b16 v[90:91], v107 offset:1024\nv_mfma_f32_16x16x32_bf16 v[204:207], v[32:35], v[8:11], v[204:207]\nds_read_b64_tr_b16 v[92:93], v107 offset:8192\n"
      "v_mfma_f32_16x16x32_bf16 v[220:223], v[32:35], v[16:19], v[220:223]\nds_read_b128 v[8:11], v120 offset:0\nv_mfma_f32_16x16x32_bf16 v[220:223], v[36:39], v[20:23], v[220:223]\n"
      "ds_read_b128 v[12:15], v121 offset:0\nv_mfma_f32_16x16x32_bf16 v[224:227], v[44:47], v[20:23], v[224:227]\nds_read_b64_tr_b16 v[94:95], v107 offset:9216\n"
      "v_mfma_f32_16x16x32_bf16 v[224:227], v[40:43], v[16:19], v[224:227]\nv_mfma_f32_16x16x32_bf16 v[228:231], v[48:51], v[16:19], v[228:231]\n"
      "v_mfma_f32_16x16x32_bf16 v[228:231], v[52:55], v[20:23], v[228:231]\nv_mfma_f32_16x16x32_bf16 v[232:235], v[60:63], v[20:23], v[232:235]\ns_waitcnt lgkmcnt(0)\n"
      "v_mfma_f32_16x16x32_bf16 v[232:235], v[56:59], v[16:19], v[232:235]\nds_read_b128 v[16:19], v96 offset:0\nds_read_b128 v[20:23], v97 offset:0\n"
      "v_mfma_f32_16x16x32_bf16 v[124:127], v[64:67], v[0:3], v[124:127]\nds_read_b128 v[24:27], v96 offset:2048\nv_mfma_f32_16x16x32_bf16 v[124:127], v[68:71], v[4:7], v[124:127]\n"
      "ds_read_b128 v[28:31], v97 offset:2048\nv_mfma_f32_16x16x32_bf16 v[128:131], v[76:79], v[4:7], v[128:131]\nv_mfma_f32_16x16x32_bf16 v[128:131], v[72:75], v[0:3], v[128:131]\n"
      "v_mfma_f32_16x16x32_bf16 v[152:155], v[88:91], v[8:11], v[152:155]\nds_read_b128 v[0:3], v96 offset:4096\nv_mfma_f32_16x16x32_bf16 v[152:155], v[92:95], v[12:15], v[152:155]\n"
      "ds_read_b128 v[4:7], v97 offset:4096\nv_mfma_f32_16x16x32_bf16 v[148:151], v[84:87], v[12:15], v[148:151]\nv_mfma_f32_16x16x32_bf16 v[148:151], v[80:83], v[8:11], v[148:151]\n"
      "v_mfma_f32_16x16x32_bf16 v[144:147], v[72:75], v[8:11], v[144:147]\nv_mfma_f32_16x16x32_bf16 v[144:147], v[76:79], v[12:15], v[144:147]\n"
      "v_mfma_f32_16x16x32_bf16 v[140:143], v[68:71], v[12:15], v[140:143]\ns_waitcnt lgkmcnt(4)\nv_mfma_f32_16x16x32_bf16 v[140:143], v[64:67], v[8:11], v[140:143]\n"
      "v_mfma_f32_16x16x32_bf16 v[156:159], v[64:67], v[16:19], v[156:159]\nds_read_b128 v[8:11], v96 offset:6144\nv_mfma_f32_16x16x32_bf16 v[156:159], v[68:71], v[20:23], v[156:159]\n"
      "ds_read_b128 v[12:15], v97 offset:6144\nv_mfma_f32_16x16x32_bf16 v[160:163], v[76:79], v[20:23], v[160:163]\nv_mfma_f32_16x16x32_bf16 v[160:163], v[72:75], v[16:19], v[160:163]\n"
      "v_mfma_f32_16x16x32_bf16 v[164:167], v[80:83], v[16:19], v[164:167]\nv_mfma_f32_16x16x32_bf16 v[164:167], v[84:87], v[20:23], v[164:167]\n"
      "v_mfma_f32_16x16x32_bf16 v[168:171], v[92:95], v[20:23], v[168:171]\ns_waitcnt lgkmcnt(2)\nv_mfma_f32_16x16x32_bf16 v[168:171], v[88:91], v[16:19], v[168:171]\n"
      "v_mfma_f32_16x16x32_bf16 v[184:187], v[88:91], v[24:27], v[184:187]\nds_read_b128 v[16:19], v96 offset:8192\nv_mfma_f32_16x16x32_bf16 v[184:187], v[92:95], v[28:31], v[184:187]\n"
      "ds_read_b128 v[20:23], v97 offset:8192\nv_mfma_f32_16x16x32_bf16 v[180:183], v[84:87], v[28:31], v[180:183]\nv_mfma_f32_16x16x32_bf16 v[180:183], v[80:83], v[24:27], v[180:183]\n"
      "v_mfma_f32_16x16x32_bf16 v[176:179], v[72:75], v[24:27], v[176:179]\nv_mfma_f32_16x16x32_bf16 v[176:179], v[76:79], v[28:31], v[176:179]\n"
      "v_mfma_f32_16x16x32_bf16 v[172:175], v[68:71], v[28:31], v[172:175]\ns_waitcnt vmcnt(0) lgkmcnt(0)\ns_barrier\nv_mfma_f32_16x16x32_bf16 v[172:175], v[64:67], v[24:27], v[172:175]\n"
      "ds_read_b64_tr_b16 v[32:33], v104 offset:45056\nv_mfma_f32_16x16x32_bf16 v[188:191], v[64:67], v[0:3], v[188:191]\nds_read_b64_tr_b16 v[34:35], v104 offset:46080\n"
      "v_mfma_f32_16x16x32_bf16 v[188:191], v[68:71], v[4:7], v[188:191]\nds_read_b64_tr_b16 v[36:37], v104 offset:53248\nv_mfma_f32_16x16x32_bf16 v[192:195], v[76:79], v[4:7], v[192:195]\n"
      "ds_read_b64_tr_b16 v[38:39], v104 offset:54272\nv_mfma_f32_16x16x32_bf16 v[192:195], v[72:75], v[0:3], v[192:195]\nds_read_b64_tr_b16 v[40:41], v105 offset:45056\n"
      "v_mfma_f32_16x16x32_bf16 v[196:199], v[80:83], v[0:3], v[196:199]\nds_read_b64_tr_b16 v[42:43], v105 offset:46080\nv_mfma_f32_16x16x32_bf16 v[196:199], v[84:87], v[4:7], v[196:199]\n"
      "ds_read_b64_tr_b16 v[44:45], v105 offset:53248\nv_mfma_f32_16x16x32_bf16 v[200:203], v[92:95], v[4:7], v[200:203]\nds_read_b64_tr_b16 v[46:47], v105 offset:54272\n"
      "v_mfma_f32_16x16x32_bf16 v[200:203], v[88:91], v[0:3], v[200:203]\nds_read_b64_tr_b16 v[48:49], v106 offset:45056\nv_mfma_f32_16x16x32_bf16 v[216:219], v[88:91], v[8:11], v[216:219]\n"
      "ds_read_b128 v[0:3], v100 offset:45056\nv_mfma_f32_16x16x32_bf16 v[216:219], v[92:95], v[12:15], v[216:219]\nds_read_b128 v[4:7], v101 offset:45056\n"
      "v_mfma_f32_16x16x32_bf16 v[212:215], v[84:87], v[12:15], v[212:215]\nds_read_b64_tr_b16 v[50:51], v106 offset:46080\nv_mfma_f32_16x16x32_bf16 v[212:215], v[80:83], v[8:11], v[212:215]\n"
      "ds_read_b64_tr_b16 v[52:53], v106 offset:53248\nv_mfma_f32_16x16x32_bf16 v[208:211], v[72:75], v[8:11], v[208:211]\nds_read_b64_tr_b16 v[54:55], v106 offset:54272\n"
      "v_mfma_f32_16x16x32_bf16 v[208:211], v[76:79], v[12:15], v[208:211]\nds_read_b64_tr_b16 v[56:57], v107 offset:45056\nv_mfma_f32_16x16x32_bf16 v[204:207], v[68:71], v[12:15], v[204:207]\n"
      "ds_read_b64_tr_b16 v[58:59], v107 offset:46080\nv_mfma_f32_16x16x32_bf16 v[204:207], v[64:67], v[8:11], v[204:207]\nds_read_b64_tr_b16 v[60:61], v107 offset:53248\n"
      "v_mfma_f32_16x16x32_bf16 v[220:223], v[64:67], v[16:19], v[220:223]\nds_read_b128 v[8:11], v120 offset:45056\nv_mfma_f32_16x16x32_bf16 v[220:223], v[68:71], v[20:23], v[220:223]\n"
      "ds_read_b128 v[12:15], v121 offset:45056\nv_mfma_f32_16x16x32_bf16 v[224:227], v[76:79], v[20:23], v[224:227]\nds_read_b64_tr_b16 v[62:63], v107 offset:54272\n"
      "v_mfma_f32_16x16x32_bf16 v[224:227], v[72:75], v[16:19], v[224:227]\nv_mfma_f32_16x16x32_bf16 v[228:231], v[80:83], v[16:19], v[228:231]\n"
      "v_mfma_f32_16x16x32_bf16 v[228:231], v[84:87], v[20:23], v[228:231]\nv_mfma_f32_16x16x32_bf16 v[232:235], v[92:95], v[20:23], v[232:235]\ns_waitcnt lgkmcnt(0)\n"
      "v_mfma_f32_16x16x32_bf16 v[232:235], v[88:91], v[16:19], v[232:235]\nds_read_b128 v[16:19], v96 offset:45056\nds_read_b128 v[20:23], v97 offset:45056\n"
      "v_mfma_f32_16x16x32_bf16 v[124:127], v[32:35], v[0:3], v[124:127]\nds_read_b128 v[24:27], v96 offset:47104\nv_mfma_f32_16x16x32_bf16 v[124:127], v[36:39], v[4:7], v[124:127]\n"
      "ds_read_b128 v[28:31], v97 offset:47104\nv_mfma_f32_16x16x32_bf16 v[128:131], v[44:47], v[4:7], v[128:131]\nv_mfma_f32_16x16x32_bf16 v[128:131], v[40:43], v[0:3], v[128:131]\n"
      "v_mfma_f32_16x16x32_bf16 v[152:155], v[56:59], v[8:11], v[152:155]\nds_read_b128 v[0:3], v96 offset:49152\nv_mfma_f32_16x16x32_bf16 v[152:155], v[60:63], v[12:15], v[152:155]\n"
      "ds_read_b128 v[4:7], v97 offset:49152\nv_mfma_f32_16x16x32_bf16 v[148:151], v[52:55], v[12:15], v[148:151]\nv_mfma_f32_16x16x32_bf16 v[148:151], v[48:51], v[8:11], v[148:151]\n"
      "v_mfma_f32_16x16x32_bf16 v[144:147], v[40:43], v[8:11], v[144:147]\nv_mfma_f32_16x16x32_bf16 v[144:147], v[44:47], v[12:15], v[144:147]\n"
      "v_mfma_f32_16x16x32_bf16 v[140:143], v[36:39], v[12:15], v[140:143]\ns_waitcnt lgkmcnt(4)\nv_mfma_f32_16x16x32_bf16 v[140:143], v[32:35], v[8:11], v[140:143]\n"
      "v_mfma_f32_16x16x32_bf16 v[156:159], v[32:35], v[16:19], v[156:159]\nds_read_b128 v[8:11], v96 offset:51200\nv_mfma_f32_16x16x32_bf16 v[156:159], v[36:39], v[20:23], v[156:159]\n"
      "ds_read_b128 v[12:15], v97 offset:51200\nv_mfma_f32_16x16x32_bf16 v[160:163], v[44:47], v[20:23], v[160:163]\nv_mfma_f32_16x16x32_bf16 v[160:163], v[40:43], v[16:19], v[160:163]\n"
      "v_mfma_f32_16x16x32_bf16 v[164:167], v[48:51], v[16:19], v[164:167]\nv_mfma_f32_16x16x32_bf16 v[164:167], v[52:55], v[20:23], v[164:167]\n"
      "v_mfma_f32_16x16x32_bf16 v[168:171], v[60:63], v[20:23], v[168:171]\ns_waitcnt lgkmcnt(2)\nv_mfma_f32_16x16x32_bf16 v[168:171], v[56:59], v[16:19], v[168:171]\n"
      "v_mfma_f32_16x16x32_bf16 v[184:187], v[56:59], v[24:27], v[184:187]\nds_read_b128 v[16:19], v96 offset:53248\nv_mfma_f32_16x16x32_bf16 v[184:187], v[60:63], v[28:31], v[184:187]\n"
      "ds_read_b128 v[20:23], v97 offset:53248\nv_mfma_f32_16x16x32_bf16 v[180:183], v[52:55], v[28:31], v[180:183]\nv_mfma_f32_16x16x32_bf16 v[180:183], v[48:51], v[24:27], v[180:183]\n"
      "v_mfma_f32_16x16x32_bf16 v[176:179], v[40:43], v[24:27], v[176:179]\nv_mfma_f32_16x16x32_bf16 v[176:179], v[44:47], v[28:31], v[176:179]\n"
      "v_mfma_f32_16x16x32_bf16 v[172:175], v[36:39], v[28:31], v[172:175]\ns_waitcnt vmcnt(0) lgkmcnt(0)\ns_barrier\nv_mfma_f32_16x16x32_bf16 v[172:175], v[32:35], v[24:27], v[172:175]\n"
      "ds_read_b64_tr_b16 v[64:65], v108 offset:0\nv_mfma_f32_16x16x32_bf16 v[188:191], v[32:35], v[0:3], v[188:191]\nds_read_b64_tr_b16 v[66:67], v108 offset:1024\n"
      "v_mfma_f32_16x16x32_bf16 v[188:191], v[36:39], v[4:7], v[188:191]\nds_read_b64_tr_b16 v[68:69], v108 offset:8192\nv_mfma_f32_16x16x32_bf16 v[192:195], v[44:47], v[4:7], v[192:195]\n"
      "ds_read_b64_tr_b16 v[70:71], v108 offset:9216\nv_mfma_f32_16x16x32_bf16 v[192:195], v[40:43], v[0:3], v[192:195]\nds_read_b64_tr_b16 v[72:73], v109 offset:0\n"
      "v_mfma_f32_16x16x32_bf16 v[196:199], v[48:51], v[0:3], v[196:199]\nds_read_b64_tr_b16 v[74:75], v109 offset:1024\nv_mfma_f32_16x16x32_bf16 v[196:199], v[52:55], v[4:7], v[196:199]\n"
      "ds_read_b64_tr_b16 v[76:77], v109 offset:8192\nv_mfma_f32_16x16x32_bf16 v[200:203], v[60:63], v[4:7], v[200:203]\nds_read_b64_tr_b16 v[78:79], v109 offset:9216\n"
      "v_mfma_f32_16x16x32_bf16 v[200:203], v[56:59], v[0:3], v[200:203]\nds_read_b64_tr_b16 v[80:81], v110 offset:0\nv_mfma_f32_16x16x32_bf16 v[216:219], v[56:59], v[8:11], v[216:219]\n"
      "ds_read_b128 v[0:3], v102 offset:0\nv_mfma_f32_16x16x32_bf16 v[216:219], v[60:63], v[12:15], v[216:219]\nds_read_b128 v[4:7], v103 offset:0\n"
      "v_mfma_f32_16x16x32_bf16 v[212:215], v[52:55], v[12:15], v[212:215]\nds_read_b64_tr_b16 v[82:83], v110 offset:1024\nv_mfma_f32_16x16x32_bf16 v[212:215], v[48:51], v[8:11], v[212:215]\n"
      "ds_read_b64_tr_b16 v[84:85], v110 offset:8192\nv_mfma_f32_16x16x32_bf16 v[208:211], v[40:43], v[8:11], v[208:211]\nds_read_b64_tr_b16 v[86:87], v110 offset:9216\n"
      "v_mfma_f32_16x16x32_bf16 v[208:211], v[44:47], v[12:15], v[208:211]\nds_read_b64_tr_b16 v[88:89], v111 offset:0\nv_mfma_f32_16x16x32_bf16 v[204:207], v[36:39], v[12:15], v[204:207]\n"
      "ds_read_b64_tr_b16 v[90:91], v111 offset:1024\nv_mfma_f32_16x16x32_bf16 v[204:207], v[32:35], v[8:11], v[204:207]\nds_read_b64_tr_b16 v[92:93], v111 offset:8192\n"
      "v_mfma_f32_16x16x32_bf16 v[220:223], v[32:35], v[16:19], v[220:223]\nds_read_b128 v[8:11], v122 offset:0\nv_mfma_f32_16x16x32_bf16 v[220:223], v[36:39], v[20:23], v[220:223]\n"
      "ds_read_b128 v[12:15], v123 offset:0\nv_mfma_f32_16x16x32_bf16 v[224:227], v[44:47], v[20:23], v[224:227]\nds_read_b64_tr_b16 v[94:95], v111 offset:9216\n"
      "v_mfma_f32_16x16x32_bf16 v[224:227], v[40:43], v[16:19], v[224:227]\nv_mfma_f32_16x16x32_bf16 v[228:231], v[48:51], v[16:19], v[228:231]\n"
      "v_mfma_f32_16x16x32_bf16 v[228:231], v[52:55], v[20:23], v[228:231]\nv_mfma_f32_16x16x32_bf16 v[232:235], v[60:63], v[20:23], v[232:235]\ns_waitcnt lgkmcnt(0)\n"
      "v_mfma_f32_16x16x32_bf16 v[232:235], v[56:59], v[16:19], v[232:235]\nds_read_b128 v[16:19], v98 offset:0\nds_read_b128 v[20:23], v99 offset:0\n"
      "v_mfma_f32_16x16x32_bf16 v[124:127], v[64:67], v[0:3], v[124:127]\nds_read_b128 v[24:27], v98 offset:2048\nv_mfma_f32_16x16x32_bf16 v[124:127], v[68:71], v[4:7], v[124:127]\n"
      "ds_read_b128 v[28:31], v99 offset:2048\nv_mfma_f32_16x16x32_bf16 v[128:131], v[76:79], v[4:7], v[128:131]\nv_mfma_f32_16x16x32_bf16 v[128:131], v[72:75], v[0:3], v[128:131]\n"
      "v_mfma_f32_16x16x32_bf16 v[152:155], v[88:91], v[8:11], v[152:155]\nds_read_b128 v[0:3], v98 offset:4096\nv_mfma_f32_16x16x32_bf16 v[152:155], v[92:95], v[12:15], v[152:155]\n"
      "ds_read_b128 v[4:7], v99 offset:4096\nv_mfma_f32_16x16x32_bf16 v[148:151], v[84:87], v[12:15], v[148:151]\nv_mfma_f32_16x16x32_bf16 v[148:151], v[80:83], v[8:11], v[148:151]\n"
      "v_mfma_f32_16x16x32_bf16 v[144:147], v[72:75], v[8:11], v[144:147]\nv_mfma_f32_16x16x32_bf16 v[144:147], v[76:79], v[12:15], v[144:147]\n"
      "v_mfma_f32_16x16x32_bf16 v[140:143], v[68:71], v[12:15], v[140:143]\ns_waitcnt lgkmcnt(4)\nv_mfma_f32_16x16x32_bf16 v[140:143], v[64:67], v[8:11], v[140:143]\n"
      "v_mfma_f32_16x16x32_bf16 v[156:159], v[64:67], v[16:19], v[156:159]\nds_read_b128 v[8:11], v98 offset:6144\nv_mfma_f32_16x16x32_bf16 v[156:159], v[68:71], v[20:23], v[156:159]\n"
      "ds_read_b128 v[12:15], v99 offset:6144\nv_mfma_f32_16x16x32_bf16 v[160:163], v[76:79], v[20:23], v[160:163]\nv_mfma_f32_16x16x32_bf16 v[160:163], v[72:75], v[16:19], v[160:163]\n"
      "v_mfma_f32_16x16x32_bf16 v[164:167], v[80:83], v[16:19], v[164:167]\nv_mfma_f32_16x16x32_bf16 v[164:167], v[84:87], v[20:23], v[164:167]\n"
      "v_mfma_f32_16x16x32_bf16 v[168:171], v[92:95], v[20:23], v[168:171]\ns_waitcnt lgkmcnt(2)\nv_mfma_f32_16x16x32_bf16 v[168:171], v[88:91], v[16:19], v[168:171]\n"
      "v_mfma_f32_16x16x32_bf16 v[184:187], v[88:91], v[24:27], v[184:187]\nds_read_b128 v[16:19], v98 offset:8192\nv_mfma_f32_16x16x32_bf16 v[184:187], v[92:95], v[28:31], v[184:187]\n"
      "ds_read_b128 v[20:23], v99 offset:8192\nv_mfma_f32_16x16x32_bf16 v[180:183], v[84:87], v[28:31], v[180:183]\nv_mfma_f32_16x16x32_bf16 v[180:183], v[80:83], v[24:27], v[180:183]\n"
      "v_mfma_f32_16x16x32_bf16 v[176:179], v[72:75], v[24:27], v[176:179]\nv_mfma_f32_16x16x32_bf16 v[176:179], v[76:79], v[28:31], v[176:179]\n"
      "v_mfma_f32_16x16x32_bf16 v[172:175], v[68:71], v[28:31], v[172:175]\ns_waitcnt vmcnt(0) lgkmcnt(0)\ns_barrier\nv_mfma_f32_16x16x32_bf16 v[172:175], v[64:67], v[24:27], v[172:175]\n"
      "ds_read_b64_tr_b16 v[32:33], v104 offset:0\nv_mfma_f32_16x16x32_bf16 v[188:191], v[64:67], v[0:3], v[188:191]\nds_read_b64_tr_b16 v[34:35], v104 offset:1024\n"
      "v_mfma_f32_16x16x32_bf16 v[188:191], v[68:71], v[4:7], v[188:191]\nds_read_b64_tr_b16 v[36:37], v104 offset:8192\nv_mfma_f32_16x16x32_bf16 v[192:195], v[76:79], v[4:7], v[192:195]\n"
      "ds_read_b64_tr_b16 v[38:39], v104 offset:9216\nv_mfma_f32_16x16x32_bf16 v[192:195], v[72:75], v[0:3], v[192:195]\nds_read_b64_tr_b16 v[40:41], v105 offset:0\n"
      "v_mfma_f32_16x16x32_bf16 v[196:199], v[80:83], v[0:3], v[196:199]\nds_read_b64_tr_b16 v[42:43], v105 offset:1024\nv_mfma_f32_16x16x32_bf16 v[196:199], v[84:87], v[4:7], v[196:199]\n"
      "ds_read_b64_tr_b16 v[44:45], v105 offset:8192\nv_mfma_f32_16x16x32_bf16 v[200:203], v[92:95], v[4:7], v[200:203]\nds_read_b64_tr_b16 v[46:47], v105 offset:9216\n"
      "v_mfma_f32_16x16x32_bf16 v[200:203], v[88:91], v[0:3], v[200:203]\nds_read_b64_tr_b16 v[48:49], v106 offset:0\nv_mfma_f32_16x16x32_bf16 v[216:219], v[88:91], v[8:11], v[216:219]\n"
      "ds_read_b128 v[0:3], v100 offset:0\nv_mfma_f32_16x16x32_bf16 v[216:219], v[92:95], v[12:15], v[216:219]\nds_read_b128 v[4:7], v101 offset:0\n"
      "v_mfma_f32_16x16x32_bf16 v[212:215], v[84:87], v[12:15], v[212:215]\nds_read_b64_tr_b16 v[50:51], v106 offset:1024\nv_mfma_f32_16x16x32_bf16 v[212:215], v[80:83], v[8:11], v[212:215]\n"
      "ds_read_b64_tr_b16 v[52:53], v106 offset:8192\nv_mfma_f32_16x16x32_bf16 v[208:211], v[72:75], v[8:11], v[208:211]\nds_read_b64_tr_b16 v[54:55], v106 offset:9216\n"
      "v_mfma_f32_16x16x32_bf16 v[208:211], v[76:79], v[12:15], v[208:211]\nds_read_b64_tr_b16 v[56:57], v107 offset:0\nv_mfma_f32_16x16x32_bf16 v[204:207], v[68:71], v[12:15], v[204:207]\n"
      "ds_read_b64_tr_b16 v[58:59], v107 offset:1024\nv_mfma_f32_16x16x32_bf16 v[204:207], v[64:67], v[8:11], v[204:207]\nds_read_b64_tr_b16 v[60:61], v107 offset:8192\n"
      "v_mfma_f32_16x16x32_bf16 v[220:223], v[64:67], v[16:19], v[220:223]\nds_read_b128 v[8:11], v120 offset:0\nv_mfma_f32_16x16x32_bf16 v[220:223], v[68:71], v[20:23], v[220:223]\n"
      "ds_read_b128 v[12:15], v121 offset:0\nv_mfma_f32_16x16x32_bf16 v[224:227], v[76:79], v[20:23], v[224:227]\nds_read_b64_tr_b16 v[62:63], v107 offset:9216\n"
      "v_mfma_f32_16x16x32_bf16 v[224:227], v[72:75], v[16:19], v[224:227]\nv_mfma_f32_16x16x32_bf16 v[228:231], v[80:83], v[16:19], v[228:231]\n"
      "v_mfma_f32_16x16x32_bf16 v[228:231], v[84:87], v[20:23], v[228:231]\nv_mfma_f32_16x16x32_bf16 v[232:235], v[92:95], v[20:23], v[232:235]\ns_waitcnt lgkmcnt(0)\n"
      "v_mfma_f32_16x16x32_bf16 v[232:235], v[88:91], v[16:19], v[232:235]\nds_read_b128 v[16:19], v96 offset:0\nds_read_b128 v[20:23], v97 offset:0\n"
      "v_mfma_f32_16x16x32_bf16 v[124:127], v[32:35], v[0:3], v[124:127]\nds_read_b128 v[24:27], v96 offset:2048\nv_mfma_f32_16x16x32_bf16 v[124:127], v[36:39], v[4:7], v[124:127]\n"
      "ds_read_b128 v[28:31], v97 offset:2048\nv_mfma_f32_16x16x32_bf16 v[128:131], v[44:47], v[4:7], v[128:131]\nv_mfma_f32_16x16x32_bf16 v[128:131], v[40:43], v[0:3], v[128:131]\n"
      "v_mfma_f32_16x16x32_bf16 v[152:155], v[56:59], v[8:11], v[152:155]\nds_read_b128 v[0:3], v96 offset:4096\nv_mfma_f32_16x16x32_bf16 v[152:155], v[60:63], v[12:15], v[152:155]\n"
      "ds_read_b128 v[4:7], v97 offset:4096\nv_mfma_f32_16x16x32_bf16 v[148:151], v[52:55], v[12:15], v[148:151]\nv_mfma_f32_16x16x32_bf16 v[148:151], v[48:51], v[8:11], v[148:151]\n"
      "v_mfma_f32_16x16x32_bf16 v[144:147], v[40:43], v[8:11], v[144:147]\nv_mfma_f32_16x16x32_bf16 v[144:147], v[44:47], v[12:15], v[144:147]\n"
      "v_mfma_f32_16x16x32_bf16 v[140:143], v[36:39], v[12:15], v[140:143]\ns_waitcnt lgkmcnt(4)\nv_mfma_f32_16x16x32_bf16 v[140:143], v[32:35], v[8:11], v[140:143]\n"
      "v_mfma_f32_16x16x32_bf16 v[156:159], v[32:35], v[16:19], v[156:159]\nds_read_b128 v[8:11], v96 offset:6144\nv_mfma_f32_16x16x32_bf16 v[156:159], v[36:39], v[20:23], v[156:159]\n"
      "ds_read_b128 v[12:15], v97 offset:6144\nv_mfma_f32_16x16x32_bf16 v[160:163], v[44:47], v[20:23], v[160:163]\nv_mfma_f32_16x16x32_bf16 v[160:163], v[40:43], v[16:19], v[160:163]\n"
      "v_mfma_f32_16x16x32_bf16 v[164:167], v[48:51], v[16:19], v[164:167]\nv_mfma_f32_16x16x32_bf16 v[164:167], v[52:55], v[20:23], v[164:167]\n"
      "v_mfma_f32_16x16x32_bf16 v[168:171], v[60:63], v[20:23], v[168:171]\ns_waitcnt lgkmcnt(2)\nv_mfma_f32_16x16x32_bf16 v[168:171], v[56:59], v[16:19], v[168:171]\n"
      "v_mfma_f32_16x16x32_bf16 v[184:187], v[56:59], v[24:27], v[184:187]\nds_read_b128 v[16:19], v96 offset:8192\nv_mfma_f32_16x16x32_bf16 v[184:187], v[60:63], v[28:31], v[184:187]\n"
      "ds_read_b128 v[20:23], v97 offset:8192\nv_mfma_f32_16x16x32_bf16 v[180:183], v[52:55], v[28:31], v[180:183]\nv_mfma_f32_16x16x32_bf16 v[180:183], v[48:51], v[24:27], v[180:183]\n"
      "v_mfma_f32_16x16x32_bf16 v[176:179], v[40:43], v[24:27], v[176:179]\nv_mfma_f32_16x16x32_bf16 v[176:179], v[44:47], v[28:31], v[176:179]\n"
      "v_mfma_f32_16x16x32_bf16 v[172:175], v[36:39], v[28:31], v[172:175]\ns_waitcnt vmcnt(0) lgkmcnt(0)\ns_barrier\nv_mfma_f32_16x16x32_bf16 v[172:175], v[32:35], v[24:27], v[172:175]\n"
      "ds_read_b64_tr_b16 v[64:65], v104 offset:45056\nv_mfma_f32_16x16x32_bf16 v[188:191], v[32:35], v[0:3], v[188:191]\nds_read_b64_tr_b16 v[66:67], v104 offset:46080\n"
      "v_mfma_f32_16x16x32_bf16 v[188:191], v[36:39], v[4:7], v[188:191]\nds_read_b64_tr_b16 v[68:69], v104 offset:53248\nv_mfma_f32_16x16x32_bf16 v[192:195], v[44:47], v[4:7], v[192:195]\n"
      "ds_read_b64_tr_b16 v[70:71], v104 offset:54272\nv_mfma_f32_16x16x32_bf16 v[192:195], v[40:43], v[0:3], v[192:195]\nds_read_b64_tr_b16 v[72:73], v105 offset:45056\n"
      "v_mfma_f32_16x16x32_bf16 v[196:199], v[48:51], v[0:3], v[196:199]\nds_read_b64_tr_b16 v[74:75], v105 offset:46080\nv_mfma_f32_16x16x32_bf16 v[196:199], v[52:55], v[4:7], v[196:199]\n"
      "ds_read_b64_tr_b16 v[76:77], v105 offset:53248\nv_mfma_f32_16x16x32_bf16 v[200:203], v[60:63], v[4:7], v[200:203]\nds_read_b64_tr_b16 v[78:79], v105 offset:54272\n"
      "v_mfma_f32_16x16x32_bf16 v[200:203], v[56:59], v[0:3], v[200:203]\nds_read_b64_tr_b16 v[80:81], v106 offset:45056\nv_mfma_f32_16x16x32_bf16 v[216:219], v[56:59], v[8:11], v[216:219]\n"
      "ds_read_b128 v[0:3], v100 offset:45056\nv_mfma_f32_16x16x32_bf16 v[216:219], v[60:63], v[12:15], v[216:219]\nds_read_b128 v[4:7], v101 offset:45056\n"
      "v_mfma_f32_16x16x32_bf16 v[212:215], v[52:55], v[12:15], v[212:215]\nds_read_b64_tr_b16 v[82:83], v106 offset:46080\nv_mfma_f32_16x16x32_bf16 v[212:215], v[48:51], v[8:11], v[212:215]\n"
      "ds_read_b64_tr_b16 v[84:85], v106 offset:53248\nv_mfma_f32_16x16x32_bf16 v[208:211], v[40:43], v[8:11], v[208:211]\nds_read_b64_tr_b16 v[86:87], v106 offset:54272\n"
      "v_mfma_f32_16x16x32_bf16 v[208:211], v[44:47], v[12:15], v[208:211]\nds_read_b64_tr_b16 v[88:89], v107 offset:45056\nv_mfma_f32_16x16x32_bf16 v[204:207], v[36:39], v[12:15], v[204:207]\n"
      "ds_read_b64_tr_b16 v[90:91], v107 offset:46080\nv_mfma_f32_16x16x32_bf16 v[204:207], v[32:35], v[8:11], v[204:207]\nds_read_b64_tr_b16 v[92:93], v107 offset:53248\n"
      "v_mfma_f32_16x16x32_bf16 v[220:223], v[32:35], v[16:19], v[220:223]\nds_read_b128 v[8:11], v120 offset:45056\nv_mfma_f32_16x16x32_bf16 v[220:223], v[36:39], v[20:23], v[220:223]\n"
      "ds_read_b128 v[12:15], v121 offset:45056\nv_mfma_f32_16x16x32_bf16 v[224:227], v[44:47], v[20:23], v[224:227]\nds_read_b64_tr_b16 v[94:95], v107 offset:54272\n"
      "v_mfma_f32_16x16x32_bf16 v[224:227], v[40:43], v[16:19], v[224:227]\nv_mfma_f32_16x16x32_bf16 v[228:231], v[48:51], v[16:19], v[228:231]\ns_sub_u32 s58, s58, 1\ns_cmp_lg_u32 s58, 0\n"
      "v_mfma_f32_16x16x32_bf16 v[228:231], v[52:55], v[20:23], v[228:231]\nv_mfma_f32_16x16x32_bf16 v[232:235], v[60:63], v[20:23], v[232:235]\ns_waitcnt lgkmcnt(0)\n"
      "v_mfma_f32_16x16x32_bf16 v[232:235], v[56:59], v[16:19], v[232:235]\nds_read_b128 v[16:19], v96 offset:45056\nds_read_b128 v[20:23], v97 offset:45056\ns_cbranch_scc1 .Lgl13\n"
      "v_mfma_f32_16x16x32_bf16 v[124:127], v[64:67], v[0:3], v[124:127]\nds_read_b128 v[24:27], v96 offset:47104\nv_cmp_eq_u32 s[62:63], 0, v240\n"
      "v_mfma_f32_16x16x32_bf16 v[124:127], v[68:71], v[4:7], v[124:127]\nds_read_b128 v[28:31], v97 offset:47104\nv_mov_b32 v253, 262144\n"
      "v_mfma_f32_16x16x32_bf16 v[128:131], v[76:79], v[4:7], v[128:131]\nv_mov_b32 v248, 162048\nv_mov_b32 v241, 1\nv_mfma_f32_16x16x32_bf16 v[128:131], v[72:75], v[0:3], v[128:131]\n"
      "v_cndmask_b32 v253, v253, v248, s[62:63]\nv_lshrrev_b32 v112, 4, v240\nv_mfma_f32_16x16x32_bf16 v[152:155], v[88:91], v[8:11], v[152:155]\nds_read_b128 v[0:3], v96 offset:49152\n"
      "v_bfe_u32 v113, v240, 2, 2\nv_mfma_f32_16x16x32_bf16 v[152:155], v[92:95], v[12:15], v[152:155]\nds_read_b128 v[4:7], v97 offset:49152\nv_and_b32 v114, 3, v240\n"
      "v_mfma_f32_16x16x32_bf16 v[148:151], v[84:87], v[12:15], v[148:151]\nv_mul_u32_u24 v114, 0x80008, v114\nv_add_u32 v244, v112, v113\n"
      "v_mfma_f32_16x16x32_bf16 v[148:151], v[80:83], v[8:11], v[148:151]\nv_and_b32 v244, 3, v244\nv_add_u32 v116, 22, v113\nv_mfma_f32_16x16x32_bf16 v[144:147], v[72:75], v[8:11], v[144:147]\n"
      "v_cmp_gt_u32 s[62:63], 2, v113\nv_and_b32 v113, 60, v240\nv_mfma_f32_16x16x32_bf16 v[144:147], v[76:79], v[12:15], v[144:147]\ns_mov_b32 s42, 0\nv_cndmask_b32 v116, v116, v244, s[62:63]\n"
      "v_mfma_f32_16x16x32_bf16 v[140:143], v[68:71], v[12:15], v[140:143]\ns_waitcnt lgkmcnt(4)\nv_lshl_or_b32 v244, v244, 16, v116\n"
      "v_mfma_f32_16x16x32_bf16 v[140:143], v[64:67], v[8:11], v[140:143]\nv_lshl_add_u32 v244, v244, 5, v114\nv_lshl_add_u32 v252, v240, 4, s42\n"
      "v_mfma_f32_16x16x32_bf16 v[156:159], v[64:67], v[16:19], v[156:159]\nds_read_b128 v[8:11], v96 offset:51200\ns_mov_b32 s42, 0x76325410\n"
      "v_mfma_f32_16x16x32_bf16 v[156:159], v[68:71], v[20:23], v[156:159]\nds_read_b128 v[12:15], v97 offset:51200\ns_mov_b32 s43, 0x21005410\n"
      "v_mfma_f32_16x16x32_bf16 v[160:163], v[76:79], v[20:23], v[160:163]\nv_lshrrev_b64 v[116:117], v113, s[42:43]\nv_and_b32 v116, 7, v116\n"
      "v_mfma_f32_16x16x32_bf16 v[160:163], v[72:75], v[16:19], v[160:163]\nv_lshl_add_u32 v116, v112, 3, v116\ns_mov_b32 s42, 0x54107632\n"
      "v_mfma_f32_16x16x32_bf16 v[164:167], v[80:83], v[16:19], v[164:167]\ns_mov_b32 s43, 0x21107632\nv_lshrrev_b64 v[118:119], v113, s[42:43]\n"
      "v_mfma_f32_16x16x32_bf16 v[164:167], v[84:87], v[20:23], v[164:167]\nv_and_b32 v118, 7, v118\nv_lshl_add_u32 v118, v112, 3, v118\n"
      "v_mfma_f32_16x16x32_bf16 v[168:171], v[92:95], v[20:23], v[168:171]\ns_waitcnt lgkmcnt(2)\nv_lshl_or_b32 v245, v118, 16, v116\n"
      "v_mfma_f32_16x16x32_bf16 v[168:171], v[88:91], v[16:19], v[168:171]\nv_lshl_add_u32 v245, v245, 5, v114\nv_and_b32 v113, 1, v240\n"
      "v_mfma_f32_16x16x32_bf16 v[184:187], v[88:91], v[24:27], v[184:187]\nds_read_b128 v[16:19], v96 offset:53248\nv_mul_u32_u24 v113, 0xaaaa, v113\n"
      "v_mfma_f32_16x16x32_bf16 v[184:187], v[92:95], v[28:31], v[184:187]\nds_read_b128 v[20:23], v97 offset:53248\nv_add_u32 v242, 0x4444, v113\n"
      "v_mfma_f32_16x16x32_bf16 v[180:183], v[84:87], v[28:31], v[180:183]\ns_lshl_b32 s42, s36, 4\ns_add_u32 s42, s42, 160640\nv_mfma_f32_16x16x32_bf16 v[180:183], v[80:83], v[24:27], v[180:183]\n"
      "v_lshl_add_u32 v243, v112, 2, s42\nv_and_b32 v112, 1, v112\nv_mfma_f32_16x16x32_bf16 v[176:179], v[72:75], v[24:27], v[176:179]\nv_lshl_add_u32 v112, v112, 3, s24\ns_sub_u32 s42, s64, 1\n"
      "v_mfma_f32_16x16x32_bf16 v[176:179], v[76:79], v[28:31], v[176:179]\nv_min_u32 v112, s42, v112\nv_lshlrev_b32 v112, 3, v112\n"
      "v_mfma_f32_16x16x32_bf16 v[172:175], v[68:71], v[28:31], v[172:175]\ns_waitcnt vmcnt(0) lgkmcnt(0)\ns_barrier\nv_mfma_f32_16x16x32_bf16 v[172:175], v[64:67], v[24:27], v[172:175]\n"
      "ds_read_b64_tr_b16 v[32:33], v108 offset:0\nv_add_u32 v250, 160256, v112\nv_mfma_f32_16x16x32_bf16 v[188:191], v[64:67], v[0:3], v[188:191]\nds_read_b64_tr_b16 v[34:35], v108 offset:1024\n"
      "v_and_b32 v113, 15, v240\nv_mfma_f32_16x16x32_bf16 v[188:191], v[68:71], v[4:7], v[188:191]\nds_read_b64_tr_b16 v[36:37], v108 offset:8192\nv_min_u32 v113, 12, v113\n"
      "v_mfma_f32_16x16x32_bf16 v[192:195], v[76:79], v[4:7], v[192:195]\nds_read_b64_tr_b16 v[38:39], v108 offset:9216\ns_lshl_b32 s42, s30, 7\n"
      "v_mfma_f32_16x16x32_bf16 v[192:195], v[72:75], v[0:3], v[192:195]\nds_read_b64_tr_b16 v[40:41], v109 offset:0\nv_lshl_add_u32 v113, v113, 15, s42\n"
      "v_mfma_f32_16x16x32_bf16 v[196:199], v[80:83], v[0:3], v[196:199]\nds_read_b64_tr_b16 v[42:43], v109 offset:1024\nv_add_u32 v249, v113, v112\n"
      "v_mfma_f32_16x16x32_bf16 v[196:199], v[84:87], v[4:7], v[196:199]\nds_read_b64_tr_b16 v[44:45], v109 offset:8192\ns_mul_i32 s65, s36, 22272\n"
      "v_mfma_f32_16x16x32_bf16 v[200:203], v[92:95], v[4:7], v[200:203]\nds_read_b64_tr_b16 v[46:47], v109 offset:9216\ns_add_u32 s65, s65, 45056\n"
      "v_mfma_f32_16x16x32_bf16 v[200:203], v[88:91], v[0:3], v[200:203]\nds_read_b64_tr_b16 v[48:49], v110 offset:0\ns_add_u32 s68, s64, 8\n"
      "v_mfma_f32_16x16x32_bf16 v[216:219], v[88:91], v[8:11], v[216:219]\nds_read_b128 v[0:3], v102 offset:0\ns_min_u32 s68, s68, 21\n"
      "v_mfma_f32_16x16x32_bf16 v[216:219], v[92:95], v[12:15], v[216:219]\nds_read_b128 v[4:7], v103 offset:0\ns_lshl_b32 s38, s38, 16\n"
      "v_mfma_f32_16x16x32_bf16 v[212:215], v[84:87], v[12:15], v[212:215]\nds_read_b64_tr_b16 v[50:51], v110 offset:1024\ns_lshr_b32 s42, s24, 2\n"
      "v_mfma_f32_16x16x32_bf16 v[212:215], v[80:83], v[8:11], v[212:215]\nds_read_b64_tr_b16 v[52:53], v110 offset:8192\ns_mul_i32 s43, s42, 3\n"
      "v_mfma_f32_16x16x32_bf16 v[208:211], v[72:75], v[8:11], v[208:211]\nds_read_b64_tr_b16 v[54:55], v110 offset:9216\ns_xor_b32 s43, s43, s36\n"
      "v_mfma_f32_16x16x32_bf16 v[208:211], v[76:79], v[12:15], v[208:211]\nds_read_b64_tr_b16 v[56:57], v111 offset:0\ns_add_u32 s42, s42, s64\n"
      "v_mfma_f32_16x16x32_bf16 v[204:207], v[68:71], v[12:15], v[204:207]\nds_read_b64_tr_b16 v[58:59], v111 offset:1024\ns_add_u32 s42, s42, 9\n"
      "v_mfma_f32_16x16x32_bf16 v[204:207], v[64:67], v[8:11], v[204:207]\nds_read_b64_tr_b16 v[60:61], v111 offset:8192\ns_and_b32 s42, s42, 1\n"
      "v_mfma_f32_16x16x32_bf16 v[220:223], v[64:67], v[16:19], v[220:223]\nds_read_b128 v[8:11], v122 offset:0\ns_lshl1_add_u32 s67, s43, s42\n"
      "v_mfma_f32_16x16x32_bf16 v[220:223], v[68:71], v[20:23], v[220:223]\nds_read_b128 v[12:15], v123 offset:0\ns_add_u32 s66, s64, s67\n"
      "v_mfma_f32_16x16x32_bf16 v[224:227], v[76:79], v[20:23], v[224:227]\nds_read_b64_tr_b16 v[62:63], v111 offset:9216\ns_cmp_lt_u32 s66, s68\n"
      "v_mfma_f32_16x16x32_bf16 v[224:227], v[72:75], v[16:19], v[224:227]\ns_cselect_b32 s66, s66, 127\ns_sub_u32 s66, s66, 8\nv_mfma_f32_16x16x32_bf16 v[228:231], v[80:83], v[16:19], v[228:231]\n"
      "s_max_i32 s66, s66, 0\ns_getpc_b64 s[34:35]\n"
      ".Lk1066:\nv_mfma_f32_16x16x32_bf16 v[228:231], v[84:87], v[20:23], v[228:231]\ns_add_u32 s34, s34, .Lt_g-.Lk1066\ns_addc_u32 s35, s35, 0\n"
      "v_mfma_f32_16x16x32_bf16 v[232:235], v[92:95], v[20:23], v[232:235]\ns_waitcnt lgkmcnt(0)\ns_lshr_b32 s42, s2, 3\nv_mfma_f32_16x16x32_bf16 v[232:235], v[88:91], v[16:19], v[232:235]\n"
      "ds_read_b128 v[16:19], v98 offset:0\nds_read_b128 v[20:23], v99 offset:0\nv_mfma_f32_16x16x32_bf16 v[124:127], v[32:35], v[0:3], v[124:127]\nds_read_b128 v[24:27], v98 offset:2048\n"
      "s_lshl1_add_u32 s42, s2, s42\nv_mfma_f32_16x16x32_bf16 v[124:127], v[36:39], v[4:7], v[124:127]\nds_read_b128 v[28:31], v99 offset:2048\ns_and_b32 s69, s42, 15\n"
      "v_mfma_f32_16x16x32_bf16 v[128:131], v[44:47], v[4:7], v[128:131]\ns_lshl_b32 s43, s69, 7\ns_add_u32 s43, s43, 32768\nv_mfma_f32_16x16x32_bf16 v[128:131], v[40:43], v[0:3], v[128:131]\n"
      "s_add_u32 s8, s20, s43\ns_addc_u32 s9, s21, 0\nv_mfma_f32_16x16x32_bf16 v[152:155], v[56:59], v[8:11], v[152:155]\nds_read_b128 v[0:3], v98 offset:4096\ns_add_u32 s12, s20, 65536\n"
      "v_mfma_f32_16x16x32_bf16 v[152:155], v[60:63], v[12:15], v[152:155]\nds_read_b128 v[4:7], v99 offset:4096\ns_addc_u32 s13, s21, 0\n"
      "v_mfma_f32_16x16x32_bf16 v[148:151], v[52:55], v[12:15], v[148:151]\ns_add_u32 s16, s20, 2097152\ns_addc_u32 s17, s21, 0\nv_mfma_f32_16x16x32_bf16 v[148:151], v[48:51], v[8:11], v[148:151]\n"
      "s_lshl_b32 s42, s69, 4\ns_max_u32 s42, s42, 24\nv_mfma_f32_16x16x32_bf16 v[144:147], v[40:43], v[8:11], v[144:147]\ns_sub_u32 s42, s42, 24\ns_min_u32 s69, s42, 192\n"
      "v_mfma_f32_16x16x32_bf16 v[144:147], v[44:47], v[12:15], v[144:147]\ns_lshl_b32 s42, s69, 3\ns_add_u32 s42, s42, 24576\nv_mfma_f32_16x16x32_bf16 v[140:143], v[36:39], v[12:15], v[140:143]\n"
      "s_waitcnt lgkmcnt(4)\ns_add_u32 s14, s20, s42\nv_mfma_f32_16x16x32_bf16 v[140:143], v[32:35], v[8:11], v[140:143]\ns_addc_u32 s15, s21, 0\n"
      "v_mfma_f32_16x16x32_bf16 v[156:159], v[32:35], v[16:19], v[156:159]\nds_read_b128 v[8:11], v98 offset:6144\nv_mfma_f32_16x16x32_bf16 v[156:159], v[36:39], v[20:23], v[156:159]\n"
      "ds_read_b128 v[12:15], v99 offset:6144\nv_mfma_f32_16x16x32_bf16 v[160:163], v[44:47], v[20:23], v[160:163]\nv_mfma_f32_16x16x32_bf16 v[160:163], v[40:43], v[16:19], v[160:163]\n"
      "v_mfma_f32_16x16x32_bf16 v[164:167], v[48:51], v[16:19], v[164:167]\nv_mfma_f32_16x16x32_bf16 v[164:167], v[52:55], v[20:23], v[164:167]\n"
      "v_mfma_f32_16x16x32_bf16 v[168:171], v[60:63], v[20:23], v[168:171]\ns_waitcnt lgkmcnt(2)\nv_mfma_f32_16x16x32_bf16 v[168:171], v[56:59], v[16:19], v[168:171]\n"
      "v_mfma_f32_16x16x32_bf16 v[184:187], v[56:59], v[24:27], v[184:187]\nds_read_b128 v[16:19], v98 offset:8192\nv_mfma_f32_16x16x32_bf16 v[184:187], v[60:63], v[28:31], v[184:187]\n"
      "ds_read_b128 v[20:23], v99 offset:8192\nv_mfma_f32_16x16x32_bf16 v[180:183], v[52:55], v[28:31], v[180:183]\nv_mfma_f32_16x16x32_bf16 v[180:183], v[48:51], v[24:27], v[180:183]\n"
      "v_mfma_f32_16x16x32_bf16 v[176:179], v[40:43], v[24:27], v[176:179]\nv_mfma_f32_16x16x32_bf16 v[176:179], v[44:47], v[28:31], v[176:179]\n"
      "v_mfma_f32_16x16x32_bf16 v[172:175], v[36:39], v[28:31], v[172:175]\ns_waitcnt vmcnt(0) lgkmcnt(0)\ns_barrier\nv_mfma_f32_16x16x32_bf16 v[172:175], v[32:35], v[24:27], v[172:175]\n"
      "ds_read_b128 v[72:75], v252 offset:0\nv_lshl_add_u32 v246, v240, 1, v248\nv_mfma_f32_16x16x32_bf16 v[188:191], v[32:35], v[0:3], v[188:191]\nds_read_b128 v[24:27], v236 offset:512\n"
      "ds_read_u16 v251, v246 offset:64\nv_mfma_f32_16x16x32_bf16 v[188:191], v[36:39], v[4:7], v[188:191]\nds_read_b128 v[28:31], v236 offset:576\nds_read_b32 v247, v248 offset:24\n"
      "v_mfma_f32_16x16x32_bf16 v[192:195], v[44:47], v[4:7], v[192:195]\nds_read_b128 v[76:79], v252 offset:1024\nv_and_b32 v246, 48, v240\n"
      "v_mfma_f32_16x16x32_bf16 v[192:195], v[40:43], v[0:3], v[192:195]\nds_read_b128 v[80:83], v252 offset:2048\ns_mov_b32 s42, 42240\n"
      "v_mfma_f32_16x16x32_bf16 v[196:199], v[48:51], v[0:3], v[196:199]\nds_read_b128 v[84:87], v252 offset:3072\nv_lshl_add_u32 v246, v246, 2, s42\n"
      "v_mfma_f32_16x16x32_bf16 v[196:199], v[52:55], v[4:7], v[196:199]\nds_read_b128 v[88:91], v252 offset:4096\ns_mov_b32 s58, 0xbdd2d3e7\n"
      "v_mfma_f32_16x16x32_bf16 v[200:203], v[60:63], v[4:7], v[200:203]\nds_read_b128 v[92:95], v252 offset:5120\ns_mov_b32 s59, 0xc0135761\n"
      "v_mfma_f32_16x16x32_bf16 v[200:203], v[56:59], v[0:3], v[200:203]\nds_read_b128 v[96:99], v252 offset:6144\ns_mov_b32 s60, 0xc1e00000\n"
      "v_mfma_f32_16x16x32_bf16 v[216:219], v[56:59], v[8:11], v[216:219]\nds_read_b128 v[0:3], v236 offset:0\ns_add_u32 s49, s65, 1856\n"
      "v_mfma_f32_16x16x32_bf16 v[216:219], v[60:63], v[12:15], v[216:219]\nds_read_b128 v[4:7], v236 offset:64\ns_mov_b32 s45, 2\n"
      "v_mfma_f32_16x16x32_bf16 v[212:215], v[52:55], v[12:15], v[212:215]\nds_read_b128 v[100:103], v252 offset:7168\ns_mov_b32 s46, 0\n"
      "v_mfma_f32_16x16x32_bf16 v[212:215], v[48:51], v[8:11], v[212:215]\nds_read_b128 v[104:107], v252 offset:8192\ns_waitcnt lgkmcnt(8)\n"
      "v_mfma_f32_16x16x32_bf16 v[208:211], v[40:43], v[8:11], v[208:211]\nds_read_b128 v[108:111], v252 offset:9216\nv_readfirstlane_b32 s39, v247\n"
      "v_mfma_f32_16x16x32_bf16 v[208:211], v[44:47], v[12:15], v[208:211]\nds_read_b128 v[64:67], v237 offset:512\nv_readlane_b32 s55, v251, 0\n"
      "v_mfma_f32_16x16x32_bf16 v[204:207], v[36:39], v[12:15], v[204:207]\nds_read_b128 v[68:71], v237 offset:576\ns_setprio 3\nv_mfma_f32_16x16x32_bf16 v[204:207], v[32:35], v[8:11], v[204:207]\n"
      "ds_read_b128 v[112:115], v252 offset:10240\ns_xor_b32 s40, s39, 0xa5c30f96\nv_mfma_f32_16x16x32_bf16 v[220:223], v[32:35], v[16:19], v[220:223]\nds_read_b128 v[8:11], v237 offset:0\n"
      "s_mul_i32 s41, s39, 0x9E3779B1\nv_mfma_f32_16x16x32_bf16 v[220:223], v[36:39], v[20:23], v[220:223]\nds_read_b128 v[12:15], v237 offset:64\ns_lshr_b32 s41, s41, 24\n"
      "v_mfma_f32_16x16x32_bf16 v[224:227], v[44:47], v[20:23], v[224:227]\nds_read_b128 v[116:119], v252 offset:11264\nv_mfma_f32_16x16x32_bf16 v[224:227], v[40:43], v[16:19], v[224:227]\n"
      "ds_read_b128 v[120:123], v252 offset:12288\nv_add_u32_sdwa v239, s49, v245 dst_sel:DWORD dst_unused:UNUSED_PAD src0_sel:DWORD src1_sel:WORD_1\n"
      "v_mfma_f32_16x16x32_bf16 v[228:231], v[48:51], v[16:19], v[228:231]\nds_read_b128 v[132:135], v252 offset:15360\n"
      "v_add_u32_sdwa v236, s49, v244 dst_sel:DWORD dst_unused:UNUSED_PAD src0_sel:DWORD src1_sel:WORD_0\nv_mfma_f32_16x16x32_bf16 v[228:231], v[52:55], v[20:23], v[228:231]\n"
      "ds_read_b128 v[136:139], v252 offset:16384\nv_add_u32_sdwa v237, s49, v244 dst_sel:DWORD dst_unused:UNUSED_PAD src0_sel:DWORD src1_sel:WORD_1\n"
      "v_mfma_f32_16x16x32_bf16 v[232:235], v[60:63], v[20:23], v[232:235]\nv_add_u32_sdwa v238, s49, v245 dst_sel:DWORD dst_unused:UNUSED_PAD src0_sel:DWORD src1_sel:WORD_0\ns_waitcnt lgkmcnt(0)\n"
      "v_mfma_f32_16x16x32_bf16 v[232:235], v[56:59], v[16:19], v[232:235]\nv_pk_mul_f32 v[44:45], v[124:125], v[124:125]\nv_pk_mul_f32 v[46:47], v[126:127], v[126:127]\n"
      "v_pk_mul_f32 v[48:49], v[128:129], v[128:129]\nv_pk_mul_f32 v[50:51], v[130:131], v[130:131]\nv_pk_fma_f32 v[44:45], v[44:45], s[58:59], s[58:59] op_sel:[0,0,1] op_sel_hi:[1,0,1]\n"
      "v_pk_fma_f32 v[46:47], v[46:47], s[58:59], s[58:59] op_sel:[0,0,1] op_sel_hi:[1,0,1]\nv_pk_fma_f32 v[48:49], v[48:49], s[58:59], s[58:59] op_sel:[0,0,1] op_sel_hi:[1,0,1]\n"
      "v_pk_fma_f32 v[50:51], v[50:51], s[58:59], s[58:59] op_sel:[0,0,1] op_sel_hi:[1,0,1]\nv_pk_fma_f32 v[44:45], v[44:45], v[124:125], s[60:61] op_sel_hi:[1,1,0]\n"
      "v_pk_fma_f32 v[46:47], v[46:47], v[126:127], s[60:61] op_sel_hi:[1,1,0]\nv_pk_fma_f32 v[48:49], v[48:49], v[128:129], s[60:61] op_sel_hi:[1,1,0]\n"
      "v_pk_fma_f32 v[50:51], v[50:51], v[130:131], s[60:61] op_sel_hi:[1,1,0]\nv_exp_f32 v44, v44 clamp\nv_pk_mul_f32 v[52:53], v[140:141], v[140:141]\nv_exp_f32 v45, v45 clamp\n"
      "v_pk_mul_f32 v[54:55], v[142:143], v[142:143]\nv_pk_mul_f32 v[56:57], v[144:145], v[144:145]\nv_exp_f32 v46, v46 clamp\nv_pk_mul_f32 v[58:59], v[146:147], v[146:147]\n"
      "v_exp_f32 v47, v47 clamp\nv_pk_fma_f32 v[52:53], v[52:53], s[58:59], s[58:59] op_sel:[0,0,1] op_sel_hi:[1,0,1]\n"
      "v_pk_fma_f32 v[54:55], v[54:55], s[58:59], s[58:59] op_sel:[0,0,1] op_sel_hi:[1,0,1]\nv_exp_f32 v48, v48 clamp\n"
      "v_pk_fma_f32 v[56:57], v[56:57], s[58:59], s[58:59] op_sel:[0,0,1] op_sel_hi:[1,0,1]\nv_exp_f32 v49, v49 clamp\n"
      "v_pk_fma_f32 v[58:59], v[58:59], s[58:59], s[58:59] op_sel:[0,0,1] op_sel_hi:[1,0,1]\nv_pk_fma_f32 v[52:53], v[52:53], v[140:141], s[60:61] op_sel_hi:[1,1,0]\nv_exp_f32 v50, v50 clamp\n"
      "v_pk_fma_f32 v[54:55], v[54:55], v[142:143], s[60:61] op_sel_hi:[1,1,0]\nv_exp_f32 v51, v51 clamp\nv_pk_fma_f32 v[56:57], v[56:57], v[144:145], s[60:61] op_sel_hi:[1,1,0]\n"
      "v_pk_fma_f32 v[58:59], v[58:59], v[146:147], s[60:61] op_sel_hi:[1,1,0]\nv_exp_f32 v52, v52 clamp\nv_pk_fma_f32 v[46:47], v[46:47], v[2:3], v[26:27]\n"
      "v_pk_fma_f32 v[44:45], v[44:45], v[0:1], v[24:25]\nv_pk_fma_f32 v[48:49], v[48:49], v[4:5], v[28:29]\nv_exp_f32 v53, v53 clamp\nv_pk_fma_f32 v[50:51], v[50:51], v[6:7], v[30:31]\n"
      "v_pk_mul_f32 v[124:125], v[124:125], v[46:47]\nv_pk_mul_f32 v[128:129], v[128:129], v[50:51]\nv_exp_f32 v54, v54 clamp\nv_pk_fma_f32 v[124:125], v[126:127], v[44:45], v[124:125]\n"
      "v_pk_fma_f32 v[128:129], v[130:131], v[48:49], v[128:129]\nv_pk_mul_f32 v[44:45], v[44:45], v[46:47]\nv_exp_f32 v55, v55 clamp\nv_pk_mul_f32 v[48:49], v[48:49], v[50:51]\n"
      "v_pk_mul_f32 v[46:47], v[44:45], v[48:49]\nv_rcp_f32 v46, v46\nv_pk_mul_f32 v[124:125], v[124:125], v[48:49]\nv_rcp_f32 v47, v47\nv_pk_fma_f32 v[124:125], v[128:129], v[44:45], v[124:125]\n"
      "v_pk_mul_f32 v[32:33], v[124:125], v[46:47]\nv_add_f32 v42, v32, v33\nv_pk_mul_f32 v[16:17], v[148:149], v[148:149]\nv_pk_mul_f32 v[18:19], v[150:151], v[150:151]\nv_exp_f32 v56, v56 clamp\n"
      "v_pk_mul_f32 v[20:21], v[152:153], v[152:153]\nv_pk_mul_f32 v[22:23], v[154:155], v[154:155]\nv_exp_f32 v57, v57 clamp\n"
      "v_pk_fma_f32 v[16:17], v[16:17], s[58:59], s[58:59] op_sel:[0,0,1] op_sel_hi:[1,0,1]\nv_pk_fma_f32 v[18:19], v[18:19], s[58:59], s[58:59] op_sel:[0,0,1] op_sel_hi:[1,0,1]\n"
      "v_pk_fma_f32 v[20:21], v[20:21], s[58:59], s[58:59] op_sel:[0,0,1] op_sel_hi:[1,0,1]\nv_exp_f32 v58, v58 clamp\n"
      "v_pk_fma_f32 v[22:23], v[22:23], s[58:59], s[58:59] op_sel:[0,0,1] op_sel_hi:[1,0,1]\nv_pk_fma_f32 v[16:17], v[16:17], v[148:149], s[60:61] op_sel_hi:[1,1,0]\n"
      "v_pk_fma_f32 v[18:19], v[18:19], v[150:151], s[60:61] op_sel_hi:[1,1,0]\nv_exp_f32 v59, v59 clamp\nv_pk_fma_f32 v[20:21], v[20:21], v[152:153], s[60:61] op_sel_hi:[1,1,0]\n"
      "v_pk_fma_f32 v[22:23], v[22:23], v[154:155], s[60:61] op_sel_hi:[1,1,0]\nv_exp_f32 v16, v16 clamp\nv_pk_fma_f32 v[54:55], v[54:55], v[2:3], v[26:27]\n"
      "v_pk_fma_f32 v[52:53], v[52:53], v[0:1], v[24:25]\nv_pk_fma_f32 v[56:57], v[56:57], v[4:5], v[28:29]\nv_exp_f32 v17, v17 clamp\nv_pk_fma_f32 v[58:59], v[58:59], v[6:7], v[30:31]\n"
      "v_pk_mul_f32 v[140:141], v[140:141], v[54:55]\nv_pk_mul_f32 v[144:145], v[144:145], v[58:59]\nv_exp_f32 v18, v18 clamp\nv_pk_fma_f32 v[140:141], v[142:143], v[52:53], v[140:141]\n"
      "v_pk_fma_f32 v[144:145], v[146:147], v[56:57], v[144:145]\nv_pk_mul_f32 v[52:53], v[52:53], v[54:55]\nv_exp_f32 v19, v19 clamp\nv_pk_mul_f32 v[56:57], v[56:57], v[58:59]\n"
      "v_pk_mul_f32 v[54:55], v[52:53], v[56:57]\nv_rcp_f32 v54, v54\nv_pk_mul_f32 v[140:141], v[140:141], v[56:57]\nv_rcp_f32 v55, v55\nv_pk_fma_f32 v[140:141], v[144:145], v[52:53], v[140:141]\n"
      "v_pk_mul_f32 v[32:33], v[140:141], v[54:55]\nv_pk_mul_f32 v[44:45], v[156:157], v[156:157]\nv_pk_mul_f32 v[46:47], v[158:159], v[158:159]\nv_exp_f32 v20, v20 clamp\n"
      "v_pk_mul_f32 v[48:49], v[160:161], v[160:161]\nv_pk_mul_f32 v[50:51], v[162:163], v[162:163]\nv_exp_f32 v21, v21 clamp\n"
      "v_pk_fma_f32 v[44:45], v[44:45], s[58:59], s[58:59] op_sel:[0,0,1] op_sel_hi:[1,0,1]\nv_pk_fma_f32 v[46:47], v[46:47], s[58:59], s[58:59] op_sel:[0,0,1] op_sel_hi:[1,0,1]\n"
      "v_pk_fma_f32 v[48:49], v[48:49], s[58:59], s[58:59] op_sel:[0,0,1] op_sel_hi:[1,0,1]\nv_exp_f32 v22, v22 clamp\n"
      "v_pk_fma_f32 v[50:51], v[50:51], s[58:59], s[58:59] op_sel:[0,0,1] op_sel_hi:[1,0,1]\nv_pk_fma_f32 v[44:45], v[44:45], v[156:157], s[60:61] op_sel_hi:[1,1,0]\n"
      "v_pk_fma_f32 v[46:47], v[46:47], v[158:159], s[60:61] op_sel_hi:[1,1,0]\nv_exp_f32 v23, v23 clamp\nv_pk_fma_f32 v[48:49], v[48:49], v[160:161], s[60:61] op_sel_hi:[1,1,0]\n"
      "v_pk_fma_f32 v[50:51], v[50:51], v[162:163], s[60:61] op_sel_hi:[1,1,0]\nv_exp_f32 v44, v44 clamp\nv_pk_fma_f32 v[18:19], v[18:19], v[10:11], v[66:67]\n"
      "v_pk_fma_f32 v[16:17], v[16:17], v[8:9], v[64:65]\nv_pk_fma_f32 v[20:21], v[20:21], v[12:13], v[68:69]\nv_exp_f32 v45, v45 clamp\nv_pk_fma_f32 v[22:23], v[22:23], v[14:15], v[70:71]\n"
      "v_pk_mul_f32 v[148:149], v[148:149], v[18:19]\nv_pk_mul_f32 v[152:153], v[152:153], v[22:23]\nv_exp_f32 v46, v46 clamp\nv_pk_fma_f32 v[148:149], v[150:151], v[16:17], v[148:149]\n"
      "v_pk_fma_f32 v[152:153], v[154:155], v[20:21], v[152:153]\nv_pk_mul_f32 v[16:17], v[16:17], v[18:19]\nv_exp_f32 v47, v47 clamp\nv_pk_mul_f32 v[20:21], v[20:21], v[22:23]\n"
      "v_pk_mul_f32 v[18:19], v[16:17], v[20:21]\nv_rcp_f32 v18, v18\nv_pk_mul_f32 v[148:149], v[148:149], v[20:21]\nv_rcp_f32 v19, v19\nv_pk_fma_f32 v[148:149], v[152:153], v[16:17], v[148:149]\n"
      "v_pk_fma_f32 v[32:33], v[148:149], v[18:19], v[32:33]\nv_add_f32 v41, v32, v33\nv_pk_mul_f32 v[52:53], v[164:165], v[164:165]\nv_pk_mul_f32 v[54:55], v[166:167], v[166:167]\n"
      "v_exp_f32 v48, v48 clamp\nv_pk_mul_f32 v[56:57], v[168:169], v[168:169]\nv_pk_mul_f32 v[58:59], v[170:171], v[170:171]\nv_exp_f32 v49, v49 clamp\n"
      "v_pk_fma_f32 v[52:53], v[52:53], s[58:59], s[58:59] op_sel:[0,0,1] op_sel_hi:[1,0,1]\nv_pk_fma_f32 v[54:55], v[54:55], s[58:59], s[58:59] op_sel:[0,0,1] op_sel_hi:[1,0,1]\n"
      "v_pk_fma_f32 v[56:57], v[56:57], s[58:59], s[58:59] op_sel:[0,0,1] op_sel_hi:[1,0,1]\nv_exp_f32 v50, v50 clamp\n"
      "v_pk_fma_f32 v[58:59], v[58:59], s[58:59], s[58:59] op_sel:[0,0,1] op_sel_hi:[1,0,1]\nv_pk_fma_f32 v[52:53], v[52:53], v[164:165], s[60:61] op_sel_hi:[1,1,0]\n"
      "v_pk_fma_f32 v[54:55], v[54:55], v[166:167], s[60:61] op_sel_hi:[1,1,0]\nv_exp_f32 v51, v51 clamp\nv_pk_fma_f32 v[56:57], v[56:57], v[168:169], s[60:61] op_sel_hi:[1,1,0]\n"
      "v_pk_fma_f32 v[58:59], v[58:59], v[170:171], s[60:61] op_sel_hi:[1,1,0]\nv_exp_f32 v52, v52 clamp\nv_pk_fma_f32 v[46:47], v[46:47], v[2:3], v[26:27]\n"
      "v_pk_fma_f32 v[44:45], v[44:45], v[0:1], v[24:25]\nv_pk_fma_f32 v[48:49], v[48:49], v[4:5], v[28:29]\nv_exp_f32 v53, v53 clamp\nv_pk_fma_f32 v[50:51], v[50:51], v[6:7], v[30:31]\n"
      "v_pk_mul_f32 v[156:157], v[156:157], v[46:47]\nv_pk_mul_f32 v[160:161], v[160:161], v[50:51]\nv_exp_f32 v54, v54 clamp\nv_pk_fma_f32 v[156:157], v[158:159], v[44:45], v[156:157]\n"
      "v_pk_fma_f32 v[160:161], v[162:163], v[48:49], v[160:161]\nv_pk_mul_f32 v[44:45], v[44:45], v[46:47]\nv_exp_f32 v55, v55 clamp\nv_pk_mul_f32 v[48:49], v[48:49], v[50:51]\n"
      "v_pk_mul_f32 v[46:47], v[44:45], v[48:49]\nv_rcp_f32 v46, v46\nv_pk_mul_f32 v[156:157], v[156:157], v[48:49]\nv_rcp_f32 v47, v47\nv_pk_fma_f32 v[156:157], v[160:161], v[44:45], v[156:157]\n"
      "v_pk_mul_f32 v[32:33], v[156:157], v[46:47]\nv_pk_mul_f32 v[16:17], v[172:173], v[172:173]\nv_pk_mul_f32 v[18:19], v[174:175], v[174:175]\nv_exp_f32 v56, v56 clamp\n"
      "v_pk_mul_f32 v[20:21], v[176:177], v[176:177]\nv_pk_mul_f32 v[22:23], v[178:179], v[178:179]\nv_exp_f32 v57, v57 clamp\n"
      "v_pk_fma_f32 v[16:17], v[16:17], s[58:59], s[58:59] op_sel:[0,0,1] op_sel_hi:[1,0,1]\nv_pk_fma_f32 v[18:19], v[18:19], s[58:59], s[58:59] op_sel:[0,0,1] op_sel_hi:[1,0,1]\n"
      "v_pk_fma_f32 v[20:21], v[20:21], s[58:59], s[58:59] op_sel:[0,0,1] op_sel_hi:[1,0,1]\nv_exp_f32 v58, v58 clamp\n"
      "v_pk_fma_f32 v[22:23], v[22:23], s[58:59], s[58:59] op_sel:[0,0,1] op_sel_hi:[1,0,1]\nv_pk_fma_f32 v[16:17], v[16:17], v[172:173], s[60:61] op_sel_hi:[1,1,0]\n"
      "v_pk_fma_f32 v[18:19], v[18:19], v[174:175], s[60:61] op_sel_hi:[1,1,0]\nv_exp_f32 v59, v59 clamp\nv_pk_fma_f32 v[20:21], v[20:21], v[176:177], s[60:61] op_sel_hi:[1,1,0]\n"
      "v_pk_fma_f32 v[22:23], v[22:23], v[178:179], s[60:61] op_sel_hi:[1,1,0]\nv_exp_f32 v16, v16 clamp\nv_pk_fma_f32 v[54:55], v[54:55], v[10:11], v[66:67]\n"
      "v_pk_fma_f32 v[52:53], v[52:53], v[8:9], v[64:65]\nv_pk_fma_f32 v[56:57], v[56:57], v[12:13], v[68:69]\nv_exp_f32 v17, v17 clamp\nv_pk_fma_f32 v[58:59], v[58:59], v[14:15], v[70:71]\n"
      "v_pk_mul_f32 v[164:165], v[164:165], v[54:55]\nv_pk_mul_f32 v[168:169], v[168:169], v[58:59]\nv_exp_f32 v18, v18 clamp\nv_pk_fma_f32 v[164:165], v[166:167], v[52:53], v[164:165]\n"
      "v_pk_fma_f32 v[168:169], v[170:171], v[56:57], v[168:169]\nv_pk_mul_f32 v[52:53], v[52:53], v[54:55]\nv_exp_f32 v19, v19 clamp\nv_pk_mul_f32 v[56:57], v[56:57], v[58:59]\n"
      "v_pk_mul_f32 v[54:55], v[52:53], v[56:57]\nv_rcp_f32 v54, v54\nv_pk_mul_f32 v[164:165], v[164:165], v[56:57]\nv_rcp_f32 v55, v55\nv_pk_fma_f32 v[164:165], v[168:169], v[52:53], v[164:165]\n"
      "v_pk_fma_f32 v[32:33], v[164:165], v[54:55], v[32:33]\nv_add_f32 v36, v32, v33\nv_pk_mul_f32 v[44:45], v[180:181], v[180:181]\nv_pk_mul_f32 v[46:47], v[182:183], v[182:183]\n"
      "v_exp_f32 v20, v20 clamp\nv_pk_mul_f32 v[48:49], v[184:185], v[184:185]\nv_pk_mul_f32 v[50:51], v[186:187], v[186:187]\nv_exp_f32 v21, v21 clamp\n"
      "v_pk_fma_f32 v[44:45], v[44:45], s[58:59], s[58:59] op_sel:[0,0,1] op_sel_hi:[1,0,1]\nv_pk_fma_f32 v[46:47], v[46:47], s[58:59], s[58:59] op_sel:[0,0,1] op_sel_hi:[1,0,1]\n"
      "v_pk_fma_f32 v[48:49], v[48:49], s[58:59], s[58:59] op_sel:[0,0,1] op_sel_hi:[1,0,1]\nv_exp_f32 v22, v22 clamp\n"
      "v_pk_fma_f32 v[50:51], v[50:51], s[58:59], s[58:59] op_sel:[0,0,1] op_sel_hi:[1,0,1]\nv_pk_fma_f32 v[44:45], v[44:45], v[180:181], s[60:61] op_sel_hi:[1,1,0]\n"
      "v_pk_fma_f32 v[46:47], v[46:47], v[182:183], s[60:61] op_sel_hi:[1,1,0]\nv_exp_f32 v23, v23 clamp\nv_pk_fma_f32 v[48:49], v[48:49], v[184:185], s[60:61] op_sel_hi:[1,1,0]\n"
      "v_pk_fma_f32 v[50:51], v[50:51], v[186:187], s[60:61] op_sel_hi:[1,1,0]\nv_exp_f32 v44, v44 clamp\nv_pk_fma_f32 v[18:19], v[18:19], v[2:3], v[26:27]\n"
      "v_pk_fma_f32 v[16:17], v[16:17], v[0:1], v[24:25]\nv_pk_fma_f32 v[20:21], v[20:21], v[4:5], v[28:29]\nv_exp_f32 v45, v45 clamp\nv_pk_fma_f32 v[22:23], v[22:23], v[6:7], v[30:31]\n"
      "v_pk_mul_f32 v[172:173], v[172:173], v[18:19]\nv_pk_mul_f32 v[176:177], v[176:177], v[22:23]\nv_exp_f32 v46, v46 clamp\nv_pk_fma_f32 v[172:173], v[174:175], v[16:17], v[172:173]\n"
      "v_pk_fma_f32 v[176:177], v[178:179], v[20:21], v[176:177]\nv_pk_mul_f32 v[16:17], v[16:17], v[18:19]\nv_exp_f32 v47, v47 clamp\nv_pk_mul_f32 v[20:21], v[20:21], v[22:23]\n"
      "v_pk_mul_f32 v[18:19], v[16:17], v[20:21]\nv_rcp_f32 v18, v18\nv_pk_mul_f32 v[172:173], v[172:173], v[20:21]\nv_rcp_f32 v19, v19\nv_pk_fma_f32 v[172:173], v[176:177], v[16:17], v[172:173]\n"
      "v_pk_mul_f32 v[32:33], v[172:173], v[18:19]\nv_pk_mul_f32 v[52:53], v[188:189], v[188:189]\nv_pk_mul_f32 v[54:55], v[190:191], v[190:191]\nv_exp_f32 v48, v48 clamp\n"
      "v_pk_mul_f32 v[56:57], v[192:193], v[192:193]\nv_pk_mul_f32 v[58:59], v[194:195], v[194:195]\nv_exp_f32 v49, v49 clamp\n"
      "v_pk_fma_f32 v[52:53], v[52:53], s[58:59], s[58:59] op_sel:[0,0,1] op_sel_hi:[1,0,1]\nv_pk_fma_f32 v[54:55], v[54:55], s[58:59], s[58:59] op_sel:[0,0,1] op_sel_hi:[1,0,1]\n"
      "v_pk_fma_f32 v[56:57], v[56:57], s[58:59], s[58:59] op_sel:[0,0,1] op_sel_hi:[1,0,1]\nv_exp_f32 v50, v50 clamp\n"
      "v_pk_fma_f32 v[58:59], v[58:59], s[58:59], s[58:59] op_sel:[0,0,1] op_sel_hi:[1,0,1]\nv_pk_fma_f32 v[52:53], v[52:53], v[188:189], s[60:61] op_sel_hi:[1,1,0]\n"
      "v_pk_fma_f32 v[54:55], v[54:55], v[190:191], s[60:61] op_sel_hi:[1,1,0]\nv_exp_f32 v51, v51 clamp\nv_pk_fma_f32 v[56:57], v[56:57], v[192:193], s[60:61] op_sel_hi:[1,1,0]\n"
      "v_pk_fma_f32 v[58:59], v[58:59], v[194:195], s[60:61] op_sel_hi:[1,1,0]\nv_exp_f32 v52, v52 clamp\nv_pk_fma_f32 v[46:47], v[46:47], v[10:11], v[66:67]\n"
      "v_pk_fma_f32 v[44:45], v[44:45], v[8:9], v[64:65]\nv_pk_fma_f32 v[48:49], v[48:49], v[12:13], v[68:69]\nv_exp_f32 v53, v53 clamp\nv_pk_fma_f32 v[50:51], v[50:51], v[14:15], v[70:71]\n"
      "v_pk_mul_f32 v[180:181], v[180:181], v[46:47]\nv_pk_mul_f32 v[184:185], v[184:185], v[50:51]\nv_exp_f32 v54, v54 clamp\nv_pk_fma_f32 v[180:181], v[182:183], v[44:45], v[180:181]\n"
      "v_pk_fma_f32 v[184:185], v[186:187], v[48:49], v[184:185]\nv_pk_mul_f32 v[44:45], v[44:45], v[46:47]\nv_exp_f32 v55, v55 clamp\nv_pk_mul_f32 v[48:49], v[48:49], v[50:51]\n"
      "v_pk_mul_f32 v[46:47], v[44:45], v[48:49]\nv_rcp_f32 v46, v46\nv_pk_mul_f32 v[180:181], v[180:181], v[48:49]\nv_rcp_f32 v47, v47\nv_pk_fma_f32 v[180:181], v[184:185], v[44:45], v[180:181]\n"
      "v_pk_fma_f32 v[32:33], v[180:181], v[46:47], v[32:33]\nv_add_f32 v37, v32, v33\nv_pk_mul_f32 v[16:17], v[196:197], v[196:197]\nv_pk_mul_f32 v[18:19], v[198:199], v[198:199]\n"
      "v_exp_f32 v56, v56 clamp\nv_pk_mul_f32 v[20:21], v[200:201], v[200:201]\nv_pk_mul_f32 v[22:23], v[202:203], v[202:203]\nv_exp_f32 v57, v57 clamp\n"
      "v_pk_fma_f32 v[16:17], v[16:17], s[58:59], s[58:59] op_sel:[0,0,1] op_sel_hi:[1,0,1]\nv_pk_fma_f32 v[18:19], v[18:19], s[58:59], s[58:59] op_sel:[0,0,1] op_sel_hi:[1,0,1]\n"
      "v_pk_fma_f32 v[20:21], v[20:21], s[58:59], s[58:59] op_sel:[0,0,1] op_sel_hi:[1,0,1]\nv_exp_f32 v58, v58 clamp\n"
      "v_pk_fma_f32 v[22:23], v[22:23], s[58:59], s[58:59] op_sel:[0,0,1] op_sel_hi:[1,0,1]\nv_pk_fma_f32 v[16:17], v[16:17], v[196:197], s[60:61] op_sel_hi:[1,1,0]\n"
      "v_pk_fma_f32 v[18:19], v[18:19], v[198:199], s[60:61] op_sel_hi:[1,1,0]\nv_exp_f32 v59, v59 clamp\nv_pk_fma_f32 v[20:21], v[20:21], v[200:201], s[60:61] op_sel_hi:[1,1,0]\n"
      "v_pk_fma_f32 v[22:23], v[22:23], v[202:203], s[60:61] op_sel_hi:[1,1,0]\nv_exp_f32 v16, v16 clamp\nv_pk_fma_f32 v[54:55], v[54:55], v[2:3], v[26:27]\n"
      "v_pk_fma_f32 v[52:53], v[52:53], v[0:1], v[24:25]\nv_pk_fma_f32 v[56:57], v[56:57], v[4:5], v[28:29]\nv_exp_f32 v17, v17 clamp\nv_pk_fma_f32 v[58:59], v[58:59], v[6:7], v[30:31]\n"
      "v_pk_mul_f32 v[188:189], v[188:189], v[54:55]\nv_pk_mul_f32 v[192:193], v[192:193], v[58:59]\nv_exp_f32 v18, v18 clamp\nv_pk_fma_f32 v[188:189], v[190:191], v[52:53], v[188:189]\n"
      "v_pk_fma_f32 v[192:193], v[194:195], v[56:57], v[192:193]\nv_pk_mul_f32 v[52:53], v[52:53], v[54:55]\nv_exp_f32 v19, v19 clamp\nv_pk_mul_f32 v[56:57], v[56:57], v[58:59]\n"
      "v_pk_mul_f32 v[54:55], v[52:53], v[56:57]\nv_rcp_f32 v54, v54\nv_pk_mul_f32 v[188:189], v[188:189], v[56:57]\nv_rcp_f32 v55, v55\nv_pk_fma_f32 v[188:189], v[192:193], v[52:53], v[188:189]\n"
      "v_pk_mul_f32 v[32:33], v[188:189], v[54:55]\nv_pk_mul_f32 v[44:45], v[204:205], v[204:205]\nv_pk_mul_f32 v[46:47], v[206:207], v[206:207]\nv_exp_f32 v20, v20 clamp\n"
      "v_pk_mul_f32 v[48:49], v[208:209], v[208:209]\nv_pk_mul_f32 v[50:51], v[210:211], v[210:211]\nv_exp_f32 v21, v21 clamp\n"
      "v_pk_fma_f32 v[44:45], v[44:45], s[58:59], s[58:59] op_sel:[0,0,1] op_sel_hi:[1,0,1]\nv_pk_fma_f32 v[46:47], v[46:47], s[58:59], s[58:59] op_sel:[0,0,1] op_sel_hi:[1,0,1]\n"
      "v_pk_fma_f32 v[48:49], v[48:49], s[58:59], s[58:59] op_sel:[0,0,1] op_sel_hi:[1,0,1]\nv_exp_f32 v22, v22 clamp\n"
      "v_pk_fma_f32 v[50:51], v[50:51], s[58:59], s[58:59] op_sel:[0,0,1] op_sel_hi:[1,0,1]\nv_pk_fma_f32 v[44:45], v[44:45], v[204:205], s[60:61] op_sel_hi:[1,1,0]\n"
      "v_pk_fma_f32 v[46:47], v[46:47], v[206:207], s[60:61] op_sel_hi:[1,1,0]\nv_exp_f32 v23, v23 clamp\nv_pk_fma_f32 v[48:49], v[48:49], v[208:209], s[60:61] op_sel_hi:[1,1,0]\n"
      "v_pk_fma_f32 v[50:51], v[50:51], v[210:211], s[60:61] op_sel_hi:[1,1,0]\nv_exp_f32 v44, v44 clamp\nv_pk_fma_f32 v[18:19], v[18:19], v[10:11], v[66:67]\n"
      "v_pk_fma_f32 v[16:17], v[16:17], v[8:9], v[64:65]\nv_pk_fma_f32 v[20:21], v[20:21], v[12:13], v[68:69]\nv_exp_f32 v45, v45 clamp\nv_pk_fma_f32 v[22:23], v[22:23], v[14:15], v[70:71]\n"
      "v_pk_mul_f32 v[196:197], v[196:197], v[18:19]\nv_pk_mul_f32 v[200:201], v[200:201], v[22:23]\nv_exp_f32 v46, v46 clamp\nv_pk_fma_f32 v[196:197], v[198:199], v[16:17], v[196:197]\n"
      "v_pk_fma_f32 v[200:201], v[202:203], v[20:21], v[200:201]\nv_pk_mul_f32 v[16:17], v[16:17], v[18:19]\nv_exp_f32 v47, v47 clamp\nv_pk_mul_f32 v[20:21], v[20:21], v[22:23]\n"
      "v_pk_mul_f32 v[18:19], v[16:17], v[20:21]\nv_rcp_f32 v18, v18\nv_pk_mul_f32 v[196:197], v[196:197], v[20:21]\nv_rcp_f32 v19, v19\nv_pk_fma_f32 v[196:197], v[200:201], v[16:17], v[196:197]\n"
      "v_pk_fma_f32 v[32:33], v[196:197], v[18:19], v[32:33]\nv_add_f32 v38, v32, v33\nv_pk_mul_f32 v[52:53], v[212:213], v[212:213]\nv_pk_mul_f32 v[54:55], v[214:215], v[214:215]\n"
      "v_exp_f32 v48, v48 clamp\nv_pk_mul_f32 v[56:57], v[216:217], v[216:217]\nv_pk_mul_f32 v[58:59], v[218:219], v[218:219]\nv_exp_f32 v49, v49 clamp\n"
      "v_pk_fma_f32 v[52:53], v[52:53], s[58:59], s[58:59] op_sel:[0,0,1] op_sel_hi:[1,0,1]\nv_pk_fma_f32 v[54:55], v[54:55], s[58:59], s[58:59] op_sel:[0,0,1] op_sel_hi:[1,0,1]\n"
      "v_pk_fma_f32 v[56:57], v[56:57], s[58:59], s[58:59] op_sel:[0,0,1] op_sel_hi:[1,0,1]\nv_exp_f32 v50, v50 clamp\n"
      "v_pk_fma_f32 v[58:59], v[58:59], s[58:59], s[58:59] op_sel:[0,0,1] op_sel_hi:[1,0,1]\nv_pk_fma_f32 v[52:53], v[52:53], v[212:213], s[60:61] op_sel_hi:[1,1,0]\n"
      "v_pk_fma_f32 v[54:55], v[54:55], v[214:215], s[60:61] op_sel_hi:[1,1,0]\nv_exp_f32 v51, v51 clamp\nv_pk_fma_f32 v[56:57], v[56:57], v[216:217], s[60:61] op_sel_hi:[1,1,0]\n"
      "v_pk_fma_f32 v[58:59], v[58:59], v[218:219], s[60:61] op_sel_hi:[1,1,0]\nv_exp_f32 v52, v52 clamp\nv_pk_fma_f32 v[46:47], v[46:47], v[2:3], v[26:27]\n"
      "v_pk_fma_f32 v[44:45], v[44:45], v[0:1], v[24:25]\nv_pk_fma_f32 v[48:49], v[48:49], v[4:5], v[28:29]\nv_exp_f32 v53, v53 clamp\nv_pk_fma_f32 v[50:51], v[50:51], v[6:7], v[30:31]\n"
      "v_pk_mul_f32 v[204:205], v[204:205], v[46:47]\nv_pk_mul_f32 v[208:209], v[208:209], v[50:51]\nv_exp_f32 v54, v54 clamp\nv_pk_fma_f32 v[204:205], v[206:207], v[44:45], v[204:205]\n"
      "v_pk_fma_f32 v[208:209], v[210:211], v[48:49], v[208:209]\nv_pk_mul_f32 v[44:45], v[44:45], v[46:47]\nv_exp_f32 v55, v55 clamp\nv_pk_mul_f32 v[48:49], v[48:49], v[50:51]\n"
      "v_pk_mul_f32 v[46:47], v[44:45], v[48:49]\nv_rcp_f32 v46, v46\nv_pk_mul_f32 v[204:205], v[204:205], v[48:49]\nv_rcp_f32 v47, v47\nv_pk_fma_f32 v[204:205], v[208:209], v[44:45], v[204:205]\n"
      "v_pk_mul_f32 v[32:33], v[204:205], v[46:47]\nv_pk_mul_f32 v[16:17], v[220:221], v[220:221]\nv_pk_mul_f32 v[18:19], v[222:223], v[222:223]\nv_exp_f32 v56, v56 clamp\n"
      "v_pk_mul_f32 v[20:21], v[224:225], v[224:225]\nv_pk_mul_f32 v[22:23], v[226:227], v[226:227]\nv_exp_f32 v57, v57 clamp\n"
      "v_pk_fma_f32 v[16:17], v[16:17], s[58:59], s[58:59] op_sel:[0,0,1] op_sel_hi:[1,0,1]\nv_pk_fma_f32 v[18:19], v[18:19], s[58:59], s[58:59] op_sel:[0,0,1] op_sel_hi:[1,0,1]\n"
      "v_pk_fma_f32 v[20:21], v[20:21], s[58:59], s[58:59] op_sel:[0,0,1] op_sel_hi:[1,0,1]\nv_exp_f32 v58, v58 clamp\n"
      "v_pk_fma_f32 v[22:23], v[22:23], s[58:59], s[58:59] op_sel:[0,0,1] op_sel_hi:[1,0,1]\nv_pk_fma_f32 v[16:17], v[16:17], v[220:221], s[60:61] op_sel_hi:[1,1,0]\n"
      "v_pk_fma_f32 v[18:19], v[18:19], v[222:223], s[60:61] op_sel_hi:[1,1,0]\nv_exp_f32 v59, v59 clamp\nv_pk_fma_f32 v[20:21], v[20:21], v[224:225], s[60:61] op_sel_hi:[1,1,0]\n"
      "v_pk_fma_f32 v[22:23], v[22:23], v[226:227], s[60:61] op_sel_hi:[1,1,0]\nv_exp_f32 v16, v16 clamp\nv_pk_fma_f32 v[54:55], v[54:55], v[10:11], v[66:67]\n"
      "v_pk_fma_f32 v[52:53], v[52:53], v[8:9], v[64:65]\nv_pk_fma_f32 v[56:57], v[56:57], v[12:13], v[68:69]\nv_exp_f32 v17, v17 clamp\nv_pk_fma_f32 v[58:59], v[58:59], v[14:15], v[70:71]\n"
      "v_pk_mul_f32 v[212:213], v[212:213], v[54:55]\nv_pk_mul_f32 v[216:217], v[216:217], v[58:59]\nv_exp_f32 v18, v18 clamp\nv_pk_fma_f32 v[212:213], v[214:215], v[52:53], v[212:213]\n"
      "v_pk_fma_f32 v[216:217], v[218:219], v[56:57], v[216:217]\nv_pk_mul_f32 v[52:53], v[52:53], v[54:55]\nv_exp_f32 v19, v19 clamp\nv_pk_mul_f32 v[56:57], v[56:57], v[58:59]\n"
      "v_pk_mul_f32 v[54:55], v[52:53], v[56:57]\nv_rcp_f32 v54, v54\nv_pk_mul_f32 v[212:213], v[212:213], v[56:57]\nv_rcp_f32 v55, v55\nv_pk_fma_f32 v[212:213], v[216:217], v[52:53], v[212:213]\n"
      "v_pk_fma_f32 v[32:33], v[212:213], v[54:55], v[32:33]\nv_add_f32 v39, v32, v33\nv_pk_mul_f32 v[44:45], v[228:229], v[228:229]\nv_pk_mul_f32 v[46:47], v[230:231], v[230:231]\n"
      "v_exp_f32 v20, v20 clamp\nv_pk_mul_f32 v[48:49], v[232:233], v[232:233]\nv_pk_mul_f32 v[50:51], v[234:235], v[234:235]\nv_exp_f32 v21, v21 clamp\n"
      "v_pk_fma_f32 v[44:45], v[44:45], s[58:59], s[58:59] op_sel:[0,0,1] op_sel_hi:[1,0,1]\nv_pk_fma_f32 v[46:47], v[46:47], s[58:59], s[58:59] op_sel:[0,0,1] op_sel_hi:[1,0,1]\n"
      "v_pk_fma_f32 v[48:49], v[48:49], s[58:59], s[58:59] op_sel:[0,0,1] op_sel_hi:[1,0,1]\nv_exp_f32 v22, v22 clamp\n"
      "v_pk_fma_f32 v[50:51], v[50:51], s[58:59], s[58:59] op_sel:[0,0,1] op_sel_hi:[1,0,1]\nv_pk_fma_f32 v[44:45], v[44:45], v[228:229], s[60:61] op_sel_hi:[1,1,0]\n"
      "v_pk_fma_f32 v[46:47], v[46:47], v[230:231], s[60:61] op_sel_hi:[1,1,0]\nv_exp_f32 v23, v23 clamp\nv_pk_fma_f32 v[48:49], v[48:49], v[232:233], s[60:61] op_sel_hi:[1,1,0]\n"
      "v_pk_fma_f32 v[50:51], v[50:51], v[234:235], s[60:61] op_sel_hi:[1,1,0]\nv_exp_f32 v44, v44 clamp\nv_pk_fma_f32 v[18:19], v[18:19], v[2:3], v[26:27]\nv_exp_f32 v45, v45 clamp\n"
      "v_pk_fma_f32 v[16:17], v[16:17], v[0:1], v[24:25]\nv_pk_fma_f32 v[20:21], v[20:21], v[4:5], v[28:29]\nv_exp_f32 v46, v46 clamp\nv_pk_fma_f32 v[22:23], v[22:23], v[6:7], v[30:31]\n"
      "v_pk_mul_f32 v[220:221], v[220:221], v[18:19]\nv_exp_f32 v47, v47 clamp\nv_pk_mul_f32 v[224:225], v[224:225], v[22:23]\nv_pk_fma_f32 v[220:221], v[222:223], v[16:17], v[220:221]\n"
      "v_exp_f32 v48, v48 clamp\nv_pk_fma_f32 v[224:225], v[226:227], v[20:21], v[224:225]\nv_exp_f32 v49, v49 clamp\nv_pk_mul_f32 v[16:17], v[16:17], v[18:19]\n"
      "v_pk_mul_f32 v[20:21], v[20:21], v[22:23]\nv_exp_f32 v50, v50 clamp\nv_pk_mul_f32 v[18:19], v[16:17], v[20:21]\nv_rcp_f32 v18, v18\nv_pk_mul_f32 v[220:221], v[220:221], v[20:21]\n"
      "v_exp_f32 v51, v51 clamp\nv_rcp_f32 v19, v19\nv_pk_fma_f32 v[220:221], v[224:225], v[16:17], v[220:221]\nv_pk_mul_f32 v[32:33], v[220:221], v[18:19]\n"
      "v_pk_fma_f32 v[46:47], v[46:47], v[10:11], v[66:67]\nv_pk_fma_f32 v[44:45], v[44:45], v[8:9], v[64:65]\nv_pk_fma_f32 v[48:49], v[48:49], v[12:13], v[68:69]\n"
      "v_pk_fma_f32 v[50:51], v[50:51], v[14:15], v[70:71]\nv_pk_mul_f32 v[228:229], v[228:229], v[46:47]\nv_pk_mul_f32 v[232:233], v[232:233], v[50:51]\n"
      "v_pk_fma_f32 v[228:229], v[230:231], v[44:45], v[228:229]\nv_pk_fma_f32 v[232:233], v[234:235], v[48:49], v[232:233]\nv_pk_mul_f32 v[44:45], v[44:45], v[46:47]\n"
      "v_pk_mul_f32 v[48:49], v[48:49], v[50:51]\nv_pk_mul_f32 v[46:47], v[44:45], v[48:49]\nv_rcp_f32 v46, v46\nv_pk_mul_f32 v[228:229], v[228:229], v[48:49]\nv_rcp_f32 v47, v47\n"
      "v_pk_fma_f32 v[228:229], v[232:233], v[44:45], v[228:229]\nv_pk_fma_f32 v[32:33], v[228:229], v[46:47], v[32:33]\nv_add_f32 v40, v32, v33\ns_barrier\n"
      ".Lge:\nv_mov_b32 v43, v42\nds_read_b128 v[48:51], v246\nds_read_b128 v[52:55], v246 offset:16\nds_read_b64_tr_b16 v[68:69], v238 offset:0\nds_read_b64_tr_b16 v[70:71], v239 offset:0\n"
      "ds_read_b64_tr_b16 v[56:57], v236 offset:0\nds_read_b64_tr_b16 v[58:59], v237 offset:128\nds_read_b64_tr_b16 v[60:61], v237 offset:256\nds_read_b64_tr_b16 v[62:63], v237 offset:384\n"
      "ds_read_b64_tr_b16 v[64:65], v237 offset:512\nds_read_b64_tr_b16 v[66:67], v237 offset:640\nds_read_b128 v[44:47], v248 offset:48\nv_permlane32_swap_b32 v36, v38\n"
      "v_permlane32_swap_b32 v37, v39\nv_permlane32_swap_b32 v40, v42\nv_permlane32_swap_b32 v41, v43\nv_add_f32 v36, v36, v38\nv_add_f32 v37, v37, v39\nv_add_f32 v40, v40, v42\n"
      "v_add_f32 v41, v41, v43\ns_nop 0\nv_permlane16_swap_b32 v36, v37\nv_permlane16_swap_b32 v40, v41\nv_add_f32 v36, v36, v37\nv_add_f32 v40, v40, v41\nds_write_b32 v254, v36\n"
      "ds_write_b32 v255, v40 offset:256\nds_add_rtn_u32 v2, v253, v241 offset:20\ns_waitcnt lgkmcnt(11)\nv_readlane_b32 s48, v69, 48\ns_mov_b32 s56, 0\ns_mov_b32 s44, 0\ns_mov_b32 s33, s45\n"
      "s_waitcnt lgkmcnt(10)\nv_pk_mul_f16 v20, v68, v70\nv_pk_mul_f16 v21, v69, v71\ns_waitcnt lgkmcnt(9)\nv_pk_mul_f16 v22, v56, v56 op_sel_hi:[0,1]\nv_pk_mul_f16 v23, v56, v57 op_sel_hi:[0,1]\n"
      "v_pk_mul_f16 v16, v68, s48 op_sel:[0,1]\nv_pk_mul_f16 v17, v69, s48 op_sel:[0,1]\nv_pk_mul_f16 v18, v70, s48 op_sel:[0,1]\nv_pk_mul_f16 v19, v71, s48 op_sel:[0,1]\ns_nop 1\n"
      "v_mfma_f32_16x16x32_f16 v[40:43], v[76:79], v[16:19], 0\nv_mfma_f32_16x16x32_f16 v[40:43], v[88:91], v[20:23], v[40:43]\ns_add_u32 s45, s45, 2\ns_cmp_ge_u32 s45, s46\ns_cbranch_scc1 .Lc_tg\n"
      ".Lc_bg:\nv_mfma_f32_16x16x32_f16 v[36:39], v[84:87], v[20:23], 0\ns_waitcnt lgkmcnt(4)\nv_pk_mul_f16 v24, v66, v66 op_sel_hi:[0,1]\nv_pk_mul_f16 v25, v66, v57 op_sel_hi:[0,1]\n"
      "ds_read_b128 v[124:127], v252 offset:13312\nv_mfma_f32_16x16x32_f16 v[36:39], v[72:75], v[16:19], v[36:39]\nv_pk_mul_f16 v26, v56, v66 op_sel_hi:[0,1]\n"
      "v_pk_mul_f16 v27, v56, v67 op_sel_hi:[0,1]\nds_read_b128 v[128:131], v252 offset:14336\nv_mfma_f32_16x16x32_f16 v[32:35], v[80:83], v[20:23], 0\nv_pk_mul_f16 v28, v58, v66 op_sel_hi:[0,1]\n"
      "v_pk_mul_f16 v29, v58, v67 op_sel_hi:[0,1]\nds_read_b128 v[132:135], v252 offset:15360\ns_waitcnt lgkmcnt(6)\nv_smfmac_f32_16x16x64_f16 v[44:47], v[92:95], v[16:23], v242\n"
      "v_pk_mul_f16 v30, v60, v66 op_sel_hi:[0,1]\nv_pk_mul_f16 v31, v60, v67 op_sel_hi:[0,1]\nds_read_b128 v[136:139], v252 offset:16384\n"
      "v_mfma_f32_16x16x32_f16 v[32:35], v[96:99], v[24:27], v[32:35]\nv_pk_mul_f16 v16, v62, v66 op_sel_hi:[0,1]\nv_pk_mul_f16 v17, v62, v67 op_sel_hi:[0,1]\n"
      "ds_read_b128 v[140:143], v252 offset:17408\nv_mfma_f32_16x16x32_f16 v[32:35], v[108:111], v[28:31], v[32:35]\nv_pk_mul_f16 v18, v64, v66 op_sel_hi:[0,1]\n"
      "ds_read_b128 v[144:147], v252 offset:18432\nv_mfma_f32_16x16x32_f16 v[36:39], v[112:115], v[28:31], v[36:39]\nv_pk_mul_f16 v19, v64, v67 op_sel_hi:[0,1]\ns_cmp_ge_u32 s45, s46\n"
      "s_cbranch_scc1 .Lc_th\n"
      ".Lc_bh:\nds_read_b128 v[148:151], v252 offset:19456\nv_mfma_f32_16x16x32_f16 v[36:39], v[100:103], v[24:27], v[36:39]\nv_pk_mul_f16 v20, v64, v64 op_sel_hi:[0,1]\n"
      "ds_read_b128 v[152:155], v252 offset:20480\nv_mfma_f32_16x16x32_f16 v[40:43], v[104:107], v[24:27], v[40:43]\nv_pk_mul_f16 v21, v64, v57 op_sel_hi:[0,1]\n"
      "ds_read_b128 v[156:159], v252 offset:21504\nv_mfma_f32_16x16x32_f16 v[40:43], v[116:119], v[28:31], v[40:43]\nv_pk_mul_f16 v22, v56, v64 op_sel_hi:[0,1]\n"
      "ds_read_b128 v[160:163], v252 offset:22528\nv_smfmac_f32_16x16x64_f16 v[44:47], v[120:123], v[24:31], v242\nv_pk_mul_f16 v23, v56, v65 op_sel_hi:[0,1]\n"
      "ds_read_b128 v[164:167], v252 offset:23552\ns_waitcnt lgkmcnt(10)\nv_mfma_f32_16x16x32_f16 v[32:35], v[124:127], v[16:19], v[32:35]\nv_pk_mul_f16 v24, v58, v64 op_sel_hi:[0,1]\n"
      "v_pk_mul_f16 v25, v58, v65 op_sel_hi:[0,1]\nds_read_b64_tr_b16 v[70:71], v239 offset:1856\nds_read_b128 v[168:171], v252 offset:24576\ns_waitcnt lgkmcnt(9)\n"
      "v_mfma_f32_16x16x32_f16 v[32:35], v[136:139], v[20:23], v[32:35]\nv_pk_mul_f16 v26, v60, v64 op_sel_hi:[0,1]\nds_read_b64_tr_b16 v[68:69], v238 offset:1856\n"
      "ds_read_b128 v[172:175], v252 offset:25600\ns_waitcnt lgkmcnt(10)\nv_mfma_f32_16x16x32_f16 v[36:39], v[140:143], v[20:23], v[36:39]\nv_pk_mul_f16 v27, v60, v65 op_sel_hi:[0,1]\n"
      "ds_read_b64_tr_b16 v[66:67], v237 offset:2496\nds_read_b128 v[176:179], v252 offset:26624\nv_mfma_f32_16x16x32_f16 v[36:39], v[128:131], v[16:19], v[36:39]\n"
      "v_pk_mul_f16 v28, v62, v64 op_sel_hi:[0,1]\nds_read_b128 v[180:183], v252 offset:27648\nv_mfma_f32_16x16x32_f16 v[40:43], v[132:135], v[16:19], v[40:43]\n"
      "v_pk_mul_f16 v29, v62, v65 op_sel_hi:[0,1]\nds_read_b128 v[184:187], v252 offset:28672\ns_waitcnt lgkmcnt(13)\nv_mfma_f32_16x16x32_f16 v[40:43], v[144:147], v[20:23], v[40:43]\n"
      "v_pk_mul_f16 v30, v62, v62 op_sel_hi:[0,1]\nds_read_b128 v[188:191], v252 offset:29696\ns_waitcnt lgkmcnt(13)\nv_smfmac_f32_16x16x64_f16 v[44:47], v[148:151], v[16:23], v242\n"
      "v_pk_mul_f16 v31, v62, v57 op_sel_hi:[0,1]\nds_read_b128 v[192:195], v252 offset:30720\ns_waitcnt lgkmcnt(13)\nv_mfma_f32_16x16x32_f16 v[32:35], v[152:155], v[24:27], v[32:35]\n"
      "v_pk_mul_f16 v16, v56, v62 op_sel_hi:[0,1]\nv_pk_mul_f16 v17, v56, v63 op_sel_hi:[0,1]\nds_read_b128 v[196:199], v252 offset:31744\ns_waitcnt lgkmcnt(11)\n"
      "v_mfma_f32_16x16x32_f16 v[32:35], v[164:167], v[28:31], v[32:35]\nv_pk_mul_f16 v18, v58, v62 op_sel_hi:[0,1]\nds_read_b128 v[200:203], v252 offset:32768\n"
      "ds_read_b64_tr_b16 v[64:65], v237 offset:2368\ns_waitcnt lgkmcnt(11)\nv_mfma_f32_16x16x32_f16 v[36:39], v[168:171], v[28:31], v[36:39]\nv_pk_mul_f16 v19, v58, v63 op_sel_hi:[0,1]\n"
      "ds_read_b128 v[204:207], v252 offset:33792\nv_mfma_f32_16x16x32_f16 v[36:39], v[156:159], v[24:27], v[36:39]\nv_pk_mul_f16 v20, v60, v62 op_sel_hi:[0,1]\n"
      "ds_read_b128 v[208:211], v252 offset:34816\nv_mfma_f32_16x16x32_f16 v[40:43], v[160:163], v[24:27], v[40:43]\nv_pk_mul_f16 v21, v60, v63 op_sel_hi:[0,1]\n"
      "ds_read_b128 v[212:215], v252 offset:35840\ns_waitcnt lgkmcnt(13)\nv_readlane_b32 s48, v69, 48\ns_waitcnt lgkmcnt(12)\nv_mfma_f32_16x16x32_f16 v[40:43], v[172:175], v[28:31], v[40:43]\n"
      "v_pk_mul_f16 v22, v60, v60 op_sel_hi:[0,1]\nds_read_b128 v[216:219], v252 offset:36864\ns_waitcnt lgkmcnt(11)\nv_smfmac_f32_16x16x64_f16 v[44:47], v[176:179], v[24:31], v242\n"
      "v_pk_mul_f16 v23, v60, v57 op_sel_hi:[0,1]\nds_read_b128 v[220:223], v252 offset:37888\ns_waitcnt lgkmcnt(11)\nv_mfma_f32_16x16x32_f16 v[32:35], v[180:183], v[16:19], v[32:35]\n"
      "v_pk_mul_f16 v24, v56, v60 op_sel_hi:[0,1]\nv_pk_mul_f16 v25, v56, v61 op_sel_hi:[0,1]\nds_read_b128 v[224:227], v252 offset:38912\ns_waitcnt lgkmcnt(9)\n"
      "v_mfma_f32_16x16x32_f16 v[32:35], v[192:195], v[20:23], v[32:35]\nv_pk_mul_f16 v26, v58, v60 op_sel_hi:[0,1]\nv_pk_mul_f16 v27, v58, v61 op_sel_hi:[0,1]\n"
      "ds_read_b128 v[228:231], v252 offset:39936\ns_waitcnt lgkmcnt(9)\nv_mfma_f32_16x16x32_f16 v[36:39], v[196:199], v[20:23], v[36:39]\nv_pk_mul_f16 v28, v58, v58 op_sel_hi:[0,1]\n"
      "ds_read_b128 v[232:235], v252 offset:40960\nds_read_b64_tr_b16 v[62:63], v237 offset:2240\nv_mfma_f32_16x16x32_f16 v[36:39], v[184:187], v[16:19], v[36:39]\n"
      "v_pk_mul_f16 v29, v58, v57 op_sel_hi:[0,1]\nds_read_b64_tr_b16 v[60:61], v237 offset:2112\nv_mfma_f32_16x16x32_f16 v[40:43], v[188:191], v[16:19], v[40:43]\n"
      "v_pk_mul_f16 v30, v56, v58 op_sel_hi:[0,1]\ns_waitcnt lgkmcnt(11)\nv_mfma_f32_16x16x32_f16 v[40:43], v[200:203], v[20:23], v[40:43]\nv_pk_mul_f16 v31, v56, v59 op_sel_hi:[0,1]\n"
      "s_waitcnt lgkmcnt(9)\nv_smfmac_f32_16x16x64_f16 v[44:47], v[204:207], v[16:23], v242\nds_read_b64_tr_b16 v[56:57], v236 offset:1856\nds_read_b64_tr_b16 v[58:59], v237 offset:1984\n"
      "s_waitcnt lgkmcnt(4)\nv_smfmac_f32_16x16x64_f16 v[44:47], v[232:235], v[24:31], v242\nv_pk_mul_f16 v16, v68, s48 op_sel:[0,1]\nv_pk_mul_f16 v17, v69, s48 op_sel:[0,1]\n"
      "v_mfma_f32_16x16x32_f16 v[32:35], v[208:211], v[24:27], v[32:35]\nv_pk_mul_f16 v18, v70, s48 op_sel:[0,1]\nv_pk_mul_f16 v19, v71, s48 op_sel:[0,1]\n"
      "v_mfma_f32_16x16x32_f16 v[32:35], v[220:223], v[28:31], v[32:35]\nv_pk_mul_f16 v20, v68, v70\nv_pk_mul_f16 v21, v69, v71\nv_mfma_f32_16x16x32_f16 v[36:39], v[224:227], v[28:31], v[36:39]\n"
      "v_add_f32 v44, v44, v45\nv_add_f32 v46, v46, v47\nv_mfma_f32_16x16x32_f16 v[36:39], v[212:215], v[24:27], v[36:39]\nv_mul_f32 v55, v44, v54\nv_mul_f32 v55, v55, v46\n"
      "ds_read_b128 v[44:47], v248 offset:48\nv_mfma_f32_16x16x32_f16 v[40:43], v[216:219], v[24:27], v[40:43]\ns_waitcnt lgkmcnt(2)\nv_pk_mul_f16 v22, v56, v56 op_sel_hi:[0,1]\n"
      "v_pk_mul_f16 v23, v56, v57 op_sel_hi:[0,1]\nv_mfma_f32_16x16x32_f16 v[40:43], v[228:231], v[28:31], v[40:43]\nv_mul_f32 v32, v32, v48\nv_mul_f32 v33, v33, v49\nv_mul_f32 v34, v34, v50\n"
      "s_waitcnt lgkmcnt(1)\ns_branch .Lc_u1\n"
      ".Lfeed:\ns_waitcnt vmcnt(7) lgkmcnt(0)\ns_cbranch_vccnz .Lw1004\ns_waitcnt vmcnt(6)\n"
      ".Lw1004:\ns_barrier\nds_read_b64 v[44:45], v30 offset:45056\ns_add_u32 m0, s54, 0\ns_nop 0\nglobal_load_lds_dwordx4 v236, s[50:51] offset:0\n"
      "global_load_lds_dwordx4 v237, s[50:51] offset:1024\nglobal_load_lds_dwordx4 v238, s[50:51] offset:2048\nglobal_load_lds_dwordx4 v239, s[50:51] offset:3072\ns_add_u32 s50, s50, 212992\n"
      "s_addc_u32 s51, s51, 0\ns_add_u32 m0, s26, 20480\ns_nop 0\nglobal_load_lds_dwordx4 v27, s[6:7] offset:0\nv_lshl_add_u32 v27, v197, 7, v4\ns_cbranch_vccz .La1003\ns_add_u32 m0, s26, 24576\n"
      "s_nop 0\nglobal_load_lds_dwordx4 v28, s[6:7] offset:0\n"
      ".La1003:\nv_lshl_add_u32 v28, v203, 7, v4\nds_read2_b64 v[200:203], v9 offset0:2 offset1:3\ns_add_u32 m0, s53, 0\ns_nop 0\nglobal_load_lds_dwordx4 v22, s[6:7] offset:0\n"
      "v_lshl_add_u32 v22, v179, 7, v4\nds_read2_b64 v[176:179], v6 offset0:2 offset1:3\nglobal_load_lds_dwordx4 v23, s[44:45] offset:1024\nv_lshl_add_u32 v23, v181, 7, v5\n"
      "global_load_lds_dwordx4 v24, s[46:47] offset:2048\nv_lshl_add_u32 v24, v187, 7, v4\nds_read2_b64 v[184:187], v7 offset0:2 offset1:3\nglobal_load_lds_dwordx4 v25, s[48:49] offset:3072\n"
      "v_lshl_add_u32 v25, v189, 7, v5\ns_add_u32 m0, s53, 4096\ns_nop 0\nglobal_load_lds_dwordx4 v26, s[6:7] offset:0\nv_lshl_add_u32 v26, v195, 7, v4\n"
      "ds_read2_b64 v[192:195], v8 offset0:2 offset1:3\ns_waitcnt vmcnt(11) lgkmcnt(0)\ns_cbranch_vccnz .Lw1006\ns_waitcnt vmcnt(10)\n"
      ".Lw1006:\ns_barrier\ns_add_u32 m0, s54, 45056\ns_nop 0\nglobal_load_lds_dwordx4 v236, s[50:51] offset:0\nds_read_b64 v[46:47], v31 offset:0\n"
      "global_load_lds_dwordx4 v237, s[50:51] offset:1024\nglobal_load_lds_dwordx4 v238, s[50:51] offset:2048\nglobal_load_lds_dwordx4 v239, s[50:51] offset:3072\ns_add_u32 s50, s50, 212992\n"
      "s_addc_u32 s51, s51, 0\ns_cmp_eq_u32 s27, 22\ns_cselect_b64 s[50:51], s[10:11], s[50:51]\ns_add_u32 m0, s26, 65536\nv_max3_f16 v32, |v44|, |v45|, v32\n"
      "global_load_lds_dwordx4 v27, s[6:7] offset:0\nv_lshl_add_u32 v27, v198, 7, v4\nv_max3_f32 v33, |v44|, |v45|, v33\ns_cbranch_vccz .La1007\ns_add_u32 m0, s26, 69632\ns_nop 0\n"
      "global_load_lds_dwordx4 v28, s[6:7] offset:0\n"
      ".La1007:\nv_lshl_add_u32 v28, v200, 7, v4\ns_add_u32 m0, s53, 45056\nv_and_b32 v96, s55, v44\nglobal_load_lds_dwordx4 v22, s[6:7] offset:0\nv_lshl_add_u32 v22, v176, 7, v4\n"
      "v_xor_b32 v44, v44, v96\nglobal_load_lds_dwordx4 v23, s[44:45] offset:1024\nv_lshl_add_u32 v23, v182, 7, v5\nv_and_b32 v97, s55, v45\nglobal_load_lds_dwordx4 v24, s[46:47] offset:2048\n"
      "v_lshl_add_u32 v24, v184, 7, v4\nv_xor_b32 v45, v45, v97\nglobal_load_lds_dwordx4 v25, s[48:49] offset:3072\ns_add_u32 m0, s53, 49152\nv_lshl_add_u32 v25, v190, 7, v5\n"
      "global_load_lds_dwordx4 v26, s[6:7] offset:0\nv_lshl_add_u32 v26, v192, 7, v4\ns_cmp_lg_u32 s31, 0\ns_cbranch_scc1 .Lb1008\ns_lshl_b32 s42, s24, 9\nv_lshl_add_u32 v39, v240, 3, s42\n"
      "s_and_b32 s42, s2, 7\ns_lshl_b32 s42, s42, 11\ns_add_u32 s42, s42, 4096\ns_add_u32 s58, s20, s42\ns_addc_u32 s59, s21, 0\nglobal_store_dwordx2 v39, v[254:255], s[58:59] sc1\n"
      ".Lb1008:\ns_waitcnt vmcnt(11) lgkmcnt(0)\ns_cbranch_vccnz .Lw1009\ns_waitcnt vmcnt(10)\n"
      ".Lw1009:\ns_barrier\ns_add_u32 m0, s54, 90112\ns_nop 0\nglobal_load_lds_dwordx4 v236, s[50:51] offset:0\nds_read_b64 v[48:49], v30 offset:0\n"
      "global_load_lds_dwordx4 v237, s[50:51] offset:1024\nglobal_load_lds_dwordx4 v238, s[50:51] offset:2048\nglobal_load_lds_dwordx4 v239, s[50:51] offset:3072\ns_add_u32 s50, s50, 212992\n"
      "s_addc_u32 s51, s51, 0\ns_add_u32 m0, s26, 110592\nv_max3_f16 v32, |v46|, |v47|, v32\nglobal_load_lds_dwordx4 v27, s[6:7] offset:0\nv_lshl_add_u32 v27, v199, 7, v4\n"
      "ds_read2_b64 v[196:199], v8 offset0:115 offset1:116\nv_max3_f32 v33, |v46|, |v47|, v33\ns_cbranch_vccz .La1010\ns_add_u32 m0, s26, 114688\ns_nop 0\n"
      "global_load_lds_dwordx4 v28, s[6:7] offset:0\n"
      ".La1010:\nv_lshl_add_u32 v28, v201, 7, v4\ns_add_u32 m0, s53, 90112\nv_and_b32 v98, s55, v46\nglobal_load_lds_dwordx4 v22, s[6:7] offset:0\nv_lshl_add_u32 v22, v177, 7, v4\n"
      "v_xor_b32 v46, v46, v98\nglobal_load_lds_dwordx4 v23, s[44:45] offset:1024\nv_lshl_add_u32 v23, v183, 7, v5\nds_read2_b64 v[180:183], v6 offset0:115 offset1:116\nv_and_b32 v99, s55, v47\n"
      "global_load_lds_dwordx4 v24, s[46:47] offset:2048\nv_lshl_add_u32 v24, v185, 7, v4\nv_xor_b32 v47, v47, v99\nglobal_load_lds_dwordx4 v25, s[48:49] offset:3072\n"
      "v_lshl_add_u32 v25, v191, 7, v5\ns_add_u32 m0, s53, 94208\nds_read2_b64 v[188:191], v7 offset0:115 offset1:116\nglobal_load_lds_dwordx4 v26, s[6:7] offset:0\n"
      "v_lshl_add_u32 v26, v193, 7, v4\ns_cmp_lg_u32 s24, 2\ns_cbranch_scc1 .Lo1011\ns_getpc_b64 s[62:63]\ns_add_u32 s62, s62, _ZL5g_tab@rel32@lo+2052\n"
      "s_addc_u32 s63, s63, _ZL5g_tab@rel32@hi+2060\ns_lshr_b32 s42, s2, 3\ns_lshl1_add_u32 s42, s2, s42\ns_and_b32 s42, s42, 15\ns_lshl_b32 s42, s42, 7\nv_lshl_add_u32 v40, v240, 4, s42\n"
      "s_mov_b32 m0, 162112\ns_mov_b64 exec, 0xff\nglobal_load_lds_dwordx4 v40, s[62:63]\ns_mov_b64 exec, -1\n"
      ".Lo1011:\ns_waitcnt vmcnt(11) lgkmcnt(0)\ns_cbranch_vccnz .Lw1012\ns_waitcnt vmcnt(10)\n"
      ".Lw1012:\ns_barrier\ns_add_u32 m0, s54, 0\ns_nop 0\nglobal_load_lds_dwordx4 v236, s[50:51] offset:0\nds_read_b64 v[50:51], v30 offset:45056\n"
      "global_load_lds_dwordx4 v237, s[50:51] offset:1024\nglobal_load_lds_dwordx4 v238, s[50:51] offset:2048\nglobal_load_lds_dwordx4 v239, s[50:51] offset:3072\ns_add_u32 s50, s50, 212992\n"
      "s_addc_u32 s51, s51, 0\ns_cmp_eq_u32 s27, 20\ns_cselect_b64 s[50:51], s[10:11], s[50:51]\ns_add_u32 m0, s26, 20480\nv_max3_f16 v32, |v48|, |v49|, v32\n"
      "global_load_lds_dwordx4 v27, s[6:7] offset:0\nv_lshl_add_u32 v27, v196, 7, v4\nv_max3_f32 v33, |v48|, |v49|, v33\ns_cbranch_vccz .La1013\ns_add_u32 m0, s26, 24576\ns_nop 0\n"
      "global_load_lds_dwordx4 v28, s[6:7] offset:0\n"
      ".La1013:\nv_lshl_add_u32 v28, v202, 7, v4\ns_add_u32 m0, s53, 0\nv_and_b32 v100, s55, v48\nglobal_load_lds_dwordx4 v22, s[6:7] offset:0\nv_lshl_add_u32 v22, v178, 7, v4\n"
      "v_xor_b32 v48, v48, v100\nglobal_load_lds_dwordx4 v23, s[44:45] offset:1024\nv_lshl_add_u32 v23, v180, 7, v5\nv_and_b32 v101, s55, v49\nglobal_load_lds_dwordx4 v24, s[46:47] offset:2048\n"
      "v_lshl_add_u32 v24, v186, 7, v4\nv_xor_b32 v49, v49, v101\nglobal_load_lds_dwordx4 v25, s[48:49] offset:3072\ns_add_u32 m0, s53, 4096\nv_lshl_add_u32 v25, v188, 7, v5\n"
      "global_load_lds_dwordx4 v26, s[6:7] offset:0\nv_lshl_add_u32 v26, v194, 7, v4\ns_waitcnt vmcnt(11) lgkmcnt(0)\ns_cbranch_vccnz .Lw1014\ns_waitcnt vmcnt(10)\n"
      ".Lw1014:\ns_barrier\ns_add_u32 m0, s54, 45056\ns_nop 0\nglobal_load_lds_dwordx4 v236, s[50:51] offset:0\nds_read_b64 v[52:53], v31 offset:0\n"
      "global_load_lds_dwordx4 v237, s[50:51] offset:1024\nglobal_load_lds_dwordx4 v238, s[50:51] offset:2048\nglobal_load_lds_dwordx4 v239, s[50:51] offset:3072\ns_add_u32 s50, s50, 212992\n"
      "s_addc_u32 s51, s51, 0\ns_add_u32 m0, s26, 65536\nv_max3_f16 v32, |v50|, |v51|, v32\nglobal_load_lds_dwordx4 v27, s[6:7] offset:0\nv_lshl_add_u32 v27, v197, 7, v4\n"
      "v_max3_f32 v33, |v50|, |v51|, v33\ns_cbranch_vccz .La1015\ns_add_u32 m0, s26, 69632\ns_nop 0\nglobal_load_lds_dwordx4 v28, s[6:7] offset:0\n"
      ".La1015:\nv_lshl_add_u32 v28, v203, 7, v4\nds_read2_b64 v[200:203], v9 offset0:4 offset1:5\ns_add_u32 m0, s53, 45056\nv_and_b32 v102, s55, v50\nglobal_load_lds_dwordx4 v22, s[6:7] offset:0\n"
      "v_lshl_add_u32 v22, v179, 7, v4\nds_read2_b64 v[176:179], v6 offset0:4 offset1:5\nv_xor_b32 v50, v50, v102\nglobal_load_lds_dwordx4 v23, s[44:45] offset:1024\n"
      "v_lshl_add_u32 v23, v181, 7, v5\nv_and_b32 v103, s55, v51\nglobal_load_lds_dwordx4 v24, s[46:47] offset:2048\nv_lshl_add_u32 v24, v187, 7, v4\n"
      "ds_read2_b64 v[184:187], v7 offset0:4 offset1:5\nv_xor_b32 v51, v51, v103\nglobal_load_lds_dwordx4 v25, s[48:49] offset:3072\ns_add_u32 m0, s53, 49152\nv_lshl_add_u32 v25, v189, 7, v5\n"
      "global_load_lds_dwordx4 v26, s[6:7] offset:0\nv_lshl_add_u32 v26, v195, 7, v4\nds_read2_b64 v[192:195], v8 offset0:4 offset1:5\ns_waitcnt vmcnt(11) lgkmcnt(0)\ns_cbranch_vccnz .Lw1016\n"
      "s_waitcnt vmcnt(10)\n"
      ".Lw1016:\ns_barrier\ns_add_u32 m0, s54, 90112\ns_nop 0\nglobal_load_lds_dwordx4 v236, s[50:51] offset:0\nds_read_b64 v[54:55], v30 offset:0\n"
      "global_load_lds_dwordx4 v237, s[50:51] offset:1024\nglobal_load_lds_dwordx4 v238, s[50:51] offset:2048\nglobal_load_lds_dwordx4 v239, s[50:51] offset:3072\ns_add_u32 s50, s50, 212992\n"
      "s_addc_u32 s51, s51, 0\ns_cmp_eq_u32 s27, 18\ns_cselect_b64 s[50:51], s[10:11], s[50:51]\ns_add_u32 m0, s26, 110592\nv_max3_f16 v32, |v52|, |v53|, v32\n"
      "global_load_lds_dwordx4 v27, s[6:7] offset:0\nv_lshl_add_u32 v27, v198, 7, v4\nv_max3_f32 v33, |v52|, |v53|, v33\ns_cbranch_vccz .La1017\ns_add_u32 m0, s26, 114688\ns_nop 0\n"
      "global_load_lds_dwordx4 v28, s[6:7] offset:0\n"
      ".La1017:\nv_lshl_add_u32 v28, v200, 7, v4\ns_add_u32 m0, s53, 90112\nv_and_b32 v104, s55, v52\nglobal_load_lds_dwordx4 v22, s[6:7] offset:0\nv_lshl_add_u32 v22, v176, 7, v4\n"
      "v_xor_b32 v52, v52, v104\nglobal_load_lds_dwordx4 v23, s[44:45] offset:1024\nv_lshl_add_u32 v23, v182, 7, v5\nv_and_b32 v105, s55, v53\nglobal_load_lds_dwordx4 v24, s[46:47] offset:2048\n"
      "v_lshl_add_u32 v24, v184, 7, v4\nv_xor_b32 v53, v53, v105\nglobal_load_lds_dwordx4 v25, s[48:49] offset:3072\ns_add_u32 m0, s53, 94208\nv_lshl_add_u32 v25, v190, 7, v5\n"
      "global_load_lds_dwordx4 v26, s[6:7] offset:0\nv_lshl_add_u32 v26, v192, 7, v4\ns_waitcnt vmcnt(11) lgkmcnt(0)\ns_cbranch_vccnz .Lw1018\ns_waitcnt vmcnt(10)\n"
      ".Lw1018:\ns_barrier\ns_add_u32 m0, s54, 0\ns_nop 0\nglobal_load_lds_dwordx4 v236, s[50:51] offset:0\nds_read_b64 v[56:57], v30 offset:45056\n"
      "global_load_lds_dwordx4 v237, s[50:51] offset:1024\nglobal_load_lds_dwordx4 v238, s[50:51] offset:2048\nglobal_load_lds_dwordx4 v239, s[50:51] offset:3072\ns_add_u32 s50, s50, 212992\n"
      "s_addc_u32 s51, s51, 0\nv_max3_f16 v32, |v54|, |v55|, v32\nv_max3_f32 v33, |v54|, |v55|, v33\ns_add_u32 m0, s26, 20480\nv_cmp_eq_u32 s[62:63], 0, v240\n"
      "global_load_lds_dwordx4 v27, s[6:7] offset:0\nv_lshl_add_u32 v27, v199, 7, v4\nds_read2_b64 v[196:199], v8 offset0:117 offset1:118\nv_mov_b32 v253, 262144\nv_mov_b32 v248, 162048\n"
      "v_mov_b32 v241, 1\ns_cbranch_vccz .La1019\ns_add_u32 m0, s26, 24576\ns_nop 0\nglobal_load_lds_dwordx4 v28, s[6:7] offset:0\n"
      ".La1019:\nv_lshl_add_u32 v28, v201, 7, v4\nv_cndmask_b32 v253, v253, v248, s[62:63]\ns_add_u32 m0, s53, 0\nv_and_b32 v106, s55, v54\nglobal_load_lds_dwordx4 v22, s[6:7] offset:0\n"
      "v_lshl_add_u32 v22, v177, 7, v4\nv_xor_b32 v54, v54, v106\nv_and_b32 v107, s55, v55\nglobal_load_lds_dwordx4 v23, s[44:45] offset:1024\nv_lshl_add_u32 v23, v183, 7, v5\n"
      "ds_read2_b64 v[180:183], v6 offset0:117 offset1:118\nv_xor_b32 v55, v55, v107\nv_lshrrev_b32 v164, 4, v240\nglobal_load_lds_dwordx4 v24, s[46:47] offset:2048\n"
      "v_lshl_add_u32 v24, v185, 7, v4\nv_bfe_u32 v165, v240, 2, 2\nv_and_b32 v166, 3, v240\nglobal_load_lds_dwordx4 v25, s[48:49] offset:3072\nv_lshl_add_u32 v25, v191, 7, v5\n"
      "ds_read2_b64 v[188:191], v7 offset0:117 offset1:118\nv_mul_u32_u24 v166, 0x80008, v166\ns_add_u32 m0, s53, 4096\nv_add_u32 v244, v164, v165\nglobal_load_lds_dwordx4 v26, s[6:7] offset:0\n"
      "v_lshl_add_u32 v26, v193, 7, v4\nv_and_b32 v244, 3, v244\nv_add_u32 v168, 22, v165\ns_waitcnt vmcnt(11) lgkmcnt(0)\ns_cbranch_vccnz .Lw1020\ns_waitcnt vmcnt(10)\n"
      ".Lw1020:\ns_barrier\ns_add_u32 m0, s54, 45056\ns_nop 0\nglobal_load_lds_dwordx4 v236, s[50:51] offset:0\nds_read_b64 v[58:59], v31 offset:0\n"
      "global_load_lds_dwordx4 v237, s[50:51] offset:1024\nglobal_load_lds_dwordx4 v238, s[50:51] offset:2048\nglobal_load_lds_dwordx4 v239, s[50:51] offset:3072\ns_add_u32 s50, s50, 212992\n"
      "s_addc_u32 s51, s51, 0\ns_cmp_eq_u32 s27, 16\ns_cselect_b64 s[50:51], s[10:11], s[50:51]\nv_max3_f16 v32, |v56|, |v57|, v32\nv_max3_f32 v33, |v56|, |v57|, v33\ns_add_u32 m0, s26, 65536\n"
      "v_cmp_gt_u32 s[62:63], 2, v165\nglobal_load_lds_dwordx4 v27, s[6:7] offset:0\nv_lshl_add_u32 v27, v196, 7, v4\nv_and_b32 v165, 60, v240\ns_mov_b32 s42, 0\n"
      "v_cndmask_b32 v168, v168, v244, s[62:63]\ns_cbranch_vccz .La1021\ns_add_u32 m0, s26, 69632\ns_nop 0\nglobal_load_lds_dwordx4 v28, s[6:7] offset:0\n"
      ".La1021:\nv_lshl_add_u32 v28, v202, 7, v4\nv_lshl_or_b32 v244, v244, 16, v168\nv_lshl_add_u32 v244, v244, 5, v166\ns_add_u32 m0, s53, 45056\nv_lshl_add_u32 v252, v240, 4, s42\n"
      "global_load_lds_dwordx4 v22, s[6:7] offset:0\nv_lshl_add_u32 v22, v178, 7, v4\nv_and_b32 v108, s55, v56\nv_xor_b32 v56, v56, v108\nv_and_b32 v109, s55, v57\n"
      "global_load_lds_dwordx4 v23, s[44:45] offset:1024\nv_lshl_add_u32 v23, v180, 7, v5\nv_xor_b32 v57, v57, v109\ns_mov_b32 s42, 0x76325410\ns_mov_b32 s43, 0x21005410\n"
      "global_load_lds_dwordx4 v24, s[46:47] offset:2048\nv_lshl_add_u32 v24, v186, 7, v4\nv_lshrrev_b64 v[168:169], v165, s[42:43]\nv_and_b32 v168, 7, v168\nv_lshl_add_u32 v168, v164, 3, v168\n"
      "global_load_lds_dwordx4 v25, s[48:49] offset:3072\nv_lshl_add_u32 v25, v188, 7, v5\ns_mov_b32 s42, 0x54107632\ns_mov_b32 s43, 0x21107632\ns_add_u32 m0, s53, 49152\n"
      "v_lshrrev_b64 v[170:171], v165, s[42:43]\nglobal_load_lds_dwordx4 v26, s[6:7] offset:0\nv_lshl_add_u32 v26, v194, 7, v4\nv_and_b32 v170, 7, v170\nv_lshl_add_u32 v170, v164, 3, v170\n"
      "s_waitcnt vmcnt(11) lgkmcnt(0)\ns_cbranch_vccnz .Lw1022\ns_waitcnt vmcnt(10)\n"
      ".Lw1022:\ns_barrier\ns_add_u32 m0, s54, 90112\ns_nop 0\nglobal_load_lds_dwordx4 v236, s[50:51] offset:0\nds_read_b64 v[60:61], v30 offset:0\n"
      "global_load_lds_dwordx4 v237, s[50:51] offset:1024\nglobal_load_lds_dwordx4 v238, s[50:51] offset:2048\nglobal_load_lds_dwordx4 v239, s[50:51] offset:3072\ns_add_u32 s50, s50, 212992\n"
      "s_addc_u32 s51, s51, 0\nv_max3_f16 v32, |v58|, |v59|, v32\ns_add_u32 m0, s26, 110592\nv_max3_f32 v33, |v58|, |v59|, v33\nglobal_load_lds_dwordx4 v27, s[6:7] offset:0\n"
      "v_lshl_add_u32 v27, v197, 7, v4\nv_lshl_or_b32 v245, v170, 16, v168\nv_lshl_add_u32 v245, v245, 5, v166\ns_cbranch_vccz .La1023\ns_add_u32 m0, s26, 114688\ns_nop 0\n"
      "global_load_lds_dwordx4 v28, s[6:7] offset:0\n"
      ".La1023:\nv_lshl_add_u32 v28, v203, 7, v4\nds_read2_b64 v[200:203], v9 offset0:6 offset1:7\nv_and_b32 v165, 1, v240\ns_add_u32 m0, s53, 90112\nv_mul_u32_u24 v165, 0xaaaa, v165\n"
      "global_load_lds_dwordx4 v22, s[6:7] offset:0\nv_lshl_add_u32 v22, v179, 7, v4\nds_read2_b64 v[176:179], v6 offset0:6 offset1:7\nv_add_u32 v242, 0x4444, v165\ns_lshl_b32 s42, s36, 4\n"
      "global_load_lds_dwordx4 v23, s[44:45] offset:1024\nv_lshl_add_u32 v23, v181, 7, v5\ns_add_u32 s42, s42, 160448\nv_lshl_add_u32 v243, v164, 2, s42\n"
      "global_load_lds_dwordx4 v24, s[46:47] offset:2048\nv_lshl_add_u32 v24, v187, 7, v4\nds_read2_b64 v[184:187], v7 offset0:6 offset1:7\nv_and_b32 v110, s55, v58\nv_xor_b32 v58, v58, v110\n"
      "global_load_lds_dwordx4 v25, s[48:49] offset:3072\nv_lshl_add_u32 v25, v189, 7, v5\ns_add_u32 m0, s53, 94208\nv_and_b32 v111, s55, v59\nglobal_load_lds_dwordx4 v26, s[6:7] offset:0\n"
      "v_lshl_add_u32 v26, v195, 7, v4\nds_read2_b64 v[192:195], v8 offset0:6 offset1:7\nv_xor_b32 v59, v59, v111\ns_waitcnt vmcnt(11) lgkmcnt(0)\ns_cbranch_vccnz .Lw1024\ns_waitcnt vmcnt(10)\n"
      ".Lw1024:\ns_barrier\ns_add_u32 m0, s54, 0\ns_nop 0\nglobal_load_lds_dwordx4 v236, s[50:51] offset:0\nds_read_b64 v[62:63], v30 offset:45056\n"
      "global_load_lds_dwordx4 v237, s[50:51] offset:1024\nglobal_load_lds_dwordx4 v238, s[50:51] offset:2048\nglobal_load_lds_dwordx4 v239, s[50:51] offset:3072\ns_add_u32 s50, s50, 212992\n"
      "s_addc_u32 s51, s51, 0\ns_cmp_eq_u32 s27, 14\ns_cselect_b64 s[50:51], s[10:11], s[50:51]\nv_max3_f16 v32, |v60|, |v61|, v32\nv_max3_f32 v33, |v60|, |v61|, v33\ns_add_u32 m0, s26, 20480\n"
      "v_and_b32 v164, 1, v164\nglobal_load_lds_dwordx4 v27, s[6:7] offset:0\nv_lshl_add_u32 v27, v198, 7, v4\nv_lshl_add_u32 v164, v164, 3, s24\ns_sub_u32 s42, s64, 1\ns_cbranch_vccz .La1025\n"
      "s_add_u32 m0, s26, 24576\ns_nop 0\nglobal_load_lds_dwordx4 v28, s[6:7] offset:0\n"
      ".La1025:\nv_lshl_add_u32 v28, v200, 7, v4\nv_min_u32 v164, s42, v164\ns_add_u32 m0, s53, 0\nv_lshlrev_b32 v164, 3, v164\nglobal_load_lds_dwordx4 v22, s[6:7] offset:0\n"
      "v_lshl_add_u32 v22, v176, 7, v4\nv_add_u32 v250, 160256, v164\nv_and_b32 v165, 15, v240\nglobal_load_lds_dwordx4 v23, s[44:45] offset:1024\nv_lshl_add_u32 v23, v182, 7, v5\n"
      "v_min_u32 v165, 12, v165\ns_lshl_b32 s42, s30, 7\nglobal_load_lds_dwordx4 v24, s[46:47] offset:2048\nv_lshl_add_u32 v24, v184, 7, v4\nv_lshl_add_u32 v165, v165, 15, s42\n"
      "v_add_u32 v249, v165, v164\nglobal_load_lds_dwordx4 v25, s[48:49] offset:3072\nv_lshl_add_u32 v25, v190, 7, v5\nv_and_b32 v112, s55, v60\ns_add_u32 m0, s53, 4096\nv_xor_b32 v60, v60, v112\n"
      "global_load_lds_dwordx4 v26, s[6:7] offset:0\nv_lshl_add_u32 v26, v192, 7, v4\nv_and_b32 v113, s55, v61\nv_xor_b32 v61, v61, v113\ns_waitcnt vmcnt(11) lgkmcnt(0)\ns_cbranch_vccnz .Lw1026\n"
      "s_waitcnt vmcnt(10)\n"
      ".Lw1026:\ns_barrier\ns_add_u32 m0, s54, 45056\ns_nop 0\nglobal_load_lds_dwordx4 v236, s[50:51] offset:0\nds_read_b64 v[64:65], v31 offset:0\n"
      "global_load_lds_dwordx4 v237, s[50:51] offset:1024\nglobal_load_lds_dwordx4 v238, s[50:51] offset:2048\nglobal_load_lds_dwordx4 v239, s[50:51] offset:3072\ns_add_u32 s50, s50, 212992\n"
      "s_addc_u32 s51, s51, 0\nv_max3_f16 v32, |v62|, |v63|, v32\ns_add_u32 m0, s26, 65536\nv_max3_f32 v33, |v62|, |v63|, v33\nglobal_load_lds_dwordx4 v27, s[6:7] offset:0\n"
      "v_lshl_add_u32 v27, v199, 7, v4\nds_read2_b64 v[196:199], v8 offset0:119 offset1:120\ns_mul_i32 s65, s36, 22272\ns_add_u32 s65, s65, 45056\ns_cbranch_vccz .La1027\ns_add_u32 m0, s26, 69632\n"
      "s_nop 0\nglobal_load_lds_dwordx4 v28, s[6:7] offset:0\n"
      ".La1027:\nv_lshl_add_u32 v28, v201, 7, v4\ns_add_u32 s68, s64, 8\ns_min_u32 s68, s68, 21\ns_add_u32 m0, s53, 45056\ns_nop 0\nglobal_load_lds_dwordx4 v22, s[6:7] offset:0\n"
      "v_lshl_add_u32 v22, v177, 7, v4\ns_lshl_b32 s38, s38, 16\nglobal_load_lds_dwordx4 v23, s[44:45] offset:1024\nv_lshl_add_u32 v23, v183, 7, v5\n"
      "ds_read2_b64 v[180:183], v6 offset0:119 offset1:120\nv_and_b32 v114, s55, v62\nglobal_load_lds_dwordx4 v24, s[46:47] offset:2048\nv_lshl_add_u32 v24, v185, 7, v4\nv_xor_b32 v62, v62, v114\n"
      "global_load_lds_dwordx4 v25, s[48:49] offset:3072\nv_lshl_add_u32 v25, v191, 7, v5\nds_read2_b64 v[188:191], v7 offset0:119 offset1:120\ns_add_u32 m0, s53, 49152\nv_and_b32 v115, s55, v63\n"
      "global_load_lds_dwordx4 v26, s[6:7] offset:0\nv_lshl_add_u32 v26, v193, 7, v4\nv_xor_b32 v63, v63, v115\ns_waitcnt vmcnt(11) lgkmcnt(0)\ns_cbranch_vccnz .Lw1028\ns_waitcnt vmcnt(10)\n"
      ".Lw1028:\ns_barrier\ns_add_u32 m0, s54, 90112\ns_nop 0\nglobal_load_lds_dwordx4 v236, s[50:51] offset:0\nds_read_b64 v[66:67], v30 offset:0\n"
      "global_load_lds_dwordx4 v237, s[50:51] offset:1024\nglobal_load_lds_dwordx4 v238, s[50:51] offset:2048\nglobal_load_lds_dwordx4 v239, s[50:51] offset:3072\ns_add_u32 s50, s50, 212992\n"
      "s_addc_u32 s51, s51, 0\ns_cmp_eq_u32 s27, 12\ns_cselect_b64 s[50:51], s[10:11], s[50:51]\nv_max3_f16 v32, |v64|, |v65|, v32\nv_max3_f32 v33, |v64|, |v65|, v33\ns_lshr_b32 s42, s24, 2\n"
      "s_add_u32 m0, s26, 110592\ns_nop 0\nglobal_load_lds_dwordx4 v27, s[6:7] offset:0\nv_lshl_add_u32 v27, v196, 7, v4\ns_mul_i32 s43, s42, 3\ns_xor_b32 s43, s43, s36\ns_cbranch_vccz .La1029\n"
      "s_add_u32 m0, s26, 114688\ns_nop 0\nglobal_load_lds_dwordx4 v28, s[6:7] offset:0\n"
      ".La1029:\nv_lshl_add_u32 v28, v202, 7, v4\ns_add_u32 s42, s42, s64\ns_add_u32 s42, s42, 9\ns_add_u32 m0, s53, 90112\ns_nop 0\nglobal_load_lds_dwordx4 v22, s[6:7] offset:0\n"
      "v_lshl_add_u32 v22, v178, 7, v4\ns_and_b32 s42, s42, 1\ns_lshl1_add_u32 s67, s43, s42\nglobal_load_lds_dwordx4 v23, s[44:45] offset:1024\nv_lshl_add_u32 v23, v180, 7, v5\n"
      "s_add_u32 s66, s64, s67\ns_cmp_lt_u32 s66, s68\ns_cselect_b32 s66, s66, 127\nglobal_load_lds_dwordx4 v24, s[46:47] offset:2048\nv_lshl_add_u32 v24, v186, 7, v4\ns_sub_u32 s66, s66, 8\n"
      "s_max_i32 s66, s66, 0\nglobal_load_lds_dwordx4 v25, s[48:49] offset:3072\nv_lshl_add_u32 v25, v188, 7, v5\nv_and_b32 v116, s55, v64\ns_add_u32 m0, s53, 94208\nv_xor_b32 v64, v64, v116\n"
      "global_load_lds_dwordx4 v26, s[6:7] offset:0\nv_lshl_add_u32 v26, v194, 7, v4\nv_and_b32 v117, s55, v65\nv_xor_b32 v65, v65, v117\ns_waitcnt vmcnt(11) lgkmcnt(0)\ns_cbranch_vccnz .Lw1030\n"
      "s_waitcnt vmcnt(10)\n"
      ".Lw1030:\ns_barrier\ns_add_u32 m0, s54, 0\ns_nop 0\nglobal_load_lds_dwordx4 v236, s[50:51] offset:0\nds_read_b64 v[68:69], v30 offset:45056\n"
      "global_load_lds_dwordx4 v237, s[50:51] offset:1024\nglobal_load_lds_dwordx4 v238, s[50:51] offset:2048\nglobal_load_lds_dwordx4 v239, s[50:51] offset:3072\ns_add_u32 s50, s50, 212992\n"
      "s_addc_u32 s51, s51, 0\ns_add_u32 m0, s26, 20480\nv_max3_f16 v32, |v66|, |v67|, v32\nglobal_load_lds_dwordx4 v27, s[6:7] offset:0\nv_lshl_add_u32 v27, v197, 7, v4\n"
      "v_max3_f32 v33, |v66|, |v67|, v33\ns_cbranch_vccz .La1031\ns_add_u32 m0, s26, 24576\ns_nop 0\nglobal_load_lds_dwordx4 v28, s[6:7] offset:0\n"
      ".La1031:\nv_lshl_add_u32 v28, v203, 7, v4\nds_read2_b64 v[200:203], v9 offset0:8 offset1:9\ns_getpc_b64 s[34:35]\n"
      ".Lk1005:\ns_add_u32 m0, s53, 0\ns_nop 0\nglobal_load_lds_dwordx4 v22, s[6:7] offset:0\nv_lshl_add_u32 v22, v179, 7, v4\nds_read2_b64 v[176:179], v6 offset0:8 offset1:9\n"
      "s_add_u32 s34, s34, .Lt_p-.Lk1005\ns_addc_u32 s35, s35, 0\nglobal_load_lds_dwordx4 v23, s[44:45] offset:1024\nv_lshl_add_u32 v23, v181, 7, v5\nv_and_b32 v118, s55, v66\n"
      "global_load_lds_dwordx4 v24, s[46:47] offset:2048\nv_lshl_add_u32 v24, v187, 7, v4\nds_read2_b64 v[184:187], v7 offset0:8 offset1:9\nv_xor_b32 v66, v66, v118\n"
      "global_load_lds_dwordx4 v25, s[48:49] offset:3072\nv_lshl_add_u32 v25, v189, 7, v5\ns_add_u32 m0, s53, 4096\nv_and_b32 v119, s55, v67\nglobal_load_lds_dwordx4 v26, s[6:7] offset:0\n"
      "v_lshl_add_u32 v26, v195, 7, v4\nds_read2_b64 v[192:195], v8 offset0:8 offset1:9\nv_xor_b32 v67, v67, v119\ns_waitcnt vmcnt(11) lgkmcnt(0)\ns_cbranch_vccnz .Lw1032\ns_waitcnt vmcnt(10)\n"
      ".Lw1032:\ns_barrier\ns_add_u32 m0, s54, 45056\ns_nop 0\nglobal_load_lds_dwordx4 v236, s[50:51] offset:0\nds_read_b64 v[70:71], v31 offset:0\n"
      "global_load_lds_dwordx4 v237, s[50:51] offset:1024\nglobal_load_lds_dwordx4 v238, s[50:51] offset:2048\nglobal_load_lds_dwordx4 v239, s[50:51] offset:3072\ns_add_u32 s50, s50, 212992\n"
      "s_addc_u32 s51, s51, 0\ns_cmp_eq_u32 s27, 10\ns_cselect_b64 s[50:51], s[10:11], s[50:51]\nv_max3_f16 v32, |v68|, |v69|, v32\ns_add_u32 m0, s26, 65536\nv_max3_f32 v33, |v68|, |v69|, v33\n"
      "global_load_lds_dwordx4 v27, s[6:7] offset:0\nv_lshl_add_u32 v27, v198, 7, v4\ns_lshr_b32 s42, s2, 3\ns_lshl1_add_u32 s42, s2, s42\ns_cbranch_vccz .La1033\ns_add_u32 m0, s26, 69632\n"
      "s_nop 0\nglobal_load_lds_dwordx4 v28, s[6:7] offset:0\n"
      ".La1033:\nv_lshl_add_u32 v28, v200, 7, v4\ns_and_b32 s69, s42, 15\ns_lshl_b32 s43, s69, 7\ns_add_u32 m0, s53, 45056\ns_nop 0\nglobal_load_lds_dwordx4 v22, s[6:7] offset:0\n"
      "v_lshl_add_u32 v22, v176, 7, v4\ns_add_u32 s43, s43, 32768\ns_add_u32 s8, s20, s43\ns_addc_u32 s9, s21, 0\nglobal_load_lds_dwordx4 v23, s[44:45] offset:1024\n"
      "v_lshl_add_u32 v23, v182, 7, v5\ns_add_u32 s12, s20, 65536\ns_addc_u32 s13, s21, 0\ns_add_u32 s16, s20, 2097152\ns_addc_u32 s17, s21, 0\nglobal_load_lds_dwordx4 v24, s[46:47] offset:2048\n"
      "v_lshl_add_u32 v24, v184, 7, v4\nv_and_b32 v120, s55, v68\nv_xor_b32 v68, v68, v120\nglobal_load_lds_dwordx4 v25, s[48:49] offset:3072\nv_lshl_add_u32 v25, v190, 7, v5\n"
      "s_add_u32 m0, s53, 49152\nv_and_b32 v121, s55, v69\nglobal_load_lds_dwordx4 v26, s[6:7] offset:0\nv_lshl_add_u32 v26, v192, 7, v4\nv_xor_b32 v69, v69, v121\ns_waitcnt vmcnt(11) lgkmcnt(0)\n"
      "s_cbranch_vccnz .Lw1034\ns_waitcnt vmcnt(10)\n"
      ".Lw1034:\ns_barrier\ns_add_u32 m0, s54, 90112\ns_nop 0\nglobal_load_lds_dwordx4 v236, s[50:51] offset:0\nds_read_b64 v[72:73], v30 offset:0\n"
      "global_load_lds_dwordx4 v237, s[50:51] offset:1024\nglobal_load_lds_dwordx4 v238, s[50:51] offset:2048\nglobal_load_lds_dwordx4 v239, s[50:51] offset:3072\ns_add_u32 s50, s50, 212992\n"
      "s_addc_u32 s51, s51, 0\nv_max3_f16 v32, |v70|, |v71|, v32\ns_add_u32 m0, s26, 110592\nv_max3_f32 v33, |v70|, |v71|, v33\nglobal_load_lds_dwordx4 v27, s[6:7] offset:0\n"
      "v_lshl_add_u32 v27, v199, 7, v4\nds_read2_b64 v[196:199], v8 offset0:121 offset1:122\ns_lshl_b32 s42, s69, 4\ns_max_u32 s42, s42, 24\ns_cbranch_vccz .La1035\ns_add_u32 m0, s26, 114688\n"
      "s_nop 0\nglobal_load_lds_dwordx4 v28, s[6:7] offset:0\n"
      ".La1035:\nv_lshl_add_u32 v28, v201, 7, v4\ns_sub_u32 s42, s42, 24\ns_min_u32 s69, s42, 192\ns_add_u32 m0, s53, 90112\ns_nop 0\nglobal_load_lds_dwordx4 v22, s[6:7] offset:0\n"
      "v_lshl_add_u32 v22, v177, 7, v4\ns_lshl_b32 s42, s69, 3\ns_add_u32 s42, s42, 24576\nglobal_load_lds_dwordx4 v23, s[44:45] offset:1024\nv_lshl_add_u32 v23, v183, 7, v5\n"
      "ds_read2_b64 v[180:183], v6 offset0:121 offset1:122\ns_add_u32 s14, s20, s42\ns_addc_u32 s15, s21, 0\nv_and_b32 v122, s55, v70\nglobal_load_lds_dwordx4 v24, s[46:47] offset:2048\n"
      "v_lshl_add_u32 v24, v185, 7, v4\nv_xor_b32 v70, v70, v122\nglobal_load_lds_dwordx4 v25, s[48:49] offset:3072\nv_lshl_add_u32 v25, v191, 7, v5\n"
      "ds_read2_b64 v[188:191], v7 offset0:121 offset1:122\ns_add_u32 m0, s53, 94208\nv_and_b32 v123, s55, v71\nglobal_load_lds_dwordx4 v26, s[6:7] offset:0\nv_lshl_add_u32 v26, v193, 7, v4\n"
      "v_xor_b32 v71, v71, v123\ns_waitcnt vmcnt(11) lgkmcnt(0)\ns_cbranch_vccnz .Lw1036\ns_waitcnt vmcnt(10)\n"
      ".Lw1036:\ns_barrier\ns_add_u32 m0, s54, 0\ns_nop 0\nglobal_load_lds_dwordx4 v236, s[50:51] offset:0\nds_read_b64 v[74:75], v30 offset:45056\n"
      "global_load_lds_dwordx4 v237, s[50:51] offset:1024\nglobal_load_lds_dwordx4 v238, s[50:51] offset:2048\nglobal_load_lds_dwordx4 v239, s[50:51] offset:3072\ns_add_u32 s50, s50, 212992\n"
      "s_addc_u32 s51, s51, 0\ns_cmp_eq_u32 s27, 8\ns_cselect_b64 s[50:51], s[10:11], s[50:51]\nv_max3_f16 v32, |v72|, |v73|, v32\ns_add_u32 m0, s26, 20480\nv_max3_f32 v33, |v72|, |v73|, v33\n"
      "global_load_lds_dwordx4 v27, s[6:7] offset:0\nv_lshl_add_u32 v27, v196, 7, v4\ns_lshl_b32 s42, s2, 3\ns_add_u32 s42, s42, 4096\ns_cbranch_vccz .La1037\ns_add_u32 m0, s26, 24576\ns_nop 0\n"
      "global_load_lds_dwordx4 v28, s[6:7] offset:0\n"
      ".La1037:\nv_lshl_add_u32 v28, v202, 7, v4\ns_add_u32 s58, s20, s42\ns_addc_u32 s59, s21, 0\ns_add_u32 m0, s53, 0\nv_and_b32 v38, 7, v240\nglobal_load_lds_dwordx4 v22, s[6:7] offset:0\n"
      "v_lshl_add_u32 v22, v178, 7, v4\nv_lshlrev_b32 v38, 11, v38\ns_xor_b32 s40, s37, 0xa5c30f96\nglobal_load_lds_dwordx4 v23, s[44:45] offset:1024\nv_lshl_add_u32 v23, v180, 7, v5\n"
      "s_mul_i32 s41, s37, 0x9E3779B1\ns_lshr_b32 s41, s41, 24\nglobal_load_lds_dwordx4 v24, s[46:47] offset:2048\nv_lshl_add_u32 v24, v186, 7, v4\nv_and_b32 v124, s55, v72\n"
      "v_xor_b32 v72, v72, v124\nglobal_load_lds_dwordx4 v25, s[48:49] offset:3072\nv_lshl_add_u32 v25, v188, 7, v5\ns_add_u32 m0, s53, 4096\nv_and_b32 v125, s55, v73\n"
      "global_load_lds_dwordx4 v26, s[6:7] offset:0\nv_lshl_add_u32 v26, v194, 7, v4\nv_xor_b32 v73, v73, v125\ns_waitcnt vmcnt(11) lgkmcnt(0)\ns_cbranch_vccnz .Lw1038\ns_waitcnt vmcnt(10)\n"
      ".Lw1038:\ns_barrier\ns_add_u32 m0, s54, 45056\ns_nop 0\nglobal_load_lds_dwordx4 v236, s[50:51] offset:0\nds_read_b64 v[76:77], v31 offset:0\n"
      "global_load_lds_dwordx4 v237, s[50:51] offset:1024\nglobal_load_lds_dwordx4 v238, s[50:51] offset:2048\nglobal_load_lds_dwordx4 v239, s[50:51] offset:3072\ns_add_u32 s50, s50, 212992\n"
      "s_addc_u32 s51, s51, 0\nv_max3_f16 v32, |v74|, |v75|, v32\ns_add_u32 m0, s26, 65536\nv_max3_f32 v33, |v74|, |v75|, v33\nglobal_load_lds_dwordx4 v27, s[6:7] offset:0\n"
      "v_lshl_add_u32 v27, v197, 7, v4\nv_lshrrev_b32 v164, 2, v240\nv_and_b32 v148, 3, v240\ns_cbranch_vccz .La1039\ns_add_u32 m0, s26, 69632\ns_nop 0\n"
      "global_load_lds_dwordx4 v28, s[6:7] offset:0\n"
      ".La1039:\nv_lshl_add_u32 v28, v203, 7, v4\nds_read2_b64 v[200:203], v9 offset0:10 offset1:11\nv_lshl_add_u32 v148, v148, 3, s65\ns_add_u32 m0, s53, 45056\nv_mul_u32_u24 v164, 928, v164\n"
      "global_load_lds_dwordx4 v22, s[6:7] offset:0\nv_lshl_add_u32 v22, v179, 7, v4\nds_read2_b64 v[176:179], v6 offset0:10 offset1:11\nv_add_u32 v149, s65, v164\nv_add_u32 v148, v148, v164\n"
      "global_load_lds_dwordx4 v23, s[44:45] offset:1024\nv_lshl_add_u32 v23, v181, 7, v5\ns_add_u32 s42, s65, 7424\nv_add_u32 v163, s42, v252\nglobal_load_lds_dwordx4 v24, s[46:47] offset:2048\n"
      "v_lshl_add_u32 v24, v187, 7, v4\nds_read2_b64 v[184:187], v7 offset0:10 offset1:11\nv_and_b32 v126, s55, v74\nv_xor_b32 v74, v74, v126\nglobal_load_lds_dwordx4 v25, s[48:49] offset:3072\n"
      "v_lshl_add_u32 v25, v189, 7, v5\ns_add_u32 m0, s53, 49152\nv_and_b32 v127, s55, v75\nglobal_load_lds_dwordx4 v26, s[6:7] offset:0\nv_lshl_add_u32 v26, v195, 7, v4\n"
      "ds_read2_b64 v[192:195], v8 offset0:10 offset1:11\nv_xor_b32 v75, v75, v127\ns_waitcnt vmcnt(11) lgkmcnt(0)\ns_cbranch_vccnz .Lw1040\ns_waitcnt vmcnt(10)\n"
      ".Lw1040:\ns_barrier\ns_add_u32 m0, s54, 90112\ns_nop 0\nglobal_load_lds_dwordx4 v236, s[50:51] offset:0\nds_read_b64 v[78:79], v30 offset:0\n"
      "global_load_lds_dwordx4 v237, s[50:51] offset:1024\nglobal_load_lds_dwordx4 v238, s[50:51] offset:2048\nglobal_load_lds_dwordx4 v239, s[50:51] offset:3072\ns_add_u32 s50, s50, 212992\n"
      "s_addc_u32 s51, s51, 0\ns_cmp_eq_u32 s27, 6\ns_cselect_b64 s[50:51], s[10:11], s[50:51]\nv_max3_f16 v32, |v76|, |v77|, v32\ns_add_u32 m0, s26, 110592\nv_max3_f32 v33, |v76|, |v77|, v33\n"
      "global_load_lds_dwordx4 v27, s[6:7] offset:0\nv_lshl_add_u32 v27, v198, 7, v4\ns_mul_i32 s42, s36, 10752\ns_add_u32 s56, s42, 0\ns_cbranch_vccz .La1041\ns_add_u32 m0, s26, 114688\ns_nop 0\n"
      "global_load_lds_dwordx4 v28, s[6:7] offset:0\n"
      ".La1041:\nv_lshl_add_u32 v28, v200, 7, v4\ns_add_u32 s42, s42, 1048576\ns_add_u32 m0, s53, 90112\ns_nop 0\nglobal_load_lds_dwordx4 v22, s[6:7] offset:0\nv_lshl_add_u32 v22, v176, 7, v4\n"
      "s_add_u32 s60, s20, s42\ns_addc_u32 s61, s21, 0\nglobal_load_lds_dwordx4 v23, s[44:45] offset:1024\nv_lshl_add_u32 v23, v182, 7, v5\nv_and_b32 v128, s55, v76\n"
      "global_load_lds_dwordx4 v24, s[46:47] offset:2048\nv_lshl_add_u32 v24, v184, 7, v4\nv_xor_b32 v76, v76, v128\nglobal_load_lds_dwordx4 v25, s[48:49] offset:3072\n"
      "v_lshl_add_u32 v25, v190, 7, v5\ns_add_u32 m0, s53, 94208\nv_and_b32 v129, s55, v77\nglobal_load_lds_dwordx4 v26, s[6:7] offset:0\nv_lshl_add_u32 v26, v192, 7, v4\nv_xor_b32 v77, v77, v129\n"
      "s_waitcnt vmcnt(11) lgkmcnt(0)\ns_cbranch_vccnz .Lw1042\ns_waitcnt vmcnt(10)\n"
      ".Lw1042:\ns_barrier\ns_add_u32 m0, s54, 0\ns_nop 0\nglobal_load_lds_dwordx4 v236, s[50:51] offset:0\nds_read_b64 v[80:81], v30 offset:45056\n"
      "global_load_lds_dwordx4 v237, s[50:51] offset:1024\nglobal_load_lds_dwordx4 v238, s[50:51] offset:2048\nglobal_load_lds_dwordx4 v239, s[50:51] offset:3072\ns_add_u32 s50, s50, 212992\n"
      "s_addc_u32 s51, s51, 0\nv_max3_f16 v32, |v78|, |v79|, v32\nv_max3_f32 v33, |v78|, |v79|, v33\ns_add_u32 s42, s27, 0\ns_add_u32 m0, s26, 20480\ns_nop 0\n"
      "global_load_lds_dwordx4 v27, s[6:7] offset:0\nv_lshl_add_u32 v27, v199, 7, v4\nds_read2_b64 v[196:199], v8 offset0:123 offset1:124\ns_sub_u32 s43, s42, 26\ns_min_u32 s42, s42, s43\n"
      "v_lshl_add_u32 v150, s42, 5, v148\ns_cbranch_vccz .La1043\ns_add_u32 m0, s26, 24576\ns_nop 0\nglobal_load_lds_dwordx4 v28, s[6:7] offset:0\n"
      ".La1043:\nv_lshl_add_u32 v28, v201, 7, v4\ns_add_u32 s42, s27, 2\ns_sub_u32 s43, s42, 26\ns_add_u32 m0, s53, 0\ns_nop 0\nglobal_load_lds_dwordx4 v22, s[6:7] offset:0\n"
      "v_lshl_add_u32 v22, v177, 7, v4\ns_min_u32 s42, s42, s43\nv_lshl_add_u32 v151, s42, 5, v148\nglobal_load_lds_dwordx4 v23, s[44:45] offset:1024\nv_lshl_add_u32 v23, v183, 7, v5\n"
      "ds_read2_b64 v[180:183], v6 offset0:123 offset1:124\ns_add_u32 s42, s27, 4\ns_sub_u32 s43, s42, 26\nglobal_load_lds_dwordx4 v24, s[46:47] offset:2048\nv_lshl_add_u32 v24, v185, 7, v4\n"
      "s_min_u32 s42, s42, s43\nv_lshl_add_u32 v152, s42, 5, v148\nglobal_load_lds_dwordx4 v25, s[48:49] offset:3072\nv_lshl_add_u32 v25, v191, 7, v5\n"
      "ds_read2_b64 v[188:191], v7 offset0:123 offset1:124\nv_and_b32 v130, s55, v78\ns_add_u32 m0, s53, 4096\nv_xor_b32 v78, v78, v130\nglobal_load_lds_dwordx4 v26, s[6:7] offset:0\n"
      "v_lshl_add_u32 v26, v193, 7, v4\nv_and_b32 v131, s55, v79\nv_xor_b32 v79, v79, v131\ns_waitcnt vmcnt(11) lgkmcnt(0)\ns_cbranch_vccnz .Lw1044\ns_waitcnt vmcnt(10)\n"
      ".Lw1044:\ns_barrier\ns_add_u32 m0, s54, 45056\ns_nop 0\nglobal_load_lds_dwordx4 v236, s[50:51] offset:0\nds_read_b64 v[82:83], v31 offset:0\n"
      "global_load_lds_dwordx4 v237, s[50:51] offset:1024\nglobal_load_lds_dwordx4 v238, s[50:51] offset:2048\nglobal_load_lds_dwordx4 v239, s[50:51] offset:3072\ns_add_u32 s50, s50, 212992\n"
      "s_addc_u32 s51, s51, 0\ns_cmp_eq_u32 s27, 4\ns_cselect_b64 s[50:51], s[10:11], s[50:51]\nv_max3_f16 v32, |v80|, |v81|, v32\nv_max3_f32 v33, |v80|, |v81|, v33\ns_add_u32 s42, s27, 6\n"
      "s_add_u32 m0, s26, 65536\ns_nop 0\nglobal_load_lds_dwordx4 v27, s[6:7] offset:0\nv_lshl_add_u32 v27, v196, 7, v4\ns_sub_u32 s43, s42, 26\ns_min_u32 s42, s42, s43\n"
      "v_lshl_add_u32 v153, s42, 5, v148\ns_cbranch_vccz .La1045\ns_add_u32 m0, s26, 69632\ns_nop 0\nglobal_load_lds_dwordx4 v28, s[6:7] offset:0\n"
      ".La1045:\nv_lshl_add_u32 v28, v202, 7, v4\ns_add_u32 s42, s27, 8\ns_sub_u32 s43, s42, 26\ns_add_u32 m0, s53, 45056\ns_nop 0\nglobal_load_lds_dwordx4 v22, s[6:7] offset:0\n"
      "v_lshl_add_u32 v22, v178, 7, v4\ns_min_u32 s42, s42, s43\nv_lshl_add_u32 v154, s42, 5, v148\nglobal_load_lds_dwordx4 v23, s[44:45] offset:1024\nv_lshl_add_u32 v23, v180, 7, v5\n"
      "s_add_u32 s42, s27, 10\ns_sub_u32 s43, s42, 26\nglobal_load_lds_dwordx4 v24, s[46:47] offset:2048\nv_lshl_add_u32 v24, v186, 7, v4\ns_min_u32 s42, s42, s43\n"
      "v_lshl_add_u32 v155, s42, 5, v148\nglobal_load_lds_dwordx4 v25, s[48:49] offset:3072\nv_lshl_add_u32 v25, v188, 7, v5\nv_and_b32 v132, s55, v80\ns_add_u32 m0, s53, 49152\n"
      "v_xor_b32 v80, v80, v132\nglobal_load_lds_dwordx4 v26, s[6:7] offset:0\nv_lshl_add_u32 v26, v194, 7, v4\nv_and_b32 v133, s55, v81\nv_xor_b32 v81, v81, v133\ns_cmp_lg_u32 s24, 3\n"
      "s_cbranch_scc1 .Lo1046\nglobal_load_dwordx2 v[36:37], v38, s[58:59] sc1\n"
      ".Lo1046:\ns_waitcnt vmcnt(11) lgkmcnt(0)\ns_cbranch_vccnz .Lw1047\ns_waitcnt vmcnt(10)\n"
      ".Lw1047:\ns_barrier\ns_add_u32 m0, s54, 90112\ns_nop 0\nglobal_load_lds_dwordx4 v236, s[50:51] offset:0\nds_read_b64 v[84:85], v30 offset:0\n"
      "global_load_lds_dwordx4 v237, s[50:51] offset:1024\nglobal_load_lds_dwordx4 v238, s[50:51] offset:2048\nglobal_load_lds_dwordx4 v239, s[50:51] offset:3072\ns_add_u32 s50, s50, 212992\n"
      "s_addc_u32 s51, s51, 0\nv_max3_f16 v32, |v82|, |v83|, v32\nv_max3_f32 v33, |v82|, |v83|, v33\ns_add_u32 s42, s27, 12\ns_add_u32 m0, s26, 110592\ns_nop 0\n"
      "global_load_lds_dwordx4 v27, s[6:7] offset:0\nv_lshl_add_u32 v27, v197, 7, v4\ns_sub_u32 s43, s42, 26\ns_min_u32 s42, s42, s43\nv_lshl_add_u32 v156, s42, 5, v148\ns_cbranch_vccz .La1048\n"
      "s_add_u32 m0, s26, 114688\ns_nop 0\nglobal_load_lds_dwordx4 v28, s[6:7] offset:0\n"
      ".La1048:\nv_lshl_add_u32 v28, v203, 7, v4\nds_read2_b64 v[200:203], v9 offset0:12 offset1:13\ns_add_u32 s42, s27, 14\ns_sub_u32 s43, s42, 26\ns_add_u32 m0, s53, 90112\ns_nop 0\n"
      "global_load_lds_dwordx4 v22, s[6:7] offset:0\nv_lshl_add_u32 v22, v179, 7, v4\nds_read2_b64 v[176:179], v6 offset0:12 offset1:13\ns_min_u32 s42, s42, s43\nv_lshl_add_u32 v157, s42, 5, v148\n"
      "global_load_lds_dwordx4 v23, s[44:45] offset:1024\nv_lshl_add_u32 v23, v181, 7, v5\ns_add_u32 s42, s27, 16\ns_sub_u32 s43, s42, 26\nglobal_load_lds_dwordx4 v24, s[46:47] offset:2048\n"
      "v_lshl_add_u32 v24, v187, 7, v4\nds_read2_b64 v[184:187], v7 offset0:12 offset1:13\ns_min_u32 s42, s42, s43\nv_lshl_add_u32 v158, s42, 5, v148\n"
      "global_load_lds_dwordx4 v25, s[48:49] offset:3072\nv_lshl_add_u32 v25, v189, 7, v5\nv_and_b32 v134, s55, v82\ns_add_u32 m0, s53, 94208\nv_xor_b32 v82, v82, v134\n"
      "global_load_lds_dwordx4 v26, s[6:7] offset:0\nv_lshl_add_u32 v26, v195, 7, v4\nds_read2_b64 v[192:195], v8 offset0:12 offset1:13\nv_and_b32 v135, s55, v83\nv_xor_b32 v83, v83, v135\n"
      "s_waitcnt vmcnt(11) lgkmcnt(0)\ns_cbranch_vccnz .Lw1049\ns_waitcnt vmcnt(10)\n"
      ".Lw1049:\ns_barrier\ns_add_u32 m0, s54, 0\ns_nop 0\nglobal_load_lds_dwordx4 v236, s[50:51] offset:0\nds_read_b64 v[86:87], v30 offset:45056\n"
      "global_load_lds_dwordx4 v237, s[50:51] offset:1024\nglobal_load_lds_dwordx4 v238, s[50:51] offset:2048\nglobal_load_lds_dwordx4 v239, s[50:51] offset:3072\ns_add_u32 s50, s50, 212992\n"
      "s_addc_u32 s51, s51, 0\ns_cmp_eq_u32 s27, 2\ns_cselect_b64 s[50:51], s[10:11], s[50:51]\nv_max3_f16 v32, |v84|, |v85|, v32\nv_max3_f32 v33, |v84|, |v85|, v33\ns_add_u32 s42, s27, 18\n"
      "s_add_u32 m0, s26, 20480\ns_nop 0\nglobal_load_lds_dwordx4 v27, s[6:7] offset:0\nv_lshl_add_u32 v27, v198, 7, v4\ns_sub_u32 s43, s42, 26\ns_min_u32 s42, s42, s43\n"
      "v_lshl_add_u32 v159, s42, 5, v148\ns_cbranch_vccz .La1050\ns_add_u32 m0, s26, 24576\ns_nop 0\nglobal_load_lds_dwordx4 v28, s[6:7] offset:0\n"
      ".La1050:\nv_lshl_add_u32 v28, v200, 7, v4\ns_add_u32 s42, s27, 20\ns_sub_u32 s43, s42, 26\ns_add_u32 m0, s53, 0\ns_nop 0\nglobal_load_lds_dwordx4 v22, s[6:7] offset:0\n"
      "v_lshl_add_u32 v22, v176, 7, v4\ns_min_u32 s42, s42, s43\nv_lshl_add_u32 v160, s42, 5, v148\nglobal_load_lds_dwordx4 v23, s[44:45] offset:1024\nv_lshl_add_u32 v23, v182, 7, v5\n"
      "s_add_u32 s42, s27, 22\ns_sub_u32 s43, s42, 26\nglobal_load_lds_dwordx4 v24, s[46:47] offset:2048\nv_lshl_add_u32 v24, v184, 7, v4\ns_min_u32 s42, s42, s43\n"
      "v_lshl_add_u32 v161, s42, 5, v148\nglobal_load_lds_dwordx4 v25, s[48:49] offset:3072\nv_lshl_add_u32 v25, v190, 7, v5\nv_and_b32 v136, s55, v84\ns_add_u32 m0, s53, 4096\n"
      "v_xor_b32 v84, v84, v136\nglobal_load_lds_dwordx4 v26, s[6:7] offset:0\nv_lshl_add_u32 v26, v192, 7, v4\nv_and_b32 v137, s55, v85\nv_xor_b32 v85, v85, v137\ns_waitcnt vmcnt(11) lgkmcnt(0)\n"
      "s_cbranch_vccnz .Lw1051\ns_waitcnt vmcnt(10)\n"
      ".Lw1051:\ns_barrier\ns_add_u32 m0, s54, 45056\ns_nop 0\nglobal_load_lds_dwordx4 v236, s[50:51] offset:0\nds_read_b64 v[88:89], v31 offset:0\n"
      "global_load_lds_dwordx4 v237, s[50:51] offset:1024\nglobal_load_lds_dwordx4 v238, s[50:51] offset:2048\nglobal_load_lds_dwordx4 v239, s[50:51] offset:3072\ns_add_u32 s50, s50, 212992\n"
      "s_addc_u32 s51, s51, 0\nv_max3_f16 v32, |v86|, |v87|, v32\ns_add_u32 m0, s26, 65536\nv_max3_f32 v33, |v86|, |v87|, v33\nglobal_load_lds_dwordx4 v27, s[6:7] offset:0\n"
      "v_lshl_add_u32 v27, v199, 7, v4\ns_add_u32 s42, s27, 24\ns_sub_u32 s43, s42, 26\ns_cbranch_vccz .La1052\ns_add_u32 m0, s26, 69632\ns_nop 0\nglobal_load_lds_dwordx4 v28, s[6:7] offset:0\n"
      ".La1052:\nv_lshl_add_u32 v28, v201, 7, v4\ns_min_u32 s42, s42, s43\ns_add_u32 m0, s53, 45056\ns_nop 0\nglobal_load_lds_dwordx4 v22, s[6:7] offset:0\nv_lshl_add_u32 v22, v177, 7, v4\n"
      "v_lshl_add_u32 v162, s42, 5, v148\nglobal_load_lds_dwordx4 v23, s[44:45] offset:1024\nv_lshl_add_u32 v23, v183, 7, v5\nv_and_b32 v138, s55, v86\n"
      "global_load_lds_dwordx4 v24, s[46:47] offset:2048\nv_lshl_add_u32 v24, v185, 7, v4\nv_xor_b32 v86, v86, v138\nglobal_load_lds_dwordx4 v25, s[48:49] offset:3072\n"
      "v_lshl_add_u32 v25, v191, 7, v5\ns_add_u32 m0, s53, 49152\nv_and_b32 v139, s55, v87\nglobal_load_lds_dwordx4 v26, s[6:7] offset:0\nv_lshl_add_u32 v26, v193, 7, v4\nv_xor_b32 v87, v87, v139\n"
      "s_cmp_lg_u32 s24, 3\ns_cbranch_scc1 .Lo1055\ns_mov_b32 s42, 0\n"
      ".Lq1053:\nv_and_b32 v164, 0xff, v36\nv_xor_b32 v164, s41, v164\nv_xor_b32 v165, s40, v37\nv_or_b32 v164, v164, v165\nv_cmp_ne_u32 s[62:63], 0, v164\nv_lshrrev_b32 v164, 8, v36\n"
      "s_cmp_eq_u64 s[62:63], 0\ns_cbranch_scc1 .Lr1054\ns_sleep 1\nglobal_load_dwordx2 v[36:37], v38, s[58:59] sc1\ns_add_u32 s42, s42, 1\ns_bitcmp0_b32 s42, 22\ns_waitcnt vmcnt(0)\n"
      "s_cbranch_scc1 .Lq1053\n"
      ".Lr1054:\nds_write_b32 v253, v164 offset:24\n"
      ".Lo1055:\ns_waitcnt vmcnt(11) lgkmcnt(0)\ns_cbranch_vccnz .Lw1056\ns_waitcnt vmcnt(10)\n"
      ".Lw1056:\ns_barrier\ns_add_u32 m0, s54, 90112\ns_nop 0\nglobal_load_lds_dwordx4 v236, s[50:51] offset:0\nds_read_b64 v[90:91], v30 offset:0\n"
      "global_load_lds_dwordx4 v237, s[50:51] offset:1024\nglobal_load_lds_dwordx4 v238, s[50:51] offset:2048\nglobal_load_lds_dwordx4 v239, s[50:51] offset:3072\n"
      "v_max3_f16 v32, |v88|, |v89|, v32\nv_max3_f32 v33, |v88|, |v89|, v33\nv_and_b32 v140, s55, v88\ns_add_u32 m0, s26, 110592\nv_xor_b32 v88, v88, v140\n"
      "global_load_lds_dwordx4 v27, s[6:7] offset:0\nv_and_b32 v141, s55, v89\nv_xor_b32 v89, v89, v141\ns_lshl_b32 s42, s24, 6\nv_add_u32 v10, s42, v240\ns_cbranch_vccz .La1057\n"
      "s_add_u32 m0, s26, 114688\ns_nop 0\nglobal_load_lds_dwordx4 v28, s[6:7] offset:0\n"
      ".La1057:\nv_min_u32 v10, 83, v10\nv_and_b32 v11, 3, v10\nv_lshrrev_b32 v12, 2, v10\ns_add_u32 m0, s53, 90112\nv_mul_u32_u24 v11, 22272, v11\nglobal_load_lds_dwordx4 v22, s[6:7] offset:0\n"
      "v_mul_u32_u24 v13, 928, v12\ns_mov_b32 s42, 45920\nv_add3_u32 v11, v11, v13, s42\nglobal_load_lds_dwordx4 v23, s[44:45] offset:1024\ns_mov_b32 s42, 160256\nv_lshl_add_u32 v13, v12, 3, s42\n"
      "s_mov_b32 s42, 160448\nglobal_load_lds_dwordx4 v24, s[46:47] offset:2048\nv_lshl_add_u32 v10, v10, 4, s42\nv_min_u32 v14, 1, v12\nv_lshl_add_u32 v10, v14, 6, v10\n"
      "global_load_lds_dwordx4 v25, s[48:49] offset:3072\ns_bfm_b32 s42, s64, 0\nv_lshrrev_b32 v15, v12, s42\ns_add_u32 m0, s53, 94208\nv_subrev_u32 v14, s64, v12\n"
      "global_load_lds_dwordx4 v26, s[6:7] offset:0\nv_lshl_add_u32 v12, v240, 1, v248\nds_read_u16 v251, v12 offset:64\nds_read_b32 v9, v248 offset:24\ns_waitcnt vmcnt(11) lgkmcnt(0)\n"
      "s_cbranch_vccnz .Lw1058\ns_waitcnt vmcnt(10)\n"
      ".Lw1058:\ns_barrier\ns_add_u32 m0, s56, 0\ns_nop 0\nglobal_load_lds_dwordx4 v252, s[60:61] offset:0 sc1\nds_read_b64 v[92:93], v30 offset:45056\n"
      "global_load_lds_dwordx4 v252, s[60:61] offset:1024 sc1\nglobal_load_lds_dwordx4 v252, s[60:61] offset:2048 sc1\nglobal_load_lds_dwordx4 v252, s[60:61] offset:3072 sc1\n"
      "s_add_u32 s60, s60, 4096\ns_addc_u32 s61, s61, 0\nv_max3_f16 v32, |v90|, |v91|, v32\nv_max3_f32 v33, |v90|, |v91|, v33\nv_and_b32 v142, s55, v90\ns_add_u32 m0, s56, 4096\n"
      "v_xor_b32 v90, v90, v142\nglobal_load_lds_dwordx4 v252, s[60:61] offset:0 sc1\nv_and_b32 v143, s55, v91\nv_xor_b32 v91, v91, v143\nv_readfirstlane_b32 s39, v9\nv_readlane_b32 s57, v251, 0\n"
      "global_load_lds_dwordx4 v252, s[60:61] offset:1024 sc1\ns_xor_b32 s40, s39, 0xa5c30f96\ns_mul_i32 s41, s39, 0x9E3779B1\ns_lshr_b32 s41, s41, 24\ns_lshl_b32 s42, s2, 3\n"
      "global_load_lds_dwordx4 v252, s[60:61] offset:2048 sc1\ns_add_u32 s42, s42, 24576\nv_mov_b32 v7, s42\nv_mov_b32 v8, s41\nv_mov_b32 v9, s40\n"
      "global_load_lds_dwordx4 v252, s[60:61] offset:3072 sc1\ns_add_u32 s60, s60, 4096\ns_addc_u32 s61, s61, 0\ns_lshl_b32 s42, s2, 2\nv_mov_b32 v12, s42\ns_add_u32 s42, s37, 1\n"
      "s_add_u32 m0, s56, 8192\ns_nop 0\nglobal_load_lds_dwordx4 v252, s[60:61] offset:0 sc1\nv_mov_b32 v6, s42\nv_and_b32 v1, 48, v240\ns_mov_b32 s42, 41984\n"
      "global_load_lds_dwordx4 v252, s[60:61] offset:1024 sc1\nv_lshl_add_u32 v1, v1, 2, s42\ns_sub_u32 s49, s65, 928\n"
      "v_add_u32_sdwa v236, s49, v244 dst_sel:DWORD dst_unused:UNUSED_PAD src0_sel:DWORD src1_sel:WORD_0\nglobal_load_lds_dwordx4 v252, s[60:61] offset:1536 sc1\n"
      "v_add_u32_sdwa v237, s49, v244 dst_sel:DWORD dst_unused:UNUSED_PAD src0_sel:DWORD src1_sel:WORD_1\n"
      "v_add_u32_sdwa v238, s49, v245 dst_sel:DWORD dst_unused:UNUSED_PAD src0_sel:DWORD src1_sel:WORD_0\n"
      "v_add_u32_sdwa v239, s49, v245 dst_sel:DWORD dst_unused:UNUSED_PAD src0_sel:DWORD src1_sel:WORD_1\ns_waitcnt vmcnt(11) lgkmcnt(0)\ns_barrier\ns_setprio 3\n"
      "ds_read_b64 v[94:95], v31 offset:0\nv_max3_f16 v32, |v92|, |v93|, v32\nv_max3_f32 v33, |v92|, |v93|, v33\nv_and_b32 v144, s55, v92\nv_xor_b32 v92, v92, v144\nv_and_b32 v145, s55, v93\n"
      "v_xor_b32 v93, v93, v145\ns_lshl_b32 s42, s30, 6\ns_add_u32 s42, s42, s36\ns_add_u32 s42, s42, 60\ns_mulk_i32 s42, 896\ns_add_u32 s44, s16, s42\ns_addc_u32 s45, s17, 0\n"
      "s_waitcnt lgkmcnt(0)\nv_max3_f16 v32, |v94|, |v95|, v32\nv_max3_f32 v33, |v94|, |v95|, v33\nv_and_b32 v146, s55, v94\nv_xor_b32 v94, v94, v146\nv_and_b32 v147, s55, v95\n"
      "v_xor_b32 v95, v95, v147\nv_lshlrev_b32 v32, 16, v32\nv_max_u32 v33, v32, v33\ns_nop 1\nv_max_u32_dpp v33, v33, v33 quad_perm:[1,0,3,2] row_mask:0xf bank_mask:0xf\ns_nop 1\n"
      "v_max_u32_dpp v33, v33, v33 quad_perm:[2,3,0,1] row_mask:0xf bank_mask:0xf\nv_lshrrev_b32 v33, 23, v33\nv_subrev_u32 v32, 22, v33\nv_mul_u32_u24 v32, 0x800080, v32\n"
      "v_pk_sub_u16 v44, v44, v32 clamp\nv_pk_sub_u16 v45, v45, v32 clamp\nv_pk_sub_u16 v46, v46, v32 clamp\nv_pk_sub_u16 v47, v47, v32 clamp\nv_lshl_or_b32 v44, v44, 3, v96\n"
      "v_lshl_or_b32 v45, v45, 3, v97\nv_lshl_or_b32 v46, v46, 3, v98\nv_lshl_or_b32 v47, v47, 3, v99\nv_pk_sub_u16 v48, v48, v32 clamp\nv_pk_sub_u16 v49, v49, v32 clamp\n"
      "v_pk_sub_u16 v50, v50, v32 clamp\nv_pk_sub_u16 v51, v51, v32 clamp\nv_lshl_or_b32 v48, v48, 3, v100\nv_lshl_or_b32 v49, v49, 3, v101\nv_lshl_or_b32 v50, v50, 3, v102\n"
      "v_lshl_or_b32 v51, v51, 3, v103\nv_pk_sub_u16 v52, v52, v32 clamp\nv_pk_sub_u16 v53, v53, v32 clamp\nv_pk_sub_u16 v54, v54, v32 clamp\nv_pk_sub_u16 v55, v55, v32 clamp\n"
      "v_lshl_or_b32 v52, v52, 3, v104\nv_lshl_or_b32 v53, v53, 3, v105\nv_lshl_or_b32 v54, v54, 3, v106\nv_lshl_or_b32 v55, v55, 3, v107\nv_pk_sub_u16 v56, v56, v32 clamp\n"
      "v_pk_sub_u16 v57, v57, v32 clamp\nv_pk_sub_u16 v58, v58, v32 clamp\nv_pk_sub_u16 v59, v59, v32 clamp\nv_lshl_or_b32 v56, v56, 3, v108\nv_lshl_or_b32 v57, v57, 3, v109\n"
      "v_lshl_or_b32 v58, v58, 3, v110\nv_lshl_or_b32 v59, v59, 3, v111\nv_pk_sub_u16 v60, v60, v32 clamp\nv_pk_sub_u16 v61, v61, v32 clamp\nv_pk_sub_u16 v62, v62, v32 clamp\n"
      "v_pk_sub_u16 v63, v63, v32 clamp\nv_lshl_or_b32 v60, v60, 3, v112\nv_lshl_or_b32 v61, v61, 3, v113\nv_lshl_or_b32 v62, v62, 3, v114\nv_lshl_or_b32 v63, v63, 3, v115\n"
      "v_pk_sub_u16 v64, v64, v32 clamp\nv_pk_sub_u16 v65, v65, v32 clamp\nv_pk_sub_u16 v66, v66, v32 clamp\nv_pk_sub_u16 v67, v67, v32 clamp\nv_lshl_or_b32 v64, v64, 3, v116\n"
      "v_lshl_or_b32 v65, v65, 3, v117\nv_lshl_or_b32 v66, v66, 3, v118\nv_lshl_or_b32 v67, v67, 3, v119\ns_waitcnt vmcnt(0)\ns_barrier\nv_pk_sub_u16 v68, v68, v32 clamp\n"
      "v_pk_sub_u16 v69, v69, v32 clamp\nv_pk_sub_u16 v70, v70, v32 clamp\nv_pk_sub_u16 v71, v71, v32 clamp\nv_lshl_or_b32 v68, v68, 3, v120\nv_lshl_or_b32 v69, v69, 3, v121\n"
      "v_lshl_or_b32 v70, v70, 3, v122\nv_lshl_or_b32 v71, v71, 3, v123\nv_pk_sub_u16 v72, v72, v32 clamp\nv_pk_sub_u16 v73, v73, v32 clamp\nv_pk_sub_u16 v74, v74, v32 clamp\n"
      "v_pk_sub_u16 v75, v75, v32 clamp\nv_lshl_or_b32 v72, v72, 3, v124\nv_lshl_or_b32 v73, v73, 3, v125\nv_lshl_or_b32 v74, v74, 3, v126\nv_lshl_or_b32 v75, v75, 3, v127\n"
      "v_pk_sub_u16 v76, v76, v32 clamp\nv_pk_sub_u16 v77, v77, v32 clamp\nv_pk_sub_u16 v78, v78, v32 clamp\nv_pk_sub_u16 v79, v79, v32 clamp\nv_lshl_or_b32 v76, v76, 3, v128\n"
      "v_lshl_or_b32 v77, v77, 3, v129\nv_lshl_or_b32 v78, v78, 3, v130\nv_lshl_or_b32 v79, v79, 3, v131\nv_pk_sub_u16 v80, v80, v32 clamp\nv_pk_sub_u16 v81, v81, v32 clamp\n"
      "v_pk_sub_u16 v82, v82, v32 clamp\nv_pk_sub_u16 v83, v83, v32 clamp\nv_lshl_or_b32 v80, v80, 3, v132\nv_lshl_or_b32 v81, v81, 3, v133\nv_lshl_or_b32 v82, v82, 3, v134\n"
      "v_lshl_or_b32 v83, v83, 3, v135\nv_pk_sub_u16 v84, v84, v32 clamp\nv_pk_sub_u16 v85, v85, v32 clamp\nv_pk_sub_u16 v86, v86, v32 clamp\nv_pk_sub_u16 v87, v87, v32 clamp\n"
      "v_lshl_or_b32 v84, v84, 3, v136\nv_lshl_or_b32 v85, v85, 3, v137\nv_lshl_or_b32 v86, v86, 3, v138\nv_lshl_or_b32 v87, v87, 3, v139\nv_pk_sub_u16 v88, v88, v32 clamp\n"
      "v_pk_sub_u16 v89, v89, v32 clamp\nv_pk_sub_u16 v90, v90, v32 clamp\nv_pk_sub_u16 v91, v91, v32 clamp\nv_lshl_or_b32 v88, v88, 3, v140\nv_lshl_or_b32 v89, v89, 3, v141\n"
      "v_lshl_or_b32 v90, v90, 3, v142\nv_lshl_or_b32 v91, v91, 3, v143\nv_pk_sub_u16 v92, v92, v32 clamp\nv_pk_sub_u16 v93, v93, v32 clamp\nv_pk_sub_u16 v94, v94, v32 clamp\n"
      "v_pk_sub_u16 v95, v95, v32 clamp\nv_lshl_or_b32 v92, v92, 3, v144\nv_lshl_or_b32 v93, v93, 3, v145\nv_lshl_or_b32 v94, v94, 3, v146\nv_lshl_or_b32 v95, v95, 3, v147\nv_sub_u32 v4, 257, v33\n"
      "v_lshlrev_b32 v4, 23, v4\nv_cvt_pkrtz_f16_f32 v4, v4, v4\nv_lshlrev_b32 v33, 2, v33\nv_mov_b32 v5, v4\nv_subrev_u32 v33, 536, v33\ns_setprio 0\nds_write_b64 v148, v[4:5] offset:832\n"
      "ds_write_b32 v149, v33 offset:864\nds_write2_b64 v150, v[44:45], v[46:47] offset1:4\nds_write2_b64 v151, v[48:49], v[50:51] offset1:4\nds_write2_b64 v152, v[52:53], v[54:55] offset1:4\n"
      "ds_write2_b64 v153, v[56:57], v[58:59] offset1:4\nds_write2_b64 v154, v[60:61], v[62:63] offset1:4\nds_write2_b64 v155, v[64:65], v[66:67] offset1:4\n"
      "ds_write2_b64 v156, v[68:69], v[70:71] offset1:4\nds_write2_b64 v157, v[72:73], v[74:75] offset1:4\nds_write2_b64 v158, v[76:77], v[78:79] offset1:4\n"
      "ds_write2_b64 v159, v[80:81], v[82:83] offset1:4\nds_write2_b64 v160, v[84:85], v[86:87] offset1:4\nds_write2_b64 v161, v[88:89], v[90:91] offset1:4\n"
      "ds_write2_b64 v162, v[92:93], v[94:95] offset1:4\nds_read_b128 v[16:19], v163 offset:6496\nds_read_b128 v[20:23], v163 offset:5568\nds_read_b128 v[24:27], v163 offset:4640\n"
      "s_cmp_gt_u32 s64, 12\ns_cbranch_scc1 .Ld1060\nds_read_b128 v[28:31], v163 offset:3712\nds_read_b128 v[32:35], v163 offset:2784\nds_read_b128 v[36:39], v163 offset:1856\n"
      "ds_read_b128 v[40:43], v163 offset:928\nds_read_b128 v[44:47], v163 offset:0\n"
      ".Ld1060:\ns_mov_b32 exec_hi, 0x7fffff\ns_waitcnt lgkmcnt(0)\ns_cmp_gt_u32 s64, 15\ns_cbranch_scc1 .Ld1061\nglobal_store_dwordx4 v252, v[16:19], s[44:45] sc1\ns_sub_u32 s44, s44, 3584\n"
      "s_subb_u32 s45, s45, 0\ns_cmp_gt_u32 s64, 14\ns_cbranch_scc1 .Ld1061\nglobal_store_dwordx4 v252, v[20:23], s[44:45] sc1\ns_sub_u32 s44, s44, 3584\ns_subb_u32 s45, s45, 0\n"
      "s_cmp_gt_u32 s64, 13\ns_cbranch_scc1 .Ld1061\nglobal_store_dwordx4 v252, v[24:27], s[44:45] sc1\ns_sub_u32 s44, s44, 3584\ns_subb_u32 s45, s45, 0\ns_cmp_gt_u32 s64, 12\n"
      "s_cbranch_scc1 .Ld1061\nglobal_store_dwordx4 v252, v[28:31], s[44:45] sc1\ns_sub_u32 s44, s44, 3584\ns_subb_u32 s45, s45, 0\ns_cmp_gt_u32 s64, 11\ns_cbranch_scc1 .Ld1061\n"
      "global_store_dwordx4 v252, v[32:35], s[44:45] sc1\ns_sub_u32 s44, s44, 3584\ns_subb_u32 s45, s45, 0\ns_cmp_gt_u32 s64, 10\ns_cbranch_scc1 .Ld1061\n"
      "global_store_dwordx4 v252, v[36:39], s[44:45] sc1\ns_sub_u32 s44, s44, 3584\ns_subb_u32 s45, s45, 0\ns_cmp_gt_u32 s64, 9\ns_cbranch_scc1 .Ld1061\n"
      "global_store_dwordx4 v252, v[40:43], s[44:45] sc1\ns_sub_u32 s44, s44, 3584\ns_subb_u32 s45, s45, 0\ns_cmp_gt_u32 s64, 8\ns_cbranch_scc1 .Ld1061\n"
      "global_store_dwordx4 v252, v[44:47], s[44:45] sc1\ns_sub_u32 s44, s44, 3584\ns_subb_u32 s45, s45, 0\n"
      ".Ld1061:\ns_mov_b32 exec_hi, -1\nds_read_b128 v[48:51], v1 offset:0\nds_read_b128 v[52:55], v1 offset:16\nds_read_b128 v[56:59], v1 offset:32\nds_read_b128 v[60:63], v1 offset:48\n"
      "ds_read_b128 v[72:75], v252 offset:0\nds_read_b128 v[76:79], v252 offset:1024\nds_read_b128 v[80:83], v252 offset:2048\nds_read_b128 v[84:87], v252 offset:3072\n"
      "ds_read_b128 v[88:91], v252 offset:4096\nds_read_b128 v[92:95], v252 offset:5120\nds_read_b128 v[96:99], v252 offset:6144\nds_read_b128 v[100:103], v252 offset:7168\n"
      "ds_read_b128 v[104:107], v252 offset:8192\nds_read_b128 v[108:111], v252 offset:9216\nds_read_b128 v[112:115], v252 offset:10240\nds_read_b128 v[116:119], v252 offset:11264\n"
      "ds_read_b128 v[120:123], v252 offset:12288\nds_read_b128 v[124:127], v252 offset:13312\nds_read_b128 v[128:131], v252 offset:14336\nds_read_b128 v[132:135], v252 offset:15360\n"
      "ds_read_b128 v[136:139], v252 offset:16384\nds_read_b128 v[140:143], v252 offset:17408\nds_read_b128 v[144:147], v252 offset:18432\nds_read_b128 v[148:151], v252 offset:19456\n"
      "ds_read_b128 v[152:155], v252 offset:20480\nds_read_b128 v[156:159], v252 offset:21504\nds_read_b128 v[160:163], v252 offset:22528\nds_read_b128 v[164:167], v252 offset:23552\n"
      "ds_read_b128 v[168:171], v252 offset:24576\nds_read_b128 v[172:175], v252 offset:25600\nds_read_b128 v[176:179], v252 offset:26624\nds_read_b128 v[180:183], v252 offset:27648\n"
      "ds_read_b128 v[184:187], v252 offset:28672\nds_read_b128 v[188:191], v252 offset:29696\nds_read_b128 v[192:195], v252 offset:30720\nds_read_b128 v[196:199], v252 offset:31744\n"
      "ds_read_b128 v[200:203], v252 offset:32768\nds_read_b128 v[204:207], v252 offset:33792\nds_read_b128 v[208:211], v252 offset:34816\nds_read_b128 v[212:215], v252 offset:35840\n"
      "ds_read_b128 v[216:219], v252 offset:36864\nds_read_b128 v[220:223], v252 offset:37888\nds_read_b128 v[224:227], v252 offset:38912\nds_read_b128 v[228:231], v252 offset:39936\n"
      "ds_read_b128 v[232:235], v252 offset:40960\ns_mov_b32 s55, s57\ns_mov_b32 s45, -1\ns_mov_b32 s46, 0\ns_setprio 3\ns_waitcnt lgkmcnt(15)\nv_mul_f32 v48, v48, v56\nv_mul_f32 v49, v49, v57\n"
      "v_mul_f32 v50, v50, v58\nv_mul_f32 v51, v51, v59\nv_mul_f32 v52, v52, v60\nv_mul_f32 v53, v53, v61\nv_mul_f32 v54, v54, v62\nds_write_b128 v1, v[48:51] offset:256\n"
      "ds_write_b128 v1, v[52:55] offset:272\nds_read_b64_tr_b16 v[68:69], v238 offset:928\nds_read_b64_tr_b16 v[70:71], v239 offset:928\nds_read_b64_tr_b16 v[56:57], v236 offset:928\n"
      "ds_read_b64_tr_b16 v[58:59], v237 offset:1056\nds_read_b64_tr_b16 v[60:61], v237 offset:1184\nds_read_b64_tr_b16 v[62:63], v237 offset:1312\nds_read_b64_tr_b16 v[64:65], v237 offset:1440\n"
      "ds_read_b64_tr_b16 v[66:67], v237 offset:1568\ns_swappc_b64 s[4:5], s[34:35]\ns_waitcnt vmcnt(0)\nds_read_b128 v[44:47], v248 offset:48\nds_add_rtn_u32 v0, v253, v241 offset:32\n"
      "s_waitcnt lgkmcnt(0)\nv_readfirstlane_b32 s42, v0\ns_cmp_lg_u32 s42, 3\ns_cbranch_scc1 .Lf1062\ns_mov_b64 exec, 1\nglobal_store_dwordx2 v7, v[8:9], s[20:21] sc1\n"
      "global_store_dword v12, v6, s[20:21] sc1\ns_mov_b64 exec, -1\n"
      ".Lf1062:\nv_readlane_b32 s48, v69, 48\ns_mov_b32 s56, 0\ns_mov_b32 s44, 0\ns_mov_b32 s33, s45\nv_pk_mul_f16 v20, v68, v70\nv_pk_mul_f16 v21, v69, v71\n"
      "v_pk_mul_f16 v22, v56, v56 op_sel_hi:[0,1]\nv_pk_mul_f16 v23, v56, v57 op_sel_hi:[0,1]\nv_pk_mul_f16 v16, v68, s48 op_sel:[0,1]\nv_pk_mul_f16 v17, v69, s48 op_sel:[0,1]\n"
      "v_pk_mul_f16 v18, v70, s48 op_sel:[0,1]\nv_pk_mul_f16 v19, v71, s48 op_sel:[0,1]\n.p2align 3\ns_nop 0\ns_barrier\nv_mfma_f32_16x16x32_f16 v[40:43], v[76:79], v[16:19], 0\ns_setprio 0\n"
      "v_mfma_f32_16x16x32_f16 v[40:43], v[88:91], v[20:23], v[40:43]\ns_add_u32 s45, s45, 2\ns_cmp_ge_u32 s45, s46\ns_cbranch_scc1 .Lc_t0\n"
      ".Lc_b0:\nv_mfma_f32_16x16x32_f16 v[36:39], v[84:87], v[20:23], 0\nv_pk_mul_f16 v24, v66, v66 op_sel_hi:[0,1]\nv_pk_mul_f16 v25, v66, v57 op_sel_hi:[0,1]\n"
      "v_mfma_f32_16x16x32_f16 v[36:39], v[72:75], v[16:19], v[36:39]\nv_pk_mul_f16 v26, v56, v66 op_sel_hi:[0,1]\nv_pk_mul_f16 v27, v56, v67 op_sel_hi:[0,1]\n"
      "v_mfma_f32_16x16x32_f16 v[32:35], v[80:83], v[20:23], 0\nv_pk_mul_f16 v28, v58, v66 op_sel_hi:[0,1]\nv_pk_mul_f16 v29, v58, v67 op_sel_hi:[0,1]\ns_setprio 1\ns_waitcnt lgkmcnt(0)\n"
      "v_smfmac_f32_16x16x64_f16 v[44:47], v[92:95], v[16:23], v242\nv_pk_mul_f16 v30, v60, v66 op_sel_hi:[0,1]\nv_pk_mul_f16 v31, v60, v67 op_sel_hi:[0,1]\n"
      "v_mfma_f32_16x16x32_f16 v[32:35], v[96:99], v[24:27], v[32:35]\nv_pk_mul_f16 v16, v62, v66 op_sel_hi:[0,1]\nv_pk_mul_f16 v17, v62, v67 op_sel_hi:[0,1]\n"
      "v_mfma_f32_16x16x32_f16 v[32:35], v[108:111], v[28:31], v[32:35]\nv_pk_mul_f16 v18, v64, v66 op_sel_hi:[0,1]\nv_mfma_f32_16x16x32_f16 v[36:39], v[112:115], v[28:31], v[36:39]\n"
      "v_pk_mul_f16 v19, v64, v67 op_sel_hi:[0,1]\nv_mfma_f32_16x16x32_f16 v[36:39], v[100:103], v[24:27], v[36:39]\nv_pk_mul_f16 v20, v64, v64 op_sel_hi:[0,1]\n"
      "v_mfma_f32_16x16x32_f16 v[40:43], v[104:107], v[24:27], v[40:43]\nv_pk_mul_f16 v21, v64, v57 op_sel_hi:[0,1]\nv_mfma_f32_16x16x32_f16 v[40:43], v[116:119], v[28:31], v[40:43]\n"
      "v_pk_mul_f16 v22, v56, v64 op_sel_hi:[0,1]\nv_smfmac_f32_16x16x64_f16 v[44:47], v[120:123], v[24:31], v242\nv_pk_mul_f16 v23, v56, v65 op_sel_hi:[0,1]\ns_setprio 2\n"
      "v_mfma_f32_16x16x32_f16 v[32:35], v[124:127], v[16:19], v[32:35]\nv_pk_mul_f16 v24, v58, v64 op_sel_hi:[0,1]\nv_pk_mul_f16 v25, v58, v65 op_sel_hi:[0,1]\n"
      "ds_read_b64_tr_b16 v[70:71], v239 offset:1856\nv_mfma_f32_16x16x32_f16 v[32:35], v[136:139], v[20:23], v[32:35]\nv_pk_mul_f16 v26, v60, v64 op_sel_hi:[0,1]\n"
      "ds_read_b64_tr_b16 v[68:69], v238 offset:1856\nv_mfma_f32_16x16x32_f16 v[36:39], v[140:143], v[20:23], v[36:39]\nv_pk_mul_f16 v27, v60, v65 op_sel_hi:[0,1]\n"
      "ds_read_b64_tr_b16 v[66:67], v237 offset:2496\nv_mfma_f32_16x16x32_f16 v[36:39], v[128:131], v[16:19], v[36:39]\nv_pk_mul_f16 v28, v62, v64 op_sel_hi:[0,1]\n"
      "v_mfma_f32_16x16x32_f16 v[40:43], v[132:135], v[16:19], v[40:43]\nv_pk_mul_f16 v29, v62, v65 op_sel_hi:[0,1]\nv_mfma_f32_16x16x32_f16 v[40:43], v[144:147], v[20:23], v[40:43]\n"
      "v_pk_mul_f16 v30, v62, v62 op_sel_hi:[0,1]\nv_smfmac_f32_16x16x64_f16 v[44:47], v[148:151], v[16:23], v242\nv_pk_mul_f16 v31, v62, v57 op_sel_hi:[0,1]\n"
      "v_mfma_f32_16x16x32_f16 v[32:35], v[152:155], v[24:27], v[32:35]\nv_pk_mul_f16 v16, v56, v62 op_sel_hi:[0,1]\nv_pk_mul_f16 v17, v56, v63 op_sel_hi:[0,1]\n"
      "v_mfma_f32_16x16x32_f16 v[32:35], v[164:167], v[28:31], v[32:35]\nv_pk_mul_f16 v18, v58, v62 op_sel_hi:[0,1]\nds_read_b64_tr_b16 v[64:65], v237 offset:2368\n"
      "v_mfma_f32_16x16x32_f16 v[36:39], v[168:171], v[28:31], v[36:39]\nv_pk_mul_f16 v19, v58, v63 op_sel_hi:[0,1]\ns_setprio 3\nv_mfma_f32_16x16x32_f16 v[36:39], v[156:159], v[24:27], v[36:39]\n"
      "v_pk_mul_f16 v20, v60, v62 op_sel_hi:[0,1]\nv_mfma_f32_16x16x32_f16 v[40:43], v[160:163], v[24:27], v[40:43]\nv_pk_mul_f16 v21, v60, v63 op_sel_hi:[0,1]\ns_waitcnt lgkmcnt(1)\n"
      "v_readlane_b32 s48, v69, 48\nv_mfma_f32_16x16x32_f16 v[40:43], v[172:175], v[28:31], v[40:43]\nv_pk_mul_f16 v22, v60, v60 op_sel_hi:[0,1]\n"
      "v_smfmac_f32_16x16x64_f16 v[44:47], v[176:179], v[24:31], v242\nv_pk_mul_f16 v23, v60, v57 op_sel_hi:[0,1]\nv_mfma_f32_16x16x32_f16 v[32:35], v[180:183], v[16:19], v[32:35]\n"
      "v_pk_mul_f16 v24, v56, v60 op_sel_hi:[0,1]\nv_pk_mul_f16 v25, v56, v61 op_sel_hi:[0,1]\nv_mfma_f32_16x16x32_f16 v[32:35], v[192:195], v[20:23], v[32:35]\n"
      "v_pk_mul_f16 v26, v58, v60 op_sel_hi:[0,1]\nv_pk_mul_f16 v27, v58, v61 op_sel_hi:[0,1]\nv_mfma_f32_16x16x32_f16 v[36:39], v[196:199], v[20:23], v[36:39]\n"
      "v_pk_mul_f16 v28, v58, v58 op_sel_hi:[0,1]\nds_read_b64_tr_b16 v[62:63], v237 offset:2240\nv_mfma_f32_16x16x32_f16 v[36:39], v[184:187], v[16:19], v[36:39]\n"
      "v_pk_mul_f16 v29, v58, v57 op_sel_hi:[0,1]\nds_read_b64_tr_b16 v[60:61], v237 offset:2112\nv_mfma_f32_16x16x32_f16 v[40:43], v[188:191], v[16:19], v[40:43]\n"
      "v_pk_mul_f16 v30, v56, v58 op_sel_hi:[0,1]\nv_mfma_f32_16x16x32_f16 v[40:43], v[200:203], v[20:23], v[40:43]\nv_pk_mul_f16 v31, v56, v59 op_sel_hi:[0,1]\n"
      "v_smfmac_f32_16x16x64_f16 v[44:47], v[204:207], v[16:23], v242\nds_read_b64_tr_b16 v[56:57], v236 offset:1856\nds_read_b64_tr_b16 v[58:59], v237 offset:1984\n"
      "v_smfmac_f32_16x16x64_f16 v[44:47], v[232:235], v[24:31], v242\nv_pk_mul_f16 v16, v68, s48 op_sel:[0,1]\nv_pk_mul_f16 v17, v69, s48 op_sel:[0,1]\n"
      "v_mfma_f32_16x16x32_f16 v[32:35], v[208:211], v[24:27], v[32:35]\nv_pk_mul_f16 v18, v70, s48 op_sel:[0,1]\nv_pk_mul_f16 v19, v71, s48 op_sel:[0,1]\n"
      "v_mfma_f32_16x16x32_f16 v[32:35], v[220:223], v[28:31], v[32:35]\nv_pk_mul_f16 v20, v68, v70\nv_pk_mul_f16 v21, v69, v71\nv_mfma_f32_16x16x32_f16 v[36:39], v[224:227], v[28:31], v[36:39]\n"
      "v_add_f32 v44, v44, v45\nv_add_f32 v46, v46, v47\nv_mfma_f32_16x16x32_f16 v[36:39], v[212:215], v[24:27], v[36:39]\nv_mul_f32 v55, v44, v54\nv_mul_f32 v55, v55, v46\n"
      "ds_read_b128 v[44:47], v248 offset:48\nv_mfma_f32_16x16x32_f16 v[40:43], v[216:219], v[24:27], v[40:43]\ns_waitcnt lgkmcnt(1)\nv_pk_mul_f16 v22, v56, v56 op_sel_hi:[0,1]\n"
      "v_pk_mul_f16 v23, v56, v57 op_sel_hi:[0,1]\nv_mfma_f32_16x16x32_f16 v[40:43], v[228:231], v[28:31], v[40:43]\nv_mul_f32 v32, v32, v48\nv_mul_f32 v33, v33, v49\nv_mul_f32 v34, v34, v50\n"
      ".Lc_u1:\nv_mfma_f32_16x16x32_f16 v[244:247], v[76:79], v[16:19], 0\nv_fmac_f32 v55, v32, v36\nv_mul_f32 v35, v35, v51\nv_fmac_f32 v55, v33, v37\ns_setprio 0\n"
      "v_mfma_f32_16x16x32_f16 v[244:247], v[88:91], v[20:23], v[244:247]\nv_fmac_f32 v55, v34, v38\nv_fmac_f32 v55, v35, v39\ns_add_u32 s45, s45, 2\ns_cmp_ge_u32 s45, s46\ns_cbranch_scc1 .Lc_t1\n"
      ".Lc_b1:\nv_mfma_f32_16x16x32_f16 v[36:39], v[84:87], v[20:23], 0\nv_pk_mul_f16 v24, v66, v66 op_sel_hi:[0,1]\nv_pk_mul_f16 v25, v66, v57 op_sel_hi:[0,1]\n"
      "v_mfma_f32_16x16x32_f16 v[36:39], v[72:75], v[16:19], v[36:39]\nv_pk_mul_f16 v26, v56, v66 op_sel_hi:[0,1]\nv_pk_mul_f16 v27, v56, v67 op_sel_hi:[0,1]\n"
      "v_mfma_f32_16x16x32_f16 v[32:35], v[80:83], v[20:23], 0\nv_pk_mul_f16 v28, v58, v66 op_sel_hi:[0,1]\nv_pk_mul_f16 v29, v58, v67 op_sel_hi:[0,1]\ns_setprio 1\ns_waitcnt lgkmcnt(0)\n"
      "v_smfmac_f32_16x16x64_f16 v[44:47], v[92:95], v[16:23], v242\nv_pk_mul_f16 v30, v60, v66 op_sel_hi:[0,1]\nv_pk_mul_f16 v31, v60, v67 op_sel_hi:[0,1]\n"
      "v_mfma_f32_16x16x32_f16 v[32:35], v[96:99], v[24:27], v[32:35]\nv_pk_mul_f16 v16, v62, v66 op_sel_hi:[0,1]\nv_pk_mul_f16 v17, v62, v67 op_sel_hi:[0,1]\n"
      "v_mfma_f32_16x16x32_f16 v[32:35], v[108:111], v[28:31], v[32:35]\nv_pk_mul_f16 v18, v64, v66 op_sel_hi:[0,1]\nv_mul_f32 v40, v40, v52\n"
      "v_mfma_f32_16x16x32_f16 v[36:39], v[112:115], v[28:31], v[36:39]\nv_pk_mul_f16 v19, v64, v67 op_sel_hi:[0,1]\nv_mul_f32 v41, v41, v53\n"
      "v_mfma_f32_16x16x32_f16 v[36:39], v[100:103], v[24:27], v[36:39]\nv_pk_mul_f16 v20, v64, v64 op_sel_hi:[0,1]\nv_fmac_f32 v55, v40, v42\n"
      "v_mfma_f32_16x16x32_f16 v[244:247], v[104:107], v[24:27], v[244:247]\nv_pk_mul_f16 v21, v64, v57 op_sel_hi:[0,1]\nv_fmac_f32 v55, v41, v43\n"
      "v_mfma_f32_16x16x32_f16 v[244:247], v[116:119], v[28:31], v[244:247]\nv_pk_mul_f16 v22, v56, v64 op_sel_hi:[0,1]\nv_smfmac_f32_16x16x64_f16 v[44:47], v[120:123], v[24:31], v242\n"
      "v_pk_mul_f16 v23, v56, v65 op_sel_hi:[0,1]\ns_setprio 2\nv_mfma_f32_16x16x32_f16 v[32:35], v[124:127], v[16:19], v[32:35]\nv_pk_mul_f16 v24, v58, v64 op_sel_hi:[0,1]\n"
      "v_pk_mul_f16 v25, v58, v65 op_sel_hi:[0,1]\nds_read_b64_tr_b16 v[70:71], v239 offset:3712\nv_mfma_f32_16x16x32_f16 v[32:35], v[136:139], v[20:23], v[32:35]\n"
      "v_pk_mul_f16 v26, v60, v64 op_sel_hi:[0,1]\nds_read_b64_tr_b16 v[68:69], v238 offset:3712\nv_mfma_f32_16x16x32_f16 v[36:39], v[140:143], v[20:23], v[36:39]\n"
      "v_pk_mul_f16 v27, v60, v65 op_sel_hi:[0,1]\nds_read_b64_tr_b16 v[66:67], v237 offset:4352\nv_mfma_f32_16x16x32_f16 v[36:39], v[128:131], v[16:19], v[36:39]\n"
      "v_pk_mul_f16 v28, v62, v64 op_sel_hi:[0,1]\nv_add_f32_dpp v55, v55, v55 quad_perm:[1,0,3,2] row_mask:0xf bank_mask:0xf\nv_mfma_f32_16x16x32_f16 v[244:247], v[132:135], v[16:19], v[244:247]\n"
      "v_pk_mul_f16 v29, v62, v65 op_sel_hi:[0,1]\nv_add_f32_dpp v55, v55, v55 quad_perm:[2,3,0,1] row_mask:0xf bank_mask:0xf\nv_mfma_f32_16x16x32_f16 v[244:247], v[144:147], v[20:23], v[244:247]\n"
      "v_pk_mul_f16 v30, v62, v62 op_sel_hi:[0,1]\nv_add_f32_dpp v55, v55, v55 row_half_mirror row_mask:0xf bank_mask:0xf\nv_smfmac_f32_16x16x64_f16 v[44:47], v[148:151], v[16:23], v242\n"
      "v_pk_mul_f16 v31, v62, v57 op_sel_hi:[0,1]\nv_add_f32_dpp v55, v55, v55 row_mirror row_mask:0xf bank_mask:0xf\nv_mfma_f32_16x16x32_f16 v[32:35], v[152:155], v[24:27], v[32:35]\n"
      "v_pk_mul_f16 v16, v56, v62 op_sel_hi:[0,1]\nv_pk_mul_f16 v17, v56, v63 op_sel_hi:[0,1]\nds_write_b32 v243, v55 offset:0\nv_mfma_f32_16x16x32_f16 v[32:35], v[164:167], v[28:31], v[32:35]\n"
      "v_pk_mul_f16 v18, v58, v62 op_sel_hi:[0,1]\nds_read_b64_tr_b16 v[64:65], v237 offset:4224\nv_mfma_f32_16x16x32_f16 v[36:39], v[168:171], v[28:31], v[36:39]\n"
      "v_pk_mul_f16 v19, v58, v63 op_sel_hi:[0,1]\ns_setprio 3\nv_mfma_f32_16x16x32_f16 v[36:39], v[156:159], v[24:27], v[36:39]\nv_pk_mul_f16 v20, v60, v62 op_sel_hi:[0,1]\n"
      "v_mfma_f32_16x16x32_f16 v[244:247], v[160:163], v[24:27], v[244:247]\nv_pk_mul_f16 v21, v60, v63 op_sel_hi:[0,1]\ns_waitcnt lgkmcnt(1)\nv_readlane_b32 s48, v69, 48\n"
      "v_mfma_f32_16x16x32_f16 v[244:247], v[172:175], v[28:31], v[244:247]\nv_pk_mul_f16 v22, v60, v60 op_sel_hi:[0,1]\nv_smfmac_f32_16x16x64_f16 v[44:47], v[176:179], v[24:31], v242\n"
      "v_pk_mul_f16 v23, v60, v57 op_sel_hi:[0,1]\nv_mfma_f32_16x16x32_f16 v[32:35], v[180:183], v[16:19], v[32:35]\nv_pk_mul_f16 v24, v56, v60 op_sel_hi:[0,1]\n"
      "v_pk_mul_f16 v25, v56, v61 op_sel_hi:[0,1]\nv_mfma_f32_16x16x32_f16 v[32:35], v[192:195], v[20:23], v[32:35]\nv_pk_mul_f16 v26, v58, v60 op_sel_hi:[0,1]\n"
      "v_pk_mul_f16 v27, v58, v61 op_sel_hi:[0,1]\nv_mfma_f32_16x16x32_f16 v[36:39], v[196:199], v[20:23], v[36:39]\nv_pk_mul_f16 v28, v58, v58 op_sel_hi:[0,1]\n"
      "ds_read_b64_tr_b16 v[62:63], v237 offset:4096\nv_mfma_f32_16x16x32_f16 v[36:39], v[184:187], v[16:19], v[36:39]\nv_pk_mul_f16 v29, v58, v57 op_sel_hi:[0,1]\n"
      "ds_read_b64_tr_b16 v[60:61], v237 offset:3968\nv_mfma_f32_16x16x32_f16 v[244:247], v[188:191], v[16:19], v[244:247]\nv_pk_mul_f16 v30, v56, v58 op_sel_hi:[0,1]\n"
      "v_mfma_f32_16x16x32_f16 v[244:247], v[200:203], v[20:23], v[244:247]\nv_pk_mul_f16 v31, v56, v59 op_sel_hi:[0,1]\nv_smfmac_f32_16x16x64_f16 v[44:47], v[204:207], v[16:23], v242\n"
      "ds_read_b64_tr_b16 v[56:57], v236 offset:3712\nds_read_b64_tr_b16 v[58:59], v237 offset:3840\nv_smfmac_f32_16x16x64_f16 v[44:47], v[232:235], v[24:31], v242\n"
      "v_pk_mul_f16 v16, v68, s48 op_sel:[0,1]\nv_pk_mul_f16 v17, v69, s48 op_sel:[0,1]\nv_mfma_f32_16x16x32_f16 v[32:35], v[208:211], v[24:27], v[32:35]\nv_pk_mul_f16 v18, v70, s48 op_sel:[0,1]\n"
      "v_pk_mul_f16 v19, v71, s48 op_sel:[0,1]\nv_mfma_f32_16x16x32_f16 v[32:35], v[220:223], v[28:31], v[32:35]\nv_pk_mul_f16 v20, v68, v70\nv_pk_mul_f16 v21, v69, v71\n"
      "v_mfma_f32_16x16x32_f16 v[36:39], v[224:227], v[28:31], v[36:39]\nv_add_f32 v44, v44, v45\nv_add_f32 v46, v46, v47\nv_mfma_f32_16x16x32_f16 v[36:39], v[212:215], v[24:27], v[36:39]\n"
      "v_mul_f32 v55, v44, v54\nv_mul_f32 v55, v55, v46\nds_read_b128 v[44:47], v248 offset:48\nv_mfma_f32_16x16x32_f16 v[244:247], v[216:219], v[24:27], v[244:247]\ns_waitcnt lgkmcnt(1)\n"
      "v_pk_mul_f16 v22, v56, v56 op_sel_hi:[0,1]\nv_pk_mul_f16 v23, v56, v57 op_sel_hi:[0,1]\nv_mfma_f32_16x16x32_f16 v[244:247], v[228:231], v[28:31], v[244:247]\nv_mul_f32 v32, v32, v48\n"
      "v_mul_f32 v33, v33, v49\nv_mul_f32 v34, v34, v50\nv_mfma_f32_16x16x32_f16 v[40:43], v[76:79], v[16:19], 0\nds_read_b32 v4, v248\nv_fmac_f32 v55, v32, v36\nv_mul_f32 v35, v35, v51\n"
      "v_fmac_f32 v55, v33, v37\ns_setprio 0\nv_mfma_f32_16x16x32_f16 v[40:43], v[88:91], v[20:23], v[40:43]\nv_fmac_f32 v55, v34, v38\nv_fmac_f32 v55, v35, v39\ns_add_u32 s45, s45, 2\n"
      "s_cmp_ge_u32 s45, s46\ns_cbranch_scc1 .Lc_t2\n"
      ".Lc_b2:\nv_mfma_f32_16x16x32_f16 v[36:39], v[84:87], v[20:23], 0\nv_pk_mul_f16 v24, v66, v66 op_sel_hi:[0,1]\nv_pk_mul_f16 v25, v66, v57 op_sel_hi:[0,1]\n"
      "v_mfma_f32_16x16x32_f16 v[36:39], v[72:75], v[16:19], v[36:39]\nv_pk_mul_f16 v26, v56, v66 op_sel_hi:[0,1]\nv_pk_mul_f16 v27, v56, v67 op_sel_hi:[0,1]\n"
      "v_mfma_f32_16x16x32_f16 v[32:35], v[80:83], v[20:23], 0\nv_pk_mul_f16 v28, v58, v66 op_sel_hi:[0,1]\nv_pk_mul_f16 v29, v58, v67 op_sel_hi:[0,1]\ns_setprio 1\ns_waitcnt lgkmcnt(0)\n"
      "v_smfmac_f32_16x16x64_f16 v[44:47], v[92:95], v[16:23], v242\nv_pk_mul_f16 v30, v60, v66 op_sel_hi:[0,1]\nv_pk_mul_f16 v31, v60, v67 op_sel_hi:[0,1]\n"
      "v_mfma_f32_16x16x32_f16 v[32:35], v[96:99], v[24:27], v[32:35]\nv_pk_mul_f16 v16, v62, v66 op_sel_hi:[0,1]\nv_pk_mul_f16 v17, v62, v67 op_sel_hi:[0,1]\n"
      "v_mfma_f32_16x16x32_f16 v[32:35], v[108:111], v[28:31], v[32:35]\nv_pk_mul_f16 v18, v64, v66 op_sel_hi:[0,1]\nv_mul_f32 v244, v244, v52\n"
      "v_mfma_f32_16x16x32_f16 v[36:39], v[112:115], v[28:31], v[36:39]\nv_pk_mul_f16 v19, v64, v67 op_sel_hi:[0,1]\nv_mul_f32 v245, v245, v53\n"
      "v_mfma_f32_16x16x32_f16 v[36:39], v[100:103], v[24:27], v[36:39]\nv_pk_mul_f16 v20, v64, v64 op_sel_hi:[0,1]\nv_fmac_f32 v55, v244, v246\n"
      "v_mfma_f32_16x16x32_f16 v[40:43], v[104:107], v[24:27], v[40:43]\nv_pk_mul_f16 v21, v64, v57 op_sel_hi:[0,1]\nv_fmac_f32 v55, v245, v247\n"
      "v_mfma_f32_16x16x32_f16 v[40:43], v[116:119], v[28:31], v[40:43]\nv_pk_mul_f16 v22, v56, v64 op_sel_hi:[0,1]\nv_readfirstlane_b32 s44, v4\n"
      "v_smfmac_f32_16x16x64_f16 v[44:47], v[120:123], v[24:31], v242\nv_pk_mul_f16 v23, v56, v65 op_sel_hi:[0,1]\ns_setprio 2\nv_mfma_f32_16x16x32_f16 v[32:35], v[124:127], v[16:19], v[32:35]\n"
      "v_pk_mul_f16 v24, v58, v64 op_sel_hi:[0,1]\nv_pk_mul_f16 v25, v58, v65 op_sel_hi:[0,1]\nds_read_b64_tr_b16 v[70:71], v239 offset:5568\n"
      "v_mfma_f32_16x16x32_f16 v[32:35], v[136:139], v[20:23], v[32:35]\nv_pk_mul_f16 v26, v60, v64 op_sel_hi:[0,1]\nds_read_b64_tr_b16 v[68:69], v238 offset:5568\n"
      "v_mfma_f32_16x16x32_f16 v[36:39], v[140:143], v[20:23], v[36:39]\nv_pk_mul_f16 v27, v60, v65 op_sel_hi:[0,1]\nds_read_b64_tr_b16 v[66:67], v237 offset:6208\n"
      "v_mfma_f32_16x16x32_f16 v[36:39], v[128:131], v[16:19], v[36:39]\nv_pk_mul_f16 v28, v62, v64 op_sel_hi:[0,1]\nv_add_f32_dpp v55, v55, v55 quad_perm:[1,0,3,2] row_mask:0xf bank_mask:0xf\n"
      "v_mfma_f32_16x16x32_f16 v[40:43], v[132:135], v[16:19], v[40:43]\nv_pk_mul_f16 v29, v62, v65 op_sel_hi:[0,1]\nv_add_f32_dpp v55, v55, v55 quad_perm:[2,3,0,1] row_mask:0xf bank_mask:0xf\n"
      "v_mfma_f32_16x16x32_f16 v[40:43], v[144:147], v[20:23], v[40:43]\nv_pk_mul_f16 v30, v62, v62 op_sel_hi:[0,1]\nv_add_f32_dpp v55, v55, v55 row_half_mirror row_mask:0xf bank_mask:0xf\n"
      "v_smfmac_f32_16x16x64_f16 v[44:47], v[148:151], v[16:23], v242\nv_pk_mul_f16 v31, v62, v57 op_sel_hi:[0,1]\nv_add_f32_dpp v55, v55, v55 row_mirror row_mask:0xf bank_mask:0xf\n"
      "v_mfma_f32_16x16x32_f16 v[32:35], v[152:155], v[24:27], v[32:35]\nv_pk_mul_f16 v16, v56, v62 op_sel_hi:[0,1]\nv_pk_mul_f16 v17, v56, v63 op_sel_hi:[0,1]\nds_write_b32 v243, v55 offset:128\n"
      "v_mfma_f32_16x16x32_f16 v[32:35], v[164:167], v[28:31], v[32:35]\nv_pk_mul_f16 v18, v58, v62 op_sel_hi:[0,1]\nds_read_b64_tr_b16 v[64:65], v237 offset:6080\n"
      "v_mfma_f32_16x16x32_f16 v[36:39], v[168:171], v[28:31], v[36:39]\nv_pk_mul_f16 v19, v58, v63 op_sel_hi:[0,1]\ns_setprio 3\nv_mfma_f32_16x16x32_f16 v[36:39], v[156:159], v[24:27], v[36:39]\n"
      "v_pk_mul_f16 v20, v60, v62 op_sel_hi:[0,1]\ns_ff0_i32_b32 s53, s44\nv_mfma_f32_16x16x32_f16 v[40:43], v[160:163], v[24:27], v[40:43]\nv_pk_mul_f16 v21, v60, v63 op_sel_hi:[0,1]\n"
      "s_waitcnt lgkmcnt(1)\nv_readlane_b32 s48, v69, 48\nv_mfma_f32_16x16x32_f16 v[40:43], v[172:175], v[28:31], v[40:43]\nv_pk_mul_f16 v22, v60, v60 op_sel_hi:[0,1]\ns_add_u32 s53, s53, s64\n"
      "v_smfmac_f32_16x16x64_f16 v[44:47], v[176:179], v[24:31], v242\nv_pk_mul_f16 v23, v60, v57 op_sel_hi:[0,1]\nv_mfma_f32_16x16x32_f16 v[32:35], v[180:183], v[16:19], v[32:35]\n"
      "v_pk_mul_f16 v24, v56, v60 op_sel_hi:[0,1]\nv_pk_mul_f16 v25, v56, v61 op_sel_hi:[0,1]\nv_mfma_f32_16x16x32_f16 v[32:35], v[192:195], v[20:23], v[32:35]\n"
      "v_pk_mul_f16 v26, v58, v60 op_sel_hi:[0,1]\nv_pk_mul_f16 v27, v58, v61 op_sel_hi:[0,1]\nv_mfma_f32_16x16x32_f16 v[36:39], v[196:199], v[20:23], v[36:39]\n"
      "v_pk_mul_f16 v28, v58, v58 op_sel_hi:[0,1]\nds_read_b64_tr_b16 v[62:63], v237 offset:5952\nv_mfma_f32_16x16x32_f16 v[36:39], v[184:187], v[16:19], v[36:39]\n"
      "v_pk_mul_f16 v29, v58, v57 op_sel_hi:[0,1]\nds_read_b64_tr_b16 v[60:61], v237 offset:5824\nv_mfma_f32_16x16x32_f16 v[40:43], v[188:191], v[16:19], v[40:43]\n"
      "v_pk_mul_f16 v30, v56, v58 op_sel_hi:[0,1]\nv_mfma_f32_16x16x32_f16 v[40:43], v[200:203], v[20:23], v[40:43]\nv_pk_mul_f16 v31, v56, v59 op_sel_hi:[0,1]\n"
      "v_smfmac_f32_16x16x64_f16 v[44:47], v[204:207], v[16:23], v242\nds_read_b64_tr_b16 v[56:57], v236 offset:5568\nds_read_b64_tr_b16 v[58:59], v237 offset:5696\n"
      "v_smfmac_f32_16x16x64_f16 v[44:47], v[232:235], v[24:31], v242\nv_pk_mul_f16 v16, v68, s48 op_sel:[0,1]\nv_pk_mul_f16 v17, v69, s48 op_sel:[0,1]\n"
      "v_mfma_f32_16x16x32_f16 v[32:35], v[208:211], v[24:27], v[32:35]\nv_pk_mul_f16 v18, v70, s48 op_sel:[0,1]\nv_pk_mul_f16 v19, v71, s48 op_sel:[0,1]\n"
      "v_mfma_f32_16x16x32_f16 v[32:35], v[220:223], v[28:31], v[32:35]\nv_pk_mul_f16 v20, v68, v70\nv_pk_mul_f16 v21, v69, v71\nv_mfma_f32_16x16x32_f16 v[36:39], v[224:227], v[28:31], v[36:39]\n"
      "v_add_f32 v44, v44, v45\nv_add_f32 v46, v46, v47\nv_mfma_f32_16x16x32_f16 v[36:39], v[212:215], v[24:27], v[36:39]\nv_mul_f32 v55, v44, v54\nv_mul_f32 v55, v55, v46\n"
      "ds_read_b128 v[44:47], v248 offset:48\nv_mfma_f32_16x16x32_f16 v[40:43], v[216:219], v[24:27], v[40:43]\ns_waitcnt lgkmcnt(1)\nv_pk_mul_f16 v22, v56, v56 op_sel_hi:[0,1]\n"
      "v_pk_mul_f16 v23, v56, v57 op_sel_hi:[0,1]\nv_mfma_f32_16x16x32_f16 v[40:43], v[228:231], v[28:31], v[40:43]\ns_cmp_lt_u32 s45, s53\nv_mul_f32 v32, v32, v48\nv_mul_f32 v33, v33, v49\n"
      "v_mul_f32 v34, v34, v50\ns_cbranch_scc0 .Lc_s2\n"
      ".Lc_u3:\nv_mfma_f32_16x16x32_f16 v[244:247], v[76:79], v[16:19], 0\nds_read_b32 v4, v248\nv_fmac_f32 v55, v32, v36\nv_mul_f32 v35, v35, v51\nv_fmac_f32 v55, v33, v37\ns_setprio 0\n"
      "v_mfma_f32_16x16x32_f16 v[244:247], v[88:91], v[20:23], v[244:247]\nv_fmac_f32 v55, v34, v38\nv_fmac_f32 v55, v35, v39\ns_add_u32 s45, s45, 2\ns_cmp_ge_u32 s45, s46\ns_cbranch_scc1 .Lc_t3\n"
      ".Lc_b3:\nv_mfma_f32_16x16x32_f16 v[36:39], v[84:87], v[20:23], 0\nv_pk_mul_f16 v24, v66, v66 op_sel_hi:[0,1]\nv_pk_mul_f16 v25, v66, v57 op_sel_hi:[0,1]\n"
      "v_mfma_f32_16x16x32_f16 v[36:39], v[72:75], v[16:19], v[36:39]\nv_pk_mul_f16 v26, v56, v66 op_sel_hi:[0,1]\nv_pk_mul_f16 v27, v56, v67 op_sel_hi:[0,1]\n"
      "v_mfma_f32_16x16x32_f16 v[32:35], v[80:83], v[20:23], 0\nv_pk_mul_f16 v28, v58, v66 op_sel_hi:[0,1]\nv_pk_mul_f16 v29, v58, v67 op_sel_hi:[0,1]\ns_setprio 1\ns_waitcnt lgkmcnt(0)\n"
      "v_smfmac_f32_16x16x64_f16 v[44:47], v[92:95], v[16:23], v242\nv_pk_mul_f16 v30, v60, v66 op_sel_hi:[0,1]\nv_pk_mul_f16 v31, v60, v67 op_sel_hi:[0,1]\n"
      "v_mfma_f32_16x16x32_f16 v[32:35], v[96:99], v[24:27], v[32:35]\nv_pk_mul_f16 v16, v62, v66 op_sel_hi:[0,1]\nv_pk_mul_f16 v17, v62, v67 op_sel_hi:[0,1]\n"
      "v_mfma_f32_16x16x32_f16 v[32:35], v[108:111], v[28:31], v[32:35]\nv_pk_mul_f16 v18, v64, v66 op_sel_hi:[0,1]\nv_mul_f32 v40, v40, v52\n"
      "v_mfma_f32_16x16x32_f16 v[36:39], v[112:115], v[28:31], v[36:39]\nv_pk_mul_f16 v19, v64, v67 op_sel_hi:[0,1]\nv_mul_f32 v41, v41, v53\n"
      "v_mfma_f32_16x16x32_f16 v[36:39], v[100:103], v[24:27], v[36:39]\nv_pk_mul_f16 v20, v64, v64 op_sel_hi:[0,1]\nv_fmac_f32 v55, v40, v42\n"
      "v_mfma_f32_16x16x32_f16 v[244:247], v[104:107], v[24:27], v[244:247]\nv_pk_mul_f16 v21, v64, v57 op_sel_hi:[0,1]\nv_fmac_f32 v55, v41, v43\n"
      "v_mfma_f32_16x16x32_f16 v[244:247], v[116:119], v[28:31], v[244:247]\nv_pk_mul_f16 v22, v56, v64 op_sel_hi:[0,1]\nv_readfirstlane_b32 s44, v4\n"
      "v_smfmac_f32_16x16x64_f16 v[44:47], v[120:123], v[24:31], v242\nv_pk_mul_f16 v23, v56, v65 op_sel_hi:[0,1]\ns_setprio 2\nv_mfma_f32_16x16x32_f16 v[32:35], v[124:127], v[16:19], v[32:35]\n"
      "v_pk_mul_f16 v24, v58, v64 op_sel_hi:[0,1]\nv_pk_mul_f16 v25, v58, v65 op_sel_hi:[0,1]\nds_read_b64_tr_b16 v[70:71], v239 offset:7424\n"
      "v_mfma_f32_16x16x32_f16 v[32:35], v[136:139], v[20:23], v[32:35]\nv_pk_mul_f16 v26, v60, v64 op_sel_hi:[0,1]\nds_read_b64_tr_b16 v[68:69], v238 offset:7424\n"
      "v_mfma_f32_16x16x32_f16 v[36:39], v[140:143], v[20:23], v[36:39]\nv_pk_mul_f16 v27, v60, v65 op_sel_hi:[0,1]\nds_read_b64_tr_b16 v[66:67], v237 offset:8064\n"
      "v_mfma_f32_16x16x32_f16 v[36:39], v[128:131], v[16:19], v[36:39]\nv_pk_mul_f16 v28, v62, v64 op_sel_hi:[0,1]\nv_add_f32_dpp v55, v55, v55 quad_perm:[1,0,3,2] row_mask:0xf bank_mask:0xf\n"
      "v_mfma_f32_16x16x32_f16 v[244:247], v[132:135], v[16:19], v[244:247]\nv_pk_mul_f16 v29, v62, v65 op_sel_hi:[0,1]\nv_add_f32_dpp v55, v55, v55 quad_perm:[2,3,0,1] row_mask:0xf bank_mask:0xf\n"
      "v_mfma_f32_16x16x32_f16 v[244:247], v[144:147], v[20:23], v[244:247]\nv_pk_mul_f16 v30, v62, v62 op_sel_hi:[0,1]\nv_add_f32_dpp v55, v55, v55 row_half_mirror row_mask:0xf bank_mask:0xf\n"
      "v_smfmac_f32_16x16x64_f16 v[44:47], v[148:151], v[16:23], v242\nv_pk_mul_f16 v31, v62, v57 op_sel_hi:[0,1]\nv_add_f32_dpp v55, v55, v55 row_mirror row_mask:0xf bank_mask:0xf\n"
      "v_mfma_f32_16x16x32_f16 v[32:35], v[152:155], v[24:27], v[32:35]\nv_pk_mul_f16 v16, v56, v62 op_sel_hi:[0,1]\nv_pk_mul_f16 v17, v56, v63 op_sel_hi:[0,1]\nds_write_b32 v243, v55 offset:256\n"
      "v_mfma_f32_16x16x32_f16 v[32:35], v[164:167], v[28:31], v[32:35]\nv_pk_mul_f16 v18, v58, v62 op_sel_hi:[0,1]\nds_read_b64_tr_b16 v[64:65], v237 offset:7936\n"
      "v_mfma_f32_16x16x32_f16 v[36:39], v[168:171], v[28:31], v[36:39]\nv_pk_mul_f16 v19, v58, v63 op_sel_hi:[0,1]\ns_setprio 3\nv_mfma_f32_16x16x32_f16 v[36:39], v[156:159], v[24:27], v[36:39]\n"
      "v_pk_mul_f16 v20, v60, v62 op_sel_hi:[0,1]\ns_ff0_i32_b32 s53, s44\nv_mfma_f32_16x16x32_f16 v[244:247], v[160:163], v[24:27], v[244:247]\nv_pk_mul_f16 v21, v60, v63 op_sel_hi:[0,1]\n"
      "s_waitcnt lgkmcnt(1)\nv_readlane_b32 s48, v69, 48\nv_mfma_f32_16x16x32_f16 v[244:247], v[172:175], v[28:31], v[244:247]\nv_pk_mul_f16 v22, v60, v60 op_sel_hi:[0,1]\ns_add_u32 s53, s53, s64\n"
      "v_smfmac_f32_16x16x64_f16 v[44:47], v[176:179], v[24:31], v242\nv_pk_mul_f16 v23, v60, v57 op_sel_hi:[0,1]\nv_mfma_f32_16x16x32_f16 v[32:35], v[180:183], v[16:19], v[32:35]\n"
      "v_pk_mul_f16 v24, v56, v60 op_sel_hi:[0,1]\nv_pk_mul_f16 v25, v56, v61 op_sel_hi:[0,1]\nv_mfma_f32_16x16x32_f16 v[32:35], v[192:195], v[20:23], v[32:35]\n"
      "v_pk_mul_f16 v26, v58, v60 op_sel_hi:[0,1]\nv_pk_mul_f16 v27, v58, v61 op_sel_hi:[0,1]\nv_mfma_f32_16x16x32_f16 v[36:39], v[196:199], v[20:23], v[36:39]\n"
      "v_pk_mul_f16 v28, v58, v58 op_sel_hi:[0,1]\nds_read_b64_tr_b16 v[62:63], v237 offset:7808\nv_mfma_f32_16x16x32_f16 v[36:39], v[184:187], v[16:19], v[36:39]\n"
      "v_pk_mul_f16 v29, v58, v57 op_sel_hi:[0,1]\nds_read_b64_tr_b16 v[60:61], v237 offset:7680\nv_mfma_f32_16x16x32_f16 v[244:247], v[188:191], v[16:19], v[244:247]\n"
      "v_pk_mul_f16 v30, v56, v58 op_sel_hi:[0,1]\nv_mfma_f32_16x16x32_f16 v[244:247], v[200:203], v[20:23], v[244:247]\nv_pk_mul_f16 v31, v56, v59 op_sel_hi:[0,1]\n"
      "v_smfmac_f32_16x16x64_f16 v[44:47], v[204:207], v[16:23], v242\nds_read_b64_tr_b16 v[56:57], v236 offset:7424\nds_read_b64_tr_b16 v[58:59], v237 offset:7552\n"
      "v_smfmac_f32_16x16x64_f16 v[44:47], v[232:235], v[24:31], v242\nv_pk_mul_f16 v16, v68, s48 op_sel:[0,1]\nv_pk_mul_f16 v17, v69, s48 op_sel:[0,1]\n"
      "v_mfma_f32_16x16x32_f16 v[32:35], v[208:211], v[24:27], v[32:35]\nv_pk_mul_f16 v18, v70, s48 op_sel:[0,1]\nv_pk_mul_f16 v19, v71, s48 op_sel:[0,1]\n"
      "v_mfma_f32_16x16x32_f16 v[32:35], v[220:223], v[28:31], v[32:35]\nv_pk_mul_f16 v20, v68, v70\nv_pk_mul_f16 v21, v69, v71\nv_mfma_f32_16x16x32_f16 v[36:39], v[224:227], v[28:31], v[36:39]\n"
      "v_add_f32 v44, v44, v45\nv_add_f32 v46, v46, v47\nv_mfma_f32_16x16x32_f16 v[36:39], v[212:215], v[24:27], v[36:39]\nv_mul_f32 v55, v44, v54\nv_mul_f32 v55, v55, v46\n"
      "ds_read_b128 v[44:47], v248 offset:48\nv_mfma_f32_16x16x32_f16 v[244:247], v[216:219], v[24:27], v[244:247]\ns_waitcnt lgkmcnt(1)\nv_pk_mul_f16 v22, v56, v56 op_sel_hi:[0,1]\n"
      "v_pk_mul_f16 v23, v56, v57 op_sel_hi:[0,1]\nv_mfma_f32_16x16x32_f16 v[244:247], v[228:231], v[28:31], v[244:247]\ns_cmp_lt_u32 s45, s53\nv_mul_f32 v32, v32, v48\nv_mul_f32 v33, v33, v49\n"
      "v_mul_f32 v34, v34, v50\ns_cbranch_scc0 .Lc_s3\n"
      ".Lc_u4:\nv_mfma_f32_16x16x32_f16 v[40:43], v[76:79], v[16:19], 0\nds_read_b32 v4, v248\nv_fmac_f32 v55, v32, v36\nv_mul_f32 v35, v35, v51\nv_fmac_f32 v55, v33, v37\ns_setprio 0\n"
      "v_mfma_f32_16x16x32_f16 v[40:43], v[88:91], v[20:23], v[40:43]\nv_fmac_f32 v55, v34, v38\nv_fmac_f32 v55, v35, v39\ns_add_u32 s45, s45, 2\ns_cmp_ge_u32 s45, s46\ns_cbranch_scc1 .Lc_t4\n"
      ".Lc_b4:\nv_mfma_f32_16x16x32_f16 v[36:39], v[84:87], v[20:23], 0\nv_pk_mul_f16 v24, v66, v66 op_sel_hi:[0,1]\nv_pk_mul_f16 v25, v66, v57 op_sel_hi:[0,1]\n"
      "v_mfma_f32_16x16x32_f16 v[36:39], v[72:75], v[16:19], v[36:39]\nv_pk_mul_f16 v26, v56, v66 op_sel_hi:[0,1]\nv_pk_mul_f16 v27, v56, v67 op_sel_hi:[0,1]\n"
      "v_mfma_f32_16x16x32_f16 v[32:35], v[80:83], v[20:23], 0\nv_pk_mul_f16 v28, v58, v66 op_sel_hi:[0,1]\nv_pk_mul_f16 v29, v58, v67 op_sel_hi:[0,1]\ns_setprio 1\ns_waitcnt lgkmcnt(0)\n"
      "v_smfmac_f32_16x16x64_f16 v[44:47], v[92:95], v[16:23], v242\nv_pk_mul_f16 v30, v60, v66 op_sel_hi:[0,1]\nv_pk_mul_f16 v31, v60, v67 op_sel_hi:[0,1]\n"
      "v_mfma_f32_16x16x32_f16 v[32:35], v[96:99], v[24:27], v[32:35]\nv_pk_mul_f16 v16, v62, v66 op_sel_hi:[0,1]\nv_pk_mul_f16 v17, v62, v67 op_sel_hi:[0,1]\n"
      "v_mfma_f32_16x16x32_f16 v[32:35], v[108:111], v[28:31], v[32:35]\nv_pk_mul_f16 v18, v64, v66 op_sel_hi:[0,1]\nv_mul_f32 v244, v244, v52\n"
      "v_mfma_f32_16x16x32_f16 v[36:39], v[112:115], v[28:31], v[36:39]\nv_pk_mul_f16 v19, v64, v67 op_sel_hi:[0,1]\nv_mul_f32 v245, v245, v53\n"
      "v_mfma_f32_16x16x32_f16 v[36:39], v[100:103], v[24:27], v[36:39]\nv_pk_mul_f16 v20, v64, v64 op_sel_hi:[0,1]\nv_fmac_f32 v55, v244, v246\n"
      "v_mfma_f32_16x16x32_f16 v[40:43], v[104:107], v[24:27], v[40:43]\nv_pk_mul_f16 v21, v64, v57 op_sel_hi:[0,1]\nv_fmac_f32 v55, v245, v247\n"
      "v_mfma_f32_16x16x32_f16 v[40:43], v[116:119], v[28:31], v[40:43]\nv_pk_mul_f16 v22, v56, v64 op_sel_hi:[0,1]\nv_readfirstlane_b32 s44, v4\n"
      "v_smfmac_f32_16x16x64_f16 v[44:47], v[120:123], v[24:31], v242\nv_pk_mul_f16 v23, v56, v65 op_sel_hi:[0,1]\ns_setprio 2\nv_mfma_f32_16x16x32_f16 v[32:35], v[124:127], v[16:19], v[32:35]\n"
      "v_pk_mul_f16 v24, v58, v64 op_sel_hi:[0,1]\nv_pk_mul_f16 v25, v58, v65 op_sel_hi:[0,1]\nds_read_b64_tr_b16 v[70:71], v239 offset:9280\n"
      "v_mfma_f32_16x16x32_f16 v[32:35], v[136:139], v[20:23], v[32:35]\nv_pk_mul_f16 v26, v60, v64 op_sel_hi:[0,1]\nds_read_b64_tr_b16 v[68:69], v238 offset:9280\n"
      "v_mfma_f32_16x16x32_f16 v[36:39], v[140:143], v[20:23], v[36:39]\nv_pk_mul_f16 v27, v60, v65 op_sel_hi:[0,1]\nds_read_b64_tr_b16 v[66:67], v237 offset:9920\n"
      "v_mfma_f32_16x16x32_f16 v[36:39], v[128:131], v[16:19], v[36:39]\nv_pk_mul_f16 v28, v62, v64 op_sel_hi:[0,1]\nv_add_f32_dpp v55, v55, v55 quad_perm:[1,0,3,2] row_mask:0xf bank_mask:0xf\n"
      "v_mfma_f32_16x16x32_f16 v[40:43], v[132:135], v[16:19], v[40:43]\nv_pk_mul_f16 v29, v62, v65 op_sel_hi:[0,1]\nv_add_f32_dpp v55, v55, v55 quad_perm:[2,3,0,1] row_mask:0xf bank_mask:0xf\n"
      "v_mfma_f32_16x16x32_f16 v[40:43], v[144:147], v[20:23], v[40:43]\nv_pk_mul_f16 v30, v62, v62 op_sel_hi:[0,1]\nv_add_f32_dpp v55, v55, v55 row_half_mirror row_mask:0xf bank_mask:0xf\n"
      "v_smfmac_f32_16x16x64_f16 v[44:47], v[148:151], v[16:23], v242\nv_pk_mul_f16 v31, v62, v57 op_sel_hi:[0,1]\nv_add_f32_dpp v55, v55, v55 row_mirror row_mask:0xf bank_mask:0xf\n"
      "v_mfma_f32_16x16x32_f16 v[32:35], v[152:155], v[24:27], v[32:35]\nv_pk_mul_f16 v16, v56, v62 op_sel_hi:[0,1]\nv_pk_mul_f16 v17, v56, v63 op_sel_hi:[0,1]\nds_write_b32 v243, v55 offset:384\n"
      "v_mfma_f32_16x16x32_f16 v[32:35], v[164:167], v[28:31], v[32:35]\nv_pk_mul_f16 v18, v58, v62 op_sel_hi:[0,1]\nds_read_b64_tr_b16 v[64:65], v237 offset:9792\n"
      "v_mfma_f32_16x16x32_f16 v[36:39], v[168:171], v[28:31], v[36:39]\nv_pk_mul_f16 v19, v58, v63 op_sel_hi:[0,1]\ns_setprio 3\nv_mfma_f32_16x16x32_f16 v[36:39], v[156:159], v[24:27], v[36:39]\n"
      "v_pk_mul_f16 v20, v60, v62 op_sel_hi:[0,1]\ns_ff0_i32_b32 s53, s44\nv_mfma_f32_16x16x32_f16 v[40:43], v[160:163], v[24:27], v[40:43]\nv_pk_mul_f16 v21, v60, v63 op_sel_hi:[0,1]\n"
      "s_waitcnt lgkmcnt(1)\nv_readlane_b32 s48, v69, 48\nv_mfma_f32_16x16x32_f16 v[40:43], v[172:175], v[28:31], v[40:43]\nv_pk_mul_f16 v22, v60, v60 op_sel_hi:[0,1]\ns_add_u32 s53, s53, s64\n"
      "v_smfmac_f32_16x16x64_f16 v[44:47], v[176:179], v[24:31], v242\nv_pk_mul_f16 v23, v60, v57 op_sel_hi:[0,1]\nv_mfma_f32_16x16x32_f16 v[32:35], v[180:183], v[16:19], v[32:35]\n"
      "v_pk_mul_f16 v24, v56, v60 op_sel_hi:[0,1]\nv_pk_mul_f16 v25, v56, v61 op_sel_hi:[0,1]\nv_mfma_f32_16x16x32_f16 v[32:35], v[192:195], v[20:23], v[32:35]\n"
      "v_pk_mul_f16 v26, v58, v60 op_sel_hi:[0,1]\nv_pk_mul_f16 v27, v58, v61 op_sel_hi:[0,1]\nv_mfma_f32_16x16x32_f16 v[36:39], v[196:199], v[20:23], v[36:39]\n"
      "v_pk_mul_f16 v28, v58, v58 op_sel_hi:[0,1]\nds_read_b64_tr_b16 v[62:63], v237 offset:9664\nv_mfma_f32_16x16x32_f16 v[36:39], v[184:187], v[16:19], v[36:39]\n"
      "v_pk_mul_f16 v29, v58, v57 op_sel_hi:[0,1]\nds_read_b64_tr_b16 v[60:61], v237 offset:9536\nv_mfma_f32_16x16x32_f16 v[40:43], v[188:191], v[16:19], v[40:43]\n"
      "v_pk_mul_f16 v30, v56, v58 op_sel_hi:[0,1]\nv_mfma_f32_16x16x32_f16 v[40:43], v[200:203], v[20:23], v[40:43]\nv_pk_mul_f16 v31, v56, v59 op_sel_hi:[0,1]\n"
      "v_smfmac_f32_16x16x64_f16 v[44:47], v[204:207], v[16:23], v242\nds_read_b64_tr_b16 v[56:57], v236 offset:9280\nds_read_b64_tr_b16 v[58:59], v237 offset:9408\n"
      "v_smfmac_f32_16x16x64_f16 v[44:47], v[232:235], v[24:31], v242\nv_pk_mul_f16 v16, v68, s48 op_sel:[0,1]\nv_pk_mul_f16 v17, v69, s48 op_sel:[0,1]\n"
      "v_mfma_f32_16x16x32_f16 v[32:35], v[208:211], v[24:27], v[32:35]\nv_pk_mul_f16 v18, v70, s48 op_sel:[0,1]\nv_pk_mul_f16 v19, v71, s48 op_sel:[0,1]\n"
      "v_mfma_f32_16x16x32_f16 v[32:35], v[220:223], v[28:31], v[32:35]\nv_pk_mul_f16 v20, v68, v70\nv_pk_mul_f16 v21, v69, v71\nv_mfma_f32_16x16x32_f16 v[36:39], v[224:227], v[28:31], v[36:39]\n"
      "v_add_f32 v44, v44, v45\nv_add_f32 v46, v46, v47\nv_mfma_f32_16x16x32_f16 v[36:39], v[212:215], v[24:27], v[36:39]\nv_mul_f32 v55, v44, v54\nv_mul_f32 v55, v55, v46\n"
      "ds_read_b128 v[44:47], v248 offset:48\nv_mfma_f32_16x16x32_f16 v[40:43], v[216:219], v[24:27], v[40:43]\ns_waitcnt lgkmcnt(1)\nv_pk_mul_f16 v22, v56, v56 op_sel_hi:[0,1]\n"
      "v_pk_mul_f16 v23, v56, v57 op_sel_hi:[0,1]\nv_mfma_f32_16x16x32_f16 v[40:43], v[228:231], v[28:31], v[40:43]\ns_cmp_lt_u32 s45, s53\nv_mul_f32 v32, v32, v48\nv_mul_f32 v33, v33, v49\n"
      "v_mul_f32 v34, v34, v50\ns_cbranch_scc0 .Lc_s4\n"
      ".Lc_u5:\nv_mfma_f32_16x16x32_f16 v[244:247], v[76:79], v[16:19], 0\nds_read_b32 v4, v248\nv_fmac_f32 v55, v32, v36\nv_mul_f32 v35, v35, v51\nv_fmac_f32 v55, v33, v37\ns_setprio 0\n"
      "v_mfma_f32_16x16x32_f16 v[244:247], v[88:91], v[20:23], v[244:247]\nv_fmac_f32 v55, v34, v38\nv_fmac_f32 v55, v35, v39\ns_add_u32 s45, s45, 2\ns_cmp_ge_u32 s45, s46\ns_cbranch_scc1 .Lc_t5\n"
      ".Lc_b5:\nv_mfma_f32_16x16x32_f16 v[36:39], v[84:87], v[20:23], 0\nv_pk_mul_f16 v24, v66, v66 op_sel_hi:[0,1]\nv_pk_mul_f16 v25, v66, v57 op_sel_hi:[0,1]\n"
      "v_mfma_f32_16x16x32_f16 v[36:39], v[72:75], v[16:19], v[36:39]\nv_pk_mul_f16 v26, v56, v66 op_sel_hi:[0,1]\nv_pk_mul_f16 v27, v56, v67 op_sel_hi:[0,1]\n"
      "v_mfma_f32_16x16x32_f16 v[32:35], v[80:83], v[20:23], 0\nv_pk_mul_f16 v28, v58, v66 op_sel_hi:[0,1]\nv_pk_mul_f16 v29, v58, v67 op_sel_hi:[0,1]\ns_setprio 1\ns_waitcnt lgkmcnt(0)\n"
      "v_smfmac_f32_16x16x64_f16 v[44:47], v[92:95], v[16:23], v242\nv_pk_mul_f16 v30, v60, v66 op_sel_hi:[0,1]\nv_pk_mul_f16 v31, v60, v67 op_sel_hi:[0,1]\n"
      "v_mfma_f32_16x16x32_f16 v[32:35], v[96:99], v[24:27], v[32:35]\nv_pk_mul_f16 v16, v62, v66 op_sel_hi:[0,1]\nv_pk_mul_f16 v17, v62, v67 op_sel_hi:[0,1]\n"
      "v_mfma_f32_16x16x32_f16 v[32:35], v[108:111], v[28:31], v[32:35]\nv_pk_mul_f16 v18, v64, v66 op_sel_hi:[0,1]\nv_mul_f32 v40, v40, v52\n"
      "v_mfma_f32_16x16x32_f16 v[36:39], v[112:115], v[28:31], v[36:39]\nv_pk_mul_f16 v19, v64, v67 op_sel_hi:[0,1]\nv_mul_f32 v41, v41, v53\n"
      "v_mfma_f32_16x16x32_f16 v[36:39], v[100:103], v[24:27], v[36:39]\nv_pk_mul_f16 v20, v64, v64 op_sel_hi:[0,1]\nv_fmac_f32 v55, v40, v42\n"
      "v_mfma_f32_16x16x32_f16 v[244:247], v[104:107], v[24:27], v[244:247]\nv_pk_mul_f16 v21, v64, v57 op_sel_hi:[0,1]\nv_fmac_f32 v55, v41, v43\n"
      "v_mfma_f32_16x16x32_f16 v[244:247], v[116:119], v[28:31], v[244:247]\nv_pk_mul_f16 v22, v56, v64 op_sel_hi:[0,1]\nv_readfirstlane_b32 s44, v4\n"
      "v_smfmac_f32_16x16x64_f16 v[44:47], v[120:123], v[24:31], v242\nv_pk_mul_f16 v23, v56, v65 op_sel_hi:[0,1]\ns_setprio 2\nv_mfma_f32_16x16x32_f16 v[32:35], v[124:127], v[16:19], v[32:35]\n"
      "v_pk_mul_f16 v24, v58, v64 op_sel_hi:[0,1]\nv_pk_mul_f16 v25, v58, v65 op_sel_hi:[0,1]\nds_read_b64_tr_b16 v[70:71], v239 offset:11136\n"
      "v_mfma_f32_16x16x32_f16 v[32:35], v[136:139], v[20:23], v[32:35]\nv_pk_mul_f16 v26, v60, v64 op_sel_hi:[0,1]\nds_read_b64_tr_b16 v[68:69], v238 offset:11136\n"
      "v_mfma_f32_16x16x32_f16 v[36:39], v[140:143], v[20:23], v[36:39]\nv_pk_mul_f16 v27, v60, v65 op_sel_hi:[0,1]\nds_read_b64_tr_b16 v[66:67], v237 offset:11776\n"
      "v_mfma_f32_16x16x32_f16 v[36:39], v[128:131], v[16:19], v[36:39]\nv_pk_mul_f16 v28, v62, v64 op_sel_hi:[0,1]\nv_add_f32_dpp v55, v55, v55 quad_perm:[1,0,3,2] row_mask:0xf bank_mask:0xf\n"
      "v_mfma_f32_16x16x32_f16 v[244:247], v[132:135], v[16:19], v[244:247]\nv_pk_mul_f16 v29, v62, v65 op_sel_hi:[0,1]\nv_add_f32_dpp v55, v55, v55 quad_perm:[2,3,0,1] row_mask:0xf bank_mask:0xf\n"
      "v_mfma_f32_16x16x32_f16 v[244:247], v[144:147], v[20:23], v[244:247]\nv_pk_mul_f16 v30, v62, v62 op_sel_hi:[0,1]\nv_add_f32_dpp v55, v55, v55 row_half_mirror row_mask:0xf bank_mask:0xf\n"
      "v_smfmac_f32_16x16x64_f16 v[44:47], v[148:151], v[16:23], v242\nv_pk_mul_f16 v31, v62, v57 op_sel_hi:[0,1]\nv_add_f32_dpp v55, v55, v55 row_mirror row_mask:0xf bank_mask:0xf\n"
      "v_mfma_f32_16x16x32_f16 v[32:35], v[152:155], v[24:27], v[32:35]\nv_pk_mul_f16 v16, v56, v62 op_sel_hi:[0,1]\nv_pk_mul_f16 v17, v56, v63 op_sel_hi:[0,1]\nds_write_b32 v243, v55 offset:512\n"
      "v_mfma_f32_16x16x32_f16 v[32:35], v[164:167], v[28:31], v[32:35]\nv_pk_mul_f16 v18, v58, v62 op_sel_hi:[0,1]\nds_read_b64_tr_b16 v[64:65], v237 offset:11648\n"
      "v_mfma_f32_16x16x32_f16 v[36:39], v[168:171], v[28:31], v[36:39]\nv_pk_mul_f16 v19, v58, v63 op_sel_hi:[0,1]\ns_setprio 3\nv_mfma_f32_16x16x32_f16 v[36:39], v[156:159], v[24:27], v[36:39]\n"
      "v_pk_mul_f16 v20, v60, v62 op_sel_hi:[0,1]\ns_ff0_i32_b32 s53, s44\nv_mfma_f32_16x16x32_f16 v[244:247], v[160:163], v[24:27], v[244:247]\nv_pk_mul_f16 v21, v60, v63 op_sel_hi:[0,1]\n"
      "s_waitcnt lgkmcnt(1)\nv_readlane_b32 s48, v69, 48\nv_mfma_f32_16x16x32_f16 v[244:247], v[172:175], v[28:31], v[244:247]\nv_pk_mul_f16 v22, v60, v60 op_sel_hi:[0,1]\ns_add_u32 s53, s53, s64\n"
      "v_smfmac_f32_16x16x64_f16 v[44:47], v[176:179], v[24:31], v242\nv_pk_mul_f16 v23, v60, v57 op_sel_hi:[0,1]\nv_mfma_f32_16x16x32_f16 v[32:35], v[180:183], v[16:19], v[32:35]\n"
      "v_pk_mul_f16 v24, v56, v60 op_sel_hi:[0,1]\nv_pk_mul_f16 v25, v56, v61 op_sel_hi:[0,1]\nv_mfma_f32_16x16x32_f16 v[32:35], v[192:195], v[20:23], v[32:35]\n"
      "v_pk_mul_f16 v26, v58, v60 op_sel_hi:[0,1]\nv_pk_mul_f16 v27, v58, v61 op_sel_hi:[0,1]\nv_mfma_f32_16x16x32_f16 v[36:39], v[196:199], v[20:23], v[36:39]\n"
      "v_pk_mul_f16 v28, v58, v58 op_sel_hi:[0,1]\nds_read_b64_tr_b16 v[62:63], v237 offset:11520\nv_mfma_f32_16x16x32_f16 v[36:39], v[184:187], v[16:19], v[36:39]\n"
      "v_pk_mul_f16 v29, v58, v57 op_sel_hi:[0,1]\nds_read_b64_tr_b16 v[60:61], v237 offset:11392\nv_mfma_f32_16x16x32_f16 v[244:247], v[188:191], v[16:19], v[244:247]\n"
      "v_pk_mul_f16 v30, v56, v58 op_sel_hi:[0,1]\nv_mfma_f32_16x16x32_f16 v[244:247], v[200:203], v[20:23], v[244:247]\nv_pk_mul_f16 v31, v56, v59 op_sel_hi:[0,1]\n"
      "v_smfmac_f32_16x16x64_f16 v[44:47], v[204:207], v[16:23], v242\nds_read_b64_tr_b16 v[56:57], v236 offset:11136\nds_read_b64_tr_b16 v[58:59], v237 offset:11264\n"
      "v_smfmac_f32_16x16x64_f16 v[44:47], v[232:235], v[24:31], v242\nv_pk_mul_f16 v16, v68, s48 op_sel:[0,1]\nv_pk_mul_f16 v17, v69, s48 op_sel:[0,1]\n"
      "v_mfma_f32_16x16x32_f16 v[32:35], v[208:211], v[24:27], v[32:35]\nv_pk_mul_f16 v18, v70, s48 op_sel:[0,1]\nv_pk_mul_f16 v19, v71, s48 op_sel:[0,1]\n"
      "v_mfma_f32_16x16x32_f16 v[32:35], v[220:223], v[28:31], v[32:35]\nv_pk_mul_f16 v20, v68, v70\nv_pk_mul_f16 v21, v69, v71\nv_mfma_f32_16x16x32_f16 v[36:39], v[224:227], v[28:31], v[36:39]\n"
      "v_add_f32 v44, v44, v45\nv_add_f32 v46, v46, v47\nv_mfma_f32_16x16x32_f16 v[36:39], v[212:215], v[24:27], v[36:39]\nv_mul_f32 v55, v44, v54\nv_mul_f32 v55, v55, v46\n"
      "ds_read_b128 v[44:47], v248 offset:48\nv_mfma_f32_16x16x32_f16 v[244:247], v[216:219], v[24:27], v[244:247]\ns_waitcnt lgkmcnt(1)\nv_pk_mul_f16 v22, v56, v56 op_sel_hi:[0,1]\n"
      "v_pk_mul_f16 v23, v56, v57 op_sel_hi:[0,1]\nv_mfma_f32_16x16x32_f16 v[244:247], v[228:231], v[28:31], v[244:247]\ns_cmp_lt_u32 s45, s53\nv_mul_f32 v32, v32, v48\nv_mul_f32 v33, v33, v49\n"
      "v_mul_f32 v34, v34, v50\ns_cbranch_scc0 .Lc_s5\n"
      ".Lc_u6:\nv_mfma_f32_16x16x32_f16 v[40:43], v[76:79], v[16:19], 0\nds_read_b32 v4, v248\nv_fmac_f32 v55, v32, v36\nv_mul_f32 v35, v35, v51\nv_fmac_f32 v55, v33, v37\ns_setprio 0\n"
      "v_mfma_f32_16x16x32_f16 v[40:43], v[88:91], v[20:23], v[40:43]\nv_fmac_f32 v55, v34, v38\nv_fmac_f32 v55, v35, v39\ns_add_u32 s45, s45, 2\ns_cmp_ge_u32 s45, s46\ns_cbranch_scc1 .Lc_t6\n"
      ".Lc_b6:\nv_mfma_f32_16x16x32_f16 v[36:39], v[84:87], v[20:23], 0\nv_pk_mul_f16 v24, v66, v66 op_sel_hi:[0,1]\nv_pk_mul_f16 v25, v66, v57 op_sel_hi:[0,1]\n"
      "v_mfma_f32_16x16x32_f16 v[36:39], v[72:75], v[16:19], v[36:39]\nv_pk_mul_f16 v26, v56, v66 op_sel_hi:[0,1]\nv_pk_mul_f16 v27, v56, v67 op_sel_hi:[0,1]\n"
      "v_mfma_f32_16x16x32_f16 v[32:35], v[80:83], v[20:23], 0\nv_pk_mul_f16 v28, v58, v66 op_sel_hi:[0,1]\nv_pk_mul_f16 v29, v58, v67 op_sel_hi:[0,1]\ns_setprio 1\ns_waitcnt lgkmcnt(0)\n"
      "v_smfmac_f32_16x16x64_f16 v[44:47], v[92:95], v[16:23], v242\nv_pk_mul_f16 v30, v60, v66 op_sel_hi:[0,1]\nv_pk_mul_f16 v31, v60, v67 op_sel_hi:[0,1]\n"
      "v_mfma_f32_16x16x32_f16 v[32:35], v[96:99], v[24:27], v[32:35]\nv_pk_mul_f16 v16, v62, v66 op_sel_hi:[0,1]\nv_pk_mul_f16 v17, v62, v67 op_sel_hi:[0,1]\n"
      "v_mfma_f32_16x16x32_f16 v[32:35], v[108:111], v[28:31], v[32:35]\nv_pk_mul_f16 v18, v64, v66 op_sel_hi:[0,1]\nv_mul_f32 v244, v244, v52\n"
      "v_mfma_f32_16x16x32_f16 v[36:39], v[112:115], v[28:31], v[36:39]\nv_pk_mul_f16 v19, v64, v67 op_sel_hi:[0,1]\nv_mul_f32 v245, v245, v53\n"
      "v_mfma_f32_16x16x32_f16 v[36:39], v[100:103], v[24:27], v[36:39]\nv_pk_mul_f16 v20, v64, v64 op_sel_hi:[0,1]\nv_fmac_f32 v55, v244, v246\n"
      "v_mfma_f32_16x16x32_f16 v[40:43], v[104:107], v[24:27], v[40:43]\nv_pk_mul_f16 v21, v64, v57 op_sel_hi:[0,1]\nv_fmac_f32 v55, v245, v247\n"
      "v_mfma_f32_16x16x32_f16 v[40:43], v[116:119], v[28:31], v[40:43]\nv_pk_mul_f16 v22, v56, v64 op_sel_hi:[0,1]\nv_readfirstlane_b32 s44, v4\n"
      "v_smfmac_f32_16x16x64_f16 v[44:47], v[120:123], v[24:31], v242\nv_pk_mul_f16 v23, v56, v65 op_sel_hi:[0,1]\ns_setprio 2\nv_mfma_f32_16x16x32_f16 v[32:35], v[124:127], v[16:19], v[32:35]\n"
      "v_pk_mul_f16 v24, v58, v64 op_sel_hi:[0,1]\nv_pk_mul_f16 v25, v58, v65 op_sel_hi:[0,1]\nds_read_b64_tr_b16 v[70:71], v239 offset:12992\n"
      "v_mfma_f32_16x16x32_f16 v[32:35], v[136:139], v[20:23], v[32:35]\nv_pk_mul_f16 v26, v60, v64 op_sel_hi:[0,1]\nds_read_b64_tr_b16 v[68:69], v238 offset:12992\n"
      "v_mfma_f32_16x16x32_f16 v[36:39], v[140:143], v[20:23], v[36:39]\nv_pk_mul_f16 v27, v60, v65 op_sel_hi:[0,1]\nds_read_b64_tr_b16 v[66:67], v237 offset:13632\n"
      "v_mfma_f32_16x16x32_f16 v[36:39], v[128:131], v[16:19], v[36:39]\nv_pk_mul_f16 v28, v62, v64 op_sel_hi:[0,1]\nv_add_f32_dpp v55, v55, v55 quad_perm:[1,0,3,2] row_mask:0xf bank_mask:0xf\n"
      "v_mfma_f32_16x16x32_f16 v[40:43], v[132:135], v[16:19], v[40:43]\nv_pk_mul_f16 v29, v62, v65 op_sel_hi:[0,1]\nv_add_f32_dpp v55, v55, v55 quad_perm:[2,3,0,1] row_mask:0xf bank_mask:0xf\n"
      "v_mfma_f32_16x16x32_f16 v[40:43], v[144:147], v[20:23], v[40:43]\nv_pk_mul_f16 v30, v62, v62 op_sel_hi:[0,1]\nv_add_f32_dpp v55, v55, v55 row_half_mirror row_mask:0xf bank_mask:0xf\n"
      "v_smfmac_f32_16x16x64_f16 v[44:47], v[148:151], v[16:23], v242\nv_pk_mul_f16 v31, v62, v57 op_sel_hi:[0,1]\nv_add_f32_dpp v55, v55, v55 row_mirror row_mask:0xf bank_mask:0xf\n"
      "v_mfma_f32_16x16x32_f16 v[32:35], v[152:155], v[24:27], v[32:35]\nv_pk_mul_f16 v16, v56, v62 op_sel_hi:[0,1]\nv_pk_mul_f16 v17, v56, v63 op_sel_hi:[0,1]\nds_write_b32 v243, v55 offset:640\n"
      "v_mfma_f32_16x16x32_f16 v[32:35], v[164:167], v[28:31], v[32:35]\nv_pk_mul_f16 v18, v58, v62 op_sel_hi:[0,1]\nds_read_b64_tr_b16 v[64:65], v237 offset:13504\n"
      "v_mfma_f32_16x16x32_f16 v[36:39], v[168:171], v[28:31], v[36:39]\nv_pk_mul_f16 v19, v58, v63 op_sel_hi:[0,1]\ns_setprio 3\nv_mfma_f32_16x16x32_f16 v[36:39], v[156:159], v[24:27], v[36:39]\n"
      "v_pk_mul_f16 v20, v60, v62 op_sel_hi:[0,1]\ns_ff0_i32_b32 s53, s44\nv_mfma_f32_16x16x32_f16 v[40:43], v[160:163], v[24:27], v[40:43]\nv_pk_mul_f16 v21, v60, v63 op_sel_hi:[0,1]\n"
      "s_waitcnt lgkmcnt(1)\nv_readlane_b32 s48, v69, 48\nv_mfma_f32_16x16x32_f16 v[40:43], v[172:175], v[28:31], v[40:43]\nv_pk_mul_f16 v22, v60, v60 op_sel_hi:[0,1]\ns_add_u32 s53, s53, s64\n"
      "v_smfmac_f32_16x16x64_f16 v[44:47], v[176:179], v[24:31], v242\nv_pk_mul_f16 v23, v60, v57 op_sel_hi:[0,1]\nv_mfma_f32_16x16x32_f16 v[32:35], v[180:183], v[16:19], v[32:35]\n"
      "v_pk_mul_f16 v24, v56, v60 op_sel_hi:[0,1]\nv_pk_mul_f16 v25, v56, v61 op_sel_hi:[0,1]\nv_mfma_f32_16x16x32_f16 v[32:35], v[192:195], v[20:23], v[32:35]\n"
      "v_pk_mul_f16 v26, v58, v60 op_sel_hi:[0,1]\nv_pk_mul_f16 v27, v58, v61 op_sel_hi:[0,1]\nv_mfma_f32_16x16x32_f16 v[36:39], v[196:199], v[20:23], v[36:39]\n"
      "v_pk_mul_f16 v28, v58, v58 op_sel_hi:[0,1]\nds_read_b64_tr_b16 v[62:63], v237 offset:13376\nv_mfma_f32_16x16x32_f16 v[36:39], v[184:187], v[16:19], v[36:39]\n"
      "v_pk_mul_f16 v29, v58, v57 op_sel_hi:[0,1]\nds_read_b64_tr_b16 v[60:61], v237 offset:13248\nv_mfma_f32_16x16x32_f16 v[40:43], v[188:191], v[16:19], v[40:43]\n"
      "v_pk_mul_f16 v30, v56, v58 op_sel_hi:[0,1]\nv_mfma_f32_16x16x32_f16 v[40:43], v[200:203], v[20:23], v[40:43]\nv_pk_mul_f16 v31, v56, v59 op_sel_hi:[0,1]\n"
      "v_smfmac_f32_16x16x64_f16 v[44:47], v[204:207], v[16:23], v242\nds_read_b64_tr_b16 v[56:57], v236 offset:12992\nds_read_b64_tr_b16 v[58:59], v237 offset:13120\n"
      "v_smfmac_f32_16x16x64_f16 v[44:47], v[232:235], v[24:31], v242\nv_pk_mul_f16 v16, v68, s48 op_sel:[0,1]\nv_pk_mul_f16 v17, v69, s48 op_sel:[0,1]\n"
      "v_mfma_f32_16x16x32_f16 v[32:35], v[208:211], v[24:27], v[32:35]\nv_pk_mul_f16 v18, v70, s48 op_sel:[0,1]\nv_pk_mul_f16 v19, v71, s48 op_sel:[0,1]\n"
      "v_mfma_f32_16x16x32_f16 v[32:35], v[220:223], v[28:31], v[32:35]\nv_pk_mul_f16 v20, v68, v70\nv_pk_mul_f16 v21, v69, v71\nv_mfma_f32_16x16x32_f16 v[36:39], v[224:227], v[28:31], v[36:39]\n"
      "v_add_f32 v44, v44, v45\nv_add_f32 v46, v46, v47\nv_mfma_f32_16x16x32_f16 v[36:39], v[212:215], v[24:27], v[36:39]\nv_mul_f32 v55, v44, v54\nv_mul_f32 v55, v55, v46\n"
      "ds_read_b128 v[44:47], v248 offset:48\nv_mfma_f32_16x16x32_f16 v[40:43], v[216:219], v[24:27], v[40:43]\ns_waitcnt lgkmcnt(1)\nv_pk_mul_f16 v22, v56, v56 op_sel_hi:[0,1]\n"
      "v_pk_mul_f16 v23, v56, v57 op_sel_hi:[0,1]\nv_mfma_f32_16x16x32_f16 v[40:43], v[228:231], v[28:31], v[40:43]\ns_cmp_lt_u32 s45, s53\nv_mul_f32 v32, v32, v48\nv_mul_f32 v33, v33, v49\n"
      "v_mul_f32 v34, v34, v50\ns_cbranch_scc0 .Lc_s6\n"
      ".Lc_u7:\nv_mfma_f32_16x16x32_f16 v[244:247], v[76:79], v[16:19], 0\nds_read_b32 v4, v248\nv_fmac_f32 v55, v32, v36\nv_mul_f32 v35, v35, v51\nv_fmac_f32 v55, v33, v37\ns_setprio 0\n"
      "v_mfma_f32_16x16x32_f16 v[244:247], v[88:91], v[20:23], v[244:247]\nv_fmac_f32 v55, v34, v38\nv_fmac_f32 v55, v35, v39\ns_add_u32 s45, s45, 2\ns_cmp_ge_u32 s45, s46\ns_cbranch_scc1 .Lc_t7\n"
      ".Lc_b7:\nv_mfma_f32_16x16x32_f16 v[36:39], v[84:87], v[20:23], 0\nv_pk_mul_f16 v24, v66, v66 op_sel_hi:[0,1]\nv_pk_mul_f16 v25, v66, v57 op_sel_hi:[0,1]\n"
      "v_mfma_f32_16x16x32_f16 v[36:39], v[72:75], v[16:19], v[36:39]\nv_pk_mul_f16 v26, v56, v66 op_sel_hi:[0,1]\nv_pk_mul_f16 v27, v56, v67 op_sel_hi:[0,1]\n"
      "v_mfma_f32_16x16x32_f16 v[32:35], v[80:83], v[20:23], 0\nv_pk_mul_f16 v28, v58, v66 op_sel_hi:[0,1]\nv_pk_mul_f16 v29, v58, v67 op_sel_hi:[0,1]\ns_setprio 1\ns_waitcnt lgkmcnt(0)\n"
      "v_smfmac_f32_16x16x64_f16 v[44:47], v[92:95], v[16:23], v242\nv_pk_mul_f16 v30, v60, v66 op_sel_hi:[0,1]\nv_pk_mul_f16 v31, v60, v67 op_sel_hi:[0,1]\n"
      "v_mfma_f32_16x16x32_f16 v[32:35], v[96:99], v[24:27], v[32:35]\nv_pk_mul_f16 v16, v62, v66 op_sel_hi:[0,1]\nv_pk_mul_f16 v17, v62, v67 op_sel_hi:[0,1]\n"
      "v_mfma_f32_16x16x32_f16 v[32:35], v[108:111], v[28:31], v[32:35]\nv_pk_mul_f16 v18, v64, v66 op_sel_hi:[0,1]\nv_mul_f32 v40, v40, v52\n"
      "v_mfma_f32_16x16x32_f16 v[36:39], v[112:115], v[28:31], v[36:39]\nv_pk_mul_f16 v19, v64, v67 op_sel_hi:[0,1]\nv_mul_f32 v41, v41, v53\n"
      "v_mfma_f32_16x16x32_f16 v[36:39], v[100:103], v[24:27], v[36:39]\nv_pk_mul_f16 v20, v64, v64 op_sel_hi:[0,1]\nv_fmac_f32 v55, v40, v42\n"
      "v_mfma_f32_16x16x32_f16 v[244:247], v[104:107], v[24:27], v[244:247]\nv_pk_mul_f16 v21, v64, v57 op_sel_hi:[0,1]\nv_fmac_f32 v55, v41, v43\n"
      "v_mfma_f32_16x16x32_f16 v[244:247], v[116:119], v[28:31], v[244:247]\nv_pk_mul_f16 v22, v56, v64 op_sel_hi:[0,1]\nv_readfirstlane_b32 s44, v4\n"
      "v_smfmac_f32_16x16x64_f16 v[44:47], v[120:123], v[24:31], v242\nv_pk_mul_f16 v23, v56, v65 op_sel_hi:[0,1]\ns_setprio 2\nv_mfma_f32_16x16x32_f16 v[32:35], v[124:127], v[16:19], v[32:35]\n"
      "v_pk_mul_f16 v24, v58, v64 op_sel_hi:[0,1]\nv_pk_mul_f16 v25, v58, v65 op_sel_hi:[0,1]\nds_read_b64_tr_b16 v[70:71], v239 offset:14848\n"
      "v_mfma_f32_16x16x32_f16 v[32:35], v[136:139], v[20:23], v[32:35]\nv_pk_mul_f16 v26, v60, v64 op_sel_hi:[0,1]\nds_read_b64_tr_b16 v[68:69], v238 offset:14848\n"
      "v_mfma_f32_16x16x32_f16 v[36:39], v[140:143], v[20:23], v[36:39]\nv_pk_mul_f16 v27, v60, v65 op_sel_hi:[0,1]\nds_read_b64_tr_b16 v[66:67], v237 offset:15488\n"
      "v_mfma_f32_16x16x32_f16 v[36:39], v[128:131], v[16:19], v[36:39]\nv_pk_mul_f16 v28, v62, v64 op_sel_hi:[0,1]\nv_add_f32_dpp v55, v55, v55 quad_perm:[1,0,3,2] row_mask:0xf bank_mask:0xf\n"
      "v_mfma_f32_16x16x32_f16 v[244:247], v[132:135], v[16:19], v[244:247]\nv_pk_mul_f16 v29, v62, v65 op_sel_hi:[0,1]\nv_add_f32_dpp v55, v55, v55 quad_perm:[2,3,0,1] row_mask:0xf bank_mask:0xf\n"
      "v_mfma_f32_16x16x32_f16 v[244:247], v[144:147], v[20:23], v[244:247]\nv_pk_mul_f16 v30, v62, v62 op_sel_hi:[0,1]\nv_add_f32_dpp v55, v55, v55 row_half_mirror row_mask:0xf bank_mask:0xf\n"
      "v_smfmac_f32_16x16x64_f16 v[44:47], v[148:151], v[16:23], v242\nv_pk_mul_f16 v31, v62, v57 op_sel_hi:[0,1]\nv_add_f32_dpp v55, v55, v55 row_mirror row_mask:0xf bank_mask:0xf\n"
      "v_mfma_f32_16x16x32_f16 v[32:35], v[152:155], v[24:27], v[32:35]\nv_pk_mul_f16 v16, v56, v62 op_sel_hi:[0,1]\nv_pk_mul_f16 v17, v56, v63 op_sel_hi:[0,1]\nds_write_b32 v243, v55 offset:768\n"
      "v_mfma_f32_16x16x32_f16 v[32:35], v[164:167], v[28:31], v[32:35]\nv_pk_mul_f16 v18, v58, v62 op_sel_hi:[0,1]\nds_read_b64_tr_b16 v[64:65], v237 offset:15360\n"
      "v_mfma_f32_16x16x32_f16 v[36:39], v[168:171], v[28:31], v[36:39]\nv_pk_mul_f16 v19, v58, v63 op_sel_hi:[0,1]\ns_setprio 3\nv_mfma_f32_16x16x32_f16 v[36:39], v[156:159], v[24:27], v[36:39]\n"
      "v_pk_mul_f16 v20, v60, v62 op_sel_hi:[0,1]\ns_ff0_i32_b32 s53, s44\nv_mfma_f32_16x16x32_f16 v[244:247], v[160:163], v[24:27], v[244:247]\nv_pk_mul_f16 v21, v60, v63 op_sel_hi:[0,1]\n"
      "s_waitcnt lgkmcnt(1)\nv_readlane_b32 s48, v69, 48\nv_mfma_f32_16x16x32_f16 v[244:247], v[172:175], v[28:31], v[244:247]\nv_pk_mul_f16 v22, v60, v60 op_sel_hi:[0,1]\ns_add_u32 s53, s53, s64\n"
      "v_smfmac_f32_16x16x64_f16 v[44:47], v[176:179], v[24:31], v242\nv_pk_mul_f16 v23, v60, v57 op_sel_hi:[0,1]\nv_mfma_f32_16x16x32_f16 v[32:35], v[180:183], v[16:19], v[32:35]\n"
      "v_pk_mul_f16 v24, v56, v60 op_sel_hi:[0,1]\nv_pk_mul_f16 v25, v56, v61 op_sel_hi:[0,1]\nv_mfma_f32_16x16x32_f16 v[32:35], v[192:195], v[20:23], v[32:35]\n"
      "v_pk_mul_f16 v26, v58, v60 op_sel_hi:[0,1]\nv_pk_mul_f16 v27, v58, v61 op_sel_hi:[0,1]\nv_mfma_f32_16x16x32_f16 v[36:39], v[196:199], v[20:23], v[36:39]\n"
      "v_pk_mul_f16 v28, v58, v58 op_sel_hi:[0,1]\nds_read_b64_tr_b16 v[62:63], v237 offset:15232\nv_mfma_f32_16x16x32_f16 v[36:39], v[184:187], v[16:19], v[36:39]\n"
      "v_pk_mul_f16 v29, v58, v57 op_sel_hi:[0,1]\nds_read_b64_tr_b16 v[60:61], v237 offset:15104\nv_mfma_f32_16x16x32_f16 v[244:247], v[188:191], v[16:19], v[244:247]\n"
      "v_pk_mul_f16 v30, v56, v58 op_sel_hi:[0,1]\nv_mfma_f32_16x16x32_f16 v[244:247], v[200:203], v[20:23], v[244:247]\nv_pk_mul_f16 v31, v56, v59 op_sel_hi:[0,1]\n"
      "v_smfmac_f32_16x16x64_f16 v[44:47], v[204:207], v[16:23], v242\nds_read_b64_tr_b16 v[56:57], v236 offset:14848\nds_read_b64_tr_b16 v[58:59], v237 offset:14976\n"
      "v_smfmac_f32_16x16x64_f16 v[44:47], v[232:235], v[24:31], v242\nv_pk_mul_f16 v16, v68, s48 op_sel:[0,1]\nv_pk_mul_f16 v17, v69, s48 op_sel:[0,1]\n"
      "v_mfma_f32_16x16x32_f16 v[32:35], v[208:211], v[24:27], v[32:35]\nv_pk_mul_f16 v18, v70, s48 op_sel:[0,1]\nv_pk_mul_f16 v19, v71, s48 op_sel:[0,1]\n"
      "v_mfma_f32_16x16x32_f16 v[32:35], v[220:223], v[28:31], v[32:35]\nv_pk_mul_f16 v20, v68, v70\nv_pk_mul_f16 v21, v69, v71\nv_mfma_f32_16x16x32_f16 v[36:39], v[224:227], v[28:31], v[36:39]\n"
      "v_add_f32 v44, v44, v45\nv_add_f32 v46, v46, v47\nv_mfma_f32_16x16x32_f16 v[36:39], v[212:215], v[24:27], v[36:39]\nv_mul_f32 v55, v44, v54\nv_mul_f32 v55, v55, v46\n"
      "ds_read_b128 v[44:47], v248 offset:48\nv_mfma_f32_16x16x32_f16 v[244:247], v[216:219], v[24:27], v[244:247]\ns_waitcnt lgkmcnt(1)\nv_pk_mul_f16 v22, v56, v56 op_sel_hi:[0,1]\n"
      "v_pk_mul_f16 v23, v56, v57 op_sel_hi:[0,1]\nv_mfma_f32_16x16x32_f16 v[244:247], v[228:231], v[28:31], v[244:247]\ns_cmp_lt_u32 s45, s53\nv_mul_f32 v32, v32, v48\nv_mul_f32 v33, v33, v49\n"
      "v_mul_f32 v34, v34, v50\ns_cbranch_scc0 .Lc_s7\n"
      ".Lc_u8:\nv_mfma_f32_16x16x32_f16 v[40:43], v[76:79], v[16:19], 0\nds_read_b32 v4, v248\nv_fmac_f32 v55, v32, v36\nv_mul_f32 v35, v35, v51\nv_fmac_f32 v55, v33, v37\ns_setprio 0\n"
      "v_mfma_f32_16x16x32_f16 v[40:43], v[88:91], v[20:23], v[40:43]\nv_fmac_f32 v55, v34, v38\nv_fmac_f32 v55, v35, v39\ns_add_u32 s45, s45, 2\ns_cmp_ge_u32 s45, s46\ns_cbranch_scc1 .Lc_t8\n"
      ".Lc_b8:\nv_mfma_f32_16x16x32_f16 v[36:39], v[84:87], v[20:23], 0\nv_pk_mul_f16 v24, v66, v66 op_sel_hi:[0,1]\nv_pk_mul_f16 v25, v66, v57 op_sel_hi:[0,1]\n"
      "v_mfma_f32_16x16x32_f16 v[36:39], v[72:75], v[16:19], v[36:39]\nv_pk_mul_f16 v26, v56, v66 op_sel_hi:[0,1]\nv_pk_mul_f16 v27, v56, v67 op_sel_hi:[0,1]\n"
      "v_mfma_f32_16x16x32_f16 v[32:35], v[80:83], v[20:23], 0\nv_pk_mul_f16 v28, v58, v66 op_sel_hi:[0,1]\nv_pk_mul_f16 v29, v58, v67 op_sel_hi:[0,1]\ns_setprio 1\ns_waitcnt lgkmcnt(0)\n"
      "v_smfmac_f32_16x16x64_f16 v[44:47], v[92:95], v[16:23], v242\nv_pk_mul_f16 v30, v60, v66 op_sel_hi:[0,1]\nv_pk_mul_f16 v31, v60, v67 op_sel_hi:[0,1]\n"
      "v_mfma_f32_16x16x32_f16 v[32:35], v[96:99], v[24:27], v[32:35]\nv_pk_mul_f16 v16, v62, v66 op_sel_hi:[0,1]\nv_pk_mul_f16 v17, v62, v67 op_sel_hi:[0,1]\n"
      "v_mfma_f32_16x16x32_f16 v[32:35], v[108:111], v[28:31], v[32:35]\nv_pk_mul_f16 v18, v64, v66 op_sel_hi:[0,1]\nv_mul_f32 v244, v244, v52\n"
      "v_mfma_f32_16x16x32_f16 v[36:39], v[112:115], v[28:31], v[36:39]\nv_pk_mul_f16 v19, v64, v67 op_sel_hi:[0,1]\nv_mul_f32 v245, v245, v53\n"
      "v_mfma_f32_16x16x32_f16 v[36:39], v[100:103], v[24:27], v[36:39]\nv_pk_mul_f16 v20, v64, v64 op_sel_hi:[0,1]\nv_fmac_f32 v55, v244, v246\n"
      "v_mfma_f32_16x16x32_f16 v[40:43], v[104:107], v[24:27], v[40:43]\nv_pk_mul_f16 v21, v64, v57 op_sel_hi:[0,1]\nv_fmac_f32 v55, v245, v247\n"
      "v_mfma_f32_16x16x32_f16 v[40:43], v[116:119], v[28:31], v[40:43]\nv_pk_mul_f16 v22, v56, v64 op_sel_hi:[0,1]\nv_readfirstlane_b32 s44, v4\n"
      "v_smfmac_f32_16x16x64_f16 v[44:47], v[120:123], v[24:31], v242\nv_pk_mul_f16 v23, v56, v65 op_sel_hi:[0,1]\ns_setprio 2\nv_mfma_f32_16x16x32_f16 v[32:35], v[124:127], v[16:19], v[32:35]\n"
      "v_pk_mul_f16 v24, v58, v64 op_sel_hi:[0,1]\nv_pk_mul_f16 v25, v58, v65 op_sel_hi:[0,1]\nds_read_b64_tr_b16 v[70:71], v239 offset:16704\n"
      "v_mfma_f32_16x16x32_f16 v[32:35], v[136:139], v[20:23], v[32:35]\nv_pk_mul_f16 v26, v60, v64 op_sel_hi:[0,1]\nds_read_b64_tr_b16 v[68:69], v238 offset:16704\n"
      "v_mfma_f32_16x16x32_f16 v[36:39], v[140:143], v[20:23], v[36:39]\nv_pk_mul_f16 v27, v60, v65 op_sel_hi:[0,1]\nds_read_b64_tr_b16 v[66:67], v237 offset:17344\n"
      "v_mfma_f32_16x16x32_f16 v[36:39], v[128:131], v[16:19], v[36:39]\nv_pk_mul_f16 v28, v62, v64 op_sel_hi:[0,1]\nv_add_f32_dpp v55, v55, v55 quad_perm:[1,0,3,2] row_mask:0xf bank_mask:0xf\n"
      "v_mfma_f32_16x16x32_f16 v[40:43], v[132:135], v[16:19], v[40:43]\nv_pk_mul_f16 v29, v62, v65 op_sel_hi:[0,1]\nv_add_f32_dpp v55, v55, v55 quad_perm:[2,3,0,1] row_mask:0xf bank_mask:0xf\n"
      "v_mfma_f32_16x16x32_f16 v[40:43], v[144:147], v[20:23], v[40:43]\nv_pk_mul_f16 v30, v62, v62 op_sel_hi:[0,1]\nv_add_f32_dpp v55, v55, v55 row_half_mirror row_mask:0xf bank_mask:0xf\n"
      "v_smfmac_f32_16x16x64_f16 v[44:47], v[148:151], v[16:23], v242\nv_pk_mul_f16 v31, v62, v57 op_sel_hi:[0,1]\nv_add_f32_dpp v55, v55, v55 row_mirror row_mask:0xf bank_mask:0xf\n"
      "v_mfma_f32_16x16x32_f16 v[32:35], v[152:155], v[24:27], v[32:35]\nv_pk_mul_f16 v16, v56, v62 op_sel_hi:[0,1]\nv_pk_mul_f16 v17, v56, v63 op_sel_hi:[0,1]\nds_write_b32 v243, v55 offset:896\n"
      "v_mfma_f32_16x16x32_f16 v[32:35], v[164:167], v[28:31], v[32:35]\nv_pk_mul_f16 v18, v58, v62 op_sel_hi:[0,1]\nds_read_b64_tr_b16 v[64:65], v237 offset:17216\n"
      "v_mfma_f32_16x16x32_f16 v[36:39], v[168:171], v[28:31], v[36:39]\nv_pk_mul_f16 v19, v58, v63 op_sel_hi:[0,1]\ns_setprio 3\nv_mfma_f32_16x16x32_f16 v[36:39], v[156:159], v[24:27], v[36:39]\n"
      "v_pk_mul_f16 v20, v60, v62 op_sel_hi:[0,1]\ns_ff0_i32_b32 s53, s44\nv_mfma_f32_16x16x32_f16 v[40:43], v[160:163], v[24:27], v[40:43]\nv_pk_mul_f16 v21, v60, v63 op_sel_hi:[0,1]\n"
      "s_waitcnt lgkmcnt(1)\nv_readlane_b32 s48, v69, 48\nv_mfma_f32_16x16x32_f16 v[40:43], v[172:175], v[28:31], v[40:43]\nv_pk_mul_f16 v22, v60, v60 op_sel_hi:[0,1]\ns_add_u32 s53, s53, s64\n"
      "v_smfmac_f32_16x16x64_f16 v[44:47], v[176:179], v[24:31], v242\nv_pk_mul_f16 v23, v60, v57 op_sel_hi:[0,1]\nv_mfma_f32_16x16x32_f16 v[32:35], v[180:183], v[16:19], v[32:35]\n"
      "v_pk_mul_f16 v24, v56, v60 op_sel_hi:[0,1]\nv_pk_mul_f16 v25, v56, v61 op_sel_hi:[0,1]\nv_mfma_f32_16x16x32_f16 v[32:35], v[192:195], v[20:23], v[32:35]\n"
      "v_pk_mul_f16 v26, v58, v60 op_sel_hi:[0,1]\nv_pk_mul_f16 v27, v58, v61 op_sel_hi:[0,1]\nv_mfma_f32_16x16x32_f16 v[36:39], v[196:199], v[20:23], v[36:39]\n"
      "v_pk_mul_f16 v28, v58, v58 op_sel_hi:[0,1]\nds_read_b64_tr_b16 v[62:63], v237 offset:17088\nv_mfma_f32_16x16x32_f16 v[36:39], v[184:187], v[16:19], v[36:39]\n"
      "v_pk_mul_f16 v29, v58, v57 op_sel_hi:[0,1]\nds_read_b64_tr_b16 v[60:61], v237 offset:16960\nv_mfma_f32_16x16x32_f16 v[40:43], v[188:191], v[16:19], v[40:43]\n"
      "v_pk_mul_f16 v30, v56, v58 op_sel_hi:[0,1]\nv_mfma_f32_16x16x32_f16 v[40:43], v[200:203], v[20:23], v[40:43]\nv_pk_mul_f16 v31, v56, v59 op_sel_hi:[0,1]\n"
      "v_smfmac_f32_16x16x64_f16 v[44:47], v[204:207], v[16:23], v242\nds_read_b64_tr_b16 v[56:57], v236 offset:16704\nds_read_b64_tr_b16 v[58:59], v237 offset:16832\n"
      "v_smfmac_f32_16x16x64_f16 v[44:47], v[232:235], v[24:31], v242\nv_pk_mul_f16 v16, v68, s48 op_sel:[0,1]\nv_pk_mul_f16 v17, v69, s48 op_sel:[0,1]\n"
      "v_mfma_f32_16x16x32_f16 v[32:35], v[208:211], v[24:27], v[32:35]\nv_pk_mul_f16 v18, v70, s48 op_sel:[0,1]\nv_pk_mul_f16 v19, v71, s48 op_sel:[0,1]\n"
      "v_mfma_f32_16x16x32_f16 v[32:35], v[220:223], v[28:31], v[32:35]\nv_pk_mul_f16 v20, v68, v70\nv_pk_mul_f16 v21, v69, v71\nv_mfma_f32_16x16x32_f16 v[36:39], v[224:227], v[28:31], v[36:39]\n"
      "v_add_f32 v44, v44, v45\nv_add_f32 v46, v46, v47\nv_mfma_f32_16x16x32_f16 v[36:39], v[212:215], v[24:27], v[36:39]\nv_mul_f32 v55, v44, v54\nv_mul_f32 v55, v55, v46\n"
      "ds_read_b128 v[44:47], v248 offset:48\nv_mfma_f32_16x16x32_f16 v[40:43], v[216:219], v[24:27], v[40:43]\ns_waitcnt lgkmcnt(1)\nv_pk_mul_f16 v22, v56, v56 op_sel_hi:[0,1]\n"
      "v_pk_mul_f16 v23, v56, v57 op_sel_hi:[0,1]\nv_mfma_f32_16x16x32_f16 v[40:43], v[228:231], v[28:31], v[40:43]\ns_cmp_lt_u32 s45, s53\nv_mul_f32 v32, v32, v48\nv_mul_f32 v33, v33, v49\n"
      "v_mul_f32 v34, v34, v50\ns_cbranch_scc0 .Lc_s8\n"
      ".Lc_u9:\nv_mfma_f32_16x16x32_f16 v[244:247], v[76:79], v[16:19], 0\nds_read_b32 v4, v248\nv_fmac_f32 v55, v32, v36\nv_mul_f32 v35, v35, v51\nv_fmac_f32 v55, v33, v37\ns_setprio 0\n"
      "v_mfma_f32_16x16x32_f16 v[244:247], v[88:91], v[20:23], v[244:247]\nv_fmac_f32 v55, v34, v38\nv_fmac_f32 v55, v35, v39\ns_add_u32 s45, s45, 2\ns_cmp_ge_u32 s45, s46\ns_cbranch_scc1 .Lc_t9\n"
      ".Lc_b9:\nv_mfma_f32_16x16x32_f16 v[36:39], v[84:87], v[20:23], 0\nv_pk_mul_f16 v24, v66, v66 op_sel_hi:[0,1]\nv_pk_mul_f16 v25, v66, v57 op_sel_hi:[0,1]\n"
      "v_mfma_f32_16x16x32_f16 v[36:39], v[72:75], v[16:19], v[36:39]\nv_pk_mul_f16 v26, v56, v66 op_sel_hi:[0,1]\nv_pk_mul_f16 v27, v56, v67 op_sel_hi:[0,1]\n"
      "v_mfma_f32_16x16x32_f16 v[32:35], v[80:83], v[20:23], 0\nv_pk_mul_f16 v28, v58, v66 op_sel_hi:[0,1]\nv_pk_mul_f16 v29, v58, v67 op_sel_hi:[0,1]\ns_setprio 1\ns_waitcnt lgkmcnt(0)\n"
      "v_smfmac_f32_16x16x64_f16 v[44:47], v[92:95], v[16:23], v242\nv_pk_mul_f16 v30, v60, v66 op_sel_hi:[0,1]\nv_pk_mul_f16 v31, v60, v67 op_sel_hi:[0,1]\n"
      "v_mfma_f32_16x16x32_f16 v[32:35], v[96:99], v[24:27], v[32:35]\nv_pk_mul_f16 v16, v62, v66 op_sel_hi:[0,1]\nv_pk_mul_f16 v17, v62, v67 op_sel_hi:[0,1]\n"
      "v_mfma_f32_16x16x32_f16 v[32:35], v[108:111], v[28:31], v[32:35]\nv_pk_mul_f16 v18, v64, v66 op_sel_hi:[0,1]\nv_mul_f32 v40, v40, v52\n"
      "v_mfma_f32_16x16x32_f16 v[36:39], v[112:115], v[28:31], v[36:39]\nv_pk_mul_f16 v19, v64, v67 op_sel_hi:[0,1]\nv_mul_f32 v41, v41, v53\n"
      "v_mfma_f32_16x16x32_f16 v[36:39], v[100:103], v[24:27], v[36:39]\nv_pk_mul_f16 v20, v64, v64 op_sel_hi:[0,1]\nv_fmac_f32 v55, v40, v42\n"
      "v_mfma_f32_16x16x32_f16 v[244:247], v[104:107], v[24:27], v[244:247]\nv_pk_mul_f16 v21, v64, v57 op_sel_hi:[0,1]\nv_fmac_f32 v55, v41, v43\n"
      "v_mfma_f32_16x16x32_f16 v[244:247], v[116:119], v[28:31], v[244:247]\nv_pk_mul_f16 v22, v56, v64 op_sel_hi:[0,1]\nv_readfirstlane_b32 s44, v4\n"
      "v_smfmac_f32_16x16x64_f16 v[44:47], v[120:123], v[24:31], v242\nv_pk_mul_f16 v23, v56, v65 op_sel_hi:[0,1]\ns_setprio 2\nv_mfma_f32_16x16x32_f16 v[32:35], v[124:127], v[16:19], v[32:35]\n"
      "v_pk_mul_f16 v24, v58, v64 op_sel_hi:[0,1]\nv_pk_mul_f16 v25, v58, v65 op_sel_hi:[0,1]\nds_read_b64_tr_b16 v[70:71], v239 offset:18560\n"
      "v_mfma_f32_16x16x32_f16 v[32:35], v[136:139], v[20:23], v[32:35]\nv_pk_mul_f16 v26, v60, v64 op_sel_hi:[0,1]\nds_read_b64_tr_b16 v[68:69], v238 offset:18560\n"
      "v_mfma_f32_16x16x32_f16 v[36:39], v[140:143], v[20:23], v[36:39]\nv_pk_mul_f16 v27, v60, v65 op_sel_hi:[0,1]\nds_read_b64_tr_b16 v[66:67], v237 offset:19200\n"
      "v_mfma_f32_16x16x32_f16 v[36:39], v[128:131], v[16:19], v[36:39]\nv_pk_mul_f16 v28, v62, v64 op_sel_hi:[0,1]\nv_add_f32_dpp v55, v55, v55 quad_perm:[1,0,3,2] row_mask:0xf bank_mask:0xf\n"
      "v_mfma_f32_16x16x32_f16 v[244:247], v[132:135], v[16:19], v[244:247]\nv_pk_mul_f16 v29, v62, v65 op_sel_hi:[0,1]\nv_add_f32_dpp v55, v55, v55 quad_perm:[2,3,0,1] row_mask:0xf bank_mask:0xf\n"
      "v_mfma_f32_16x16x32_f16 v[244:247], v[144:147], v[20:23], v[244:247]\nv_pk_mul_f16 v30, v62, v62 op_sel_hi:[0,1]\nv_add_f32_dpp v55, v55, v55 row_half_mirror row_mask:0xf bank_mask:0xf\n"
      "v_smfmac_f32_16x16x64_f16 v[44:47], v[148:151], v[16:23], v242\nv_pk_mul_f16 v31, v62, v57 op_sel_hi:[0,1]\nv_add_f32_dpp v55, v55, v55 row_mirror row_mask:0xf bank_mask:0xf\n"
      "v_mfma_f32_16x16x32_f16 v[32:35], v[152:155], v[24:27], v[32:35]\nv_pk_mul_f16 v16, v56, v62 op_sel_hi:[0,1]\nv_pk_mul_f16 v17, v56, v63 op_sel_hi:[0,1]\nds_write_b32 v243, v55 offset:1024\n"
      "v_mfma_f32_16x16x32_f16 v[32:35], v[164:167], v[28:31], v[32:35]\nv_pk_mul_f16 v18, v58, v62 op_sel_hi:[0,1]\nds_read_b64_tr_b16 v[64:65], v237 offset:19072\n"
      "v_mfma_f32_16x16x32_f16 v[36:39], v[168:171], v[28:31], v[36:39]\nv_pk_mul_f16 v19, v58, v63 op_sel_hi:[0,1]\ns_setprio 3\nv_mfma_f32_16x16x32_f16 v[36:39], v[156:159], v[24:27], v[36:39]\n"
      "v_pk_mul_f16 v20, v60, v62 op_sel_hi:[0,1]\ns_ff0_i32_b32 s53, s44\nv_mfma_f32_16x16x32_f16 v[244:247], v[160:163], v[24:27], v[244:247]\nv_pk_mul_f16 v21, v60, v63 op_sel_hi:[0,1]\n"
      "s_waitcnt lgkmcnt(1)\nv_readlane_b32 s48, v69, 48\nv_mfma_f32_16x16x32_f16 v[244:247], v[172:175], v[28:31], v[244:247]\nv_pk_mul_f16 v22, v60, v60 op_sel_hi:[0,1]\ns_add_u32 s53, s53, s64\n"
      "v_smfmac_f32_16x16x64_f16 v[44:47], v[176:179], v[24:31], v242\nv_pk_mul_f16 v23, v60, v57 op_sel_hi:[0,1]\nv_mfma_f32_16x16x32_f16 v[32:35], v[180:183], v[16:19], v[32:35]\n"
      "v_pk_mul_f16 v24, v56, v60 op_sel_hi:[0,1]\nv_pk_mul_f16 v25, v56, v61 op_sel_hi:[0,1]\nv_mfma_f32_16x16x32_f16 v[32:35], v[192:195], v[20:23], v[32:35]\n"
      "v_pk_mul_f16 v26, v58, v60 op_sel_hi:[0,1]\nv_pk_mul_f16 v27, v58, v61 op_sel_hi:[0,1]\nv_mfma_f32_16x16x32_f16 v[36:39], v[196:199], v[20:23], v[36:39]\n"
      "v_pk_mul_f16 v28, v58, v58 op_sel_hi:[0,1]\nds_read_b64_tr_b16 v[62:63], v237 offset:18944\nv_mfma_f32_16x16x32_f16 v[36:39], v[184:187], v[16:19], v[36:39]\n"
      "v_pk_mul_f16 v29, v58, v57 op_sel_hi:[0,1]\nds_read_b64_tr_b16 v[60:61], v237 offset:18816\nv_mfma_f32_16x16x32_f16 v[244:247], v[188:191], v[16:19], v[244:247]\n"
      "v_pk_mul_f16 v30, v56, v58 op_sel_hi:[0,1]\nv_mfma_f32_16x16x32_f16 v[244:247], v[200:203], v[20:23], v[244:247]\nv_pk_mul_f16 v31, v56, v59 op_sel_hi:[0,1]\n"
      "v_smfmac_f32_16x16x64_f16 v[44:47], v[204:207], v[16:23], v242\nds_read_b64_tr_b16 v[56:57], v236 offset:18560\nds_read_b64_tr_b16 v[58:59], v237 offset:18688\n"
      "v_smfmac_f32_16x16x64_f16 v[44:47], v[232:235], v[24:31], v242\nv_pk_mul_f16 v16, v68, s48 op_sel:[0,1]\nv_pk_mul_f16 v17, v69, s48 op_sel:[0,1]\n"
      "v_mfma_f32_16x16x32_f16 v[32:35], v[208:211], v[24:27], v[32:35]\nv_pk_mul_f16 v18, v70, s48 op_sel:[0,1]\nv_pk_mul_f16 v19, v71, s48 op_sel:[0,1]\n"
      "v_mfma_f32_16x16x32_f16 v[32:35], v[220:223], v[28:31], v[32:35]\nv_pk_mul_f16 v20, v68, v70\nv_pk_mul_f16 v21, v69, v71\nv_mfma_f32_16x16x32_f16 v[36:39], v[224:227], v[28:31], v[36:39]\n"
      "v_add_f32 v44, v44, v45\nv_add_f32 v46, v46, v47\nv_mfma_f32_16x16x32_f16 v[36:39], v[212:215], v[24:27], v[36:39]\nv_mul_f32 v55, v44, v54\nv_mul_f32 v55, v55, v46\n"
      "ds_read_b128 v[44:47], v248 offset:48\nv_mfma_f32_16x16x32_f16 v[244:247], v[216:219], v[24:27], v[244:247]\ns_waitcnt lgkmcnt(1)\nv_pk_mul_f16 v22, v56, v56 op_sel_hi:[0,1]\n"
      "v_pk_mul_f16 v23, v56, v57 op_sel_hi:[0,1]\nv_mfma_f32_16x16x32_f16 v[244:247], v[228:231], v[28:31], v[244:247]\ns_cmp_lt_u32 s45, s53\nv_mul_f32 v32, v32, v48\nv_mul_f32 v33, v33, v49\n"
      "v_mul_f32 v34, v34, v50\ns_cbranch_scc0 .Lc_s9\n"
      ".Lc_u10:\nv_mfma_f32_16x16x32_f16 v[40:43], v[76:79], v[16:19], 0\nds_read_b32 v4, v248\nv_fmac_f32 v55, v32, v36\nv_mul_f32 v35, v35, v51\nv_fmac_f32 v55, v33, v37\ns_setprio 0\n"
      "v_mfma_f32_16x16x32_f16 v[40:43], v[88:91], v[20:23], v[40:43]\nv_fmac_f32 v55, v34, v38\nv_fmac_f32 v55, v35, v39\ns_add_u32 s45, s45, 2\ns_cmp_ge_u32 s45, s46\ns_cbranch_scc1 .Lc_t10\n"
      ".Lc_b10:\nv_mfma_f32_16x16x32_f16 v[36:39], v[84:87], v[20:23], 0\nv_pk_mul_f16 v24, v66, v66 op_sel_hi:[0,1]\nv_pk_mul_f16 v25, v66, v57 op_sel_hi:[0,1]\n"
      "v_mfma_f32_16x16x32_f16 v[36:39], v[72:75], v[16:19], v[36:39]\nv_pk_mul_f16 v26, v56, v66 op_sel_hi:[0,1]\nv_pk_mul_f16 v27, v56, v67 op_sel_hi:[0,1]\n"
      "v_mfma_f32_16x16x32_f16 v[32:35], v[80:83], v[20:23], 0\nv_pk_mul_f16 v28, v58, v66 op_sel_hi:[0,1]\nv_pk_mul_f16 v29, v58, v67 op_sel_hi:[0,1]\ns_setprio 1\ns_waitcnt lgkmcnt(0)\n"
      "v_smfmac_f32_16x16x64_f16 v[44:47], v[92:95], v[16:23], v242\nv_pk_mul_f16 v30, v60, v66 op_sel_hi:[0,1]\nv_pk_mul_f16 v31, v60, v67 op_sel_hi:[0,1]\n"
      "v_mfma_f32_16x16x32_f16 v[32:35], v[96:99], v[24:27], v[32:35]\nv_pk_mul_f16 v16, v62, v66 op_sel_hi:[0,1]\nv_pk_mul_f16 v17, v62, v67 op_sel_hi:[0,1]\n"
      "v_mfma_f32_16x16x32_f16 v[32:35], v[108:111], v[28:31], v[32:35]\nv_pk_mul_f16 v18, v64, v66 op_sel_hi:[0,1]\nv_mul_f32 v244, v244, v52\n"
      "v_mfma_f32_16x16x32_f16 v[36:39], v[112:115], v[28:31], v[36:39]\nv_pk_mul_f16 v19, v64, v67 op_sel_hi:[0,1]\nv_mul_f32 v245, v245, v53\n"
      "v_mfma_f32_16x16x32_f16 v[36:39], v[100:103], v[24:27], v[36:39]\nv_pk_mul_f16 v20, v64, v64 op_sel_hi:[0,1]\nv_fmac_f32 v55, v244, v246\n"
      "v_mfma_f32_16x16x32_f16 v[40:43], v[104:107], v[24:27], v[40:43]\nv_pk_mul_f16 v21, v64, v57 op_sel_hi:[0,1]\nv_fmac_f32 v55, v245, v247\n"
      "v_mfma_f32_16x16x32_f16 v[40:43], v[116:119], v[28:31], v[40:43]\nv_pk_mul_f16 v22, v56, v64 op_sel_hi:[0,1]\nv_readfirstlane_b32 s44, v4\n"
      "v_smfmac_f32_16x16x64_f16 v[44:47], v[120:123], v[24:31], v242\nv_pk_mul_f16 v23, v56, v65 op_sel_hi:[0,1]\ns_setprio 2\nv_mfma_f32_16x16x32_f16 v[32:35], v[124:127], v[16:19], v[32:35]\n"
      "v_pk_mul_f16 v24, v58, v64 op_sel_hi:[0,1]\nv_pk_mul_f16 v25, v58, v65 op_sel_hi:[0,1]\nv_mfma_f32_16x16x32_f16 v[32:35], v[136:139], v[20:23], v[32:35]\n"
      "v_pk_mul_f16 v26, v60, v64 op_sel_hi:[0,1]\nv_mfma_f32_16x16x32_f16 v[36:39], v[140:143], v[20:23], v[36:39]\nv_pk_mul_f16 v27, v60, v65 op_sel_hi:[0,1]\n"
      "v_mfma_f32_16x16x32_f16 v[36:39], v[128:131], v[16:19], v[36:39]\nv_pk_mul_f16 v28, v62, v64 op_sel_hi:[0,1]\nv_add_f32_dpp v55, v55, v55 quad_perm:[1,0,3,2] row_mask:0xf bank_mask:0xf\n"
      "v_mfma_f32_16x16x32_f16 v[40:43], v[132:135], v[16:19], v[40:43]\nv_pk_mul_f16 v29, v62, v65 op_sel_hi:[0,1]\nv_add_f32_dpp v55, v55, v55 quad_perm:[2,3,0,1] row_mask:0xf bank_mask:0xf\n"
      "v_mfma_f32_16x16x32_f16 v[40:43], v[144:147], v[20:23], v[40:43]\nv_pk_mul_f16 v30, v62, v62 op_sel_hi:[0,1]\nv_add_f32_dpp v55, v55, v55 row_half_mirror row_mask:0xf bank_mask:0xf\n"
      "v_smfmac_f32_16x16x64_f16 v[44:47], v[148:151], v[16:23], v242\nv_pk_mul_f16 v31, v62, v57 op_sel_hi:[0,1]\nv_add_f32_dpp v55, v55, v55 row_mirror row_mask:0xf bank_mask:0xf\n"
      "v_mfma_f32_16x16x32_f16 v[32:35], v[152:155], v[24:27], v[32:35]\nv_pk_mul_f16 v16, v56, v62 op_sel_hi:[0,1]\nv_pk_mul_f16 v17, v56, v63 op_sel_hi:[0,1]\nds_write_b32 v243, v55 offset:1152\n"
      "v_mfma_f32_16x16x32_f16 v[32:35], v[164:167], v[28:31], v[32:35]\nv_pk_mul_f16 v18, v58, v62 op_sel_hi:[0,1]\nv_mfma_f32_16x16x32_f16 v[36:39], v[168:171], v[28:31], v[36:39]\n"
      "v_pk_mul_f16 v19, v58, v63 op_sel_hi:[0,1]\ns_setprio 3\nv_mfma_f32_16x16x32_f16 v[36:39], v[156:159], v[24:27], v[36:39]\nv_pk_mul_f16 v20, v60, v62 op_sel_hi:[0,1]\n"
      "s_ff0_i32_b32 s53, s44\nv_mfma_f32_16x16x32_f16 v[40:43], v[160:163], v[24:27], v[40:43]\nv_pk_mul_f16 v21, v60, v63 op_sel_hi:[0,1]\n"
      "v_mfma_f32_16x16x32_f16 v[40:43], v[172:175], v[28:31], v[40:43]\nv_pk_mul_f16 v22, v60, v60 op_sel_hi:[0,1]\ns_add_u32 s53, s53, s64\n"
      "v_smfmac_f32_16x16x64_f16 v[44:47], v[176:179], v[24:31], v242\nv_pk_mul_f16 v23, v60, v57 op_sel_hi:[0,1]\nv_mfma_f32_16x16x32_f16 v[32:35], v[180:183], v[16:19], v[32:35]\n"
      "v_pk_mul_f16 v24, v56, v60 op_sel_hi:[0,1]\nv_pk_mul_f16 v25, v56, v61 op_sel_hi:[0,1]\nv_mfma_f32_16x16x32_f16 v[32:35], v[192:195], v[20:23], v[32:35]\n"
      "v_pk_mul_f16 v26, v58, v60 op_sel_hi:[0,1]\nv_pk_mul_f16 v27, v58, v61 op_sel_hi:[0,1]\nv_mfma_f32_16x16x32_f16 v[36:39], v[196:199], v[20:23], v[36:39]\n"
      "v_pk_mul_f16 v28, v58, v58 op_sel_hi:[0,1]\nv_mfma_f32_16x16x32_f16 v[36:39], v[184:187], v[16:19], v[36:39]\nv_pk_mul_f16 v29, v58, v57 op_sel_hi:[0,1]\n"
      "v_mfma_f32_16x16x32_f16 v[40:43], v[188:191], v[16:19], v[40:43]\nv_pk_mul_f16 v30, v56, v58 op_sel_hi:[0,1]\nv_mfma_f32_16x16x32_f16 v[40:43], v[200:203], v[20:23], v[40:43]\n"
      "v_pk_mul_f16 v31, v56, v59 op_sel_hi:[0,1]\nv_smfmac_f32_16x16x64_f16 v[44:47], v[204:207], v[16:23], v242\ns_nop 0\nv_smfmac_f32_16x16x64_f16 v[44:47], v[232:235], v[24:31], v242\ns_nop 1\n"
      "v_mfma_f32_16x16x32_f16 v[32:35], v[208:211], v[24:27], v[32:35]\ns_nop 1\nv_mfma_f32_16x16x32_f16 v[32:35], v[220:223], v[28:31], v[32:35]\ns_nop 1\n"
      "v_mfma_f32_16x16x32_f16 v[36:39], v[224:227], v[28:31], v[36:39]\nv_add_f32 v44, v44, v45\nv_add_f32 v46, v46, v47\nv_mfma_f32_16x16x32_f16 v[36:39], v[212:215], v[24:27], v[36:39]\n"
      "v_mul_f32 v55, v44, v54\nv_mul_f32 v55, v55, v46\nds_read_b128 v[44:47], v248 offset:48\nv_mfma_f32_16x16x32_f16 v[40:43], v[216:219], v[24:27], v[40:43]\ns_nop 2\n"
      "v_mfma_f32_16x16x32_f16 v[40:43], v[228:231], v[28:31], v[40:43]\nv_mul_f32 v32, v32, v48\nv_mul_f32 v33, v33, v49\nv_mul_f32 v34, v34, v50\n"
      ".Lc_slow:\ns_sub_u32 s49, s45, s64\ns_lshr_b32 s50, s44, 16\ns_or_b32 s50, s50, s56\ns_ashr_i32 s50, s50, s49\ns_cmp_lg_u32 s50, -1\ns_cbranch_scc1 .Lc_more\n"
      ".Lc_fin:\ns_sub_u32 s50, s45, s33\ns_bitcmp1_b32 s50, 1\ns_cselect_b64 vcc, -1, 0\ns_lshl_b32 s50, s50, 6\ns_sub_u32 s50, s50, 128\nv_fmac_f32 v55, v32, v36\nv_mul_f32 v35, v35, v51\n"
      "v_fmac_f32 v55, v33, v37\nv_fmac_f32 v55, v34, v38\nv_fmac_f32 v55, v35, v39\nv_cndmask_b32 v40, v244, v40, vcc\nv_cndmask_b32 v41, v245, v41, vcc\nv_cndmask_b32 v42, v246, v42, vcc\n"
      "v_cndmask_b32 v43, v247, v43, vcc\nv_mul_f32 v40, v40, v52\nv_mul_f32 v41, v41, v53\nv_fmac_f32 v55, v40, v42\nv_fmac_f32 v55, v41, v43\nv_add_u32 v243, s50, v243\ns_nop 0\n"
      "v_add_f32_dpp v55, v55, v55 quad_perm:[1,0,3,2] row_mask:0xf bank_mask:0xf\ns_nop 1\nv_add_f32_dpp v55, v55, v55 quad_perm:[2,3,0,1] row_mask:0xf bank_mask:0xf\ns_nop 1\n"
      "v_add_f32_dpp v55, v55, v55 row_half_mirror row_mask:0xf bank_mask:0xf\ns_nop 1\nv_add_f32_dpp v55, v55, v55 row_mirror row_mask:0xf bank_mask:0xf\nds_write_b32 v243, v55\n"
      "s_cmp_gt_u32 s24, 1\ns_waitcnt lgkmcnt(0)\ns_barrier\ns_cbranch_scc1 .Lc_end\nds_read_b128 v[4:7], v10\nds_read_b32 v3, v11\nds_read_b32 v2, v248\nds_read_b64 v[8:9], v13\n"
      "s_mov_b32 s42, 0x11111111\ns_cmp_eq_u32 s24, 0\ns_cselect_b32 s43, s42, 0\ns_cselect_b32 s42, s42, 0x11111\ns_waitcnt lgkmcnt(2)\nv_add_f32 v4, v4, v5\nv_add_f32 v6, v6, v7\n"
      "v_add_f32 v4, v4, v6\nv_ldexp_f32 v4, v4, v3\ns_waitcnt lgkmcnt(1)\nv_and_b32 v2, 0xff, v2\nv_add_f32_dpp v4, v4, v4 quad_perm:[1,0,3,2] row_mask:0xf bank_mask:0xf\n"
      "v_lshrrev_b32 v2, v14, v2\nv_or_b32 v2, v2, v15\nv_add_f32_dpp v4, v4, v4 quad_perm:[2,3,0,1] row_mask:0xf bank_mask:0xf\nv_and_b32 v2, 1, v2\nv_cmp_eq_u32 vcc, 1, v2\ns_waitcnt lgkmcnt(0)\n"
      "v_add_f32 v4, v4, v9\nv_and_b32 v8, 0x1ffe, v8\nv_cvt_pk_bf16_f32 v4, v4, v4\ns_and_b64 exec, vcc, s[42:43]\ns_cbranch_execz .Lc_end\nglobal_store_short v8, v4, s[22:23]\ns_branch .Lc_end\n"
      ".Lc_s2:\ns_getpc_b64 s[60:61]\ns_branch .Lc_slow\ns_branch .Lc_u3\n"
      ".Lc_s3:\ns_getpc_b64 s[60:61]\ns_branch .Lc_slow\ns_branch .Lc_u4\n"
      ".Lc_s4:\ns_getpc_b64 s[60:61]\ns_branch .Lc_slow\ns_branch .Lc_u5\n"
      ".Lc_s5:\ns_getpc_b64 s[60:61]\ns_branch .Lc_slow\ns_branch .Lc_u6\n"
      ".Lc_s6:\ns_getpc_b64 s[60:61]\ns_branch .Lc_slow\ns_branch .Lc_u7\n"
      ".Lc_s7:\ns_getpc_b64 s[60:61]\ns_branch .Lc_slow\ns_branch .Lc_u8\n"
      ".Lc_s8:\ns_getpc_b64 s[60:61]\ns_branch .Lc_slow\ns_branch .Lc_u9\n"
      ".Lc_s9:\ns_getpc_b64 s[60:61]\ns_branch .Lc_slow\ns_branch .Lc_u10\n"
      ".Lc_t0:\ns_swappc_b64 s[4:5], s[34:35]\ns_branch .Lc_b0\n"
      ".Lc_t1:\ns_swappc_b64 s[4:5], s[34:35]\ns_branch .Lc_b1\n"
      ".Lc_t2:\ns_swappc_b64 s[4:5], s[34:35]\ns_branch .Lc_b2\n"
      ".Lc_t3:\ns_swappc_b64 s[4:5], s[34:35]\ns_branch .Lc_b3\n"
      ".Lc_t4:\ns_swappc_b64 s[4:5], s[34:35]\ns_branch .Lc_b4\n"
      ".Lc_t5:\ns_swappc_b64 s[4:5], s[34:35]\ns_branch .Lc_b5\n"
      ".Lc_t6:\ns_swappc_b64 s[4:5], s[34:35]\ns_branch .Lc_b6\n"
      ".Lc_t7:\ns_swappc_b64 s[4:5], s[34:35]\ns_branch .Lc_b7\n"
      ".Lc_t8:\ns_swappc_b64 s[4:5], s[34:35]\ns_branch .Lc_b8\n"
      ".Lc_t9:\ns_swappc_b64 s[4:5], s[34:35]\ns_branch .Lc_b9\n"
      ".Lc_t10:\ns_swappc_b64 s[4:5], s[34:35]\ns_branch .Lc_b10\n"
      ".Lc_tg:\ns_swappc_b64 s[4:5], s[34:35]\ns_branch .Lc_bg\n"
      ".Lc_th:\ns_swappc_b64 s[4:5], s[34:35]\ns_branch .Lc_bh\n"
      ".Lc_more:\ns_mov_b32 s51, 0\ns_add_u32 s60, s60, 4\ns_addc_u32 s61, s61, 0\n"
      ".Lc_chk:\ns_sub_u32 s49, s45, s64\ns_sub_u32 s53, s68, s64\ns_lshl_b32 s53, -1, s53\ns_lshr_b32 s50, s44, 16\ns_or_b32 s50, s50, s53\ns_ashr_i32 s50, s50, s49\ns_cmp_eq_u32 s50, -1\n"
      "s_cbranch_scc0 .Lc_c1\ns_cmp_lg_u32 s56, 0\ns_cbranch_scc1 .Lc_fin\ns_branch .Lc_c2\n"
      ".Lc_c1:\ns_lshr_b32 s50, s44, s49\ns_and_b32 s50, s50, 0x10001\ns_cbranch_scc1 .Lc_have\n"
      ".Lc_c2:\ns_add_u32 s51, s51, 1\ns_bitcmp1_b32 s51, 22\ns_cbranch_scc1 .Lc_fin\ns_cmp_lg_u32 s56, 0\ns_cbranch_scc1 .Lc_rd\ns_swappc_b64 s[4:5], s[34:35]\n"
      ".Lc_rd:\nds_read_b32 v4, v248\ns_waitcnt lgkmcnt(0)\nv_readfirstlane_b32 s44, v4\ns_branch .Lc_chk\n"
      ".Lc_have:\ns_cmp_eq_u32 s51, 0\ns_cbranch_scc1 .Lc_go\ns_sub_u32 s49, s45, s33\ns_mulk_i32 s49, 928\nv_add_u32 v24, s49, v236\nv_add_u32 v25, s49, v237\nv_add_u32 v26, s49, v238\n"
      "v_add_u32 v27, s49, v239\nds_read_b64_tr_b16 v[68:69], v26 offset:0\nds_read_b64_tr_b16 v[70:71], v27 offset:0\nds_read_b64_tr_b16 v[56:57], v24 offset:0\n"
      "ds_read_b64_tr_b16 v[58:59], v25 offset:128\nds_read_b64_tr_b16 v[60:61], v25 offset:256\nds_read_b64_tr_b16 v[62:63], v25 offset:384\nds_read_b64_tr_b16 v[64:65], v25 offset:512\n"
      "ds_read_b64_tr_b16 v[66:67], v25 offset:640\ns_waitcnt lgkmcnt(0)\nv_readlane_b32 s48, v69, 48\nv_pk_mul_f16 v20, v68, v70\nv_pk_mul_f16 v21, v69, v71\n"
      "v_pk_mul_f16 v22, v56, v56 op_sel_hi:[0,1]\nv_pk_mul_f16 v23, v56, v57 op_sel_hi:[0,1]\nv_pk_mul_f16 v16, v68, s48 op_sel:[0,1]\nv_pk_mul_f16 v17, v69, s48 op_sel:[0,1]\n"
      "v_pk_mul_f16 v18, v70, s48 op_sel:[0,1]\nv_pk_mul_f16 v19, v71, s48 op_sel:[0,1]\n"
      ".Lc_go:\ns_setpc_b64 s[60:61]\n"
      ".Lt_g:\ns_waitcnt lgkmcnt(0)\nv_readfirstlane_b32 s42, v2\ns_mov_b32 s43, 43008\nv_lshl_add_u32 v1, v240, 2, s43\ns_cmp_lg_u32 s42, 3\ns_cbranch_scc1 .Lt_p\nds_read_b32 v3, v248 offset:764\n"
      "ds_read2st64_b32 v[6:7], v1 offset0:0 offset1:4\nds_read2st64_b32 v[8:9], v1 offset0:1 offset1:5\nds_read2st64_b32 v[10:11], v1 offset0:2 offset1:6\n"
      "ds_read2st64_b32 v[12:13], v1 offset0:3 offset1:7\ns_lshl_b32 s42, s28, 8\ns_add_u32 s42, s42, s29\ns_lshl_b32 s42, s42, 7\ns_add_u32 s58, s12, s42\ns_addc_u32 s59, s13, 0\n"
      "v_lshlrev_b32 v14, 3, v240\ns_cmp_eq_u32 s25, 2\ns_cselect_b64 vcc, -1, 0\ns_movk_i32 s42, 0xff00\nv_mov_b32 v1, s41\ns_lshl_b32 s43, s25, 3\ns_waitcnt lgkmcnt(0)\nv_add_f32 v12, v12, v13\n"
      "v_add_f32 v6, v6, v7\nv_add_f32 v8, v8, v9\nv_add_f32 v10, v10, v11\nv_mov_b32 v13, v12\nv_mov_b32 v15, v12\ns_nop 1\nv_permlane16_swap_b32 v13, v15\nv_add_f32 v13, v13, v15\n"
      "v_cndmask_b32 v12, v12, v13, vcc\nv_mul_f32 v6, v3, v6\nv_mul_f32 v8, v3, v8\nv_mul_f32 v10, v3, v10\nv_mul_f32 v12, v3, v12\nv_add_u32 v6, 0x80, v6\nv_and_or_b32 v6, v6, s42, v1\n"
      "v_mov_b32 v7, s40\nv_add_u32 v8, 0x80, v8\nv_and_or_b32 v8, v8, s42, v1\nv_mov_b32 v9, s40\nv_add_u32 v10, 0x80, v10\nv_and_or_b32 v10, v10, s42, v1\nv_mov_b32 v11, s40\n"
      "v_add_u32 v12, 0x80, v12\nv_and_or_b32 v12, v12, s42, v1\nv_mov_b32 v13, s40\nglobal_store_dwordx2 v14, v[6:7], s[58:59] offset:0 sc1\n"
      "global_store_dwordx2 v14, v[8:9], s[58:59] offset:512 sc1\nglobal_store_dwordx2 v14, v[10:11], s[58:59] offset:1024 sc1\ns_cmp_eq_u32 s43, 0\ns_cbranch_scc1 .Lt_p\ns_bfm_b64 exec, s43, 0\n"
      "global_store_dwordx2 v14, v[12:13], s[58:59] offset:1536 sc1\ns_mov_b64 exec, -1\n"
      ".Lt_p:\ns_cmp_gt_u32 s66, 100\ns_cbranch_scc1 .Lt_d\ns_mov_b32 s46, s66\ns_swappc_b64 s[34:35], s[4:5]\ns_lshl_b32 s43, 0x10000, s67\ns_cmp_gt_u32 s44, 0xffff\ns_cbranch_scc1 .Lt_a\n"
      "v_lshlrev_b32 v1, 3, v240\ns_mov_b64 exec, 1\nglobal_atomic_add v5, v1, v241, s[8:9] sc0 sc1\ns_mov_b64 exec, -1\nglobal_load_dwordx2 v[254:255], v249, s[12:13] sc1\n"
      ".Lt_f:\nglobal_load_dwordx2 v[2:3], v1, s[14:15] sc1\ns_swappc_b64 s[34:35], s[4:5]\ns_waitcnt vmcnt(0)\nv_readfirstlane_b32 s50, v5\nv_cmp_eq_u32 vcc, s41, v2\n"
      "v_cmp_eq_u32 s[62:63], s40, v3\nv_mov_b32 v7, s43\ns_cmp_lt_u32 s50, s55\ns_cbranch_scc1 .Lt_r\nds_add_u32 v253, v7\ns_branch .Lt_c\n"
      ".Lt_r:\ns_add_u32 s50, s50, 1\nv_readlane_b32 s54, v251, s50\ns_and_b64 s[62:63], s[62:63], vcc\ns_bfe_u32 s50, s54, 0x50004\ns_lshr_b32 s49, s54, 9\ns_lshl3_add_u32 s50, s50, s49\n"
      "s_sub_u32 s50, s50, s69\ns_bitcmp0_b64 s[62:63], s50\ns_cbranch_scc1 .Lt_f\ns_mul_i32 s50, s54, 3584\ns_add_u32 s62, s16, s50\ns_addc_u32 s63, s17, 0\ns_add_u32 s50, s64, s67\n"
      "s_mul_i32 s49, s50, 928\ns_add_u32 m0, s49, 45056\ns_mov_b32 exec_hi, 0x7fffff\nglobal_load_lds_dwordx4 v252, s[62:63] offset:0 sc1\ns_add_u32 m0, m0, 21376\ns_lshl_b32 s54, s54, 3\n"
      "global_load_lds_dwordx4 v252, s[62:63] offset:896 sc1\ns_add_u32 m0, m0, 21376\ns_lshl3_add_u32 s50, s50, 160256\nglobal_load_lds_dwordx4 v252, s[62:63] offset:1792 sc1\n"
      "s_add_u32 m0, m0, 21376\ns_lshl_b32 s43, 1, s67\nglobal_load_lds_dwordx4 v252, s[62:63] offset:2688 sc1\ns_mov_b32 exec_hi, -1\ns_mov_b32 exec_lo, 0\nv_and_b32 v249, 0xffff8000, v249\n"
      "v_or_b32 v249, s54, v249\nv_mov_b32 v250, s50\ns_mov_b32 exec_lo, -1\ns_swappc_b64 s[34:35], s[4:5]\ns_waitcnt vmcnt(0)\n"
      ".Lt_a:\nv_mov_b32 v0, s43\nds_add_u32 v253, v0\n"
      ".Lt_d:\ns_sub_u32 s46, s64, 4\ns_cmp_ge_i32 s45, s46\ns_cbranch_scc1 .Lt_l\ns_swappc_b64 s[34:35], s[4:5]\n"
      ".Lt_l:\nglobal_load_dwordx2 v[254:255], v249, s[12:13] sc1\ns_swappc_b64 s[34:35], s[4:5]\ns_waitcnt vmcnt(0)\n"
      ".Lt_c:\nv_and_b32 v6, 0xff, v254\nv_cmp_ne_u32 vcc, s40, v255\nv_cmp_ne_u32 s[62:63], s41, v6\nv_and_b32 v9, 0xffffff00, v254\ns_or_b64 vcc, vcc, s[62:63]\ns_cbranch_vccnz .Lt_d\n"
      "s_mov_b32 vcc_lo, 0x1fff1fff\ns_mov_b32 vcc_hi, vcc_lo\nv_bfe_u32 v8, v249, 2, 13\nv_cndmask_b32 v9, 0, v9, vcc\ns_sub_u32 s56, s68, s64\ns_lshl_b32 s56, -1, s56\n"
      "v_add_f32_dpp v9, v9, v9 quad_perm:[1,0,3,2] row_mask:0xf bank_mask:0xf\ns_movk_i32 s46, 99\ns_nop 0\nv_add_f32_dpp v9, v9, v9 quad_perm:[2,3,0,1] row_mask:0xf bank_mask:0xf\ns_nop 1\n"
      "v_add_f32_dpp v9, v9, v9 row_half_mirror row_mask:0xf bank_mask:0xf\ns_nop 1\nv_add_f32_dpp v9, v9, v9 row_mirror row_mask:0xf bank_mask:0xf\nv_add_f32 v9, s38, v9\n"
      "ds_write_b64 v250, v[8:9]\ns_setpc_b64 s[4:5]\n"
      ".Lc_end:\n"
      ".Lxd_end:"
      :: "{v240}"(lane), "{v253}"(tch), "{v[248:251]}"(mrow), "{v252}"(msc), "{v[254:255]}"(rsp), "{s[4:19]}"(ka0), "{s[20:23]}"(ka1), "{s[24:31]}"(gs), "{s2}"(blk)
      : "memory", "vcc", "scc", "s33", "s34", "s35", "s36", "s37", "s38", "s39", "s40", "s41", "s42", "s43", "s44", "s45", "s46", "s47", "s48", "s49", "s50", "s51", "s52", "s53", "s54", "s55", "s56", "s57", "s58", "s59", "s60", "s61", "s62", "s63", "s64", "s65", "s66", "s67", "s68", "s69",
        "v0", "v1", "v2", "v3", "v4", "v5", "v6", "v7", "v8", "v9", "v10", "v11", "v12", "v13", "v14", "v15", "v16", "v17", "v18", "v19", "v20", "v21", "v22", "v23", "v24", "v25", "v26", "v27",
        "v28", "v29", "v30", "v31", "v32", "v33", "v34", "v35", "v36", "v37", "v38", "v39", "v40", "v41", "v42", "v43", "v44", "v45", "v46", "v47", "v48", "v49", "v50", "v51", "v52", "v53", "v54",
        "v55", "v56", "v57", "v58", "v59", "v60", "v61", "v62", "v63", "v64", "v65", "v66", "v67", "v68", "v69", "v70", "v71", "v72", "v73", "v74", "v75", "v76", "v77", "v78", "v79", "v80", "v81",
        "v82", "v83", "v84", "v85", "v86", "v87", "v88", "v89", "v90", "v91", "v92", "v93", "v94", "v95", "v96", "v97", "v98", "v99", "v100", "v101", "v102", "v103", "v104", "v105", "v106", "v107",
        "v108", "v109", "v110", "v111", "v112", "v113", "v114", "v115", "v116", "v117", "v118", "v119", "v120", "v121", "v122", "v123", "v124", "v125", "v126", "v127", "v128", "v129", "v130",
        "v131", "v132", "v133", "v134", "v135", "v136", "v137", "v138", "v139", "v140", "v141", "v142", "v143", "v144", "v145", "v146", "v147", "v148", "v149", "v150", "v151", "v152", "v153",
        "v154", "v155", "v156", "v157", "v158", "v159", "v160", "v161", "v162", "v163", "v164", "v165", "v166", "v167", "v168", "v169", "v170", "v171", "v172", "v173", "v174", "v175", "v176",
        "v177", "v178", "v179", "v180", "v181", "v182", "v183", "v184", "v185", "v186", "v187", "v188", "v189", "v190", "v191", "v192", "v193", "v194", "v195", "v196", "v197", "v198", "v199",
        "v200", "v201", "v202", "v203", "v204", "v205", "v206", "v207", "v208", "v209", "v210", "v211", "v212", "v213", "v214", "v215", "v216", "v217", "v218", "v219", "v220", "v221", "v222",
        "v223", "v224", "v225", "v226", "v227", "v228", "v229", "v230", "v231", "v232", "v233", "v234", "v235", "v236", "v237", "v238", "v239", "v241", "v242", "v243", "v244", "v245", "v246", "v247");
}

extern "C" void kernel_launch(void* const* d_in, const int* in_sizes, int n_in, void* d_out, int out_size,
                              void* d_ws, size_t ws_size, hipStream_t stream) {
  const int* idx = (const int*)d_in[0];
  const u16* table = (const u16*)d_in[1];
  const u16* ws_cin = (const u16*)d_in[2];
  const u16* wout = (const u16*)d_in[3];
  const u16* w1 = (const u16*)d_in[4];
  const u16* b1 = (const u16*)d_in[5];
  const u16* w2 = (const u16*)d_in[6];
  const u16* b_out = (const u16*)d_in[7];
  dnn_kernel<<<256, 512, 0, stream>>>(idx, table, ws_cin, w1, b1, w2, wout, b_out, (char*)d_ws, (u16*)d_out);
}
```

```cpp
#include <hip/hip_runtime.h>
#include <hip/hip_bf16.h>
#include <stdint.h>
#include <utility>

typedef __attribute__((ext_vector_type(8))) short bf16x8;
typedef __attribute__((ext_vector_type(4))) short s16x4;
typedef __attribute__((ext_vector_type(4))) float f32x4;
typedef __attribute__((ext_vector_type(2))) float f32x2;
typedef __attribute__((ext_vector_type(2))) __bf16 bf16x2;
typedef __attribute__((ext_vector_type(16))) float f32x16;
typedef unsigned short u16;
typedef __attribute__((ext_vector_type(2))) unsigned u32x2;
typedef __attribute__((ext_vector_type(4))) unsigned u32x4;

constexpr int NB = 4096, NF = 26, ND = 64, DIN = 1664;

#define STAMP(k,i)
#define WSTAMP(i)
template <class F, int... I>
__device__ __forceinline__ void static_for(F& f, std::integer_sequence<int, I...>) { (f(std::integral_constant<int, I>{}), ...); }
template <int CTRL, int RM = 0xf>
__device__ __forceinline__ float dpp(float v) { return __builtin_bit_cast(float, __builtin_amdgcn_update_dpp(0, __builtin_bit_cast(int, v), CTRL, RM, 0xf, false)); }
__device__ __forceinline__ unsigned pk2(float a, float b) { return __builtin_bit_cast(unsigned, __builtin_convertvector((f32x2){a, b}, bf16x2)); }

template <int V> using IC = std::integral_constant<int, V>;
typedef __attribute__((ext_vector_type(8))) unsigned u32x8;
typedef __attribute__((ext_vector_type(16))) unsigned u32x16;
constexpr int STAGE = 224 * 128 + 64 * 256, G_LDS = 163840;
constexpr int MC = 41 * 1024;
constexpr int W3S = 0, U_OFF = W3S + 35840, NC_OFF = U_OFF + 2048, GT_OFF = NC_OFF + 1024, WO_OFF = GT_OFF + 2048, DP = 4352, W2S = 2 * STAGE;
static_assert(WO_OFF + 256 <= STAGE, "lds");
constexpr int WS_RESP = 4096, WS_POOL = 32768, WS_M = 1 << 20;
#define NCLV 8
constexpr int NSLOT = 21, NCL = NCLV;

struct Geo { unsigned nt, s0, ns; };
constexpr __host__ __device__ Geo geo(unsigned e) {
  const unsigned i = (e * 79) >> 10, nt = e - 13 * i, c = nt < 9 ? 13108 : 13798, t = i * c + ((nt * 247) & 1023);
  return Geo{nt, t >> 10, ((t + c) >> 10) - (t >> 10)};
}
constexpr unsigned long long builders() {
  unsigned long long k = 0;
  for (unsigned x = 0; x < 8; ++x)
    for (unsigned s = 32; s-- > 0;)
      if (geo(32 * x + s).ns == 12) k = (k & ~(31ull << (5 * x))) | (unsigned long long)s << (5 * x);
  return k;
}
constexpr unsigned long long BK = builders();
#define KEEP 10, 13, 15, 11, 8
constexpr int KP[5] = {KEEP};
constexpr int WANT[5] = {138, 163, 177, 135, 113};
constexpr int kind(unsigned b) { return ((BK >> (5 * (b & 7))) & 31) == b >> 3 ? 3 + ((b & 7) >> 2) : 14 - geo((b & 7) * 32 + (b >> 3)).ns; }
constexpr __host__ __device__ unsigned group(unsigned b) { return ((b >> 3) + 2 * b) & 15; }
constexpr bool geo_ok() {
  for (unsigned nt = 0; nt < 13; ++nt) {
    unsigned s = 0;
    for (unsigned e = nt; e < 256; e += 13) {
      const Geo g = geo(e);
      if (g.nt != nt || g.s0 != s || g.ns < 12 || g.ns > 14 || e < g.s0 || e >= g.s0 + g.ns) return false;
      s += g.ns;
    }
    if (s != 256) return false;
  }
  return true;
}
static_assert(geo_ok(), "tiles");

struct PI { int a, b; };
constexpr PI prod(int n) {
  int m = n - 2;
  for (int jj = 0; jj < 6; ++jj) {
    const int j = jj ? 6 - jj : 0;
    if (m < 2 + 2 * j) return m == 0 ? PI{2 * j, 2 * j} : m == 1 ? PI{2 * j, 1} : PI{(m - 2) & ~1, 2 * j + (m & 1)};
    m -= 2 + 2 * j;
  }
  return PI{0, 0};
}
constexpr int xfield(int k, int h, int g) { return k == 1 ? 24 + h : 4 * (k >> 1) + ((g + 2 * (k & 1) + h) & 3); }
constexpr int pqfield(int q, int sl, int g) { return g < 3 ? 8 * g + 4 * (sl >> 1) + (sl & 1) + (((g ^ q) & 1) << 1) : sl == 3 ? 26 : 24 + ((sl + q) >> 1); }
constexpr unsigned short slot_code(int f, int t, int g, int h) {
  if (f == 0) {
    const int sl = 2 * (t & 1) + h, q = t >> 1;
    return g == 3 && (q || (sl & 1)) ? 0x8000 : 0x4000 | 6 << 10 | (26 * 33 + pqfield(q, sl, g));
  }
  const int n = 4 * (f - 1) + t;
  const int p = n < 2 ? pqfield(0, 2 * n + h, g) : xfield(prod(n).a, 0, g), q = n < 2 ? pqfield(1, 2 * n + h, g) : xfield(prod(n).b, h, g);
  return (p == 26 ? 0x4000 | 10 << 10 : p == q ? 1 << 10 : 2 << 10) | (p * 33 + q);
}
struct GT { unsigned short t[2][64][8], pool[16][64]; };
constexpr GT make_gt() {
  GT r{};
  for (int l = 0; l < 64; ++l)
    for (int e = 0; e < 8; ++e) {
      r.t[0][l][e] = l < 48 ? slot_code(l >> 2, e >> 1, l & 3, e & 1) : 0x8000;
      r.t[1][l][e] = l < 48 ? slot_code(2 * (l >> 3) + ((l >> 1) & 1), 2 * ((e >> 1) & 1) + ((l >> 2) & 1), 2 * (l & 1) + (e >> 2), e & 1) : 0x8000;
    }
  int tot = 0, dem = 0, acc = 0, k = 15, b = 0;
  for (int i = 0; i < 256; ++i) { tot += 16 - KP[kind(i)]; dem += WANT[kind(i)] - 10 * KP[kind(i)]; }
  for (int q = 0, n = 0; q < 16; ++q) {
    for (int i = 0; i < 256; ++i) acc += group(i) == q ? WANT[kind(i)] - 10 * KP[kind(i)] : 0;
    const int len = (acc * tot + dem / 2) / dem - n;
    n += len;
    r.pool[q][0] = len;
    for (int i = 1; i <= len; ++i) {
      while (k < KP[kind(b)]) { ++b; k = 15; }
      r.pool[q][i] = ((b & 7) * 32 + (b >> 3)) * 16 + k--;
    }
  }
  return r;
}
constexpr bool pool_ok() {
  const GT t = make_gt();
  for (int q = 0; q < 16; ++q) {
    int n = 0;
    for (int i = 0; i < 256; ++i) n += group(i) != q ? 0 : NSLOT - KP[kind(i)] < NCL ? NSLOT - KP[kind(i)] : NCL;
    if (t.pool[q][0] > 62 || t.pool[q][0] > n) return false;
    const int fb = 16 * q < 24 ? 0 : 16 * q - 24 > 192 ? 192 : 16 * q - 24;
    for (int i = 1; i <= t.pool[q][0]; ++i) {
      const int b = ((t.pool[q][i] >> 4) & 31) * 8 + (t.pool[q][i] >> 9);
      if (b < fb || b >= fb + 64) return false;
    }
  }
  for (int k = 0; k < 5; ++k) if (KP[k] < 8 || KP[k] > 16) return false;
  return true;
}
static_assert(pool_ok(), "pools");
static __device__ const __attribute__((aligned(16))) GT g_tab = make_gt();

__global__ __launch_bounds__(512, 2) void dnn_kernel(const int* __restrict__ idx, const u16* __restrict__ table,
                                                     const u16* __restrict__ ws_cin, const u16* __restrict__ w1,
                                                     const u16* __restrict__ b1, const u16* __restrict__ w2,
                                                     const u16* __restrict__ wout, const u16* __restrict__ b_out,
                                                     char* __restrict__ ws, u16* __restrict__ out) {
  __shared__ __attribute__((aligned(1024))) char shm[G_LDS];
  u32x4 ka1;
  u32x8 gs;
  u32x16 ka0;
  unsigned tch, blk = blockIdx.x;
  int lane;
  asm volatile(
      "s_load_dwordx16 s[4:19], %[ka], 0\ns_load_dwordx4 s[20:23], %[ka], 64\nv_readfirstlane_b32 s24, %[tid]\ns_lshr_b32 s37, s2, 3\ns_cmp_lt_u32 s24, 448\ns_cbranch_scc1 .Lf3\n"
      "s_getpc_b64 s[42:43]\n"
      ".Lxd_pc0:\nv_and_b32 v245, 15, %[tid]\nv_lshl_add_u32 v245, s37, 4, v245\nv_lshlrev_b32 v245, 7, v245\nv_min_u32 v245, .Lxd_end-.Lxd_pc0-4, v245\nglobal_load_dword v253, v245, s[42:43]\n"
      ".Lf3:\ns_and_b32 s36, s2, 7\ns_lshl_b32 s30, s36, 5\ns_add_u32 s30, s30, s37\ns_lshr_b32 s24, s24, 6\ns_mul_i32 s38, s30, 79\ns_lshr_b32 s38, s38, 10\ns_mul_i32 s28, s38, 13\n"
      "s_sub_u32 s28, s30, s28\nv_and_b32 v240, 63, %[tid]\ns_lshr_b32 s27, s36, 1\ns_add_u32 s27, s27, s36\ns_and_b32 s39, s28, 3\ns_mul_i32 s39, s39, 3\ns_add_u32 s27, s27, s39\n"
      "s_sub_u32 s39, s27, 12\ns_min_u32 s27, s27, s39\ns_lshl_b32 s27, s27, 1\ns_cmp_ge_u32 s24, 4\ns_cbranch_scc1 .Lf1\ns_movk_i32 s39, 13798\nv_mul_u32_u24 v245, 9363, v240\n"
      "s_cmp_lt_u32 s28, 9\ns_cselect_b32 s39, 13108, s39\ns_or_b32 s41, s27, 2\ns_mul_i32 s40, s28, 247\nv_lshrrev_b32 v245, 16, v245\ns_and_b32 s40, s40, 1023\n"
      "v_mad_i32_i24 v246, v245, -7, v240\ns_mul_i32 s38, s38, s39\nv_lshl_add_u32 v246, v246, 2, s41\ns_add_u32 s38, s38, s40\nv_subrev_u32 v247, 26, v246\ns_lshr_b32 s29, s38, 10\n"
      "v_mul_u32_u24 v245, 26, v245\ns_add_u32 s38, s38, s39\nv_min_u32 v246, v246, v247\ns_lshr_b32 s38, s38, 10\nv_add_lshl_u32 v241, v246, v245, 2\ns_sub_u32 s38, s38, s29\n"
      "v_add_u32 v244, 192, v241\ns_lshl_b32 s38, s38, 1\nv_add_u32 v243, 128, v241\ns_sub_u32 s25, s38, 24\nv_add_u32 v242, 64, v241\ns_mul_i32 s38, s24, 6272\ns_add_u32 m0, s38, 139648\n"
      "s_lshl1_add_u32 s38, s29, s24\ns_mul_i32 s38, s38, 832\ns_mul_i32 s40, s24, 3328\ns_add_u32 s38, s38, 16640\ns_sub_u32 s40, 16896, s40\ns_mov_b32 exec_hi, 0xffffff\n"
      "v_add_u32 v245, 256, v241\nv_add_u32 v246, 2432, v241\ns_waitcnt lgkmcnt(0)\ns_add_u32 s38, s4, s38\ns_addc_u32 s39, s5, 0\ns_cmp_ge_u32 s24, s25\nglobal_load_lds_dwordx4 v241, s[38:39]\n"
      "s_cbranch_scc1 .Li0\nglobal_load_lds_dwordx4 v246, s[38:39] offset:896\n"
      ".Li0:\ns_sub_u32 m0, m0, 4480\ns_sub_u32 s38, s38, s40\ns_subb_u32 s39, s39, 0\nglobal_load_lds_dwordx4 v245, s[38:39] offset:0\nglobal_load_lds_dwordx4 v244, s[38:39] offset:896\n"
      "global_load_lds_dwordx4 v243, s[38:39] offset:1792\nglobal_load_lds_dwordx4 v242, s[38:39] offset:2688\nglobal_load_lds_dwordx4 v241, s[38:39] offset:3584\ns_mov_b32 exec_hi, -1\n"
      "s_add_u32 s27, s27, 2\ns_mul_i32 s36, s36, 5\ns_mov_b32 s38, %[bkl]\ns_mov_b32 s39, %[bkh]\ns_lshr_b64 s[38:39], s[38:39], s36\ns_and_b32 s38, s38, 31\ns_xor_b32 s31, s38, s37\n"
      "s_cbranch_scc0 .Lbld\n"
      ".Lbp:\ns_mov_b32 s36, s24\ns_mul_i32 s42, s36, 6272\ns_cmp_ge_u32 s36, s25\ns_cselect_b64 vcc, 0, -1\ns_and_b32 s44, s27, 2\ns_xor_b32 s44, s44, 2\ns_lshl_b32 s44, s44, 2\n"
      "s_add_u32 s42, s42, s44\ns_add_u32 s42, s42, 135168\nv_lshrrev_b32 v22, 3, v240\nv_mul_u32_u24 v6, 112, v22\nv_add_u32 v6, s42, v6\nv_lshrrev_b32 v22, 4, v240\ns_lshl_b32 s42, s36, 2\n"
      "v_xor_b32 v22, v22, v240\nv_xor_b32 v22, s42, v22\nv_and_b32 v22, 7, v22\nv_lshlrev_b32 v4, 4, v22\nv_xor_b32 v5, 64, v4\ns_sub_u32 s44, s6, 1024\ns_subb_u32 s45, s7, 0\n"
      "s_sub_u32 s46, s6, 2048\ns_subb_u32 s47, s7, 0\ns_sub_u32 s48, s6, 3072\ns_subb_u32 s49, s7, 0\ns_mul_i32 s53, s36, 5120\ns_lshl_b32 s26, s36, 10\ns_lshl_b32 s54, s36, 12\n"
      "s_add_u32 s54, s54, 28672\nv_add_u32 v7, 1792, v6\nv_add_u32 v8, 3584, v6\nv_add_u32 v9, 5376, v6\ns_waitcnt vmcnt(0)\nds_read2_b64 v[196:199], v8 offset0:112 offset1:113\n"
      "ds_read2_b64 v[200:203], v9 offset0:0 offset1:1\nds_read2_b64 v[176:179], v6 offset0:0 offset1:1\nds_read2_b64 v[180:183], v6 offset0:112 offset1:113\n"
      "ds_read2_b64 v[184:187], v7 offset0:0 offset1:1\nds_read2_b64 v[188:191], v7 offset0:112 offset1:113\nds_read2_b64 v[192:195], v8 offset0:0 offset1:1\ns_waitcnt lgkmcnt(0)\n"
      "v_lshl_add_u32 v22, v176, 7, v4\nv_lshl_add_u32 v23, v180, 7, v5\nv_lshl_add_u32 v24, v184, 7, v4\nv_lshl_add_u32 v25, v188, 7, v5\nv_lshl_add_u32 v26, v192, 7, v4\n"
      "v_lshl_add_u32 v27, v196, 7, v4\nv_lshl_add_u32 v28, v200, 7, v4\ns_add_u32 m0, s26, 65536\ns_nop 0\nglobal_load_lds_dwordx4 v27, s[6:7] offset:0\nv_lshl_add_u32 v27, v197, 7, v4\n"
      "ds_read2_b64 v[196:199], v8 offset0:113 offset1:114\ns_cbranch_vccz .La1001\ns_add_u32 m0, s26, 69632\ns_nop 0\nglobal_load_lds_dwordx4 v28, s[6:7] offset:0\n"
      ".La1001:\nv_lshl_add_u32 v28, v201, 7, v4\ns_add_u32 m0, s53, 45056\ns_nop 0\nglobal_load_lds_dwordx4 v22, s[6:7] offset:0\nv_lshl_add_u32 v22, v177, 7, v4\n"
      "global_load_lds_dwordx4 v23, s[44:45] offset:1024\nv_lshl_add_u32 v23, v181, 7, v5\nds_read2_b64 v[180:183], v6 offset0:113 offset1:114\nglobal_load_lds_dwordx4 v24, s[46:47] offset:2048\n"
      "v_lshl_add_u32 v24, v185, 7, v4\nglobal_load_lds_dwordx4 v25, s[48:49] offset:3072\nv_lshl_add_u32 v25, v189, 7, v5\nds_read2_b64 v[188:191], v7 offset0:113 offset1:114\n"
      "s_add_u32 m0, s53, 49152\ns_nop 0\nglobal_load_lds_dwordx4 v26, s[6:7] offset:0\nv_lshl_add_u32 v26, v193, 7, v4\ns_waitcnt lgkmcnt(0)\ns_setprio 3\ns_barrier\ns_add_u32 m0, s26, 110592\n"
      "s_nop 0\nglobal_load_lds_dwordx4 v27, s[6:7] offset:0\nv_lshl_add_u32 v27, v196, 7, v4\ns_cbranch_vccz .La1002\ns_add_u32 m0, s26, 114688\ns_nop 0\n"
      "global_load_lds_dwordx4 v28, s[6:7] offset:0\n"
      ".La1002:\nv_lshl_add_u32 v28, v202, 7, v4\ns_add_u32 m0, s53, 90112\ns_nop 0\nglobal_load_lds_dwordx4 v22, s[6:7] offset:0\nv_lshl_add_u32 v22, v178, 7, v4\n"
      "global_load_lds_dwordx4 v23, s[44:45] offset:1024\nv_lshl_add_u32 v23, v180, 7, v5\nglobal_load_lds_dwordx4 v24, s[46:47] offset:2048\nv_lshl_add_u32 v24, v186, 7, v4\n"
      "global_load_lds_dwordx4 v25, s[48:49] offset:3072\nv_lshl_add_u32 v25, v188, 7, v5\ns_add_u32 m0, s53, 94208\ns_nop 0\nglobal_load_lds_dwordx4 v26, s[6:7] offset:0\n"
      "v_lshl_add_u32 v26, v194, 7, v4\nv_lshrrev_b32 v30, 2, v240\nv_bfe_u32 v31, v240, 1, 1\ns_lshl_b32 s42, s36, 1\nv_lshrrev_b32 v32, 3, v240\nv_or_b32 v31, s42, v31\ns_sub_u32 s33, s30, s29\n"
      "s_lshl_b32 s42, s33, 11\nv_xor_b32 v31, v31, v32\nv_lshl_add_u32 v30, v30, 7, s42\nv_and_b32 v32, 1, v240\nv_lshl_add_u32 v30, v31, 4, v30\nv_lshl_add_u32 v30, v32, 3, v30\n"
      "v_add_u32 v31, 90112, v30\nv_mov_b32 v32, 0\nv_mov_b32 v33, 0x3d800000\ns_mov_b32 s55, 0x80008000\ns_and_b32 s41, s24, 3\nv_lshrrev_b32 v245, 4, v240\nv_and_b32 v246, 15, v240\n"
      "s_mul_i32 s40, s41, 53248\nv_lshlrev_b32 v238, 1, v245\nv_mul_u32_u24 v245, 3328, v245\nv_xor_b32 v247, v246, v238\nv_add_u32 v245, s40, v245\nv_lshl_add_u32 v236, v247, 4, v245\n"
      "v_xor_b32 v238, 128, v236\nv_add_u32 v237, 12288, v236\nv_add_u32 v239, 36864, v238\nv_add_u32 v238, 24576, v238\ns_lshl_b32 s40, s28, 8\ns_add_u32 s10, s10, s40\ns_addc_u32 s11, s11, 0\n"
      "s_add_u32 s52, s27, 1\ns_mul_i32 s52, s52, 212992\ns_add_u32 s50, s10, s52\ns_addc_u32 s51, s11, 0\ns_add_u32 s50, s50, 212992\ns_addc_u32 s51, s51, 0\ns_cmp_eq_u32 s27, 24\n"
      "s_cselect_b64 s[50:51], s[10:11], s[50:51]\ns_lshl_b32 s42, s2, 2\ns_load_dword s37, s[20:21], s42\ns_load_dword s38, s[18:19], 0\ns_bfe_u32 s42, s2, 0x10002\ns_add_u32 s42, s42, 3\n"
      "s_lshr_b32 s43, s25, 1\ns_sub_u32 s43, 2, s43\ns_cmp_eq_u32 s31, 0\ns_cselect_b32 s42, s42, s43\ns_mul_i32 s42, s42, 5\ns_lshr_b32 s64, %[kp], s42\ns_and_b32 s64, s64, 31\ns_branch .Lfeed\n"
      ".Lf1:\ns_and_b32 s41, s24, 3\nv_lshrrev_b32 v245, 4, v240\nv_and_b32 v246, 15, v240\ns_mul_i32 s40, s41, 53248\nv_lshlrev_b32 v238, 1, v245\nv_mul_u32_u24 v245, 3328, v245\n"
      "v_xor_b32 v247, v246, v238\nv_add_u32 v245, s40, v245\nv_lshl_add_u32 v236, v247, 4, v245\nv_xor_b32 v238, 128, v236\nv_add_u32 v237, 12288, v236\nv_add_u32 v239, 36864, v238\n"
      "v_add_u32 v238, 24576, v238\ns_lshl_b32 s41, s41, 12\ns_mov_b32 m0, 160512\ns_lshl_b32 s40, s28, 8\ns_add_u32 s39, s27, 2\ns_mul_i32 s39, s39, 212992\ns_add_u32 s39, s39, s40\n"
      "v_lshl_add_u32 v244, v240, 4, s40\nv_lshl_add_u32 v243, v240, 2, s40\ns_mov_b64 exec, 0xffff\ns_cmp_lg_u32 s24, 5\ns_waitcnt lgkmcnt(0)\nglobal_load_lds_dwordx4 v244, s[12:13]\n"
      "s_mov_b64 exec, -1\ns_cbranch_scc1 .Lf4\nglobal_load_dword v253, v243, s[14:15]\n"
      ".Lf4:\ns_add_u32 m0, s41, 73728\ns_add_u32 s42, s10, s39\ns_addc_u32 s43, s11, 0\nglobal_load_lds_dwordx4 v236, s[42:43] offset:0\nglobal_load_lds_dwordx4 v237, s[42:43] offset:1024\n"
      "global_load_lds_dwordx4 v238, s[42:43] offset:2048\nglobal_load_lds_dwordx4 v239, s[42:43] offset:3072\ns_add_u32 m0, s41, 118784\ns_add_u32 s42, s42, 212992\ns_addc_u32 s43, s43, 0\n"
      "global_load_lds_dwordx4 v236, s[42:43] offset:0\nglobal_load_lds_dwordx4 v237, s[42:43] offset:1024\nglobal_load_lds_dwordx4 v238, s[42:43] offset:2048\n"
      "global_load_lds_dwordx4 v239, s[42:43] offset:3072\ns_movk_i32 s39, 13798\ns_cmp_lt_u32 s28, 9\ns_cselect_b32 s39, 13108, s39\ns_mul_i32 s40, s28, 247\ns_and_b32 s40, s40, 1023\n"
      "s_mul_i32 s38, s38, s39\ns_add_u32 s38, s38, s40\ns_lshr_b32 s29, s38, 10\ns_add_u32 s38, s38, s39\ns_lshr_b32 s38, s38, 10\ns_sub_u32 s38, s38, s29\ns_lshl_b32 s38, s38, 1\n"
      "s_sub_u32 s25, s38, 24\ns_add_u32 s27, s27, 2\ns_mul_i32 s36, s36, 5\ns_mov_b32 s38, %[bkl]\ns_mov_b32 s39, %[bkh]\ns_lshr_b64 s[38:39], s[38:39], s36\ns_and_b32 s38, s38, 31\n"
      "s_xor_b32 s31, s38, s37\ns_cbranch_scc0 .Lbld\n"
      ".Lbg:\ns_and_b32 s36, s24, 3\ns_lshl_b32 s42, s2, 2\ns_load_dword s37, s[20:21], s42\ns_load_dword s38, s[18:19], 0\ns_bfe_u32 s42, s2, 0x10002\ns_add_u32 s42, s42, 3\n"
      "s_lshr_b32 s43, s25, 1\ns_sub_u32 s43, 2, s43\ns_cmp_eq_u32 s31, 0\ns_cselect_b32 s42, s42, s43\ns_mul_i32 s42, s42, 5\ns_lshr_b32 s64, %[kp], s42\ns_and_b32 s64, s64, 31\ns_setprio 2\n"
      "s_and_b32 s56, s36, 1\ns_lshr_b32 s57, s36, 1\nv_lshrrev_b32 v1, 4, v240\nv_and_b32 v2, 15, v240\nv_lshlrev_b32 v3, 2, v2\nv_add_u32 v3, 162048, v3\nv_mov_b32 v4, 0\nds_write_b32 v3, v4\n"
      "s_mul_i32 s42, s56, 10240\nv_lshrrev_b32 v3, 1, v2\nv_xor_b32 v3, v3, v1\nv_lshl_add_u32 v4, v2, 7, s42\nv_lshl_add_u32 v96, v3, 4, v4\nv_xor_b32 v97, 64, v96\nv_add_u32 v98, 90112, v96\n"
      "v_add_u32 v99, 90112, v97\ns_cmp_eq_u32 s25, 4\ns_cselect_b32 s42, 4, 5\ns_mul_i32 s42, s42, s56\ns_sub_u32 s42, 12, s42\ns_lshl_b32 s42, s42, 11\ns_lshl_b32 s43, s56, 13\n"
      "s_sub_u32 s43, 20480, s43\nv_add_u32 v100, s42, v96\nv_add_u32 v120, s43, v96\nv_add_u32 v101, s42, v97\nv_add_u32 v121, s43, v97\nv_add_u32 v102, s42, v98\nv_add_u32 v122, s43, v98\n"
      "v_add_u32 v103, s42, v99\nv_add_u32 v123, s43, v99\nv_lshrrev_b32 v3, 2, v2\nv_lshl_add_u32 v4, v1, 3, v3\nv_and_b32 v5, 1, v1\nv_lshl_add_u32 v3, v5, 2, v3\ns_lshl_b32 s42, s36, 1\n"
      "v_xor_b32 v3, s42, v3\nv_and_b32 v5, 3, v2\nv_lshlrev_b32 v5, 3, v5\nv_lshl_add_u32 v4, v4, 8, v5\nv_lshl_add_u32 v4, v3, 5, v4\nv_add_u32 v104, 28672, v4\nv_xor_b32 v105, 32, v104\n"
      "v_xor_b32 v106, 64, v104\nv_xor_b32 v107, 96, v104\nv_add_u32 v108, 90112, v104\nv_add_u32 v109, 90112, v105\nv_add_u32 v110, 90112, v106\nv_add_u32 v111, 90112, v107\n"
      "s_lshl_b32 s42, s57, 10\ns_add_u32 s42, s42, 43008\ns_mul_i32 s43, s56, 320\ns_add_u32 s43, s43, s42\nv_lshl_add_u32 v254, v240, 2, s43\nv_cmp_gt_u32 vcc, 16, v240\ns_lshl_b32 s43, s56, 6\n"
      "s_add_u32 s42, s42, s43\ns_add_u32 s42, s42, 256\nv_min_u32 v5, 2, v1\nv_lshl_add_u32 v255, v2, 2, s42\nv_lshl_add_u32 v255, v5, 7, v255\nv_cndmask_b32 v255, v255, v254, vcc\n"
      "s_lshl_b32 s42, s36, 6\ns_add_u32 s42, s42, 160512\nv_lshl_add_u32 v3, v1, 3, s42\nv_xor_b32 v4, 64, v3\ns_lshl_b32 s42, s36, 7\ns_add_u32 s42, s42, 162816\nv_lshl_add_u32 v236, v1, 4, s42\n"
      "v_xor_b32 v237, 128, v236\ns_waitcnt lgkmcnt(0)\ns_mul_i32 s65, s36, 22272\ns_add_u32 s65, s65, 45056\ns_add_u32 s68, s64, 8\ns_min_u32 s68, s68, 21\ns_lshl_b32 s38, s38, 16\n"
      "s_lshr_b32 s42, s24, 2\ns_mul_i32 s43, s42, 3\ns_xor_b32 s43, s43, s36\ns_add_u32 s42, s42, s64\ns_add_u32 s42, s42, 9\ns_and_b32 s42, s42, 1\ns_lshl1_add_u32 s67, s43, s42\n"
      "s_add_u32 s66, s64, s67\ns_cmp_lt_u32 s66, s68\ns_cselect_b32 s66, s66, 127\ns_sub_u32 s66, s66, 8\ns_max_i32 s66, s66, 0\ns_getpc_b64 s[34:35]\n"
      ".Lk1063:\ns_add_u32 s34, s34, .Lt_g-.Lk1063\ns_addc_u32 s35, s35, 0\ns_lshr_b32 s42, s2, 3\ns_lshl1_add_u32 s42, s2, s42\ns_and_b32 s69, s42, 15\ns_lshl_b32 s43, s69, 7\n"
      "s_add_u32 s43, s43, 32768\ns_add_u32 s8, s20, s43\ns_addc_u32 s9, s21, 0\ns_add_u32 s12, s20, 65536\ns_addc_u32 s13, s21, 0\ns_add_u32 s16, s20, 2097152\ns_addc_u32 s17, s21, 0\n"
      "s_lshl_b32 s42, s69, 4\ns_max_u32 s42, s42, 24\ns_sub_u32 s42, s42, 24\ns_min_u32 s69, s42, 192\ns_lshl_b32 s42, s69, 3\ns_add_u32 s42, s42, 24576\ns_add_u32 s14, s20, s42\n"
      "s_addc_u32 s15, s21, 0\ns_mov_b32 s59, 0xc0135761\ns_mov_b32 s60, 0xc1e00000\ns_add_u32 s49, s65, 1856\ns_mov_b32 s45, 2\ns_mov_b32 s46, 0\ns_cmp_lg_u32 s24, 5\ns_cbranch_scc1 .Lj1064\n"
      "s_waitcnt vmcnt(8)\nv_lshlrev_b32 v16, 16, v253\nv_and_b32 v17, 0xffff0000, v253\ns_mov_b32 s42, 0xf000000\nv_max3_f32 v18, |v16|, |v17|, s42\ns_nop 1\n"
      "v_max_u32_dpp v18, v18, v18 quad_perm:[1,0,3,2] row_mask:0xf bank_mask:0xf\ns_nop 1\nv_max_u32_dpp v18, v18, v18 quad_perm:[2,3,0,1] row_mask:0xf bank_mask:0xf\ns_nop 1\n"
      "v_max_u32_dpp v18, v18, v18 row_half_mirror row_mask:0xf bank_mask:0xf\ns_nop 1\nv_max_u32_dpp v18, v18, v18 row_mirror row_mask:0xf bank_mask:0xf\nv_mov_b32 v19, v18\n"
      "s_mov_b32 s42, 162816\nv_lshl_add_u32 v20, v240, 3, s42\nv_permlane16_swap_b32 v18, v19\nv_max_u32 v18, v18, v19\nv_mov_b32 v19, v18\nv_mov_b32 v21, 162812\ns_nop 0\n"
      "v_permlane32_swap_b32 v18, v19\nv_max_u32 v18, v18, v19\nv_and_b32 v18, 0x7f800000, v18\nv_subrev_u32 v19, 0xc000000, v18\nds_write_b32 v21, v19\nv_lshrrev_b32 v18, 23, v18\n"
      "v_sub_u32 v18, 123, v18\nv_ldexp_f32 v16, v16, v18\nv_ldexp_f32 v17, v17, v18\ns_mov_b32 s42, 0x32000000\nv_max_f32 v18, |v16|, s42\nv_max_f32 v19, |v17|, s42\ns_brev_b32 s42, -2\n"
      "v_bfi_b32 v16, s42, v18, v16\nv_bfi_b32 v17, s42, v19, v17\nv_rcp_f32 v16, v16\nv_rcp_f32 v17, v17\ns_nop 0\nv_mul_f32 v18, 0x31800000, v16\nv_mul_f32 v19, 0x31800000, v17\n"
      "ds_write2st64_b64 v20, v[16:17], v[18:19] offset1:1\n"
      ".Lj1064:\ns_barrier\ns_waitcnt vmcnt(8)\nds_read_b64 v[8:9], v3 offset:0\nds_read_b64 v[10:11], v3 offset:32\nds_read_b64 v[12:13], v4 offset:0\nds_read_b64 v[14:15], v4 offset:32\n"
      "s_waitcnt lgkmcnt(0)\nv_lshlrev_b32 v124, 16, v8\nv_and_b32 v125, 0xffff0000, v8\nv_lshlrev_b32 v126, 16, v9\nv_and_b32 v127, 0xffff0000, v9\nv_lshlrev_b32 v128, 16, v10\n"
      "v_and_b32 v129, 0xffff0000, v10\nv_lshlrev_b32 v130, 16, v11\nv_and_b32 v131, 0xffff0000, v11\nv_lshlrev_b32 v132, 16, v12\nv_and_b32 v133, 0xffff0000, v12\nv_lshlrev_b32 v134, 16, v13\n"
      "v_and_b32 v135, 0xffff0000, v13\nv_lshlrev_b32 v136, 16, v14\nv_and_b32 v137, 0xffff0000, v14\nv_lshlrev_b32 v138, 16, v15\nv_and_b32 v139, 0xffff0000, v15\n"
      "v_mov_b64 v[140:141], v[124:125]\nv_mov_b64 v[142:143], v[126:127]\nv_mov_b64 v[144:145], v[128:129]\nv_mov_b64 v[146:147], v[130:131]\nv_mov_b64 v[148:149], v[132:133]\n"
      "v_mov_b64 v[150:151], v[134:135]\nv_mov_b64 v[152:153], v[136:137]\nv_mov_b64 v[154:155], v[138:139]\nv_mov_b64 v[156:157], v[124:125]\nv_mov_b64 v[158:159], v[126:127]\n"
      "v_mov_b64 v[160:161], v[128:129]\nv_mov_b64 v[162:163], v[130:131]\nv_mov_b64 v[164:165], v[132:133]\nv_mov_b64 v[166:167], v[134:135]\nv_mov_b64 v[168:169], v[136:137]\n"
      "v_mov_b64 v[170:171], v[138:139]\nv_mov_b64 v[172:173], v[124:125]\nv_mov_b64 v[174:175], v[126:127]\nv_mov_b64 v[176:177], v[128:129]\nv_mov_b64 v[178:179], v[130:131]\n"
      "v_mov_b64 v[180:181], v[132:133]\nv_mov_b64 v[182:183], v[134:135]\nv_mov_b64 v[184:185], v[136:137]\nv_mov_b64 v[186:187], v[138:139]\nv_mov_b64 v[188:189], v[124:125]\n"
      "v_mov_b64 v[190:191], v[126:127]\nv_mov_b64 v[192:193], v[128:129]\nv_mov_b64 v[194:195], v[130:131]\nv_mov_b64 v[196:197], v[132:133]\nv_mov_b64 v[198:199], v[134:135]\n"
      "v_mov_b64 v[200:201], v[136:137]\nv_mov_b64 v[202:203], v[138:139]\nv_mov_b64 v[204:205], v[124:125]\nv_mov_b64 v[206:207], v[126:127]\nv_mov_b64 v[208:209], v[128:129]\n"
      "v_mov_b64 v[210:211], v[130:131]\nv_mov_b64 v[212:213], v[132:133]\nv_mov_b64 v[214:215], v[134:135]\nv_mov_b64 v[216:217], v[136:137]\nv_mov_b64 v[218:219], v[138:139]\n"
      "v_mov_b64 v[220:221], v[124:125]\nv_mov_b64 v[222:223], v[126:127]\nv_mov_b64 v[224:225], v[128:129]\nv_mov_b64 v[226:227], v[130:131]\nv_mov_b64 v[228:229], v[132:133]\n"
      "v_mov_b64 v[230:231], v[134:135]\nv_mov_b64 v[232:233], v[136:137]\nv_mov_b64 v[234:235], v[138:139]\ns_mov_b32 s58, 4\ns_cmp_eq_u32 s25, 2\ns_cbranch_scc1 .Lg13\ns_cmp_eq_u32 s25, 0\n"
      "s_cbranch_scc1 .Lg12\ns_branch .Lg14\n"
      ".Lbld:\n"
      : "=&{v240}"(lane), "=&{v253}"(tch), "=&{s[4:19]}"(ka0), "=&{s[20:23]}"(ka1), "=&{s[24:31]}"(gs), "+{s2}"(blk)
      : [ka] "s"(__builtin_amdgcn_kernarg_segment_ptr()), [tid] "v"(threadIdx.x), [bkl] "i"((unsigned)BK), [bkh] "i"((unsigned)(BK >> 32)), [kp] "i"(KP[0] | KP[1] << 5 | KP[2] << 10 | KP[3] << 15 | KP[4] << 20)
      : "memory", "scc", "s36", "s37", "s38", "s39", "s40", "s41", "s42", "s43", "v236", "v237", "v238", "v239", "v241", "v242", "v243", "v244", "v245", "v246", "v247");
#define SB __builtin_amdgcn_sched_barrier(0)
#define PIN(x) asm volatile("" : "+v"(x))
  const int w = gs[0], tid = w * 64 + lane;
  ws_cin = (const u16*)((unsigned long long)ka0[5] << 32 | ka0[4]);
  wout = (const u16*)((unsigned long long)ka0[13] << 32 | ka0[12]);
  ws = (char*)((unsigned long long)ka1[1] << 32 | ka1[0]);
  const unsigned sb = (unsigned)(size_t)(__attribute__((address_space(3))) char*)shm;
  const int g = lane >> 4, fr = lane & 15, hf = lane >> 5, l31 = lane & 31;
  constexpr unsigned TX = 0xA5C30F96u;
  auto chk = [](unsigned t) -> unsigned { return (t * 0x9E3779B1u) >> 24; };
  const int bid = blk & 7;
  const bool rblk = bid >= 4;
  const int fi = (bid & 3) * 8 + w, mi_ = min(fi, NF - 1);
  const unsigned tc = sb + (g & 1) * 32 + (fr & 3) * 8, h0 = 8 * (g >> 1) + (fr >> 2);
  u32x4 mrow;
  u32x2 rsp;
  unsigned msc;
  {
    unsigned ma_, ur[2], uw[6], fa[2][2];
    if (rblk) {
      const unsigned o3 = min(w * 4480, NF * NF * NF * 2 - 4480);
      asm volatile("s_mov_b32 m0, %2\n\ts_nop 0\n\tglobal_load_lds_dwordx4 %0, %1\n\tglobal_load_lds_dwordx4 %0, %1 offset:1024\n\tglobal_load_lds_dwordx4 %0, %1 offset:2048\n\t"
                   "global_load_lds_dwordx4 %0, %1 offset:3072\n\tglobal_load_lds_dwordx4 %0, %1 offset:3456"
                   :: "v"(lane * 16), "s"((const char*)(ws_cin + 2 * NF * NF * NF) + o3), "s"(sb + W3S + o3) : "memory");
      const unsigned rr = (bid & 3) * 8 + ((lane >> 2) & 7);
      const unsigned vo = (hf * NF * NF + min(rr, 25u) * NF) * 2 + (lane & 3) * 16;
#pragma unroll
      for (int m = 0; m < 2; ++m) {
        const unsigned p = min(w + 8 * m, 12);
        asm volatile("s_mov_b32 m0, %2\n\ts_nop 0\n\tglobal_load_lds_dwordx4 %0, %1" :: "v"(vo), "s"(ws_cin + (NF + 2 * p) * NF * NF), "s"(sb + W2S + p * 1024) : "memory");
      }
      asm volatile("ds_write_b32 %0, %1" :: "v"(sb + U_OFF + tid * 4), "v"(0) : "memory");
    } else {
#pragma unroll
      for (int s = 0; s < 2; ++s)
        asm volatile("s_mov_b32 m0, %2\n\ts_nop 0\n\tglobal_load_lds_dwordx4 %0, %1" :: "v"(min(l31, NF - 1) * NF * 2 + hf * 16), "s"(ws_cin + mi_ * NF * NF + s * 16), "s"(sb + W3S + w * DP + s * 1024) : "memory");
    }
    if (w < 3) asm volatile("s_mov_b32 m0, %2\n\ts_nop 0\n\tglobal_load_lds_dwordx4 %0, %1 sc1" :: "v"(lane * 16), "s"(w ? (const char*)&g_tab + (w - 1) * 1024 : ws), "s"(sb + NC_OFF + w * 1024) : "memory");
    if (w == 3) asm volatile("s_mov_b32 m0, %2\n\ts_nop 0\n\tglobal_load_lds_dword %0, %1" :: "v"(min(lane, 38) * 4), "s"(wout), "s"(sb + WO_OFF) : "memory");
    {
      const int ty = bid >> 2, f = lane >> 2;
      const bool sp = fi >= 24;
      int c0 = sp ? 5 : fi < 16 ? ty - 1 : 1, c1 = sp ? 16 : 3072, rho = sp ? 4 * (fi - 24) + 2 * ty : fi < 16 ? fi : 2 * (fi & 6) + (fi & 1) + 2 * ty;
      asm volatile("" : "+s"(c0), "+s"(c1), "+s"(rho));
      int t = (7 * (f >> 1) + c0) * 1024 + (f & 1) * c1 + ((lane & 3) * 16 + rho) * 16;
      PIN(t);
      ma_ = min((unsigned)t | (fi < 28 ? 0u : ~0u), 41u * 1024 + 1024);
      ma_ = lane < 48 ? ma_ : 43008;
    }
#pragma unroll
    for (int p = 0; p < 2; ++p) {
      ur[p] = sb + W3S + min(8 * g + 4 * p + (fr >> 2), NF - 1) * (NF * NF * 2) + w * 32 + (fr & 3) * 8;
      fa[0][p] = tc + W2S + ((h0 + 4 * p) * 8 + w) * 64; fa[1][p] = tc + W2S + (min(h0 + 16 + 4 * p, 25u) * 8 + w) * 64;
      PIN(ur[p]); PIN(fa[0][p]); PIN(fa[1][p]);
    }
#pragma unroll
    for (int m = 0; m < 6; ++m) {
      const unsigned e = (w + 8 * m) * 16 + fr;
      uw[m] = sb + U_OFF + e * 2 + ((e * 2521u) >> 16) * 12;
      PIN(uw[m]);
      if (m == 5) uw[m] = e < NF * NF ? uw[m] : 0x40000u;
    }
    PIN(uw[5]); PIN(ma_);
    asm volatile("s_waitcnt vmcnt(0) lgkmcnt(0)\n\ts_barrier" :: "{v253}"(tch) : "memory");
    const unsigned wov = *(const unsigned*)(shm + WO_OFF + lane * 4);
    const u32x4 tb = *(const u32x4*)(shm + GT_OFF + (fi >= 24) * 1024 + lane * 16);
    bf16x8 P[2], Q[2];
    if (rblk) {
      s16x4 wl[6][2];
      u32x4 wa;
      unsigned wo2;
      asm volatile("ds_read2_b64 %0, %1 offset0:13 offset1:14" : "=v"(wa) : "v"(sb + WO_OFF + g * 16));
      asm volatile("ds_read_u16 %0, %1 offset:52" : "=v"(wo2) : "v"(sb + WO_OFF + lane * 2));
#pragma unroll
      for (int m = 0; m < 6; ++m)
#pragma unroll
        for (int p = 0; p < 2; ++p) asm volatile("ds_read_b64_tr_b16 %0, %1 offset:%2" : "=v"(wl[m][p]) : "v"(ur[p]), "i"(m * 256));
      u32x2 fl[2][2];
#pragma unroll
      for (int s = 0; s < 2; ++s)
#pragma unroll
        for (int p = 0; p < 2; ++p) asm volatile("ds_read_b64_tr_b16 %0, %1" : "=v"(fl[s][p]) : "v"(fa[s][p]));
      asm volatile("s_waitcnt lgkmcnt(0)" : "+v"(wa), "+v"(wo2), "+v"(wl[0][0]), "+v"(wl[0][1]), "+v"(wl[1][0]), "+v"(wl[1][1]), "+v"(wl[2][0]), "+v"(wl[2][1]), "+v"(wl[3][0]), "+v"(wl[3][1]),
                   "+v"(wl[4][0]), "+v"(wl[4][1]), "+v"(wl[5][0]), "+v"(wl[5][1]), "+v"(fl[0][0]), "+v"(fl[0][1]), "+v"(fl[1][0]), "+v"(fl[1][1]));
      const unsigned km = g < 3 ? ~0u : 0u;
      const u32x4 wm = {wa[0], wa[1] & km, wa[2] & km, wa[3] & km};
      f32x4 du[6];
#pragma unroll
      for (int m = 0; m < 6; ++m)
        du[m] = __builtin_amdgcn_mfma_f32_16x16x32_bf16(__builtin_bit_cast(bf16x8, wm), __builtin_shufflevector(wl[m][0], wl[m][1], 0, 1, 2, 3, 4, 5, 6, 7), (f32x4){0.f, 0.f, 0.f, 0.f}, 0, 0, 0);
      asm volatile("ds_write_b16 %0, %1" :: "v"(lane < NF ? sb + U_OFF + lane * 64 + 52 : 0x40000u), "v"(wo2) : "memory");
#pragma unroll
      for (int m = 0; m < 6; ++m) {
        asm volatile("ds_write_b16 %0, %1" :: "v"(uw[m]), "v"(pk2(du[m][0], du[m][0])) : "memory");
        asm volatile("" :: "v"(du[m]));
      }
      asm volatile("s_waitcnt lgkmcnt(0)\n\ts_barrier" ::: "memory");
      s16x4 ul[2][2];
#pragma unroll
      for (int s = 0; s < 2; ++s)
#pragma unroll
        for (int p = 0; p < 2; ++p)
          asm volatile("ds_read_b64_tr_b16 %0, %1 offset:%2" : "=v"(ul[s][p]) : "v"(tc + U_OFF + h0 * 64), "i"(s * 1024 + p * 256));
      const unsigned fmask = l31 < NF ? ~0u : 0u;
#pragma unroll
      for (int s = 0; s < 2; ++s) {
        u32x4 q4 = {fl[s][0][0] & fmask, fl[s][0][1] & fmask, fl[s][1][0] & fmask, fl[s][1][1] & fmask};
        Q[s] = __builtin_bit_cast(bf16x8, q4);
      }
      asm volatile("s_waitcnt lgkmcnt(0)" : "+v"(ul[0][0]), "+v"(ul[0][1]), "+v"(ul[1][0]), "+v"(ul[1][1]));
#pragma unroll
      for (int s = 0; s < 2; ++s) P[s] = __builtin_shufflevector(ul[s][0], ul[s][1], 0, 1, 2, 3, 4, 5, 6, 7);
    } else {
#pragma unroll
      for (int s = 0; s < 2; ++s) {
        u32x4 xf = *(const u32x4*)(shm + W3S + w * DP + s * 1024 + lane * 16);
        if (s == 1) { const unsigned m = hf ? 0u : ~0u; xf[1] &= m; xf[2] &= m; xf[3] &= m; }
        Q[s] = __builtin_bit_cast(bf16x8, xf);
        const unsigned e = (l31 >> 1) - 8 * s - 4 * hf, v = 0x3f80u << ((lane & 1) * 16);
        u32x4 q4 = {e == 0 ? v : 0u, e == 1 ? v : 0u, e == 2 ? v : 0u, e == 3 ? v : 0u};
        P[s] = __builtin_bit_cast(bf16x8, q4);
      }
    }
    f32x16 D = {};
#pragma unroll
    for (int s = 0; s < 2; ++s) {
      D = __builtin_amdgcn_mfma_f32_32x32x16_bf16(P[s], Q[s], D, 0, 0, 0);
      D = __builtin_amdgcn_mfma_f32_32x32x16_bf16(Q[s], P[s], D, 0, 0, 0);
    }
    float* dl = (float*)(shm + W3S + w * DP);
    D[14] = lane == NF ? __uint_as_float(((unsigned)__builtin_amdgcn_readlane(wov, mi_ >> 1) >> ((mi_ & 1) * 16)) << 16) : D[14];
#pragma unroll
    for (int r = 0; r < 15; ++r) dl[((r & 3) + 8 * (r >> 2) + 4 * hf) * 33 + l31] = D[r];
    __builtin_amdgcn_wave_barrier();
    float v[8], mx = 0.f;
    const unsigned zm = fi >= NF ? 0xffff : rblk ? 0x8000 : 0xc000;
#pragma unroll
    for (int e = 0; e < 8; ++e) v[e] = dl[(tb[e >> 1] >> (16 * (e & 1))) & 0x3ff];
#pragma unroll
    for (int e = 0; e < 8; ++e) {
      const unsigned c = tb[e >> 1] >> (16 * (e & 1));
      float x = __builtin_amdgcn_ldexpf(v[e], ((c >> 10) & 15) - 2);
      PIN(x);
      v[e] = c & zm ? 0.f : x;
      mx = fmaxf(mx, fabsf(v[e]));
    }
    mx = fmaxf(mx, dpp<0xB1>(mx)); mx = fmaxf(mx, dpp<0x4E>(mx)); mx = fmaxf(mx, dpp<0x141>(mx)); mx = fmaxf(mx, dpp<0x140>(mx));
    const unsigned mxu = max(max((unsigned)__builtin_amdgcn_readlane(__float_as_int(mx), 0), (unsigned)__builtin_amdgcn_readlane(__float_as_int(mx), 16)), (unsigned)__builtin_amdgcn_readlane(__float_as_int(mx), 32));
    const unsigned ex = min(max(mxu >> 23, 40u), 250u);
    const float al = __uint_as_float((268 - ex) << 23);
#pragma unroll
    for (int e = 0; e < 4; ++e) {
      const unsigned q = pk2(v[2 * e] * al, v[2 * e + 1] * al);
      mrow[e] = __builtin_bit_cast(unsigned, __builtin_amdgcn_cvt_pkrtz(__uint_as_float(q << 16), __uint_as_float(q & 0xffff0000u)));
    }
    msc = fi < NF ? (ex - 14) << 23 : 0u;
    PIN(msc);
    asm volatile("global_store_dwordx4 %0, %1, %2 sc1" :: "v"(ma_), "{v[248:251]}"(mrow), "s"(ws + WS_M) : "memory");
    asm volatile("global_store_dword %0, %1, %2 sc1" :: "v"((fi < 16 ? (fi >> 2) * 64 + (fi & 3) * 4 : fi < 24 ? ((fi & 6) << 5) + (fi & 1) * 4 + 16 : (fi & 3) * 64 + (fi & 4) + 24) + (bid & 4) * 8), "{v252}"(msc), "s"(ws + WS_M + MC) : "memory");
    if (fi == 31) asm volatile("global_store_dword %0, %1, %2 sc1\n\ts_waitcnt vmcnt(0)" :: "v"((lane & 15) * 128), "{v252}"(msc), "s"(ws + WS_POOL) : "memory");
    const unsigned mval = *(const unsigned*)(shm + NC_OFF + (tid & 255) * 4), qval = *(const unsigned*)(shm + NC_OFF);
    rsp = (u32x2){qval << 8 | chk(mval), mval ^ TX};
    PIN(rsp);
    asm volatile("s_waitcnt lgkmcnt(0)\n\ts_barrier" ::: "memory");
  }
  static_assert(STAGE == 45056 && MC == 41984 && NSLOT == 21 && WS_RESP == 4096 && WS_POOL == 32768 && WS_M == 1048576 && NCL == 8 && sizeof(g_tab.t) == 2048, "constants of the code below");
  asm volatile(
      "s_cmp_lt_u32 s24, 4\ns_cbranch_scc1 .Lbp\ns_branch .Lbg\n"
      ".Lg14:\ns_waitcnt vmcnt(4)\ns_barrier\nds_read_b64_tr_b16 v[64:65], v104 offset:45056\nds_read_b64_tr_b16 v[66:67], v104 offset:46080\nds_read_b64_tr_b16 v[68:69], v104 offset:53248\n"
      "ds_read_b64_tr_b16 v[70:71], v104 offset:54272\nds_read_b64_tr_b16 v[72:73], v105 offset:45056\nds_read_b64_tr_b16 v[74:75], v105 offset:46080\n"
      "ds_read_b64_tr_b16 v[76:77], v105 offset:53248\nds_read_b64_tr_b16 v[78:79], v105 offset:54272\nds_read_b64_tr_b16 v[80:81], v106 offset:45056\n"
      "ds_read_b64_tr_b16 v[82:83], v106 offset:46080\nds_read_b64_tr_b16 v[84:85], v106 offset:53248\nds_read_b64_tr_b16 v[86:87], v106 offset:54272\n"
      "ds_read_b64_tr_b16 v[88:89], v107 offset:45056\nds_read_b64_tr_b16 v[90:91], v107 offset:46080\nds_read_b64_tr_b16 v[92:93], v107 offset:53248\n"
      "ds_read_b64_tr_b16 v[94:95], v107 offset:54272\nds_read_b128 v[0:3], v100 offset:45056\nds_read_b128 v[4:7], v101 offset:45056\nds_read_b128 v[8:11], v120 offset:45056\n"
      "ds_read_b128 v[12:15], v121 offset:45056\nds_read_b128 v[16:19], v96 offset:45056\ns_waitcnt lgkmcnt(1)\n.p2align 3\n"
      ".Lgl14:\nv_mfma_f32_16x16x32_bf16 v[124:127], v[64:67], v[0:3], v[124:127]\nds_read_b128 v[20:23], v97 offset:45056\nv_mfma_f32_16x16x32_bf16 v[124:127], v[68:71], v[4:7], v[124:127]\n"
      "ds_read_b128 v[24:27], v96 offset:47104\nv_mfma_f32_16x16x32_bf16 v[128:131], v[76:79], v[4:7], v[128:131]\nds_read_b128 v[28:31], v97 offset:47104\n"
      "v_mfma_f32_16x16x32_bf16 v[128:131], v[72:75], v[0:3], v[128:131]\nv_mfma_f32_16x16x32_bf16 v[132:135], v[80:83], v[0:3], v[132:135]\n"
      "v_mfma_f32_16x16x32_bf16 v[132:135], v[84:87], v[4:7], v[132:135]\nv_mfma_f32_16x16x32_bf16 v[136:139], v[92:95], v[4:7], v[136:139]\n"
      "v_mfma_f32_16x16x32_bf16 v[136:139], v[88:91], v[0:3], v[136:139]\nv_mfma_f32_16x16x32_bf16 v[152:155], v[88:91], v[8:11], v[152:155]\nds_read_b128 v[0:3], v96 offset:49152\n"
      "v_mfma_f32_16x16x32_bf16 v[152:155], v[92:95], v[12:15], v[152:155]\nds_read_b128 v[4:7], v97 offset:49152\nv_mfma_f32_16x16x32_bf16 v[148:151], v[84:87], v[12:15], v[148:151]\n"
      "v_mfma_f32_16x16x32_bf16 v[148:151], v[80:83], v[8:11], v[148:151]\nv_mfma_f32_16x16x32_bf16 v[144:147], v[72:75], v[8:11], v[144:147]\n"
      "v_mfma_f32_16x16x32_bf16 v[144:147], v[76:79], v[12:15], v[144:147]\nv_mfma_f32_16x16x32_bf16 v[140:143], v[68:71], v[12:15], v[140:143]\ns_waitcnt lgkmcnt(4)\n"
      "v_mfma_f32_16x16x32_bf16 v[140:143], v[64:67], v[8:11], v[140:143]\nv_mfma_f32_16x16x32_bf16 v[156:159], v[64:67], v[16:19], v[156:159]\nds_read_b128 v[8:11], v96 offset:51200\n"
      "v_mfma_f32_16x16x32_bf16 v[156:159], v[68:71], v[20:23], v[156:159]\nds_read_b128 v[12:15], v97 offset:51200\nv_mfma_f32_16x16x32_bf16 v[160:163], v[76:79], v[20:23], v[160:163]\n"
      "v_mfma_f32_16x16x32_bf16 v[160:163], v[72:75], v[16:19], v[160:163]\nv_mfma_f32_16x16x32_bf16 v[164:167], v[80:83], v[16:19], v[164:167]\n"
      "v_mfma_f32_16x16x32_bf16 v[164:167], v[84:87], v[20:23], v[164:167]\nv_mfma_f32_16x16x32_bf16 v[168:171], v[92:95], v[20:23], v[168:171]\ns_waitcnt lgkmcnt(2)\n"
      "v_mfma_f32_16x16x32_bf16 v[168:171], v[88:91], v[16:19], v[168:171]\nv_mfma_f32_16x16x32_bf16 v[184:187], v[88:91], v[24:27], v[184:187]\nds_read_b128 v[16:19], v96 offset:53248\n"
      "v_mfma_f32_16x16x32_bf16 v[184:187], v[92:95], v[28:31], v[184:187]\nds_read_b128 v[20:23], v97 offset:53248\nv_mfma_f32_16x16x32_bf16 v[180:183], v[84:87], v[28:31], v[180:183]\n"
      "v_mfma_f32_16x16x32_bf16 v[180:183], v[80:83], v[24:27], v[180:183]\nv_mfma_f32_16x16x32_bf16 v[176:179], v[72:75], v[24:27], v[176:179]\n"
      "v_mfma_f32_16x16x32_bf16 v[176:179], v[76:79], v[28:31], v[176:179]\ns_waitcnt vmcnt(0) lgkmcnt(0)\nv_mfma_f32_16x16x32_bf16 v[172:175], v[68:71], v[28:31], v[172:175]\ns_barrier\n"
      "v_mfma_f32_16x16x32_bf16 v[172:175], v[64:67], v[24:27], v[172:175]\nds_read_b64_tr_b16 v[32:33], v108 offset:0\nv_mfma_f32_16x16x32_bf16 v[188:191], v[64:67], v[0:3], v[188:191]\n"
      "ds_read_b64_tr_b16 v[34:35], v108 offset:1024\nv_mfma_f32_16x16x32_bf16 v[188:191], v[68:71], v[4:7], v[188:191]\nds_read_b64_tr_b16 v[36:37], v108 offset:8192\n"
      "v_mfma_f32_16x16x32_bf16 v[192:195], v[76:79], v[4:7], v[192:195]\nds_read_b64_tr_b16 v[38:39], v108 offset:9216\nv_mfma_f32_16x16x32_bf16 v[192:195], v[72:75], v[0:3], v[192:195]\n"
      "ds_read_b64_tr_b16 v[40:41], v109 offset:0\nv_mfma_f32_16x16x32_bf16 v[196:199], v[80:83], v[0:3], v[196:199]\nds_read_b64_tr_b16 v[42:43], v109 offset:1024\n"
      "v_mfma_f32_16x16x32_bf16 v[196:199], v[84:87], v[4:7], v[196:199]\nds_read_b64_tr_b16 v[44:45], v109 offset:8192\nv_mfma_f32_16x16x32_bf16 v[200:203], v[92:95], v[4:7], v[200:203]\n"
      "ds_read_b64_tr_b16 v[46:47], v109 offset:9216\nv_mfma_f32_16x16x32_bf16 v[200:203], v[88:91], v[0:3], v[200:203]\nds_read_b64_tr_b16 v[48:49], v110 offset:0\n"
      "v_mfma_f32_16x16x32_bf16 v[216:219], v[88:91], v[8:11], v[216:219]\nds_read_b128 v[0:3], v102 offset:0\nv_mfma_f32_16x16x32_bf16 v[216:219], v[92:95], v[12:15], v[216:219]\n"
      "ds_read_b128 v[4:7], v103 offset:0\nv_mfma_f32_16x16x32_bf16 v[212:215], v[84:87], v[12:15], v[212:215]\nds_read_b64_tr_b16 v[50:51], v110 offset:1024\n"
      "v_mfma_f32_16x16x32_bf16 v[212:215], v[80:83], v[8:11], v[212:215]\nds_read_b64_tr_b16 v[52:53], v110 offset:8192\nv_mfma_f32_16x16x32_bf16 v[208:211], v[72:75], v[8:11], v[208:211]\n"
      "ds_read_b64_tr_b16 v[54:55], v110 offset:9216\nv_mfma_f32_16x16x32_bf16 v[208:211], v[76:79], v[12:15], v[208:211]\nds_read_b64_tr_b16 v[56:57], v111 offset:0\n"
      "v_mfma_f32_16x16x32_bf16 v[204:207], v[68:71], v[12:15], v[204:207]\nds_read_b64_tr_b16 v[58:59], v111 offset:1024\nv_mfma_f32_16x16x32_bf16 v[204:207], v[64:67], v[8:11], v[204:207]\n"
      "ds_read_b64_tr_b16 v[60:61], v111 offset:8192\nv_mfma_f32_16x16x32_bf16 v[220:223], v[64:67], v[16:19], v[220:223]\nds_read_b128 v[8:11], v122 offset:0\n"
      "v_mfma_f32_16x16x32_bf16 v[220:223], v[68:71], v[20:23], v[220:223]\nds_read_b128 v[12:15], v123 offset:0\nv_mfma_f32_16x16x32_bf16 v[224:227], v[76:79], v[20:23], v[224:227]\n"
      "ds_read_b64_tr_b16 v[62:63], v111 offset:9216\nv_mfma_f32_16x16x32_bf16 v[224:227], v[72:75], v[16:19], v[224:227]\nv_mfma_f32_16x16x32_bf16 v[228:231], v[80:83], v[16:19], v[228:231]\n"
      "v_mfma_f32_16x16x32_bf16 v[228:231], v[84:87], v[20:23], v[228:231]\nv_mfma_f32_16x16x32_bf16 v[232:235], v[92:95], v[20:23], v[232:235]\ns_waitcnt lgkmcnt(0)\n"
      "v_mfma_f32_16x16x32_bf16 v[232:235], v[88:91], v[16:19], v[232:235]\nds_read_b128 v[16:19], v98 offset:0\nv_mfma_f32_16x16x32_bf16 v[124:127], v[32:35], v[0:3], v[124:127]\n"
      "ds_read_b128 v[20:23], v99 offset:0\nv_mfma_f32_16x16x32_bf16 v[124:127], v[36:39], v[4:7], v[124:127]\nds_read_b128 v[24:27], v98 offset:2048\n"
      "v_mfma_f32_16x16x32_bf16 v[128:131], v[44:47], v[4:7], v[128:131]\nds_read_b128 v[28:31], v99 offset:2048\nv_mfma_f32_16x16x32_bf16 v[128:131], v[40:43], v[0:3], v[128:131]\n"
      "v_mfma_f32_16x16x32_bf16 v[132:135], v[48:51], v[0:3], v[132:135]\nv_mfma_f32_16x16x32_bf16 v[132:135], v[52:55], v[4:7], v[132:135]\n"
      "v_mfma_f32_16x16x32_bf16 v[136:139], v[60:63], v[4:7], v[136:139]\nv_mfma_f32_16x16x32_bf16 v[136:139], v[56:59], v[0:3], v[136:139]\n"
      "v_mfma_f32_16x16x32_bf16 v[152:155], v[56:59], v[8:11], v[152:155]\nds_read_b128 v[0:3], v98 offset:4096\nv_mfma_f32_16x16x32_bf16 v[152:155], v[60:63], v[12:15], v[152:155]\n"
      "ds_read_b128 v[4:7], v99 offset:4096\nv_mfma_f32_16x16x32_bf16 v[148:151], v[52:55], v[12:15], v[148:151]\nv_mfma_f32_16x16x32_bf16 v[148:151], v[48:51], v[8:11], v[148:151]\n"
      "v_mfma_f32_16x16x32_bf16 v[144:147], v[40:43], v[8:11], v[144:147]\nv_mfma_f32_16x16x32_bf16 v[144:147], v[44:47], v[12:15], v[144:147]\n"
      "v_mfma_f32_16x16x32_bf16 v[140:143], v[36:39], v[12:15], v[140:143]\ns_waitcnt lgkmcnt(4)\nv_mfma_f32_16x16x32_bf16 v[140:143], v[32:35], v[8:11], v[140:143]\n"
      "v_mfma_f32_16x16x32_bf16 v[156:159], v[32:35], v[16:19], v[156:159]\nds_read_b128 v[8:11], v98 offset:6144\nv_mfma_f32_16x16x32_bf16 v[156:159], v[36:39], v[20:23], v[156:159]\n"
      "ds_read_b128 v[12:15], v99 offset:6144\nv_mfma_f32_16x16x32_bf16 v[160:163], v[44:47], v[20:23], v[160:163]\nv_mfma_f32_16x16x32_bf16 v[160:163], v[40:43], v[16:19], v[160:163]\n"
      "v_mfma_f32_16x16x32_bf16 v[164:167], v[48:51], v[16:19], v[164:167]\nv_mfma_f32_16x16x32_bf16 v[164:167], v[52:55], v[20:23], v[164:167]\n"
      "v_mfma_f32_16x16x32_bf16 v[168:171], v[60:63], v[20:23], v[168:171]\ns_waitcnt lgkmcnt(2)\nv_mfma_f32_16x16x32_bf16 v[168:171], v[56:59], v[16:19], v[168:171]\n"
      "v_mfma_f32_16x16x32_bf16 v[184:187], v[56:59], v[24:27], v[184:187]\nds_read_b128 v[16:19], v98 offset:8192\nv_mfma_f32_16x16x32_bf16 v[184:187], v[60:63], v[28:31], v[184:187]\n"
      "ds_read_b128 v[20:23], v99 offset:8192\nv_mfma_f32_16x16x32_bf16 v[180:183], v[52:55], v[28:31], v[180:183]\nv_mfma_f32_16x16x32_bf16 v[180:183], v[48:51], v[24:27], v[180:183]\n"
      "v_mfma_f32_16x16x32_bf16 v[176:179], v[40:43], v[24:27], v[176:179]\nv_mfma_f32_16x16x32_bf16 v[176:179], v[44:47], v[28:31], v[176:179]\ns_waitcnt vmcnt(0) lgkmcnt(0)\n"
      "v_mfma_f32_16x16x32_bf16 v[172:175], v[36:39], v[28:31], v[172:175]\ns_barrier\nv_mfma_f32_16x16x32_bf16 v[172:175], v[32:35], v[24:27], v[172:175]\n"
      "ds_read_b64_tr_b16 v[64:65], v104 offset:0\nv_mfma_f32_16x16x32_bf16 v[188:191], v[32:35], v[0:3], v[188:191]\nds_read_b64_tr_b16 v[66:67], v104 offset:1024\n"
      "v_mfma_f32_16x16x32_bf16 v[188:191], v[36:39], v[4:7], v[188:191]\nds_read_b64_tr_b16 v[68:69], v104 offset:8192\nv_mfma_f32_16x16x32_bf16 v[192:195], v[44:47], v[4:7], v[192:195]\n"
      "ds_read_b64_tr_b16 v[70:71], v104 offset:9216\nv_mfma_f32_16x16x32_bf16 v[192:195], v[40:43], v[0:3], v[192:195]\nds_read_b64_tr_b16 v[72:73], v105 offset:0\n"
      "v_mfma_f32_16x16x32_bf16 v[196:199], v[48:51], v[0:3], v[196:199]\nds_read_b64_tr_b16 v[74:75], v105 offset:1024\nv_mfma_f32_16x16x32_bf16 v[196:199], v[52:55], v[4:7], v[196:199]\n"
      "ds_read_b64_tr_b16 v[76:77], v105 offset:8192\nv_mfma_f32_16x16x32_bf16 v[200:203], v[60:63], v[4:7], v[200:203]\nds_read_b64_tr_b16 v[78:79], v105 offset:9216\n"
      "v_mfma_f32_16x16x32_bf16 v[200:203], v[56:59], v[0:3], v[200:203]\nds_read_b64_tr_b16 v[80:81], v106 offset:0\nv_mfma_f32_16x16x32_bf16 v[216:219], v[56:59], v[8:11], v[216:219]\n"
      "ds_read_b128 v[0:3], v100 offset:0\nv_mfma_f32_16x16x32_bf16 v[216:219], v[60:63], v[12:15], v[216:219]\nds_read_b128 v[4:7], v101 offset:0\n"
      "v_mfma_f32_16x16x32_bf16 v[212:215], v[52:55], v[12:15], v[212:215]\nds_read_b64_tr_b16 v[82:83], v106 offset:1024\nv_mfma_f32_16x16x32_bf16 v[212:215], v[48:51], v[8:11], v[212:215]\n"
      "ds_read_b64_tr_b16 v[84:85], v106 offset:8192\nv_mfma_f32_16x16x32_bf16 v[208:211], v[40:43], v[8:11], v[208:211]\nds_read_b64_tr_b16 v[86:87], v106 offset:9216\n"
      "v_mfma_f32_16x16x32_bf16 v[208:211], v[44:47], v[12:15], v[208:211]\nds_read_b64_tr_b16 v[88:89], v107 offset:0\nv_mfma_f32_16x16x32_bf16 v[204:207], v[36:39], v[12:15], v[204:207]\n"
      "ds_read_b64_tr_b16 v[90:91], v107 offset:1024\nv_mfma_f32_16x16x32_bf16 v[204:207], v[32:35], v[8:11], v[204:207]\nds_read_b64_tr_b16 v[92:93], v107 offset:8192\n"
      "v_mfma_f32_16x16x32_bf16 v[220:223], v[32:35], v[16:19], v[220:223]\nds_read_b128 v[8:11], v120 offset:0\nv_mfma_f32_16x16x32_bf16 v[220:223], v[36:39], v[20:23], v[220:223]\n"
      "ds_read_b128 v[12:15], v121 offset:0\nv_mfma_f32_16x16x32_bf16 v[224:227], v[44:47], v[20:23], v[224:227]\nds_read_b64_tr_b16 v[94:95], v107 offset:9216\n"
      "v_mfma_f32_16x16x32_bf16 v[224:227], v[40:43], v[16:19], v[224:227]\nv_mfma_f32_16x16x32_bf16 v[228:231], v[48:51], v[16:19], v[228:231]\n"
      "v_mfma_f32_16x16x32_bf16 v[228:231], v[52:55], v[20:23], v[228:231]\nv_mfma_f32_16x16x32_bf16 v[232:235], v[60:63], v[20:23], v[232:235]\ns_waitcnt lgkmcnt(0)\n"
      "v_mfma_f32_16x16x32_bf16 v[232:235], v[56:59], v[16:19], v[232:235]\nds_read_b128 v[16:19], v96 offset:0\nv_mfma_f32_16x16x32_bf16 v[124:127], v[64:67], v[0:3], v[124:127]\n"
      "ds_read_b128 v[20:23], v97 offset:0\nv_mfma_f32_16x16x32_bf16 v[124:127], v[68:71], v[4:7], v[124:127]\nds_read_b128 v[24:27], v96 offset:2048\n"
      "v_mfma_f32_16x16x32_bf16 v[128:131], v[76:79], v[4:7], v[128:131]\nds_read_b128 v[28:31], v97 offset:2048\nv_mfma_f32_16x16x32_bf16 v[128:131], v[72:75], v[0:3], v[128:131]\n"
      "v_mfma_f32_16x16x32_bf16 v[132:135], v[80:83], v[0:3], v[132:135]\nv_mfma_f32_16x16x32_bf16 v[132:135], v[84:87], v[4:7], v[132:135]\n"
      "v_mfma_f32_16x16x32_bf16 v[136:139], v[92:95], v[4:7], v[136:139]\nv_mfma_f32_16x16x32_bf16 v[136:139], v[88:91], v[0:3], v[136:139]\n"
      "v_mfma_f32_16x16x32_bf16 v[152:155], v[88:91], v[8:11], v[152:155]\nds_read_b128 v[0:3], v96 offset:4096\nv_mfma_f32_16x16x32_bf16 v[152:155], v[92:95], v[12:15], v[152:155]\n"
      "ds_read_b128 v[4:7], v97 offset:4096\nv_mfma_f32_16x16x32_bf16 v[148:151], v[84:87], v[12:15], v[148:151]\nv_mfma_f32_16x16x32_bf16 v[148:151], v[80:83], v[8:11], v[148:151]\n"
      "v_mfma_f32_16x16x32_bf16 v[144:147], v[72:75], v[8:11], v[144:147]\nv_mfma_f32_16x16x32_bf16 v[144:147], v[76:79], v[12:15], v[144:147]\n"
      "v_mfma_f32_16x16x32_bf16 v[140:143], v[68:71], v[12:15], v[140:143]\ns_waitcnt lgkmcnt(4)\nv_mfma_f32_16x16x32_bf16 v[140:143], v[64:67], v[8:11], v[140:143]\n"
      "v_mfma_f32_16x16x32_bf16 v[156:159], v[64:67], v[16:19], v[156:159]\nds_read_b128 v[8:11], v96 offset:6144\nv_mfma_f32_16x16x32_bf16 v[156:159], v[68:71], v[20:23], v[156:159]\n"
      "ds_read_b128 v[12:15], v97 offset:6144\nv_mfma_f32_16x16x32_bf16 v[160:163], v[76:79], v[20:23], v[160:163]\nv_mfma_f32_16x16x32_bf16 v[160:163], v[72:75], v[16:19], v[160:163]\n"
      "v_mfma_f32_16x16x32_bf16 v[164:167], v[80:83], v[16:19], v[164:167]\nv_mfma_f32_16x16x32_bf16 v[164:167], v[84:87], v[20:23], v[164:167]\n"
      "v_mfma_f32_16x16x32_bf16 v[168:171], v[92:95], v[20:23], v[168:171]\ns_waitcnt lgkmcnt(2)\nv_mfma_f32_16x16x32_bf16 v[168:171], v[88:91], v[16:19], v[168:171]\n"
      "v_mfma_f32_16x16x32_bf16 v[184:187], v[88:91], v[24:27], v[184:187]\nds_read_b128 v[16:19], v96 offset:8192\nv_mfma_f32_16x16x32_bf16 v[184:187], v[92:95], v[28:31], v[184:187]\n"
      "ds_read_b128 v[20:23], v97 offset:8192\nv_mfma_f32_16x16x32_bf16 v[180:183], v[84:87], v[28:31], v[180:183]\nv_mfma_f32_16x16x32_bf16 v[180:183], v[80:83], v[24:27], v[180:183]\n"
      "v_mfma_f32_16x16x32_bf16 v[176:179], v[72:75], v[24:27], v[176:179]\nv_mfma_f32_16x16x32_bf16 v[176:179], v[76:79], v[28:31], v[176:179]\ns_waitcnt vmcnt(0) lgkmcnt(0)\n"
      "v_mfma_f32_16x16x32_bf16 v[172:175], v[68:71], v[28:31], v[172:175]\ns_barrier\nv_mfma_f32_16x16x32_bf16 v[172:175], v[64:67], v[24:27], v[172:175]\n"
      "ds_read_b64_tr_b16 v[32:33], v104 offset:45056\nv_mfma_f32_16x16x32_bf16 v[188:191], v[64:67], v[0:3], v[188:191]\nds_read_b64_tr_b16 v[34:35], v104 offset:46080\n"
      "v_mfma_f32_16x16x32_bf16 v[188:191], v[68:71], v[4:7], v[188:191]\nds_read_b64_tr_b16 v[36:37], v104 offset:53248\nv_mfma_f32_16x16x32_bf16 v[192:195], v[76:79], v[4:7], v[192:195]\n"
      "ds_read_b64_tr_b16 v[38:39], v104 offset:54272\nv_mfma_f32_16x16x32_bf16 v[192:195], v[72:75], v[0:3], v[192:195]\nds_read_b64_tr_b16 v[40:41], v105 offset:45056\n"
      "v_mfma_f32_16x16x32_bf16 v[196:199], v[80:83], v[0:3], v[196:199]\nds_read_b64_tr_b16 v[42:43], v105 offset:46080\nv_mfma_f32_16x16x32_bf16 v[196:199], v[84:87], v[4:7], v[196:199]\n"
      "ds_read_b64_tr_b16 v[44:45], v105 offset:53248\nv_mfma_f32_16x16x32_bf16 v[200:203], v[92:95], v[4:7], v[200:203]\nds_read_b64_tr_b16 v[46:47], v105 offset:54272\n"
      "v_mfma_f32_16x16x32_bf16 v[200:203], v[88:91], v[0:3], v[200:203]\nds_read_b64_tr_b16 v[48:49], v106 offset:45056\nv_mfma_f32_16x16x32_bf16 v[216:219], v[88:91], v[8:11], v[216:219]\n"
      "ds_read_b128 v[0:3], v100 offset:45056\nv_mfma_f32_16x16x32_bf16 v[216:219], v[92:95], v[12:15], v[216:219]\nds_read_b128 v[4:7], v101 offset:45056\n"
      "v_mfma_f32_16x16x32_bf16 v[212:215], v[84:87], v[12:15], v[212:215]\nds_read_b64_tr_b16 v[50:51], v106 offset:46080\nv_mfma_f32_16x16x32_bf16 v[212:215], v[80:83], v[8:11], v[212:215]\n"
      "ds_read_b64_tr_b16 v[52:53], v106 offset:53248\nv_mfma_f32_16x16x32_bf16 v[208:211], v[72:75], v[8:11], v[208:211]\nds_read_b64_tr_b16 v[54:55], v106 offset:54272\n"
      "v_mfma_f32_16x16x32_bf16 v[208:211], v[76:79], v[12:15], v[208:211]\nds_read_b64_tr_b16 v[56:57], v107 offset:45056\nv_mfma_f32_16x16x32_bf16 v[204:207], v[68:71], v[12:15], v[204:207]\n"
      "ds_read_b64_tr_b16 v[58:59], v107 offset:46080\nv_mfma_f32_16x16x32_bf16 v[204:207], v[64:67], v[8:11], v[204:207]\nds_read_b64_tr_b16 v[60:61], v107 offset:53248\n"
      "v_mfma_f32_16x16x32_bf16 v[220:223], v[64:67], v[16:19], v[220:223]\nds_read_b128 v[8:11], v120 offset:45056\nv_mfma_f32_16x16x32_bf16 v[220:223], v[68:71], v[20:23], v[220:223]\n"
      "ds_read_b128 v[12:15], v121 offset:45056\nv_mfma_f32_16x16x32_bf16 v[224:227], v[76:79], v[20:23], v[224:227]\nds_read_b64_tr_b16 v[62:63], v107 offset:54272\n"
      "v_mfma_f32_16x16x32_bf16 v[224:227], v[72:75], v[16:19], v[224:227]\nv_mfma_f32_16x16x32_bf16 v[228:231], v[80:83], v[16:19], v[228:231]\n"
      "v_mfma_f32_16x16x32_bf16 v[228:231], v[84:87], v[20:23], v[228:231]\nv_mfma_f32_16x16x32_bf16 v[232:235], v[92:95], v[20:23], v[232:235]\ns_waitcnt lgkmcnt(0)\n"
      "v_mfma_f32_16x16x32_bf16 v[232:235], v[88:91], v[16:19], v[232:235]\nds_read_b128 v[16:19], v96 offset:45056\nv_mfma_f32_16x16x32_bf16 v[124:127], v[32:35], v[0:3], v[124:127]\n"
      "ds_read_b128 v[20:23], v97 offset:45056\nv_mfma_f32_16x16x32_bf16 v[124:127], v[36:39], v[4:7], v[124:127]\nds_read_b128 v[24:27], v96 offset:47104\n"
      "v_mfma_f32_16x16x32_bf16 v[128:131], v[44:47], v[4:7], v[128:131]\nds_read_b128 v[28:31], v97 offset:47104\nv_mfma_f32_16x16x32_bf16 v[128:131], v[40:43], v[0:3], v[128:131]\n"
      "v_mfma_f32_16x16x32_bf16 v[132:135], v[48:51], v[0:3], v[132:135]\nv_mfma_f32_16x16x32_bf16 v[132:135], v[52:55], v[4:7], v[132:135]\n"
      "v_mfma_f32_16x16x32_bf16 v[136:139], v[60:63], v[4:7], v[136:139]\nv_mfma_f32_16x16x32_bf16 v[136:139], v[56:59], v[0:3], v[136:139]\n"
      "v_mfma_f32_16x16x32_bf16 v[152:155], v[56:59], v[8:11], v[152:155]\nds_read_b128 v[0:3], v96 offset:49152\nv_mfma_f32_16x16x32_bf16 v[152:155], v[60:63], v[12:15], v[152:155]\n"
      "ds_read_b128 v[4:7], v97 offset:49152\nv_mfma_f32_16x16x32_bf16 v[148:151], v[52:55], v[12:15], v[148:151]\nv_mfma_f32_16x16x32_bf16 v[148:151], v[48:51], v[8:11], v[148:151]\n"
      "v_mfma_f32_16x16x32_bf16 v[144:147], v[40:43], v[8:11], v[144:147]\nv_mfma_f32_16x16x32_bf16 v[144:147], v[44:47], v[12:15], v[144:147]\n"
      "v_mfma_f32_16x16x32_bf16 v[140:143], v[36:39], v[12:15], v[140:143]\ns_waitcnt lgkmcnt(4)\nv_mfma_f32_16x16x32_bf16 v[140:143], v[32:35], v[8:11], v[140:143]\n"
      "v_mfma_f32_16x16x32_bf16 v[156:159], v[32:35], v[16:19], v[156:159]\nds_read_b128 v[8:11], v96 offset:51200\nv_mfma_f32_16x16x32_bf16 v[156:159], v[36:39], v[20:23], v[156:159]\n"
      "ds_read_b128 v[12:15], v97 offset:51200\nv_mfma_f32_16x16x32_bf16 v[160:163], v[44:47], v[20:23], v[160:163]\nv_mfma_f32_16x16x32_bf16 v[160:163], v[40:43], v[16:19], v[160:163]\n"
      "v_mfma_f32_16x16x32_bf16 v[164:167], v[48:51], v[16:19], v[164:167]\nv_mfma_f32_16x16x32_bf16 v[164:167], v[52:55], v[20:23], v[164:167]\n"
      "v_mfma_f32_16x16x32_bf16 v[168:171], v[60:63], v[20:23], v[168:171]\ns_waitcnt lgkmcnt(2)\nv_mfma_f32_16x16x32_bf16 v[168:171], v[56:59], v[16:19], v[168:171]\n"
      "v_mfma_f32_16x16x32_bf16 v[184:187], v[56:59], v[24:27], v[184:187]\nds_read_b128 v[16:19], v96 offset:53248\nv_mfma_f32_16x16x32_bf16 v[184:187], v[60:63], v[28:31], v[184:187]\n"
      "ds_read_b128 v[20:23], v97 offset:53248\nv_mfma_f32_16x16x32_bf16 v[180:183], v[52:55], v[28:31], v[180:183]\nv_mfma_f32_16x16x32_bf16 v[180:183], v[48:51], v[24:27], v[180:183]\n"
      "v_mfma_f32_16x16x32_bf16 v[176:179], v[40:43], v[24:27], v[176:179]\nv_mfma_f32_16x16x32_bf16 v[176:179], v[44:47], v[28:31], v[176:179]\ns_waitcnt vmcnt(0) lgkmcnt(0)\n"
      "v_mfma_f32_16x16x32_bf16 v[172:175], v[36:39], v[28:31], v[172:175]\ns_barrier\nv_mfma_f32_16x16x32_bf16 v[172:175], v[32:35], v[24:27], v[172:175]\n"
      "ds_read_b64_tr_b16 v[64:65], v108 offset:0\nv_mfma_f32_16x16x32_bf16 v[188:191], v[32:35], v[0:3], v[188:191]\nds_read_b64_tr_b16 v[66:67], v108 offset:1024\n"
      "v_mfma_f32_16x16x32_bf16 v[188:191], v[36:39], v[4:7], v[188:191]\nds_read_b64_tr_b16 v[68:69], v108 offset:8192\nv_mfma_f32_16x16x32_bf16 v[192:195], v[44:47], v[4:7], v[192:195]\n"
      "ds_read_b64_tr_b16 v[70:71], v108 offset:9216\nv_mfma_f32_16x16x32_bf16 v[192:195], v[40:43], v[0:3], v[192:195]\nds_read_b64_tr_b16 v[72:73], v109 offset:0\n"
      "v_mfma_f32_16x16x32_bf16 v[196:199], v[48:51], v[0:3], v[196:199]\nds_read_b64_tr_b16 v[74:75], v109 offset:1024\nv_mfma_f32_16x16x32_bf16 v[196:199], v[52:55], v[4:7], v[196:199]\n"
      "ds_read_b64_tr_b16 v[76:77], v109 offset:8192\nv_mfma_f32_16x16x32_bf16 v[200:203], v[60:63], v[4:7], v[200:203]\nds_read_b64_tr_b16 v[78:79], v109 offset:9216\n"
      "v_mfma_f32_16x16x32_bf16 v[200:203], v[56:59], v[0:3], v[200:203]\nds_read_b64_tr_b16 v[80:81], v110 offset:0\nv_mfma_f32_16x16x32_bf16 v[216:219], v[56:59], v[8:11], v[216:219]\n"
      "ds_read_b128 v[0:3], v102 offset:0\nv_mfma_f32_16x16x32_bf16 v[216:219], v[60:63], v[12:15], v[216:219]\nds_read_b128 v[4:7], v103 offset:0\n"
      "v_mfma_f32_16x16x32_bf16 v[212:215], v[52:55], v[12:15], v[212:215]\nds_read_b64_tr_b16 v[82:83], v110 offset:1024\nv_mfma_f32_16x16x32_bf16 v[212:215], v[48:51], v[8:11], v[212:215]\n"
      "ds_read_b64_tr_b16 v[84:85], v110 offset:8192\nv_mfma_f32_16x16x32_bf16 v[208:211], v[40:43], v[8:11], v[208:211]\nds_read_b64_tr_b16 v[86:87], v110 offset:9216\n"
      "v_mfma_f32_16x16x32_bf16 v[208:211], v[44:47], v[12:15], v[208:211]\nds_read_b64_tr_b16 v[88:89], v111 offset:0\nv_mfma_f32_16x16x32_bf16 v[204:207], v[36:39], v[12:15], v[204:207]\n"
      "ds_read_b64_tr_b16 v[90:91], v111 offset:1024\nv_mfma_f32_16x16x32_bf16 v[204:207], v[32:35], v[8:11], v[204:207]\nds_read_b64_tr_b16 v[92:93], v111 offset:8192\n"
      "v_mfma_f32_16x16x32_bf16 v[220:223], v[32:35], v[16:19], v[220:223]\nds_read_b128 v[8:11], v122 offset:0\nv_mfma_f32_16x16x32_bf16 v[220:223], v[36:39], v[20:23], v[220:223]\n"
      "ds_read_b128 v[12:15], v123 offset:0\nv_mfma_f32_16x16x32_bf16 v[224:227], v[44:47], v[20:23], v[224:227]\nds_read_b64_tr_b16 v[94:95], v111 offset:9216\n"
      "v_mfma_f32_16x16x32_bf16 v[224:227], v[40:43], v[16:19], v[224:227]\nv_mfma_f32_16x16x32_bf16 v[228:231], v[48:51], v[16:19], v[228:231]\n"
      "v_mfma_f32_16x16x32_bf16 v[228:231], v[52:55], v[20:23], v[228:231]\nv_mfma_f32_16x16x32_bf16 v[232:235], v[60:63], v[20:23], v[232:235]\ns_waitcnt lgkmcnt(0)\n"
      "v_mfma_f32_16x16x32_bf16 v[232:235], v[56:59], v[16:19], v[232:235]\nds_read_b128 v[16:19], v98 offset:0\nv_mfma_f32_16x16x32_bf16 v[124:127], v[64:67], v[0:3], v[124:127]\n"
      "ds_read_b128 v[20:23], v99 offset:0\nv_mfma_f32_16x16x32_bf16 v[124:127], v[68:71], v[4:7], v[124:127]\nds_read_b128 v[24:27], v98 offset:2048\n"
      "v_mfma_f32_16x16x32_bf16 v[128:131], v[76:79], v[4:7], v[128:131]\nds_read_b128 v[28:31], v99 offset:2048\nv_mfma_f32_16x16x32_bf16 v[128:131], v[72:75], v[0:3], v[128:131]\n"
      "v_mfma_f32_16x16x32_bf16 v[132:135], v[80:83], v[0:3], v[132:135]\nv_mfma_f32_16x16x32_bf16 v[132:135], v[84:87], v[4:7], v[132:135]\n"
      "v_mfma_f32_16x16x32_bf16 v[136:139], v[92:95], v[4:7], v[136:139]\nv_mfma_f32_16x16x32_bf16 v[136:139], v[88:91], v[0:3], v[136:139]\n"
      "v_mfma_f32_16x16x32_bf16 v[152:155], v[88:91], v[8:11], v[152:155]\nds_read_b128 v[0:3], v98 offset:4096\nv_mfma_f32_16x16x32_bf16 v[152:155], v[92:95], v[12:15], v[152:155]\n"
      "ds_read_b128 v[4:7], v99 offset:4096\nv_mfma_f32_16x16x32_bf16 v[148:151], v[84:87], v[12:15], v[148:151]\nv_mfma_f32_16x16x32_bf16 v[148:151], v[80:83], v[8:11], v[148:151]\n"
      "v_mfma_f32_16x16x32_bf16 v[144:147], v[72:75], v[8:11], v[144:147]\nv_mfma_f32_16x16x32_bf16 v[144:147], v[76:79], v[12:15], v[144:147]\n"
      "v_mfma_f32_16x16x32_bf16 v[140:143], v[68:71], v[12:15], v[140:143]\ns_waitcnt lgkmcnt(4)\nv_mfma_f32_16x16x32_bf16 v[140:143], v[64:67], v[8:11], v[140:143]\n"
      "v_mfma_f32_16x16x32_bf16 v[156:159], v[64:67], v[16:19], v[156:159]\nds_read_b128 v[8:11], v98 offset:6144\nv_mfma_f32_16x16x32_bf16 v[156:159], v[68:71], v[20:23], v[156:159]\n"
      "ds_read_b128 v[12:15], v99 offset:6144\nv_mfma_f32_16x16x32_bf16 v[160:163], v[76:79], v[20:23], v[160:163]\nv_mfma_f32_16x16x32_bf16 v[160:163], v[72:75], v[16:19], v[160:163]\n"
      "v_mfma_f32_16x16x32_bf16 v[164:167], v[80:83], v[16:19], v[164:167]\nv_mfma_f32_16x16x32_bf16 v[164:167], v[84:87], v[20:23], v[164:167]\n"
      "v_mfma_f32_16x16x32_bf16 v[168:171], v[92:95], v[20:23], v[168:171]\ns_waitcnt lgkmcnt(2)\nv_mfma_f32_16x16x32_bf16 v[168:171], v[88:91], v[16:19], v[168:171]\n"
      "v_mfma_f32_16x16x32_bf16 v[184:187], v[88:91], v[24:27], v[184:187]\nds_read_b128 v[16:19], v98 offset:8192\nv_mfma_f32_16x16x32_bf16 v[184:187], v[92:95], v[28:31], v[184:187]\n"
      "ds_read_b128 v[20:23], v99 offset:8192\nv_mfma_f32_16x16x32_bf16 v[180:183], v[84:87], v[28:31], v[180:183]\nv_mfma_f32_16x16x32_bf16 v[180:183], v[80:83], v[24:27], v[180:183]\n"
      "v_mfma_f32_16x16x32_bf16 v[176:179], v[72:75], v[24:27], v[176:179]\nv_mfma_f32_16x16x32_bf16 v[176:179], v[76:79], v[28:31], v[176:179]\ns_waitcnt vmcnt(0) lgkmcnt(0)\n"
      "v_mfma_f32_16x16x32_bf16 v[172:175], v[68:71], v[28:31], v[172:175]\ns_barrier\nv_mfma_f32_16x16x32_bf16 v[172:175], v[64:67], v[24:27], v[172:175]\n"
      "ds_read_b64_tr_b16 v[32:33], v104 offset:0\nv_mfma_f32_16x16x32_bf16 v[188:191], v[64:67], v[0:3], v[188:191]\nds_read_b64_tr_b16 v[34:35], v104 offset:1024\n"
      "v_mfma_f32_16x16x32_bf16 v[188:191], v[68:71], v[4:7], v[188:191]\nds_read_b64_tr_b16 v[36:37], v104 offset:8192\nv_mfma_f32_16x16x32_bf16 v[192:195], v[76:79], v[4:7], v[192:195]\n"
      "ds_read_b64_tr_b16 v[38:39], v104 offset:9216\nv_mfma_f32_16x16x32_bf16 v[192:195], v[72:75], v[0:3], v[192:195]\nds_read_b64_tr_b16 v[40:41], v105 offset:0\n"
      "v_mfma_f32_16x16x32_bf16 v[196:199], v[80:83], v[0:3], v[196:199]\nds_read_b64_tr_b16 v[42:43], v105 offset:1024\nv_mfma_f32_16x16x32_bf16 v[196:199], v[84:87], v[4:7], v[196:199]\n"
      "ds_read_b64_tr_b16 v[44:45], v105 offset:8192\nv_mfma_f32_16x16x32_bf16 v[200:203], v[92:95], v[4:7], v[200:203]\nds_read_b64_tr_b16 v[46:47], v105 offset:9216\n"
      "v_mfma_f32_16x16x32_bf16 v[200:203], v[88:91], v[0:3], v[200:203]\nds_read_b64_tr_b16 v[48:49], v106 offset:0\nv_mfma_f32_16x16x32_bf16 v[216:219], v[88:91], v[8:11], v[216:219]\n"
      "ds_read_b128 v[0:3], v100 offset:0\nv_mfma_f32_16x16x32_bf16 v[216:219], v[92:95], v[12:15], v[216:219]\nds_read_b128 v[4:7], v101 offset:0\n"
      "v_mfma_f32_16x16x32_bf16 v[212:215], v[84:87], v[12:15], v[212:215]\nds_read_b64_tr_b16 v[50:51], v106 offset:1024\nv_mfma_f32_16x16x32_bf16 v[212:215], v[80:83], v[8:11], v[212:215]\n"
      "ds_read_b64_tr_b16 v[52:53], v106 offset:8192\nv_mfma_f32_16x16x32_bf16 v[208:211], v[72:75], v[8:11], v[208:211]\nds_read_b64_tr_b16 v[54:55], v106 offset:9216\n"
      "v_mfma_f32_16x16x32_bf16 v[208:211], v[76:79], v[12:15], v[208:211]\nds_read_b64_tr_b16 v[56:57], v107 offset:0\nv_mfma_f32_16x16x32_bf16 v[204:207], v[68:71], v[12:15], v[204:207]\n"
      "ds_read_b64_tr_b16 v[58:59], v107 offset:1024\nv_mfma_f32_16x16x32_bf16 v[204:207], v[64:67], v[8:11], v[204:207]\nds_read_b64_tr_b16 v[60:61], v107 offset:8192\n"
      "v_mfma_f32_16x16x32_bf16 v[220:223], v[64:67], v[16:19], v[220:223]\nds_read_b128 v[8:11], v120 offset:0\nv_mfma_f32_16x16x32_bf16 v[220:223], v[68:71], v[20:23], v[220:223]\n"
      "ds_read_b128 v[12:15], v121 offset:0\nv_mfma_f32_16x16x32_bf16 v[224:227], v[76:79], v[20:23], v[224:227]\nds_read_b64_tr_b16 v[62:63], v107 offset:9216\n"
      "v_mfma_f32_16x16x32_bf16 v[224:227], v[72:75], v[16:19], v[224:227]\nv_mfma_f32_16x16x32_bf16 v[228:231], v[80:83], v[16:19], v[228:231]\n"
      "v_mfma_f32_16x16x32_bf16 v[228:231], v[84:87], v[20:23], v[228:231]\nv_mfma_f32_16x16x32_bf16 v[232:235], v[92:95], v[20:23], v[232:235]\ns_waitcnt lgkmcnt(0)\n"
      "v_mfma_f32_16x16x32_bf16 v[232:235], v[88:91], v[16:19], v[232:235]\nds_read_b128 v[16:19], v96 offset:0\nv_mfma_f32_16x16x32_bf16 v[124:127], v[32:35], v[0:3], v[124:127]\n"
      "ds_read_b128 v[20:23], v97 offset:0\nv_mfma_f32_16x16x32_bf16 v[124:127], v[36:39], v[4:7], v[124:127]\nds_read_b128 v[24:27], v96 offset:2048\n"
      "v_mfma_f32_16x16x32_bf16 v[128:131], v[44:47], v[4:7], v[128:131]\nds_read_b128 v[28:31], v97 offset:2048\nv_mfma_f32_16x16x32_bf16 v[128:131], v[40:43], v[0:3], v[128:131]\n"
      "v_mfma_f32_16x16x32_bf16 v[132:135], v[48:51], v[0:3], v[132:135]\nv_mfma_f32_16x16x32_bf16 v[132:135], v[52:55], v[4:7], v[132:135]\n"
      "v_mfma_f32_16x16x32_bf16 v[136:139], v[60:63], v[4:7], v[136:139]\nv_mfma_f32_16x16x32_bf16 v[136:139], v[56:59], v[0:3], v[136:139]\n"
      "v_mfma_f32_16x16x32_bf16 v[152:155], v[56:59], v[8:11], v[152:155]\nds_read_b128 v[0:3], v96 offset:4096\nv_mfma_f32_16x16x32_bf16 v[152:155], v[60:63], v[12:15], v[152:155]\n"
      "ds_read_b128 v[4:7], v97 offset:4096\nv_mfma_f32_16x16x32_bf16 v[148:151], v[52:55], v[12:15], v[148:151]\nv_mfma_f32_16x16x32_bf16 v[148:151], v[48:51], v[8:11], v[148:151]\n"
      "v_mfma_f32_16x16x32_bf16 v[144:147], v[40:43], v[8:11], v[144:147]\nv_mfma_f32_16x16x32_bf16 v[144:147], v[44:47], v[12:15], v[144:147]\n"
      "v_mfma_f32_16x16x32_bf16 v[140:143], v[36:39], v[12:15], v[140:143]\ns_waitcnt lgkmcnt(4)\nv_mfma_f32_16x16x32_bf16 v[140:143], v[32:35], v[8:11], v[140:143]\n"
      "v_mfma_f32_16x16x32_bf16 v[156:159], v[32:35], v[16:19], v[156:159]\nds_read_b128 v[8:11], v96 offset:6144\nv_mfma_f32_16x16x32_bf16 v[156:159], v[36:39], v[20:23], v[156:159]\n"
      "ds_read_b128 v[12:15], v97 offset:6144\nv_mfma_f32_16x16x32_bf16 v[160:163], v[44:47], v[20:23], v[160:163]\nv_mfma_f32_16x16x32_bf16 v[160:163], v[40:43], v[16:19], v[160:163]\n"
      "v_mfma_f32_16x16x32_bf16 v[164:167], v[48:51], v[16:19], v[164:167]\nv_mfma_f32_16x16x32_bf16 v[164:167], v[52:55], v[20:23], v[164:167]\n"
      "v_mfma_f32_16x16x32_bf16 v[168:171], v[60:63], v[20:23], v[168:171]\ns_waitcnt lgkmcnt(2)\nv_mfma_f32_16x16x32_bf16 v[168:171], v[56:59], v[16:19], v[168:171]\n"
      "v_mfma_f32_16x16x32_bf16 v[184:187], v[56:59], v[24:27], v[184:187]\nds_read_b128 v[16:19], v96 offset:8192\nv_mfma_f32_16x16x32_bf16 v[184:187], v[60:63], v[28:31], v[184:187]\n"
      "ds_read_b128 v[20:23], v97 offset:8192\nv_mfma_f32_16x16x32_bf16 v[180:183], v[52:55], v[28:31], v[180:183]\nv_mfma_f32_16x16x32_bf16 v[180:183], v[48:51], v[24:27], v[180:183]\n"
      "v_mfma_f32_16x16x32_bf16 v[176:179], v[40:43], v[24:27], v[176:179]\nv_mfma_f32_16x16x32_bf16 v[176:179], v[44:47], v[28:31], v[176:179]\ns_waitcnt vmcnt(0) lgkmcnt(0)\n"
      "v_mfma_f32_16x16x32_bf16 v[172:175], v[36:39], v[28:31], v[172:175]\ns_barrier\nv_mfma_f32_16x16x32_bf16 v[172:175], v[32:35], v[24:27], v[172:175]\n"
      "ds_read_b64_tr_b16 v[64:65], v104 offset:45056\nv_mfma_f32_16x16x32_bf16 v[188:191], v[32:35], v[0:3], v[188:191]\nds_read_b64_tr_b16 v[66:67], v104 offset:46080\n"
      "v_mfma_f32_16x16x32_bf16 v[188:191], v[36:39], v[4:7], v[188:191]\nds_read_b64_tr_b16 v[68:69], v104 offset:53248\nv_mfma_f32_16x16x32_bf16 v[192:195], v[44:47], v[4:7], v[192:195]\n"
      "ds_read_b64_tr_b16 v[70:71], v104 offset:54272\nv_mfma_f32_16x16x32_bf16 v[192:195], v[40:43], v[0:3], v[192:195]\nds_read_b64_tr_b16 v[72:73], v105 offset:45056\n"
      "v_mfma_f32_16x16x32_bf16 v[196:199], v[48:51], v[0:3], v[196:199]\nds_read_b64_tr_b16 v[74:75], v105 offset:46080\nv_mfma_f32_16x16x32_bf16 v[196:199], v[52:55], v[4:7], v[196:199]\n"
      "ds_read_b64_tr_b16 v[76:77], v105 offset:53248\nv_mfma_f32_16x16x32_bf16 v[200:203], v[60:63], v[4:7], v[200:203]\nds_read_b64_tr_b16 v[78:79], v105 offset:54272\n"
      "v_mfma_f32_16x16x32_bf16 v[200:203], v[56:59], v[0:3], v[200:203]\nds_read_b64_tr_b16 v[80:81], v106 offset:45056\nv_mfma_f32_16x16x32_bf16 v[216:219], v[56:59], v[8:11], v[216:219]\n"
      "ds_read_b128 v[0:3], v100 offset:45056\nv_mfma_f32_16x16x32_bf16 v[216:219], v[60:63], v[12:15], v[216:219]\nds_read_b128 v[4:7], v101 offset:45056\n"
      "v_mfma_f32_16x16x32_bf16 v[212:215], v[52:55], v[12:15], v[212:215]\nds_read_b64_tr_b16 v[82:83], v106 offset:46080\nv_mfma_f32_16x16x32_bf16 v[212:215], v[48:51], v[8:11], v[212:215]\n"
      "ds_read_b64_tr_b16 v[84:85], v106 offset:53248\nv_mfma_f32_16x16x32_bf16 v[208:211], v[40:43], v[8:11], v[208:211]\nds_read_b64_tr_b16 v[86:87], v106 offset:54272\n"
      "v_mfma_f32_16x16x32_bf16 v[208:211], v[44:47], v[12:15], v[208:211]\nds_read_b64_tr_b16 v[88:89], v107 offset:45056\nv_mfma_f32_16x16x32_bf16 v[204:207], v[36:39], v[12:15], v[204:207]\n"
      "ds_read_b64_tr_b16 v[90:91], v107 offset:46080\nv_mfma_f32_16x16x32_bf16 v[204:207], v[32:35], v[8:11], v[204:207]\nds_read_b64_tr_b16 v[92:93], v107 offset:53248\n"
      "v_mfma_f32_16x16x32_bf16 v[220:223], v[32:35], v[16:19], v[220:223]\nds_read_b128 v[8:11], v120 offset:45056\nv_mfma_f32_16x16x32_bf16 v[220:223], v[36:39], v[20:23], v[220:223]\n"
      "ds_read_b128 v[12:15], v121 offset:45056\nv_mfma_f32_16x16x32_bf16 v[224:227], v[44:47], v[20:23], v[224:227]\nds_read_b64_tr_b16 v[94:95], v107 offset:54272\n"
      "v_mfma_f32_16x16x32_bf16 v[224:227], v[40:43], v[16:19], v[224:227]\nv_mfma_f32_16x16x32_bf16 v[228:231], v[48:51], v[16:19], v[228:231]\ns_sub_u32 s58, s58, 1\ns_cmp_lg_u32 s58, 0\n"
      "v_mfma_f32_16x16x32_bf16 v[228:231], v[52:55], v[20:23], v[228:231]\nv_mfma_f32_16x16x32_bf16 v[232:235], v[60:63], v[20:23], v[232:235]\ns_waitcnt lgkmcnt(0)\n"
      "v_mfma_f32_16x16x32_bf16 v[232:235], v[56:59], v[16:19], v[232:235]\nds_read_b128 v[16:19], v96 offset:45056\ns_cbranch_scc1 .Lgl14\n"
      "v_mfma_f32_16x16x32_bf16 v[124:127], v[64:67], v[0:3], v[124:127]\nds_read_b128 v[20:23], v97 offset:45056\nv_cmp_eq_u32 s[62:63], 0, v240\n"
      "v_mfma_f32_16x16x32_bf16 v[124:127], v[68:71], v[4:7], v[124:127]\nds_read_b128 v[24:27], v96 offset:47104\nv_mov_b32 v253, 262144\n"
      "v_mfma_f32_16x16x32_bf16 v[128:131], v[76:79], v[4:7], v[128:131]\nds_read_b128 v[28:31], v97 offset:47104\nv_mov_b32 v248, 162048\n"
      "v_mfma_f32_16x16x32_bf16 v[128:131], v[72:75], v[0:3], v[128:131]\nv_mov_b32 v241, 1\nv_cndmask_b32 v253, v253, v248, s[62:63]\n"
      "v_mfma_f32_16x16x32_bf16 v[132:135], v[80:83], v[0:3], v[132:135]\nv_lshrrev_b32 v112, 4, v240\nv_bfe_u32 v113, v240, 2, 2\n"
      "v_mfma_f32_16x16x32_bf16 v[132:135], v[84:87], v[4:7], v[132:135]\nv_and_b32 v114, 3, v240\nv_mul_u32_u24 v114, 0x80008, v114\n"
      "v_mfma_f32_16x16x32_bf16 v[136:139], v[92:95], v[4:7], v[136:139]\nv_add_u32 v244, v112, v113\nv_and_b32 v244, 3, v244\nv_mfma_f32_16x16x32_bf16 v[136:139], v[88:91], v[0:3], v[136:139]\n"
      "v_add_u32 v116, 22, v113\nv_cmp_gt_u32 s[62:63], 2, v113\nv_mfma_f32_16x16x32_bf16 v[152:155], v[88:91], v[8:11], v[152:155]\nds_read_b128 v[0:3], v96 offset:49152\n"
      "v_and_b32 v113, 60, v240\nv_mfma_f32_16x16x32_bf16 v[152:155], v[92:95], v[12:15], v[152:155]\nds_read_b128 v[4:7], v97 offset:49152\ns_mov_b32 s42, 0\n"
      "v_mfma_f32_16x16x32_bf16 v[148:151], v[84:87], v[12:15], v[148:151]\nv_cndmask_b32 v116, v116, v244, s[62:63]\nv_lshl_or_b32 v244, v244, 16, v116\n"
      "v_mfma_f32_16x16x32_bf16 v[148:151], v[80:83], v[8:11], v[148:151]\nv_lshl_add_u32 v244, v244, 5, v114\nv_lshl_add_u32 v252, v240, 4, s42\n"
      "v_mfma_f32_16x16x32_bf16 v[144:147], v[72:75], v[8:11], v[144:147]\ns_mov_b32 s42, 0x76325410\ns_mov_b32 s43, 0x21005410\n"
      "v_mfma_f32_16x16x32_bf16 v[144:147], v[76:79], v[12:15], v[144:147]\nv_lshrrev_b64 v[116:117], v113, s[42:43]\nv_and_b32 v116, 7, v116\n"
      "v_mfma_f32_16x16x32_bf16 v[140:143], v[68:71], v[12:15], v[140:143]\ns_waitcnt lgkmcnt(4)\nv_lshl_add_u32 v116, v112, 3, v116\n"
      "v_mfma_f32_16x16x32_bf16 v[140:143], v[64:67], v[8:11], v[140:143]\ns_mov_b32 s42, 0x54107632\ns_mov_b32 s43, 0x21107632\n"
      "v_mfma_f32_16x16x32_bf16 v[156:159], v[64:67], v[16:19], v[156:159]\nds_read_b128 v[8:11], v96 offset:51200\nv_lshrrev_b64 v[118:119], v113, s[42:43]\n"
      "v_mfma_f32_16x16x32_bf16 v[156:159], v[68:71], v[20:23], v[156:159]\nds_read_b128 v[12:15], v97 offset:51200\nv_and_b32 v118, 7, v118\n"
      "v_mfma_f32_16x16x32_bf16 v[160:163], v[76:79], v[20:23], v[160:163]\nv_lshl_add_u32 v118, v112, 3, v118\nv_lshl_or_b32 v245, v118, 16, v116\n"
      "v_mfma_f32_16x16x32_bf16 v[160:163], v[72:75], v[16:19], v[160:163]\nv_lshl_add_u32 v245, v245, 5, v114\nv_and_b32 v113, 1, v240\n"
      "v_mfma_f32_16x16x32_bf16 v[164:167], v[80:83], v[16:19], v[164:167]\nv_mul_u32_u24 v113, 0xaaaa, v113\nv_add_u32 v242, 0x4444, v113\n"
      "v_mfma_f32_16x16x32_bf16 v[164:167], v[84:87], v[20:23], v[164:167]\ns_lshl_b32 s42, s36, 4\ns_add_u32 s42, s42, 160640\nv_mfma_f32_16x16x32_bf16 v[168:171], v[92:95], v[20:23], v[168:171]\n"
      "s_waitcnt lgkmcnt(2)\nv_lshl_add_u32 v243, v112, 2, s42\nv_mfma_f32_16x16x32_bf16 v[168:171], v[88:91], v[16:19], v[168:171]\nv_and_b32 v112, 1, v112\nv_lshl_add_u32 v112, v112, 3, s24\n"
      "v_mfma_f32_16x16x32_bf16 v[184:187], v[88:91], v[24:27], v[184:187]\nds_read_b128 v[16:19], v96 offset:53248\ns_sub_u32 s42, s64, 1\n"
      "v_mfma_f32_16x16x32_bf16 v[184:187], v[92:95], v[28:31], v[184:187]\nds_read_b128 v[20:23], v97 offset:53248\nv_min_u32 v112, s42, v112\n"
      "v_mfma_f32_16x16x32_bf16 v[180:183], v[84:87], v[28:31], v[180:183]\nv_lshlrev_b32 v112, 3, v112\nv_and_b32 v113, 15, v240\n"
      "v_mfma_f32_16x16x32_bf16 v[180:183], v[80:83], v[24:27], v[180:183]\nv_lshl_add_u32 v250, v112, 1, v113\ns_mov_b32 s42, 136192\n"
      "v_mfma_f32_16x16x32_bf16 v[176:179], v[72:75], v[24:27], v[176:179]\nv_lshl_add_u32 v250, v250, 2, s42\nv_min_u32 v113, 12, v113\n"
      "v_mfma_f32_16x16x32_bf16 v[176:179], v[76:79], v[28:31], v[176:179]\ns_waitcnt vmcnt(0) lgkmcnt(0)\ns_lshl_b32 s42, s30, 7\n"
      "v_mfma_f32_16x16x32_bf16 v[172:175], v[68:71], v[28:31], v[172:175]\ns_barrier\nv_lshl_add_u32 v113, v113, 15, s42\nv_mfma_f32_16x16x32_bf16 v[172:175], v[64:67], v[24:27], v[172:175]\n"
      "ds_read_b64_tr_b16 v[32:33], v108 offset:0\nv_add_u32 v249, v113, v112\nv_mfma_f32_16x16x32_bf16 v[188:191], v[64:67], v[0:3], v[188:191]\nds_read_b64_tr_b16 v[34:35], v108 offset:1024\n"
      "v_mfma_f32_16x16x32_bf16 v[188:191], v[68:71], v[4:7], v[188:191]\nds_read_b64_tr_b16 v[36:37], v108 offset:8192\nv_mfma_f32_16x16x32_bf16 v[192:195], v[76:79], v[4:7], v[192:195]\n"
      "ds_read_b64_tr_b16 v[38:39], v108 offset:9216\nv_mfma_f32_16x16x32_bf16 v[192:195], v[72:75], v[0:3], v[192:195]\nds_read_b64_tr_b16 v[40:41], v109 offset:0\n"
      "v_mfma_f32_16x16x32_bf16 v[196:199], v[80:83], v[0:3], v[196:199]\nds_read_b64_tr_b16 v[42:43], v109 offset:1024\nv_mfma_f32_16x16x32_bf16 v[196:199], v[84:87], v[4:7], v[196:199]\n"
      "ds_read_b64_tr_b16 v[44:45], v109 offset:8192\nv_mfma_f32_16x16x32_bf16 v[200:203], v[92:95], v[4:7], v[200:203]\nds_read_b64_tr_b16 v[46:47], v109 offset:9216\n"
      "v_mfma_f32_16x16x32_bf16 v[200:203], v[88:91], v[0:3], v[200:203]\nds_read_b64_tr_b16 v[48:49], v110 offset:0\nv_mfma_f32_16x16x32_bf16 v[216:219], v[88:91], v[8:11], v[216:219]\n"
      "ds_read_b128 v[0:3], v102 offset:0\nv_mfma_f32_16x16x32_bf16 v[216:219], v[92:95], v[12:15], v[216:219]\nds_read_b128 v[4:7], v103 offset:0\n"
      "v_mfma_f32_16x16x32_bf16 v[212:215], v[84:87], v[12:15], v[212:215]\nds_read_b64_tr_b16 v[50:51], v110 offset:1024\nv_mfma_f32_16x16x32_bf16 v[212:215], v[80:83], v[8:11], v[212:215]\n"
      "ds_read_b64_tr_b16 v[52:53], v110 offset:8192\nv_mfma_f32_16x16x32_bf16 v[208:211], v[72:75], v[8:11], v[208:211]\nds_read_b64_tr_b16 v[54:55], v110 offset:9216\n"
      "v_mfma_f32_16x16x32_bf16 v[208:211], v[76:79], v[12:15], v[208:211]\nds_read_b64_tr_b16 v[56:57], v111 offset:0\nv_mfma_f32_16x16x32_bf16 v[204:207], v[68:71], v[12:15], v[204:207]\n"
      "ds_read_b64_tr_b16 v[58:59], v111 offset:1024\nv_mfma_f32_16x16x32_bf16 v[204:207], v[64:67], v[8:11], v[204:207]\nds_read_b64_tr_b16 v[60:61], v111 offset:8192\n"
      "v_mfma_f32_16x16x32_bf16 v[220:223], v[64:67], v[16:19], v[220:223]\nds_read_b128 v[8:11], v122 offset:0\nv_mfma_f32_16x16x32_bf16 v[220:223], v[68:71], v[20:23], v[220:223]\n"
      "ds_read_b128 v[12:15], v123 offset:0\nv_mfma_f32_16x16x32_bf16 v[224:227], v[76:79], v[20:23], v[224:227]\nds_read_b64_tr_b16 v[62:63], v111 offset:9216\n"
      "v_mfma_f32_16x16x32_bf16 v[224:227], v[72:75], v[16:19], v[224:227]\nv_mfma_f32_16x16x32_bf16 v[228:231], v[80:83], v[16:19], v[228:231]\n"
      "v_mfma_f32_16x16x32_bf16 v[228:231], v[84:87], v[20:23], v[228:231]\nv_mfma_f32_16x16x32_bf16 v[232:235], v[92:95], v[20:23], v[232:235]\ns_waitcnt lgkmcnt(0)\n"
      "v_mfma_f32_16x16x32_bf16 v[232:235], v[88:91], v[16:19], v[232:235]\nds_read_b128 v[16:19], v98 offset:0\nv_mfma_f32_16x16x32_bf16 v[124:127], v[32:35], v[0:3], v[124:127]\n"
      "ds_read_b128 v[20:23], v99 offset:0\nv_mfma_f32_16x16x32_bf16 v[124:127], v[36:39], v[4:7], v[124:127]\nds_read_b128 v[24:27], v98 offset:2048\n"
      "v_mfma_f32_16x16x32_bf16 v[128:131], v[44:47], v[4:7], v[128:131]\nds_read_b128 v[28:31], v99 offset:2048\nv_mfma_f32_16x16x32_bf16 v[128:131], v[40:43], v[0:3], v[128:131]\n"
      "v_mfma_f32_16x16x32_bf16 v[132:135], v[48:51], v[0:3], v[132:135]\nv_mfma_f32_16x16x32_bf16 v[132:135], v[52:55], v[4:7], v[132:135]\n"
      "v_mfma_f32_16x16x32_bf16 v[136:139], v[60:63], v[4:7], v[136:139]\nv_mfma_f32_16x16x32_bf16 v[136:139], v[56:59], v[0:3], v[136:139]\n"
      "v_mfma_f32_16x16x32_bf16 v[152:155], v[56:59], v[8:11], v[152:155]\nds_read_b128 v[0:3], v98 offset:4096\nv_mfma_f32_16x16x32_bf16 v[152:155], v[60:63], v[12:15], v[152:155]\n"
      "ds_read_b128 v[4:7], v99 offset:4096\nv_mfma_f32_16x16x32_bf16 v[148:151], v[52:55], v[12:15], v[148:151]\nv_mfma_f32_16x16x32_bf16 v[148:151], v[48:51], v[8:11], v[148:151]\n"
      "v_mfma_f32_16x16x32_bf16 v[144:147], v[40:43], v[8:11], v[144:147]\nv_mfma_f32_16x16x32_bf16 v[144:147], v[44:47], v[12:15], v[144:147]\n"
      "v_mfma_f32_16x16x32_bf16 v[140:143], v[36:39], v[12:15], v[140:143]\ns_waitcnt lgkmcnt(4)\nv_mfma_f32_16x16x32_bf16 v[140:143], v[32:35], v[8:11], v[140:143]\n"
      "v_mfma_f32_16x16x32_bf16 v[156:159], v[32:35], v[16:19], v[156:159]\nds_read_b128 v[8:11], v98 offset:6144\nv_mfma_f32_16x16x32_bf16 v[156:159], v[36:39], v[20:23], v[156:159]\n"
      "ds_read_b128 v[12:15], v99 offset:6144\nv_mfma_f32_16x16x32_bf16 v[160:163], v[44:47], v[20:23], v[160:163]\nv_mfma_f32_16x16x32_bf16 v[160:163], v[40:43], v[16:19], v[160:163]\n"
      "v_mfma_f32_16x16x32_bf16 v[164:167], v[48:51], v[16:19], v[164:167]\nv_mfma_f32_16x16x32_bf16 v[164:167], v[52:55], v[20:23], v[164:167]\n"
      "v_mfma_f32_16x16x32_bf16 v[168:171], v[60:63], v[20:23], v[168:171]\ns_waitcnt lgkmcnt(2)\nv_mfma_f32_16x16x32_bf16 v[168:171], v[56:59], v[16:19], v[168:171]\n"
      "v_mfma_f32_16x16x32_bf16 v[184:187], v[56:59], v[24:27], v[184:187]\nds_read_b128 v[16:19], v98 offset:8192\nv_mfma_f32_16x16x32_bf16 v[184:187], v[60:63], v[28:31], v[184:187]\n"
      "ds_read_b128 v[20:23], v99 offset:8192\nv_mfma_f32_16x16x32_bf16 v[180:183], v[52:55], v[28:31], v[180:183]\nv_mfma_f32_16x16x32_bf16 v[180:183], v[48:51], v[24:27], v[180:183]\n"
      "v_mfma_f32_16x16x32_bf16 v[176:179], v[40:43], v[24:27], v[176:179]\nv_mfma_f32_16x16x32_bf16 v[176:179], v[44:47], v[28:31], v[176:179]\ns_waitcnt vmcnt(0) lgkmcnt(0)\n"
      "v_mfma_f32_16x16x32_bf16 v[172:175], v[36:39], v[28:31], v[172:175]\ns_barrier\nv_mfma_f32_16x16x32_bf16 v[172:175], v[32:35], v[24:27], v[172:175]\nds_read_b128 v[72:75], v252 offset:0\n"
      "v_lshl_add_u32 v246, v240, 1, v248\nv_mfma_f32_16x16x32_bf16 v[188:191], v[32:35], v[0:3], v[188:191]\nds_read_b128 v[24:27], v236 offset:512\nds_read_u16 v251, v246 offset:64\n"
      "v_mfma_f32_16x16x32_bf16 v[188:191], v[36:39], v[4:7], v[188:191]\nds_read_b128 v[28:31], v236 offset:576\nds_read_b32 v247, v248 offset:24\n"
      "v_mfma_f32_16x16x32_bf16 v[192:195], v[44:47], v[4:7], v[192:195]\nds_read_b128 v[76:79], v252 offset:1024\nv_and_b32 v246, 48, v240\n"
      "v_mfma_f32_16x16x32_bf16 v[192:195], v[40:43], v[0:3], v[192:195]\nds_read_b128 v[80:83], v252 offset:2048\ns_mov_b32 s42, 42240\n"
      "v_mfma_f32_16x16x32_bf16 v[196:199], v[48:51], v[0:3], v[196:199]\nds_read_b128 v[84:87], v252 offset:3072\nv_lshl_add_u32 v246, v246, 2, s42\n"
      "v_mfma_f32_16x16x32_bf16 v[196:199], v[52:55], v[4:7], v[196:199]\nds_read_b128 v[88:91], v252 offset:4096\ns_mov_b32 s58, 0xbdd2d3e7\n"
      "v_mfma_f32_16x16x32_bf16 v[200:203], v[60:63], v[4:7], v[200:203]\nds_read_b128 v[92:95], v252 offset:5120\ns_waitcnt lgkmcnt(4)\n"
      "v_mfma_f32_16x16x32_bf16 v[200:203], v[56:59], v[0:3], v[200:203]\nds_read_b128 v[96:99], v252 offset:6144\nv_readfirstlane_b32 s39, v247\n"
      "v_mfma_f32_16x16x32_bf16 v[216:219], v[56:59], v[8:11], v[216:219]\nds_read_b128 v[0:3], v236 offset:0\nv_readlane_b32 s55, v251, 0\n"
      "v_mfma_f32_16x16x32_bf16 v[216:219], v[60:63], v[12:15], v[216:219]\nds_read_b128 v[4:7], v236 offset:64\ns_setprio 3\nv_mfma_f32_16x16x32_bf16 v[212:215], v[52:55], v[12:15], v[212:215]\n"
      "ds_read_b128 v[100:103], v252 offset:7168\ns_xor_b32 s40, s39, 0xa5c30f96\nv_mfma_f32_16x16x32_bf16 v[212:215], v[48:51], v[8:11], v[212:215]\nds_read_b128 v[104:107], v252 offset:8192\n"
      "s_mul_i32 s41, s39, 0x9E3779B1\nv_mfma_f32_16x16x32_bf16 v[208:211], v[40:43], v[8:11], v[208:211]\nds_read_b128 v[108:111], v252 offset:9216\ns_lshr_b32 s41, s41, 24\n"
      "v_mfma_f32_16x16x32_bf16 v[208:211], v[44:47], v[12:15], v[208:211]\nds_read_b128 v[64:67], v237 offset:512\nv_mfma_f32_16x16x32_bf16 v[204:207], v[36:39], v[12:15], v[204:207]\n"
      "ds_read_b128 v[68:71], v237 offset:576\nv_mfma_f32_16x16x32_bf16 v[204:207], v[32:35], v[8:11], v[204:207]\nds_read_b128 v[112:115], v252 offset:10240\n"
      "v_mfma_f32_16x16x32_bf16 v[220:223], v[32:35], v[16:19], v[220:223]\nds_read_b128 v[8:11], v237 offset:0\nv_mfma_f32_16x16x32_bf16 v[220:223], v[36:39], v[20:23], v[220:223]\n"
      "ds_read_b128 v[12:15], v237 offset:64\nv_mfma_f32_16x16x32_bf16 v[224:227], v[44:47], v[20:23], v[224:227]\nds_read_b128 v[116:119], v252 offset:11264\n"
      "v_mfma_f32_16x16x32_bf16 v[224:227], v[40:43], v[16:19], v[224:227]\nds_read_b128 v[120:123], v252 offset:12288\n"
      "v_add_u32_sdwa v239, s49, v245 dst_sel:DWORD dst_unused:UNUSED_PAD src0_sel:DWORD src1_sel:WORD_1\nv_mfma_f32_16x16x32_bf16 v[228:231], v[48:51], v[16:19], v[228:231]\n"
      "v_add_u32_sdwa v236, s49, v244 dst_sel:DWORD dst_unused:UNUSED_PAD src0_sel:DWORD src1_sel:WORD_0\nv_mfma_f32_16x16x32_bf16 v[228:231], v[52:55], v[20:23], v[228:231]\n"
      "v_add_u32_sdwa v237, s49, v244 dst_sel:DWORD dst_unused:UNUSED_PAD src0_sel:DWORD src1_sel:WORD_1\nv_mfma_f32_16x16x32_bf16 v[232:235], v[60:63], v[20:23], v[232:235]\n"
      "v_add_u32_sdwa v238, s49, v245 dst_sel:DWORD dst_unused:UNUSED_PAD src0_sel:DWORD src1_sel:WORD_0\ns_waitcnt lgkmcnt(0)\nv_mfma_f32_16x16x32_bf16 v[232:235], v[56:59], v[16:19], v[232:235]\n"
      "v_pk_mul_f32 v[44:45], v[124:125], v[124:125]\nv_pk_mul_f32 v[46:47], v[126:127], v[126:127]\nv_pk_mul_f32 v[48:49], v[128:129], v[128:129]\nv_pk_mul_f32 v[50:51], v[130:131], v[130:131]\n"
      "v_pk_fma_f32 v[44:45], v[44:45], s[58:59], s[58:59] op_sel:[0,0,1] op_sel_hi:[1,0,1]\nv_pk_fma_f32 v[46:47], v[46:47], s[58:59], s[58:59] op_sel:[0,0,1] op_sel_hi:[1,0,1]\n"
      "v_pk_fma_f32 v[48:49], v[48:49], s[58:59], s[58:59] op_sel:[0,0,1] op_sel_hi:[1,0,1]\nv_pk_fma_f32 v[50:51], v[50:51], s[58:59], s[58:59] op_sel:[0,0,1] op_sel_hi:[1,0,1]\n"
      "v_pk_fma_f32 v[44:45], v[44:45], v[124:125], s[60:61] op_sel_hi:[1,1,0]\nv_pk_fma_f32 v[46:47], v[46:47], v[126:127], s[60:61] op_sel_hi:[1,1,0]\n"
      "v_pk_fma_f32 v[48:49], v[48:49], v[128:129], s[60:61] op_sel_hi:[1,1,0]\nv_pk_fma_f32 v[50:51], v[50:51], v[130:131], s[60:61] op_sel_hi:[1,1,0]\nv_exp_f32 v44, v44 clamp\n"
      "v_pk_mul_f32 v[52:53], v[132:133], v[132:133]\nv_exp_f32 v45, v45 clamp\nv_pk_mul_f32 v[54:55], v[134:135], v[134:135]\nv_pk_mul_f32 v[56:57], v[136:137], v[136:137]\n"
      "v_exp_f32 v46, v46 clamp\nv_pk_mul_f32 v[58:59], v[138:139], v[138:139]\nv_exp_f32 v47, v47 clamp\nv_pk_fma_f32 v[52:53], v[52:53], s[58:59], s[58:59] op_sel:[0,0,1] op_sel_hi:[1,0,1]\n"
      "v_pk_fma_f32 v[54:55], v[54:55], s[58:59], s[58:59] op_sel:[0,0,1] op_sel_hi:[1,0,1]\nv_exp_f32 v48, v48 clamp\n"
      "v_pk_fma_f32 v[56:57], v[56:57], s[58:59], s[58:59] op_sel:[0,0,1] op_sel_hi:[1,0,1]\nv_exp_f32 v49, v49 clamp\n"
      "v_pk_fma_f32 v[58:59], v[58:59], s[58:59], s[58:59] op_sel:[0,0,1] op_sel_hi:[1,0,1]\nv_pk_fma_f32 v[52:53], v[52:53], v[132:133], s[60:61] op_sel_hi:[1,1,0]\nv_exp_f32 v50, v50 clamp\n"
      "v_pk_fma_f32 v[54:55], v[54:55], v[134:135], s[60:61] op_sel_hi:[1,1,0]\nv_exp_f32 v51, v51 clamp\nv_pk_fma_f32 v[56:57], v[56:57], v[136:137], s[60:61] op_sel_hi:[1,1,0]\n"
      "v_pk_fma_f32 v[58:59], v[58:59], v[138:139], s[60:61] op_sel_hi:[1,1,0]\nv_exp_f32 v52, v52 clamp\nv_pk_fma_f32 v[46:47], v[46:47], v[2:3], v[26:27]\n"
      "v_pk_fma_f32 v[44:45], v[44:45], v[0:1], v[24:25]\nv_pk_fma_f32 v[48:49], v[48:49], v[4:5], v[28:29]\nv_exp_f32 v53, v53 clamp\nv_pk_fma_f32 v[50:51], v[50:51], v[6:7], v[30:31]\n"
      "v_pk_mul_f32 v[124:125], v[124:125], v[46:47]\nv_pk_mul_f32 v[128:129], v[128:129], v[50:51]\nv_exp_f32 v54, v54 clamp\nv_pk_fma_f32 v[124:125], v[126:127], v[44:45], v[124:125]\n"
      "v_pk_fma_f32 v[128:129], v[130:131], v[48:49], v[128:129]\nv_pk_mul_f32 v[44:45], v[44:45], v[46:47]\nv_exp_f32 v55, v55 clamp\nv_pk_mul_f32 v[48:49], v[48:49], v[50:51]\n"
      "v_pk_mul_f32 v[46:47], v[44:45], v[48:49]\nv_rcp_f32 v46, v46\nv_pk_mul_f32 v[124:125], v[124:125], v[48:49]\nv_rcp_f32 v47, v47\nv_pk_fma_f32 v[124:125], v[128:129], v[44:45], v[124:125]\n"
      "v_pk_mul_f32 v[32:33], v[124:125], v[46:47]\nv_pk_mul_f32 v[16:17], v[140:141], v[140:141]\nv_pk_mul_f32 v[18:19], v[142:143], v[142:143]\nv_exp_f32 v56, v56 clamp\n"
      "v_pk_mul_f32 v[20:21], v[144:145], v[144:145]\nv_pk_mul_f32 v[22:23], v[146:147], v[146:147]\nv_exp_f32 v57, v57 clamp\n"
      "v_pk_fma_f32 v[16:17], v[16:17], s[58:59], s[58:59] op_sel:[0,0,1] op_sel_hi:[1,0,1]\nv_pk_fma_f32 v[18:19], v[18:19], s[58:59], s[58:59] op_sel:[0,0,1] op_sel_hi:[1,0,1]\n"
      "v_pk_fma_f32 v[20:21], v[20:21], s[58:59], s[58:59] op_sel:[0,0,1] op_sel_hi:[1,0,1]\nv_exp_f32 v58, v58 clamp\n"
      "v_pk_fma_f32 v[22:23], v[22:23], s[58:59], s[58:59] op_sel:[0,0,1] op_sel_hi:[1,0,1]\nv_pk_fma_f32 v[16:17], v[16:17], v[140:141], s[60:61] op_sel_hi:[1,1,0]\n"
      "v_pk_fma_f32 v[18:19], v[18:19], v[142:143], s[60:61] op_sel_hi:[1,1,0]\nv_exp_f32 v59, v59 clamp\nv_pk_fma_f32 v[20:21], v[20:21], v[144:145], s[60:61] op_sel_hi:[1,1,0]\n"
      "v_pk_fma_f32 v[22:23], v[22:23], v[146:147], s[60:61] op_sel_hi:[1,1,0]\nv_exp_f32 v16, v16 clamp\nv_pk_fma_f32 v[54:55], v[54:55], v[10:11], v[66:67]\n"
      "v_pk_fma_f32 v[52:53], v[52:53], v[8:9], v[64:65]\nv_pk_fma_f32 v[56:57], v[56:57], v[12:13], v[68:69]\nv_exp_f32 v17, v17 clamp\nv_pk_fma_f32 v[58:59], v[58:59], v[14:15], v[70:71]\n"
      "v_pk_mul_f32 v[132:133], v[132:133], v[54:55]\nv_pk_mul_f32 v[136:137], v[136:137], v[58:59]\nv_exp_f32 v18, v18 clamp\nv_pk_fma_f32 v[132:133], v[134:135], v[52:53], v[132:133]\n"
      "v_pk_fma_f32 v[136:137], v[138:139], v[56:57], v[136:137]\nv_pk_mul_f32 v[52:53], v[52:53], v[54:55]\nv_exp_f32 v19, v19 clamp\nv_pk_mul_f32 v[56:57], v[56:57], v[58:59]\n"
      "v_pk_mul_f32 v[54:55], v[52:53], v[56:57]\nv_rcp_f32 v54, v54\nv_pk_mul_f32 v[132:133], v[132:133], v[56:57]\nv_rcp_f32 v55, v55\nv_pk_fma_f32 v[132:133], v[136:137], v[52:53], v[132:133]\n"
      "v_pk_fma_f32 v[32:33], v[132:133], v[54:55], v[32:33]\nv_add_f32 v42, v32, v33\nv_pk_mul_f32 v[44:45], v[148:149], v[148:149]\nv_pk_mul_f32 v[46:47], v[150:151], v[150:151]\n"
      "v_exp_f32 v20, v20 clamp\nv_pk_mul_f32 v[48:49], v[152:153], v[152:153]\nv_pk_mul_f32 v[50:51], v[154:155], v[154:155]\nv_exp_f32 v21, v21 clamp\n"
      "v_pk_fma_f32 v[44:45], v[44:45], s[58:59], s[58:59] op_sel:[0,0,1] op_sel_hi:[1,0,1]\nv_pk_fma_f32 v[46:47], v[46:47], s[58:59], s[58:59] op_sel:[0,0,1] op_sel_hi:[1,0,1]\n"
      "v_pk_fma_f32 v[48:49], v[48:49], s[58:59], s[58:59] op_sel:[0,0,1] op_sel_hi:[1,0,1]\nv_exp_f32 v22, v22 clamp\n"
      "v_pk_fma_f32 v[50:51], v[50:51], s[58:59], s[58:59] op_sel:[0,0,1] op_sel_hi:[1,0,1]\nv_pk_fma_f32 v[44:45], v[44:45], v[148:149], s[60:61] op_sel_hi:[1,1,0]\n"
      "v_pk_fma_f32 v[46:47], v[46:47], v[150:151], s[60:61] op_sel_hi:[1,1,0]\nv_exp_f32 v23, v23 clamp\nv_pk_fma_f32 v[48:49], v[48:49], v[152:153], s[60:61] op_sel_hi:[1,1,0]\n"
      "v_pk_fma_f32 v[50:51], v[50:51], v[154:155], s[60:61] op_sel_hi:[1,1,0]\nv_exp_f32 v44, v44 clamp\nv_pk_fma_f32 v[18:19], v[18:19], v[2:3], v[26:27]\n"
      "v_pk_fma_f32 v[16:17], v[16:17], v[0:1], v[24:25]\nv_pk_fma_f32 v[20:21], v[20:21], v[4:5], v[28:29]\nv_exp_f32 v45, v45 clamp\nv_pk_fma_f32 v[22:23], v[22:23], v[6:7], v[30:31]\n"
      "v_pk_mul_f32 v[140:141], v[140:141], v[18:19]\nv_pk_mul_f32 v[144:145], v[144:145], v[22:23]\nv_exp_f32 v46, v46 clamp\nv_pk_fma_f32 v[140:141], v[142:143], v[16:17], v[140:141]\n"
      "v_pk_fma_f32 v[144:145], v[146:147], v[20:21], v[144:145]\nv_pk_mul_f32 v[16:17], v[16:17], v[18:19]\nv_exp_f32 v47, v47 clamp\nv_pk_mul_f32 v[20:21], v[20:21], v[22:23]\n"
      "v_pk_mul_f32 v[18:19], v[16:17], v[20:21]\nv_rcp_f32 v18, v18\nv_pk_mul_f32 v[140:141], v[140:141], v[20:21]\nv_rcp_f32 v19, v19\nv_pk_fma_f32 v[140:141], v[144:145], v[16:17], v[140:141]\n"
      "v_pk_mul_f32 v[32:33], v[140:141], v[18:19]\nv_pk_mul_f32 v[52:53], v[156:157], v[156:157]\nv_pk_mul_f32 v[54:55], v[158:159], v[158:159]\nv_exp_f32 v48, v48 clamp\n"
      "v_pk_mul_f32 v[56:57], v[160:161], v[160:161]\nv_pk_mul_f32 v[58:59], v[162:163], v[162:163]\nv_exp_f32 v49, v49 clamp\n"
      "v_pk_fma_f32 v[52:53], v[52:53], s[58:59], s[58:59] op_sel:[0,0,1] op_sel_hi:[1,0,1]\nv_pk_fma_f32 v[54:55], v[54:55], s[58:59], s[58:59] op_sel:[0,0,1] op_sel_hi:[1,0,1]\n"
      "v_pk_fma_f32 v[56:57], v[56:57], s[58:59], s[58:59] op_sel:[0,0,1] op_sel_hi:[1,0,1]\nv_exp_f32 v50, v50 clamp\n"
      "v_pk_fma_f32 v[58:59], v[58:59], s[58:59], s[58:59] op_sel:[0,0,1] op_sel_hi:[1,0,1]\nv_pk_fma_f32 v[52:53], v[52:53], v[156:157], s[60:61] op_sel_hi:[1,1,0]\n"
      "v_pk_fma_f32 v[54:55], v[54:55], v[158:159], s[60:61] op_sel_hi:[1,1,0]\nv_exp_f32 v51, v51 clamp\nv_pk_fma_f32 v[56:57], v[56:57], v[160:161], s[60:61] op_sel_hi:[1,1,0]\n"
      "v_pk_fma_f32 v[58:59], v[58:59], v[162:163], s[60:61] op_sel_hi:[1,1,0]\nv_exp_f32 v52, v52 clamp\nv_pk_fma_f32 v[46:47], v[46:47], v[10:11], v[66:67]\n"
      "v_pk_fma_f32 v[44:45], v[44:45], v[8:9], v[64:65]\nv_pk_fma_f32 v[48:49], v[48:49], v[12:13], v[68:69]\nv_exp_f32 v53, v53 clamp\nv_pk_fma_f32 v[50:51], v[50:51], v[14:15], v[70:71]\n"
      "v_pk_mul_f32 v[148:149], v[148:149], v[46:47]\nv_pk_mul_f32 v[152:153], v[152:153], v[50:51]\nv_exp_f32 v54, v54 clamp\nv_pk_fma_f32 v[148:149], v[150:151], v[44:45], v[148:149]\n"
      "v_pk_fma_f32 v[152:153], v[154:155], v[48:49], v[152:153]\nv_pk_mul_f32 v[44:45], v[44:45], v[46:47]\nv_exp_f32 v55, v55 clamp\nv_pk_mul_f32 v[48:49], v[48:49], v[50:51]\n"
      "v_pk_mul_f32 v[46:47], v[44:45], v[48:49]\nv_rcp_f32 v46, v46\nv_pk_mul_f32 v[148:149], v[148:149], v[48:49]\nv_rcp_f32 v47, v47\nv_pk_fma_f32 v[148:149], v[152:153], v[44:45], v[148:149]\n"
      "v_pk_fma_f32 v[32:33], v[148:149], v[46:47], v[32:33]\nv_add_f32 v41, v32, v33\nv_pk_mul_f32 v[16:17], v[164:165], v[164:165]\nv_pk_mul_f32 v[18:19], v[166:167], v[166:167]\n"
      "v_exp_f32 v56, v56 clamp\nv_pk_mul_f32 v[20:21], v[168:169], v[168:169]\nv_pk_mul_f32 v[22:23], v[170:171], v[170:171]\nv_exp_f32 v57, v57 clamp\n"
      "v_pk_fma_f32 v[16:17], v[16:17], s[58:59], s[58:59] op_sel:[0,0,1] op_sel_hi:[1,0,1]\nv_pk_fma_f32 v[18:19], v[18:19], s[58:59], s[58:59] op_sel:[0,0,1] op_sel_hi:[1,0,1]\n"
      "v_pk_fma_f32 v[20:21], v[20:21], s[58:59], s[58:59] op_sel:[0,0,1] op_sel_hi:[1,0,1]\nv_exp_f32 v58, v58 clamp\n"
      "v_pk_fma_f32 v[22:23], v[22:23], s[58:59], s[58:59] op_sel:[0,0,1] op_sel_hi:[1,0,1]\nv_pk_fma_f32 v[16:17], v[16:17], v[164:165], s[60:61] op_sel_hi:[1,1,0]\n"
      "v_pk_fma_f32 v[18:19], v[18:19], v[166:167], s[60:61] op_sel_hi:[1,1,0]\nv_exp_f32 v59, v59 clamp\nv_pk_fma_f32 v[20:21], v[20:21], v[168:169], s[60:61] op_sel_hi:[1,1,0]\n"
      "v_pk_fma_f32 v[22:23], v[22:23], v[170:171], s[60:61] op_sel_hi:[1,1,0]\nv_exp_f32 v16, v16 clamp\nv_pk_fma_f32 v[54:55], v[54:55], v[2:3], v[26:27]\n"
      "v_pk_fma_f32 v[52:53], v[52:53], v[0:1], v[24:25]\nv_pk_fma_f32 v[56:57], v[56:57], v[4:5], v[28:29]\nv_exp_f32 v17, v17 clamp\nv_pk_fma_f32 v[58:59], v[58:59], v[6:7], v[30:31]\n"
      "v_pk_mul_f32 v[156:157], v[156:157], v[54:55]\nv_pk_mul_f32 v[160:161], v[160:161], v[58:59]\nv_exp_f32 v18, v18 clamp\nv_pk_fma_f32 v[156:157], v[158:159], v[52:53], v[156:157]\n"
      "v_pk_fma_f32 v[160:161], v[162:163], v[56:57], v[160:161]\nv_pk_mul_f32 v[52:53], v[52:53], v[54:55]\nv_exp_f32 v19, v19 clamp\nv_pk_mul_f32 v[56:57], v[56:57], v[58:59]\n"
      "v_pk_mul_f32 v[54:55], v[52:53], v[56:57]\nv_rcp_f32 v54, v54\nv_pk_mul_f32 v[156:157], v[156:157], v[56:57]\nv_rcp_f32 v55, v55\nv_pk_fma_f32 v[156:157], v[160:161], v[52:53], v[156:157]\n"
      "v_pk_mul_f32 v[32:33], v[156:157], v[54:55]\nv_pk_mul_f32 v[44:45], v[172:173], v[172:173]\nv_pk_mul_f32 v[46:47], v[174:175], v[174:175]\nv_exp_f32 v20, v20 clamp\n"
      "v_pk_mul_f32 v[48:49], v[176:177], v[176:177]\nv_pk_mul_f32 v[50:51], v[178:179], v[178:179]\nv_exp_f32 v21, v21 clamp\n"
      "v_pk_fma_f32 v[44:45], v[44:45], s[58:59], s[58:59] op_sel:[0,0,1] op_sel_hi:[1,0,1]\nv_pk_fma_f32 v[46:47], v[46:47], s[58:59], s[58:59] op_sel:[0,0,1] op_sel_hi:[1,0,1]\n"
      "v_pk_fma_f32 v[48:49], v[48:49], s[58:59], s[58:59] op_sel:[0,0,1] op_sel_hi:[1,0,1]\nv_exp_f32 v22, v22 clamp\n"
      "v_pk_fma_f32 v[50:51], v[50:51], s[58:59], s[58:59] op_sel:[0,0,1] op_sel_hi:[1,0,1]\nv_pk_fma_f32 v[44:45], v[44:45], v[172:173], s[60:61] op_sel_hi:[1,1,0]\n"
      "v_pk_fma_f32 v[46:47], v[46:47], v[174:175], s[60:61] op_sel_hi:[1,1,0]\nv_exp_f32 v23, v23 clamp\nv_pk_fma_f32 v[48:49], v[48:49], v[176:177], s[60:61] op_sel_hi:[1,1,0]\n"
      "v_pk_fma_f32 v[50:51], v[50:51], v[178:179], s[60:61] op_sel_hi:[1,1,0]\nv_exp_f32 v44, v44 clamp\nv_pk_fma_f32 v[18:19], v[18:19], v[10:11], v[66:67]\n"
      "v_pk_fma_f32 v[16:17], v[16:17], v[8:9], v[64:65]\nv_pk_fma_f32 v[20:21], v[20:21], v[12:13], v[68:69]\nv_exp_f32 v45, v45 clamp\nv_pk_fma_f32 v[22:23], v[22:23], v[14:15], v[70:71]\n"
      "v_pk_mul_f32 v[164:165], v[164:165], v[18:19]\nv_pk_mul_f32 v[168:169], v[168:169], v[22:23]\nv_exp_f32 v46, v46 clamp\nv_pk_fma_f32 v[164:165], v[166:167], v[16:17], v[164:165]\n"
      "v_pk_fma_f32 v[168:169], v[170:171], v[20:21], v[168:169]\nv_pk_mul_f32 v[16:17], v[16:17], v[18:19]\nv_exp_f32 v47, v47 clamp\nv_pk_mul_f32 v[20:21], v[20:21], v[22:23]\n"
      "v_pk_mul_f32 v[18:19], v[16:17], v[20:21]\nv_rcp_f32 v18, v18\nv_pk_mul_f32 v[164:165], v[164:165], v[20:21]\nv_rcp_f32 v19, v19\nv_pk_fma_f32 v[164:165], v[168:169], v[16:17], v[164:165]\n"
      "v_pk_fma_f32 v[32:33], v[164:165], v[18:19], v[32:33]\nv_add_f32 v36, v32, v33\nv_pk_mul_f32 v[52:53], v[180:181], v[180:181]\nv_pk_mul_f32 v[54:55], v[182:183], v[182:183]\n"
      "v_exp_f32 v48, v48 clamp\nv_pk_mul_f32 v[56:57], v[184:185], v[184:185]\nv_pk_mul_f32 v[58:59], v[186:187], v[186:187]\nv_exp_f32 v49, v49 clamp\n"
      "v_pk_fma_f32 v[52:53], v[52:53], s[58:59], s[58:59] op_sel:[0,0,1] op_sel_hi:[1,0,1]\nv_pk_fma_f32 v[54:55], v[54:55], s[58:59], s[58:59] op_sel:[0,0,1] op_sel_hi:[1,0,1]\n"
      "v_pk_fma_f32 v[56:57], v[56:57], s[58:59], s[58:59] op_sel:[0,0,1] op_sel_hi:[1,0,1]\nv_exp_f32 v50, v50 clamp\n"
      "v_pk_fma_f32 v[58:59], v[58:59], s[58:59], s[58:59] op_sel:[0,0,1] op_sel_hi:[1,0,1]\nv_pk_fma_f32 v[52:53], v[52:53], v[180:181], s[60:61] op_sel_hi:[1,1,0]\n"
      "v_pk_fma_f32 v[54:55], v[54:55], v[182:183], s[60:61] op_sel_hi:[1,1,0]\nv_exp_f32 v51, v51 clamp\nv_pk_fma_f32 v[56:57], v[56:57], v[184:185], s[60:61] op_sel_hi:[1,1,0]\n"
      "v_pk_fma_f32 v[58:59], v[58:59], v[186:187], s[60:61] op_sel_hi:[1,1,0]\nv_exp_f32 v52, v52 clamp\nv_pk_fma_f32 v[46:47], v[46:47], v[2:3], v[26:27]\n"
      "v_pk_fma_f32 v[44:45], v[44:45], v[0:1], v[24:25]\nv_pk_fma_f32 v[48:49], v[48:49], v[4:5], v[28:29]\nv_exp_f32 v53, v53 clamp\nv_pk_fma_f32 v[50:51], v[50:51], v[6:7], v[30:31]\n"
      "v_pk_mul_f32 v[172:173], v[172:173], v[46:47]\nv_pk_mul_f32 v[176:177], v[176:177], v[50:51]\nv_exp_f32 v54, v54 clamp\nv_pk_fma_f32 v[172:173], v[174:175], v[44:45], v[172:173]\n"
      "v_pk_fma_f32 v[176:177], v[178:179], v[48:49], v[176:177]\nv_pk_mul_f32 v[44:45], v[44:45], v[46:47]\nv_exp_f32 v55, v55 clamp\nv_pk_mul_f32 v[48:49], v[48:49], v[50:51]\n"
      "v_pk_mul_f32 v[46:47], v[44:45], v[48:49]\nv_rcp_f32 v46, v46\nv_pk_mul_f32 v[172:173], v[172:173], v[48:49]\nv_rcp_f32 v47, v47\nv_pk_fma_f32 v[172:173], v[176:177], v[44:45], v[172:173]\n"
      "v_pk_mul_f32 v[32:33], v[172:173], v[46:47]\nv_pk_mul_f32 v[16:17], v[188:189], v[188:189]\nv_pk_mul_f32 v[18:19], v[190:191], v[190:191]\nv_exp_f32 v56, v56 clamp\n"
      "v_pk_mul_f32 v[20:21], v[192:193], v[192:193]\nv_pk_mul_f32 v[22:23], v[194:195], v[194:195]\nv_exp_f32 v57, v57 clamp\n"
      "v_pk_fma_f32 v[16:17], v[16:17], s[58:59], s[58:59] op_sel:[0,0,1] op_sel_hi:[1,0,1]\nv_pk_fma_f32 v[18:19], v[18:19], s[58:59], s[58:59] op_sel:[0,0,1] op_sel_hi:[1,0,1]\n"
      "v_pk_fma_f32 v[20:21], v[20:21], s[58:59], s[58:59] op_sel:[0,0,1] op_sel_hi:[1,0,1]\nv_exp_f32 v58, v58 clamp\n"
      "v_pk_fma_f32 v[22:23], v[22:23], s[58:59], s[58:59] op_sel:[0,0,1] op_sel_hi:[1,0,1]\nv_pk_fma_f32 v[16:17], v[16:17], v[188:189], s[60:61] op_sel_hi:[1,1,0]\n"
      "v_pk_fma_f32 v[18:19], v[18:19], v[190:191], s[60:61] op_sel_hi:[1,1,0]\nv_exp_f32 v59, v59 clamp\nv_pk_fma_f32 v[20:21], v[20:21], v[192:193], s[60:61] op_sel_hi:[1,1,0]\n"
      "v_pk_fma_f32 v[22:23], v[22:23], v[194:195], s[60:61] op_sel_hi:[1,1,0]\nv_exp_f32 v16, v16 clamp\nv_pk_fma_f32 v[54:55], v[54:55], v[10:11], v[66:67]\n"
      "v_pk_fma_f32 v[52:53], v[52:53], v[8:9], v[64:65]\nv_pk_fma_f32 v[56:57], v[56:57], v[12:13], v[68:69]\nv_exp_f32 v17, v17 clamp\nv_pk_fma_f32 v[58:59], v[58:59], v[14:15], v[70:71]\n"
      "v_pk_mul_f32 v[180:181], v[180:181], v[54:55]\nv_pk_mul_f32 v[184:185], v[184:185], v[58:59]\nv_exp_f32 v18, v18 clamp\nv_pk_fma_f32 v[180:181], v[182:183], v[52:53], v[180:181]\n"
      "v_pk_fma_f32 v[184:185], v[186:187], v[56:57], v[184:185]\nv_pk_mul_f32 v[52:53], v[52:53], v[54:55]\nv_exp_f32 v19, v19 clamp\nv_pk_mul_f32 v[56:57], v[56:57], v[58:59]\n"
      "v_pk_mul_f32 v[54:55], v[52:53], v[56:57]\nv_rcp_f32 v54, v54\nv_pk_mul_f32 v[180:181], v[180:181], v[56:57]\nv_rcp_f32 v55, v55\nv_pk_fma_f32 v[180:181], v[184:185], v[52:53], v[180:181]\n"
      "v_pk_fma_f32 v[32:33], v[180:181], v[54:55], v[32:33]\nv_add_f32 v37, v32, v33\nv_pk_mul_f32 v[44:45], v[196:197], v[196:197]\nv_pk_mul_f32 v[46:47], v[198:199], v[198:199]\n"
      "v_exp_f32 v20, v20 clamp\nv_pk_mul_f32 v[48:49], v[200:201], v[200:201]\nv_pk_mul_f32 v[50:51], v[202:203], v[202:203]\nv_exp_f32 v21, v21 clamp\n"
      "v_pk_fma_f32 v[44:45], v[44:45], s[58:59], s[58:59] op_sel:[0,0,1] op_sel_hi:[1,0,1]\nv_pk_fma_f32 v[46:47], v[46:47], s[58:59], s[58:59] op_sel:[0,0,1] op_sel_hi:[1,0,1]\n"
      "v_pk_fma_f32 v[48:49], v[48:49], s[58:59], s[58:59] op_sel:[0,0,1] op_sel_hi:[1,0,1]\nv_exp_f32 v22, v22 clamp\n"
      "v_pk_fma_f32 v[50:51], v[50:51], s[58:59], s[58:59] op_sel:[0,0,1] op_sel_hi:[1,0,1]\nv_pk_fma_f32 v[44:45], v[44:45], v[196:197], s[60:61] op_sel_hi:[1,1,0]\n"
      "v_pk_fma_f32 v[46:47], v[46:47], v[198:199], s[60:61] op_sel_hi:[1,1,0]\nv_exp_f32 v23, v23 clamp\nv_pk_fma_f32 v[48:49], v[48:49], v[200:201], s[60:61] op_sel_hi:[1,1,0]\n"
      "v_pk_fma_f32 v[50:51], v[50:51], v[202:203], s[60:61] op_sel_hi:[1,1,0]\nv_exp_f32 v44, v44 clamp\nv_pk_fma_f32 v[18:19], v[18:19], v[2:3], v[26:27]\n"
      "v_pk_fma_f32 v[16:17], v[16:17], v[0:1], v[24:25]\nv_pk_fma_f32 v[20:21], v[20:21], v[4:5], v[28:29]\nv_exp_f32 v45, v45 clamp\nv_pk_fma_f32 v[22:23], v[22:23], v[6:7], v[30:31]\n"
      "v_pk_mul_f32 v[188:189], v[188:189], v[18:19]\nv_pk_mul_f32 v[192:193], v[192:193], v[22:23]\nv_exp_f32 v46, v46 clamp\nv_pk_fma_f32 v[188:189], v[190:191], v[16:17], v[188:189]\n"
      "v_pk_fma_f32 v[192:193], v[194:195], v[20:21], v[192:193]\nv_pk_mul_f32 v[16:17], v[16:17], v[18:19]\nv_exp_f32 v47, v47 clamp\nv_pk_mul_f32 v[20:21], v[20:21], v[22:23]\n"
      "v_pk_mul_f32 v[18:19], v[16:17], v[20:21]\nv_rcp_f32 v18, v18\nv_pk_mul_f32 v[188:189], v[188:189], v[20:21]\nv_rcp_f32 v19, v19\nv_pk_fma_f32 v[188:189], v[192:193], v[16:17], v[188:189]\n"
      "v_pk_mul_f32 v[32:33], v[188:189], v[18:19]\nv_pk_mul_f32 v[52:53], v[204:205], v[204:205]\nv_pk_mul_f32 v[54:55], v[206:207], v[206:207]\nv_exp_f32 v48, v48 clamp\n"
      "v_pk_mul_f32 v[56:57], v[208:209], v[208:209]\nv_pk_mul_f32 v[58:59], v[210:211], v[210:211]\nv_exp_f32 v49, v49 clamp\n"
      "v_pk_fma_f32 v[52:53], v[52:53], s[58:59], s[58:59] op_sel:[0,0,1] op_sel_hi:[1,0,1]\nv_pk_fma_f32 v[54:55], v[54:55], s[58:59], s[58:59] op_sel:[0,0,1] op_sel_hi:[1,0,1]\n"
      "v_pk_fma_f32 v[56:57], v[56:57], s[58:59], s[58:59] op_sel:[0,0,1] op_sel_hi:[1,0,1]\nv_exp_f32 v50, v50 clamp\n"
      "v_pk_fma_f32 v[58:59], v[58:59], s[58:59], s[58:59] op_sel:[0,0,1] op_sel_hi:[1,0,1]\nv_pk_fma_f32 v[52:53], v[52:53], v[204:205], s[60:61] op_sel_hi:[1,1,0]\n"
      "v_pk_fma_f32 v[54:55], v[54:55], v[206:207], s[60:61] op_sel_hi:[1,1,0]\nv_exp_f32 v51, v51 clamp\nv_pk_fma_f32 v[56:57], v[56:57], v[208:209], s[60:61] op_sel_hi:[1,1,0]\n"
      "v_pk_fma_f32 v[58:59], v[58:59], v[210:211], s[60:61] op_sel_hi:[1,1,0]\nv_exp_f32 v52, v52 clamp\nv_pk_fma_f32 v[46:47], v[46:47], v[10:11], v[66:67]\n"
      "v_pk_fma_f32 v[44:45], v[44:45], v[8:9], v[64:65]\nv_pk_fma_f32 v[48:49], v[48:49], v[12:13], v[68:69]\nv_exp_f32 v53, v53 clamp\nv_pk_fma_f32 v[50:51], v[50:51], v[14:15], v[70:71]\n"
      "v_pk_mul_f32 v[196:197], v[196:197], v[46:47]\nv_pk_mul_f32 v[200:201], v[200:201], v[50:51]\nv_exp_f32 v54, v54 clamp\nv_pk_fma_f32 v[196:197], v[198:199], v[44:45], v[196:197]\n"
      "v_pk_fma_f32 v[200:201], v[202:203], v[48:49], v[200:201]\nv_pk_mul_f32 v[44:45], v[44:45], v[46:47]\nv_exp_f32 v55, v55 clamp\nv_pk_mul_f32 v[48:49], v[48:49], v[50:51]\n"
      "v_pk_mul_f32 v[46:47], v[44:45], v[48:49]\nv_rcp_f32 v46, v46\nv_pk_mul_f32 v[196:197], v[196:197], v[48:49]\nv_rcp_f32 v47, v47\nv_pk_fma_f32 v[196:197], v[200:201], v[44:45], v[196:197]\n"
      "v_pk_fma_f32 v[32:33], v[196:197], v[46:47], v[32:33]\nv_add_f32 v38, v32, v33\nv_pk_mul_f32 v[16:17], v[212:213], v[212:213]\nv_pk_mul_f32 v[18:19], v[214:215], v[214:215]\n"
      "v_exp_f32 v56, v56 clamp\nv_pk_mul_f32 v[20:21], v[216:217], v[216:217]\nv_pk_mul_f32 v[22:23], v[218:219], v[218:219]\nv_exp_f32 v57, v57 clamp\n"
      "v_pk_fma_f32 v[16:17], v[16:17], s[58:59], s[58:59] op_sel:[0,0,1] op_sel_hi:[1,0,1]\nv_pk_fma_f32 v[18:19], v[18:19], s[58:59], s[58:59] op_sel:[0,0,1] op_sel_hi:[1,0,1]\n"
      "v_pk_fma_f32 v[20:21], v[20:21], s[58:59], s[58:59] op_sel:[0,0,1] op_sel_hi:[1,0,1]\nv_exp_f32 v58, v58 clamp\n"
      "v_pk_fma_f32 v[22:23], v[22:23], s[58:59], s[58:59] op_sel:[0,0,1] op_sel_hi:[1,0,1]\nv_pk_fma_f32 v[16:17], v[16:17], v[212:213], s[60:61] op_sel_hi:[1,1,0]\n"
      "v_pk_fma_f32 v[18:19], v[18:19], v[214:215], s[60:61] op_sel_hi:[1,1,0]\nv_exp_f32 v59, v59 clamp\nv_pk_fma_f32 v[20:21], v[20:21], v[216:217], s[60:61] op_sel_hi:[1,1,0]\n"
      "v_pk_fma_f32 v[22:23], v[22:23], v[218:219], s[60:61] op_sel_hi:[1,1,0]\nv_exp_f32 v16, v16 clamp\nv_pk_fma_f32 v[54:55], v[54:55], v[2:3], v[26:27]\n"
      "v_pk_fma_f32 v[52:53], v[52:53], v[0:1], v[24:25]\nv_pk_fma_f32 v[56:57], v[56:57], v[4:5], v[28:29]\nv_exp_f32 v17, v17 clamp\nv_pk_fma_f32 v[58:59], v[58:59], v[6:7], v[30:31]\n"
      "v_pk_mul_f32 v[204:205], v[204:205], v[54:55]\nv_pk_mul_f32 v[208:209], v[208:209], v[58:59]\nv_exp_f32 v18, v18 clamp\nv_pk_fma_f32 v[204:205], v[206:207], v[52:53], v[204:205]\n"
      "v_pk_fma_f32 v[208:209], v[210:211], v[56:57], v[208:209]\nv_pk_mul_f32 v[52:53], v[52:53], v[54:55]\nv_exp_f32 v19, v19 clamp\nv_pk_mul_f32 v[56:57], v[56:57], v[58:59]\n"
      "v_pk_mul_f32 v[54:55], v[52:53], v[56:57]\nv_rcp_f32 v54, v54\nv_pk_mul_f32 v[204:205], v[204:205], v[56:57]\nv_rcp_f32 v55, v55\nv_pk_fma_f32 v[204:205], v[208:209], v[52:53], v[204:205]\n"
      "v_pk_mul_f32 v[32:33], v[204:205], v[54:55]\nv_pk_mul_f32 v[44:45], v[220:221], v[220:221]\nv_pk_mul_f32 v[46:47], v[222:223], v[222:223]\nv_exp_f32 v20, v20 clamp\n"
      "v_pk_mul_f32 v[48:49], v[224:225], v[224:225]\nv_pk_mul_f32 v[50:51], v[226:227], v[226:227]\nv_exp_f32 v21, v21 clamp\n"
      "v_pk_fma_f32 v[44:45], v[44:45], s[58:59], s[58:59] op_sel:[0,0,1] op_sel_hi:[1,0,1]\nv_pk_fma_f32 v[46:47], v[46:47], s[58:59], s[58:59] op_sel:[0,0,1] op_sel_hi:[1,0,1]\n"
      "v_pk_fma_f32 v[48:49], v[48:49], s[58:59], s[58:59] op_sel:[0,0,1] op_sel_hi:[1,0,1]\nv_exp_f32 v22, v22 clamp\n"
      "v_pk_fma_f32 v[50:51], v[50:51], s[58:59], s[58:59] op_sel:[0,0,1] op_sel_hi:[1,0,1]\nv_pk_fma_f32 v[44:45], v[44:45], v[220:221], s[60:61] op_sel_hi:[1,1,0]\n"
      "v_pk_fma_f32 v[46:47], v[46:47], v[222:223], s[60:61] op_sel_hi:[1,1,0]\nv_exp_f32 v23, v23 clamp\nv_pk_fma_f32 v[48:49], v[48:49], v[224:225], s[60:61] op_sel_hi:[1,1,0]\n"
      "v_pk_fma_f32 v[50:51], v[50:51], v[226:227], s[60:61] op_sel_hi:[1,1,0]\nv_exp_f32 v44, v44 clamp\nv_pk_fma_f32 v[18:19], v[18:19], v[10:11], v[66:67]\n"
      "v_pk_fma_f32 v[16:17], v[16:17], v[8:9], v[64:65]\nv_pk_fma_f32 v[20:21], v[20:21], v[12:13], v[68:69]\nv_exp_f32 v45, v45 clamp\nv_pk_fma_f32 v[22:23], v[22:23], v[14:15], v[70:71]\n"
      "v_pk_mul_f32 v[212:213], v[212:213], v[18:19]\nv_pk_mul_f32 v[216:217], v[216:217], v[22:23]\nv_exp_f32 v46, v46 clamp\nv_pk_fma_f32 v[212:213], v[214:215], v[16:17], v[212:213]\n"
      "v_pk_fma_f32 v[216:217], v[218:219], v[20:21], v[216:217]\nv_pk_mul_f32 v[16:17], v[16:17], v[18:19]\nv_exp_f32 v47, v47 clamp\nv_pk_mul_f32 v[20:21], v[20:21], v[22:23]\n"
      "v_pk_mul_f32 v[18:19], v[16:17], v[20:21]\nv_rcp_f32 v18, v18\nv_pk_mul_f32 v[212:213], v[212:213], v[20:21]\nv_rcp_f32 v19, v19\nv_pk_fma_f32 v[212:213], v[216:217], v[16:17], v[212:213]\n"
      "v_pk_fma_f32 v[32:33], v[212:213], v[18:19], v[32:33]\nv_add_f32 v39, v32, v33\nv_pk_mul_f32 v[52:53], v[228:229], v[228:229]\nv_pk_mul_f32 v[54:55], v[230:231], v[230:231]\n"
      "v_exp_f32 v48, v48 clamp\nv_pk_mul_f32 v[56:57], v[232:233], v[232:233]\nv_pk_mul_f32 v[58:59], v[234:235], v[234:235]\nv_exp_f32 v49, v49 clamp\n"
      "v_pk_fma_f32 v[52:53], v[52:53], s[58:59], s[58:59] op_sel:[0,0,1] op_sel_hi:[1,0,1]\nv_pk_fma_f32 v[54:55], v[54:55], s[58:59], s[58:59] op_sel:[0,0,1] op_sel_hi:[1,0,1]\n"
      "v_pk_fma_f32 v[56:57], v[56:57], s[58:59], s[58:59] op_sel:[0,0,1] op_sel_hi:[1,0,1]\nv_exp_f32 v50, v50 clamp\n"
      "v_pk_fma_f32 v[58:59], v[58:59], s[58:59], s[58:59] op_sel:[0,0,1] op_sel_hi:[1,0,1]\nv_pk_fma_f32 v[52:53], v[52:53], v[228:229], s[60:61] op_sel_hi:[1,1,0]\n"
      "v_pk_fma_f32 v[54:55], v[54:55], v[230:231], s[60:61] op_sel_hi:[1,1,0]\nv_exp_f32 v51, v51 clamp\nv_pk_fma_f32 v[56:57], v[56:57], v[232:233], s[60:61] op_sel_hi:[1,1,0]\n"
      "v_pk_fma_f32 v[58:59], v[58:59], v[234:235], s[60:61] op_sel_hi:[1,1,0]\nv_exp_f32 v52, v52 clamp\nv_pk_fma_f32 v[46:47], v[46:47], v[2:3], v[26:27]\nv_exp_f32 v53, v53 clamp\n"
      "v_pk_fma_f32 v[44:45], v[44:45], v[0:1], v[24:25]\nv_pk_fma_f32 v[48:49], v[48:49], v[4:5], v[28:29]\nv_exp_f32 v54, v54 clamp\nv_pk_fma_f32 v[50:51], v[50:51], v[6:7], v[30:31]\n"
      "v_pk_mul_f32 v[220:221], v[220:221], v[46:47]\nv_exp_f32 v55, v55 clamp\nv_pk_mul_f32 v[224:225], v[224:225], v[50:51]\nv_pk_fma_f32 v[220:221], v[222:223], v[44:45], v[220:221]\n"
      "v_exp_f32 v56, v56 clamp\nv_pk_fma_f32 v[224:225], v[226:227], v[48:49], v[224:225]\nv_exp_f32 v57, v57 clamp\nv_pk_mul_f32 v[44:45], v[44:45], v[46:47]\n"
      "v_pk_mul_f32 v[48:49], v[48:49], v[50:51]\nv_exp_f32 v58, v58 clamp\nv_pk_mul_f32 v[46:47], v[44:45], v[48:49]\nv_rcp_f32 v46, v46\nv_pk_mul_f32 v[220:221], v[220:221], v[48:49]\n"
      "v_exp_f32 v59, v59 clamp\nv_rcp_f32 v47, v47\nv_pk_fma_f32 v[220:221], v[224:225], v[44:45], v[220:221]\nv_pk_mul_f32 v[32:33], v[220:221], v[46:47]\n"
      "v_pk_fma_f32 v[54:55], v[54:55], v[10:11], v[66:67]\nv_pk_fma_f32 v[52:53], v[52:53], v[8:9], v[64:65]\nv_pk_fma_f32 v[56:57], v[56:57], v[12:13], v[68:69]\n"
      "v_pk_fma_f32 v[58:59], v[58:59], v[14:15], v[70:71]\nv_pk_mul_f32 v[228:229], v[228:229], v[54:55]\nv_pk_mul_f32 v[232:233], v[232:233], v[58:59]\n"
      "v_pk_fma_f32 v[228:229], v[230:231], v[52:53], v[228:229]\nv_pk_fma_f32 v[232:233], v[234:235], v[56:57], v[232:233]\nv_pk_mul_f32 v[52:53], v[52:53], v[54:55]\n"
      "v_pk_mul_f32 v[56:57], v[56:57], v[58:59]\nv_pk_mul_f32 v[54:55], v[52:53], v[56:57]\nv_rcp_f32 v54, v54\nv_pk_mul_f32 v[228:229], v[228:229], v[56:57]\nv_rcp_f32 v55, v55\n"
      "v_pk_fma_f32 v[228:229], v[232:233], v[52:53], v[228:229]\nv_pk_fma_f32 v[32:33], v[228:229], v[54:55], v[32:33]\nv_add_f32 v40, v32, v33\ns_barrier\ns_branch .Lge\n"
      ".Lg12:\ns_waitcnt vmcnt(4)\ns_barrier\nds_read_b64_tr_b16 v[64:65], v104 offset:45056\nds_read_b64_tr_b16 v[66:67], v104 offset:46080\nds_read_b64_tr_b16 v[68:69], v104 offset:53248\n"
      "ds_read_b64_tr_b16 v[70:71], v104 offset:54272\nds_read_b64_tr_b16 v[72:73], v105 offset:45056\nds_read_b64_tr_b16 v[74:75], v105 offset:46080\n"
      "ds_read_b64_tr_b16 v[76:77], v105 offset:53248\nds_read_b64_tr_b16 v[78:79], v105 offset:54272\nds_read_b64_tr_b16 v[80:81], v106 offset:45056\n"
      "ds_read_b64_tr_b16 v[82:83], v106 offset:46080\nds_read_b64_tr_b16 v[84:85], v106 offset:53248\nds_read_b64_tr_b16 v[86:87], v106 offset:54272\n"
      "ds_read_b64_tr_b16 v[88:89], v107 offset:45056\nds_read_b64_tr_b16 v[90:91], v107 offset:46080\nds_read_b64_tr_b16 v[92:93], v107 offset:53248\n"
      "ds_read_b64_tr_b16 v[94:95], v107 offset:54272\nds_read_b128 v[8:11], v120 offset:45056\nds_read_b128 v[12:15], v121 offset:45056\nds_read_b128 v[16:19], v96 offset:45056\n"
      "s_waitcnt lgkmcnt(1)\n.p2align 3\n"
      ".Lgl12:\nv_mfma_f32_16x16x32_bf16 v[152:155], v[88:91], v[8:11], v[152:155]\nds_read_b128 v[20:23], v97 offset:45056\nv_mfma_f32_16x16x32_bf16 v[152:155], v[92:95], v[12:15], v[152:155]\n"
      "ds_read_b128 v[24:27], v96 offset:47104\nv_mfma_f32_16x16x32_bf16 v[148:151], v[84:87], v[12:15], v[148:151]\nds_read_b128 v[28:31], v97 offset:47104\nds_read_b128 v[0:3], v96 offset:49152\n"
      "v_mfma_f32_16x16x32_bf16 v[148:151], v[80:83], v[8:11], v[148:151]\nds_read_b128 v[4:7], v97 offset:49152\nv_mfma_f32_16x16x32_bf16 v[144:147], v[72:75], v[8:11], v[144:147]\n"
      "v_mfma_f32_16x16x32_bf16 v[144:147], v[76:79], v[12:15], v[144:147]\nv_mfma_f32_16x16x32_bf16 v[140:143], v[68:71], v[12:15], v[140:143]\ns_waitcnt lgkmcnt(4)\n"
      "v_mfma_f32_16x16x32_bf16 v[140:143], v[64:67], v[8:11], v[140:143]\nv_mfma_f32_16x16x32_bf16 v[156:159], v[64:67], v[16:19], v[156:159]\nds_read_b128 v[8:11], v96 offset:51200\n"
      "v_mfma_f32_16x16x32_bf16 v[156:159], v[68:71], v[20:23], v[156:159]\nds_read_b128 v[12:15], v97 offset:51200\nv_mfma_f32_16x16x32_bf16 v[160:163], v[76:79], v[20:23], v[160:163]\n"
      "v_mfma_f32_16x16x32_bf16 v[160:163], v[72:75], v[16:19], v[160:163]\nv_mfma_f32_16x16x32_bf16 v[164:167], v[80:83], v[16:19], v[164:167]\n"
      "v_mfma_f32_16x16x32_bf16 v[164:167], v[84:87], v[20:23], v[164:167]\nv_mfma_f32_16x16x32_bf16 v[168:171], v[92:95], v[20:23], v[168:171]\ns_waitcnt lgkmcnt(2)\n"
      "v_mfma_f32_16x16x32_bf16 v[168:171], v[88:91], v[16:19], v[168:171]\nv_mfma_f32_16x16x32_bf16 v[184:187], v[88:91], v[24:27], v[184:187]\nds_read_b128 v[16:19], v96 offset:53248\n"
      "v_mfma_f32_16x16x32_bf16 v[184:187], v[92:95], v[28:31], v[184:187]\nds_read_b128 v[20:23], v97 offset:53248\nv_mfma_f32_16x16x32_bf16 v[180:183], v[84:87], v[28:31], v[180:183]\n"
      "v_mfma_f32_16x16x32_bf16 v[180:183], v[80:83], v[24:27], v[180:183]\nv_mfma_f32_16x16x32_bf16 v[176:179], v[72:75], v[24:27], v[176:179]\n"
      "v_mfma_f32_16x16x32_bf16 v[176:179], v[76:79], v[28:31], v[176:179]\ns_waitcnt vmcnt(0) lgkmcnt(0)\nv_mfma_f32_16x16x32_bf16 v[172:175], v[68:71], v[28:31], v[172:175]\ns_barrier\n"
      "v_mfma_f32_16x16x32_bf16 v[172:175], v[64:67], v[24:27], v[172:175]\nds_read_b64_tr_b16 v[32:33], v108 offset:0\nv_mfma_f32_16x16x32_bf16 v[188:191], v[64:67], v[0:3], v[188:191]\n"
      "ds_read_b64_tr_b16 v[34:35], v108 offset:1024\nv_mfma_f32_16x16x32_bf16 v[188:191], v[68:71], v[4:7], v[188:191]\nds_read_b64_tr_b16 v[36:37], v108 offset:8192\n"
      "v_mfma_f32_16x16x32_bf16 v[192:195], v[76:79], v[4:7], v[192:195]\nds_read_b64_tr_b16 v[38:39], v108 offset:9216\nv_mfma_f32_16x16x32_bf16 v[192:195], v[72:75], v[0:3], v[192:195]\n"
      "ds_read_b64_tr_b16 v[40:41], v109 offset:0\nv_mfma_f32_16x16x32_bf16 v[196:199], v[80:83], v[0:3], v[196:199]\nds_read_b64_tr_b16 v[42:43], v109 offset:1024\n"
      "v_mfma_f32_16x16x32_bf16 v[196:199], v[84:87], v[4:7], v[196:199]\nds_read_b64_tr_b16 v[44:45], v109 offset:8192\nv_mfma_f32_16x16x32_bf16 v[200:203], v[92:95], v[4:7], v[200:203]\n"
      "ds_read_b64_tr_b16 v[46:47], v109 offset:9216\nv_mfma_f32_16x16x32_bf16 v[200:203], v[88:91], v[0:3], v[200:203]\nds_read_b64_tr_b16 v[48:49], v110 offset:0\n"
      "v_mfma_f32_16x16x32_bf16 v[216:219], v[88:91], v[8:11], v[216:219]\nv_mfma_f32_16x16x32_bf16 v[216:219], v[92:95], v[12:15], v[216:219]\n"
      "v_mfma_f32_16x16x32_bf16 v[212:215], v[84:87], v[12:15], v[212:215]\nds_read_b64_tr_b16 v[50:51], v110 offset:1024\nv_mfma_f32_16x16x32_bf16 v[212:215], v[80:83], v[8:11], v[212:215]\n"
      "ds_read_b64_tr_b16 v[52:53], v110 offset:8192\nv_mfma_f32_16x16x32_bf16 v[208:211], v[72:75], v[8:11], v[208:211]\nds_read_b64_tr_b16 v[54:55], v110 offset:9216\n"
      "v_mfma_f32_16x16x32_bf16 v[208:211], v[76:79], v[12:15], v[208:211]\nds_read_b64_tr_b16 v[56:57], v111 offset:0\nv_mfma_f32_16x16x32_bf16 v[204:207], v[68:71], v[12:15], v[204:207]\n"
      "ds_read_b64_tr_b16 v[58:59], v111 offset:1024\nv_mfma_f32_16x16x32_bf16 v[204:207], v[64:67], v[8:11], v[204:207]\nds_read_b64_tr_b16 v[60:61], v111 offset:8192\n"
      "v_mfma_f32_16x16x32_bf16 v[220:223], v[64:67], v[16:19], v[220:223]\nds_read_b128 v[8:11], v122 offset:0\nv_mfma_f32_16x16x32_bf16 v[220:223], v[68:71], v[20:23], v[220:223]\n"
      "ds_read_b128 v[12:15], v123 offset:0\nv_mfma_f32_16x16x32_bf16 v[224:227], v[76:79], v[20:23], v[224:227]\nds_read_b64_tr_b16 v[62:63], v111 offset:9216\n"
      "v_mfma_f32_16x16x32_bf16 v[224:227], v[72:75], v[16:19], v[224:227]\nv_mfma_f32_16x16x32_bf16 v[228:231], v[80:83], v[16:19], v[228:231]\n"
      "v_mfma_f32_16x16x32_bf16 v[228:231], v[84:87], v[20:23], v[228:231]\nv_mfma_f32_16x16x32_bf16 v[232:235], v[92:95], v[20:23], v[232:235]\ns_waitcnt lgkmcnt(0)\n"
      "v_mfma_f32_16x16x32_bf16 v[232:235], v[88:91], v[16:19], v[232:235]\nds_read_b128 v[16:19], v98 offset:0\nv_mfma_f32_16x16x32_bf16 v[152:155], v[56:59], v[8:11], v[152:155]\n"
      "ds_read_b128 v[20:23], v99 offset:0\nv_mfma_f32_16x16x32_bf16 v[152:155], v[60:63], v[12:15], v[152:155]\nds_read_b128 v[24:27], v98 offset:2048\n"
      "v_mfma_f32_16x16x32_bf16 v[148:151], v[52:55], v[12:15], v[148:151]\nds_read_b128 v[28:31], v99 offset:2048\nds_read_b128 v[0:3], v98 offset:4096\n"
      "v_mfma_f32_16x16x32_bf16 v[148:151], v[48:51], v[8:11], v[148:151]\nds_read_b128 v[4:7], v99 offset:4096\nv_mfma_f32_16x16x32_bf16 v[144:147], v[40:43], v[8:11], v[144:147]\n"
      "v_mfma_f32_16x16x32_bf16 v[144:147], v[44:47], v[12:15], v[144:147]\nv_mfma_f32_16x16x32_bf16 v[140:143], v[36:39], v[12:15], v[140:143]\ns_waitcnt lgkmcnt(4)\n"
      "v_mfma_f32_16x16x32_bf16 v[140:143], v[32:35], v[8:11], v[140:143]\nv_mfma_f32_16x16x32_bf16 v[156:159], v[32:35], v[16:19], v[156:159]\nds_read_b128 v[8:11], v98 offset:6144\n"
      "v_mfma_f32_16x16x32_bf16 v[156:159], v[36:39], v[20:23], v[156:159]\nds_read_b128 v[12:15], v99 offset:6144\nv_mfma_f32_16x16x32_bf16 v[160:163], v[44:47], v[20:23], v[160:163]\n"
      "v_mfma_f32_16x16x32_bf16 v[160:163], v[40:43], v[16:19], v[160:163]\nv_mfma_f32_16x16x32_bf16 v[164:167], v[48:51], v[16:19], v[164:167]\n"
      "v_mfma_f32_16x16x32_bf16 v[164:167], v[52:55], v[20:23], v[164:167]\nv_mfma_f32_16x16x32_bf16 v[168:171], v[60:63], v[20:23], v[168:171]\ns_waitcnt lgkmcnt(2)\n"
      "v_mfma_f32_16x16x32_bf16 v[168:171], v[56:59], v[16:19], v[168:171]\nv_mfma_f32_16x16x32_bf16 v[184:187], v[56:59], v[24:27], v[184:187]\nds_read_b128 v[16:19], v98 offset:8192\n"
      "v_mfma_f32_16x16x32_bf16 v[184:187], v[60:63], v[28:31], v[184:187]\nds_read_b128 v[20:23], v99 offset:8192\nv_mfma_f32_16x16x32_bf16 v[180:183], v[52:55], v[28:31], v[180:183]\n"
      "v_mfma_f32_16x16x32_bf16 v[180:183], v[48:51], v[24:27], v[180:183]\nv_mfma_f32_16x16x32_bf16 v[176:179], v[40:43], v[24:27], v[176:179]\n"
      "v_mfma_f32_16x16x32_bf16 v[176:179], v[44:47], v[28:31], v[176:179]\ns_waitcnt vmcnt(0) lgkmcnt(0)\nv_mfma_f32_16x16x32_bf16 v[172:175], v[36:39], v[28:31], v[172:175]\ns_barrier\n"
      "v_mfma_f32_16x16x32_bf16 v[172:175], v[32:35], v[24:27], v[172:175]\nds_read_b64_tr_b16 v[64:65], v104 offset:0\nv_mfma_f32_16x16x32_bf16 v[188:191], v[32:35], v[0:3], v[188:191]\n"
      "ds_read_b64_tr_b16 v[66:67], v104 offset:1024\nv_mfma_f32_16x16x32_bf16 v[188:191], v[36:39], v[4:7], v[188:191]\nds_read_b64_tr_b16 v[68:69], v104 offset:8192\n"
      "v_mfma_f32_16x16x32_bf16 v[192:195], v[44:47], v[4:7], v[192:195]\nds_read_b64_tr_b16 v[70:71], v104 offset:9216\nv_mfma_f32_16x16x32_bf16 v[192:195], v[40:43], v[0:3], v[192:195]\n"
      "ds_read_b64_tr_b16 v[72:73], v105 offset:0\nv_mfma_f32_16x16x32_bf16 v[196:199], v[48:51], v[0:3], v[196:199]\nds_read_b64_tr_b16 v[74:75], v105 offset:1024\n"
      "v_mfma_f32_16x16x32_bf16 v[196:199], v[52:55], v[4:7], v[196:199]\nds_read_b64_tr_b16 v[76:77], v105 offset:8192\nv_mfma_f32_16x16x32_bf16 v[200:203], v[60:63], v[4:7], v[200:203]\n"
      "ds_read_b64_tr_b16 v[78:79], v105 offset:9216\nv_mfma_f32_16x16x32_bf16 v[200:203], v[56:59], v[0:3], v[200:203]\nds_read_b64_tr_b16 v[80:81], v106 offset:0\n"
      "v_mfma_f32_16x16x32_bf16 v[216:219], v[56:59], v[8:11], v[216:219]\nv_mfma_f32_16x16x32_bf16 v[216:219], v[60:63], v[12:15], v[216:219]\n"
      "v_mfma_f32_16x16x32_bf16 v[212:215], v[52:55], v[12:15], v[212:215]\nds_read_b64_tr_b16 v[82:83], v106 offset:1024\nv_mfma_f32_16x16x32_bf16 v[212:215], v[48:51], v[8:11], v[212:215]\n"
      "ds_read_b64_tr_b16 v[84:85], v106 offset:8192\nv_mfma_f32_16x16x32_bf16 v[208:211], v[40:43], v[8:11], v[208:211]\nds_read_b64_tr_b16 v[86:87], v106 offset:9216\n"
      "v_mfma_f32_16x16x32_bf16 v[208:211], v[44:47], v[12:15], v[208:211]\nds_read_b64_tr_b16 v[88:89], v107 offset:0\nv_mfma_f32_16x16x32_bf16 v[204:207], v[36:39], v[12:15], v[204:207]\n"
      "ds_read_b64_tr_b16 v[90:91], v107 offset:1024\nv_mfma_f32_16x16x32_bf16 v[204:207], v[32:35], v[8:11], v[204:207]\nds_read_b64_tr_b16 v[92:93], v107 offset:8192\n"
      "v_mfma_f32_16x16x32_bf16 v[220:223], v[32:35], v[16:19], v[220:223]\nds_read_b128 v[8:11], v120 offset:0\nv_mfma_f32_16x16x32_bf16 v[220:223], v[36:39], v[20:23], v[220:223]\n"
      "ds_read_b128 v[12:15], v121 offset:0\nv_mfma_f32_16x16x32_bf16 v[224:227], v[44:47], v[20:23], v[224:227]\nds_read_b64_tr_b16 v[94:95], v107 offset:9216\n"
      "v_mfma_f32_16x16x32_bf16 v[224:227], v[40:43], v[16:19], v[224:227]\nv_mfma_f32_16x16x32_bf16 v[228:231], v[48:51], v[16:19], v[228:231]\n"
      "v_mfma_f32_16x16x32_bf16 v[228:231], v[52:55], v[20:23], v[228:231]\nv_mfma_f32_16x16x32_bf16 v[232:235], v[60:63], v[20:23], v[232:235]\ns_waitcnt lgkmcnt(0)\n"
      "v_mfma_f32_16x16x32_bf16 v[232:235], v[56:59], v[16:19], v[232:235]\nds_read_b128 v[16:19], v96 offset:0\nv_mfma_f32_16x16x32_bf16 v[152:155], v[88:91], v[8:11], v[152:155]\n"
      "ds_read_b128 v[20:23], v97 offset:0\nv_mfma_f32_16x16x32_bf16 v[152:155], v[92:95], v[12:15], v[152:155]\nds_read_b128 v[24:27], v96 offset:2048\n"
      "v_mfma_f32_16x16x32_bf16 v[148:151], v[84:87], v[12:15], v[148:151]\nds_read_b128 v[28:31], v97 offset:2048\nds_read_b128 v[0:3], v96 offset:4096\n"
      "v_mfma_f32_16x16x32_bf16 v[148:151], v[80:83], v[8:11], v[148:151]\nds_read_b128 v[4:7], v97 offset:4096\nv_mfma_f32_16x16x32_bf16 v[144:147], v[72:75], v[8:11], v[144:147]\n"
      "v_mfma_f32_16x16x32_bf16 v[144:147], v[76:79], v[12:15], v[144:147]\nv_mfma_f32_16x16x32_bf16 v[140:143], v[68:71], v[12:15], v[140:143]\ns_waitcnt lgkmcnt(4)\n"
      "v_mfma_f32_16x16x32_bf16 v[140:143], v[64:67], v[8:11], v[140:143]\nv_mfma_f32_16x16x32_bf16 v[156:159], v[64:67], v[16:19], v[156:159]\nds_read_b128 v[8:11], v96 offset:6144\n"
      "v_mfma_f32_16x16x32_bf16 v[156:159], v[68:71], v[20:23], v[156:159]\nds_read_b128 v[12:15], v97 offset:6144\nv_mfma_f32_16x16x32_bf16 v[160:163], v[76:79], v[20:23], v[160:163]\n"
      "v_mfma_f32_16x16x32_bf16 v[160:163], v[72:75], v[16:19], v[160:163]\nv_mfma_f32_16x16x32_bf16 v[164:167], v[80:83], v[16:19], v[164:167]\n"
      "v_mfma_f32_16x16x32_bf16 v[164:167], v[84:87], v[20:23], v[164:167]\nv_mfma_f32_16x16x32_bf16 v[168:171], v[92:95], v[20:23], v[168:171]\ns_waitcnt lgkmcnt(2)\n"
      "v_mfma_f32_16x16x32_bf16 v[168:171], v[88:91], v[16:19], v[168:171]\nv_mfma_f32_16x16x32_bf16 v[184:187], v[88:91], v[24:27], v[184:187]\nds_read_b128 v[16:19], v96 offset:8192\n"
      "v_mfma_f32_16x16x32_bf16 v[184:187], v[92:95], v[28:31], v[184:187]\nds_read_b128 v[20:23], v97 offset:8192\nv_mfma_f32_16x16x32_bf16 v[180:183], v[84:87], v[28:31], v[180:183]\n"
      "v_mfma_f32_16x16x32_bf16 v[180:183], v[80:83], v[24:27], v[180:183]\nv_mfma_f32_16x16x32_bf16 v[176:179], v[72:75], v[24:27], v[176:179]\n"
      "v_mfma_f32_16x16x32_bf16 v[176:179], v[76:79], v[28:31], v[176:179]\ns_waitcnt vmcnt(0) lgkmcnt(0)\nv_mfma_f32_16x16x32_bf16 v[172:175], v[68:71], v[28:31], v[172:175]\ns_barrier\n"
      "v_mfma_f32_16x16x32_bf16 v[172:175], v[64:67], v[24:27], v[172:175]\nds_read_b64_tr_b16 v[32:33], v104 offset:45056\nv_mfma_f32_16x16x32_bf16 v[188:191], v[64:67], v[0:3], v[188:191]\n"
      "ds_read_b64_tr_b16 v[34:35], v104 offset:46080\nv_mfma_f32_16x16x32_bf16 v[188:191], v[68:71], v[4:7], v[188:191]\nds_read_b64_tr_b16 v[36:37], v104 offset:53248\n"
      "v_mfma_f32_16x16x32_bf16 v[192:195], v[76:79], v[4:7], v[192:195]\nds_read_b64_tr_b16 v[38:39], v104 offset:54272\nv_mfma_f32_16x16x32_bf16 v[192:195], v[72:75], v[0:3], v[192:195]\n"
      "ds_read_b64_tr_b16 v[40:41], v105 offset:45056\nv_mfma_f32_16x16x32_bf16 v[196:199], v[80:83], v[0:3], v[196:199]\nds_read_b64_tr_b16 v[42:43], v105 offset:46080\n"
      "v_mfma_f32_16x16x32_bf16 v[196:199], v[84:87], v[4:7], v[196:199]\nds_read_b64_tr_b16 v[44:45], v105 offset:53248\nv_mfma_f32_16x16x32_bf16 v[200:203], v[92:95], v[4:7], v[200:203]\n"
      "ds_read_b64_tr_b16 v[46:47], v105 offset:54272\nv_mfma_f32_16x16x32_bf16 v[200:203], v[88:91], v[0:3], v[200:203]\nds_read_b64_tr_b16 v[48:49], v106 offset:45056\n"
      "v_mfma_f32_16x16x32_bf16 v[216:219], v[88:91], v[8:11], v[216:219]\nv_mfma_f32_16x16x32_bf16 v[216:219], v[92:95], v[12:15], v[216:219]\n"
      "v_mfma_f32_16x16x32_bf16 v[212:215], v[84:87], v[12:15], v[212:215]\nds_read_b64_tr_b16 v[50:51], v106 offset:46080\nv_mfma_f32_16x16x32_bf16 v[212:215], v[80:83], v[8:11], v[212:215]\n"
      "ds_read_b64_tr_b16 v[52:53], v106 offset:53248\nv_mfma_f32_16x16x32_bf16 v[208:211], v[72:75], v[8:11], v[208:211]\nds_read_b64_tr_b16 v[54:55], v106 offset:54272\n"
      "v_mfma_f32_16x16x32_bf16 v[208:211], v[76:79], v[12:15], v[208:211]\nds_read_b64_tr_b16 v[56:57], v107 offset:45056\nv_mfma_f32_16x16x32_bf16 v[204:207], v[68:71], v[12:15], v[204:207]\n"
      "ds_read_b64_tr_b16 v[58:59], v107 offset:46080\nv_mfma_f32_16x16x32_bf16 v[204:207], v[64:67], v[8:11], v[204:207]\nds_read_b64_tr_b16 v[60:61], v107 offset:53248\n"
      "v_mfma_f32_16x16x32_bf16 v[220:223], v[64:67], v[16:19], v[220:223]\nds_read_b128 v[8:11], v120 offset:45056\nv_mfma_f32_16x16x32_bf16 v[220:223], v[68:71], v[20:23], v[220:223]\n"
      "ds_read_b128 v[12:15], v121 offset:45056\nv_mfma_f32_16x16x32_bf16 v[224:227], v[76:79], v[20:23], v[224:227]\nds_read_b64_tr_b16 v[62:63], v107 offset:54272\n"
      "v_mfma_f32_16x16x32_bf16 v[224:227], v[72:75], v[16:19], v[224:227]\nv_mfma_f32_16x16x32_bf16 v[228:231], v[80:83], v[16:19], v[228:231]\n"
      "v_mfma_f32_16x16x32_bf16 v[228:231], v[84:87], v[20:23], v[228:231]\nv_mfma_f32_16x16x32_bf16 v[232:235], v[92:95], v[20:23], v[232:235]\ns_waitcnt lgkmcnt(0)\n"
      "v_mfma_f32_16x16x32_bf16 v[232:235], v[88:91], v[16:19], v[232:235]\nds_read_b128 v[16:19], v96 offset:45056\nv_mfma_f32_16x16x32_bf16 v[152:155], v[56:59], v[8:11], v[152:155]\n"
      "ds_read_b128 v[20:23], v97 offset:45056\nv_mfma_f32_16x16x32_bf16 v[152:155], v[60:63], v[12:15], v[152:155]\nds_read_b128 v[24:27], v96 offset:47104\n"
      "v_mfma_f32_16x16x32_bf16 v[148:151], v[52:55], v[12:15], v[148:151]\nds_read_b128 v[28:31], v97 offset:47104\nds_read_b128 v[0:3], v96 offset:49152\n"
      "v_mfma_f32_16x16x32_bf16 v[148:151], v[48:51], v[8:11], v[148:151]\nds_read_b128 v[4:7], v97 offset:49152\nv_mfma_f32_16x16x32_bf16 v[144:147], v[40:43], v[8:11], v[144:147]\n"
      "v_mfma_f32_16x16x32_bf16 v[144:147], v[44:47], v[12:15], v[144:147]\nv_mfma_f32_16x16x32_bf16 v[140:143], v[36:39], v[12:15], v[140:143]\ns_waitcnt lgkmcnt(4)\n"
      "v_mfma_f32_16x16x32_bf16 v[140:143], v[32:35], v[8:11], v[140:143]\nv_mfma_f32_16x16x32_bf16 v[156:159], v[32:35], v[16:19], v[156:159]\nds_read_b128 v[8:11], v96 offset:51200\n"
      "v_mfma_f32_16x16x32_bf16 v[156:159], v[36:39], v[20:23], v[156:159]\nds_read_b128 v[12:15], v97 offset:51200\nv_mfma_f32_16x16x32_bf16 v[160:163], v[44:47], v[20:23], v[160:163]\n"
      "v_mfma_f32_16x16x32_bf16 v[160:163], v[40:43], v[16:19], v[160:163]\nv_mfma_f32_16x16x32_bf16 v[164:167], v[48:51], v[16:19], v[164:167]\n"
      "v_mfma_f32_16x16x32_bf16 v[164:167], v[52:55], v[20:23], v[164:167]\nv_mfma_f32_16x16x32_bf16 v[168:171], v[60:63], v[20:23], v[168:171]\ns_waitcnt lgkmcnt(2)\n"
      "v_mfma_f32_16x16x32_bf16 v[168:171], v[56:59], v[16:19], v[168:171]\nv_mfma_f32_16x16x32_bf16 v[184:187], v[56:59], v[24:27], v[184:187]\nds_read_b128 v[16:19], v96 offset:53248\n"
      "v_mfma_f32_16x16x32_bf16 v[184:187], v[60:63], v[28:31], v[184:187]\nds_read_b128 v[20:23], v97 offset:53248\nv_mfma_f32_16x16x32_bf16 v[180:183], v[52:55], v[28:31], v[180:183]\n"
      "v_mfma_f32_16x16x32_bf16 v[180:183], v[48:51], v[24:27], v[180:183]\nv_mfma_f32_16x16x32_bf16 v[176:179], v[40:43], v[24:27], v[176:179]\n"
      "v_mfma_f32_16x16x32_bf16 v[176:179], v[44:47], v[28:31], v[176:179]\ns_waitcnt vmcnt(0) lgkmcnt(0)\nv_mfma_f32_16x16x32_bf16 v[172:175], v[36:39], v[28:31], v[172:175]\ns_barrier\n"
      "v_mfma_f32_16x16x32_bf16 v[172:175], v[32:35], v[24:27], v[172:175]\nds_read_b64_tr_b16 v[64:65], v108 offset:0\nv_mfma_f32_16x16x32_bf16 v[188:191], v[32:35], v[0:3], v[188:191]\n"
      "ds_read_b64_tr_b16 v[66:67], v108 offset:1024\nv_mfma_f32_16x16x32_bf16 v[188:191], v[36:39], v[4:7], v[188:191]\nds_read_b64_tr_b16 v[68:69], v108 offset:8192\n"
      "v_mfma_f32_16x16x32_bf16 v[192:195], v[44:47], v[4:7], v[192:195]\nds_read_b64_tr_b16 v[70:71], v108 offset:9216\nv_mfma_f32_16x16x32_bf16 v[192:195], v[40:43], v[0:3], v[192:195]\n"
      "ds_read_b64_tr_b16 v[72:73], v109 offset:0\nv_mfma_f32_16x16x32_bf16 v[196:199], v[48:51], v[0:3], v[196:199]\nds_read_b64_tr_b16 v[74:75], v109 offset:1024\n"
      "v_mfma_f32_16x16x32_bf16 v[196:199], v[52:55], v[4:7], v[196:199]\nds_read_b64_tr_b16 v[76:77], v109 offset:8192\nv_mfma_f32_16x16x32_bf16 v[200:203], v[60:63], v[4:7], v[200:203]\n"
      "ds_read_b64_tr_b16 v[78:79], v109 offset:9216\nv_mfma_f32_16x16x32_bf16 v[200:203], v[56:59], v[0:3], v[200:203]\nds_read_b64_tr_b16 v[80:81], v110 offset:0\n"
      "v_mfma_f32_16x16x32_bf16 v[216:219], v[56:59], v[8:11], v[216:219]\nv_mfma_f32_16x16x32_bf16 v[216:219], v[60:63], v[12:15], v[216:219]\n"
      "v_mfma_f32_16x16x32_bf16 v[212:215], v[52:55], v[12:15], v[212:215]\nds_read_b64_tr_b16 v[82:83], v110 offset:1024\nv_mfma_f32_16x16x32_bf16 v[212:215], v[48:51], v[8:11], v[212:215]\n"
      "ds_read_b64_tr_b16 v[84:85], v110 offset:8192\nv_mfma_f32_16x16x32_bf16 v[208:211], v[40:43], v[8:11], v[208:211]\nds_read_b64_tr_b16 v[86:87], v110 offset:9216\n"
      "v_mfma_f32_16x16x32_bf16 v[208:211], v[44:47], v[12:15], v[208:211]\nds_read_b64_tr_b16 v[88:89], v111 offset:0\nv_mfma_f32_16x16x32_bf16 v[204:207], v[36:39], v[12:15], v[204:207]\n"
      "ds_read_b64_tr_b16 v[90:91], v111 offset:1024\nv_mfma_f32_16x16x32_bf16 v[204:207], v[32:35], v[8:11], v[204:207]\nds_read_b64_tr_b16 v[92:93], v111 offset:8192\n"
      "v_mfma_f32_16x16x32_bf16 v[220:223], v[32:35], v[16:19], v[220:223]\nds_read_b128 v[8:11], v122 offset:0\nv_mfma_f32_16x16x32_bf16 v[220:223], v[36:39], v[20:23], v[220:223]\n"
      "ds_read_b128 v[12:15], v123 offset:0\nv_mfma_f32_16x16x32_bf16 v[224:227], v[44:47], v[20:23], v[224:227]\nds_read_b64_tr_b16 v[94:95], v111 offset:9216\n"
      "v_mfma_f32_16x16x32_bf16 v[224:227], v[40:43], v[16:19], v[224:227]\nv_mfma_f32_16x16x32_bf16 v[228:231], v[48:51], v[16:19], v[228:231]\n"
      "v_mfma_f32_16x16x32_bf16 v[228:231], v[52:55], v[20:23], v[228:231]\nv_mfma_f32_16x16x32_bf16 v[232:235], v[60:63], v[20:23], v[232:235]\ns_waitcnt lgkmcnt(0)\n"
      "v_mfma_f32_16x16x32_bf16 v[232:235], v[56:59], v[16:19], v[232:235]\nds_read_b128 v[16:19], v98 offset:0\nv_mfma_f32_16x16x32_bf16 v[152:155], v[88:91], v[8:11], v[152:155]\n"
      "ds_read_b128 v[20:23], v99 offset:0\nv_mfma_f32_16x16x32_bf16 v[152:155], v[92:95], v[12:15], v[152:155]\nds_read_b128 v[24:27], v98 offset:2048\n"
      "v_mfma_f32_16x16x32_bf16 v[148:151], v[84:87], v[12:15], v[148:151]\nds_read_b128 v[28:31], v99 offset:2048\nds_read_b128 v[0:3], v98 offset:4096\n"
      "v_mfma_f32_16x16x32_bf16 v[148:151], v[80:83], v[8:11], v[148:151]\nds_read_b128 v[4:7], v99 offset:4096\nv_mfma_f32_16x16x32_bf16 v[144:147], v[72:75], v[8:11], v[144:147]\n"
      "v_mfma_f32_16x16x32_bf16 v[144:147], v[76:79], v[12:15], v[144:147]\nv_mfma_f32_16x16x32_bf16 v[140:143], v[68:71], v[12:15], v[140:143]\ns_waitcnt lgkmcnt(4)\n"
      "v_mfma_f32_16x16x32_bf16 v[140:143], v[64:67], v[8:11], v[140:143]\nv_mfma_f32_16x16x32_bf16 v[156:159], v[64:67], v[16:19], v[156:159]\nds_read_b128 v[8:11], v98 offset:6144\n"
      "v_mfma_f32_16x16x32_bf16 v[156:159], v[68:71], v[20:23], v[156:159]\nds_read_b128 v[12:15], v99 offset:6144\nv_mfma_f32_16x16x32_bf16 v[160:163], v[76:79], v[20:23], v[160:163]\n"
      "v_mfma_f32_16x16x32_bf16 v[160:163], v[72:75], v[16:19], v[160:163]\nv_mfma_f32_16x16x32_bf16 v[164:167], v[80:83], v[16:19], v[164:167]\n"
      "v_mfma_f32_16x16x32_bf16 v[164:167], v[84:87], v[20:23], v[164:167]\nv_mfma_f32_16x16x32_bf16 v[168:171], v[92:95], v[20:23], v[168:171]\ns_waitcnt lgkmcnt(2)\n"
      "v_mfma_f32_16x16x32_bf16 v[168:171], v[88:91], v[16:19], v[168:171]\nv_mfma_f32_16x16x32_bf16 v[184:187], v[88:91], v[24:27], v[184:187]\nds_read_b128 v[16:19], v98 offset:8192\n"
      "v_mfma_f32_16x16x32_bf16 v[184:187], v[92:95], v[28:31], v[184:187]\nds_read_b128 v[20:23], v99 offset:8192\nv_mfma_f32_16x16x32_bf16 v[180:183], v[84:87], v[28:31], v[180:183]\n"
      "v_mfma_f32_16x16x32_bf16 v[180:183], v[80:83], v[24:27], v[180:183]\nv_mfma_f32_16x16x32_bf16 v[176:179], v[72:75], v[24:27], v[176:179]\n"
      "v_mfma_f32_16x16x32_bf16 v[176:179], v[76:79], v[28:31], v[176:179]\ns_waitcnt vmcnt(0) lgkmcnt(0)\nv_mfma_f32_16x16x32_bf16 v[172:175], v[68:71], v[28:31], v[172:175]\ns_barrier\n"
      "v_mfma_f32_16x16x32_bf16 v[172:175], v[64:67], v[24:27], v[172:175]\nds_read_b64_tr_b16 v[32:33], v104 offset:0\nv_mfma_f32_16x16x32_bf16 v[188:191], v[64:67], v[0:3], v[188:191]\n"
      "ds_read_b64_tr_b16 v[34:35], v104 offset:1024\nv_mfma_f32_16x16x32_bf16 v[188:191], v[68:71], v[4:7], v[188:191]\nds_read_b64_tr_b16 v[36:37], v104 offset:8192\n"
      "v_mfma_f32_16x16x32_bf16 v[192:195], v[76:79], v[4:7], v[192:195]\nds_read_b64_tr_b16 v[38:39], v104 offset:9216\nv_mfma_f32_16x16x32_bf16 v[192:195], v[72:75], v[0:3], v[192:195]\n"
      "ds_read_b64_tr_b16 v[40:41], v105 offset:0\nv_mfma_f32_16x16x32_bf16 v[196:199], v[80:83], v[0:3], v[196:199]\nds_read_b64_tr_b16 v[42:43], v105 offset:1024\n"
      "v_mfma_f32_16x16x32_bf16 v[196:199], v[84:87], v[4:7], v[196:199]\nds_read_b64_tr_b16 v[44:45], v105 offset:8192\nv_mfma_f32_16x16x32_bf16 v[200:203], v[92:95], v[4:7], v[200:203]\n"
      "ds_read_b64_tr_b16 v[46:47], v105 offset:9216\nv_mfma_f32_16x16x32_bf16 v[200:203], v[88:91], v[0:3], v[200:203]\nds_read_b64_tr_b16 v[48:49], v106 offset:0\n"
      "v_mfma_f32_16x16x32_bf16 v[216:219], v[88:91], v[8:11], v[216:219]\nv_mfma_f32_16x16x32_bf16 v[216:219], v[92:95], v[12:15], v[216:219]\n"
      "v_mfma_f32_16x16x32_bf16 v[212:215], v[84:87], v[12:15], v[212:215]\nds_read_b64_tr_b16 v[50:51], v106 offset:1024\nv_mfma_f32_16x16x32_bf16 v[212:215], v[80:83], v[8:11], v[212:215]\n"
      "ds_read_b64_tr_b16 v[52:53], v106 offset:8192\nv_mfma_f32_16x16x32_bf16 v[208:211], v[72:75], v[8:11], v[208:211]\nds_read_b64_tr_b16 v[54:55], v106 offset:9216\n"
      "v_mfma_f32_16x16x32_bf16 v[208:211], v[76:79], v[12:15], v[208:211]\nds_read_b64_tr_b16 v[56:57], v107 offset:0\nv_mfma_f32_16x16x32_bf16 v[204:207], v[68:71], v[12:15], v[204:207]\n"
      "ds_read_b64_tr_b16 v[58:59], v107 offset:1024\nv_mfma_f32_16x16x32_bf16 v[204:207], v[64:67], v[8:11], v[204:207]\nds_read_b64_tr_b16 v[60:61], v107 offset:8192\n"
      "v_mfma_f32_16x16x32_bf16 v[220:223], v[64:67], v[16:19], v[220:223]\nds_read_b128 v[8:11], v120 offset:0\nv_mfma_f32_16x16x32_bf16 v[220:223], v[68:71], v[20:23], v[220:223]\n"
      "ds_read_b128 v[12:15], v121 offset:0\nv_mfma_f32_16x16x32_bf16 v[224:227], v[76:79], v[20:23], v[224:227]\nds_read_b64_tr_b16 v[62:63], v107 offset:9216\n"
      "v_mfma_f32_16x16x32_bf16 v[224:227], v[72:75], v[16:19], v[224:227]\nv_mfma_f32_16x16x32_bf16 v[228:231], v[80:83], v[16:19], v[228:231]\n"
      "v_mfma_f32_16x16x32_bf16 v[228:231], v[84:87], v[20:23], v[228:231]\nv_mfma_f32_16x16x32_bf16 v[232:235], v[92:95], v[20:23], v[232:235]\ns_waitcnt lgkmcnt(0)\n"
      "v_mfma_f32_16x16x32_bf16 v[232:235], v[88:91], v[16:19], v[232:235]\nds_read_b128 v[16:19], v96 offset:0\nv_mfma_f32_16x16x32_bf16 v[152:155], v[56:59], v[8:11], v[152:155]\n"
      "ds_read_b128 v[20:23], v97 offset:0\nv_mfma_f32_16x16x32_bf16 v[152:155], v[60:63], v[12:15], v[152:155]\nds_read_b128 v[24:27], v96 offset:2048\n"
      "v_mfma_f32_16x16x32_bf16 v[148:151], v[52:55], v[12:15], v[148:151]\nds_read_b128 v[28:31], v97 offset:2048\nds_read_b128 v[0:3], v96 offset:4096\n"
      "v_mfma_f32_16x16x32_bf16 v[148:151], v[48:51], v[8:11], v[148:151]\nds_read_b128 v[4:7], v97 offset:4096\nv_mfma_f32_16x16x32_bf16 v[144:147], v[40:43], v[8:11], v[144:147]\n"
      "v_mfma_f32_16x16x32_bf16 v[144:147], v[44:47], v[12:15], v[144:147]\nv_mfma_f32_16x16x32_bf16 v[140:143], v[36:39], v[12:15], v[140:143]\ns_waitcnt lgkmcnt(4)\n"
      "v_mfma_f32_16x16x32_bf16 v[140:143], v[32:35], v[8:11], v[140:143]\nv_mfma_f32_16x16x32_bf16 v[156:159], v[32:35], v[16:19], v[156:159]\nds_read_b128 v[8:11], v96 offset:6144\n"
      "v_mfma_f32_16x16x32_bf16 v[156:159], v[36:39], v[20:23], v[156:159]\nds_read_b128 v[12:15], v97 offset:6144\nv_mfma_f32_16x16x32_bf16 v[160:163], v[44:47], v[20:23], v[160:163]\n"
      "v_mfma_f32_16x16x32_bf16 v[160:163], v[40:43], v[16:19], v[160:163]\nv_mfma_f32_16x16x32_bf16 v[164:167], v[48:51], v[16:19], v[164:167]\n"
      "v_mfma_f32_16x16x32_bf16 v[164:167], v[52:55], v[20:23], v[164:167]\nv_mfma_f32_16x16x32_bf16 v[168:171], v[60:63], v[20:23], v[168:171]\ns_waitcnt lgkmcnt(2)\n"
      "v_mfma_f32_16x16x32_bf16 v[168:171], v[56:59], v[16:19], v[168:171]\nv_mfma_f32_16x16x32_bf16 v[184:187], v[56:59], v[24:27], v[184:187]\nds_read_b128 v[16:19], v96 offset:8192\n"
      "v_mfma_f32_16x16x32_bf16 v[184:187], v[60:63], v[28:31], v[184:187]\nds_read_b128 v[20:23], v97 offset:8192\nv_mfma_f32_16x16x32_bf16 v[180:183], v[52:55], v[28:31], v[180:183]\n"
      "v_mfma_f32_16x16x32_bf16 v[180:183], v[48:51], v[24:27], v[180:183]\nv_mfma_f32_16x16x32_bf16 v[176:179], v[40:43], v[24:27], v[176:179]\n"
      "v_mfma_f32_16x16x32_bf16 v[176:179], v[44:47], v[28:31], v[176:179]\ns_waitcnt vmcnt(0) lgkmcnt(0)\nv_mfma_f32_16x16x32_bf16 v[172:175], v[36:39], v[28:31], v[172:175]\ns_barrier\n"
      "v_mfma_f32_16x16x32_bf16 v[172:175], v[32:35], v[24:27], v[172:175]\nds_read_b64_tr_b16 v[64:65], v104 offset:45056\nv_mfma_f32_16x16x32_bf16 v[188:191], v[32:35], v[0:3], v[188:191]\n"
      "ds_read_b64_tr_b16 v[66:67], v104 offset:46080\nv_mfma_f32_16x16x32_bf16 v[188:191], v[36:39], v[4:7], v[188:191]\nds_read_b64_tr_b16 v[68:69], v104 offset:53248\n"
      "v_mfma_f32_16x16x32_bf16 v[192:195], v[44:47], v[4:7], v[192:195]\nds_read_b64_tr_b16 v[70:71], v104 offset:54272\nv_mfma_f32_16x16x32_bf16 v[192:195], v[40:43], v[0:3], v[192:195]\n"
      "ds_read_b64_tr_b16 v[72:73], v105 offset:45056\nv_mfma_f32_16x16x32_bf16 v[196:199], v[48:51], v[0:3], v[196:199]\nds_read_b64_tr_b16 v[74:75], v105 offset:46080\n"
      "v_mfma_f32_16x16x32_bf16 v[196:199], v[52:55], v[4:7], v[196:199]\nds_read_b64_tr_b16 v[76:77], v105 offset:53248\nv_mfma_f32_16x16x32_bf16 v[200:203], v[60:63], v[4:7], v[200:203]\n"
      "ds_read_b64_tr_b16 v[78:79], v105 offset:54272\nv_mfma_f32_16x16x32_bf16 v[200:203], v[56:59], v[0:3], v[200:203]\nds_read_b64_tr_b16 v[80:81], v106 offset:45056\n"
      "v_mfma_f32_16x16x32_bf16 v[216:219], v[56:59], v[8:11], v[216:219]\nv_mfma_f32_16x16x32_bf16 v[216:219], v[60:63], v[12:15], v[216:219]\n"
      "v_mfma_f32_16x16x32_bf16 v[212:215], v[52:55], v[12:15], v[212:215]\nds_read_b64_tr_b16 v[82:83], v106 offset:46080\nv_mfma_f32_16x16x32_bf16 v[212:215], v[48:51], v[8:11], v[212:215]\n"
      "ds_read_b64_tr_b16 v[84:85], v106 offset:53248\nv_mfma_f32_16x16x32_bf16 v[208:211], v[40:43], v[8:11], v[208:211]\nds_read_b64_tr_b16 v[86:87], v106 offset:54272\n"
      "v_mfma_f32_16x16x32_bf16 v[208:211], v[44:47], v[12:15], v[208:211]\nds_read_b64_tr_b16 v[88:89], v107 offset:45056\nv_mfma_f32_16x16x32_bf16 v[204:207], v[36:39], v[12:15], v[204:207]\n"
      "ds_read_b64_tr_b16 v[90:91], v107 offset:46080\nv_mfma_f32_16x16x32_bf16 v[204:207], v[32:35], v[8:11], v[204:207]\nds_read_b64_tr_b16 v[92:93], v107 offset:53248\n"
      "v_mfma_f32_16x16x32_bf16 v[220:223], v[32:35], v[16:19], v[220:223]\nds_read_b128 v[8:11], v120 offset:45056\nv_mfma_f32_16x16x32_bf16 v[220:223], v[36:39], v[20:23], v[220:223]\n"
      "ds_read_b128 v[12:15], v121 offset:45056\nv_mfma_f32_16x16x32_bf16 v[224:227], v[44:47], v[20:23], v[224:227]\nds_read_b64_tr_b16 v[94:95], v107 offset:54272\n"
      "v_mfma_f32_16x16x32_bf16 v[224:227], v[40:43], v[16:19], v[224:227]\nv_mfma_f32_16x16x32_bf16 v[228:231], v[48:51], v[16:19], v[228:231]\ns_sub_u32 s58, s58, 1\ns_cmp_lg_u32 s58, 0\n"
      "v_mfma_f32_16x16x32_bf16 v[228:231], v[52:55], v[20:23], v[228:231]\nv_mfma_f32_16x16x32_bf16 v[232:235], v[60:63], v[20:23], v[232:235]\ns_waitcnt lgkmcnt(0)\n"
      "v_mfma_f32_16x16x32_bf16 v[232:235], v[56:59], v[16:19], v[232:235]\nds_read_b128 v[16:19], v96 offset:45056\ns_cbranch_scc1 .Lgl12\n"
      "v_mfma_f32_16x16x32_bf16 v[152:155], v[88:91], v[8:11], v[152:155]\nds_read_b128 v[20:23], v97 offset:45056\nv_cmp_eq_u32 s[62:63], 0, v240\n"
      "v_mfma_f32_16x16x32_bf16 v[152:155], v[92:95], v[12:15], v[152:155]\nds_read_b128 v[24:27], v96 offset:47104\nv_mov_b32 v253, 262144\n"
      "v_mfma_f32_16x16x32_bf16 v[148:151], v[84:87], v[12:15], v[148:151]\nds_read_b128 v[28:31], v97 offset:47104\nds_read_b128 v[0:3], v96 offset:49152\n"
      "v_mfma_f32_16x16x32_bf16 v[148:151], v[80:83], v[8:11], v[148:151]\nds_read_b128 v[4:7], v97 offset:49152\nv_mov_b32 v248, 162048\n"
      "v_mfma_f32_16x16x32_bf16 v[144:147], v[72:75], v[8:11], v[144:147]\nv_mov_b32 v241, 1\nv_cndmask_b32 v253, v253, v248, s[62:63]\n"
      "v_mfma_f32_16x16x32_bf16 v[144:147], v[76:79], v[12:15], v[144:147]\nv_lshrrev_b32 v112, 4, v240\nv_bfe_u32 v113, v240, 2, 2\n"
      "v_mfma_f32_16x16x32_bf16 v[140:143], v[68:71], v[12:15], v[140:143]\ns_waitcnt lgkmcnt(4)\nv_and_b32 v114, 3, v240\nv_mfma_f32_16x16x32_bf16 v[140:143], v[64:67], v[8:11], v[140:143]\n"
      "v_mul_u32_u24 v114, 0x80008, v114\nv_add_u32 v244, v112, v113\nv_mfma_f32_16x16x32_bf16 v[156:159], v[64:67], v[16:19], v[156:159]\nds_read_b128 v[8:11], v96 offset:51200\n"
      "v_and_b32 v244, 3, v244\nv_mfma_f32_16x16x32_bf16 v[156:159], v[68:71], v[20:23], v[156:159]\nds_read_b128 v[12:15], v97 offset:51200\nv_add_u32 v116, 22, v113\n"
      "v_mfma_f32_16x16x32_bf16 v[160:163], v[76:79], v[20:23], v[160:163]\nv_cmp_gt_u32 s[62:63], 2, v113\nv_and_b32 v113, 60, v240\n"
      "v_mfma_f32_16x16x32_bf16 v[160:163], v[72:75], v[16:19], v[160:163]\ns_mov_b32 s42, 0\nv_cndmask_b32 v116, v116, v244, s[62:63]\n"
      "v_mfma_f32_16x16x32_bf16 v[164:167], v[80:83], v[16:19], v[164:167]\nv_lshl_or_b32 v244, v244, 16, v116\nv_lshl_add_u32 v244, v244, 5, v114\n"
      "v_mfma_f32_16x16x32_bf16 v[164:167], v[84:87], v[20:23], v[164:167]\nv_lshl_add_u32 v252, v240, 4, s42\ns_mov_b32 s42, 0x76325410\n"
      "v_mfma_f32_16x16x32_bf16 v[168:171], v[92:95], v[20:23], v[168:171]\ns_waitcnt lgkmcnt(2)\ns_mov_b32 s43, 0x21005410\nv_mfma_f32_16x16x32_bf16 v[168:171], v[88:91], v[16:19], v[168:171]\n"
      "v_lshrrev_b64 v[116:117], v113, s[42:43]\nv_and_b32 v116, 7, v116\nv_mfma_f32_16x16x32_bf16 v[184:187], v[88:91], v[24:27], v[184:187]\nds_read_b128 v[16:19], v96 offset:53248\n"
      "v_lshl_add_u32 v116, v112, 3, v116\nv_mfma_f32_16x16x32_bf16 v[184:187], v[92:95], v[28:31], v[184:187]\nds_read_b128 v[20:23], v97 offset:53248\ns_mov_b32 s42, 0x54107632\n"
      "v_mfma_f32_16x16x32_bf16 v[180:183], v[84:87], v[28:31], v[180:183]\ns_mov_b32 s43, 0x21107632\nv_lshrrev_b64 v[118:119], v113, s[42:43]\n"
      "v_mfma_f32_16x16x32_bf16 v[180:183], v[80:83], v[24:27], v[180:183]\nv_and_b32 v118, 7, v118\nv_lshl_add_u32 v118, v112, 3, v118\n"
      "v_mfma_f32_16x16x32_bf16 v[176:179], v[72:75], v[24:27], v[176:179]\nv_lshl_or_b32 v245, v118, 16, v116\nv_lshl_add_u32 v245, v245, 5, v114\n"
      "v_mfma_f32_16x16x32_bf16 v[176:179], v[76:79], v[28:31], v[176:179]\ns_waitcnt vmcnt(0) lgkmcnt(0)\nv_and_b32 v113, 1, v240\n"
      "v_mfma_f32_16x16x32_bf16 v[172:175], v[68:71], v[28:31], v[172:175]\ns_barrier\nv_mul_u32_u24 v113, 0xaaaa, v113\nv_mfma_f32_16x16x32_bf16 v[172:175], v[64:67], v[24:27], v[172:175]\n"
      "ds_read_b64_tr_b16 v[32:33], v108 offset:0\nv_add_u32 v242, 0x4444, v113\nv_mfma_f32_16x16x32_bf16 v[188:191], v[64:67], v[0:3], v[188:191]\nds_read_b64_tr_b16 v[34:35], v108 offset:1024\n"
      "s_lshl_b32 s42, s36, 4\nv_mfma_f32_16x16x32_bf16 v[188:191], v[68:71], v[4:7], v[188:191]\nds_read_b64_tr_b16 v[36:37], v108 offset:8192\ns_add_u32 s42, s42, 160640\n"
      "v_mfma_f32_16x16x32_bf16 v[192:195], v[76:79], v[4:7], v[192:195]\nds_read_b64_tr_b16 v[38:39], v108 offset:9216\nv_lshl_add_u32 v243, v112, 2, s42\n"
      "v_mfma_f32_16x16x32_bf16 v[192:195], v[72:75], v[0:3], v[192:195]\nds_read_b64_tr_b16 v[40:41], v109 offset:0\nv_and_b32 v112, 1, v112\n"
      "v_mfma_f32_16x16x32_bf16 v[196:199], v[80:83], v[0:3], v[196:199]\nds_read_b64_tr_b16 v[42:43], v109 offset:1024\nv_lshl_add_u32 v112, v112, 3, s24\n"
      "v_mfma_f32_16x16x32_bf16 v[196:199], v[84:87], v[4:7], v[196:199]\nds_read_b64_tr_b16 v[44:45], v109 offset:8192\ns_sub_u32 s42, s64, 1\n"
      "v_mfma_f32_16x16x32_bf16 v[200:203], v[92:95], v[4:7], v[200:203]\nds_read_b64_tr_b16 v[46:47], v109 offset:9216\nv_min_u32 v112, s42, v112\n"
      "v_mfma_f32_16x16x32_bf16 v[200:203], v[88:91], v[0:3], v[200:203]\nds_read_b64_tr_b16 v[48:49], v110 offset:0\nv_lshlrev_b32 v112, 3, v112\n"
      "v_mfma_f32_16x16x32_bf16 v[216:219], v[88:91], v[8:11], v[216:219]\nv_and_b32 v113, 15, v240\nv_lshl_add_u32 v250, v112, 1, v113\n"
      "v_mfma_f32_16x16x32_bf16 v[216:219], v[92:95], v[12:15], v[216:219]\ns_mov_b32 s42, 136192\nv_lshl_add_u32 v250, v250, 2, s42\n"
      "v_mfma_f32_16x16x32_bf16 v[212:215], v[84:87], v[12:15], v[212:215]\nds_read_b64_tr_b16 v[50:51], v110 offset:1024\nv_min_u32 v113, 12, v113\n"
      "v_mfma_f32_16x16x32_bf16 v[212:215], v[80:83], v[8:11], v[212:215]\nds_read_b64_tr_b16 v[52:53], v110 offset:8192\ns_lshl_b32 s42, s30, 7\n"
      "v_mfma_f32_16x16x32_bf16 v[208:211], v[72:75], v[8:11], v[208:211]\nds_read_b64_tr_b16 v[54:55], v110 offset:9216\nv_lshl_add_u32 v113, v113, 15, s42\n"
      "v_mfma_f32_16x16x32_bf16 v[208:211], v[76:79], v[12:15], v[208:211]\nds_read_b64_tr_b16 v[56:57], v111 offset:0\nv_add_u32 v249, v113, v112\n"
      "v_mfma_f32_16x16x32_bf16 v[204:207], v[68:71], v[12:15], v[204:207]\nds_read_b64_tr_b16 v[58:59], v111 offset:1024\nv_mfma_f32_16x16x32_bf16 v[204:207], v[64:67], v[8:11], v[204:207]\n"
      "ds_read_b64_tr_b16 v[60:61], v111 offset:8192\nv_mfma_f32_16x16x32_bf16 v[220:223], v[64:67], v[16:19], v[220:223]\nds_read_b128 v[8:11], v122 offset:0\n"
      "v_mfma_f32_16x16x32_bf16 v[220:223], v[68:71], v[20:23], v[220:223]\nds_read_b128 v[12:15], v123 offset:0\nv_mfma_f32_16x16x32_bf16 v[224:227], v[76:79], v[20:23], v[224:227]\n"
      "ds_read_b64_tr_b16 v[62:63], v111 offset:9216\nv_mfma_f32_16x16x32_bf16 v[224:227], v[72:75], v[16:19], v[224:227]\nv_mfma_f32_16x16x32_bf16 v[228:231], v[80:83], v[16:19], v[228:231]\n"
      "v_mfma_f32_16x16x32_bf16 v[228:231], v[84:87], v[20:23], v[228:231]\nv_mfma_f32_16x16x32_bf16 v[232:235], v[92:95], v[20:23], v[232:235]\ns_waitcnt lgkmcnt(0)\n"
      "v_mfma_f32_16x16x32_bf16 v[232:235], v[88:91], v[16:19], v[232:235]\nds_read_b128 v[16:19], v98 offset:0\nv_mfma_f32_16x16x32_bf16 v[152:155], v[56:59], v[8:11], v[152:155]\n"
      "ds_read_b128 v[20:23], v99 offset:0\nv_mfma_f32_16x16x32_bf16 v[152:155], v[60:63], v[12:15], v[152:155]\nds_read_b128 v[24:27], v98 offset:2048\n"
      "v_mfma_f32_16x16x32_bf16 v[148:151], v[52:55], v[12:15], v[148:151]\nds_read_b128 v[28:31], v99 offset:2048\nds_read_b128 v[0:3], v98 offset:4096\n"
      "v_mfma_f32_16x16x32_bf16 v[148:151], v[48:51], v[8:11], v[148:151]\nds_read_b128 v[4:7], v99 offset:4096\nv_mfma_f32_16x16x32_bf16 v[144:147], v[40:43], v[8:11], v[144:147]\n"
      "v_mfma_f32_16x16x32_bf16 v[144:147], v[44:47], v[12:15], v[144:147]\nv_mfma_f32_16x16x32_bf16 v[140:143], v[36:39], v[12:15], v[140:143]\ns_waitcnt lgkmcnt(4)\n"
      "v_mfma_f32_16x16x32_bf16 v[140:143], v[32:35], v[8:11], v[140:143]\nv_mfma_f32_16x16x32_bf16 v[156:159], v[32:35], v[16:19], v[156:159]\nds_read_b128 v[8:11], v98 offset:6144\n"
      "v_mfma_f32_16x16x32_bf16 v[156:159], v[36:39], v[20:23], v[156:159]\nds_read_b128 v[12:15], v99 offset:6144\nv_mfma_f32_16x16x32_bf16 v[160:163], v[44:47], v[20:23], v[160:163]\n"
      "v_mfma_f32_16x16x32_bf16 v[160:163], v[40:43], v[16:19], v[160:163]\nv_mfma_f32_16x16x32_bf16 v[164:167], v[48:51], v[16:19], v[164:167]\n"
      "v_mfma_f32_16x16x32_bf16 v[164:167], v[52:55], v[20:23], v[164:167]\nv_mfma_f32_16x16x32_bf16 v[168:171], v[60:63], v[20:23], v[168:171]\ns_waitcnt lgkmcnt(2)\n"
      "v_mfma_f32_16x16x32_bf16 v[168:171], v[56:59], v[16:19], v[168:171]\nv_mfma_f32_16x16x32_bf16 v[184:187], v[56:59], v[24:27], v[184:187]\nds_read_b128 v[16:19], v98 offset:8192\n"
      "v_mfma_f32_16x16x32_bf16 v[184:187], v[60:63], v[28:31], v[184:187]\nds_read_b128 v[20:23], v99 offset:8192\nv_mfma_f32_16x16x32_bf16 v[180:183], v[52:55], v[28:31], v[180:183]\n"
      "v_mfma_f32_16x16x32_bf16 v[180:183], v[48:51], v[24:27], v[180:183]\nv_mfma_f32_16x16x32_bf16 v[176:179], v[40:43], v[24:27], v[176:179]\n"
      "v_mfma_f32_16x16x32_bf16 v[176:179], v[44:47], v[28:31], v[176:179]\ns_waitcnt vmcnt(0) lgkmcnt(0)\nv_mfma_f32_16x16x32_bf16 v[172:175], v[36:39], v[28:31], v[172:175]\ns_barrier\n"
      "v_mfma_f32_16x16x32_bf16 v[172:175], v[32:35], v[24:27], v[172:175]\nds_read_b128 v[72:75], v252 offset:0\nv_lshl_add_u32 v246, v240, 1, v248\n"
      "v_mfma_f32_16x16x32_bf16 v[188:191], v[32:35], v[0:3], v[188:191]\nds_read_b128 v[24:27], v236 offset:512\nds_read_u16 v251, v246 offset:64\n"
      "v_mfma_f32_16x16x32_bf16 v[188:191], v[36:39], v[4:7], v[188:191]\nds_read_b128 v[28:31], v236 offset:576\nds_read_b32 v247, v248 offset:24\n"
      "v_mfma_f32_16x16x32_bf16 v[192:195], v[44:47], v[4:7], v[192:195]\nds_read_b128 v[76:79], v252 offset:1024\nv_and_b32 v246, 48, v240\n"
      "v_mfma_f32_16x16x32_bf16 v[192:195], v[40:43], v[0:3], v[192:195]\nds_read_b128 v[80:83], v252 offset:2048\ns_mov_b32 s42, 42240\n"
      "v_mfma_f32_16x16x32_bf16 v[196:199], v[48:51], v[0:3], v[196:199]\nds_read_b128 v[84:87], v252 offset:3072\nv_lshl_add_u32 v246, v246, 2, s42\n"
      "v_mfma_f32_16x16x32_bf16 v[196:199], v[52:55], v[4:7], v[196:199]\nds_read_b128 v[88:91], v252 offset:4096\ns_mov_b32 s58, 0xbdd2d3e7\n"
      "v_mfma_f32_16x16x32_bf16 v[200:203], v[60:63], v[4:7], v[200:203]\nds_read_b128 v[92:95], v252 offset:5120\ns_waitcnt lgkmcnt(4)\n"
      "v_mfma_f32_16x16x32_bf16 v[200:203], v[56:59], v[0:3], v[200:203]\nds_read_b128 v[96:99], v252 offset:6144\nv_readfirstlane_b32 s39, v247\n"
      "v_mfma_f32_16x16x32_bf16 v[216:219], v[56:59], v[8:11], v[216:219]\nds_read_b128 v[0:3], v236 offset:0\nv_readlane_b32 s55, v251, 0\n"
      "v_mfma_f32_16x16x32_bf16 v[216:219], v[60:63], v[12:15], v[216:219]\nds_read_b128 v[4:7], v236 offset:64\ns_setprio 3\nv_mfma_f32_16x16x32_bf16 v[212:215], v[52:55], v[12:15], v[212:215]\n"
      "ds_read_b128 v[100:103], v252 offset:7168\ns_xor_b32 s40, s39, 0xa5c30f96\nv_mfma_f32_16x16x32_bf16 v[212:215], v[48:51], v[8:11], v[212:215]\nds_read_b128 v[104:107], v252 offset:8192\n"
      "s_mul_i32 s41, s39, 0x9E3779B1\nv_mfma_f32_16x16x32_bf16 v[208:211], v[40:43], v[8:11], v[208:211]\nds_read_b128 v[108:111], v252 offset:9216\ns_lshr_b32 s41, s41, 24\n"
      "v_mfma_f32_16x16x32_bf16 v[208:211], v[44:47], v[12:15], v[208:211]\nds_read_b128 v[64:67], v237 offset:512\nv_mfma_f32_16x16x32_bf16 v[204:207], v[36:39], v[12:15], v[204:207]\n"
      "ds_read_b128 v[68:71], v237 offset:576\nv_mfma_f32_16x16x32_bf16 v[204:207], v[32:35], v[8:11], v[204:207]\nds_read_b128 v[112:115], v252 offset:10240\n"
      "v_mfma_f32_16x16x32_bf16 v[220:223], v[32:35], v[16:19], v[220:223]\nds_read_b128 v[8:11], v237 offset:0\nv_mfma_f32_16x16x32_bf16 v[220:223], v[36:39], v[20:23], v[220:223]\n"
      "ds_read_b128 v[12:15], v237 offset:64\nv_mfma_f32_16x16x32_bf16 v[224:227], v[44:47], v[20:23], v[224:227]\nds_read_b128 v[116:119], v252 offset:11264\n"
      "v_mfma_f32_16x16x32_bf16 v[224:227], v[40:43], v[16:19], v[224:227]\nds_read_b128 v[120:123], v252 offset:12288\n"
      "v_add_u32_sdwa v239, s49, v245 dst_sel:DWORD dst_unused:UNUSED_PAD src0_sel:DWORD src1_sel:WORD_1\nv_mfma_f32_16x16x32_bf16 v[228:231], v[48:51], v[16:19], v[228:231]\n"
      "ds_read_b128 v[124:127], v252 offset:13312\nv_add_u32_sdwa v236, s49, v244 dst_sel:DWORD dst_unused:UNUSED_PAD src0_sel:DWORD src1_sel:WORD_0\n"
      "v_mfma_f32_16x16x32_bf16 v[228:231], v[52:55], v[20:23], v[228:231]\nds_read_b128 v[128:131], v252 offset:14336\n"
      "v_add_u32_sdwa v237, s49, v244 dst_sel:DWORD dst_unused:UNUSED_PAD src0_sel:DWORD src1_sel:WORD_1\nv_mfma_f32_16x16x32_bf16 v[232:235], v[60:63], v[20:23], v[232:235]\n"
      "ds_read_b128 v[132:135], v252 offset:15360\nv_add_u32_sdwa v238, s49, v245 dst_sel:DWORD dst_unused:UNUSED_PAD src0_sel:DWORD src1_sel:WORD_0\ns_waitcnt lgkmcnt(0)\n"
      "v_mfma_f32_16x16x32_bf16 v[232:235], v[56:59], v[16:19], v[232:235]\nds_read_b128 v[136:139], v252 offset:16384\nv_mov_b32 v42, 0\nv_pk_mul_f32 v[44:45], v[140:141], v[140:141]\n"
      "v_pk_mul_f32 v[46:47], v[142:143], v[142:143]\nv_pk_mul_f32 v[48:49], v[144:145], v[144:145]\nv_pk_mul_f32 v[50:51], v[146:147], v[146:147]\n"
      "v_pk_fma_f32 v[44:45], v[44:45], s[58:59], s[58:59] op_sel:[0,0,1] op_sel_hi:[1,0,1]\nv_pk_fma_f32 v[46:47], v[46:47], s[58:59], s[58:59] op_sel:[0,0,1] op_sel_hi:[1,0,1]\n"
      "v_pk_fma_f32 v[48:49], v[48:49], s[58:59], s[58:59] op_sel:[0,0,1] op_sel_hi:[1,0,1]\nv_pk_fma_f32 v[50:51], v[50:51], s[58:59], s[58:59] op_sel:[0,0,1] op_sel_hi:[1,0,1]\n"
      "v_pk_fma_f32 v[44:45], v[44:45], v[140:141], s[60:61] op_sel_hi:[1,1,0]\nv_pk_fma_f32 v[46:47], v[46:47], v[142:143], s[60:61] op_sel_hi:[1,1,0]\n"
      "v_pk_fma_f32 v[48:49], v[48:49], v[144:145], s[60:61] op_sel_hi:[1,1,0]\nv_pk_fma_f32 v[50:51], v[50:51], v[146:147], s[60:61] op_sel_hi:[1,1,0]\nv_exp_f32 v44, v44 clamp\n"
      "v_pk_mul_f32 v[52:53], v[148:149], v[148:149]\nv_exp_f32 v45, v45 clamp\nv_pk_mul_f32 v[54:55], v[150:151], v[150:151]\nv_pk_mul_f32 v[56:57], v[152:153], v[152:153]\n"
      "v_exp_f32 v46, v46 clamp\nv_pk_mul_f32 v[58:59], v[154:155], v[154:155]\nv_exp_f32 v47, v47 clamp\nv_pk_fma_f32 v[52:53], v[52:53], s[58:59], s[58:59] op_sel:[0,0,1] op_sel_hi:[1,0,1]\n"
      "v_pk_fma_f32 v[54:55], v[54:55], s[58:59], s[58:59] op_sel:[0,0,1] op_sel_hi:[1,0,1]\nv_exp_f32 v48, v48 clamp\n"
      "v_pk_fma_f32 v[56:57], v[56:57], s[58:59], s[58:59] op_sel:[0,0,1] op_sel_hi:[1,0,1]\nv_exp_f32 v49, v49 clamp\n"
      "v_pk_fma_f32 v[58:59], v[58:59], s[58:59], s[58:59] op_sel:[0,0,1] op_sel_hi:[1,0,1]\nv_pk_fma_f32 v[52:53], v[52:53], v[148:149], s[60:61] op_sel_hi:[1,1,0]\nv_exp_f32 v50, v50 clamp\n"
      "v_pk_fma_f32 v[54:55], v[54:55], v[150:151], s[60:61] op_sel_hi:[1,1,0]\nv_exp_f32 v51, v51 clamp\nv_pk_fma_f32 v[56:57], v[56:57], v[152:153], s[60:61] op_sel_hi:[1,1,0]\n"
      "v_pk_fma_f32 v[58:59], v[58:59], v[154:155], s[60:61] op_sel_hi:[1,1,0]\nv_exp_f32 v52, v52 clamp\nv_pk_fma_f32 v[46:47], v[46:47], v[2:3], v[26:27]\n"
      "v_pk_fma_f32 v[44:45], v[44:45], v[0:1], v[24:25]\nv_pk_fma_f32 v[48:49], v[48:49], v[4:5], v[28:29]\nv_exp_f32 v53, v53 clamp\nv_pk_fma_f32 v[50:51], v[50:51], v[6:7], v[30:31]\n"
      "v_pk_mul_f32 v[140:141], v[140:141], v[46:47]\nv_pk_mul_f32 v[144:145], v[144:145], v[50:51]\nv_exp_f32 v54, v54 clamp\nv_pk_fma_f32 v[140:141], v[142:143], v[44:45], v[140:141]\n"
      "v_pk_fma_f32 v[144:145], v[146:147], v[48:49], v[144:145]\nv_pk_mul_f32 v[44:45], v[44:45], v[46:47]\nv_exp_f32 v55, v55 clamp\nv_pk_mul_f32 v[48:49], v[48:49], v[50:51]\n"
      "v_pk_mul_f32 v[46:47], v[44:45], v[48:49]\nv_rcp_f32 v46, v46\nv_pk_mul_f32 v[140:141], v[140:141], v[48:49]\nv_rcp_f32 v47, v47\nv_pk_fma_f32 v[140:141], v[144:145], v[44:45], v[140:141]\n"
      "v_pk_mul_f32 v[32:33], v[140:141], v[46:47]\nv_pk_mul_f32 v[16:17], v[156:157], v[156:157]\nv_pk_mul_f32 v[18:19], v[158:159], v[158:159]\nv_exp_f32 v56, v56 clamp\n"
      "v_pk_mul_f32 v[20:21], v[160:161], v[160:161]\nv_pk_mul_f32 v[22:23], v[162:163], v[162:163]\nv_exp_f32 v57, v57 clamp\n"
      "v_pk_fma_f32 v[16:17], v[16:17], s[58:59], s[58:59] op_sel:[0,0,1] op_sel_hi:[1,0,1]\nv_pk_fma_f32 v[18:19], v[18:19], s[58:59], s[58:59] op_sel:[0,0,1] op_sel_hi:[1,0,1]\n"
      "v_pk_fma_f32 v[20:21], v[20:21], s[58:59], s[58:59] op_sel:[0,0,1] op_sel_hi:[1,0,1]\nv_exp_f32 v58, v58 clamp\n"
      "v_pk_fma_f32 v[22:23], v[22:23], s[58:59], s[58:59] op_sel:[0,0,1] op_sel_hi:[1,0,1]\nv_pk_fma_f32 v[16:17], v[16:17], v[156:157], s[60:61] op_sel_hi:[1,1,0]\n"
      "v_pk_fma_f32 v[18:19], v[18:19], v[158:159], s[60:61] op_sel_hi:[1,1,0]\nv_exp_f32 v59, v59 clamp\nv_pk_fma_f32 v[20:21], v[20:21], v[160:161], s[60:61] op_sel_hi:[1,1,0]\n"
      "v_pk_fma_f32 v[22:23], v[22:23], v[162:163], s[60:61] op_sel_hi:[1,1,0]\nv_exp_f32 v16, v16 clamp\nv_pk_fma_f32 v[54:55], v[54:55], v[10:11], v[66:67]\n"
      "v_pk_fma_f32 v[52:53], v[52:53], v[8:9], v[64:65]\nv_pk_fma_f32 v[56:57], v[56:57], v[12:13], v[68:69]\nv_exp_f32 v17, v17 clamp\nv_pk_fma_f32 v[58:59], v[58:59], v[14:15], v[70:71]\n"
      "v_pk_mul_f32 v[148:149], v[148:149], v[54:55]\nv_pk_mul_f32 v[152:153], v[152:153], v[58:59]\nv_exp_f32 v18, v18 clamp\nv_pk_fma_f32 v[148:149], v[150:151], v[52:53], v[148:149]\n"
      "v_pk_fma_f32 v[152:153], v[154:155], v[56:57], v[152:153]\nv_pk_mul_f32 v[52:53], v[52:53], v[54:55]\nv_exp_f32 v19, v19 clamp\nv_pk_mul_f32 v[56:57], v[56:57], v[58:59]\n"
      "v_pk_mul_f32 v[54:55], v[52:53], v[56:57]\nv_rcp_f32 v54, v54\nv_pk_mul_f32 v[148:149], v[148:149], v[56:57]\nv_rcp_f32 v55, v55\nv_pk_fma_f32 v[148:149], v[152:153], v[52:53], v[148:149]\n"
      "v_pk_fma_f32 v[32:33], v[148:149], v[54:55], v[32:33]\nv_add_f32 v41, v32, v33\nv_pk_mul_f32 v[44:45], v[164:165], v[164:165]\nv_pk_mul_f32 v[46:47], v[166:167], v[166:167]\n"
      "v_exp_f32 v20, v20 clamp\nv_pk_mul_f32 v[48:49], v[168:169], v[168:169]\nv_pk_mul_f32 v[50:51], v[170:171], v[170:171]\nv_exp_f32 v21, v21 clamp\n"
      "v_pk_fma_f32 v[44:45], v[44:45], s[58:59], s[58:59] op_sel:[0,0,1] op_sel_hi:[1,0,1]\nv_pk_fma_f32 v[46:47], v[46:47], s[58:59], s[58:59] op_sel:[0,0,1] op_sel_hi:[1,0,1]\n"
      "v_pk_fma_f32 v[48:49], v[48:49], s[58:59], s[58:59] op_sel:[0,0,1] op_sel_hi:[1,0,1]\nv_exp_f32 v22, v22 clamp\n"
      "v_pk_fma_f32 v[50:51], v[50:51], s[58:59], s[58:59] op_sel:[0,0,1] op_sel_hi:[1,0,1]\nv_pk_fma_f32 v[44:45], v[44:45], v[164:165], s[60:61] op_sel_hi:[1,1,0]\n"
      "v_pk_fma_f32 v[46:47], v[46:47], v[166:167], s[60:61] op_sel_hi:[1,1,0]\nv_exp_f32 v23, v23 clamp\nv_pk_fma_f32 v[48:49], v[48:49], v[168:169], s[60:61] op_sel_hi:[1,1,0]\n"
      "v_pk_fma_f32 v[50:51], v[50:51], v[170:171], s[60:61] op_sel_hi:[1,1,0]\nv_exp_f32 v44, v44 clamp\nv_pk_fma_f32 v[18:19], v[18:19], v[2:3], v[26:27]\n"
      "v_pk_fma_f32 v[16:17], v[16:17], v[0:1], v[24:25]\nv_pk_fma_f32 v[20:21], v[20:21], v[4:5], v[28:29]\nv_exp_f32 v45, v45 clamp\nv_pk_fma_f32 v[22:23], v[22:23], v[6:7], v[30:31]\n"
      "v_pk_mul_f32 v[156:157], v[156:157], v[18:19]\nv_pk_mul_f32 v[160:161], v[160:161], v[22:23]\nv_exp_f32 v46, v46 clamp\nv_pk_fma_f32 v[156:157], v[158:159], v[16:17], v[156:157]\n"
      "v_pk_fma_f32 v[160:161], v[162:163], v[20:21], v[160:161]\nv_pk_mul_f32 v[16:17], v[16:17], v[18:19]\nv_exp_f32 v47, v47 clamp\nv_pk_mul_f32 v[20:21], v[20:21], v[22:23]\n"
      "v_pk_mul_f32 v[18:19], v[16:17], v[20:21]\nv_rcp_f32 v18, v18\nv_pk_mul_f32 v[156:157], v[156:157], v[20:21]\nv_rcp_f32 v19, v19\nv_pk_fma_f32 v[156:157], v[160:161], v[16:17], v[156:157]\n"
      "v_pk_mul_f32 v[32:33], v[156:157], v[18:19]\nv_pk_mul_f32 v[52:53], v[172:173], v[172:173]\nv_pk_mul_f32 v[54:55], v[174:175], v[174:175]\nv_exp_f32 v48, v48 clamp\n"
      "v_pk_mul_f32 v[56:57], v[176:177], v[176:177]\nv_pk_mul_f32 v[58:59], v[178:179], v[178:179]\nv_exp_f32 v49, v49 clamp\n"
      "v_pk_fma_f32 v[52:53], v[52:53], s[58:59], s[58:59] op_sel:[0,0,1] op_sel_hi:[1,0,1]\nv_pk_fma_f32 v[54:55], v[54:55], s[58:59], s[58:59] op_sel:[0,0,1] op_sel_hi:[1,0,1]\n"
      "v_pk_fma_f32 v[56:57], v[56:57], s[58:59], s[58:59] op_sel:[0,0,1] op_sel_hi:[1,0,1]\nv_exp_f32 v50, v50 clamp\n"
      "v_pk_fma_f32 v[58:59], v[58:59], s[58:59], s[58:59] op_sel:[0,0,1] op_sel_hi:[1,0,1]\nv_pk_fma_f32 v[52:53], v[52:53], v[172:173], s[60:61] op_sel_hi:[1,1,0]\n"
      "v_pk_fma_f32 v[54:55], v[54:55], v[174:175], s[60:61] op_sel_hi:[1,1,0]\nv_exp_f32 v51, v51 clamp\nv_pk_fma_f32 v[56:57], v[56:57], v[176:177], s[60:61] op_sel_hi:[1,1,0]\n"
      "v_pk_fma_f32 v[58:59], v[58:59], v[178:179], s[60:61] op_sel_hi:[1,1,0]\nv_exp_f32 v52, v52 clamp\nv_pk_fma_f32 v[46:47], v[46:47], v[10:11], v[66:67]\n"
      "v_pk_fma_f32 v[44:45], v[44:45], v[8:9], v[64:65]\nv_pk_fma_f32 v[48:49], v[48:49], v[12:13], v[68:69]\nv_exp_f32 v53, v53 clamp\nv_pk_fma_f32 v[50:51], v[50:51], v[14:15], v[70:71]\n"
      "v_pk_mul_f32 v[164:165], v[164:165], v[46:47]\nv_pk_mul_f32 v[168:169], v[168:169], v[50:51]\nv_exp_f32 v54, v54 clamp\nv_pk_fma_f32 v[164:165], v[166:167], v[44:45], v[164:165]\n"
      "v_pk_fma_f32 v[168:169], v[170:171], v[48:49], v[168:169]\nv_pk_mul_f32 v[44:45], v[44:45], v[46:47]\nv_exp_f32 v55, v55 clamp\nv_pk_mul_f32 v[48:49], v[48:49], v[50:51]\n"
      "v_pk_mul_f32 v[46:47], v[44:45], v[48:49]\nv_rcp_f32 v46, v46\nv_pk_mul_f32 v[164:165], v[164:165], v[48:49]\nv_rcp_f32 v47, v47\nv_pk_fma_f32 v[164:165], v[168:169], v[44:45], v[164:165]\n"
      "v_pk_fma_f32 v[32:33], v[164:165], v[46:47], v[32:33]\nv_add_f32 v36, v32, v33\nv_pk_mul_f32 v[16:17], v[180:181], v[180:181]\nv_pk_mul_f32 v[18:19], v[182:183], v[182:183]\n"
      "v_exp_f32 v56, v56 clamp\nv_pk_mul_f32 v[20:21], v[184:185], v[184:185]\nv_pk_mul_f32 v[22:23], v[186:187], v[186:187]\nv_exp_f32 v57, v57 clamp\n"
      "v_pk_fma_f32 v[16:17], v[16:17], s[58:59], s[58:59] op_sel:[0,0,1] op_sel_hi:[1,0,1]\nv_pk_fma_f32 v[18:19], v[18:19], s[58:59], s[58:59] op_sel:[0,0,1] op_sel_hi:[1,0,1]\n"
      "v_pk_fma_f32 v[20:21], v[20:21], s[58:59], s[58:59] op_sel:[0,0,1] op_sel_hi:[1,0,1]\nv_exp_f32 v58, v58 clamp\n"
      "v_pk_fma_f32 v[22:23], v[22:23], s[58:59], s[58:59] op_sel:[0,0,1] op_sel_hi:[1,0,1]\nv_pk_fma_f32 v[16:17], v[16:17], v[180:181], s[60:61] op_sel_hi:[1,1,0]\n"
      "v_pk_fma_f32 v[18:19], v[18:19], v[182:183], s[60:61] op_sel_hi:[1,1,0]\nv_exp_f32 v59, v59 clamp\nv_pk_fma_f32 v[20:21], v[20:21], v[184:185], s[60:61] op_sel_hi:[1,1,0]\n"
      "v_pk_fma_f32 v[22:23], v[22:23], v[186:187], s[60:61] op_sel_hi:[1,1,0]\nv_exp_f32 v16, v16 clamp\nv_pk_fma_f32 v[54:55], v[54:55], v[2:3], v[26:27]\n"
      "v_pk_fma_f32 v[52:53], v[52:53], v[0:1], v[24:25]\nv_pk_fma_f32 v[56:57], v[56:57], v[4:5], v[28:29]\nv_exp_f32 v17, v17 clamp\nv_pk_fma_f32 v[58:59], v[58:59], v[6:7], v[30:31]\n"
      "v_pk_mul_f32 v[172:173], v[172:173], v[54:55]\nv_pk_mul_f32 v[176:177], v[176:177], v[58:59]\nv_exp_f32 v18, v18 clamp\nv_pk_fma_f32 v[172:173], v[174:175], v[52:53], v[172:173]\n"
      "v_pk_fma_f32 v[176:177], v[178:179], v[56:57], v[176:177]\nv_pk_mul_f32 v[52:53], v[52:53], v[54:55]\nv_exp_f32 v19, v19 clamp\nv_pk_mul_f32 v[56:57], v[56:57], v[58:59]\n"
      "v_pk_mul_f32 v[54:55], v[52:53], v[56:57]\nv_rcp_f32 v54, v54\nv_pk_mul_f32 v[172:173], v[172:173], v[56:57]\nv_rcp_f32 v55, v55\nv_pk_fma_f32 v[172:173], v[176:177], v[52:53], v[172:173]\n"
      "v_pk_mul_f32 v[32:33], v[172:173], v[54:55]\nv_pk_mul_f32 v[44:45], v[188:189], v[188:189]\nv_pk_mul_f32 v[46:47], v[190:191], v[190:191]\nv_exp_f32 v20, v20 clamp\n"
      "v_pk_mul_f32 v[48:49], v[192:193], v[192:193]\nv_pk_mul_f32 v[50:51], v[194:195], v[194:195]\nv_exp_f32 v21, v21 clamp\n"
      "v_pk_fma_f32 v[44:45], v[44:45], s[58:59], s[58:59] op_sel:[0,0,1] op_sel_hi:[1,0,1]\nv_pk_fma_f32 v[46:47], v[46:47], s[58:59], s[58:59] op_sel:[0,0,1] op_sel_hi:[1,0,1]\n"
      "v_pk_fma_f32 v[48:49], v[48:49], s[58:59], s[58:59] op_sel:[0,0,1] op_sel_hi:[1,0,1]\nv_exp_f32 v22, v22 clamp\n"
      "v_pk_fma_f32 v[50:51], v[50:51], s[58:59], s[58:59] op_sel:[0,0,1] op_sel_hi:[1,0,1]\nv_pk_fma_f32 v[44:45], v[44:45], v[188:189], s[60:61] op_sel_hi:[1,1,0]\n"
      "v_pk_fma_f32 v[46:47], v[46:47], v[190:191], s[60:61] op_sel_hi:[1,1,0]\nv_exp_f32 v23, v23 clamp\nv_pk_fma_f32 v[48:49], v[48:49], v[192:193], s[60:61] op_sel_hi:[1,1,0]\n"
      "v_pk_fma_f32 v[50:51], v[50:51], v[194:195], s[60:61] op_sel_hi:[1,1,0]\nv_exp_f32 v44, v44 clamp\nv_pk_fma_f32 v[18:19], v[18:19], v[10:11], v[66:67]\n"
      "v_pk_fma_f32 v[16:17], v[16:17], v[8:9], v[64:65]\nv_pk_fma_f32 v[20:21], v[20:21], v[12:13], v[68:69]\nv_exp_f32 v45, v45 clamp\nv_pk_fma_f32 v[22:23], v[22:23], v[14:15], v[70:71]\n"
      "v_pk_mul_f32 v[180:181], v[180:181], v[18:19]\nv_pk_mul_f32 v[184:185], v[184:185], v[22:23]\nv_exp_f32 v46, v46 clamp\nv_pk_fma_f32 v[180:181], v[182:183], v[16:17], v[180:181]\n"
      "v_pk_fma_f32 v[184:185], v[186:187], v[20:21], v[184:185]\nv_pk_mul_f32 v[16:17], v[16:17], v[18:19]\nv_exp_f32 v47, v47 clamp\nv_pk_mul_f32 v[20:21], v[20:21], v[22:23]\n"
      "v_pk_mul_f32 v[18:19], v[16:17], v[20:21]\nv_rcp_f32 v18, v18\nv_pk_mul_f32 v[180:181], v[180:181], v[20:21]\nv_rcp_f32 v19, v19\nv_pk_fma_f32 v[180:181], v[184:185], v[16:17], v[180:181]\n"
      "v_pk_fma_f32 v[32:33], v[180:181], v[18:19], v[32:33]\nv_add_f32 v37, v32, v33\nv_pk_mul_f32 v[52:53], v[196:197], v[196:197]\nv_pk_mul_f32 v[54:55], v[198:199], v[198:199]\n"
      "v_exp_f32 v48, v48 clamp\nv_pk_mul_f32 v[56:57], v[200:201], v[200:201]\nv_pk_mul_f32 v[58:59], v[202:203], v[202:203]\nv_exp_f32 v49, v49 clamp\n"
      "v_pk_fma_f32 v[52:53], v[52:53], s[58:59], s[58:59] op_sel:[0,0,1] op_sel_hi:[1,0,1]\nv_pk_fma_f32 v[54:55], v[54:55], s[58:59], s[58:59] op_sel:[0,0,1] op_sel_hi:[1,0,1]\n"
      "v_pk_fma_f32 v[56:57], v[56:57], s[58:59], s[58:59] op_sel:[0,0,1] op_sel_hi:[1,0,1]\nv_exp_f32 v50, v50 clamp\n"
      "v_pk_fma_f32 v[58:59], v[58:59], s[58:59], s[58:59] op_sel:[0,0,1] op_sel_hi:[1,0,1]\nv_pk_fma_f32 v[52:53], v[52:53], v[196:197], s[60:61] op_sel_hi:[1,1,0]\n"
      "v_pk_fma_f32 v[54:55], v[54:55], v[198:199], s[60:61] op_sel_hi:[1,1,0]\nv_exp_f32 v51, v51 clamp\nv_pk_fma_f32 v[56:57], v[56:57], v[200:201], s[60:61] op_sel_hi:[1,1,0]\n"
      "v_pk_fma_f32 v[58:59], v[58:59], v[202:203], s[60:61] op_sel_hi:[1,1,0]\nv_exp_f32 v52, v52 clamp\nv_pk_fma_f32 v[46:47], v[46:47], v[2:3], v[26:27]\n"
      "v_pk_fma_f32 v[44:45], v[44:45], v[0:1], v[24:25]\nv_pk_fma_f32 v[48:49], v[48:49], v[4:5], v[28:29]\nv_exp_f32 v53, v53 clamp\nv_pk_fma_f32 v[50:51], v[50:51], v[6:7], v[30:31]\n"
      "v_pk_mul_f32 v[188:189], v[188:189], v[46:47]\nv_pk_mul_f32 v[192:193], v[192:193], v[50:51]\nv_exp_f32 v54, v54 clamp\nv_pk_fma_f32 v[188:189], v[190:191], v[44:45], v[188:189]\n"
      "v_pk_fma_f32 v[192:193], v[194:195], v[48:49], v[192:193]\nv_pk_mul_f32 v[44:45], v[44:45], v[46:47]\nv_exp_f32 v55, v55 clamp\nv_pk_mul_f32 v[48:49], v[48:49], v[50:51]\n"
      "v_pk_mul_f32 v[46:47], v[44:45], v[48:49]\nv_rcp_f32 v46, v46\nv_pk_mul_f32 v[188:189], v[188:189], v[48:49]\nv_rcp_f32 v47, v47\nv_pk_fma_f32 v[188:189], v[192:193], v[44:45], v[188:189]\n"
      "v_pk_mul_f32 v[32:33], v[188:189], v[46:47]\nv_pk_mul_f32 v[16:17], v[204:205], v[204:205]\nv_pk_mul_f32 v[18:19], v[206:207], v[206:207]\nv_exp_f32 v56, v56 clamp\n"
      "v_pk_mul_f32 v[20:21], v[208:209], v[208:209]\nv_pk_mul_f32 v[22:23], v[210:211], v[210:211]\nv_exp_f32 v57, v57 clamp\n"
      "v_pk_fma_f32 v[16:17], v[16:17], s[58:59], s[58:59] op_sel:[0,0,1] op_sel_hi:[1,0,1]\nv_pk_fma_f32 v[18:19], v[18:19], s[58:59], s[58:59] op_sel:[0,0,1] op_sel_hi:[1,0,1]\n"
      "v_pk_fma_f32 v[20:21], v[20:21], s[58:59], s[58:59] op_sel:[0,0,1] op_sel_hi:[1,0,1]\nv_exp_f32 v58, v58 clamp\n"
      "v_pk_fma_f32 v[22:23], v[22:23], s[58:59], s[58:59] op_sel:[0,0,1] op_sel_hi:[1,0,1]\nv_pk_fma_f32 v[16:17], v[16:17], v[204:205], s[60:61] op_sel_hi:[1,1,0]\n"
      "v_pk_fma_f32 v[18:19], v[18:19], v[206:207], s[60:61] op_sel_hi:[1,1,0]\nv_exp_f32 v59, v59 clamp\nv_pk_fma_f32 v[20:21], v[20:21], v[208:209], s[60:61] op_sel_hi:[1,1,0]\n"
      "v_pk_fma_f32 v[22:23], v[22:23], v[210:211], s[60:61] op_sel_hi:[1,1,0]\nv_exp_f32 v16, v16 clamp\nv_pk_fma_f32 v[54:55], v[54:55], v[10:11], v[66:67]\n"
      "v_pk_fma_f32 v[52:53], v[52:53], v[8:9], v[64:65]\nv_pk_fma_f32 v[56:57], v[56:57], v[12:13], v[68:69]\nv_exp_f32 v17, v17 clamp\nv_pk_fma_f32 v[58:59], v[58:59], v[14:15], v[70:71]\n"
      "v_pk_mul_f32 v[196:197], v[196:197], v[54:55]\nv_pk_mul_f32 v[200:201], v[200:201], v[58:59]\nv_exp_f32 v18, v18 clamp\nv_pk_fma_f32 v[196:197], v[198:199], v[52:53], v[196:197]\n"
      "v_pk_fma_f32 v[200:201], v[202:203], v[56:57], v[200:201]\nv_pk_mul_f32 v[52:53], v[52:53], v[54:55]\nv_exp_f32 v19, v19 clamp\nv_pk_mul_f32 v[56:57], v[56:57], v[58:59]\n"
      "v_pk_mul_f32 v[54:55], v[52:53], v[56:57]\nv_rcp_f32 v54, v54\nv_pk_mul_f32 v[196:197], v[196:197], v[56:57]\nv_rcp_f32 v55, v55\nv_pk_fma_f32 v[196:197], v[200:201], v[52:53], v[196:197]\n"
      "v_pk_fma_f32 v[32:33], v[196:197], v[54:55], v[32:33]\nv_add_f32 v38, v32, v33\nv_pk_mul_f32 v[44:45], v[212:213], v[212:213]\nv_pk_mul_f32 v[46:47], v[214:215], v[214:215]\n"
      "v_exp_f32 v20, v20 clamp\nv_pk_mul_f32 v[48:49], v[216:217], v[216:217]\nv_pk_mul_f32 v[50:51], v[218:219], v[218:219]\nv_exp_f32 v21, v21 clamp\n"
      "v_pk_fma_f32 v[44:45], v[44:45], s[58:59], s[58:59] op_sel:[0,0,1] op_sel_hi:[1,0,1]\nv_pk_fma_f32 v[46:47], v[46:47], s[58:59], s[58:59] op_sel:[0,0,1] op_sel_hi:[1,0,1]\n"
      "v_pk_fma_f32 v[48:49], v[48:49], s[58:59], s[58:59] op_sel:[0,0,1] op_sel_hi:[1,0,1]\nv_exp_f32 v22, v22 clamp\n"
      "v_pk_fma_f32 v[50:51], v[50:51], s[58:59], s[58:59] op_sel:[0,0,1] op_sel_hi:[1,0,1]\nv_pk_fma_f32 v[44:45], v[44:45], v[212:213], s[60:61] op_sel_hi:[1,1,0]\n"
      "v_pk_fma_f32 v[46:47], v[46:47], v[214:215], s[60:61] op_sel_hi:[1,1,0]\nv_exp_f32 v23, v23 clamp\nv_pk_fma_f32 v[48:49], v[48:49], v[216:217], s[60:61] op_sel_hi:[1,1,0]\n"
      "v_pk_fma_f32 v[50:51], v[50:51], v[218:219], s[60:61] op_sel_hi:[1,1,0]\nv_exp_f32 v44, v44 clamp\nv_pk_fma_f32 v[18:19], v[18:19], v[2:3], v[26:27]\n"
      "v_pk_fma_f32 v[16:17], v[16:17], v[0:1], v[24:25]\nv_pk_fma_f32 v[20:21], v[20:21], v[4:5], v[28:29]\nv_exp_f32 v45, v45 clamp\nv_pk_fma_f32 v[22:23], v[22:23], v[6:7], v[30:31]\n"
      "v_pk_mul_f32 v[204:205], v[204:205], v[18:19]\nv_pk_mul_f32 v[208:209], v[208:209], v[22:23]\nv_exp_f32 v46, v46 clamp\nv_pk_fma_f32 v[204:205], v[206:207], v[16:17], v[204:205]\n"
      "v_pk_fma_f32 v[208:209], v[210:211], v[20:21], v[208:209]\nv_pk_mul_f32 v[16:17], v[16:17], v[18:19]\nv_exp_f32 v47, v47 clamp\nv_pk_mul_f32 v[20:21], v[20:21], v[22:23]\n"
      "v_pk_mul_f32 v[18:19], v[16:17], v[20:21]\nv_rcp_f32 v18, v18\nv_pk_mul_f32 v[204:205], v[204:205], v[20:21]\nv_rcp_f32 v19, v19\nv_pk_fma_f32 v[204:205], v[208:209], v[16:17], v[204:205]\n"
      "v_pk_mul_f32 v[32:33], v[204:205], v[18:19]\nv_pk_mul_f32 v[52:53], v[220:221], v[220:221]\nv_pk_mul_f32 v[54:55], v[222:223], v[222:223]\nv_exp_f32 v48, v48 clamp\n"
      "v_pk_mul_f32 v[56:57], v[224:225], v[224:225]\nv_pk_mul_f32 v[58:59], v[226:227], v[226:227]\nv_exp_f32 v49, v49 clamp\n"
      "v_pk_fma_f32 v[52:53], v[52:53], s[58:59], s[58:59] op_sel:[0,0,1] op_sel_hi:[1,0,1]\nv_pk_fma_f32 v[54:55], v[54:55], s[58:59], s[58:59] op_sel:[0,0,1] op_sel_hi:[1,0,1]\n"
      "v_pk_fma_f32 v[56:57], v[56:57], s[58:59], s[58:59] op_sel:[0,0,1] op_sel_hi:[1,0,1]\nv_exp_f32 v50, v50 clamp\n"
      "v_pk_fma_f32 v[58:59], v[58:59], s[58:59], s[58:59] op_sel:[0,0,1] op_sel_hi:[1,0,1]\nv_pk_fma_f32 v[52:53], v[52:53], v[220:221], s[60:61] op_sel_hi:[1,1,0]\n"
      "v_pk_fma_f32 v[54:55], v[54:55], v[222:223], s[60:61] op_sel_hi:[1,1,0]\nv_exp_f32 v51, v51 clamp\nv_pk_fma_f32 v[56:57], v[56:57], v[224:225], s[60:61] op_sel_hi:[1,1,0]\n"
      "v_pk_fma_f32 v[58:59], v[58:59], v[226:227], s[60:61] op_sel_hi:[1,1,0]\nv_exp_f32 v52, v52 clamp\nv_pk_fma_f32 v[46:47], v[46:47], v[10:11], v[66:67]\n"
      "v_pk_fma_f32 v[44:45], v[44:45], v[8:9], v[64:65]\nv_pk_fma_f32 v[48:49], v[48:49], v[12:13], v[68:69]\nv_exp_f32 v53, v53 clamp\nv_pk_fma_f32 v[50:51], v[50:51], v[14:15], v[70:71]\n"
      "v_pk_mul_f32 v[212:213], v[212:213], v[46:47]\nv_pk_mul_f32 v[216:217], v[216:217], v[50:51]\nv_exp_f32 v54, v54 clamp\nv_pk_fma_f32 v[212:213], v[214:215], v[44:45], v[212:213]\n"
      "v_pk_fma_f32 v[216:217], v[218:219], v[48:49], v[216:217]\nv_pk_mul_f32 v[44:45], v[44:45], v[46:47]\nv_exp_f32 v55, v55 clamp\nv_pk_mul_f32 v[48:49], v[48:49], v[50:51]\n"
      "v_pk_mul_f32 v[46:47], v[44:45], v[48:49]\nv_rcp_f32 v46, v46\nv_pk_mul_f32 v[212:213], v[212:213], v[48:49]\nv_rcp_f32 v47, v47\nv_pk_fma_f32 v[212:213], v[216:217], v[44:45], v[212:213]\n"
      "v_pk_fma_f32 v[32:33], v[212:213], v[46:47], v[32:33]\nv_add_f32 v39, v32, v33\nv_pk_mul_f32 v[16:17], v[228:229], v[228:229]\nv_pk_mul_f32 v[18:19], v[230:231], v[230:231]\n"
      "v_exp_f32 v56, v56 clamp\nv_pk_mul_f32 v[20:21], v[232:233], v[232:233]\nv_pk_mul_f32 v[22:23], v[234:235], v[234:235]\nv_exp_f32 v57, v57 clamp\n"
      "v_pk_fma_f32 v[16:17], v[16:17], s[58:59], s[58:59] op_sel:[0,0,1] op_sel_hi:[1,0,1]\nv_pk_fma_f32 v[18:19], v[18:19], s[58:59], s[58:59] op_sel:[0,0,1] op_sel_hi:[1,0,1]\n"
      "v_pk_fma_f32 v[20:21], v[20:21], s[58:59], s[58:59] op_sel:[0,0,1] op_sel_hi:[1,0,1]\nv_exp_f32 v58, v58 clamp\n"
      "v_pk_fma_f32 v[22:23], v[22:23], s[58:59], s[58:59] op_sel:[0,0,1] op_sel_hi:[1,0,1]\nv_pk_fma_f32 v[16:17], v[16:17], v[228:229], s[60:61] op_sel_hi:[1,1,0]\n"
      "v_pk_fma_f32 v[18:19], v[18:19], v[230:231], s[60:61] op_sel_hi:[1,1,0]\nv_exp_f32 v59, v59 clamp\nv_pk_fma_f32 v[20:21], v[20:21], v[232:233], s[60:61] op_sel_hi:[1,1,0]\n"
      "v_pk_fma_f32 v[22:23], v[22:23], v[234:235], s[60:61] op_sel_hi:[1,1,0]\nv_exp_f32 v16, v16 clamp\nv_pk_fma_f32 v[54:55], v[54:55], v[2:3], v[26:27]\nv_exp_f32 v17, v17 clamp\n"
      "v_pk_fma_f32 v[52:53], v[52:53], v[0:1], v[24:25]\nv_pk_fma_f32 v[56:57], v[56:57], v[4:5], v[28:29]\nv_exp_f32 v18, v18 clamp\nv_pk_fma_f32 v[58:59], v[58:59], v[6:7], v[30:31]\n"
      "v_pk_mul_f32 v[220:221], v[220:221], v[54:55]\nv_exp_f32 v19, v19 clamp\nv_pk_mul_f32 v[224:225], v[224:225], v[58:59]\nv_pk_fma_f32 v[220:221], v[222:223], v[52:53], v[220:221]\n"
      "v_exp_f32 v20, v20 clamp\nv_pk_fma_f32 v[224:225], v[226:227], v[56:57], v[224:225]\nv_exp_f32 v21, v21 clamp\nv_pk_mul_f32 v[52:53], v[52:53], v[54:55]\n"
      "v_pk_mul_f32 v[56:57], v[56:57], v[58:59]\nv_exp_f32 v22, v22 clamp\nv_pk_mul_f32 v[54:55], v[52:53], v[56:57]\nv_rcp_f32 v54, v54\nv_pk_mul_f32 v[220:221], v[220:221], v[56:57]\n"
      "v_exp_f32 v23, v23 clamp\nv_rcp_f32 v55, v55\nv_pk_fma_f32 v[220:221], v[224:225], v[52:53], v[220:221]\nv_pk_mul_f32 v[32:33], v[220:221], v[54:55]\n"
      "v_pk_fma_f32 v[18:19], v[18:19], v[10:11], v[66:67]\nv_pk_fma_f32 v[16:17], v[16:17], v[8:9], v[64:65]\nv_pk_fma_f32 v[20:21], v[20:21], v[12:13], v[68:69]\n"
      "v_pk_fma_f32 v[22:23], v[22:23], v[14:15], v[70:71]\nv_pk_mul_f32 v[228:229], v[228:229], v[18:19]\nv_pk_mul_f32 v[232:233], v[232:233], v[22:23]\n"
      "v_pk_fma_f32 v[228:229], v[230:231], v[16:17], v[228:229]\nv_pk_fma_f32 v[232:233], v[234:235], v[20:21], v[232:233]\nv_pk_mul_f32 v[16:17], v[16:17], v[18:19]\n"
      "v_pk_mul_f32 v[20:21], v[20:21], v[22:23]\nv_pk_mul_f32 v[18:19], v[16:17], v[20:21]\nv_rcp_f32 v18, v18\nv_pk_mul_f32 v[228:229], v[228:229], v[20:21]\nv_rcp_f32 v19, v19\n"
      "v_pk_fma_f32 v[228:229], v[232:233], v[16:17], v[228:229]\nv_pk_fma_f32 v[32:33], v[228:229], v[18:19], v[32:33]\nv_add_f32 v40, v32, v33\ns_barrier\ns_branch .Lge\n"
      ".Lg13:\ns_waitcnt vmcnt(4)\ns_barrier\nds_read_b64_tr_b16 v[64:65], v104 offset:45056\nds_read_b64_tr_b16 v[66:67], v104 offset:46080\nds_read_b64_tr_b16 v[68:69], v104 offset:53248\n"
      "ds_read_b64_tr_b16 v[70:71], v104 offset:54272\nds_read_b64_tr_b16 v[72:73], v105 offset:45056\nds_read_b64_tr_b16 v[74:75], v105 offset:46080\n"
      "ds_read_b64_tr_b16 v[76:77], v105 offset:53248\nds_read_b64_tr_b16 v[78:79], v105 offset:54272\nds_read_b64_tr_b16 v[80:81], v106 offset:45056\n"
      "ds_read_b64_tr_b16 v[82:83], v106 offset:46080\nds_read_b64_tr_b16 v[84:85], v106 offset:53248\nds_read_b64_tr_b16 v[86:87], v106 offset:54272\n"
      "ds_read_b64_tr_b16 v[88:89], v107 offset:45056\nds_read_b64_tr_b16 v[90:91], v107 offset:46080\nds_read_b64_tr_b16 v[92:93], v107 offset:53248\n"
      "ds_read_b64_tr_b16 v[94:95], v107 offset:54272\nds_read_b128 v[0:3], v100 offset:45056\nds_read_b128 v[4:7], v101 offset:45056\nds_read_b128 v[8:11], v120 offset:45056\n"
      "ds_read_b128 v[12:15], v121 offset:45056\nds_read_b128 v[16:19], v96 offset:45056\ns_waitcnt lgkmcnt(1)\n.p2align 3\n"
      ".Lgl13:\nv_mfma_f32_16x16x32_bf16 v[124:127], v[64:67], v[0:3], v[124:127]\nds_read_b128 v[20:23], v97 offset:45056\nv_mfma_f32_16x16x32_bf16 v[124:127], v[68:71], v[4:7], v[124:127]\n"
      "ds_read_b128 v[24:27], v96 offset:47104\nv_mfma_f32_16x16x32_bf16 v[128:131], v[76:79], v[4:7], v[128:131]\nds_read_b128 v[28:31], v97 offset:47104\n"
      "v_mfma_f32_16x16x32_bf16 v[128:131], v[72:75], v[0:3], v[128:131]\nv_mfma_f32_16x16x32_bf16 v[152:155], v[88:91], v[8:11], v[152:155]\nds_read_b128 v[0:3], v96 offset:49152\n"
      "v_mfma_f32_16x16x32_bf16 v[152:155], v[92:95], v[12:15], v[152:155]\nds_read_b128 v[4:7], v97 offset:49152\nv_mfma_f32_16x16x32_bf16 v[148:151], v[84:87], v[12:15], v[148:151]\n"
      "v_mfma_f32_16x16x32_bf16 v[148:151], v[80:83], v[8:11], v[148:151]\nv_mfma_f32_16x16x32_bf16 v[144:147], v[72:75], v[8:11], v[144:147]\n"
      "v_mfma_f32_16x16x32_bf16 v[144:147], v[76:79], v[12:15], v[144:147]\nv_mfma_f32_16x16x32_bf16 v[140:143], v[68:71], v[12:15], v[140:143]\ns_waitcnt lgkmcnt(4)\n"
      "v_mfma_f32_16x16x32_bf16 v[140:143], v[64:67], v[8:11], v[140:143]\nv_mfma_f32_16x16x32_bf16 v[156:159], v[64:67], v[16:19], v[156:159]\nds_read_b128 v[8:11], v96 offset:51200\n"
      "v_mfma_f32_16x16x32_bf16 v[156:159], v[68:71], v[20:23], v[156:159]\nds_read_b128 v[12:15], v97 offset:51200\nv_mfma_f32_16x16x32_bf16 v[160:163], v[76:79], v[20:23], v[160:163]\n"
      "v_mfma_f32_16x16x32_bf16 v[160:163], v[72:75], v[16:19], v[160:163]\nv_mfma_f32_16x16x32_bf16 v[164:167], v[80:83], v[16:19], v[164:167]\n"
      "v_mfma_f32_16x16x32_bf16 v[164:167], v[84:87], v[20:23], v[164:167]\nv_mfma_f32_16x16x32_bf16 v[168:171], v[92:95], v[20:23], v[168:171]\ns_waitcnt lgkmcnt(2)\n"
      "v_mfma_f32_16x16x32_bf16 v[168:171], v[88:91], v[16:19], v[168:171]\nv_mfma_f32_16x16x32_bf16 v[184:187], v[88:91], v[24:27], v[184:187]\nds_read_b128 v[16:19], v96 offset:53248\n"
      "v_mfma_f32_16x16x32_bf16 v[184:187], v[92:95], v[28:31], v[184:187]\nds_read_b128 v[20:23], v97 offset:53248\nv_mfma_f32_16x16x32_bf16 v[180:183], v[84:87], v[28:31], v[180:183]\n"
      "v_mfma_f32_16x16x32_bf16 v[180:183], v[80:83], v[24:27], v[180:183]\nv_mfma_f32_16x16x32_bf16 v[176:179], v[72:75], v[24:27], v[176:179]\n"
      "v_mfma_f32_16x16x32_bf16 v[176:179], v[76:79], v[28:31], v[176:179]\ns_waitcnt vmcnt(0) lgkmcnt(0)\nv_mfma_f32_16x16x32_bf16 v[172:175], v[68:71], v[28:31], v[172:175]\ns_barrier\n"
      "v_mfma_f32_16x16x32_bf16 v[172:175], v[64:67], v[24:27], v[172:175]\nds_read_b64_tr_b16 v[32:33], v108 offset:0\nv_mfma_f32_16x16x32_bf16 v[188:191], v[64:67], v[0:3], v[188:191]\n"
      "ds_read_b64_tr_b16 v[34:35], v108 offset:1024\nv_mfma_f32_16x16x32_bf16 v[188:191], v[68:71], v[4:7], v[188:191]\nds_read_b64_tr_b16 v[36:37], v108 offset:8192\n"
      "v_mfma_f32_16x16x32_bf16 v[192:195], v[76:79], v[4:7], v[192:195]\nds_read_b64_tr_b16 v[38:39], v108 offset:9216\nv_mfma_f32_16x16x32_bf16 v[192:195], v[72:75], v[0:3], v[192:195]\n"
      "ds_read_b64_tr_b16 v[40:41], v109 offset:0\nv_mfma_f32_16x16x32_bf16 v[196:199], v[80:83], v[0:3], v[196:199]\nds_read_b64_tr_b16 v[42:43], v109 offset:1024\n"
      "v_mfma_f32_16x16x32_bf16 v[196:199], v[84:87], v[4:7], v[196:199]\nds_read_b64_tr_b16 v[44:45], v109 offset:8192\nv_mfma_f32_16x16x32_bf16 v[200:203], v[92:95], v[4:7], v[200:203]\n"
      "ds_read_b64_tr_b16 v[46:47], v109 offset:9216\nv_mfma_f32_16x16x32_bf16 v[200:203], v[88:91], v[0:3], v[200:203]\nds_read_b64_tr_b16 v[48:49], v110 offset:0\n"
      "v_mfma_f32_16x16x32_bf16 v[216:219], v[88:91], v[8:11], v[216:219]\nds_read_b128 v[0:3], v102 offset:0\nv_mfma_f32_16x16x32_bf16 v[216:219], v[92:95], v[12:15], v[216:219]\n"
      "ds_read_b128 v[4:7], v103 offset:0\nv_mfma_f32_16x16x32_bf16 v[212:215], v[84:87], v[12:15], v[212:215]\nds_read_b64_tr_b16 v[50:51], v110 offset:1024\n"
      "v_mfma_f32_16x16x32_bf16 v[212:215], v[80:83], v[8:11], v[212:215]\nds_read_b64_tr_b16 v[52:53], v110 offset:8192\nv_mfma_f32_16x16x32_bf16 v[208:211], v[72:75], v[8:11], v[208:211]\n"
      "ds_read_b64_tr_b16 v[54:55], v110 offset:9216\nv_mfma_f32_16x16x32_bf16 v[208:211], v[76:79], v[12:15], v[208:211]\nds_read_b64_tr_b16 v[56:57], v111 offset:0\n"
      "v_mfma_f32_16x16x32_bf16 v[204:207], v[68:71], v[12:15], v[204:207]\nds_read_b64_tr_b16 v[58:59], v111 offset:1024\nv_mfma_f32_16x16x32_bf16 v[204:207], v[64:67], v[8:11], v[204:207]\n"
      "ds_read_b64_tr_b16 v[60:61], v111 offset:8192\nv_mfma_f32_16x16x32_bf16 v[220:223], v[64:67], v[16:19], v[220:223]\nds_read_b128 v[8:11], v122 offset:0\n"
      "v_mfma_f32_16x16x32_bf16 v[220:223], v[68:71], v[20:23], v[220:223]\nds_read_b128 v[12:15], v123 offset:0\nv_mfma_f32_16x16x32_bf16 v[224:227], v[76:79], v[20:23], v[224:227]\n"
      "ds_read_b64_tr_b16 v[62:63], v111 offset:9216\nv_mfma_f32_16x16x32_bf16 v[224:227], v[72:75], v[16:19], v[224:227]\nv_mfma_f32_16x16x32_bf16 v[228:231], v[80:83], v[16:19], v[228:231]\n"
      "v_mfma_f32_16x16x32_bf16 v[228:231], v[84:87], v[20:23], v[228:231]\nv_mfma_f32_16x16x32_bf16 v[232:235], v[92:95], v[20:23], v[232:235]\ns_waitcnt lgkmcnt(0)\n"
      "v_mfma_f32_16x16x32_bf16 v[232:235], v[88:91], v[16:19], v[232:235]\nds_read_b128 v[16:19], v98 offset:0\nv_mfma_f32_16x16x32_bf16 v[124:127], v[32:35], v[0:3], v[124:127]\n"
      "ds_read_b128 v[20:23], v99 offset:0\nv_mfma_f32_16x16x32_bf16 v[124:127], v[36:39], v[4:7], v[124:127]\nds_read_b128 v[24:27], v98 offset:2048\n"
      "v_mfma_f32_16x16x32_bf16 v[128:131], v[44:47], v[4:7], v[128:131]\nds_read_b128 v[28:31], v99 offset:2048\nv_mfma_f32_16x16x32_bf16 v[128:131], v[40:43], v[0:3], v[128:131]\n"
      "v_mfma_f32_16x16x32_bf16 v[152:155], v[56:59], v[8:11], v[152:155]\nds_read_b128 v[0:3], v98 offset:4096\nv_mfma_f32_16x16x32_bf16 v[152:155], v[60:63], v[12:15], v[152:155]\n"
      "ds_read_b128 v[4:7], v99 offset:4096\nv_mfma_f32_16x16x32_bf16 v[148:151], v[52:55], v[12:15], v[148:151]\nv_mfma_f32_16x16x32_bf16 v[148:151], v[48:51], v[8:11], v[148:151]\n"
      "v_mfma_f32_16x16x32_bf16 v[144:147], v[40:43], v[8:11], v[144:147]\nv_mfma_f32_16x16x32_bf16 v[144:147], v[44:47], v[12:15], v[144:147]\n"
      "v_mfma_f32_16x16x32_bf16 v[140:143], v[36:39], v[12:15], v[140:143]\ns_waitcnt lgkmcnt(4)\nv_mfma_f32_16x16x32_bf16 v[140:143], v[32:35], v[8:11], v[140:143]\n"
      "v_mfma_f32_16x16x32_bf16 v[156:159], v[32:35], v[16:19], v[156:159]\nds_read_b128 v[8:11], v98 offset:6144\nv_mfma_f32_16x16x32_bf16 v[156:159], v[36:39], v[20:23], v[156:159]\n"
      "ds_read_b128 v[12:15], v99 offset:6144\nv_mfma_f32_16x16x32_bf16 v[160:163], v[44:47], v[20:23], v[160:163]\nv_mfma_f32_16x16x32_bf16 v[160:163], v[40:43], v[16:19], v[160:163]\n"
      "v_mfma_f32_16x16x32_bf16 v[164:167], v[48:51], v[16:19], v[164:167]\nv_mfma_f32_16x16x32_bf16 v[164:167], v[52:55], v[20:23], v[164:167]\n"
      "v_mfma_f32_16x16x32_bf16 v[168:171], v[60:63], v[20:23], v[168:171]\ns_waitcnt lgkmcnt(2)\nv_mfma_f32_16x16x32_bf16 v[168:171], v[56:59], v[16:19], v[168:171]\n"
      "v_mfma_f32_16x16x32_bf16 v[184:187], v[56:59], v[24:27], v[184:187]\nds_read_b128 v[16:19], v98 offset:8192\nv_mfma_f32_16x16x32_bf16 v[184:187], v[60:63], v[28:31], v[184:187]\n"
      "ds_read_b128 v[20:23], v99 offset:8192\nv_mfma_f32_16x16x32_bf16 v[180:183], v[52:55], v[28:31], v[180:183]\nv_mfma_f32_16x16x32_bf16 v[180:183], v[48:51], v[24:27], v[180:183]\n"
      "v_mfma_f32_16x16x32_bf16 v[176:179], v[40:43], v[24:27], v[176:179]\nv_mfma_f32_16x16x32_bf16 v[176:179], v[44:47], v[28:31], v[176:179]\ns_waitcnt vmcnt(0) lgkmcnt(0)\n"
      "v_mfma_f32_16x16x32_bf16 v[172:175], v[36:39], v[28:31], v[172:175]\ns_barrier\nv_mfma_f32_16x16x32_bf16 v[172:175], v[32:35], v[24:27], v[172:175]\n"
      "ds_read_b64_tr_b16 v[64:65], v104 offset:0\nv_mfma_f32_16x16x32_bf16 v[188:191], v[32:35], v[0:3], v[188:191]\nds_read_b64_tr_b16 v[66:67], v104 offset:1024\n"
      "v_mfma_f32_16x16x32_bf16 v[188:191], v[36:39], v[4:7], v[188:191]\nds_read_b64_tr_b16 v[68:69], v104 offset:8192\nv_mfma_f32_16x16x32_bf16 v[192:195], v[44:47], v[4:7], v[192:195]\n"
      "ds_read_b64_tr_b16 v[70:71], v104 offset:9216\nv_mfma_f32_16x16x32_bf16 v[192:195], v[40:43], v[0:3], v[192:195]\nds_read_b64_tr_b16 v[72:73], v105 offset:0\n"
      "v_mfma_f32_16x16x32_bf16 v[196:199], v[48:51], v[0:3], v[196:199]\nds_read_b64_tr_b16 v[74:75], v105 offset:1024\nv_mfma_f32_16x16x32_bf16 v[196:199], v[52:55], v[4:7], v[196:199]\n"
      "ds_read_b64_tr_b16 v[76:77], v105 offset:8192\nv_mfma_f32_16x16x32_bf16 v[200:203], v[60:63], v[4:7], v[200:203]\nds_read_b64_tr_b16 v[78:79], v105 offset:9216\n"
      "v_mfma_f32_16x16x32_bf16 v[200:203], v[56:59], v[0:3], v[200:203]\nds_read_b64_tr_b16 v[80:81], v106 offset:0\nv_mfma_f32_16x16x32_bf16 v[216:219], v[56:59], v[8:11], v[216:219]\n"
      "ds_read_b128 v[0:3], v100 offset:0\nv_mfma_f32_16x16x32_bf16 v[216:219], v[60:63], v[12:15], v[216:219]\nds_read_b128 v[4:7], v101 offset:0\n"
      "v_mfma_f32_16x16x32_bf16 v[212:215], v[52:55], v[12:15], v[212:215]\nds_read_b64_tr_b16 v[82:83], v106 offset:1024\nv_mfma_f32_16x16x32_bf16 v[212:215], v[48:51], v[8:11], v[212:215]\n"
      "ds_read_b64_tr_b16 v[84:85], v106 offset:8192\nv_mfma_f32_16x16x32_bf16 v[208:211], v[40:43], v[8:11], v[208:211]\nds_read_b64_tr_b16 v[86:87], v106 offset:9216\n"
      "v_mfma_f32_16x16x32_bf16 v[208:211], v[44:47], v[12:15], v[208:211]\nds_read_b64_tr_b16 v[88:89], v107 offset:0\nv_mfma_f32_16x16x32_bf16 v[204:207], v[36:39], v[12:15], v[204:207]\n"
      "ds_read_b64_tr_b16 v[90:91], v107 offset:1024\nv_mfma_f32_16x16x32_bf16 v[204:207], v[32:35], v[8:11], v[204:207]\nds_read_b64_tr_b16 v[92:93], v107 offset:8192\n"
      "v_mfma_f32_16x16x32_bf16 v[220:223], v[32:35], v[16:19], v[220:223]\nds_read_b128 v[8:11], v120 offset:0\nv_mfma_f32_16x16x32_bf16 v[220:223], v[36:39], v[20:23], v[220:223]\n"
      "ds_read_b128 v[12:15], v121 offset:0\nv_mfma_f32_16x16x32_bf16 v[224:227], v[44:47], v[20:23], v[224:227]\nds_read_b64_tr_b16 v[94:95], v107 offset:9216\n"
      "v_mfma_f32_16x16x32_bf16 v[224:227], v[40:43], v[16:19], v[224:227]\nv_mfma_f32_16x16x32_bf16 v[228:231], v[48:51], v[16:19], v[228:231]\n"
      "v_mfma_f32_16x16x32_bf16 v[228:231], v[52:55], v[20:23], v[228:231]\nv_mfma_f32_16x16x32_bf16 v[232:235], v[60:63], v[20:23], v[232:235]\ns_waitcnt lgkmcnt(0)\n"
      "v_mfma_f32_16x16x32_bf16 v[232:235], v[56:59], v[16:19], v[232:235]\nds_read_b128 v[16:19], v96 offset:0\nv_mfma_f32_16x16x32_bf16 v[124:127], v[64:67], v[0:3], v[124:127]\n"
      "ds_read_b128 v[20:23], v97 offset:0\nv_mfma_f32_16x16x32_bf16 v[124:127], v[68:71], v[4:7], v[124:127]\nds_read_b128 v[24:27], v96 offset:2048\n"
      "v_mfma_f32_16x16x32_bf16 v[128:131], v[76:79], v[4:7], v[128:131]\nds_read_b128 v[28:31], v97 offset:2048\nv_mfma_f32_16x16x32_bf16 v[128:131], v[72:75], v[0:3], v[128:131]\n"
      "v_mfma_f32_16x16x32_bf16 v[152:155], v[88:91], v[8:11], v[152:155]\nds_read_b128 v[0:3], v96 offset:4096\nv_mfma_f32_16x16x32_bf16 v[152:155], v[92:95], v[12:15], v[152:155]\n"
      "ds_read_b128 v[4:7], v97 offset:4096\nv_mfma_f32_16x16x32_bf16 v[148:151], v[84:87], v[12:15], v[148:151]\nv_mfma_f32_16x16x32_bf16 v[148:151], v[80:83], v[8:11], v[148:151]\n"
      "v_mfma_f32_16x16x32_bf16 v[144:147], v[72:75], v[8:11], v[144:147]\nv_mfma_f32_16x16x32_bf16 v[144:147], v[76:79], v[12:15], v[144:147]\n"
      "v_mfma_f32_16x16x32_bf16 v[140:143], v[68:71], v[12:15], v[140:143]\ns_waitcnt lgkmcnt(4)\nv_mfma_f32_16x16x32_bf16 v[140:143], v[64:67], v[8:11], v[140:143]\n"
      "v_mfma_f32_16x16x32_bf16 v[156:159], v[64:67], v[16:19], v[156:159]\nds_read_b128 v[8:11], v96 offset:6144\nv_mfma_f32_16x16x32_bf16 v[156:159], v[68:71], v[20:23], v[156:159]\n"
      "ds_read_b128 v[12:15], v97 offset:6144\nv_mfma_f32_16x16x32_bf16 v[160:163], v[76:79], v[20:23], v[160:163]\nv_mfma_f32_16x16x32_bf16 v[160:163], v[72:75], v[16:19], v[160:163]\n"
      "v_mfma_f32_16x16x32_bf16 v[164:167], v[80:83], v[16:19], v[164:167]\nv_mfma_f32_16x16x32_bf16 v[164:167], v[84:87], v[20:23], v[164:167]\n"
      "v_mfma_f32_16x16x32_bf16 v[168:171], v[92:95], v[20:23], v[168:171]\ns_waitcnt lgkmcnt(2)\nv_mfma_f32_16x16x32_bf16 v[168:171], v[88:91], v[16:19], v[168:171]\n"
      "v_mfma_f32_16x16x32_bf16 v[184:187], v[88:91], v[24:27], v[184:187]\nds_read_b128 v[16:19], v96 offset:8192\nv_mfma_f32_16x16x32_bf16 v[184:187], v[92:95], v[28:31], v[184:187]\n"
      "ds_read_b128 v[20:23], v97 offset:8192\nv_mfma_f32_16x16x32_bf16 v[180:183], v[84:87], v[28:31], v[180:183]\nv_mfma_f32_16x16x32_bf16 v[180:183], v[80:83], v[24:27], v[180:183]\n"
      "v_mfma_f32_16x16x32_bf16 v[176:179], v[72:75], v[24:27], v[176:179]\nv_mfma_f32_16x16x32_bf16 v[176:179], v[76:79], v[28:31], v[176:179]\ns_waitcnt vmcnt(0) lgkmcnt(0)\n"
      "v_mfma_f32_16x16x32_bf16 v[172:175], v[68:71], v[28:31], v[172:175]\ns_barrier\nv_mfma_f32_16x16x32_bf16 v[172:175], v[64:67], v[24:27], v[172:175]\n"
      "ds_read_b64_tr_b16 v[32:33], v104 offset:45056\nv_mfma_f32_16x16x32_bf16 v[188:191], v[64:67], v[0:3], v[188:191]\nds_read_b64_tr_b16 v[34:35], v104 offset:46080\n"
      "v_mfma_f32_16x16x32_bf16 v[188:191], v[68:71], v[4:7], v[188:191]\nds_read_b64_tr_b16 v[36:37], v104 offset:53248\nv_mfma_f32_16x16x32_bf16 v[192:195], v[76:79], v[4:7], v[192:195]\n"
      "ds_read_b64_tr_b16 v[38:39], v104 offset:54272\nv_mfma_f32_16x16x32_bf16 v[192:195], v[72:75], v[0:3], v[192:195]\nds_read_b64_tr_b16 v[40:41], v105 offset:45056\n"
      "v_mfma_f32_16x16x32_bf16 v[196:199], v[80:83], v[0:3], v[196:199]\nds_read_b64_tr_b16 v[42:43], v105 offset:46080\nv_mfma_f32_16x16x32_bf16 v[196:199], v[84:87], v[4:7], v[196:199]\n"
      "ds_read_b64_tr_b16 v[44:45], v105 offset:53248\nv_mfma_f32_16x16x32_bf16 v[200:203], v[92:95], v[4:7], v[200:203]\nds_read_b64_tr_b16 v[46:47], v105 offset:54272\n"
      "v_mfma_f32_16x16x32_bf16 v[200:203], v[88:91], v[0:3], v[200:203]\nds_read_b64_tr_b16 v[48:49], v106 offset:45056\nv_mfma_f32_16x16x32_bf16 v[216:219], v[88:91], v[8:11], v[216:219]\n"
      "ds_read_b128 v[0:3], v100 offset:45056\nv_mfma_f32_16x16x32_bf16 v[216:219], v[92:95], v[12:15], v[216:219]\nds_read_b128 v[4:7], v101 offset:45056\n"
      "v_mfma_f32_16x16x32_bf16 v[212:215], v[84:87], v[12:15], v[212:215]\nds_read_b64_tr_b16 v[50:51], v106 offset:46080\nv_mfma_f32_16x16x32_bf16 v[212:215], v[80:83], v[8:11], v[212:215]\n"
      "ds_read_b64_tr_b16 v[52:53], v106 offset:53248\nv_mfma_f32_16x16x32_bf16 v[208:211], v[72:75], v[8:11], v[208:211]\nds_read_b64_tr_b16 v[54:55], v106 offset:54272\n"
      "v_mfma_f32_16x16x32_bf16 v[208:211], v[76:79], v[12:15], v[208:211]\nds_read_b64_tr_b16 v[56:57], v107 offset:45056\nv_mfma_f32_16x16x32_bf16 v[204:207], v[68:71], v[12:15], v[204:207]\n"
      "ds_read_b64_tr_b16 v[58:59], v107 offset:46080\nv_mfma_f32_16x16x32_bf16 v[204:207], v[64:67], v[8:11], v[204:207]\nds_read_b64_tr_b16 v[60:61], v107 offset:53248\n"
      "v_mfma_f32_16x16x32_bf16 v[220:223], v[64:67], v[16:19], v[220:223]\nds_read_b128 v[8:11], v120 offset:45056\nv_mfma_f32_16x16x32_bf16 v[220:223], v[68:71], v[20:23], v[220:223]\n"
      "ds_read_b128 v[12:15], v121 offset:45056\nv_mfma_f32_16x16x32_bf16 v[224:227], v[76:79], v[20:23], v[224:227]\nds_read_b64_tr_b16 v[62:63], v107 offset:54272\n"
      "v_mfma_f32_16x16x32_bf16 v[224:227], v[72:75], v[16:19], v[224:227]\nv_mfma_f32_16x16x32_bf16 v[228:231], v[80:83], v[16:19], v[228:231]\n"
      "v_mfma_f32_16x16x32_bf16 v[228:231], v[84:87], v[20:23], v[228:231]\nv_mfma_f32_16x16x32_bf16 v[232:235], v[92:95], v[20:23], v[232:235]\ns_waitcnt lgkmcnt(0)\n"
      "v_mfma_f32_16x16x32_bf16 v[232:235], v[88:91], v[16:19], v[232:235]\nds_read_b128 v[16:19], v96 offset:45056\nv_mfma_f32_16x16x32_bf16 v[124:127], v[32:35], v[0:3], v[124:127]\n"
      "ds_read_b128 v[20:23], v97 offset:45056\nv_mfma_f32_16x16x32_bf16 v[124:127], v[36:39], v[4:7], v[124:127]\nds_read_b128 v[24:27], v96 offset:47104\n"
      "v_mfma_f32_16x16x32_bf16 v[128:131], v[44:47], v[4:7], v[128:131]\nds_read_b128 v[28:31], v97 offset:47104\nv_mfma_f32_16x16x32_bf16 v[128:131], v[40:43], v[0:3], v[128:131]\n"
      "v_mfma_f32_16x16x32_bf16 v[152:155], v[56:59], v[8:11], v[152:155]\nds_read_b128 v[0:3], v96 offset:49152\nv_mfma_f32_16x16x32_bf16 v[152:155], v[60:63], v[12:15], v[152:155]\n"
      "ds_read_b128 v[4:7], v97 offset:49152\nv_mfma_f32_16x16x32_bf16 v[148:151], v[52:55], v[12:15], v[148:151]\nv_mfma_f32_16x16x32_bf16 v[148:151], v[48:51], v[8:11], v[148:151]\n"
      "v_mfma_f32_16x16x32_bf16 v[144:147], v[40:43], v[8:11], v[144:147]\nv_mfma_f32_16x16x32_bf16 v[144:147], v[44:47], v[12:15], v[144:147]\n"
      "v_mfma_f32_16x16x32_bf16 v[140:143], v[36:39], v[12:15], v[140:143]\ns_waitcnt lgkmcnt(4)\nv_mfma_f32_16x16x32_bf16 v[140:143], v[32:35], v[8:11], v[140:143]\n"
      "v_mfma_f32_16x16x32_bf16 v[156:159], v[32:35], v[16:19], v[156:159]\nds_read_b128 v[8:11], v96 offset:51200\nv_mfma_f32_16x16x32_bf16 v[156:159], v[36:39], v[20:23], v[156:159]\n"
      "ds_read_b128 v[12:15], v97 offset:51200\nv_mfma_f32_16x16x32_bf16 v[160:163], v[44:47], v[20:23], v[160:163]\nv_mfma_f32_16x16x32_bf16 v[160:163], v[40:43], v[16:19], v[160:163]\n"
      "v_mfma_f32_16x16x32_bf16 v[164:167], v[48:51], v[16:19], v[164:167]\nv_mfma_f32_16x16x32_bf16 v[164:167], v[52:55], v[20:23], v[164:167]\n"
      "v_mfma_f32_16x16x32_bf16 v[168:171], v[60:63], v[20:23], v[168:171]\ns_waitcnt lgkmcnt(2)\nv_mfma_f32_16x16x32_bf16 v[168:171], v[56:59], v[16:19], v[168:171]\n"
      "v_mfma_f32_16x16x32_bf16 v[184:187], v[56:59], v[24:27], v[184:187]\nds_read_b128 v[16:19], v96 offset:53248\nv_mfma_f32_16x16x32_bf16 v[184:187], v[60:63], v[28:31], v[184:187]\n"
      "ds_read_b128 v[20:23], v97 offset:53248\nv_mfma_f32_16x16x32_bf16 v[180:183], v[52:55], v[28:31], v[180:183]\nv_mfma_f32_16x16x32_bf16 v[180:183], v[48:51], v[24:27], v[180:183]\n"
      "v_mfma_f32_16x16x32_bf16 v[176:179], v[40:43], v[24:27], v[176:179]\nv_mfma_f32_16x16x32_bf16 v[176:179], v[44:47], v[28:31], v[176:179]\ns_waitcnt vmcnt(0) lgkmcnt(0)\n"
      "v_mfma_f32_16x16x32_bf16 v[172:175], v[36:39], v[28:31], v[172:175]\ns_barrier\nv_mfma_f32_16x16x32_bf16 v[172:175], v[32:35], v[24:27], v[172:175]\n"
      "ds_read_b64_tr_b16 v[64:65], v108 offset:0\nv_mfma_f32_16x16x32_bf16 v[188:191], v[32:35], v[0:3], v[188:191]\nds_read_b64_tr_b16 v[66:67], v108 offset:1024\n"
      "v_mfma_f32_16x16x32_bf16 v[188:191], v[36:39], v[4:7], v[188:191]\nds_read_b64_tr_b16 v[68:69], v108 offset:8192\nv_mfma_f32_16x16x32_bf16 v[192:195], v[44:47], v[4:7], v[192:195]\n"
      "ds_read_b64_tr_b16 v[70:71], v108 offset:9216\nv_mfma_f32_16x16x32_bf16 v[192:195], v[40:43], v[0:3], v[192:195]\nds_read_b64_tr_b16 v[72:73], v109 offset:0\n"
      "v_mfma_f32_16x16x32_bf16 v[196:199], v[48:51], v[0:3], v[196:199]\nds_read_b64_tr_b16 v[74:75], v109 offset:1024\nv_mfma_f32_16x16x32_bf16 v[196:199], v[52:55], v[4:7], v[196:199]\n"
      "ds_read_b64_tr_b16 v[76:77], v109 offset:8192\nv_mfma_f32_16x16x32_bf16 v[200:203], v[60:63], v[4:7], v[200:203]\nds_read_b64_tr_b16 v[78:79], v109 offset:9216\n"
      "v_mfma_f32_16x16x32_bf16 v[200:203], v[56:59], v[0:3], v[200:203]\nds_read_b64_tr_b16 v[80:81], v110 offset:0\nv_mfma_f32_16x16x32_bf16 v[216:219], v[56:59], v[8:11], v[216:219]\n"
      "ds_read_b128 v[0:3], v102 offset:0\nv_mfma_f32_16x16x32_bf16 v[216:219], v[60:63], v[12:15], v[216:219]\nds_read_b128 v[4:7], v103 offset:0\n"
      "v_mfma_f32_16x16x32_bf16 v[212:215], v[52:55], v[12:15], v[212:215]\nds_read_b64_tr_b16 v[82:83], v110 offset:1024\nv_mfma_f32_16x16x32_bf16 v[212:215], v[48:51], v[8:11], v[212:215]\n"
      "ds_read_b64_tr_b16 v[84:85], v110 offset:8192\nv_mfma_f32_16x16x32_bf16 v[208:211], v[40:43], v[8:11], v[208:211]\nds_read_b64_tr_b16 v[86:87], v110 offset:9216\n"
      "v_mfma_f32_16x16x32_bf16 v[208:211], v[44:47], v[12:15], v[208:211]\nds_read_b64_tr_b16 v[88:89], v111 offset:0\nv_mfma_f32_16x16x32_bf16 v[204:207], v[36:39], v[12:15], v[204:207]\n"
      "ds_read_b64_tr_b16 v[90:91], v111 offset:1024\nv_mfma_f32_16x16x32_bf16 v[204:207], v[32:35], v[8:11], v[204:207]\nds_read_b64_tr_b16 v[92:93], v111 offset:8192\n"
      "v_mfma_f32_16x16x32_bf16 v[220:223], v[32:35], v[16:19], v[220:223]\nds_read_b128 v[8:11], v122 offset:0\nv_mfma_f32_16x16x32_bf16 v[220:223], v[36:39], v[20:23], v[220:223]\n"
      "ds_read_b128 v[12:15], v123 offset:0\nv_mfma_f32_16x16x32_bf16 v[224:227], v[44:47], v[20:23], v[224:227]\nds_read_b64_tr_b16 v[94:95], v111 offset:9216\n"
      "v_mfma_f32_16x16x32_bf16 v[224:227], v[40:43], v[16:19], v[224:227]\nv_mfma_f32_16x16x32_bf16 v[228:231], v[48:51], v[16:19], v[228:231]\n"
      "v_mfma_f32_16x16x32_bf16 v[228:231], v[52:55], v[20:23], v[228:231]\nv_mfma_f32_16x16x32_bf16 v[232:235], v[60:63], v[20:23], v[232:235]\ns_waitcnt lgkmcnt(0)\n"
      "v_mfma_f32_16x16x32_bf16 v[232:235], v[56:59], v[16:19], v[232:235]\nds_read_b128 v[16:19], v98 offset:0\nv_mfma_f32_16x16x32_bf16 v[124:127], v[64:67], v[0:3], v[124:127]\n"
      "ds_read_b128 v[20:23], v99 offset:0\nv_mfma_f32_16x16x32_bf16 v[124:127], v[68:71], v[4:7], v[124:127]\nds_read_b128 v[24:27], v98 offset:2048\n"
      "v_mfma_f32_16x16x32_bf16 v[128:131], v[76:79], v[4:7], v[128:131]\nds_read_b128 v[28:31], v99 offset:2048\nv_mfma_f32_16x16x32_bf16 v[128:131], v[72:75], v[0:3], v[128:131]\n"
      "v_mfma_f32_16x16x32_bf16 v[152:155], v[88:91], v[8:11], v[152:155]\nds_read_b128 v[0:3], v98 offset:4096\nv_mfma_f32_16x16x32_bf16 v[152:155], v[92:95], v[12:15], v[152:155]\n"
      "ds_read_b128 v[4:7], v99 offset:4096\nv_mfma_f32_16x16x32_bf16 v[148:151], v[84:87], v[12:15], v[148:151]\nv_mfma_f32_16x16x32_bf16 v[148:151], v[80:83], v[8:11], v[148:151]\n"
      "v_mfma_f32_16x16x32_bf16 v[144:147], v[72:75], v[8:11], v[144:147]\nv_mfma_f32_16x16x32_bf16 v[144:147], v[76:79], v[12:15], v[144:147]\n"
      "v_mfma_f32_16x16x32_bf16 v[140:143], v[68:71], v[12:15], v[140:143]\ns_waitcnt lgkmcnt(4)\nv_mfma_f32_16x16x32_bf16 v[140:143], v[64:67], v[8:11], v[140:143]\n"
      "v_mfma_f32_16x16x32_bf16 v[156:159], v[64:67], v[16:19], v[156:159]\nds_read_b128 v[8:11], v98 offset:6144\nv_mfma_f32_16x16x32_bf16 v[156:159], v[68:71], v[20:23], v[156:159]\n"
      "ds_read_b128 v[12:15], v99 offset:6144\nv_mfma_f32_16x16x32_bf16 v[160:163], v[76:79], v[20:23], v[160:163]\nv_mfma_f32_16x16x32_bf16 v[160:163], v[72:75], v[16:19], v[160:163]\n"
      "v_mfma_f32_16x16x32_bf16 v[164:167], v[80:83], v[16:19], v[164:167]\nv_mfma_f32_16x16x32_bf16 v[164:167], v[84:87], v[20:23], v[164:167]\n"
      "v_mfma_f32_16x16x32_bf16 v[168:171], v[92:95], v[20:23], v[168:171]\ns_waitcnt lgkmcnt(2)\nv_mfma_f32_16x16x32_bf16 v[168:171], v[88:91], v[16:19], v[168:171]\n"
      "v_mfma_f32_16x16x32_bf16 v[184:187], v[88:91], v[24:27], v[184:187]\nds_read_b128 v[16:19], v98 offset:8192\nv_mfma_f32_16x16x32_bf16 v[184:187], v[92:95], v[28:31], v[184:187]\n"
      "ds_read_b128 v[20:23], v99 offset:8192\nv_mfma_f32_16x16x32_bf16 v[180:183], v[84:87], v[28:31], v[180:183]\nv_mfma_f32_16x16x32_bf16 v[180:183], v[80:83], v[24:27], v[180:183]\n"
      "v_mfma_f32_16x16x32_bf16 v[176:179], v[72:75], v[24:27], v[176:179]\nv_mfma_f32_16x16x32_bf16 v[176:179], v[76:79], v[28:31], v[176:179]\ns_waitcnt vmcnt(0) lgkmcnt(0)\n"
      "v_mfma_f32_16x16x32_bf16 v[172:175], v[68:71], v[28:31], v[172:175]\ns_barrier\nv_mfma_f32_16x16x32_bf16 v[172:175], v[64:67], v[24:27], v[172:175]\n"
      "ds_read_b64_tr_b16 v[32:33], v104 offset:0\nv_mfma_f32_16x16x32_bf16 v[188:191], v[64:67], v[0:3], v[188:191]\nds_read_b64_tr_b16 v[34:35], v104 offset:1024\n"
      "v_mfma_f32_16x16x32_bf16 v[188:191], v[68:71], v[4:7], v[188:191]\nds_read_b64_tr_b16 v[36:37], v104 offset:8192\nv_mfma_f32_16x16x32_bf16 v[192:195], v[76:79], v[4:7], v[192:195]\n"
      "ds_read_b64_tr_b16 v[38:39], v104 offset:9216\nv_mfma_f32_16x16x32_bf16 v[192:195], v[72:75], v[0:3], v[192:195]\nds_read_b64_tr_b16 v[40:41], v105 offset:0\n"
      "v_mfma_f32_16x16x32_bf16 v[196:199], v[80:83], v[0:3], v[196:199]\nds_read_b64_tr_b16 v[42:43], v105 offset:1024\nv_mfma_f32_16x16x32_bf16 v[196:199], v[84:87], v[4:7], v[196:199]\n"
      "ds_read_b64_tr_b16 v[44:45], v105 offset:8192\nv_mfma_f32_16x16x32_bf16 v[200:203], v[92:95], v[4:7], v[200:203]\nds_read_b64_tr_b16 v[46:47], v105 offset:9216\n"
      "v_mfma_f32_16x16x32_bf16 v[200:203], v[88:91], v[0:3], v[200:203]\nds_read_b64_tr_b16 v[48:49], v106 offset:0\nv_mfma_f32_16x16x32_bf16 v[216:219], v[88:91], v[8:11], v[216:219]\n"
      "ds_read_b128 v[0:3], v100 offset:0\nv_mfma_f32_16x16x32_bf16 v[216:219], v[92:95], v[12:15], v[216:219]\nds_read_b128 v[4:7], v101 offset:0\n"
      "v_mfma_f32_16x16x32_bf16 v[212:215], v[84:87], v[12:15], v[212:215]\nds_read_b64_tr_b16 v[50:51], v106 offset:1024\nv_mfma_f32_16x16x32_bf16 v[212:215], v[80:83], v[8:11], v[212:215]\n"
      "ds_read_b64_tr_b16 v[52:53], v106 offset:8192\nv_mfma_f32_16x16x32_bf16 v[208:211], v[72:75], v[8:11], v[208:211]\nds_read_b64_tr_b16 v[54:55], v106 offset:9216\n"
      "v_mfma_f32_16x16x32_bf16 v[208:211], v[76:79], v[12:15], v[208:211]\nds_read_b64_tr_b16 v[56:57], v107 offset:0\nv_mfma_f32_16x16x32_bf16 v[204:207], v[68:71], v[12:15], v[204:207]\n"
      "ds_read_b64_tr_b16 v[58:59], v107 offset:1024\nv_mfma_f32_16x16x32_bf16 v[204:207], v[64:67], v[8:11], v[204:207]\nds_read_b64_tr_b16 v[60:61], v107 offset:8192\n"
      "v_mfma_f32_16x16x32_bf16 v[220:223], v[64:67], v[16:19], v[220:223]\nds_read_b128 v[8:11], v120 offset:0\nv_mfma_f32_16x16x32_bf16 v[220:223], v[68:71], v[20:23], v[220:223]\n"
      "ds_read_b128 v[12:15], v121 offset:0\nv_mfma_f32_16x16x32_bf16 v[224:227], v[76:79], v[20:23], v[224:227]\nds_read_b64_tr_b16 v[62:63], v107 offset:9216\n"
      "v_mfma_f32_16x16x32_bf16 v[224:227], v[72:75], v[16:19], v[224:227]\nv_mfma_f32_16x16x32_bf16 v[228:231], v[80:83], v[16:19], v[228:231]\n"
      "v_mfma_f32_16x16x32_bf16 v[228:231], v[84:87], v[20:23], v[228:231]\nv_mfma_f32_16x16x32_bf16 v[232:235], v[92:95], v[20:23], v[232:235]\ns_waitcnt lgkmcnt(0)\n"
      "v_mfma_f32_16x16x32_bf16 v[232:235], v[88:91], v[16:19], v[232:235]\nds_read_b128 v[16:19], v96 offset:0\nv_mfma_f32_16x16x32_bf16 v[124:127], v[32:35], v[0:3], v[124:127]\n"
      "ds_read_b128 v[20:23], v97 offset:0\nv_mfma_f32_16x16x32_bf16 v[124:127], v[36:39], v[4:7], v[124:127]\nds_read_b128 v[24:27], v96 offset:2048\n"
      "v_mfma_f32_16x16x32_bf16 v[128:131], v[44:47], v[4:7], v[128:131]\nds_read_b128 v[28:31], v97 offset:2048\nv_mfma_f32_16x16x32_bf16 v[128:131], v[40:43], v[0:3], v[128:131]\n"
      "v_mfma_f32_16x16x32_bf16 v[152:155], v[56:59], v[8:11], v[152:155]\nds_read_b128 v[0:3], v96 offset:4096\nv_mfma_f32_16x16x32_bf16 v[152:155], v[60:63], v[12:15], v[152:155]\n"
      "ds_read_b128 v[4:7], v97 offset:4096\nv_mfma_f32_16x16x32_bf16 v[148:151], v[52:55], v[12:15], v[148:151]\nv_mfma_f32_16x16x32_bf16 v[148:151], v[48:51], v[8:11], v[148:151]\n"
      "v_mfma_f32_16x16x32_bf16 v[144:147], v[40:43], v[8:11], v[144:147]\nv_mfma_f32_16x16x32_bf16 v[144:147], v[44:47], v[12:15], v[144:147]\n"
      "v_mfma_f32_16x16x32_bf16 v[140:143], v[36:39], v[12:15], v[140:143]\ns_waitcnt lgkmcnt(4)\nv_mfma_f32_16x16x32_bf16 v[140:143], v[32:35], v[8:11], v[140:143]\n"
      "v_mfma_f32_16x16x32_bf16 v[156:159], v[32:35], v[16:19], v[156:159]\nds_read_b128 v[8:11], v96 offset:6144\nv_mfma_f32_16x16x32_bf16 v[156:159], v[36:39], v[20:23], v[156:159]\n"
      "ds_read_b128 v[12:15], v97 offset:6144\nv_mfma_f32_16x16x32_bf16 v[160:163], v[44:47], v[20:23], v[160:163]\nv_mfma_f32_16x16x32_bf16 v[160:163], v[40:43], v[16:19], v[160:163]\n"
      "v_mfma_f32_16x16x32_bf16 v[164:167], v[48:51], v[16:19], v[164:167]\nv_mfma_f32_16x16x32_bf16 v[164:167], v[52:55], v[20:23], v[164:167]\n"
      "v_mfma_f32_16x16x32_bf16 v[168:171], v[60:63], v[20:23], v[168:171]\ns_waitcnt lgkmcnt(2)\nv_mfma_f32_16x16x32_bf16 v[168:171], v[56:59], v[16:19], v[168:171]\n"
      "v_mfma_f32_16x16x32_bf16 v[184:187], v[56:59], v[24:27], v[184:187]\nds_read_b128 v[16:19], v96 offset:8192\nv_mfma_f32_16x16x32_bf16 v[184:187], v[60:63], v[28:31], v[184:187]\n"
      "ds_read_b128 v[20:23], v97 offset:8192\nv_mfma_f32_16x16x32_bf16 v[180:183], v[52:55], v[28:31], v[180:183]\nv_mfma_f32_16x16x32_bf16 v[180:183], v[48:51], v[24:27], v[180:183]\n"
      "v_mfma_f32_16x16x32_bf16 v[176:179], v[40:43], v[24:27], v[176:179]\nv_mfma_f32_16x16x32_bf16 v[176:179], v[44:47], v[28:31], v[176:179]\ns_waitcnt vmcnt(0) lgkmcnt(0)\n"
      "v_mfma_f32_16x16x32_bf16 v[172:175], v[36:39], v[28:31], v[172:175]\ns_barrier\nv_mfma_f32_16x16x32_bf16 v[172:175], v[32:35], v[24:27], v[172:175]\n"
      "ds_read_b64_tr_b16 v[64:65], v104 offset:45056\nv_mfma_f32_16x16x32_bf16 v[188:191], v[32:35], v[0:3], v[188:191]\nds_read_b64_tr_b16 v[66:67], v104 offset:46080\n"
      "v_mfma_f32_16x16x32_bf16 v[188:191], v[36:39], v[4:7], v[188:191]\nds_read_b64_tr_b16 v[68:69], v104 offset:53248\nv_mfma_f32_16x16x32_bf16 v[192:195], v[44:47], v[4:7], v[192:195]\n"
      "ds_read_b64_tr_b16 v[70:71], v104 offset:54272\nv_mfma_f32_16x16x32_bf16 v[192:195], v[40:43], v[0:3], v[192:195]\nds_read_b64_tr_b16 v[72:73], v105 offset:45056\n"
      "v_mfma_f32_16x16x32_bf16 v[196:199], v[48:51], v[0:3], v[196:199]\nds_read_b64_tr_b16 v[74:75], v105 offset:46080\nv_mfma_f32_16x16x32_bf16 v[196:199], v[52:55], v[4:7], v[196:199]\n"
      "ds_read_b64_tr_b16 v[76:77], v105 offset:53248\nv_mfma_f32_16x16x32_bf16 v[200:203], v[60:63], v[4:7], v[200:203]\nds_read_b64_tr_b16 v[78:79], v105 offset:54272\n"
      "v_mfma_f32_16x16x32_bf16 v[200:203], v[56:59], v[0:3], v[200:203]\nds_read_b64_tr_b16 v[80:81], v106 offset:45056\nv_mfma_f32_16x16x32_bf16 v[216:219], v[56:59], v[8:11], v[216:219]\n"
      "ds_read_b128 v[0:3], v100 offset:45056\nv_mfma_f32_16x16x32_bf16 v[216:219], v[60:63], v[12:15], v[216:219]\nds_read_b128 v[4:7], v101 offset:45056\n"
      "v_mfma_f32_16x16x32_bf16 v[212:215], v[52:55], v[12:15], v[212:215]\nds_read_b64_tr_b16 v[82:83], v106 offset:46080\nv_mfma_f32_16x16x32_bf16 v[212:215], v[48:51], v[8:11], v[212:215]\n"
      "ds_read_b64_tr_b16 v[84:85], v106 offset:53248\nv_mfma_f32_16x16x32_bf16 v[208:211], v[40:43], v[8:11], v[208:211]\nds_read_b64_tr_b16 v[86:87], v106 offset:54272\n"
      "v_mfma_f32_16x16x32_bf16 v[208:211], v[44:47], v[12:15], v[208:211]\nds_read_b64_tr_b16 v[88:89], v107 offset:45056\nv_mfma_f32_16x16x32_bf16 v[204:207], v[36:39], v[12:15], v[204:207]\n"
      "ds_read_b64_tr_b16 v[90:91], v107 offset:46080\nv_mfma_f32_16x16x32_bf16 v[204:207], v[32:35], v[8:11], v[204:207]\nds_read_b64_tr_b16 v[92:93], v107 offset:53248\n"
      "v_mfma_f32_16x16x32_bf16 v[220:223], v[32:35], v[16:19], v[220:223]\nds_read_b128 v[8:11], v120 offset:45056\nv_mfma_f32_16x16x32_bf16 v[220:223], v[36:39], v[20:23], v[220:223]\n"
      "ds_read_b128 v[12:15], v121 offset:45056\nv_mfma_f32_16x16x32_bf16 v[224:227], v[44:47], v[20:23], v[224:227]\nds_read_b64_tr_b16 v[94:95], v107 offset:54272\n"
      "v_mfma_f32_16x16x32_bf16 v[224:227], v[40:43], v[16:19], v[224:227]\nv_mfma_f32_16x16x32_bf16 v[228:231], v[48:51], v[16:19], v[228:231]\ns_sub_u32 s58, s58, 1\ns_cmp_lg_u32 s58, 0\n"
      "v_mfma_f32_16x16x32_bf16 v[228:231], v[52:55], v[20:23], v[228:231]\nv_mfma_f32_16x16x32_bf16 v[232:235], v[60:63], v[20:23], v[232:235]\ns_waitcnt lgkmcnt(0)\n"
      "v_mfma_f32_16x16x32_bf16 v[232:235], v[56:59], v[16:19], v[232:235]\nds_read_b128 v[16:19], v96 offset:45056\ns_cbranch_scc1 .Lgl13\n"
      "v_mfma_f32_16x16x32_bf16 v[124:127], v[64:67], v[0:3], v[124:127]\nds_read_b128 v[20:23], v97 offset:45056\nv_cmp_eq_u32 s[62:63], 0, v240\n"
      "v_mfma_f32_16x16x32_bf16 v[124:127], v[68:71], v[4:7], v[124:127]\nds_read_b128 v[24:27], v96 offset:47104\nv_mov_b32 v253, 262144\n"
      "v_mfma_f32_16x16x32_bf16 v[128:131], v[76:79], v[4:7], v[128:131]\nds_read_b128 v[28:31], v97 offset:47104\nv_mov_b32 v248, 162048\n"
      "v_mfma_f32_16x16x32_bf16 v[128:131], v[72:75], v[0:3], v[128:131]\nv_mov_b32 v241, 1\nv_cndmask_b32 v253, v253, v248, s[62:63]\n"
      "v_mfma_f32_16x16x32_bf16 v[152:155], v[88:91], v[8:11], v[152:155]\nds_read_b128 v[0:3], v96 offset:49152\nv_lshrrev_b32 v112, 4, v240\n"
      "v_mfma_f32_16x16x32_bf16 v[152:155], v[92:95], v[12:15], v[152:155]\nds_read_b128 v[4:7], v97 offset:49152\nv_bfe_u32 v113, v240, 2, 2\n"
      "v_mfma_f32_16x16x32_bf16 v[148:151], v[84:87], v[12:15], v[148:151]\nv_and_b32 v114, 3, v240\nv_mul_u32_u24 v114, 0x80008, v114\n"
      "v_mfma_f32_16x16x32_bf16 v[148:151], v[80:83], v[8:11], v[148:151]\nv_add_u32 v244, v112, v113\nv_and_b32 v244, 3, v244\nv_mfma_f32_16x16x32_bf16 v[144:147], v[72:75], v[8:11], v[144:147]\n"
      "v_add_u32 v116, 22, v113\nv_cmp_gt_u32 s[62:63], 2, v113\nv_mfma_f32_16x16x32_bf16 v[144:147], v[76:79], v[12:15], v[144:147]\nv_and_b32 v113, 60, v240\ns_mov_b32 s42, 0\n"
      "v_mfma_f32_16x16x32_bf16 v[140:143], v[68:71], v[12:15], v[140:143]\ns_waitcnt lgkmcnt(4)\nv_cndmask_b32 v116, v116, v244, s[62:63]\n"
      "v_mfma_f32_16x16x32_bf16 v[140:143], v[64:67], v[8:11], v[140:143]\nv_lshl_or_b32 v244, v244, 16, v116\nv_lshl_add_u32 v244, v244, 5, v114\n"
      "v_mfma_f32_16x16x32_bf16 v[156:159], v[64:67], v[16:19], v[156:159]\nds_read_b128 v[8:11], v96 offset:51200\nv_lshl_add_u32 v252, v240, 4, s42\n"
      "v_mfma_f32_16x16x32_bf16 v[156:159], v[68:71], v[20:23], v[156:159]\nds_read_b128 v[12:15], v97 offset:51200\ns_mov_b32 s42, 0x76325410\n"
      "v_mfma_f32_16x16x32_bf16 v[160:163], v[76:79], v[20:23], v[160:163]\ns_mov_b32 s43, 0x21005410\nv_lshrrev_b64 v[116:117], v113, s[42:43]\n"
      "v_mfma_f32_16x16x32_bf16 v[160:163], v[72:75], v[16:19], v[160:163]\nv_and_b32 v116, 7, v116\nv_lshl_add_u32 v116, v112, 3, v116\n"
      "v_mfma_f32_16x16x32_bf16 v[164:167], v[80:83], v[16:19], v[164:167]\ns_mov_b32 s42, 0x54107632\ns_mov_b32 s43, 0x21107632\n"
      "v_mfma_f32_16x16x32_bf16 v[164:167], v[84:87], v[20:23], v[164:167]\nv_lshrrev_b64 v[118:119], v113, s[42:43]\nv_and_b32 v118, 7, v118\n"
      "v_mfma_f32_16x16x32_bf16 v[168:171], v[92:95], v[20:23], v[168:171]\ns_waitcnt lgkmcnt(2)\nv_lshl_add_u32 v118, v112, 3, v118\n"
      "v_mfma_f32_16x16x32_bf16 v[168:171], v[88:91], v[16:19], v[168:171]\nv_lshl_or_b32 v245, v118, 16, v116\nv_lshl_add_u32 v245, v245, 5, v114\n"
      "v_mfma_f32_16x16x32_bf16 v[184:187], v[88:91], v[24:27], v[184:187]\nds_read_b128 v[16:19], v96 offset:53248\nv_and_b32 v113, 1, v240\n"
      "v_mfma_f32_16x16x32_bf16 v[184:187], v[92:95], v[28:31], v[184:187]\nds_read_b128 v[20:23], v97 offset:53248\nv_mul_u32_u24 v113, 0xaaaa, v113\n"
      "v_mfma_f32_16x16x32_bf16 v[180:183], v[84:87], v[28:31], v[180:183]\nv_add_u32 v242, 0x4444, v113\ns_lshl_b32 s42, s36, 4\n"
      "v_mfma_f32_16x16x32_bf16 v[180:183], v[80:83], v[24:27], v[180:183]\ns_add_u32 s42, s42, 160640\nv_lshl_add_u32 v243, v112, 2, s42\n"
      "v_mfma_f32_16x16x32_bf16 v[176:179], v[72:75], v[24:27], v[176:179]\nv_and_b32 v112, 1, v112\nv_lshl_add_u32 v112, v112, 3, s24\n"
      "v_mfma_f32_16x16x32_bf16 v[176:179], v[76:79], v[28:31], v[176:179]\ns_waitcnt vmcnt(0) lgkmcnt(0)\ns_sub_u32 s42, s64, 1\n"
      "v_mfma_f32_16x16x32_bf16 v[172:175], v[68:71], v[28:31], v[172:175]\ns_barrier\nv_min_u32 v112, s42, v112\nv_mfma_f32_16x16x32_bf16 v[172:175], v[64:67], v[24:27], v[172:175]\n"
      "ds_read_b64_tr_b16 v[32:33], v108 offset:0\nv_lshlrev_b32 v112, 3, v112\nv_mfma_f32_16x16x32_bf16 v[188:191], v[64:67], v[0:3], v[188:191]\nds_read_b64_tr_b16 v[34:35], v108 offset:1024\n"
      "v_and_b32 v113, 15, v240\nv_mfma_f32_16x16x32_bf16 v[188:191], v[68:71], v[4:7], v[188:191]\nds_read_b64_tr_b16 v[36:37], v108 offset:8192\nv_lshl_add_u32 v250, v112, 1, v113\n"
      "v_mfma_f32_16x16x32_bf16 v[192:195], v[76:79], v[4:7], v[192:195]\nds_read_b64_tr_b16 v[38:39], v108 offset:9216\ns_mov_b32 s42, 136192\n"
      "v_mfma_f32_16x16x32_bf16 v[192:195], v[72:75], v[0:3], v[192:195]\nds_read_b64_tr_b16 v[40:41], v109 offset:0\nv_lshl_add_u32 v250, v250, 2, s42\n"
      "v_mfma_f32_16x16x32_bf16 v[196:199], v[80:83], v[0:3], v[196:199]\nds_read_b64_tr_b16 v[42:43], v109 offset:1024\nv_min_u32 v113, 12, v113\n"
      "v_mfma_f32_16x16x32_bf16 v[196:199], v[84:87], v[4:7], v[196:199]\nds_read_b64_tr_b16 v[44:45], v109 offset:8192\ns_lshl_b32 s42, s30, 7\n"
      "v_mfma_f32_16x16x32_bf16 v[200:203], v[92:95], v[4:7], v[200:203]\nds_read_b64_tr_b16 v[46:47], v109 offset:9216\nv_lshl_add_u32 v113, v113, 15, s42\n"
      "v_mfma_f32_16x16x32_bf16 v[200:203], v[88:91], v[0:3], v[200:203]\nds_read_b64_tr_b16 v[48:49], v110 offset:0\nv_add_u32 v249, v113, v112\n"
      "v_mfma_f32_16x16x32_bf16 v[216:219], v[88:91], v[8:11], v[216:219]\nds_read_b128 v[0:3], v102 offset:0\nv_mfma_f32_16x16x32_bf16 v[216:219], v[92:95], v[12:15], v[216:219]\n"
      "ds_read_b128 v[4:7], v103 offset:0\nv_mfma_f32_16x16x32_bf16 v[212:215], v[84:87], v[12:15], v[212:215]\nds_read_b64_tr_b16 v[50:51], v110 offset:1024\n"
      "v_mfma_f32_16x16x32_bf16 v[212:215], v[80:83], v[8:11], v[212:215]\nds_read_b64_tr_b16 v[52:53], v110 offset:8192\nv_mfma_f32_16x16x32_bf16 v[208:211], v[72:75], v[8:11], v[208:211]\n"
      "ds_read_b64_tr_b16 v[54:55], v110 offset:9216\nv_mfma_f32_16x16x32_bf16 v[208:211], v[76:79], v[12:15], v[208:211]\nds_read_b64_tr_b16 v[56:57], v111 offset:0\n"
      "v_mfma_f32_16x16x32_bf16 v[204:207], v[68:71], v[12:15], v[204:207]\nds_read_b64_tr_b16 v[58:59], v111 offset:1024\nv_mfma_f32_16x16x32_bf16 v[204:207], v[64:67], v[8:11], v[204:207]\n"
      "ds_read_b64_tr_b16 v[60:61], v111 offset:8192\nv_mfma_f32_16x16x32_bf16 v[220:223], v[64:67], v[16:19], v[220:223]\nds_read_b128 v[8:11], v122 offset:0\n"
      "v_mfma_f32_16x16x32_bf16 v[220:223], v[68:71], v[20:23], v[220:223]\nds_read_b128 v[12:15], v123 offset:0\nv_mfma_f32_16x16x32_bf16 v[224:227], v[76:79], v[20:23], v[224:227]\n"
      "ds_read_b64_tr_b16 v[62:63], v111 offset:9216\nv_mfma_f32_16x16x32_bf16 v[224:227], v[72:75], v[16:19], v[224:227]\nv_mfma_f32_16x16x32_bf16 v[228:231], v[80:83], v[16:19], v[228:231]\n"
      "v_mfma_f32_16x16x32_bf16 v[228:231], v[84:87], v[20:23], v[228:231]\nv_mfma_f32_16x16x32_bf16 v[232:235], v[92:95], v[20:23], v[232:235]\ns_waitcnt lgkmcnt(0)\n"
      "v_mfma_f32_16x16x32_bf16 v[232:235], v[88:91], v[16:19], v[232:235]\nds_read_b128 v[16:19], v98 offset:0\nv_mfma_f32_16x16x32_bf16 v[124:127], v[32:35], v[0:3], v[124:127]\n"
      "ds_read_b128 v[20:23], v99 offset:0\nv_mfma_f32_16x16x32_bf16 v[124:127], v[36:39], v[4:7], v[124:127]\nds_read_b128 v[24:27], v98 offset:2048\n"
      "v_mfma_f32_16x16x32_bf16 v[128:131], v[44:47], v[4:7], v[128:131]\nds_read_b128 v[28:31], v99 offset:2048\nv_mfma_f32_16x16x32_bf16 v[128:131], v[40:43], v[0:3], v[128:131]\n"
      "v_mfma_f32_16x16x32_bf16 v[152:155], v[56:59], v[8:11], v[152:155]\nds_read_b128 v[0:3], v98 offset:4096\nv_mfma_f32_16x16x32_bf16 v[152:155], v[60:63], v[12:15], v[152:155]\n"
      "ds_read_b128 v[4:7], v99 offset:4096\nv_mfma_f32_16x16x32_bf16 v[148:151], v[52:55], v[12:15], v[148:151]\nv_mfma_f32_16x16x32_bf16 v[148:151], v[48:51], v[8:11], v[148:151]\n"
      "v_mfma_f32_16x16x32_bf16 v[144:147], v[40:43], v[8:11], v[144:147]\nv_mfma_f32_16x16x32_bf16 v[144:147], v[44:47], v[12:15], v[144:147]\n"
      "v_mfma_f32_16x16x32_bf16 v[140:143], v[36:39], v[12:15], v[140:143]\ns_waitcnt lgkmcnt(4)\nv_mfma_f32_16x16x32_bf16 v[140:143], v[32:35], v[8:11], v[140:143]\n"
      "v_mfma_f32_16x16x32_bf16 v[156:159], v[32:35], v[16:19], v[156:159]\nds_read_b128 v[8:11], v98 offset:6144\nv_mfma_f32_16x16x32_bf16 v[156:159], v[36:39], v[20:23], v[156:159]\n"
      "ds_read_b128 v[12:15], v99 offset:6144\nv_mfma_f32_16x16x32_bf16 v[160:163], v[44:47], v[20:23], v[160:163]\nv_mfma_f32_16x16x32_bf16 v[160:163], v[40:43], v[16:19], v[160:163]\n"
      "v_mfma_f32_16x16x32_bf16 v[164:167], v[48:51], v[16:19], v[164:167]\nv_mfma_f32_16x16x32_bf16 v[164:167], v[52:55], v[20:23], v[164:167]\n"
      "v_mfma_f32_16x16x32_bf16 v[168:171], v[60:63], v[20:23], v[168:171]\ns_waitcnt lgkmcnt(2)\nv_mfma_f32_16x16x32_bf16 v[168:171], v[56:59], v[16:19], v[168:171]\n"
      "v_mfma_f32_16x16x32_bf16 v[184:187], v[56:59], v[24:27], v[184:187]\nds_read_b128 v[16:19], v98 offset:8192\nv_mfma_f32_16x16x32_bf16 v[184:187], v[60:63], v[28:31], v[184:187]\n"
      "ds_read_b128 v[20:23], v99 offset:8192\nv_mfma_f32_16x16x32_bf16 v[180:183], v[52:55], v[28:31], v[180:183]\nv_mfma_f32_16x16x32_bf16 v[180:183], v[48:51], v[24:27], v[180:183]\n"
      "v_mfma_f32_16x16x32_bf16 v[176:179], v[40:43], v[24:27], v[176:179]\nv_mfma_f32_16x16x32_bf16 v[176:179], v[44:47], v[28:31], v[176:179]\ns_waitcnt vmcnt(0) lgkmcnt(0)\n"
      "v_mfma_f32_16x16x32_bf16 v[172:175], v[36:39], v[28:31], v[172:175]\ns_barrier\nv_mfma_f32_16x16x32_bf16 v[172:175], v[32:35], v[24:27], v[172:175]\nds_read_b128 v[72:75], v252 offset:0\n"
      "v_lshl_add_u32 v246, v240, 1, v248\nv_mfma_f32_16x16x32_bf16 v[188:191], v[32:35], v[0:3], v[188:191]\nds_read_b128 v[24:27], v236 offset:512\nds_read_u16 v251, v246 offset:64\n"
      "v_mfma_f32_16x16x32_bf16 v[188:191], v[36:39], v[4:7], v[188:191]\nds_read_b128 v[28:31], v236 offset:576\nds_read_b32 v247, v248 offset:24\n"
      "v_mfma_f32_16x16x32_bf16 v[192:195], v[44:47], v[4:7], v[192:195]\nds_read_b128 v[76:79], v252 offset:1024\nv_and_b32 v246, 48, v240\n"
      "v_mfma_f32_16x16x32_bf16 v[192:195], v[40:43], v[0:3], v[192:195]\nds_read_b128 v[80:83], v252 offset:2048\ns_mov_b32 s42, 42240\n"
      "v_mfma_f32_16x16x32_bf16 v[196:199], v[48:51], v[0:3], v[196:199]\nds_read_b128 v[84:87], v252 offset:3072\nv_lshl_add_u32 v246, v246, 2, s42\n"
      "v_mfma_f32_16x16x32_bf16 v[196:199], v[52:55], v[4:7], v[196:199]\nds_read_b128 v[88:91], v252 offset:4096\ns_mov_b32 s58, 0xbdd2d3e7\n"
      "v_mfma_f32_16x16x32_bf16 v[200:203], v[60:63], v[4:7], v[200:203]\nds_read_b128 v[92:95], v252 offset:5120\ns_waitcnt lgkmcnt(4)\n"
      "v_mfma_f32_16x16x32_bf16 v[200:203], v[56:59], v[0:3], v[200:203]\nds_read_b128 v[96:99], v252 offset:6144\nv_readfirstlane_b32 s39, v247\n"
      "v_mfma_f32_16x16x32_bf16 v[216:219], v[56:59], v[8:11], v[216:219]\nds_read_b128 v[0:3], v236 offset:0\nv_readlane_b32 s55, v251, 0\n"
      "v_mfma_f32_16x16x32_bf16 v[216:219], v[60:63], v[12:15], v[216:219]\nds_read_b128 v[4:7], v236 offset:64\ns_setprio 3\nv_mfma_f32_16x16x32_bf16 v[212:215], v[52:55], v[12:15], v[212:215]\n"
      "ds_read_b128 v[100:103], v252 offset:7168\ns_xor_b32 s40, s39, 0xa5c30f96\nv_mfma_f32_16x16x32_bf16 v[212:215], v[48:51], v[8:11], v[212:215]\nds_read_b128 v[104:107], v252 offset:8192\n"
      "s_mul_i32 s41, s39, 0x9E3779B1\nv_mfma_f32_16x16x32_bf16 v[208:211], v[40:43], v[8:11], v[208:211]\nds_read_b128 v[108:111], v252 offset:9216\ns_lshr_b32 s41, s41, 24\n"
      "v_mfma_f32_16x16x32_bf16 v[208:211], v[44:47], v[12:15], v[208:211]\nds_read_b128 v[64:67], v237 offset:512\nv_mfma_f32_16x16x32_bf16 v[204:207], v[36:39], v[12:15], v[204:207]\n"
      "ds_read_b128 v[68:71], v237 offset:576\nv_mfma_f32_16x16x32_bf16 v[204:207], v[32:35], v[8:11], v[204:207]\nds_read_b128 v[112:115], v252 offset:10240\n"
      "v_mfma_f32_16x16x32_bf16 v[220:223], v[32:35], v[16:19], v[220:223]\nds_read_b128 v[8:11], v237 offset:0\nv_mfma_f32_16x16x32_bf16 v[220:223], v[36:39], v[20:23], v[220:223]\n"
      "ds_read_b128 v[12:15], v237 offset:64\nv_mfma_f32_16x16x32_bf16 v[224:227], v[44:47], v[20:23], v[224:227]\nds_read_b128 v[116:119], v252 offset:11264\n"
      "v_mfma_f32_16x16x32_bf16 v[224:227], v[40:43], v[16:19], v[224:227]\nds_read_b128 v[120:123], v252 offset:12288\n"
      "v_add_u32_sdwa v239, s49, v245 dst_sel:DWORD dst_unused:UNUSED_PAD src0_sel:DWORD src1_sel:WORD_1\nv_mfma_f32_16x16x32_bf16 v[228:231], v[48:51], v[16:19], v[228:231]\n"
      "ds_read_b128 v[132:135], v252 offset:15360\nv_add_u32_sdwa v236, s49, v244 dst_sel:DWORD dst_unused:UNUSED_PAD src0_sel:DWORD src1_sel:WORD_0\n"
      "v_mfma_f32_16x16x32_bf16 v[228:231], v[52:55], v[20:23], v[228:231]\nds_read_b128 v[136:139], v252 offset:16384\n"
      "v_add_u32_sdwa v237, s49, v244 dst_sel:DWORD dst_unused:UNUSED_PAD src0_sel:DWORD src1_sel:WORD_1\nv_mfma_f32_16x16x32_bf16 v[232:235], v[60:63], v[20:23], v[232:235]\n"
      "v_add_u32_sdwa v238, s49, v245 dst_sel:DWORD dst_unused:UNUSED_PAD src0_sel:DWORD src1_sel:WORD_0\ns_waitcnt lgkmcnt(0)\nv_mfma_f32_16x16x32_bf16 v[232:235], v[56:59], v[16:19], v[232:235]\n"
      "v_pk_mul_f32 v[44:45], v[124:125], v[124:125]\nv_pk_mul_f32 v[46:47], v[126:127], v[126:127]\nv_pk_mul_f32 v[48:49], v[128:129], v[128:129]\nv_pk_mul_f32 v[50:51], v[130:131], v[130:131]\n"
      "v_pk_fma_f32 v[44:45], v[44:45], s[58:59], s[58:59] op_sel:[0,0,1] op_sel_hi:[1,0,1]\nv_pk_fma_f32 v[46:47], v[46:47], s[58:59], s[58:59] op_sel:[0,0,1] op_sel_hi:[1,0,1]\n"
      "v_pk_fma_f32 v[48:49], v[48:49], s[58:59], s[58:59] op_sel:[0,0,1] op_sel_hi:[1,0,1]\nv_pk_fma_f32 v[50:51], v[50:51], s[58:59], s[58:59] op_sel:[0,0,1] op_sel_hi:[1,0,1]\n"
      "v_pk_fma_f32 v[44:45], v[44:45], v[124:125], s[60:61] op_sel_hi:[1,1,0]\nv_pk_fma_f32 v[46:47], v[46:47], v[126:127], s[60:61] op_sel_hi:[1,1,0]\n"
      "v_pk_fma_f32 v[48:49], v[48:49], v[128:129], s[60:61] op_sel_hi:[1,1,0]\nv_pk_fma_f32 v[50:51], v[50:51], v[130:131], s[60:61] op_sel_hi:[1,1,0]\nv_exp_f32 v44, v44 clamp\n"
      "v_pk_mul_f32 v[52:53], v[140:141], v[140:141]\nv_exp_f32 v45, v45 clamp\nv_pk_mul_f32 v[54:55], v[142:143], v[142:143]\nv_pk_mul_f32 v[56:57], v[144:145], v[144:145]\n"
      "v_exp_f32 v46, v46 clamp\nv_pk_mul_f32 v[58:59], v[146:147], v[146:147]\nv_exp_f32 v47, v47 clamp\nv_pk_fma_f32 v[52:53], v[52:53], s[58:59], s[58:59] op_sel:[0,0,1] op_sel_hi:[1,0,1]\n"
      "v_pk_fma_f32 v[54:55], v[54:55], s[58:59], s[58:59] op_sel:[0,0,1] op_sel_hi:[1,0,1]\nv_exp_f32 v48, v48 clamp\n"
      "v_pk_fma_f32 v[56:57], v[56:57], s[58:59], s[58:59] op_sel:[0,0,1] op_sel_hi:[1,0,1]\nv_exp_f32 v49, v49 clamp\n"
      "v_pk_fma_f32 v[58:59], v[58:59], s[58:59], s[58:59] op_sel:[0,0,1] op_sel_hi:[1,0,1]\nv_pk_fma_f32 v[52:53], v[52:53], v[140:141], s[60:61] op_sel_hi:[1,1,0]\nv_exp_f32 v50, v50 clamp\n"
      "v_pk_fma_f32 v[54:55], v[54:55], v[142:143], s[60:61] op_sel_hi:[1,1,0]\nv_exp_f32 v51, v51 clamp\nv_pk_fma_f32 v[56:57], v[56:57], v[144:145], s[60:61] op_sel_hi:[1,1,0]\n"
      "v_pk_fma_f32 v[58:59], v[58:59], v[146:147], s[60:61] op_sel_hi:[1,1,0]\nv_exp_f32 v52, v52 clamp\nv_pk_fma_f32 v[46:47], v[46:47], v[2:3], v[26:27]\n"
      "v_pk_fma_f32 v[44:45], v[44:45], v[0:1], v[24:25]\nv_pk_fma_f32 v[48:49], v[48:49], v[4:5], v[28:29]\nv_exp_f32 v53, v53 clamp\nv_pk_fma_f32 v[50:51], v[50:51], v[6:7], v[30:31]\n"
      "v_pk_mul_f32 v[124:125], v[124:125], v[46:47]\nv_pk_mul_f32 v[128:129], v[128:129], v[50:51]\nv_exp_f32 v54, v54 clamp\nv_pk_fma_f32 v[124:125], v[126:127], v[44:45], v[124:125]\n"
      "v_pk_fma_f32 v[128:129], v[130:131], v[48:49], v[128:129]\nv_pk_mul_f32 v[44:45], v[44:45], v[46:47]\nv_exp_f32 v55, v55 clamp\nv_pk_mul_f32 v[48:49], v[48:49], v[50:51]\n"
      "v_pk_mul_f32 v[46:47], v[44:45], v[48:49]\nv_rcp_f32 v46, v46\nv_pk_mul_f32 v[124:125], v[124:125], v[48:49]\nv_rcp_f32 v47, v47\nv_pk_fma_f32 v[124:125], v[128:129], v[44:45], v[124:125]\n"
      "v_pk_mul_f32 v[32:33], v[124:125], v[46:47]\nv_add_f32 v42, v32, v33\nv_pk_mul_f32 v[16:17], v[148:149], v[148:149]\nv_pk_mul_f32 v[18:19], v[150:151], v[150:151]\nv_exp_f32 v56, v56 clamp\n"
      "v_pk_mul_f32 v[20:21], v[152:153], v[152:153]\nv_pk_mul_f32 v[22:23], v[154:155], v[154:155]\nv_exp_f32 v57, v57 clamp\n"
      "v_pk_fma_f32 v[16:17], v[16:17], s[58:59], s[58:59] op_sel:[0,0,1] op_sel_hi:[1,0,1]\nv_pk_fma_f32 v[18:19], v[18:19], s[58:59], s[58:59] op_sel:[0,0,1] op_sel_hi:[1,0,1]\n"
      "v_pk_fma_f32 v[20:21], v[20:21], s[58:59], s[58:59] op_sel:[0,0,1] op_sel_hi:[1,0,1]\nv_exp_f32 v58, v58 clamp\n"
      "v_pk_fma_f32 v[22:23], v[22:23], s[58:59], s[58:59] op_sel:[0,0,1] op_sel_hi:[1,0,1]\nv_pk_fma_f32 v[16:17], v[16:17], v[148:149], s[60:61] op_sel_hi:[1,1,0]\n"
      "v_pk_fma_f32 v[18:19], v[18:19], v[150:151], s[60:61] op_sel_hi:[1,1,0]\nv_exp_f32 v59, v59 clamp\nv_pk_fma_f32 v[20:21], v[20:21], v[152:153], s[60:61] op_sel_hi:[1,1,0]\n"
      "v_pk_fma_f32 v[22:23], v[22:23], v[154:155], s[60:61] op_sel_hi:[1,1,0]\nv_exp_f32 v16, v16 clamp\nv_pk_fma_f32 v[54:55], v[54:55], v[2:3], v[26:27]\n"
      "v_pk_fma_f32 v[52:53], v[52:53], v[0:1], v[24:25]\nv_pk_fma_f32 v[56:57], v[56:57], v[4:5], v[28:29]\nv_exp_f32 v17, v17 clamp\nv_pk_fma_f32 v[58:59], v[58:59], v[6:7], v[30:31]\n"
      "v_pk_mul_f32 v[140:141], v[140:141], v[54:55]\nv_pk_mul_f32 v[144:145], v[144:145], v[58:59]\nv_exp_f32 v18, v18 clamp\nv_pk_fma_f32 v[140:141], v[142:143], v[52:53], v[140:141]\n"
      "v_pk_fma_f32 v[144:145], v[146:147], v[56:57], v[144:145]\nv_pk_mul_f32 v[52:53], v[52:53], v[54:55]\nv_exp_f32 v19, v19 clamp\nv_pk_mul_f32 v[56:57], v[56:57], v[58:59]\n"
      "v_pk_mul_f32 v[54:55], v[52:53], v[56:57]\nv_rcp_f32 v54, v54\nv_pk_mul_f32 v[140:141], v[140:141], v[56:57]\nv_rcp_f32 v55, v55\nv_pk_fma_f32 v[140:141], v[144:145], v[52:53], v[140:141]\n"
      "v_pk_mul_f32 v[32:33], v[140:141], v[54:55]\nv_pk_mul_f32 v[44:45], v[156:157], v[156:157]\nv_pk_mul_f32 v[46:47], v[158:159], v[158:159]\nv_exp_f32 v20, v20 clamp\n"
      "v_pk_mul_f32 v[48:49], v[160:161], v[160:161]\nv_pk_mul_f32 v[50:51], v[162:163], v[162:163]\nv_exp_f32 v21, v21 clamp\n"
      "v_pk_fma_f32 v[44:45], v[44:45], s[58:59], s[58:59] op_sel:[0,0,1] op_sel_hi:[1,0,1]\nv_pk_fma_f32 v[46:47], v[46:47], s[58:59], s[58:59] op_sel:[0,0,1] op_sel_hi:[1,0,1]\n"
      "v_pk_fma_f32 v[48:49], v[48:49], s[58:59], s[58:59] op_sel:[0,0,1] op_sel_hi:[1,0,1]\nv_exp_f32 v22, v22 clamp\n"
      "v_pk_fma_f32 v[50:51], v[50:51], s[58:59], s[58:59] op_sel:[0,0,1] op_sel_hi:[1,0,1]\nv_pk_fma_f32 v[44:45], v[44:45], v[156:157], s[60:61] op_sel_hi:[1,1,0]\n"
      "v_pk_fma_f32 v[46:47], v[46:47], v[158:159], s[60:61] op_sel_hi:[1,1,0]\nv_exp_f32 v23, v23 clamp\nv_pk_fma_f32 v[48:49], v[48:49], v[160:161], s[60:61] op_sel_hi:[1,1,0]\n"
      "v_pk_fma_f32 v[50:51], v[50:51], v[162:163], s[60:61] op_sel_hi:[1,1,0]\nv_exp_f32 v44, v44 clamp\nv_pk_fma_f32 v[18:19], v[18:19], v[10:11], v[66:67]\n"
      "v_pk_fma_f32 v[16:17], v[16:17], v[8:9], v[64:65]\nv_pk_fma_f32 v[20:21], v[20:21], v[12:13], v[68:69]\nv_exp_f32 v45, v45 clamp\nv_pk_fma_f32 v[22:23], v[22:23], v[14:15], v[70:71]\n"
      "v_pk_mul_f32 v[148:149], v[148:149], v[18:19]\nv_pk_mul_f32 v[152:153], v[152:153], v[22:23]\nv_exp_f32 v46, v46 clamp\nv_pk_fma_f32 v[148:149], v[150:151], v[16:17], v[148:149]\n"
      "v_pk_fma_f32 v[152:153], v[154:155], v[20:21], v[152:153]\nv_pk_mul_f32 v[16:17], v[16:17], v[18:19]\nv_exp_f32 v47, v47 clamp\nv_pk_mul_f32 v[20:21], v[20:21], v[22:23]\n"
      "v_pk_mul_f32 v[18:19], v[16:17], v[20:21]\nv_rcp_f32 v18, v18\nv_pk_mul_f32 v[148:149], v[148:149], v[20:21]\nv_rcp_f32 v19, v19\nv_pk_fma_f32 v[148:149], v[152:153], v[16:17], v[148:149]\n"
      "v_pk_fma_f32 v[32:33], v[148:149], v[18:19], v[32:33]\nv_add_f32 v41, v32, v33\nv_pk_mul_f32 v[52:53], v[164:165], v[164:165]\nv_pk_mul_f32 v[54:55], v[166:167], v[166:167]\n"
      "v_exp_f32 v48, v48 clamp\nv_pk_mul_f32 v[56:57], v[168:169], v[168:169]\nv_pk_mul_f32 v[58:59], v[170:171], v[170:171]\nv_exp_f32 v49, v49 clamp\n"
      "v_pk_fma_f32 v[52:53], v[52:53], s[58:59], s[58:59] op_sel:[0,0,1] op_sel_hi:[1,0,1]\nv_pk_fma_f32 v[54:55], v[54:55], s[58:59], s[58:59] op_sel:[0,0,1] op_sel_hi:[1,0,1]\n"
      "v_pk_fma_f32 v[56:57], v[56:57], s[58:59], s[58:59] op_sel:[0,0,1] op_sel_hi:[1,0,1]\nv_exp_f32 v50, v50 clamp\n"
      "v_pk_fma_f32 v[58:59], v[58:59], s[58:59], s[58:59] op_sel:[0,0,1] op_sel_hi:[1,0,1]\nv_pk_fma_f32 v[52:53], v[52:53], v[164:165], s[60:61] op_sel_hi:[1,1,0]\n"
      "v_pk_fma_f32 v[54:55], v[54:55], v[166:167], s[60:61] op_sel_hi:[1,1,0]\nv_exp_f32 v51, v51 clamp\nv_pk_fma_f32 v[56:57], v[56:57], v[168:169], s[60:61] op_sel_hi:[1,1,0]\n"
      "v_pk_fma_f32 v[58:59], v[58:59], v[170:171], s[60:61] op_sel_hi:[1,1,0]\nv_exp_f32 v52, v52 clamp\nv_pk_fma_f32 v[46:47], v[46:47], v[2:3], v[26:27]\n"
      "v_pk_fma_f32 v[44:45], v[44:45], v[0:1], v[24:25]\nv_pk_fma_f32 v[48:49], v[48:49], v[4:5], v[28:29]\nv_exp_f32 v53, v53 clamp\nv_pk_fma_f32 v[50:51], v[50:51], v[6:7], v[30:31]\n"
      "v_pk_mul_f32 v[156:157], v[156:157], v[46:47]\nv_pk_mul_f32 v[160:161], v[160:161], v[50:51]\nv_exp_f32 v54, v54 clamp\nv_pk_fma_f32 v[156:157], v[158:159], v[44:45], v[156:157]\n"
      "v_pk_fma_f32 v[160:161], v[162:163], v[48:49], v[160:161]\nv_pk_mul_f32 v[44:45], v[44:45], v[46:47]\nv_exp_f32 v55, v55 clamp\nv_pk_mul_f32 v[48:49], v[48:49], v[50:51]\n"
      "v_pk_mul_f32 v[46:47], v[44:45], v[48:49]\nv_rcp_f32 v46, v46\nv_pk_mul_f32 v[156:157], v[156:157], v[48:49]\nv_rcp_f32 v47, v47\nv_pk_fma_f32 v[156:157], v[160:161], v[44:45], v[156:157]\n"
      "v_pk_mul_f32 v[32:33], v[156:157], v[46:47]\nv_pk_mul_f32 v[16:17], v[172:173], v[172:173]\nv_pk_mul_f32 v[18:19], v[174:175], v[174:175]\nv_exp_f32 v56, v56 clamp\n"
      "v_pk_mul_f32 v[20:21], v[176:177], v[176:177]\nv_pk_mul_f32 v[22:23], v[178:179], v[178:179]\nv_exp_f32 v57, v57 clamp\n"
      "v_pk_fma_f32 v[16:17], v[16:17], s[58:59], s[58:59] op_sel:[0,0,1] op_sel_hi:[1,0,1]\nv_pk_fma_f32 v[18:19], v[18:19], s[58:59], s[58:59] op_sel:[0,0,1] op_sel_hi:[1,0,1]\n"
      "v_pk_fma_f32 v[20:21], v[20:21], s[58:59], s[58:59] op_sel:[0,0,1] op_sel_hi:[1,0,1]\nv_exp_f32 v58, v58 clamp\n"
      "v_pk_fma_f32 v[22:23], v[22:23], s[58:59], s[58:59] op_sel:[0,0,1] op_sel_hi:[1,0,1]\nv_pk_fma_f32 v[16:17], v[16:17], v[172:173], s[60:61] op_sel_hi:[1,1,0]\n"
      "v_pk_fma_f32 v[18:19], v[18:19], v[174:175], s[60:61] op_sel_hi:[1,1,0]\nv_exp_f32 v59, v59 clamp\nv_pk_fma_f32 v[20:21], v[20:21], v[176:177], s[60:61] op_sel_hi:[1,1,0]\n"
      "v_pk_fma_f32 v[22:23], v[22:23], v[178:179], s[60:61] op_sel_hi:[1,1,0]\nv_exp_f32 v16, v16 clamp\nv_pk_fma_f32 v[54:55], v[54:55], v[10:11], v[66:67]\n"
      "v_pk_fma_f32 v[52:53], v[52:53], v[8:9], v[64:65]\nv_pk_fma_f32 v[56:57], v[56:57], v[12:13], v[68:69]\nv_exp_f32 v17, v17 clamp\nv_pk_fma_f32 v[58:59], v[58:59], v[14:15], v[70:71]\n"
      "v_pk_mul_f32 v[164:165], v[164:165], v[54:55]\nv_pk_mul_f32 v[168:169], v[168:169], v[58:59]\nv_exp_f32 v18, v18 clamp\nv_pk_fma_f32 v[164:165], v[166:167], v[52:53], v[164:165]\n"
      "v_pk_fma_f32 v[168:169], v[170:171], v[56:57], v[168:169]\nv_pk_mul_f32 v[52:53], v[52:53], v[54:55]\nv_exp_f32 v19, v19 clamp\nv_pk_mul_f32 v[56:57], v[56:57], v[58:59]\n"
      "v_pk_mul_f32 v[54:55], v[52:53], v[56:57]\nv_rcp_f32 v54, v54\nv_pk_mul_f32 v[164:165], v[164:165], v[56:57]\nv_rcp_f32 v55, v55\nv_pk_fma_f32 v[164:165], v[168:169], v[52:53], v[164:165]\n"
      "v_pk_fma_f32 v[32:33], v[164:165], v[54:55], v[32:33]\nv_add_f32 v36, v32, v33\nv_pk_mul_f32 v[44:45], v[180:181], v[180:181]\nv_pk_mul_f32 v[46:47], v[182:183], v[182:183]\n"
      "v_exp_f32 v20, v20 clamp\nv_pk_mul_f32 v[48:49], v[184:185], v[184:185]\nv_pk_mul_f32 v[50:51], v[186:187], v[186:187]\nv_exp_f32 v21, v21 clamp\n"
      "v_pk_fma_f32 v[44:45], v[44:45], s[58:59], s[58:59] op_sel:[0,0,1] op_sel_hi:[1,0,1]\nv_pk_fma_f32 v[46:47], v[46:47], s[58:59], s[58:59] op_sel:[0,0,1] op_sel_hi:[1,0,1]\n"
      "v_pk_fma_f32 v[48:49], v[48:49], s[58:59], s[58:59] op_sel:[0,0,1] op_sel_hi:[1,0,1]\nv_exp_f32 v22, v22 clamp\n"
      "v_pk_fma_f32 v[50:51], v[50:51], s[58:59], s[58:59] op_sel:[0,0,1] op_sel_hi:[1,0,1]\nv_pk_fma_f32 v[44:45], v[44:45], v[180:181], s[60:61] op_sel_hi:[1,1,0]\n"
      "v_pk_fma_f32 v[46:47], v[46:47], v[182:183], s[60:61] op_sel_hi:[1,1,0]\nv_exp_f32 v23, v23 clamp\nv_pk_fma_f32 v[48:49], v[48:49], v[184:185], s[60:61] op_sel_hi:[1,1,0]\n"
      "v_pk_fma_f32 v[50:51], v[50:51], v[186:187], s[60:61] op_sel_hi:[1,1,0]\nv_exp_f32 v44, v44 clamp\nv_pk_fma_f32 v[18:19], v[18:19], v[2:3], v[26:27]\n"
      "v_pk_fma_f32 v[16:17], v[16:17], v[0:1], v[24:25]\nv_pk_fma_f32 v[20:21], v[20:21], v[4:5], v[28:29]\nv_exp_f32 v45, v45 clamp\nv_pk_fma_f32 v[22:23], v[22:23], v[6:7], v[30:31]\n"
      "v_pk_mul_f32 v[172:173], v[172:173], v[18:19]\nv_pk_mul_f32 v[176:177], v[176:177], v[22:23]\nv_exp_f32 v46, v46 clamp\nv_pk_fma_f32 v[172:173], v[174:175], v[16:17], v[172:173]\n"
      "v_pk_fma_f32 v[176:177], v[178:179], v[20:21], v[176:177]\nv_pk_mul_f32 v[16:17], v[16:17], v[18:19]\nv_exp_f32 v47, v47 clamp\nv_pk_mul_f32 v[20:21], v[20:21], v[22:23]\n"
      "v_pk_mul_f32 v[18:19], v[16:17], v[20:21]\nv_rcp_f32 v18, v18\nv_pk_mul_f32 v[172:173], v[172:173], v[20:21]\nv_rcp_f32 v19, v19\nv_pk_fma_f32 v[172:173], v[176:177], v[16:17], v[172:173]\n"
      "v_pk_mul_f32 v[32:33], v[172:173], v[18:19]\nv_pk_mul_f32 v[52:53], v[188:189], v[188:189]\nv_pk_mul_f32 v[54:55], v[190:191], v[190:191]\nv_exp_f32 v48, v48 clamp\n"
      "v_pk_mul_f32 v[56:57], v[192:193], v[192:193]\nv_pk_mul_f32 v[58:59], v[194:195], v[194:195]\nv_exp_f32 v49, v49 clamp\n"
      "v_pk_fma_f32 v[52:53], v[52:53], s[58:59], s[58:59] op_sel:[0,0,1] op_sel_hi:[1,0,1]\nv_pk_fma_f32 v[54:55], v[54:55], s[58:59], s[58:59] op_sel:[0,0,1] op_sel_hi:[1,0,1]\n"
      "v_pk_fma_f32 v[56:57], v[56:57], s[58:59], s[58:59] op_sel:[0,0,1] op_sel_hi:[1,0,1]\nv_exp_f32 v50, v50 clamp\n"
      "v_pk_fma_f32 v[58:59], v[58:59], s[58:59], s[58:59] op_sel:[0,0,1] op_sel_hi:[1,0,1]\nv_pk_fma_f32 v[52:53], v[52:53], v[188:189], s[60:61] op_sel_hi:[1,1,0]\n"
      "v_pk_fma_f32 v[54:55], v[54:55], v[190:191], s[60:61] op_sel_hi:[1,1,0]\nv_exp_f32 v51, v51 clamp\nv_pk_fma_f32 v[56:57], v[56:57], v[192:193], s[60:61] op_sel_hi:[1,1,0]\n"
      "v_pk_fma_f32 v[58:59], v[58:59], v[194:195], s[60:61] op_sel_hi:[1,1,0]\nv_exp_f32 v52, v52 clamp\nv_pk_fma_f32 v[46:47], v[46:47], v[10:11], v[66:67]\n"
      "v_pk_fma_f32 v[44:45], v[44:45], v[8:9], v[64:65]\nv_pk_fma_f32 v[48:49], v[48:49], v[12:13], v[68:69]\nv_exp_f32 v53, v53 clamp\nv_pk_fma_f32 v[50:51], v[50:51], v[14:15], v[70:71]\n"
      "v_pk_mul_f32 v[180:181], v[180:181], v[46:47]\nv_pk_mul_f32 v[184:185], v[184:185], v[50:51]\nv_exp_f32 v54, v54 clamp\nv_pk_fma_f32 v[180:181], v[182:183], v[44:45], v[180:181]\n"
      "v_pk_fma_f32 v[184:185], v[186:187], v[48:49], v[184:185]\nv_pk_mul_f32 v[44:45], v[44:45], v[46:47]\nv_exp_f32 v55, v55 clamp\nv_pk_mul_f32 v[48:49], v[48:49], v[50:51]\n"
      "v_pk_mul_f32 v[46:47], v[44:45], v[48:49]\nv_rcp_f32 v46, v46\nv_pk_mul_f32 v[180:181], v[180:181], v[48:49]\nv_rcp_f32 v47, v47\nv_pk_fma_f32 v[180:181], v[184:185], v[44:45], v[180:181]\n"
      "v_pk_fma_f32 v[32:33], v[180:181], v[46:47], v[32:33]\nv_add_f32 v37, v32, v33\nv_pk_mul_f32 v[16:17], v[196:197], v[196:197]\nv_pk_mul_f32 v[18:19], v[198:199], v[198:199]\n"
      "v_exp_f32 v56, v56 clamp\nv_pk_mul_f32 v[20:21], v[200:201], v[200:201]\nv_pk_mul_f32 v[22:23], v[202:203], v[202:203]\nv_exp_f32 v57, v57 clamp\n"
      "v_pk_fma_f32 v[16:17], v[16:17], s[58:59], s[58:59] op_sel:[0,0,1] op_sel_hi:[1,0,1]\nv_pk_fma_f32 v[18:19], v[18:19], s[58:59], s[58:59] op_sel:[0,0,1] op_sel_hi:[1,0,1]\n"
      "v_pk_fma_f32 v[20:21], v[20:21], s[58:59], s[58:59] op_sel:[0,0,1] op_sel_hi:[1,0,1]\nv_exp_f32 v58, v58 clamp\n"
      "v_pk_fma_f32 v[22:23], v[22:23], s[58:59], s[58:59] op_sel:[0,0,1] op_sel_hi:[1,0,1]\nv_pk_fma_f32 v[16:17], v[16:17], v[196:197], s[60:61] op_sel_hi:[1,1,0]\n"
      "v_pk_fma_f32 v[18:19], v[18:19], v[198:199], s[60:61] op_sel_hi:[1,1,0]\nv_exp_f32 v59, v59 clamp\nv_pk_fma_f32 v[20:21], v[20:21], v[200:201], s[60:61] op_sel_hi:[1,1,0]\n"
      "v_pk_fma_f32 v[22:23], v[22:23], v[202:203], s[60:61] op_sel_hi:[1,1,0]\nv_exp_f32 v16, v16 clamp\nv_pk_fma_f32 v[54:55], v[54:55], v[2:3], v[26:27]\n"
      "v_pk_fma_f32 v[52:53], v[52:53], v[0:1], v[24:25]\nv_pk_fma_f32 v[56:57], v[56:57], v[4:5], v[28:29]\nv_exp_f32 v17, v17 clamp\nv_pk_fma_f32 v[58:59], v[58:59], v[6:7], v[30:31]\n"
      "v_pk_mul_f32 v[188:189], v[188:189], v[54:55]\nv_pk_mul_f32 v[192:193], v[192:193], v[58:59]\nv_exp_f32 v18, v18 clamp\nv_pk_fma_f32 v[188:189], v[190:191], v[52:53], v[188:189]\n"
      "v_pk_fma_f32 v[192:193], v[194:195], v[56:57], v[192:193]\nv_pk_mul_f32 v[52:53], v[52:53], v[54:55]\nv_exp_f32 v19, v19 clamp\nv_pk_mul_f32 v[56:57], v[56:57], v[58:59]\n"
      "v_pk_mul_f32 v[54:55], v[52:53], v[56:57]\nv_rcp_f32 v54, v54\nv_pk_mul_f32 v[188:189], v[188:189], v[56:57]\nv_rcp_f32 v55, v55\nv_pk_fma_f32 v[188:189], v[192:193], v[52:53], v[188:189]\n"
      "v_pk_mul_f32 v[32:33], v[188:189], v[54:55]\nv_pk_mul_f32 v[44:45], v[204:205], v[204:205]\nv_pk_mul_f32 v[46:47], v[206:207], v[206:207]\nv_exp_f32 v20, v20 clamp\n"
      "v_pk_mul_f32 v[48:49], v[208:209], v[208:209]\nv_pk_mul_f32 v[50:51], v[210:211], v[210:211]\nv_exp_f32 v21, v21 clamp\n"
      "v_pk_fma_f32 v[44:45], v[44:45], s[58:59], s[58:59] op_sel:[0,0,1] op_sel_hi:[1,0,1]\nv_pk_fma_f32 v[46:47], v[46:47], s[58:59], s[58:59] op_sel:[0,0,1] op_sel_hi:[1,0,1]\n"
      "v_pk_fma_f32 v[48:49], v[48:49], s[58:59], s[58:59] op_sel:[0,0,1] op_sel_hi:[1,0,1]\nv_exp_f32 v22, v22 clamp\n"
      "v_pk_fma_f32 v[50:51], v[50:51], s[58:59], s[58:59] op_sel:[0,0,1] op_sel_hi:[1,0,1]\nv_pk_fma_f32 v[44:45], v[44:45], v[204:205], s[60:61] op_sel_hi:[1,1,0]\n"
      "v_pk_fma_f32 v[46:47], v[46:47], v[206:207], s[60:61] op_sel_hi:[1,1,0]\nv_exp_f32 v23, v23 clamp\nv_pk_fma_f32 v[48:49], v[48:49], v[208:209], s[60:61] op_sel_hi:[1,1,0]\n"
      "v_pk_fma_f32 v[50:51], v[50:51], v[210:211], s[60:61] op_sel_hi:[1,1,0]\nv_exp_f32 v44, v44 clamp\nv_pk_fma_f32 v[18:19], v[18:19], v[10:11], v[66:67]\n"
      "v_pk_fma_f32 v[16:17], v[16:17], v[8:9], v[64:65]\nv_pk_fma_f32 v[20:21], v[20:21], v[12:13], v[68:69]\nv_exp_f32 v45, v45 clamp\nv_pk_fma_f32 v[22:23], v[22:23], v[14:15], v[70:71]\n"
      "v_pk_mul_f32 v[196:197], v[196:197], v[18:19]\nv_pk_mul_f32 v[200:201], v[200:201], v[22:23]\nv_exp_f32 v46, v46 clamp\nv_pk_fma_f32 v[196:197], v[198:199], v[16:17], v[196:197]\n"
      "v_pk_fma_f32 v[200:201], v[202:203], v[20:21], v[200:201]\nv_pk_mul_f32 v[16:17], v[16:17], v[18:19]\nv_exp_f32 v47, v47 clamp\nv_pk_mul_f32 v[20:21], v[20:21], v[22:23]\n"
      "v_pk_mul_f32 v[18:19], v[16:17], v[20:21]\nv_rcp_f32 v18, v18\nv_pk_mul_f32 v[196:197], v[196:197], v[20:21]\nv_rcp_f32 v19, v19\nv_pk_fma_f32 v[196:197], v[200:201], v[16:17], v[196:197]\n"
      "v_pk_fma_f32 v[32:33], v[196:197], v[18:19], v[32:33]\nv_add_f32 v38, v32, v33\nv_pk_mul_f32 v[52:53], v[212:213], v[212:213]\nv_pk_mul_f32 v[54:55], v[214:215], v[214:215]\n"
      "v_exp_f32 v48, v48 clamp\nv_pk_mul_f32 v[56:57], v[216:217], v[216:217]\nv_pk_mul_f32 v[58:59], v[218:219], v[218:219]\nv_exp_f32 v49, v49 clamp\n"
      "v_pk_fma_f32 v[52:53], v[52:53], s[58:59], s[58:59] op_sel:[0,0,1] op_sel_hi:[1,0,1]\nv_pk_fma_f32 v[54:55], v[54:55], s[58:59], s[58:59] op_sel:[0,0,1] op_sel_hi:[1,0,1]\n"
      "v_pk_fma_f32 v[56:57], v[56:57], s[58:59], s[58:59] op_sel:[0,0,1] op_sel_hi:[1,0,1]\nv_exp_f32 v50, v50 clamp\n"
      "v_pk_fma_f32 v[58:59], v[58:59], s[58:59], s[58:59] op_sel:[0,0,1] op_sel_hi:[1,0,1]\nv_pk_fma_f32 v[52:53], v[52:53], v[212:213], s[60:61] op_sel_hi:[1,1,0]\n"
      "v_pk_fma_f32 v[54:55], v[54:55], v[214:215], s[60:61] op_sel_hi:[1,1,0]\nv_exp_f32 v51, v51 clamp\nv_pk_fma_f32 v[56:57], v[56:57], v[216:217], s[60:61] op_sel_hi:[1,1,0]\n"
      "v_pk_fma_f32 v[58:59], v[58:59], v[218:219], s[60:61] op_sel_hi:[1,1,0]\nv_exp_f32 v52, v52 clamp\nv_pk_fma_f32 v[46:47], v[46:47], v[2:3], v[26:27]\n"
      "v_pk_fma_f32 v[44:45], v[44:45], v[0:1], v[24:25]\nv_pk_fma_f32 v[48:49], v[48:49], v[4:5], v[28:29]\nv_exp_f32 v53, v53 clamp\nv_pk_fma_f32 v[50:51], v[50:51], v[6:7], v[30:31]\n"
      "v_pk_mul_f32 v[204:205], v[204:205], v[46:47]\nv_pk_mul_f32 v[208:209], v[208:209], v[50:51]\nv_exp_f32 v54, v54 clamp\nv_pk_fma_f32 v[204:205], v[206:207], v[44:45], v[204:205]\n"
      "v_pk_fma_f32 v[208:209], v[210:211], v[48:49], v[208:209]\nv_pk_mul_f32 v[44:45], v[44:45], v[46:47]\nv_exp_f32 v55, v55 clamp\nv_pk_mul_f32 v[48:49], v[48:49], v[50:51]\n"
      "v_pk_mul_f32 v[46:47], v[44:45], v[48:49]\nv_rcp_f32 v46, v46\nv_pk_mul_f32 v[204:205], v[204:205], v[48:49]\nv_rcp_f32 v47, v47\nv_pk_fma_f32 v[204:205], v[208:209], v[44:45], v[204:205]\n"
      "v_pk_mul_f32 v[32:33], v[204:205], v[46:47]\nv_pk_mul_f32 v[16:17], v[220:221], v[220:221]\nv_pk_mul_f32 v[18:19], v[222:223], v[222:223]\nv_exp_f32 v56, v56 clamp\n"
      "v_pk_mul_f32 v[20:21], v[224:225], v[224:225]\nv_pk_mul_f32 v[22:23], v[226:227], v[226:227]\nv_exp_f32 v57, v57 clamp\n"
      "v_pk_fma_f32 v[16:17], v[16:17], s[58:59], s[58:59] op_sel:[0,0,1] op_sel_hi:[1,0,1]\nv_pk_fma_f32 v[18:19], v[18:19], s[58:59], s[58:59] op_sel:[0,0,1] op_sel_hi:[1,0,1]\n"
      "v_pk_fma_f32 v[20:21], v[20:21], s[58:59], s[58:59] op_sel:[0,0,1] op_sel_hi:[1,0,1]\nv_exp_f32 v58, v58 clamp\n"
      "v_pk_fma_f32 v[22:23], v[22:23], s[58:59], s[58:59] op_sel:[0,0,1] op_sel_hi:[1,0,1]\nv_pk_fma_f32 v[16:17], v[16:17], v[220:221], s[60:61] op_sel_hi:[1,1,0]\n"
      "v_pk_fma_f32 v[18:19], v[18:19], v[222:223], s[60:61] op_sel_hi:[1,1,0]\nv_exp_f32 v59, v59 clamp\nv_pk_fma_f32 v[20:21], v[20:21], v[224:225], s[60:61] op_sel_hi:[1,1,0]\n"
      "v_pk_fma_f32 v[22:23], v[22:23], v[226:227], s[60:61] op_sel_hi:[1,1,0]\nv_exp_f32 v16, v16 clamp\nv_pk_fma_f32 v[54:55], v[54:55], v[10:11], v[66:67]\n"
      "v_pk_fma_f32 v[52:53], v[52:53], v[8:9], v[64:65]\nv_pk_fma_f32 v[56:57], v[56:57], v[12:13], v[68:69]\nv_exp_f32 v17, v17 clamp\nv_pk_fma_f32 v[58:59], v[58:59], v[14:15], v[70:71]\n"
      "v_pk_mul_f32 v[212:213], v[212:213], v[54:55]\nv_pk_mul_f32 v[216:217], v[216:217], v[58:59]\nv_exp_f32 v18, v18 clamp\nv_pk_fma_f32 v[212:213], v[214:215], v[52:53], v[212:213]\n"
      "v_pk_fma_f32 v[216:217], v[218:219], v[56:57], v[216:217]\nv_pk_mul_f32 v[52:53], v[52:53], v[54:55]\nv_exp_f32 v19, v19 clamp\nv_pk_mul_f32 v[56:57], v[56:57], v[58:59]\n"
      "v_pk_mul_f32 v[54:55], v[52:53], v[56:57]\nv_rcp_f32 v54, v54\nv_pk_mul_f32 v[212:213], v[212:213], v[56:57]\nv_rcp_f32 v55, v55\nv_pk_fma_f32 v[212:213], v[216:217], v[52:53], v[212:213]\n"
      "v_pk_fma_f32 v[32:33], v[212:213], v[54:55], v[32:33]\nv_add_f32 v39, v32, v33\nv_pk_mul_f32 v[44:45], v[228:229], v[228:229]\nv_pk_mul_f32 v[46:47], v[230:231], v[230:231]\n"
      "v_exp_f32 v20, v20 clamp\nv_pk_mul_f32 v[48:49], v[232:233], v[232:233]\nv_pk_mul_f32 v[50:51], v[234:235], v[234:235]\nv_exp_f32 v21, v21 clamp\n"
      "v_pk_fma_f32 v[44:45], v[44:45], s[58:59], s[58:59] op_sel:[0,0,1] op_sel_hi:[1,0,1]\nv_pk_fma_f32 v[46:47], v[46:47], s[58:59], s[58:59] op_sel:[0,0,1] op_sel_hi:[1,0,1]\n"
      "v_pk_fma_f32 v[48:49], v[48:49], s[58:59], s[58:59] op_sel:[0,0,1] op_sel_hi:[1,0,1]\nv_exp_f32 v22, v22 clamp\n"
      "v_pk_fma_f32 v[50:51], v[50:51], s[58:59], s[58:59] op_sel:[0,0,1] op_sel_hi:[1,0,1]\nv_pk_fma_f32 v[44:45], v[44:45], v[228:229], s[60:61] op_sel_hi:[1,1,0]\n"
      "v_pk_fma_f32 v[46:47], v[46:47], v[230:231], s[60:61] op_sel_hi:[1,1,0]\nv_exp_f32 v23, v23 clamp\nv_pk_fma_f32 v[48:49], v[48:49], v[232:233], s[60:61] op_sel_hi:[1,1,0]\n"
      "v_pk_fma_f32 v[50:51], v[50:51], v[234:235], s[60:61] op_sel_hi:[1,1,0]\nv_exp_f32 v44, v44 clamp\nv_pk_fma_f32 v[18:19], v[18:19], v[2:3], v[26:27]\nv_exp_f32 v45, v45 clamp\n"
      "v_pk_fma_f32 v[16:17], v[16:17], v[0:1], v[24:25]\nv_pk_fma_f32 v[20:21], v[20:21], v[4:5], v[28:29]\nv_exp_f32 v46, v46 clamp\nv_pk_fma_f32 v[22:23], v[22:23], v[6:7], v[30:31]\n"
      "v_pk_mul_f32 v[220:221], v[220:221], v[18:19]\nv_exp_f32 v47, v47 clamp\nv_pk_mul_f32 v[224:225], v[224:225], v[22:23]\nv_pk_fma_f32 v[220:221], v[222:223], v[16:17], v[220:221]\n"
      "v_exp_f32 v48, v48 clamp\nv_pk_fma_f32 v[224:225], v[226:227], v[20:21], v[224:225]\nv_exp_f32 v49, v49 clamp\nv_pk_mul_f32 v[16:17], v[16:17], v[18:19]\n"
      "v_pk_mul_f32 v[20:21], v[20:21], v[22:23]\nv_exp_f32 v50, v50 clamp\nv_pk_mul_f32 v[18:19], v[16:17], v[20:21]\nv_rcp_f32 v18, v18\nv_pk_mul_f32 v[220:221], v[220:221], v[20:21]\n"
      "v_exp_f32 v51, v51 clamp\nv_rcp_f32 v19, v19\nv_pk_fma_f32 v[220:221], v[224:225], v[16:17], v[220:221]\nv_pk_mul_f32 v[32:33], v[220:221], v[18:19]\n"
      "v_pk_fma_f32 v[46:47], v[46:47], v[10:11], v[66:67]\nv_pk_fma_f32 v[44:45], v[44:45], v[8:9], v[64:65]\nv_pk_fma_f32 v[48:49], v[48:49], v[12:13], v[68:69]\n"
      "v_pk_fma_f32 v[50:51], v[50:51], v[14:15], v[70:71]\nv_pk_mul_f32 v[228:229], v[228:229], v[46:47]\nv_pk_mul_f32 v[232:233], v[232:233], v[50:51]\n"
      "v_pk_fma_f32 v[228:229], v[230:231], v[44:45], v[228:229]\nv_pk_fma_f32 v[232:233], v[234:235], v[48:49], v[232:233]\nv_pk_mul_f32 v[44:45], v[44:45], v[46:47]\n"
      "v_pk_mul_f32 v[48:49], v[48:49], v[50:51]\nv_pk_mul_f32 v[46:47], v[44:45], v[48:49]\nv_rcp_f32 v46, v46\nv_pk_mul_f32 v[228:229], v[228:229], v[48:49]\nv_rcp_f32 v47, v47\n"
      "v_pk_fma_f32 v[228:229], v[232:233], v[44:45], v[228:229]\nv_pk_fma_f32 v[32:33], v[228:229], v[46:47], v[32:33]\nv_add_f32 v40, v32, v33\ns_barrier\n"
      ".Lge:\nv_mov_b32 v43, v42\nds_read_b128 v[48:51], v246\nds_read_b128 v[52:55], v246 offset:16\nds_read_b64_tr_b16 v[68:69], v238 offset:0\nds_read_b64_tr_b16 v[70:71], v239 offset:0\n"
      "ds_read_b64_tr_b16 v[56:57], v236 offset:0\nds_read_b64_tr_b16 v[58:59], v237 offset:128\nds_read_b64_tr_b16 v[60:61], v237 offset:256\nds_read_b64_tr_b16 v[62:63], v237 offset:384\n"
      "ds_read_b64_tr_b16 v[64:65], v237 offset:512\nds_read_b64_tr_b16 v[66:67], v237 offset:640\nds_read_b128 v[44:47], v248 offset:48\nv_permlane32_swap_b32 v36, v38\n"
      "v_permlane32_swap_b32 v37, v39\nv_permlane32_swap_b32 v40, v42\nv_permlane32_swap_b32 v41, v43\nv_add_f32 v36, v36, v38\nv_add_f32 v37, v37, v39\nv_add_f32 v40, v40, v42\n"
      "v_add_f32 v41, v41, v43\ns_nop 0\nv_permlane16_swap_b32 v36, v37\nv_permlane16_swap_b32 v40, v41\nv_add_f32 v36, v36, v37\nv_add_f32 v40, v40, v41\nds_write_b32 v254, v36\n"
      "ds_write_b32 v255, v40 offset:256\nds_add_rtn_u32 v2, v253, v241 offset:20\ns_waitcnt lgkmcnt(11)\nv_readlane_b32 s48, v69, 48\ns_mov_b32 s56, 0\ns_mov_b32 s44, 0\ns_mov_b32 s33, s45\n"
      "s_waitcnt lgkmcnt(10)\nv_pk_mul_f16 v20, v68, v70\nv_pk_mul_f16 v21, v69, v71\ns_waitcnt lgkmcnt(9)\nv_pk_mul_f16 v22, v56, v56 op_sel_hi:[0,1]\nv_pk_mul_f16 v23, v56, v57 op_sel_hi:[0,1]\n"
      "v_pk_mul_f16 v16, v68, s48 op_sel:[0,1]\nv_pk_mul_f16 v17, v69, s48 op_sel:[0,1]\nv_pk_mul_f16 v18, v70, s48 op_sel:[0,1]\nv_pk_mul_f16 v19, v71, s48 op_sel:[0,1]\ns_nop 1\n"
      "v_mfma_f32_16x16x32_f16 v[40:43], v[76:79], v[16:19], 0\nv_mfma_f32_16x16x32_f16 v[40:43], v[88:91], v[20:23], v[40:43]\ns_add_u32 s45, s45, 2\ns_cmp_ge_u32 s45, s46\ns_cbranch_scc0 .Lc_bg\n"
      "s_swappc_b64 s[4:5], s[34:35]\n"
      ".Lc_bg:\nv_mfma_f32_16x16x32_f16 v[36:39], v[84:87], v[20:23], 0\ns_waitcnt lgkmcnt(4)\nv_pk_mul_f16 v24, v66, v66 op_sel_hi:[0,1]\nv_pk_mul_f16 v25, v66, v57 op_sel_hi:[0,1]\n"
      "ds_read_b128 v[124:127], v252 offset:13312\nv_mfma_f32_16x16x32_f16 v[36:39], v[72:75], v[16:19], v[36:39]\nv_pk_mul_f16 v26, v56, v66 op_sel_hi:[0,1]\n"
      "v_pk_mul_f16 v27, v56, v67 op_sel_hi:[0,1]\nds_read_b128 v[128:131], v252 offset:14336\nv_mfma_f32_16x16x32_f16 v[32:35], v[80:83], v[20:23], 0\nv_pk_mul_f16 v28, v58, v66 op_sel_hi:[0,1]\n"
      "v_pk_mul_f16 v29, v58, v67 op_sel_hi:[0,1]\nds_read_b128 v[132:135], v252 offset:15360\ns_waitcnt lgkmcnt(6)\nv_smfmac_f32_16x16x64_f16 v[44:47], v[92:95], v[16:23], v242\n"
      "v_pk_mul_f16 v30, v60, v66 op_sel_hi:[0,1]\nv_pk_mul_f16 v31, v60, v67 op_sel_hi:[0,1]\nds_read_b128 v[136:139], v252 offset:16384\n"
      "v_mfma_f32_16x16x32_f16 v[32:35], v[96:99], v[24:27], v[32:35]\nv_pk_mul_f16 v16, v62, v66 op_sel_hi:[0,1]\nv_pk_mul_f16 v17, v62, v67 op_sel_hi:[0,1]\n"
      "ds_read_b128 v[140:143], v252 offset:17408\nv_mfma_f32_16x16x32_f16 v[32:35], v[108:111], v[28:31], v[32:35]\nv_pk_mul_f16 v18, v64, v66 op_sel_hi:[0,1]\n"
      "ds_read_b128 v[144:147], v252 offset:18432\nv_mfma_f32_16x16x32_f16 v[36:39], v[112:115], v[28:31], v[36:39]\nv_pk_mul_f16 v19, v64, v67 op_sel_hi:[0,1]\ns_cmp_ge_u32 s45, s46\n"
      "s_cbranch_scc0 .Lc_bh\ns_swappc_b64 s[4:5], s[34:35]\n"
      ".Lc_bh:\nds_read_b128 v[148:151], v252 offset:19456\nv_mfma_f32_16x16x32_f16 v[36:39], v[100:103], v[24:27], v[36:39]\nv_pk_mul_f16 v20, v64, v64 op_sel_hi:[0,1]\n"
      "ds_read_b128 v[152:155], v252 offset:20480\nv_mfma_f32_16x16x32_f16 v[40:43], v[104:107], v[24:27], v[40:43]\nv_pk_mul_f16 v21, v64, v57 op_sel_hi:[0,1]\n"
      "ds_read_b128 v[156:159], v252 offset:21504\nv_mfma_f32_16x16x32_f16 v[40:43], v[116:119], v[28:31], v[40:43]\nv_pk_mul_f16 v22, v56, v64 op_sel_hi:[0,1]\n"
      "ds_read_b128 v[160:163], v252 offset:22528\nv_smfmac_f32_16x16x64_f16 v[44:47], v[120:123], v[24:31], v242\nv_pk_mul_f16 v23, v56, v65 op_sel_hi:[0,1]\n"
      "ds_read_b128 v[164:167], v252 offset:23552\ns_waitcnt lgkmcnt(10)\nv_mfma_f32_16x16x32_f16 v[32:35], v[124:127], v[16:19], v[32:35]\nv_pk_mul_f16 v24, v58, v64 op_sel_hi:[0,1]\n"
      "v_pk_mul_f16 v25, v58, v65 op_sel_hi:[0,1]\nds_read_b64_tr_b16 v[70:71], v239 offset:1856\nds_read_b128 v[168:171], v252 offset:24576\ns_waitcnt lgkmcnt(9)\n"
      "v_mfma_f32_16x16x32_f16 v[32:35], v[136:139], v[20:23], v[32:35]\nv_pk_mul_f16 v26, v60, v64 op_sel_hi:[0,1]\nds_read_b64_tr_b16 v[68:69], v238 offset:1856\n"
      "ds_read_b128 v[172:175], v252 offset:25600\ns_waitcnt lgkmcnt(10)\nv_mfma_f32_16x16x32_f16 v[36:39], v[140:143], v[20:23], v[36:39]\nv_pk_mul_f16 v27, v60, v65 op_sel_hi:[0,1]\n"
      "ds_read_b64_tr_b16 v[66:67], v237 offset:2496\nds_read_b128 v[176:179], v252 offset:26624\nv_mfma_f32_16x16x32_f16 v[36:39], v[128:131], v[16:19], v[36:39]\n"
      "v_pk_mul_f16 v28, v62, v64 op_sel_hi:[0,1]\nds_read_b128 v[180:183], v252 offset:27648\nv_mfma_f32_16x16x32_f16 v[40:43], v[132:135], v[16:19], v[40:43]\n"
      "v_pk_mul_f16 v29, v62, v65 op_sel_hi:[0,1]\nds_read_b128 v[184:187], v252 offset:28672\ns_waitcnt lgkmcnt(13)\nv_mfma_f32_16x16x32_f16 v[40:43], v[144:147], v[20:23], v[40:43]\n"
      "v_pk_mul_f16 v30, v62, v62 op_sel_hi:[0,1]\nds_read_b128 v[188:191], v252 offset:29696\ns_waitcnt lgkmcnt(13)\nv_smfmac_f32_16x16x64_f16 v[44:47], v[148:151], v[16:23], v242\n"
      "v_pk_mul_f16 v31, v62, v57 op_sel_hi:[0,1]\nds_read_b128 v[192:195], v252 offset:30720\ns_waitcnt lgkmcnt(13)\nv_mfma_f32_16x16x32_f16 v[32:35], v[152:155], v[24:27], v[32:35]\n"
      "v_pk_mul_f16 v16, v56, v62 op_sel_hi:[0,1]\nv_pk_mul_f16 v17, v56, v63 op_sel_hi:[0,1]\nds_read_b128 v[196:199], v252 offset:31744\ns_waitcnt lgkmcnt(11)\n"
      "v_mfma_f32_16x16x32_f16 v[32:35], v[164:167], v[28:31], v[32:35]\nv_pk_mul_f16 v18, v58, v62 op_sel_hi:[0,1]\nds_read_b128 v[200:203], v252 offset:32768\n"
      "ds_read_b64_tr_b16 v[64:65], v237 offset:2368\ns_waitcnt lgkmcnt(11)\nv_mfma_f32_16x16x32_f16 v[36:39], v[168:171], v[28:31], v[36:39]\nv_pk_mul_f16 v19, v58, v63 op_sel_hi:[0,1]\n"
      "ds_read_b128 v[204:207], v252 offset:33792\nv_mfma_f32_16x16x32_f16 v[36:39], v[156:159], v[24:27], v[36:39]\nv_pk_mul_f16 v20, v60, v62 op_sel_hi:[0,1]\n"
      "ds_read_b128 v[208:211], v252 offset:34816\nv_mfma_f32_16x16x32_f16 v[40:43], v[160:163], v[24:27], v[40:43]\nv_pk_mul_f16 v21, v60, v63 op_sel_hi:[0,1]\n"
      "ds_read_b128 v[212:215], v252 offset:35840\ns_waitcnt lgkmcnt(13)\nv_readlane_b32 s48, v69, 48\ns_waitcnt lgkmcnt(12)\nv_mfma_f32_16x16x32_f16 v[40:43], v[172:175], v[28:31], v[40:43]\n"
      "v_pk_mul_f16 v22, v60, v60 op_sel_hi:[0,1]\nds_read_b128 v[216:219], v252 offset:36864\ns_waitcnt lgkmcnt(11)\nv_smfmac_f32_16x16x64_f16 v[44:47], v[176:179], v[24:31], v242\n"
      "v_pk_mul_f16 v23, v60, v57 op_sel_hi:[0,1]\nds_read_b128 v[220:223], v252 offset:37888\ns_waitcnt lgkmcnt(11)\nv_mfma_f32_16x16x32_f16 v[32:35], v[180:183], v[16:19], v[32:35]\n"
      "v_pk_mul_f16 v24, v56, v60 op_sel_hi:[0,1]\nv_pk_mul_f16 v25, v56, v61 op_sel_hi:[0,1]\nds_read_b128 v[224:227], v252 offset:38912\ns_waitcnt lgkmcnt(9)\n"
      "v_mfma_f32_16x16x32_f16 v[32:35], v[192:195], v[20:23], v[32:35]\nv_pk_mul_f16 v26, v58, v60 op_sel_hi:[0,1]\nv_pk_mul_f16 v27, v58, v61 op_sel_hi:[0,1]\n"
      "ds_read_b128 v[228:231], v252 offset:39936\ns_waitcnt lgkmcnt(9)\nv_mfma_f32_16x16x32_f16 v[36:39], v[196:199], v[20:23], v[36:39]\nv_pk_mul_f16 v28, v58, v58 op_sel_hi:[0,1]\n"
      "ds_read_b128 v[232:235], v252 offset:40960\nds_read_b64_tr_b16 v[62:63], v237 offset:2240\nv_mfma_f32_16x16x32_f16 v[36:39], v[184:187], v[16:19], v[36:39]\n"
      "v_pk_mul_f16 v29, v58, v57 op_sel_hi:[0,1]\nds_read_b64_tr_b16 v[60:61], v237 offset:2112\nv_mfma_f32_16x16x32_f16 v[40:43], v[188:191], v[16:19], v[40:43]\n"
      "v_pk_mul_f16 v30, v56, v58 op_sel_hi:[0,1]\ns_waitcnt lgkmcnt(11)\nv_mfma_f32_16x16x32_f16 v[40:43], v[200:203], v[20:23], v[40:43]\nv_pk_mul_f16 v31, v56, v59 op_sel_hi:[0,1]\n"
      "s_waitcnt lgkmcnt(9)\nv_smfmac_f32_16x16x64_f16 v[44:47], v[204:207], v[16:23], v242\nds_read_b64_tr_b16 v[56:57], v236 offset:1856\nds_read_b64_tr_b16 v[58:59], v237 offset:1984\n"
      "s_waitcnt lgkmcnt(4)\nv_smfmac_f32_16x16x64_f16 v[44:47], v[232:235], v[24:31], v242\nv_pk_mul_f16 v16, v68, s48 op_sel:[0,1]\nv_pk_mul_f16 v17, v69, s48 op_sel:[0,1]\n"
      "v_mfma_f32_16x16x32_f16 v[32:35], v[208:211], v[24:27], v[32:35]\nv_pk_mul_f16 v18, v70, s48 op_sel:[0,1]\nv_pk_mul_f16 v19, v71, s48 op_sel:[0,1]\n"
      "v_mfma_f32_16x16x32_f16 v[32:35], v[220:223], v[28:31], v[32:35]\nv_pk_mul_f16 v20, v68, v70\nv_pk_mul_f16 v21, v69, v71\nv_mfma_f32_16x16x32_f16 v[36:39], v[224:227], v[28:31], v[36:39]\n"
      "v_add_f32 v44, v44, v45\nv_add_f32 v46, v46, v47\nv_mfma_f32_16x16x32_f16 v[36:39], v[212:215], v[24:27], v[36:39]\nv_mul_f32 v55, v44, v54\nv_mul_f32 v55, v55, v46\n"
      "ds_read_b128 v[44:47], v248 offset:48\nv_mfma_f32_16x16x32_f16 v[40:43], v[216:219], v[24:27], v[40:43]\ns_waitcnt lgkmcnt(2)\nv_pk_mul_f16 v22, v56, v56 op_sel_hi:[0,1]\n"
      "v_pk_mul_f16 v23, v56, v57 op_sel_hi:[0,1]\nv_mfma_f32_16x16x32_f16 v[40:43], v[228:231], v[28:31], v[40:43]\nv_mul_f32 v32, v32, v48\nv_mul_f32 v33, v33, v49\nv_mul_f32 v34, v34, v50\n"
      "s_waitcnt lgkmcnt(1)\ns_branch .Lc_u1\n"
      ".Lfeed:\ns_waitcnt vmcnt(7) lgkmcnt(0)\ns_cbranch_vccnz .Lw1004\ns_waitcnt vmcnt(6)\n"
      ".Lw1004:\ns_barrier\nds_read_b64 v[44:45], v30 offset:45056\ns_add_u32 m0, s54, 0\ns_nop 0\nglobal_load_lds_dwordx4 v236, s[50:51] offset:0\n"
      "global_load_lds_dwordx4 v237, s[50:51] offset:1024\nglobal_load_lds_dwordx4 v238, s[50:51] offset:2048\nglobal_load_lds_dwordx4 v239, s[50:51] offset:3072\ns_add_u32 s50, s50, 212992\n"
      "s_addc_u32 s51, s51, 0\ns_add_u32 m0, s26, 20480\ns_nop 0\nglobal_load_lds_dwordx4 v27, s[6:7] offset:0\nv_lshl_add_u32 v27, v197, 7, v4\ns_cbranch_vccz .La1003\ns_add_u32 m0, s26, 24576\n"
      "s_nop 0\nglobal_load_lds_dwordx4 v28, s[6:7] offset:0\n"
      ".La1003:\nv_lshl_add_u32 v28, v203, 7, v4\nds_read2_b64 v[200:203], v9 offset0:2 offset1:3\ns_add_u32 m0, s53, 0\ns_nop 0\nglobal_load_lds_dwordx4 v22, s[6:7] offset:0\n"
      "v_lshl_add_u32 v22, v179, 7, v4\nds_read2_b64 v[176:179], v6 offset0:2 offset1:3\nglobal_load_lds_dwordx4 v23, s[44:45] offset:1024\nv_lshl_add_u32 v23, v181, 7, v5\n"
      "global_load_lds_dwordx4 v24, s[46:47] offset:2048\nv_lshl_add_u32 v24, v187, 7, v4\nds_read2_b64 v[184:187], v7 offset0:2 offset1:3\nglobal_load_lds_dwordx4 v25, s[48:49] offset:3072\n"
      "v_lshl_add_u32 v25, v189, 7, v5\ns_add_u32 m0, s53, 4096\ns_nop 0\nglobal_load_lds_dwordx4 v26, s[6:7] offset:0\nv_lshl_add_u32 v26, v195, 7, v4\n"
      "ds_read2_b64 v[192:195], v8 offset0:2 offset1:3\ns_waitcnt vmcnt(11) lgkmcnt(0)\ns_cbranch_vccnz .Lw1006\ns_waitcnt vmcnt(10)\n"
      ".Lw1006:\ns_barrier\ns_add_u32 m0, s54, 45056\ns_nop 0\nglobal_load_lds_dwordx4 v236, s[50:51] offset:0\nds_read_b64 v[46:47], v31 offset:0\n"
      "global_load_lds_dwordx4 v237, s[50:51] offset:1024\nglobal_load_lds_dwordx4 v238, s[50:51] offset:2048\nglobal_load_lds_dwordx4 v239, s[50:51] offset:3072\ns_add_u32 s50, s50, 212992\n"
      "s_addc_u32 s51, s51, 0\ns_cmp_eq_u32 s27, 22\ns_cselect_b64 s[50:51], s[10:11], s[50:51]\ns_add_u32 m0, s26, 65536\nv_max3_f16 v32, |v44|, |v45|, v32\n"
      "global_load_lds_dwordx4 v27, s[6:7] offset:0\nv_lshl_add_u32 v27, v198, 7, v4\nv_max3_f32 v33, |v44|, |v45|, v33\ns_cbranch_vccz .La1007\ns_add_u32 m0, s26, 69632\ns_nop 0\n"
      "global_load_lds_dwordx4 v28, s[6:7] offset:0\n"
      ".La1007:\nv_lshl_add_u32 v28, v200, 7, v4\ns_add_u32 m0, s53, 45056\nv_and_b32 v96, s55, v44\nglobal_load_lds_dwordx4 v22, s[6:7] offset:0\nv_lshl_add_u32 v22, v176, 7, v4\n"
      "v_xor_b32 v44, v44, v96\nglobal_load_lds_dwordx4 v23, s[44:45] offset:1024\nv_lshl_add_u32 v23, v182, 7, v5\nv_and_b32 v97, s55, v45\nglobal_load_lds_dwordx4 v24, s[46:47] offset:2048\n"
      "v_lshl_add_u32 v24, v184, 7, v4\nv_xor_b32 v45, v45, v97\nglobal_load_lds_dwordx4 v25, s[48:49] offset:3072\ns_add_u32 m0, s53, 49152\nv_lshl_add_u32 v25, v190, 7, v5\n"
      "global_load_lds_dwordx4 v26, s[6:7] offset:0\nv_lshl_add_u32 v26, v192, 7, v4\ns_cmp_lg_u32 s31, 0\ns_cbranch_scc1 .Lb1008\ns_lshl_b32 s42, s24, 9\nv_lshl_add_u32 v39, v240, 3, s42\n"
      "s_and_b32 s42, s2, 7\ns_lshl_b32 s42, s42, 11\ns_add_u32 s42, s42, 4096\ns_add_u32 s58, s20, s42\ns_addc_u32 s59, s21, 0\nglobal_store_dwordx2 v39, v[254:255], s[58:59] sc1\n"
      ".Lb1008:\ns_waitcnt vmcnt(11) lgkmcnt(0)\ns_cbranch_vccnz .Lw1009\ns_waitcnt vmcnt(10)\n"
      ".Lw1009:\ns_barrier\ns_add_u32 m0, s54, 90112\ns_nop 0\nglobal_load_lds_dwordx4 v236, s[50:51] offset:0\nds_read_b64 v[48:49], v30 offset:0\n"
      "global_load_lds_dwordx4 v237, s[50:51] offset:1024\nglobal_load_lds_dwordx4 v238, s[50:51] offset:2048\nglobal_load_lds_dwordx4 v239, s[50:51] offset:3072\ns_add_u32 s50, s50, 212992\n"
      "s_addc_u32 s51, s51, 0\ns_add_u32 m0, s26, 110592\nv_max3_f16 v32, |v46|, |v47|, v32\nglobal_load_lds_dwordx4 v27, s[6:7] offset:0\nv_lshl_add_u32 v27, v199, 7, v4\n"
      "ds_read2_b64 v[196:199], v8 offset0:115 offset1:116\nv_max3_f32 v33, |v46|, |v47|, v33\ns_cbranch_vccz .La1010\ns_add_u32 m0, s26, 114688\ns_nop 0\n"
      "global_load_lds_dwordx4 v28, s[6:7] offset:0\n"
      ".La1010:\nv_lshl_add_u32 v28, v201, 7, v4\ns_add_u32 m0, s53, 90112\nv_and_b32 v98, s55, v46\nglobal_load_lds_dwordx4 v22, s[6:7] offset:0\nv_lshl_add_u32 v22, v177, 7, v4\n"
      "v_xor_b32 v46, v46, v98\nglobal_load_lds_dwordx4 v23, s[44:45] offset:1024\nv_lshl_add_u32 v23, v183, 7, v5\nds_read2_b64 v[180:183], v6 offset0:115 offset1:116\nv_and_b32 v99, s55, v47\n"
      "global_load_lds_dwordx4 v24, s[46:47] offset:2048\nv_lshl_add_u32 v24, v185, 7, v4\nv_xor_b32 v47, v47, v99\nglobal_load_lds_dwordx4 v25, s[48:49] offset:3072\n"
      "v_lshl_add_u32 v25, v191, 7, v5\ns_add_u32 m0, s53, 94208\nds_read2_b64 v[188:191], v7 offset0:115 offset1:116\nglobal_load_lds_dwordx4 v26, s[6:7] offset:0\n"
      "v_lshl_add_u32 v26, v193, 7, v4\ns_cmp_lg_u32 s24, 2\ns_cbranch_scc1 .Lo1011\ns_getpc_b64 s[62:63]\ns_add_u32 s62, s62, _ZL5g_tab@rel32@lo+2052\n"
      "s_addc_u32 s63, s63, _ZL5g_tab@rel32@hi+2060\ns_lshr_b32 s42, s2, 3\ns_lshl1_add_u32 s42, s2, s42\ns_and_b32 s42, s42, 15\ns_lshl_b32 s42, s42, 7\nv_lshl_add_u32 v40, v240, 4, s42\n"
      "s_mov_b32 m0, 162112\ns_mov_b64 exec, 0xff\nglobal_load_lds_dwordx4 v40, s[62:63]\ns_mov_b64 exec, -1\n"
      ".Lo1011:\ns_waitcnt vmcnt(11) lgkmcnt(0)\ns_cbranch_vccnz .Lw1012\ns_waitcnt vmcnt(10)\n"
      ".Lw1012:\ns_barrier\ns_add_u32 m0, s54, 0\ns_nop 0\nglobal_load_lds_dwordx4 v236, s[50:51] offset:0\nds_read_b64 v[50:51], v30 offset:45056\n"
      "global_load_lds_dwordx4 v237, s[50:51] offset:1024\nglobal_load_lds_dwordx4 v238, s[50:51] offset:2048\nglobal_load_lds_dwordx4 v239, s[50:51] offset:3072\ns_add_u32 s50, s50, 212992\n"
      "s_addc_u32 s51, s51, 0\ns_cmp_eq_u32 s27, 20\ns_cselect_b64 s[50:51], s[10:11], s[50:51]\ns_add_u32 m0, s26, 20480\nv_max3_f16 v32, |v48|, |v49|, v32\n"
      "global_load_lds_dwordx4 v27, s[6:7] offset:0\nv_lshl_add_u32 v27, v196, 7, v4\nv_max3_f32 v33, |v48|, |v49|, v33\ns_cbranch_vccz .La1013\ns_add_u32 m0, s26, 24576\ns_nop 0\n"
      "global_load_lds_dwordx4 v28, s[6:7] offset:0\n"
      ".La1013:\nv_lshl_add_u32 v28, v202, 7, v4\ns_add_u32 m0, s53, 0\nv_and_b32 v100, s55, v48\nglobal_load_lds_dwordx4 v22, s[6:7] offset:0\nv_lshl_add_u32 v22, v178, 7, v4\n"
      "v_xor_b32 v48, v48, v100\nglobal_load_lds_dwordx4 v23, s[44:45] offset:1024\nv_lshl_add_u32 v23, v180, 7, v5\nv_and_b32 v101, s55, v49\nglobal_load_lds_dwordx4 v24, s[46:47] offset:2048\n"
      "v_lshl_add_u32 v24, v186, 7, v4\nv_xor_b32 v49, v49, v101\nglobal_load_lds_dwordx4 v25, s[48:49] offset:3072\ns_add_u32 m0, s53, 4096\nv_lshl_add_u32 v25, v188, 7, v5\n"
      "global_load_lds_dwordx4 v26, s[6:7] offset:0\nv_lshl_add_u32 v26, v194, 7, v4\ns_waitcnt vmcnt(11) lgkmcnt(0)\ns_cbranch_vccnz .Lw1014\ns_waitcnt vmcnt(10)\n"
      ".Lw1014:\ns_barrier\ns_add_u32 m0, s54, 45056\ns_nop 0\nglobal_load_lds_dwordx4 v236, s[50:51] offset:0\nds_read_b64 v[52:53], v31 offset:0\n"
      "global_load_lds_dwordx4 v237, s[50:51] offset:1024\nglobal_load_lds_dwordx4 v238, s[50:51] offset:2048\nglobal_load_lds_dwordx4 v239, s[50:51] offset:3072\ns_add_u32 s50, s50, 212992\n"
      "s_addc_u32 s51, s51, 0\ns_add_u32 m0, s26, 65536\nv_max3_f16 v32, |v50|, |v51|, v32\nglobal_load_lds_dwordx4 v27, s[6:7] offset:0\nv_lshl_add_u32 v27, v197, 7, v4\n"
      "v_max3_f32 v33, |v50|, |v51|, v33\ns_cbranch_vccz .La1015\ns_add_u32 m0, s26, 69632\ns_nop 0\nglobal_load_lds_dwordx4 v28, s[6:7] offset:0\n"
      ".La1015:\nv_lshl_add_u32 v28, v203, 7, v4\nds_read2_b64 v[200:203], v9 offset0:4 offset1:5\ns_add_u32 m0, s53, 45056\nv_and_b32 v102, s55, v50\nglobal_load_lds_dwordx4 v22, s[6:7] offset:0\n"
      "v_lshl_add_u32 v22, v179, 7, v4\nds_read2_b64 v[176:179], v6 offset0:4 offset1:5\nv_xor_b32 v50, v50, v102\nglobal_load_lds_dwordx4 v23, s[44:45] offset:1024\n"
      "v_lshl_add_u32 v23, v181, 7, v5\nv_and_b32 v103, s55, v51\nglobal_load_lds_dwordx4 v24, s[46:47] offset:2048\nv_lshl_add_u32 v24, v187, 7, v4\n"
      "ds_read2_b64 v[184:187], v7 offset0:4 offset1:5\nv_xor_b32 v51, v51, v103\nglobal_load_lds_dwordx4 v25, s[48:49] offset:3072\ns_add_u32 m0, s53, 49152\nv_lshl_add_u32 v25, v189, 7, v5\n"
      "global_load_lds_dwordx4 v26, s[6:7] offset:0\nv_lshl_add_u32 v26, v195, 7, v4\nds_read2_b64 v[192:195], v8 offset0:4 offset1:5\ns_waitcnt vmcnt(11) lgkmcnt(0)\ns_cbranch_vccnz .Lw1016\n"
      "s_waitcnt vmcnt(10)\n"
      ".Lw1016:\ns_barrier\ns_add_u32 m0, s54, 90112\ns_nop 0\nglobal_load_lds_dwordx4 v236, s[50:51] offset:0\nds_read_b64 v[54:55], v30 offset:0\n"
      "global_load_lds_dwordx4 v237, s[50:51] offset:1024\nglobal_load_lds_dwordx4 v238, s[50:51] offset:2048\nglobal_load_lds_dwordx4 v239, s[50:51] offset:3072\ns_add_u32 s50, s50, 212992\n"
      "s_addc_u32 s51, s51, 0\ns_cmp_eq_u32 s27, 18\ns_cselect_b64 s[50:51], s[10:11], s[50:51]\ns_add_u32 m0, s26, 110592\nv_max3_f16 v32, |v52|, |v53|, v32\n"
      "global_load_lds_dwordx4 v27, s[6:7] offset:0\nv_lshl_add_u32 v27, v198, 7, v4\nv_max3_f32 v33, |v52|, |v53|, v33\ns_cbranch_vccz .La1017\ns_add_u32 m0, s26, 114688\ns_nop 0\n"
      "global_load_lds_dwordx4 v28, s[6:7] offset:0\n"
      ".La1017:\nv_lshl_add_u32 v28, v200, 7, v4\ns_add_u32 m0, s53, 90112\nv_and_b32 v104, s55, v52\nglobal_load_lds_dwordx4 v22, s[6:7] offset:0\nv_lshl_add_u32 v22, v176, 7, v4\n"
      "v_xor_b32 v52, v52, v104\nglobal_load_lds_dwordx4 v23, s[44:45] offset:1024\nv_lshl_add_u32 v23, v182, 7, v5\nv_and_b32 v105, s55, v53\nglobal_load_lds_dwordx4 v24, s[46:47] offset:2048\n"
      "v_lshl_add_u32 v24, v184, 7, v4\nv_xor_b32 v53, v53, v105\nglobal_load_lds_dwordx4 v25, s[48:49] offset:3072\ns_add_u32 m0, s53, 94208\nv_lshl_add_u32 v25, v190, 7, v5\n"
      "global_load_lds_dwordx4 v26, s[6:7] offset:0\nv_lshl_add_u32 v26, v192, 7, v4\ns_waitcnt vmcnt(11) lgkmcnt(0)\ns_cbranch_vccnz .Lw1018\ns_waitcnt vmcnt(10)\n"
      ".Lw1018:\ns_barrier\ns_add_u32 m0, s54, 0\ns_nop 0\nglobal_load_lds_dwordx4 v236, s[50:51] offset:0\nds_read_b64 v[56:57], v30 offset:45056\n"
      "global_load_lds_dwordx4 v237, s[50:51] offset:1024\nglobal_load_lds_dwordx4 v238, s[50:51] offset:2048\nglobal_load_lds_dwordx4 v239, s[50:51] offset:3072\ns_add_u32 s50, s50, 212992\n"
      "s_addc_u32 s51, s51, 0\nv_max3_f16 v32, |v54|, |v55|, v32\nv_max3_f32 v33, |v54|, |v55|, v33\ns_add_u32 m0, s26, 20480\nv_cmp_eq_u32 s[62:63], 0, v240\n"
      "global_load_lds_dwordx4 v27, s[6:7] offset:0\nv_lshl_add_u32 v27, v199, 7, v4\nds_read2_b64 v[196:199], v8 offset0:117 offset1:118\nv_mov_b32 v253, 262144\nv_mov_b32 v248, 162048\n"
      "v_mov_b32 v241, 1\ns_cbranch_vccz .La1019\ns_add_u32 m0, s26, 24576\ns_nop 0\nglobal_load_lds_dwordx4 v28, s[6:7] offset:0\n"
      ".La1019:\nv_lshl_add_u32 v28, v201, 7, v4\nv_cndmask_b32 v253, v253, v248, s[62:63]\ns_add_u32 m0, s53, 0\nv_and_b32 v106, s55, v54\nglobal_load_lds_dwordx4 v22, s[6:7] offset:0\n"
      "v_lshl_add_u32 v22, v177, 7, v4\nv_xor_b32 v54, v54, v106\nv_and_b32 v107, s55, v55\nglobal_load_lds_dwordx4 v23, s[44:45] offset:1024\nv_lshl_add_u32 v23, v183, 7, v5\n"
      "ds_read2_b64 v[180:183], v6 offset0:117 offset1:118\nv_xor_b32 v55, v55, v107\nv_lshrrev_b32 v164, 4, v240\nglobal_load_lds_dwordx4 v24, s[46:47] offset:2048\n"
      "v_lshl_add_u32 v24, v185, 7, v4\nv_bfe_u32 v165, v240, 2, 2\nv_and_b32 v166, 3, v240\nglobal_load_lds_dwordx4 v25, s[48:49] offset:3072\nv_lshl_add_u32 v25, v191, 7, v5\n"
      "ds_read2_b64 v[188:191], v7 offset0:117 offset1:118\nv_mul_u32_u24 v166, 0x80008, v166\ns_add_u32 m0, s53, 4096\nv_add_u32 v244, v164, v165\nglobal_load_lds_dwordx4 v26, s[6:7] offset:0\n"
      "v_lshl_add_u32 v26, v193, 7, v4\nv_and_b32 v244, 3, v244\nv_add_u32 v168, 22, v165\ns_waitcnt vmcnt(11) lgkmcnt(0)\ns_cbranch_vccnz .Lw1020\ns_waitcnt vmcnt(10)\n"
      ".Lw1020:\ns_barrier\ns_add_u32 m0, s54, 45056\ns_nop 0\nglobal_load_lds_dwordx4 v236, s[50:51] offset:0\nds_read_b64 v[58:59], v31 offset:0\n"
      "global_load_lds_dwordx4 v237, s[50:51] offset:1024\nglobal_load_lds_dwordx4 v238, s[50:51] offset:2048\nglobal_load_lds_dwordx4 v239, s[50:51] offset:3072\ns_add_u32 s50, s50, 212992\n"
      "s_addc_u32 s51, s51, 0\ns_cmp_eq_u32 s27, 16\ns_cselect_b64 s[50:51], s[10:11], s[50:51]\nv_max3_f16 v32, |v56|, |v57|, v32\nv_max3_f32 v33, |v56|, |v57|, v33\ns_add_u32 m0, s26, 65536\n"
      "v_cmp_gt_u32 s[62:63], 2, v165\nglobal_load_lds_dwordx4 v27, s[6:7] offset:0\nv_lshl_add_u32 v27, v196, 7, v4\nv_and_b32 v165, 60, v240\ns_mov_b32 s42, 0\n"
      "v_cndmask_b32 v168, v168, v244, s[62:63]\ns_cbranch_vccz .La1021\ns_add_u32 m0, s26, 69632\ns_nop 0\nglobal_load_lds_dwordx4 v28, s[6:7] offset:0\n"
      ".La1021:\nv_lshl_add_u32 v28, v202, 7, v4\nv_lshl_or_b32 v244, v244, 16, v168\nv_lshl_add_u32 v244, v244, 5, v166\ns_add_u32 m0, s53, 45056\nv_lshl_add_u32 v252, v240, 4, s42\n"
      "global_load_lds_dwordx4 v22, s[6:7] offset:0\nv_lshl_add_u32 v22, v178, 7, v4\nv_and_b32 v108, s55, v56\nv_xor_b32 v56, v56, v108\nv_and_b32 v109, s55, v57\n"
      "global_load_lds_dwordx4 v23, s[44:45] offset:1024\nv_lshl_add_u32 v23, v180, 7, v5\nv_xor_b32 v57, v57, v109\ns_mov_b32 s42, 0x76325410\ns_mov_b32 s43, 0x21005410\n"
      "global_load_lds_dwordx4 v24, s[46:47] offset:2048\nv_lshl_add_u32 v24, v186, 7, v4\nv_lshrrev_b64 v[168:169], v165, s[42:43]\nv_and_b32 v168, 7, v168\nv_lshl_add_u32 v168, v164, 3, v168\n"
      "global_load_lds_dwordx4 v25, s[48:49] offset:3072\nv_lshl_add_u32 v25, v188, 7, v5\ns_mov_b32 s42, 0x54107632\ns_mov_b32 s43, 0x21107632\ns_add_u32 m0, s53, 49152\n"
      "v_lshrrev_b64 v[170:171], v165, s[42:43]\nglobal_load_lds_dwordx4 v26, s[6:7] offset:0\nv_lshl_add_u32 v26, v194, 7, v4\nv_and_b32 v170, 7, v170\nv_lshl_add_u32 v170, v164, 3, v170\n"
      "s_waitcnt vmcnt(11) lgkmcnt(0)\ns_cbranch_vccnz .Lw1022\ns_waitcnt vmcnt(10)\n"
      ".Lw1022:\ns_barrier\ns_add_u32 m0, s54, 90112\ns_nop 0\nglobal_load_lds_dwordx4 v236, s[50:51] offset:0\nds_read_b64 v[60:61], v30 offset:0\n"
      "global_load_lds_dwordx4 v237, s[50:51] offset:1024\nglobal_load_lds_dwordx4 v238, s[50:51] offset:2048\nglobal_load_lds_dwordx4 v239, s[50:51] offset:3072\ns_add_u32 s50, s50, 212992\n"
      "s_addc_u32 s51, s51, 0\nv_max3_f16 v32, |v58|, |v59|, v32\ns_add_u32 m0, s26, 110592\nv_max3_f32 v33, |v58|, |v59|, v33\nglobal_load_lds_dwordx4 v27, s[6:7] offset:0\n"
      "v_lshl_add_u32 v27, v197, 7, v4\nv_lshl_or_b32 v245, v170, 16, v168\nv_lshl_add_u32 v245, v245, 5, v166\ns_cbranch_vccz .La1023\ns_add_u32 m0, s26, 114688\ns_nop 0\n"
      "global_load_lds_dwordx4 v28, s[6:7] offset:0\n"
      ".La1023:\nv_lshl_add_u32 v28, v203, 7, v4\nds_read2_b64 v[200:203], v9 offset0:6 offset1:7\nv_and_b32 v165, 1, v240\ns_add_u32 m0, s53, 90112\nv_mul_u32_u24 v165, 0xaaaa, v165\n"
      "global_load_lds_dwordx4 v22, s[6:7] offset:0\nv_lshl_add_u32 v22, v179, 7, v4\nds_read2_b64 v[176:179], v6 offset0:6 offset1:7\nv_add_u32 v242, 0x4444, v165\ns_lshl_b32 s42, s36, 4\n"
      "global_load_lds_dwordx4 v23, s[44:45] offset:1024\nv_lshl_add_u32 v23, v181, 7, v5\ns_add_u32 s42, s42, 160448\nv_lshl_add_u32 v243, v164, 2, s42\n"
      "global_load_lds_dwordx4 v24, s[46:47] offset:2048\nv_lshl_add_u32 v24, v187, 7, v4\nds_read2_b64 v[184:187], v7 offset0:6 offset1:7\nv_and_b32 v110, s55, v58\nv_xor_b32 v58, v58, v110\n"
      "global_load_lds_dwordx4 v25, s[48:49] offset:3072\nv_lshl_add_u32 v25, v189, 7, v5\ns_add_u32 m0, s53, 94208\nv_and_b32 v111, s55, v59\nglobal_load_lds_dwordx4 v26, s[6:7] offset:0\n"
      "v_lshl_add_u32 v26, v195, 7, v4\nds_read2_b64 v[192:195], v8 offset0:6 offset1:7\nv_xor_b32 v59, v59, v111\ns_waitcnt vmcnt(11) lgkmcnt(0)\ns_cbranch_vccnz .Lw1024\ns_waitcnt vmcnt(10)\n"
      ".Lw1024:\ns_barrier\ns_add_u32 m0, s54, 0\ns_nop 0\nglobal_load_lds_dwordx4 v236, s[50:51] offset:0\nds_read_b64 v[62:63], v30 offset:45056\n"
      "global_load_lds_dwordx4 v237, s[50:51] offset:1024\nglobal_load_lds_dwordx4 v238, s[50:51] offset:2048\nglobal_load_lds_dwordx4 v239, s[50:51] offset:3072\ns_add_u32 s50, s50, 212992\n"
      "s_addc_u32 s51, s51, 0\ns_cmp_eq_u32 s27, 14\ns_cselect_b64 s[50:51], s[10:11], s[50:51]\nv_max3_f16 v32, |v60|, |v61|, v32\nv_max3_f32 v33, |v60|, |v61|, v33\ns_add_u32 m0, s26, 20480\n"
      "v_and_b32 v164, 1, v164\nglobal_load_lds_dwordx4 v27, s[6:7] offset:0\nv_lshl_add_u32 v27, v198, 7, v4\nv_lshl_add_u32 v164, v164, 3, s24\ns_sub_u32 s42, s64, 1\nv_min_u32 v164, s42, v164\n"
      "s_cbranch_vccz .La1025\ns_add_u32 m0, s26, 24576\ns_nop 0\nglobal_load_lds_dwordx4 v28, s[6:7] offset:0\n"
      ".La1025:\nv_lshl_add_u32 v28, v200, 7, v4\nv_lshlrev_b32 v164, 3, v164\nv_and_b32 v165, 15, v240\ns_add_u32 m0, s53, 0\nv_lshl_add_u32 v250, v164, 1, v165\n"
      "global_load_lds_dwordx4 v22, s[6:7] offset:0\nv_lshl_add_u32 v22, v176, 7, v4\ns_mov_b32 s42, 136192\nv_lshl_add_u32 v250, v250, 2, s42\nglobal_load_lds_dwordx4 v23, s[44:45] offset:1024\n"
      "v_lshl_add_u32 v23, v182, 7, v5\nv_min_u32 v165, 12, v165\ns_lshl_b32 s42, s30, 7\nglobal_load_lds_dwordx4 v24, s[46:47] offset:2048\nv_lshl_add_u32 v24, v184, 7, v4\n"
      "v_lshl_add_u32 v165, v165, 15, s42\nv_add_u32 v249, v165, v164\nglobal_load_lds_dwordx4 v25, s[48:49] offset:3072\nv_lshl_add_u32 v25, v190, 7, v5\nv_and_b32 v112, s55, v60\n"
      "s_add_u32 m0, s53, 4096\nv_xor_b32 v60, v60, v112\nglobal_load_lds_dwordx4 v26, s[6:7] offset:0\nv_lshl_add_u32 v26, v192, 7, v4\nv_and_b32 v113, s55, v61\nv_xor_b32 v61, v61, v113\n"
      "s_waitcnt vmcnt(11) lgkmcnt(0)\ns_cbranch_vccnz .Lw1026\ns_waitcnt vmcnt(10)\n"
      ".Lw1026:\ns_barrier\ns_add_u32 m0, s54, 45056\ns_nop 0\nglobal_load_lds_dwordx4 v236, s[50:51] offset:0\nds_read_b64 v[64:65], v31 offset:0\n"
      "global_load_lds_dwordx4 v237, s[50:51] offset:1024\nglobal_load_lds_dwordx4 v238, s[50:51] offset:2048\nglobal_load_lds_dwordx4 v239, s[50:51] offset:3072\ns_add_u32 s50, s50, 212992\n"
      "s_addc_u32 s51, s51, 0\nv_max3_f16 v32, |v62|, |v63|, v32\ns_add_u32 m0, s26, 65536\nv_max3_f32 v33, |v62|, |v63|, v33\nglobal_load_lds_dwordx4 v27, s[6:7] offset:0\n"
      "v_lshl_add_u32 v27, v199, 7, v4\nds_read2_b64 v[196:199], v8 offset0:119 offset1:120\ns_mul_i32 s65, s36, 22272\ns_add_u32 s65, s65, 45056\ns_cbranch_vccz .La1027\ns_add_u32 m0, s26, 69632\n"
      "s_nop 0\nglobal_load_lds_dwordx4 v28, s[6:7] offset:0\n"
      ".La1027:\nv_lshl_add_u32 v28, v201, 7, v4\ns_add_u32 s68, s64, 8\ns_min_u32 s68, s68, 21\ns_add_u32 m0, s53, 45056\ns_nop 0\nglobal_load_lds_dwordx4 v22, s[6:7] offset:0\n"
      "v_lshl_add_u32 v22, v177, 7, v4\ns_lshl_b32 s38, s38, 16\nglobal_load_lds_dwordx4 v23, s[44:45] offset:1024\nv_lshl_add_u32 v23, v183, 7, v5\n"
      "ds_read2_b64 v[180:183], v6 offset0:119 offset1:120\nv_and_b32 v114, s55, v62\nglobal_load_lds_dwordx4 v24, s[46:47] offset:2048\nv_lshl_add_u32 v24, v185, 7, v4\nv_xor_b32 v62, v62, v114\n"
      "global_load_lds_dwordx4 v25, s[48:49] offset:3072\nv_lshl_add_u32 v25, v191, 7, v5\nds_read2_b64 v[188:191], v7 offset0:119 offset1:120\ns_add_u32 m0, s53, 49152\nv_and_b32 v115, s55, v63\n"
      "global_load_lds_dwordx4 v26, s[6:7] offset:0\nv_lshl_add_u32 v26, v193, 7, v4\nv_xor_b32 v63, v63, v115\ns_waitcnt vmcnt(11) lgkmcnt(0)\ns_cbranch_vccnz .Lw1028\ns_waitcnt vmcnt(10)\n"
      ".Lw1028:\ns_barrier\ns_add_u32 m0, s54, 90112\ns_nop 0\nglobal_load_lds_dwordx4 v236, s[50:51] offset:0\nds_read_b64 v[66:67], v30 offset:0\n"
      "global_load_lds_dwordx4 v237, s[50:51] offset:1024\nglobal_load_lds_dwordx4 v238, s[50:51] offset:2048\nglobal_load_lds_dwordx4 v239, s[50:51] offset:3072\ns_add_u32 s50, s50, 212992\n"
      "s_addc_u32 s51, s51, 0\ns_cmp_eq_u32 s27, 12\ns_cselect_b64 s[50:51], s[10:11], s[50:51]\nv_max3_f16 v32, |v64|, |v65|, v32\nv_max3_f32 v33, |v64|, |v65|, v33\ns_lshr_b32 s42, s24, 2\n"
      "s_add_u32 m0, s26, 110592\ns_nop 0\nglobal_load_lds_dwordx4 v27, s[6:7] offset:0\nv_lshl_add_u32 v27, v196, 7, v4\ns_mul_i32 s43, s42, 3\ns_xor_b32 s43, s43, s36\ns_cbranch_vccz .La1029\n"
      "s_add_u32 m0, s26, 114688\ns_nop 0\nglobal_load_lds_dwordx4 v28, s[6:7] offset:0\n"
      ".La1029:\nv_lshl_add_u32 v28, v202, 7, v4\ns_add_u32 s42, s42, s64\ns_add_u32 s42, s42, 9\ns_add_u32 m0, s53, 90112\ns_nop 0\nglobal_load_lds_dwordx4 v22, s[6:7] offset:0\n"
      "v_lshl_add_u32 v22, v178, 7, v4\ns_and_b32 s42, s42, 1\ns_lshl1_add_u32 s67, s43, s42\nglobal_load_lds_dwordx4 v23, s[44:45] offset:1024\nv_lshl_add_u32 v23, v180, 7, v5\n"
      "s_add_u32 s66, s64, s67\ns_cmp_lt_u32 s66, s68\ns_cselect_b32 s66, s66, 127\nglobal_load_lds_dwordx4 v24, s[46:47] offset:2048\nv_lshl_add_u32 v24, v186, 7, v4\ns_sub_u32 s66, s66, 8\n"
      "s_max_i32 s66, s66, 0\nglobal_load_lds_dwordx4 v25, s[48:49] offset:3072\nv_lshl_add_u32 v25, v188, 7, v5\nv_and_b32 v116, s55, v64\ns_add_u32 m0, s53, 94208\nv_xor_b32 v64, v64, v116\n"
      "global_load_lds_dwordx4 v26, s[6:7] offset:0\nv_lshl_add_u32 v26, v194, 7, v4\nv_and_b32 v117, s55, v65\nv_xor_b32 v65, v65, v117\ns_waitcnt vmcnt(11) lgkmcnt(0)\ns_cbranch_vccnz .Lw1030\n"
      "s_waitcnt vmcnt(10)\n"
      ".Lw1030:\ns_barrier\ns_add_u32 m0, s54, 0\ns_nop 0\nglobal_load_lds_dwordx4 v236, s[50:51] offset:0\nds_read_b64 v[68:69], v30 offset:45056\n"
      "global_load_lds_dwordx4 v237, s[50:51] offset:1024\nglobal_load_lds_dwordx4 v238, s[50:51] offset:2048\nglobal_load_lds_dwordx4 v239, s[50:51] offset:3072\ns_add_u32 s50, s50, 212992\n"
      "s_addc_u32 s51, s51, 0\ns_add_u32 m0, s26, 20480\nv_max3_f16 v32, |v66|, |v67|, v32\nglobal_load_lds_dwordx4 v27, s[6:7] offset:0\nv_lshl_add_u32 v27, v197, 7, v4\n"
      "v_max3_f32 v33, |v66|, |v67|, v33\ns_cbranch_vccz .La1031\ns_add_u32 m0, s26, 24576\ns_nop 0\nglobal_load_lds_dwordx4 v28, s[6:7] offset:0\n"
      ".La1031:\nv_lshl_add_u32 v28, v203, 7, v4\nds_read2_b64 v[200:203], v9 offset0:8 offset1:9\ns_getpc_b64 s[34:35]\n"
      ".Lk1005:\ns_add_u32 m0, s53, 0\ns_nop 0\nglobal_load_lds_dwordx4 v22, s[6:7] offset:0\nv_lshl_add_u32 v22, v179, 7, v4\nds_read2_b64 v[176:179], v6 offset0:8 offset1:9\n"
      "s_add_u32 s34, s34, .Lt_p-.Lk1005\ns_addc_u32 s35, s35, 0\nglobal_load_lds_dwordx4 v23, s[44:45] offset:1024\nv_lshl_add_u32 v23, v181, 7, v5\nv_and_b32 v118, s55, v66\n"
      "global_load_lds_dwordx4 v24, s[46:47] offset:2048\nv_lshl_add_u32 v24, v187, 7, v4\nds_read2_b64 v[184:187], v7 offset0:8 offset1:9\nv_xor_b32 v66, v66, v118\n"
      "global_load_lds_dwordx4 v25, s[48:49] offset:3072\nv_lshl_add_u32 v25, v189, 7, v5\ns_add_u32 m0, s53, 4096\nv_and_b32 v119, s55, v67\nglobal_load_lds_dwordx4 v26, s[6:7] offset:0\n"
      "v_lshl_add_u32 v26, v195, 7, v4\nds_read2_b64 v[192:195], v8 offset0:8 offset1:9\nv_xor_b32 v67, v67, v119\ns_waitcnt vmcnt(11) lgkmcnt(0)\ns_cbranch_vccnz .Lw1032\ns_waitcnt vmcnt(10)\n"
      ".Lw1032:\ns_barrier\ns_add_u32 m0, s54, 45056\ns_nop 0\nglobal_load_lds_dwordx4 v236, s[50:51] offset:0\nds_read_b64 v[70:71], v31 offset:0\n"
      "global_load_lds_dwordx4 v237, s[50:51] offset:1024\nglobal_load_lds_dwordx4 v238, s[50:51] offset:2048\nglobal_load_lds_dwordx4 v239, s[50:51] offset:3072\ns_add_u32 s50, s50, 212992\n"
      "s_addc_u32 s51, s51, 0\ns_cmp_eq_u32 s27, 10\ns_cselect_b64 s[50:51], s[10:11], s[50:51]\nv_max3_f16 v32, |v68|, |v69|, v32\ns_add_u32 m0, s26, 65536\nv_max3_f32 v33, |v68|, |v69|, v33\n"
      "global_load_lds_dwordx4 v27, s[6:7] offset:0\nv_lshl_add_u32 v27, v198, 7, v4\ns_lshr_b32 s42, s2, 3\ns_lshl1_add_u32 s42, s2, s42\ns_cbranch_vccz .La1033\ns_add_u32 m0, s26, 69632\n"
      "s_nop 0\nglobal_load_lds_dwordx4 v28, s[6:7] offset:0\n"
      ".La1033:\nv_lshl_add_u32 v28, v200, 7, v4\ns_and_b32 s69, s42, 15\ns_lshl_b32 s43, s69, 7\ns_add_u32 m0, s53, 45056\ns_nop 0\nglobal_load_lds_dwordx4 v22, s[6:7] offset:0\n"
      "v_lshl_add_u32 v22, v176, 7, v4\ns_add_u32 s43, s43, 32768\ns_add_u32 s8, s20, s43\ns_addc_u32 s9, s21, 0\nglobal_load_lds_dwordx4 v23, s[44:45] offset:1024\n"
      "v_lshl_add_u32 v23, v182, 7, v5\ns_add_u32 s12, s20, 65536\ns_addc_u32 s13, s21, 0\ns_add_u32 s16, s20, 2097152\ns_addc_u32 s17, s21, 0\nglobal_load_lds_dwordx4 v24, s[46:47] offset:2048\n"
      "v_lshl_add_u32 v24, v184, 7, v4\nv_and_b32 v120, s55, v68\nv_xor_b32 v68, v68, v120\nglobal_load_lds_dwordx4 v25, s[48:49] offset:3072\nv_lshl_add_u32 v25, v190, 7, v5\n"
      "s_add_u32 m0, s53, 49152\nv_and_b32 v121, s55, v69\nglobal_load_lds_dwordx4 v26, s[6:7] offset:0\nv_lshl_add_u32 v26, v192, 7, v4\nv_xor_b32 v69, v69, v121\ns_waitcnt vmcnt(11) lgkmcnt(0)\n"
      "s_cbranch_vccnz .Lw1034\ns_waitcnt vmcnt(10)\n"
      ".Lw1034:\ns_barrier\ns_add_u32 m0, s54, 90112\ns_nop 0\nglobal_load_lds_dwordx4 v236, s[50:51] offset:0\nds_read_b64 v[72:73], v30 offset:0\n"
      "global_load_lds_dwordx4 v237, s[50:51] offset:1024\nglobal_load_lds_dwordx4 v238, s[50:51] offset:2048\nglobal_load_lds_dwordx4 v239, s[50:51] offset:3072\ns_add_u32 s50, s50, 212992\n"
      "s_addc_u32 s51, s51, 0\nv_max3_f16 v32, |v70|, |v71|, v32\ns_add_u32 m0, s26, 110592\nv_max3_f32 v33, |v70|, |v71|, v33\nglobal_load_lds_dwordx4 v27, s[6:7] offset:0\n"
      "v_lshl_add_u32 v27, v199, 7, v4\nds_read2_b64 v[196:199], v8 offset0:121 offset1:122\ns_lshl_b32 s42, s69, 4\ns_max_u32 s42, s42, 24\ns_cbranch_vccz .La1035\ns_add_u32 m0, s26, 114688\n"
      "s_nop 0\nglobal_load_lds_dwordx4 v28, s[6:7] offset:0\n"
      ".La1035:\nv_lshl_add_u32 v28, v201, 7, v4\ns_sub_u32 s42, s42, 24\ns_min_u32 s69, s42, 192\ns_add_u32 m0, s53, 90112\ns_nop 0\nglobal_load_lds_dwordx4 v22, s[6:7] offset:0\n"
      "v_lshl_add_u32 v22, v177, 7, v4\ns_lshl_b32 s42, s69, 3\ns_add_u32 s42, s42, 24576\nglobal_load_lds_dwordx4 v23, s[44:45] offset:1024\nv_lshl_add_u32 v23, v183, 7, v5\n"
      "ds_read2_b64 v[180:183], v6 offset0:121 offset1:122\ns_add_u32 s14, s20, s42\ns_addc_u32 s15, s21, 0\nv_and_b32 v122, s55, v70\nglobal_load_lds_dwordx4 v24, s[46:47] offset:2048\n"
      "v_lshl_add_u32 v24, v185, 7, v4\nv_xor_b32 v70, v70, v122\nglobal_load_lds_dwordx4 v25, s[48:49] offset:3072\nv_lshl_add_u32 v25, v191, 7, v5\n"
      "ds_read2_b64 v[188:191], v7 offset0:121 offset1:122\ns_add_u32 m0, s53, 94208\nv_and_b32 v123, s55, v71\nglobal_load_lds_dwordx4 v26, s[6:7] offset:0\nv_lshl_add_u32 v26, v193, 7, v4\n"
      "v_xor_b32 v71, v71, v123\ns_waitcnt vmcnt(11) lgkmcnt(0)\ns_cbranch_vccnz .Lw1036\ns_waitcnt vmcnt(10)\n"
      ".Lw1036:\ns_barrier\ns_add_u32 m0, s54, 0\ns_nop 0\nglobal_load_lds_dwordx4 v236, s[50:51] offset:0\nds_read_b64 v[74:75], v30 offset:45056\n"
      "global_load_lds_dwordx4 v237, s[50:51] offset:1024\nglobal_load_lds_dwordx4 v238, s[50:51] offset:2048\nglobal_load_lds_dwordx4 v239, s[50:51] offset:3072\ns_add_u32 s50, s50, 212992\n"
      "s_addc_u32 s51, s51, 0\ns_cmp_eq_u32 s27, 8\ns_cselect_b64 s[50:51], s[10:11], s[50:51]\nv_max3_f16 v32, |v72|, |v73|, v32\ns_add_u32 m0, s26, 20480\nv_max3_f32 v33, |v72|, |v73|, v33\n"
      "global_load_lds_dwordx4 v27, s[6:7] offset:0\nv_lshl_add_u32 v27, v196, 7, v4\ns_lshl_b32 s42, s2, 3\ns_add_u32 s42, s42, 4096\ns_cbranch_vccz .La1037\ns_add_u32 m0, s26, 24576\ns_nop 0\n"
      "global_load_lds_dwordx4 v28, s[6:7] offset:0\n"
      ".La1037:\nv_lshl_add_u32 v28, v202, 7, v4\ns_add_u32 s58, s20, s42\ns_addc_u32 s59, s21, 0\ns_add_u32 m0, s53, 0\nv_and_b32 v38, 7, v240\nglobal_load_lds_dwordx4 v22, s[6:7] offset:0\n"
      "v_lshl_add_u32 v22, v178, 7, v4\nv_lshlrev_b32 v38, 11, v38\ns_xor_b32 s40, s37, 0xa5c30f96\nglobal_load_lds_dwordx4 v23, s[44:45] offset:1024\nv_lshl_add_u32 v23, v180, 7, v5\n"
      "s_mul_i32 s41, s37, 0x9E3779B1\ns_lshr_b32 s41, s41, 24\nglobal_load_lds_dwordx4 v24, s[46:47] offset:2048\nv_lshl_add_u32 v24, v186, 7, v4\nv_and_b32 v124, s55, v72\n"
      "v_xor_b32 v72, v72, v124\nglobal_load_lds_dwordx4 v25, s[48:49] offset:3072\nv_lshl_add_u32 v25, v188, 7, v5\ns_add_u32 m0, s53, 4096\nv_and_b32 v125, s55, v73\n"
      "global_load_lds_dwordx4 v26, s[6:7] offset:0\nv_lshl_add_u32 v26, v194, 7, v4\nv_xor_b32 v73, v73, v125\ns_waitcnt vmcnt(11) lgkmcnt(0)\ns_cbranch_vccnz .Lw1038\ns_waitcnt vmcnt(10)\n"
      ".Lw1038:\ns_barrier\ns_add_u32 m0, s54, 45056\ns_nop 0\nglobal_load_lds_dwordx4 v236, s[50:51] offset:0\nds_read_b64 v[76:77], v31 offset:0\n"
      "global_load_lds_dwordx4 v237, s[50:51] offset:1024\nglobal_load_lds_dwordx4 v238, s[50:51] offset:2048\nglobal_load_lds_dwordx4 v239, s[50:51] offset:3072\ns_add_u32 s50, s50, 212992\n"
      "s_addc_u32 s51, s51, 0\nv_max3_f16 v32, |v74|, |v75|, v32\ns_add_u32 m0, s26, 65536\nv_max3_f32 v33, |v74|, |v75|, v33\nglobal_load_lds_dwordx4 v27, s[6:7] offset:0\n"
      "v_lshl_add_u32 v27, v197, 7, v4\nv_lshrrev_b32 v164, 2, v240\nv_and_b32 v148, 3, v240\ns_cbranch_vccz .La1039\ns_add_u32 m0, s26, 69632\ns_nop 0\n"
      "global_load_lds_dwordx4 v28, s[6:7] offset:0\n"
      ".La1039:\nv_lshl_add_u32 v28, v203, 7, v4\nds_read2_b64 v[200:203], v9 offset0:10 offset1:11\nv_lshl_add_u32 v148, v148, 3, s65\ns_add_u32 m0, s53, 45056\nv_mul_u32_u24 v164, 928, v164\n"
      "global_load_lds_dwordx4 v22, s[6:7] offset:0\nv_lshl_add_u32 v22, v179, 7, v4\nds_read2_b64 v[176:179], v6 offset0:10 offset1:11\nv_add_u32 v149, s65, v164\nv_add_u32 v148, v148, v164\n"
      "global_load_lds_dwordx4 v23, s[44:45] offset:1024\nv_lshl_add_u32 v23, v181, 7, v5\ns_add_u32 s42, s65, 7424\nv_add_u32 v163, s42, v252\nglobal_load_lds_dwordx4 v24, s[46:47] offset:2048\n"
      "v_lshl_add_u32 v24, v187, 7, v4\nds_read2_b64 v[184:187], v7 offset0:10 offset1:11\nv_and_b32 v126, s55, v74\nv_xor_b32 v74, v74, v126\nglobal_load_lds_dwordx4 v25, s[48:49] offset:3072\n"
      "v_lshl_add_u32 v25, v189, 7, v5\ns_add_u32 m0, s53, 49152\nv_and_b32 v127, s55, v75\nglobal_load_lds_dwordx4 v26, s[6:7] offset:0\nv_lshl_add_u32 v26, v195, 7, v4\n"
      "ds_read2_b64 v[192:195], v8 offset0:10 offset1:11\nv_xor_b32 v75, v75, v127\ns_waitcnt vmcnt(11) lgkmcnt(0)\ns_cbranch_vccnz .Lw1040\ns_waitcnt vmcnt(10)\n"
      ".Lw1040:\ns_barrier\ns_add_u32 m0, s54, 90112\ns_nop 0\nglobal_load_lds_dwordx4 v236, s[50:51] offset:0\nds_read_b64 v[78:79], v30 offset:0\n"
      "global_load_lds_dwordx4 v237, s[50:51] offset:1024\nglobal_load_lds_dwordx4 v238, s[50:51] offset:2048\nglobal_load_lds_dwordx4 v239, s[50:51] offset:3072\ns_add_u32 s50, s50, 212992\n"
      "s_addc_u32 s51, s51, 0\ns_cmp_eq_u32 s27, 6\ns_cselect_b64 s[50:51], s[10:11], s[50:51]\nv_max3_f16 v32, |v76|, |v77|, v32\ns_add_u32 m0, s26, 110592\nv_max3_f32 v33, |v76|, |v77|, v33\n"
      "global_load_lds_dwordx4 v27, s[6:7] offset:0\nv_lshl_add_u32 v27, v198, 7, v4\ns_mul_i32 s42, s36, 10752\ns_add_u32 s56, s42, 0\ns_cbranch_vccz .La1041\ns_add_u32 m0, s26, 114688\ns_nop 0\n"
      "global_load_lds_dwordx4 v28, s[6:7] offset:0\n"
      ".La1041:\nv_lshl_add_u32 v28, v200, 7, v4\ns_add_u32 s42, s42, 1048576\ns_add_u32 m0, s53, 90112\ns_nop 0\nglobal_load_lds_dwordx4 v22, s[6:7] offset:0\nv_lshl_add_u32 v22, v176, 7, v4\n"
      "s_add_u32 s60, s20, s42\ns_addc_u32 s61, s21, 0\nglobal_load_lds_dwordx4 v23, s[44:45] offset:1024\nv_lshl_add_u32 v23, v182, 7, v5\nv_and_b32 v128, s55, v76\n"
      "global_load_lds_dwordx4 v24, s[46:47] offset:2048\nv_lshl_add_u32 v24, v184, 7, v4\nv_xor_b32 v76, v76, v128\nglobal_load_lds_dwordx4 v25, s[48:49] offset:3072\n"
      "v_lshl_add_u32 v25, v190, 7, v5\ns_add_u32 m0, s53, 94208\nv_and_b32 v129, s55, v77\nglobal_load_lds_dwordx4 v26, s[6:7] offset:0\nv_lshl_add_u32 v26, v192, 7, v4\nv_xor_b32 v77, v77, v129\n"
      "s_waitcnt vmcnt(11) lgkmcnt(0)\ns_cbranch_vccnz .Lw1042\ns_waitcnt vmcnt(10)\n"
      ".Lw1042:\ns_barrier\ns_add_u32 m0, s54, 0\ns_nop 0\nglobal_load_lds_dwordx4 v236, s[50:51] offset:0\nds_read_b64 v[80:81], v30 offset:45056\n"
      "global_load_lds_dwordx4 v237, s[50:51] offset:1024\nglobal_load_lds_dwordx4 v238, s[50:51] offset:2048\nglobal_load_lds_dwordx4 v239, s[50:51] offset:3072\ns_add_u32 s50, s50, 212992\n"
      "s_addc_u32 s51, s51, 0\nv_max3_f16 v32, |v78|, |v79|, v32\nv_max3_f32 v33, |v78|, |v79|, v33\ns_add_u32 s42, s27, 0\ns_add_u32 m0, s26, 20480\ns_nop 0\n"
      "global_load_lds_dwordx4 v27, s[6:7] offset:0\nv_lshl_add_u32 v27, v199, 7, v4\nds_read2_b64 v[196:199], v8 offset0:123 offset1:124\ns_sub_u32 s43, s42, 26\ns_min_u32 s42, s42, s43\n"
      "v_lshl_add_u32 v150, s42, 5, v148\ns_cbranch_vccz .La1043\ns_add_u32 m0, s26, 24576\ns_nop 0\nglobal_load_lds_dwordx4 v28, s[6:7] offset:0\n"
      ".La1043:\nv_lshl_add_u32 v28, v201, 7, v4\ns_add_u32 s42, s27, 2\ns_sub_u32 s43, s42, 26\ns_add_u32 m0, s53, 0\ns_nop 0\nglobal_load_lds_dwordx4 v22, s[6:7] offset:0\n"
      "v_lshl_add_u32 v22, v177, 7, v4\ns_min_u32 s42, s42, s43\nv_lshl_add_u32 v151, s42, 5, v148\nglobal_load_lds_dwordx4 v23, s[44:45] offset:1024\nv_lshl_add_u32 v23, v183, 7, v5\n"
      "ds_read2_b64 v[180:183], v6 offset0:123 offset1:124\ns_add_u32 s42, s27, 4\ns_sub_u32 s43, s42, 26\nglobal_load_lds_dwordx4 v24, s[46:47] offset:2048\nv_lshl_add_u32 v24, v185, 7, v4\n"
      "s_min_u32 s42, s42, s43\nv_lshl_add_u32 v152, s42, 5, v148\nglobal_load_lds_dwordx4 v25, s[48:49] offset:3072\nv_lshl_add_u32 v25, v191, 7, v5\n"
      "ds_read2_b64 v[188:191], v7 offset0:123 offset1:124\nv_and_b32 v130, s55, v78\ns_add_u32 m0, s53, 4096\nv_xor_b32 v78, v78, v130\nglobal_load_lds_dwordx4 v26, s[6:7] offset:0\n"
      "v_lshl_add_u32 v26, v193, 7, v4\nv_and_b32 v131, s55, v79\nv_xor_b32 v79, v79, v131\ns_waitcnt vmcnt(11) lgkmcnt(0)\ns_cbranch_vccnz .Lw1044\ns_waitcnt vmcnt(10)\n"
      ".Lw1044:\ns_barrier\ns_add_u32 m0, s54, 45056\ns_nop 0\nglobal_load_lds_dwordx4 v236, s[50:51] offset:0\nds_read_b64 v[82:83], v31 offset:0\n"
      "global_load_lds_dwordx4 v237, s[50:51] offset:1024\nglobal_load_lds_dwordx4 v238, s[50:51] offset:2048\nglobal_load_lds_dwordx4 v239, s[50:51] offset:3072\ns_add_u32 s50, s50, 212992\n"
      "s_addc_u32 s51, s51, 0\ns_cmp_eq_u32 s27, 4\ns_cselect_b64 s[50:51], s[10:11], s[50:51]\nv_max3_f16 v32, |v80|, |v81|, v32\nv_max3_f32 v33, |v80|, |v81|, v33\ns_add_u32 s42, s27, 6\n"
      "s_add_u32 m0, s26, 65536\ns_nop 0\nglobal_load_lds_dwordx4 v27, s[6:7] offset:0\nv_lshl_add_u32 v27, v196, 7, v4\ns_sub_u32 s43, s42, 26\ns_min_u32 s42, s42, s43\n"
      "v_lshl_add_u32 v153, s42, 5, v148\ns_cbranch_vccz .La1045\ns_add_u32 m0, s26, 69632\ns_nop 0\nglobal_load_lds_dwordx4 v28, s[6:7] offset:0\n"
      ".La1045:\nv_lshl_add_u32 v28, v202, 7, v4\ns_add_u32 s42, s27, 8\ns_sub_u32 s43, s42, 26\ns_add_u32 m0, s53, 45056\ns_nop 0\nglobal_load_lds_dwordx4 v22, s[6:7] offset:0\n"
      "v_lshl_add_u32 v22, v178, 7, v4\ns_min_u32 s42, s42, s43\nv_lshl_add_u32 v154, s42, 5, v148\nglobal_load_lds_dwordx4 v23, s[44:45] offset:1024\nv_lshl_add_u32 v23, v180, 7, v5\n"
      "s_add_u32 s42, s27, 10\ns_sub_u32 s43, s42, 26\nglobal_load_lds_dwordx4 v24, s[46:47] offset:2048\nv_lshl_add_u32 v24, v186, 7, v4\ns_min_u32 s42, s42, s43\n"
      "v_lshl_add_u32 v155, s42, 5, v148\nglobal_load_lds_dwordx4 v25, s[48:49] offset:3072\nv_lshl_add_u32 v25, v188, 7, v5\nv_and_b32 v132, s55, v80\ns_add_u32 m0, s53, 49152\n"
      "v_xor_b32 v80, v80, v132\nglobal_load_lds_dwordx4 v26, s[6:7] offset:0\nv_lshl_add_u32 v26, v194, 7, v4\nv_and_b32 v133, s55, v81\nv_xor_b32 v81, v81, v133\ns_cmp_lg_u32 s24, 3\n"
      "s_cbranch_scc1 .Lo1046\nglobal_load_dwordx2 v[36:37], v38, s[58:59] sc1\n"
      ".Lo1046:\ns_waitcnt vmcnt(11) lgkmcnt(0)\ns_cbranch_vccnz .Lw1047\ns_waitcnt vmcnt(10)\n"
      ".Lw1047:\ns_barrier\ns_add_u32 m0, s54, 90112\ns_nop 0\nglobal_load_lds_dwordx4 v236, s[50:51] offset:0\nds_read_b64 v[84:85], v30 offset:0\n"
      "global_load_lds_dwordx4 v237, s[50:51] offset:1024\nglobal_load_lds_dwordx4 v238, s[50:51] offset:2048\nglobal_load_lds_dwordx4 v239, s[50:51] offset:3072\ns_add_u32 s50, s50, 212992\n"
      "s_addc_u32 s51, s51, 0\nv_max3_f16 v32, |v82|, |v83|, v32\nv_max3_f32 v33, |v82|, |v83|, v33\ns_add_u32 s42, s27, 12\ns_add_u32 m0, s26, 110592\ns_nop 0\n"
      "global_load_lds_dwordx4 v27, s[6:7] offset:0\nv_lshl_add_u32 v27, v197, 7, v4\ns_sub_u32 s43, s42, 26\ns_min_u32 s42, s42, s43\nv_lshl_add_u32 v156, s42, 5, v148\ns_cbranch_vccz .La1048\n"
      "s_add_u32 m0, s26, 114688\ns_nop 0\nglobal_load_lds_dwordx4 v28, s[6:7] offset:0\n"
      ".La1048:\nv_lshl_add_u32 v28, v203, 7, v4\nds_read2_b64 v[200:203], v9 offset0:12 offset1:13\ns_add_u32 s42, s27, 14\ns_sub_u32 s43, s42, 26\ns_add_u32 m0, s53, 90112\ns_nop 0\n"
      "global_load_lds_dwordx4 v22, s[6:7] offset:0\nv_lshl_add_u32 v22, v179, 7, v4\nds_read2_b64 v[176:179], v6 offset0:12 offset1:13\ns_min_u32 s42, s42, s43\nv_lshl_add_u32 v157, s42, 5, v148\n"
      "global_load_lds_dwordx4 v23, s[44:45] offset:1024\nv_lshl_add_u32 v23, v181, 7, v5\ns_add_u32 s42, s27, 16\ns_sub_u32 s43, s42, 26\nglobal_load_lds_dwordx4 v24, s[46:47] offset:2048\n"
      "v_lshl_add_u32 v24, v187, 7, v4\nds_read2_b64 v[184:187], v7 offset0:12 offset1:13\ns_min_u32 s42, s42, s43\nv_lshl_add_u32 v158, s42, 5, v148\n"
      "global_load_lds_dwordx4 v25, s[48:49] offset:3072\nv_lshl_add_u32 v25, v189, 7, v5\nv_and_b32 v134, s55, v82\ns_add_u32 m0, s53, 94208\nv_xor_b32 v82, v82, v134\n"
      "global_load_lds_dwordx4 v26, s[6:7] offset:0\nv_lshl_add_u32 v26, v195, 7, v4\nds_read2_b64 v[192:195], v8 offset0:12 offset1:13\nv_and_b32 v135, s55, v83\nv_xor_b32 v83, v83, v135\n"
      "s_waitcnt vmcnt(11) lgkmcnt(0)\ns_cbranch_vccnz .Lw1049\ns_waitcnt vmcnt(10)\n"
      ".Lw1049:\ns_barrier\ns_add_u32 m0, s54, 0\ns_nop 0\nglobal_load_lds_dwordx4 v236, s[50:51] offset:0\nds_read_b64 v[86:87], v30 offset:45056\n"
      "global_load_lds_dwordx4 v237, s[50:51] offset:1024\nglobal_load_lds_dwordx4 v238, s[50:51] offset:2048\nglobal_load_lds_dwordx4 v239, s[50:51] offset:3072\ns_add_u32 s50, s50, 212992\n"
      "s_addc_u32 s51, s51, 0\ns_cmp_eq_u32 s27, 2\ns_cselect_b64 s[50:51], s[10:11], s[50:51]\nv_max3_f16 v32, |v84|, |v85|, v32\nv_max3_f32 v33, |v84|, |v85|, v33\ns_add_u32 s42, s27, 18\n"
      "s_add_u32 m0, s26, 20480\ns_nop 0\nglobal_load_lds_dwordx4 v27, s[6:7] offset:0\nv_lshl_add_u32 v27, v198, 7, v4\ns_sub_u32 s43, s42, 26\ns_min_u32 s42, s42, s43\n"
      "v_lshl_add_u32 v159, s42, 5, v148\ns_cbranch_vccz .La1050\ns_add_u32 m0, s26, 24576\ns_nop 0\nglobal_load_lds_dwordx4 v28, s[6:7] offset:0\n"
      ".La1050:\nv_lshl_add_u32 v28, v200, 7, v4\ns_add_u32 s42, s27, 20\ns_sub_u32 s43, s42, 26\ns_add_u32 m0, s53, 0\ns_nop 0\nglobal_load_lds_dwordx4 v22, s[6:7] offset:0\n"
      "v_lshl_add_u32 v22, v176, 7, v4\ns_min_u32 s42, s42, s43\nv_lshl_add_u32 v160, s42, 5, v148\nglobal_load_lds_dwordx4 v23, s[44:45] offset:1024\nv_lshl_add_u32 v23, v182, 7, v5\n"
      "s_add_u32 s42, s27, 22\ns_sub_u32 s43, s42, 26\nglobal_load_lds_dwordx4 v24, s[46:47] offset:2048\nv_lshl_add_u32 v24, v184, 7, v4\ns_min_u32 s42, s42, s43\n"
      "v_lshl_add_u32 v161, s42, 5, v148\nglobal_load_lds_dwordx4 v25, s[48:49] offset:3072\nv_lshl_add_u32 v25, v190, 7, v5\nv_and_b32 v136, s55, v84\ns_add_u32 m0, s53, 4096\n"
      "v_xor_b32 v84, v84, v136\nglobal_load_lds_dwordx4 v26, s[6:7] offset:0\nv_lshl_add_u32 v26, v192, 7, v4\nv_and_b32 v137, s55, v85\nv_xor_b32 v85, v85, v137\ns_waitcnt vmcnt(11) lgkmcnt(0)\n"
      "s_cbranch_vccnz .Lw1051\ns_waitcnt vmcnt(10)\n"
      ".Lw1051:\ns_barrier\ns_add_u32 m0, s54, 45056\ns_nop 0\nglobal_load_lds_dwordx4 v236, s[50:51] offset:0\nds_read_b64 v[88:89], v31 offset:0\n"
      "global_load_lds_dwordx4 v237, s[50:51] offset:1024\nglobal_load_lds_dwordx4 v238, s[50:51] offset:2048\nglobal_load_lds_dwordx4 v239, s[50:51] offset:3072\ns_add_u32 s50, s50, 212992\n"
      "s_addc_u32 s51, s51, 0\nv_max3_f16 v32, |v86|, |v87|, v32\ns_add_u32 m0, s26, 65536\nv_max3_f32 v33, |v86|, |v87|, v33\nglobal_load_lds_dwordx4 v27, s[6:7] offset:0\n"
      "v_lshl_add_u32 v27, v199, 7, v4\ns_add_u32 s42, s27, 24\ns_sub_u32 s43, s42, 26\ns_cbranch_vccz .La1052\ns_add_u32 m0, s26, 69632\ns_nop 0\nglobal_load_lds_dwordx4 v28, s[6:7] offset:0\n"
      ".La1052:\nv_lshl_add_u32 v28, v201, 7, v4\ns_min_u32 s42, s42, s43\ns_add_u32 m0, s53, 45056\ns_nop 0\nglobal_load_lds_dwordx4 v22, s[6:7] offset:0\nv_lshl_add_u32 v22, v177, 7, v4\n"
      "v_lshl_add_u32 v162, s42, 5, v148\nglobal_load_lds_dwordx4 v23, s[44:45] offset:1024\nv_lshl_add_u32 v23, v183, 7, v5\nv_and_b32 v138, s55, v86\n"
      "global_load_lds_dwordx4 v24, s[46:47] offset:2048\nv_lshl_add_u32 v24, v185, 7, v4\nv_xor_b32 v86, v86, v138\nglobal_load_lds_dwordx4 v25, s[48:49] offset:3072\n"
      "v_lshl_add_u32 v25, v191, 7, v5\ns_add_u32 m0, s53, 49152\nv_and_b32 v139, s55, v87\nglobal_load_lds_dwordx4 v26, s[6:7] offset:0\nv_lshl_add_u32 v26, v193, 7, v4\nv_xor_b32 v87, v87, v139\n"
      "s_cmp_lg_u32 s24, 3\ns_cbranch_scc1 .Lo1055\ns_mov_b32 s42, 0\n"
      ".Lq1053:\nv_and_b32 v164, 0xff, v36\nv_xor_b32 v164, s41, v164\nv_xor_b32 v165, s40, v37\nv_or_b32 v164, v164, v165\nv_cmp_ne_u32 s[62:63], 0, v164\nv_lshrrev_b32 v164, 8, v36\n"
      "s_cmp_eq_u64 s[62:63], 0\ns_cbranch_scc1 .Lr1054\ns_sleep 1\nglobal_load_dwordx2 v[36:37], v38, s[58:59] sc1\ns_add_u32 s42, s42, 1\ns_bitcmp0_b32 s42, 22\ns_waitcnt vmcnt(0)\n"
      "s_cbranch_scc1 .Lq1053\n"
      ".Lr1054:\nds_write_b32 v253, v164 offset:24\n"
      ".Lo1055:\ns_waitcnt vmcnt(11) lgkmcnt(0)\ns_cbranch_vccnz .Lw1056\ns_waitcnt vmcnt(10)\n"
      ".Lw1056:\ns_barrier\ns_add_u32 m0, s54, 90112\ns_nop 0\nglobal_load_lds_dwordx4 v236, s[50:51] offset:0\nds_read_b64 v[90:91], v30 offset:0\n"
      "global_load_lds_dwordx4 v237, s[50:51] offset:1024\nglobal_load_lds_dwordx4 v238, s[50:51] offset:2048\nglobal_load_lds_dwordx4 v239, s[50:51] offset:3072\n"
      "v_max3_f16 v32, |v88|, |v89|, v32\nv_max3_f32 v33, |v88|, |v89|, v33\nv_and_b32 v140, s55, v88\ns_add_u32 m0, s26, 110592\nv_xor_b32 v88, v88, v140\n"
      "global_load_lds_dwordx4 v27, s[6:7] offset:0\nv_and_b32 v141, s55, v89\nv_xor_b32 v89, v89, v141\ns_lshl_b32 s42, s24, 6\nv_add_u32 v10, s42, v240\ns_cbranch_vccz .La1057\n"
      "s_add_u32 m0, s26, 114688\ns_nop 0\nglobal_load_lds_dwordx4 v28, s[6:7] offset:0\n"
      ".La1057:\nv_min_u32 v10, 83, v10\nv_and_b32 v11, 3, v10\nv_lshrrev_b32 v12, 2, v10\ns_add_u32 m0, s53, 90112\nv_mul_u32_u24 v11, 22272, v11\nglobal_load_lds_dwordx4 v22, s[6:7] offset:0\n"
      "v_mul_u32_u24 v13, 928, v12\ns_mov_b32 s42, 45920\nv_add3_u32 v11, v11, v13, s42\nglobal_load_lds_dwordx4 v23, s[44:45] offset:1024\ns_mov_b32 s42, 136192\nv_lshl_add_u32 v13, v10, 4, s42\n"
      "s_mov_b32 s42, 160448\nglobal_load_lds_dwordx4 v24, s[46:47] offset:2048\nv_lshl_add_u32 v10, v10, 4, s42\nv_min_u32 v14, 1, v12\nv_lshl_add_u32 v10, v14, 6, v10\n"
      "global_load_lds_dwordx4 v25, s[48:49] offset:3072\ns_bfm_b32 s42, s64, 0\nv_lshrrev_b32 v15, v12, s42\ns_add_u32 m0, s53, 94208\nv_subrev_u32 v14, s64, v12\n"
      "global_load_lds_dwordx4 v26, s[6:7] offset:0\nv_lshl_add_u32 v12, v240, 1, v248\nds_read_u16 v251, v12 offset:64\nds_read_b32 v9, v248 offset:24\ns_waitcnt vmcnt(11) lgkmcnt(0)\n"
      "s_cbranch_vccnz .Lw1058\ns_waitcnt vmcnt(10)\n"
      ".Lw1058:\ns_barrier\ns_add_u32 m0, s56, 0\ns_nop 0\nglobal_load_lds_dwordx4 v252, s[60:61] offset:0 sc1\nds_read_b64 v[92:93], v30 offset:45056\n"
      "global_load_lds_dwordx4 v252, s[60:61] offset:1024 sc1\nglobal_load_lds_dwordx4 v252, s[60:61] offset:2048 sc1\nglobal_load_lds_dwordx4 v252, s[60:61] offset:3072 sc1\n"
      "s_add_u32 s60, s60, 4096\ns_addc_u32 s61, s61, 0\nv_max3_f16 v32, |v90|, |v91|, v32\nv_max3_f32 v33, |v90|, |v91|, v33\nv_and_b32 v142, s55, v90\ns_add_u32 m0, s56, 4096\n"
      "v_xor_b32 v90, v90, v142\nglobal_load_lds_dwordx4 v252, s[60:61] offset:0 sc1\nv_and_b32 v143, s55, v91\nv_xor_b32 v91, v91, v143\nv_readfirstlane_b32 s39, v9\nv_readlane_b32 s57, v251, 0\n"
      "global_load_lds_dwordx4 v252, s[60:61] offset:1024 sc1\ns_xor_b32 s40, s39, 0xa5c30f96\ns_mul_i32 s41, s39, 0x9E3779B1\ns_lshr_b32 s41, s41, 24\ns_lshl_b32 s42, s2, 3\n"
      "global_load_lds_dwordx4 v252, s[60:61] offset:2048 sc1\ns_add_u32 s42, s42, 24576\nv_mov_b32 v7, s42\nv_mov_b32 v8, s41\nv_mov_b32 v9, s40\n"
      "global_load_lds_dwordx4 v252, s[60:61] offset:3072 sc1\ns_add_u32 s60, s60, 4096\ns_addc_u32 s61, s61, 0\ns_lshl_b32 s42, s2, 2\nv_mov_b32 v12, s42\ns_add_u32 s42, s37, 1\n"
      "s_add_u32 m0, s56, 8192\ns_nop 0\nglobal_load_lds_dwordx4 v252, s[60:61] offset:0 sc1\nv_mov_b32 v6, s42\nv_and_b32 v1, 48, v240\ns_mov_b32 s42, 41984\n"
      "global_load_lds_dwordx4 v252, s[60:61] offset:1024 sc1\nv_lshl_add_u32 v1, v1, 2, s42\ns_sub_u32 s49, s65, 928\n"
      "v_add_u32_sdwa v236, s49, v244 dst_sel:DWORD dst_unused:UNUSED_PAD src0_sel:DWORD src1_sel:WORD_0\nglobal_load_lds_dwordx4 v252, s[60:61] offset:1536 sc1\n"
      "v_add_u32_sdwa v237, s49, v244 dst_sel:DWORD dst_unused:UNUSED_PAD src0_sel:DWORD src1_sel:WORD_1\n"
      "v_add_u32_sdwa v238, s49, v245 dst_sel:DWORD dst_unused:UNUSED_PAD src0_sel:DWORD src1_sel:WORD_0\n"
      "v_add_u32_sdwa v239, s49, v245 dst_sel:DWORD dst_unused:UNUSED_PAD src0_sel:DWORD src1_sel:WORD_1\ns_waitcnt vmcnt(11) lgkmcnt(0)\ns_barrier\nds_read_b64 v[94:95], v31 offset:0\n"
      "v_max3_f16 v32, |v92|, |v93|, v32\nv_max3_f32 v33, |v92|, |v93|, v33\ns_nop 1\nv_and_b32 v144, s55, v92\nv_xor_b32 v92, v92, v144\ns_nop 1\nv_and_b32 v145, s55, v93\n"
      "v_xor_b32 v93, v93, v145\ns_lshl_b32 s42, s30, 6\ns_add_u32 s42, s42, s36\ns_add_u32 s42, s42, 60\ns_mulk_i32 s42, 896\ns_add_u32 s44, s16, s42\ns_addc_u32 s45, s17, 0\n"
      "s_waitcnt lgkmcnt(0)\nv_max3_f16 v32, |v94|, |v95|, v32\nv_max3_f32 v33, |v94|, |v95|, v33\ns_nop 1\nv_and_b32 v146, s55, v94\nv_xor_b32 v94, v94, v146\ns_nop 1\nv_and_b32 v147, s55, v95\n"
      "v_xor_b32 v95, v95, v147\ns_nop 1\nv_lshlrev_b32 v32, 16, v32\nv_max_u32 v33, v32, v33\ns_nop 1\nv_max_u32_dpp v33, v33, v33 quad_perm:[1,0,3,2] row_mask:0xf bank_mask:0xf\ns_nop 1\n"
      "v_max_u32_dpp v33, v33, v33 quad_perm:[2,3,0,1] row_mask:0xf bank_mask:0xf\nv_lshrrev_b32 v33, 23, v33\ns_nop 1\nv_subrev_u32 v32, 22, v33\nv_mul_u32_u24 v32, 0x800080, v32\ns_nop 1\n"
      "v_pk_sub_u16 v44, v44, v32 clamp\nv_pk_sub_u16 v45, v45, v32 clamp\ns_nop 1\nv_pk_sub_u16 v46, v46, v32 clamp\nv_pk_sub_u16 v47, v47, v32 clamp\ns_nop 1\nv_lshl_or_b32 v44, v44, 3, v96\n"
      "v_lshl_or_b32 v45, v45, 3, v97\ns_nop 1\nv_lshl_or_b32 v46, v46, 3, v98\nv_lshl_or_b32 v47, v47, 3, v99\ns_nop 1\nv_pk_sub_u16 v48, v48, v32 clamp\nv_pk_sub_u16 v49, v49, v32 clamp\n"
      "s_nop 1\nv_pk_sub_u16 v50, v50, v32 clamp\nv_pk_sub_u16 v51, v51, v32 clamp\ns_nop 1\nv_lshl_or_b32 v48, v48, 3, v100\nv_lshl_or_b32 v49, v49, 3, v101\ns_nop 1\n"
      "v_lshl_or_b32 v50, v50, 3, v102\nv_lshl_or_b32 v51, v51, 3, v103\ns_nop 1\nv_pk_sub_u16 v52, v52, v32 clamp\nv_pk_sub_u16 v53, v53, v32 clamp\ns_nop 1\nv_pk_sub_u16 v54, v54, v32 clamp\n"
      "v_pk_sub_u16 v55, v55, v32 clamp\ns_nop 1\nv_lshl_or_b32 v52, v52, 3, v104\nv_lshl_or_b32 v53, v53, 3, v105\ns_nop 1\nv_lshl_or_b32 v54, v54, 3, v106\nv_lshl_or_b32 v55, v55, 3, v107\n"
      "s_nop 1\nv_pk_sub_u16 v56, v56, v32 clamp\nv_pk_sub_u16 v57, v57, v32 clamp\ns_nop 1\nv_pk_sub_u16 v58, v58, v32 clamp\nv_pk_sub_u16 v59, v59, v32 clamp\ns_nop 1\n"
      "v_lshl_or_b32 v56, v56, 3, v108\nv_lshl_or_b32 v57, v57, 3, v109\ns_nop 1\nv_lshl_or_b32 v58, v58, 3, v110\nv_lshl_or_b32 v59, v59, 3, v111\ns_nop 1\nv_pk_sub_u16 v60, v60, v32 clamp\n"
      "v_pk_sub_u16 v61, v61, v32 clamp\ns_nop 1\nv_pk_sub_u16 v62, v62, v32 clamp\nv_pk_sub_u16 v63, v63, v32 clamp\ns_nop 1\nv_lshl_or_b32 v60, v60, 3, v112\nv_lshl_or_b32 v61, v61, 3, v113\n"
      "s_nop 1\nv_lshl_or_b32 v62, v62, 3, v114\nv_lshl_or_b32 v63, v63, 3, v115\ns_nop 1\nv_pk_sub_u16 v64, v64, v32 clamp\nv_pk_sub_u16 v65, v65, v32 clamp\ns_nop 1\n"
      "v_pk_sub_u16 v66, v66, v32 clamp\nv_pk_sub_u16 v67, v67, v32 clamp\ns_nop 1\nv_lshl_or_b32 v64, v64, 3, v116\nv_lshl_or_b32 v65, v65, 3, v117\ns_nop 1\nv_lshl_or_b32 v66, v66, 3, v118\n"
      "v_lshl_or_b32 v67, v67, 3, v119\ns_nop 1\nv_pk_sub_u16 v68, v68, v32 clamp\nv_pk_sub_u16 v69, v69, v32 clamp\ns_nop 1\nv_pk_sub_u16 v70, v70, v32 clamp\nv_pk_sub_u16 v71, v71, v32 clamp\n"
      "s_nop 1\nv_lshl_or_b32 v68, v68, 3, v120\nv_lshl_or_b32 v69, v69, 3, v121\ns_nop 1\nv_lshl_or_b32 v70, v70, 3, v122\nv_lshl_or_b32 v71, v71, 3, v123\ns_nop 1\n"
      "v_pk_sub_u16 v72, v72, v32 clamp\nv_pk_sub_u16 v73, v73, v32 clamp\ns_nop 1\nv_pk_sub_u16 v74, v74, v32 clamp\nv_pk_sub_u16 v75, v75, v32 clamp\ns_nop 1\nv_lshl_or_b32 v72, v72, 3, v124\n"
      "v_lshl_or_b32 v73, v73, 3, v125\ns_nop 1\nv_lshl_or_b32 v74, v74, 3, v126\nv_lshl_or_b32 v75, v75, 3, v127\ns_waitcnt vmcnt(0)\ns_barrier\nv_pk_sub_u16 v76, v76, v32 clamp\n"
      "v_pk_sub_u16 v77, v77, v32 clamp\ns_nop 1\nv_pk_sub_u16 v78, v78, v32 clamp\nv_pk_sub_u16 v79, v79, v32 clamp\ns_nop 1\nv_lshl_or_b32 v76, v76, 3, v128\nv_lshl_or_b32 v77, v77, 3, v129\n"
      "s_nop 1\nv_lshl_or_b32 v78, v78, 3, v130\nv_lshl_or_b32 v79, v79, 3, v131\ns_nop 1\nv_pk_sub_u16 v80, v80, v32 clamp\nv_pk_sub_u16 v81, v81, v32 clamp\ns_nop 1\n"
      "v_pk_sub_u16 v82, v82, v32 clamp\nv_pk_sub_u16 v83, v83, v32 clamp\ns_nop 1\nv_lshl_or_b32 v80, v80, 3, v132\nv_lshl_or_b32 v81, v81, 3, v133\ns_nop 1\nv_lshl_or_b32 v82, v82, 3, v134\n"
      "v_lshl_or_b32 v83, v83, 3, v135\ns_nop 1\nv_pk_sub_u16 v84, v84, v32 clamp\nv_pk_sub_u16 v85, v85, v32 clamp\ns_nop 1\nv_pk_sub_u16 v86, v86, v32 clamp\nv_pk_sub_u16 v87, v87, v32 clamp\n"
      "s_nop 1\nv_lshl_or_b32 v84, v84, 3, v136\nv_lshl_or_b32 v85, v85, 3, v137\ns_nop 1\nv_lshl_or_b32 v86, v86, 3, v138\nv_lshl_or_b32 v87, v87, 3, v139\ns_nop 1\n"
      "v_pk_sub_u16 v88, v88, v32 clamp\nv_pk_sub_u16 v89, v89, v32 clamp\ns_nop 1\nv_pk_sub_u16 v90, v90, v32 clamp\nv_pk_sub_u16 v91, v91, v32 clamp\ns_nop 1\nv_lshl_or_b32 v88, v88, 3, v140\n"
      "v_lshl_or_b32 v89, v89, 3, v141\ns_nop 1\nv_lshl_or_b32 v90, v90, 3, v142\nv_lshl_or_b32 v91, v91, 3, v143\ns_nop 1\nv_pk_sub_u16 v92, v92, v32 clamp\nv_pk_sub_u16 v93, v93, v32 clamp\n"
      "s_nop 1\nv_pk_sub_u16 v94, v94, v32 clamp\nv_pk_sub_u16 v95, v95, v32 clamp\ns_nop 1\nv_lshl_or_b32 v92, v92, 3, v144\nv_lshl_or_b32 v93, v93, 3, v145\ns_nop 1\n"
      "v_lshl_or_b32 v94, v94, 3, v146\nv_lshl_or_b32 v95, v95, 3, v147\ns_nop 1\nv_sub_u32 v4, 257, v33\nv_lshlrev_b32 v4, 23, v4\ns_nop 1\nv_cvt_pkrtz_f16_f32 v4, v4, v4\n"
      "v_lshlrev_b32 v33, 2, v33\ns_nop 1\nv_mov_b32 v5, v4\nv_subrev_u32 v33, 536, v33\ns_setprio 0\nds_write_b64 v148, v[4:5] offset:832\nds_write_b32 v149, v33 offset:864\n"
      "ds_write2_b64 v150, v[44:45], v[46:47] offset1:4\nds_write2_b64 v151, v[48:49], v[50:51] offset1:4\nds_write2_b64 v152, v[52:53], v[54:55] offset1:4\n"
      "ds_write2_b64 v153, v[56:57], v[58:59] offset1:4\nds_write2_b64 v154, v[60:61], v[62:63] offset1:4\nds_write2_b64 v155, v[64:65], v[66:67] offset1:4\n"
      "ds_write2_b64 v156, v[68:69], v[70:71] offset1:4\nds_write2_b64 v157, v[72:73], v[74:75] offset1:4\nds_write2_b64 v158, v[76:77], v[78:79] offset1:4\n"
      "ds_write2_b64 v159, v[80:81], v[82:83] offset1:4\nds_write2_b64 v160, v[84:85], v[86:87] offset1:4\nds_write2_b64 v161, v[88:89], v[90:91] offset1:4\n"
      "ds_write2_b64 v162, v[92:93], v[94:95] offset1:4\nds_read_b128 v[16:19], v163 offset:6496\nds_read_b128 v[20:23], v163 offset:5568\nds_read_b128 v[24:27], v163 offset:4640\n"
      "s_cmp_gt_u32 s64, 12\ns_cbranch_scc1 .Ld1060\nds_read_b128 v[28:31], v163 offset:3712\nds_read_b128 v[32:35], v163 offset:2784\nds_read_b128 v[36:39], v163 offset:1856\n"
      "ds_read_b128 v[40:43], v163 offset:928\nds_read_b128 v[44:47], v163 offset:0\n"
      ".Ld1060:\ns_mov_b32 exec_hi, 0x7fffff\ns_waitcnt lgkmcnt(0)\ns_cmp_gt_u32 s64, 15\ns_cbranch_scc1 .Ld1061\nglobal_store_dwordx4 v252, v[16:19], s[44:45] sc1\ns_sub_u32 s44, s44, 3584\n"
      "s_subb_u32 s45, s45, 0\ns_cmp_gt_u32 s64, 14\ns_cbranch_scc1 .Ld1061\nglobal_store_dwordx4 v252, v[20:23], s[44:45] sc1\ns_sub_u32 s44, s44, 3584\ns_subb_u32 s45, s45, 0\n"
      "s_cmp_gt_u32 s64, 13\ns_cbranch_scc1 .Ld1061\nglobal_store_dwordx4 v252, v[24:27], s[44:45] sc1\ns_sub_u32 s44, s44, 3584\ns_subb_u32 s45, s45, 0\ns_cmp_gt_u32 s64, 12\n"
      "s_cbranch_scc1 .Ld1061\nglobal_store_dwordx4 v252, v[28:31], s[44:45] sc1\ns_sub_u32 s44, s44, 3584\ns_subb_u32 s45, s45, 0\ns_cmp_gt_u32 s64, 11\ns_cbranch_scc1 .Ld1061\n"
      "global_store_dwordx4 v252, v[32:35], s[44:45] sc1\ns_sub_u32 s44, s44, 3584\ns_subb_u32 s45, s45, 0\ns_cmp_gt_u32 s64, 10\ns_cbranch_scc1 .Ld1061\n"
      "global_store_dwordx4 v252, v[36:39], s[44:45] sc1\ns_sub_u32 s44, s44, 3584\ns_subb_u32 s45, s45, 0\ns_cmp_gt_u32 s64, 9\ns_cbranch_scc1 .Ld1061\n"
      "global_store_dwordx4 v252, v[40:43], s[44:45] sc1\ns_sub_u32 s44, s44, 3584\ns_subb_u32 s45, s45, 0\ns_cmp_gt_u32 s64, 8\ns_cbranch_scc1 .Ld1061\n"
      "global_store_dwordx4 v252, v[44:47], s[44:45] sc1\ns_sub_u32 s44, s44, 3584\ns_subb_u32 s45, s45, 0\n"
      ".Ld1061:\ns_mov_b32 exec_hi, -1\nds_read_b128 v[48:51], v1 offset:0\nds_read_b128 v[52:55], v1 offset:16\nds_read_b128 v[56:59], v1 offset:32\nds_read_b128 v[60:63], v1 offset:48\n"
      "ds_read_b128 v[72:75], v252 offset:0\nds_read_b128 v[76:79], v252 offset:1024\nds_read_b128 v[80:83], v252 offset:2048\nds_read_b128 v[84:87], v252 offset:3072\n"
      "ds_read_b128 v[88:91], v252 offset:4096\nds_read_b128 v[92:95], v252 offset:5120\nds_read_b128 v[96:99], v252 offset:6144\nds_read_b128 v[100:103], v252 offset:7168\n"
      "ds_read_b128 v[104:107], v252 offset:8192\nds_read_b128 v[108:111], v252 offset:9216\nds_read_b128 v[112:115], v252 offset:10240\nds_read_b128 v[116:119], v252 offset:11264\n"
      "ds_read_b128 v[120:123], v252 offset:12288\nds_read_b128 v[124:127], v252 offset:13312\nds_read_b128 v[128:131], v252 offset:14336\nds_read_b128 v[132:135], v252 offset:15360\n"
      "ds_read_b128 v[136:139], v252 offset:16384\nds_read_b128 v[140:143], v252 offset:17408\nds_read_b128 v[144:147], v252 offset:18432\nds_read_b128 v[148:151], v252 offset:19456\n"
      "ds_read_b128 v[152:155], v252 offset:20480\nds_read_b128 v[156:159], v252 offset:21504\nds_read_b128 v[160:163], v252 offset:22528\nds_read_b128 v[164:167], v252 offset:23552\n"
      "ds_read_b128 v[168:171], v252 offset:24576\nds_read_b128 v[172:175], v252 offset:25600\nds_read_b128 v[176:179], v252 offset:26624\nds_read_b128 v[180:183], v252 offset:27648\n"
      "ds_read_b128 v[184:187], v252 offset:28672\nds_read_b128 v[188:191], v252 offset:29696\nds_read_b128 v[192:195], v252 offset:30720\nds_read_b128 v[196:199], v252 offset:31744\n"
      "ds_read_b128 v[200:203], v252 offset:32768\nds_read_b128 v[204:207], v252 offset:33792\nds_read_b128 v[208:211], v252 offset:34816\nds_read_b128 v[212:215], v252 offset:35840\n"
      "ds_read_b128 v[216:219], v252 offset:36864\nds_read_b128 v[220:223], v252 offset:37888\nds_read_b128 v[224:227], v252 offset:38912\nds_read_b128 v[228:231], v252 offset:39936\n"
      "ds_read_b128 v[232:235], v252 offset:40960\ns_mov_b32 s55, s57\ns_mov_b32 s45, -1\ns_mov_b32 s46, 0\ns_setprio 3\ns_waitcnt lgkmcnt(15)\nv_mul_f32 v48, v48, v56\nv_mul_f32 v49, v49, v57\n"
      "v_mul_f32 v50, v50, v58\nv_mul_f32 v51, v51, v59\nv_mul_f32 v52, v52, v60\nv_mul_f32 v53, v53, v61\nv_mul_f32 v54, v54, v62\nds_write_b128 v1, v[48:51] offset:256\n"
      "ds_write_b128 v1, v[52:55] offset:272\nds_read_b64_tr_b16 v[68:69], v238 offset:928\nds_read_b64_tr_b16 v[70:71], v239 offset:928\nds_read_b64_tr_b16 v[56:57], v236 offset:928\n"
      "ds_read_b64_tr_b16 v[58:59], v237 offset:1056\nds_read_b64_tr_b16 v[60:61], v237 offset:1184\nds_read_b64_tr_b16 v[62:63], v237 offset:1312\nds_read_b64_tr_b16 v[64:65], v237 offset:1440\n"
      "ds_read_b64_tr_b16 v[66:67], v237 offset:1568\ns_swappc_b64 s[4:5], s[34:35]\ns_waitcnt vmcnt(0)\nds_read_b128 v[44:47], v248 offset:48\nds_add_rtn_u32 v0, v253, v241 offset:32\n"
      "s_waitcnt lgkmcnt(0)\nv_readfirstlane_b32 s42, v0\ns_cmp_lg_u32 s42, 3\ns_cbranch_scc1 .Lf1062\ns_mov_b64 exec, 1\nglobal_store_dwordx2 v7, v[8:9], s[20:21] sc1\n"
      "global_store_dword v12, v6, s[20:21] sc1\ns_mov_b64 exec, -1\n"
      ".Lf1062:\nv_readlane_b32 s48, v69, 48\ns_mov_b32 s56, 0\ns_mov_b32 s44, 0\ns_mov_b32 s33, s45\nv_pk_mul_f16 v20, v68, v70\nv_pk_mul_f16 v21, v69, v71\n"
      "v_pk_mul_f16 v22, v56, v56 op_sel_hi:[0,1]\nv_pk_mul_f16 v23, v56, v57 op_sel_hi:[0,1]\nv_pk_mul_f16 v16, v68, s48 op_sel:[0,1]\nv_pk_mul_f16 v17, v69, s48 op_sel:[0,1]\n"
      "v_pk_mul_f16 v18, v70, s48 op_sel:[0,1]\nv_pk_mul_f16 v19, v71, s48 op_sel:[0,1]\n.p2align 3\ns_nop 0\ns_barrier\nv_mfma_f32_16x16x32_f16 v[40:43], v[76:79], v[16:19], 0\n"
      "v_mfma_f32_16x16x32_f16 v[40:43], v[88:91], v[20:23], v[40:43]\ns_add_u32 s45, s45, 2\ns_cmp_ge_u32 s45, s46\ns_cbranch_scc0 .Lc_b0\ns_swappc_b64 s[4:5], s[34:35]\n"
      ".Lc_b0:\nv_mfma_f32_16x16x32_f16 v[36:39], v[84:87], v[20:23], 0\nv_pk_mul_f16 v24, v66, v66 op_sel_hi:[0,1]\nv_pk_mul_f16 v25, v66, v57 op_sel_hi:[0,1]\n"
      "v_mfma_f32_16x16x32_f16 v[36:39], v[72:75], v[16:19], v[36:39]\nv_pk_mul_f16 v26, v56, v66 op_sel_hi:[0,1]\nv_pk_mul_f16 v27, v56, v67 op_sel_hi:[0,1]\ns_setprio 0\n"
      "v_mfma_f32_16x16x32_f16 v[32:35], v[80:83], v[20:23], 0\nv_pk_mul_f16 v28, v58, v66 op_sel_hi:[0,1]\nv_pk_mul_f16 v29, v58, v67 op_sel_hi:[0,1]\ns_waitcnt lgkmcnt(0)\n"
      "v_smfmac_f32_16x16x64_f16 v[44:47], v[92:95], v[16:23], v242\nv_pk_mul_f16 v30, v60, v66 op_sel_hi:[0,1]\nv_pk_mul_f16 v31, v60, v67 op_sel_hi:[0,1]\n"
      "v_mfma_f32_16x16x32_f16 v[32:35], v[96:99], v[24:27], v[32:35]\nv_pk_mul_f16 v16, v62, v66 op_sel_hi:[0,1]\nv_pk_mul_f16 v17, v62, v67 op_sel_hi:[0,1]\ns_setprio 1\n"
      "v_mfma_f32_16x16x32_f16 v[32:35], v[108:111], v[28:31], v[32:35]\nv_pk_mul_f16 v18, v64, v66 op_sel_hi:[0,1]\nv_mfma_f32_16x16x32_f16 v[36:39], v[112:115], v[28:31], v[36:39]\n"
      "v_pk_mul_f16 v19, v64, v67 op_sel_hi:[0,1]\nv_mfma_f32_16x16x32_f16 v[36:39], v[100:103], v[24:27], v[36:39]\nv_pk_mul_f16 v20, v64, v64 op_sel_hi:[0,1]\n"
      "v_mfma_f32_16x16x32_f16 v[40:43], v[104:107], v[24:27], v[40:43]\nv_pk_mul_f16 v21, v64, v57 op_sel_hi:[0,1]\nv_mfma_f32_16x16x32_f16 v[40:43], v[116:119], v[28:31], v[40:43]\n"
      "v_pk_mul_f16 v22, v56, v64 op_sel_hi:[0,1]\nv_smfmac_f32_16x16x64_f16 v[44:47], v[120:123], v[24:31], v242\nv_pk_mul_f16 v23, v56, v65 op_sel_hi:[0,1]\n"
      "v_mfma_f32_16x16x32_f16 v[32:35], v[124:127], v[16:19], v[32:35]\nv_pk_mul_f16 v24, v58, v64 op_sel_hi:[0,1]\nv_pk_mul_f16 v25, v58, v65 op_sel_hi:[0,1]\ns_setprio 2\n"
      "ds_read_b64_tr_b16 v[70:71], v239 offset:1856\nv_mfma_f32_16x16x32_f16 v[32:35], v[136:139], v[20:23], v[32:35]\nv_pk_mul_f16 v26, v60, v64 op_sel_hi:[0,1]\n"
      "ds_read_b64_tr_b16 v[68:69], v238 offset:1856\nv_mfma_f32_16x16x32_f16 v[36:39], v[140:143], v[20:23], v[36:39]\nv_pk_mul_f16 v27, v60, v65 op_sel_hi:[0,1]\n"
      "ds_read_b64_tr_b16 v[66:67], v237 offset:2496\nv_mfma_f32_16x16x32_f16 v[36:39], v[128:131], v[16:19], v[36:39]\nv_pk_mul_f16 v28, v62, v64 op_sel_hi:[0,1]\n"
      "v_mfma_f32_16x16x32_f16 v[40:43], v[132:135], v[16:19], v[40:43]\nv_pk_mul_f16 v29, v62, v65 op_sel_hi:[0,1]\nv_mfma_f32_16x16x32_f16 v[40:43], v[144:147], v[20:23], v[40:43]\n"
      "v_pk_mul_f16 v30, v62, v62 op_sel_hi:[0,1]\nv_smfmac_f32_16x16x64_f16 v[44:47], v[148:151], v[16:23], v242\nv_pk_mul_f16 v31, v62, v57 op_sel_hi:[0,1]\n"
      "v_mfma_f32_16x16x32_f16 v[32:35], v[152:155], v[24:27], v[32:35]\nv_pk_mul_f16 v16, v56, v62 op_sel_hi:[0,1]\nv_pk_mul_f16 v17, v56, v63 op_sel_hi:[0,1]\n"
      "v_mfma_f32_16x16x32_f16 v[32:35], v[164:167], v[28:31], v[32:35]\nv_pk_mul_f16 v18, v58, v62 op_sel_hi:[0,1]\nds_read_b64_tr_b16 v[64:65], v237 offset:2368\n"
      "v_mfma_f32_16x16x32_f16 v[36:39], v[168:171], v[28:31], v[36:39]\nv_pk_mul_f16 v19, v58, v63 op_sel_hi:[0,1]\nv_mfma_f32_16x16x32_f16 v[36:39], v[156:159], v[24:27], v[36:39]\n"
      "v_pk_mul_f16 v20, v60, v62 op_sel_hi:[0,1]\ns_setprio 3\nv_mfma_f32_16x16x32_f16 v[40:43], v[160:163], v[24:27], v[40:43]\nv_pk_mul_f16 v21, v60, v63 op_sel_hi:[0,1]\ns_waitcnt lgkmcnt(1)\n"
      "v_readlane_b32 s48, v69, 48\nv_mfma_f32_16x16x32_f16 v[40:43], v[172:175], v[28:31], v[40:43]\nv_pk_mul_f16 v22, v60, v60 op_sel_hi:[0,1]\n"
      "v_smfmac_f32_16x16x64_f16 v[44:47], v[176:179], v[24:31], v242\nv_pk_mul_f16 v23, v60, v57 op_sel_hi:[0,1]\nv_mfma_f32_16x16x32_f16 v[32:35], v[180:183], v[16:19], v[32:35]\n"
      "v_pk_mul_f16 v24, v56, v60 op_sel_hi:[0,1]\nv_pk_mul_f16 v25, v56, v61 op_sel_hi:[0,1]\nv_mfma_f32_16x16x32_f16 v[32:35], v[192:195], v[20:23], v[32:35]\n"
      "v_pk_mul_f16 v26, v58, v60 op_sel_hi:[0,1]\nv_pk_mul_f16 v27, v58, v61 op_sel_hi:[0,1]\nv_mfma_f32_16x16x32_f16 v[36:39], v[196:199], v[20:23], v[36:39]\n"
      "v_pk_mul_f16 v28, v58, v58 op_sel_hi:[0,1]\nds_read_b64_tr_b16 v[62:63], v237 offset:2240\nv_mfma_f32_16x16x32_f16 v[36:39], v[184:187], v[16:19], v[36:39]\n"
      "v_pk_mul_f16 v29, v58, v57 op_sel_hi:[0,1]\nds_read_b64_tr_b16 v[60:61], v237 offset:2112\nv_mfma_f32_16x16x32_f16 v[40:43], v[188:191], v[16:19], v[40:43]\n"
      "v_pk_mul_f16 v30, v56, v58 op_sel_hi:[0,1]\nv_mfma_f32_16x16x32_f16 v[40:43], v[200:203], v[20:23], v[40:43]\nv_pk_mul_f16 v31, v56, v59 op_sel_hi:[0,1]\n"
      "v_smfmac_f32_16x16x64_f16 v[44:47], v[204:207], v[16:23], v242\nds_read_b64_tr_b16 v[56:57], v236 offset:1856\nds_read_b64_tr_b16 v[58:59], v237 offset:1984\n"
      "v_smfmac_f32_16x16x64_f16 v[44:47], v[232:235], v[24:31], v242\nv_pk_mul_f16 v16, v68, s48 op_sel:[0,1]\nv_pk_mul_f16 v17, v69, s48 op_sel:[0,1]\n"
      "v_mfma_f32_16x16x32_f16 v[32:35], v[208:211], v[24:27], v[32:35]\nv_pk_mul_f16 v18, v70, s48 op_sel:[0,1]\nv_pk_mul_f16 v19, v71, s48 op_sel:[0,1]\n"
      "v_mfma_f32_16x16x32_f16 v[32:35], v[220:223], v[28:31], v[32:35]\nv_pk_mul_f16 v20, v68, v70\nv_pk_mul_f16 v21, v69, v71\nv_mfma_f32_16x16x32_f16 v[36:39], v[224:227], v[28:31], v[36:39]\n"
      "v_add_f32 v44, v44, v45\nv_add_f32 v46, v46, v47\nv_mfma_f32_16x16x32_f16 v[36:39], v[212:215], v[24:27], v[36:39]\nv_mul_f32 v55, v44, v54\nv_mul_f32 v55, v55, v46\n"
      "ds_read_b128 v[44:47], v248 offset:48\nv_mfma_f32_16x16x32_f16 v[40:43], v[216:219], v[24:27], v[40:43]\ns_waitcnt lgkmcnt(1)\nv_pk_mul_f16 v22, v56, v56 op_sel_hi:[0,1]\n"
      "v_pk_mul_f16 v23, v56, v57 op_sel_hi:[0,1]\nv_mfma_f32_16x16x32_f16 v[40:43], v[228:231], v[28:31], v[40:43]\nv_mul_f32 v32, v32, v48\nv_mul_f32 v33, v33, v49\nv_mul_f32 v34, v34, v50\n"
      ".Lc_u1:\nv_mfma_f32_16x16x32_f16 v[244:247], v[76:79], v[16:19], 0\nv_fmac_f32 v55, v32, v36\nv_mul_f32 v35, v35, v51\nv_fmac_f32 v55, v33, v37\n"
      "v_mfma_f32_16x16x32_f16 v[244:247], v[88:91], v[20:23], v[244:247]\nv_fmac_f32 v55, v34, v38\nv_fmac_f32 v55, v35, v39\ns_add_u32 s45, s45, 2\ns_cmp_ge_u32 s45, s46\ns_cbranch_scc0 .Lc_b1\n"
      "s_swappc_b64 s[4:5], s[34:35]\n"
      ".Lc_b1:\nv_mfma_f32_16x16x32_f16 v[36:39], v[84:87], v[20:23], 0\nv_pk_mul_f16 v24, v66, v66 op_sel_hi:[0,1]\nv_pk_mul_f16 v25, v66, v57 op_sel_hi:[0,1]\n"
      "v_mfma_f32_16x16x32_f16 v[36:39], v[72:75], v[16:19], v[36:39]\nv_pk_mul_f16 v26, v56, v66 op_sel_hi:[0,1]\nv_pk_mul_f16 v27, v56, v67 op_sel_hi:[0,1]\ns_setprio 0\n"
      "v_mfma_f32_16x16x32_f16 v[32:35], v[80:83], v[20:23], 0\nv_pk_mul_f16 v28, v58, v66 op_sel_hi:[0,1]\nv_pk_mul_f16 v29, v58, v67 op_sel_hi:[0,1]\ns_waitcnt lgkmcnt(0)\n"
      "v_smfmac_f32_16x16x64_f16 v[44:47], v[92:95], v[16:23], v242\nv_pk_mul_f16 v30, v60, v66 op_sel_hi:[0,1]\nv_pk_mul_f16 v31, v60, v67 op_sel_hi:[0,1]\n"
      "v_mfma_f32_16x16x32_f16 v[32:35], v[96:99], v[24:27], v[32:35]\nv_pk_mul_f16 v16, v62, v66 op_sel_hi:[0,1]\nv_pk_mul_f16 v17, v62, v67 op_sel_hi:[0,1]\ns_setprio 1\n"
      "v_mfma_f32_16x16x32_f16 v[32:35], v[108:111], v[28:31], v[32:35]\nv_pk_mul_f16 v18, v64, v66 op_sel_hi:[0,1]\nv_mul_f32 v40, v40, v52\n"
      "v_mfma_f32_16x16x32_f16 v[36:39], v[112:115], v[28:31], v[36:39]\nv_pk_mul_f16 v19, v64, v67 op_sel_hi:[0,1]\nv_mul_f32 v41, v41, v53\n"
      "v_mfma_f32_16x16x32_f16 v[36:39], v[100:103], v[24:27], v[36:39]\nv_pk_mul_f16 v20, v64, v64 op_sel_hi:[0,1]\nv_fmac_f32 v55, v40, v42\n"
      "v_mfma_f32_16x16x32_f16 v[244:247], v[104:107], v[24:27], v[244:247]\nv_pk_mul_f16 v21, v64, v57 op_sel_hi:[0,1]\nv_fmac_f32 v55, v41, v43\n"
      "v_mfma_f32_16x16x32_f16 v[244:247], v[116:119], v[28:31], v[244:247]\nv_pk_mul_f16 v22, v56, v64 op_sel_hi:[0,1]\nv_smfmac_f32_16x16x64_f16 v[44:47], v[120:123], v[24:31], v242\n"
      "v_pk_mul_f16 v23, v56, v65 op_sel_hi:[0,1]\nv_mfma_f32_16x16x32_f16 v[32:35], v[124:127], v[16:19], v[32:35]\nv_pk_mul_f16 v24, v58, v64 op_sel_hi:[0,1]\n"
      "v_pk_mul_f16 v25, v58, v65 op_sel_hi:[0,1]\ns_setprio 2\nds_read_b64_tr_b16 v[70:71], v239 offset:3712\nv_mfma_f32_16x16x32_f16 v[32:35], v[136:139], v[20:23], v[32:35]\n"
      "v_pk_mul_f16 v26, v60, v64 op_sel_hi:[0,1]\nds_read_b64_tr_b16 v[68:69], v238 offset:3712\nv_mfma_f32_16x16x32_f16 v[36:39], v[140:143], v[20:23], v[36:39]\n"
      "v_pk_mul_f16 v27, v60, v65 op_sel_hi:[0,1]\nds_read_b64_tr_b16 v[66:67], v237 offset:4352\nv_mfma_f32_16x16x32_f16 v[36:39], v[128:131], v[16:19], v[36:39]\n"
      "v_pk_mul_f16 v28, v62, v64 op_sel_hi:[0,1]\nv_add_f32_dpp v55, v55, v55 quad_perm:[1,0,3,2] row_mask:0xf bank_mask:0xf\nv_mfma_f32_16x16x32_f16 v[244:247], v[132:135], v[16:19], v[244:247]\n"
      "v_pk_mul_f16 v29, v62, v65 op_sel_hi:[0,1]\nv_add_f32_dpp v55, v55, v55 quad_perm:[2,3,0,1] row_mask:0xf bank_mask:0xf\nv_mfma_f32_16x16x32_f16 v[244:247], v[144:147], v[20:23], v[244:247]\n"
      "v_pk_mul_f16 v30, v62, v62 op_sel_hi:[0,1]\nv_add_f32_dpp v55, v55, v55 row_half_mirror row_mask:0xf bank_mask:0xf\nv_smfmac_f32_16x16x64_f16 v[44:47], v[148:151], v[16:23], v242\n"
      "v_pk_mul_f16 v31, v62, v57 op_sel_hi:[0,1]\nv_add_f32_dpp v55, v55, v55 row_mirror row_mask:0xf bank_mask:0xf\nv_mfma_f32_16x16x32_f16 v[32:35], v[152:155], v[24:27], v[32:35]\n"
      "v_pk_mul_f16 v16, v56, v62 op_sel_hi:[0,1]\nv_pk_mul_f16 v17, v56, v63 op_sel_hi:[0,1]\nds_write_b32 v243, v55 offset:0\nv_mfma_f32_16x16x32_f16 v[32:35], v[164:167], v[28:31], v[32:35]\n"
      "v_pk_mul_f16 v18, v58, v62 op_sel_hi:[0,1]\nds_read_b64_tr_b16 v[64:65], v237 offset:4224\nv_mfma_f32_16x16x32_f16 v[36:39], v[168:171], v[28:31], v[36:39]\n"
      "v_pk_mul_f16 v19, v58, v63 op_sel_hi:[0,1]\nv_mfma_f32_16x16x32_f16 v[36:39], v[156:159], v[24:27], v[36:39]\nv_pk_mul_f16 v20, v60, v62 op_sel_hi:[0,1]\ns_setprio 3\n"
      "v_mfma_f32_16x16x32_f16 v[244:247], v[160:163], v[24:27], v[244:247]\nv_pk_mul_f16 v21, v60, v63 op_sel_hi:[0,1]\ns_waitcnt lgkmcnt(1)\nv_readlane_b32 s48, v69, 48\n"
      "v_mfma_f32_16x16x32_f16 v[244:247], v[172:175], v[28:31], v[244:247]\nv_pk_mul_f16 v22, v60, v60 op_sel_hi:[0,1]\nv_smfmac_f32_16x16x64_f16 v[44:47], v[176:179], v[24:31], v242\n"
      "v_pk_mul_f16 v23, v60, v57 op_sel_hi:[0,1]\nv_mfma_f32_16x16x32_f16 v[32:35], v[180:183], v[16:19], v[32:35]\nv_pk_mul_f16 v24, v56, v60 op_sel_hi:[0,1]\n"
      "v_pk_mul_f16 v25, v56, v61 op_sel_hi:[0,1]\nv_mfma_f32_16x16x32_f16 v[32:35], v[192:195], v[20:23], v[32:35]\nv_pk_mul_f16 v26, v58, v60 op_sel_hi:[0,1]\n"
      "v_pk_mul_f16 v27, v58, v61 op_sel_hi:[0,1]\nv_mfma_f32_16x16x32_f16 v[36:39], v[196:199], v[20:23], v[36:39]\nv_pk_mul_f16 v28, v58, v58 op_sel_hi:[0,1]\n"
      "ds_read_b64_tr_b16 v[62:63], v237 offset:4096\nv_mfma_f32_16x16x32_f16 v[36:39], v[184:187], v[16:19], v[36:39]\nv_pk_mul_f16 v29, v58, v57 op_sel_hi:[0,1]\n"
      "ds_read_b64_tr_b16 v[60:61], v237 offset:3968\nv_mfma_f32_16x16x32_f16 v[244:247], v[188:191], v[16:19], v[244:247]\nv_pk_mul_f16 v30, v56, v58 op_sel_hi:[0,1]\n"
      "v_mfma_f32_16x16x32_f16 v[244:247], v[200:203], v[20:23], v[244:247]\nv_pk_mul_f16 v31, v56, v59 op_sel_hi:[0,1]\nv_smfmac_f32_16x16x64_f16 v[44:47], v[204:207], v[16:23], v242\n"
      "ds_read_b64_tr_b16 v[56:57], v236 offset:3712\nds_read_b64_tr_b16 v[58:59], v237 offset:3840\nv_smfmac_f32_16x16x64_f16 v[44:47], v[232:235], v[24:31], v242\n"
      "v_pk_mul_f16 v16, v68, s48 op_sel:[0,1]\nv_pk_mul_f16 v17, v69, s48 op_sel:[0,1]\nv_mfma_f32_16x16x32_f16 v[32:35], v[208:211], v[24:27], v[32:35]\nv_pk_mul_f16 v18, v70, s48 op_sel:[0,1]\n"
      "v_pk_mul_f16 v19, v71, s48 op_sel:[0,1]\nv_mfma_f32_16x16x32_f16 v[32:35], v[220:223], v[28:31], v[32:35]\nv_pk_mul_f16 v20, v68, v70\nv_pk_mul_f16 v21, v69, v71\n"
      "v_mfma_f32_16x16x32_f16 v[36:39], v[224:227], v[28:31], v[36:39]\nv_add_f32 v44, v44, v45\nv_add_f32 v46, v46, v47\nv_mfma_f32_16x16x32_f16 v[36:39], v[212:215], v[24:27], v[36:39]\n"
      "v_mul_f32 v55, v44, v54\nv_mul_f32 v55, v55, v46\nds_read_b128 v[44:47], v248 offset:48\nv_mfma_f32_16x16x32_f16 v[244:247], v[216:219], v[24:27], v[244:247]\ns_waitcnt lgkmcnt(1)\n"
      "v_pk_mul_f16 v22, v56, v56 op_sel_hi:[0,1]\nv_pk_mul_f16 v23, v56, v57 op_sel_hi:[0,1]\nv_mfma_f32_16x16x32_f16 v[244:247], v[228:231], v[28:31], v[244:247]\nv_mul_f32 v32, v32, v48\n"
      "v_mul_f32 v33, v33, v49\nv_mul_f32 v34, v34, v50\nv_mfma_f32_16x16x32_f16 v[40:43], v[76:79], v[16:19], 0\nds_read_b32 v4, v248\nv_fmac_f32 v55, v32, v36\nv_mul_f32 v35, v35, v51\n"
      "v_fmac_f32 v55, v33, v37\nv_mfma_f32_16x16x32_f16 v[40:43], v[88:91], v[20:23], v[40:43]\nv_fmac_f32 v55, v34, v38\nv_fmac_f32 v55, v35, v39\ns_add_u32 s45, s45, 2\ns_cmp_ge_u32 s45, s46\n"
      "s_cbranch_scc0 .Lc_b2\ns_swappc_b64 s[4:5], s[34:35]\n"
      ".Lc_b2:\nv_mfma_f32_16x16x32_f16 v[36:39], v[84:87], v[20:23], 0\nv_pk_mul_f16 v24, v66, v66 op_sel_hi:[0,1]\nv_pk_mul_f16 v25, v66, v57 op_sel_hi:[0,1]\n"
      "v_mfma_f32_16x16x32_f16 v[36:39], v[72:75], v[16:19], v[36:39]\nv_pk_mul_f16 v26, v56, v66 op_sel_hi:[0,1]\nv_pk_mul_f16 v27, v56, v67 op_sel_hi:[0,1]\ns_setprio 0\n"
      "v_mfma_f32_16x16x32_f16 v[32:35], v[80:83], v[20:23], 0\nv_pk_mul_f16 v28, v58, v66 op_sel_hi:[0,1]\nv_pk_mul_f16 v29, v58, v67 op_sel_hi:[0,1]\ns_waitcnt lgkmcnt(0)\n"
      "v_smfmac_f32_16x16x64_f16 v[44:47], v[92:95], v[16:23], v242\nv_pk_mul_f16 v30, v60, v66 op_sel_hi:[0,1]\nv_pk_mul_f16 v31, v60, v67 op_sel_hi:[0,1]\n"
      "v_mfma_f32_16x16x32_f16 v[32:35], v[96:99], v[24:27], v[32:35]\nv_pk_mul_f16 v16, v62, v66 op_sel_hi:[0,1]\nv_pk_mul_f16 v17, v62, v67 op_sel_hi:[0,1]\ns_setprio 1\n"
      "v_mfma_f32_16x16x32_f16 v[32:35], v[108:111], v[28:31], v[32:35]\nv_pk_mul_f16 v18, v64, v66 op_sel_hi:[0,1]\nv_mul_f32 v244, v244, v52\n"
      "v_mfma_f32_16x16x32_f16 v[36:39], v[112:115], v[28:31], v[36:39]\nv_pk_mul_f16 v19, v64, v67 op_sel_hi:[0,1]\nv_mul_f32 v245, v245, v53\n"
      "v_mfma_f32_16x16x32_f16 v[36:39], v[100:103], v[24:27], v[36:39]\nv_pk_mul_f16 v20, v64, v64 op_sel_hi:[0,1]\nv_fmac_f32 v55, v244, v246\n"
      "v_mfma_f32_16x16x32_f16 v[40:43], v[104:107], v[24:27], v[40:43]\nv_pk_mul_f16 v21, v64, v57 op_sel_hi:[0,1]\nv_fmac_f32 v55, v245, v247\n"
      "v_mfma_f32_16x16x32_f16 v[40:43], v[116:119], v[28:31], v[40:43]\nv_pk_mul_f16 v22, v56, v64 op_sel_hi:[0,1]\nv_readfirstlane_b32 s44, v4\n"
      "v_smfmac_f32_16x16x64_f16 v[44:47], v[120:123], v[24:31], v242\nv_pk_mul_f16 v23, v56, v65 op_sel_hi:[0,1]\nv_mfma_f32_16x16x32_f16 v[32:35], v[124:127], v[16:19], v[32:35]\n"
      "v_pk_mul_f16 v24, v58, v64 op_sel_hi:[0,1]\nv_pk_mul_f16 v25, v58, v65 op_sel_hi:[0,1]\ns_setprio 2\nds_read_b64_tr_b16 v[70:71], v239 offset:5568\n"
      "v_mfma_f32_16x16x32_f16 v[32:35], v[136:139], v[20:23], v[32:35]\nv_pk_mul_f16 v26, v60, v64 op_sel_hi:[0,1]\nds_read_b64_tr_b16 v[68:69], v238 offset:5568\n"
      "v_mfma_f32_16x16x32_f16 v[36:39], v[140:143], v[20:23], v[36:39]\nv_pk_mul_f16 v27, v60, v65 op_sel_hi:[0,1]\nds_read_b64_tr_b16 v[66:67], v237 offset:6208\n"
      "v_mfma_f32_16x16x32_f16 v[36:39], v[128:131], v[16:19], v[36:39]\nv_pk_mul_f16 v28, v62, v64 op_sel_hi:[0,1]\nv_add_f32_dpp v55, v55, v55 quad_perm:[1,0,3,2] row_mask:0xf bank_mask:0xf\n"
      "v_mfma_f32_16x16x32_f16 v[40:43], v[132:135], v[16:19], v[40:43]\nv_pk_mul_f16 v29, v62, v65 op_sel_hi:[0,1]\nv_add_f32_dpp v55, v55, v55 quad_perm:[2,3,0,1] row_mask:0xf bank_mask:0xf\n"
      "v_mfma_f32_16x16x32_f16 v[40:43], v[144:147], v[20:23], v[40:43]\nv_pk_mul_f16 v30, v62, v62 op_sel_hi:[0,1]\nv_add_f32_dpp v55, v55, v55 row_half_mirror row_mask:0xf bank_mask:0xf\n"
      "v_smfmac_f32_16x16x64_f16 v[44:47], v[148:151], v[16:23], v242\nv_pk_mul_f16 v31, v62, v57 op_sel_hi:[0,1]\nv_add_f32_dpp v55, v55, v55 row_mirror row_mask:0xf bank_mask:0xf\n"
      "v_mfma_f32_16x16x32_f16 v[32:35], v[152:155], v[24:27], v[32:35]\nv_pk_mul_f16 v16, v56, v62 op_sel_hi:[0,1]\nv_pk_mul_f16 v17, v56, v63 op_sel_hi:[0,1]\nds_write_b32 v243, v55 offset:128\n"
      "v_mfma_f32_16x16x32_f16 v[32:35], v[164:167], v[28:31], v[32:35]\nv_pk_mul_f16 v18, v58, v62 op_sel_hi:[0,1]\nds_read_b64_tr_b16 v[64:65], v237 offset:6080\n"
      "v_mfma_f32_16x16x32_f16 v[36:39], v[168:171], v[28:31], v[36:39]\nv_pk_mul_f16 v19, v58, v63 op_sel_hi:[0,1]\nv_mfma_f32_16x16x32_f16 v[36:39], v[156:159], v[24:27], v[36:39]\n"
      "v_pk_mul_f16 v20, v60, v62 op_sel_hi:[0,1]\ns_setprio 3\ns_ff0_i32_b32 s53, s44\nv_mfma_f32_16x16x32_f16 v[40:43], v[160:163], v[24:27], v[40:43]\n"
      "v_pk_mul_f16 v21, v60, v63 op_sel_hi:[0,1]\ns_waitcnt lgkmcnt(1)\nv_readlane_b32 s48, v69, 48\nv_mfma_f32_16x16x32_f16 v[40:43], v[172:175], v[28:31], v[40:43]\n"
      "v_pk_mul_f16 v22, v60, v60 op_sel_hi:[0,1]\ns_add_u32 s53, s53, s64\nv_smfmac_f32_16x16x64_f16 v[44:47], v[176:179], v[24:31], v242\nv_pk_mul_f16 v23, v60, v57 op_sel_hi:[0,1]\n"
      "v_mfma_f32_16x16x32_f16 v[32:35], v[180:183], v[16:19], v[32:35]\nv_pk_mul_f16 v24, v56, v60 op_sel_hi:[0,1]\nv_pk_mul_f16 v25, v56, v61 op_sel_hi:[0,1]\n"
      "v_mfma_f32_16x16x32_f16 v[32:35], v[192:195], v[20:23], v[32:35]\nv_pk_mul_f16 v26, v58, v60 op_sel_hi:[0,1]\nv_pk_mul_f16 v27, v58, v61 op_sel_hi:[0,1]\n"
      "v_mfma_f32_16x16x32_f16 v[36:39], v[196:199], v[20:23], v[36:39]\nv_pk_mul_f16 v28, v58, v58 op_sel_hi:[0,1]\nds_read_b64_tr_b16 v[62:63], v237 offset:5952\n"
      "v_mfma_f32_16x16x32_f16 v[36:39], v[184:187], v[16:19], v[36:39]\nv_pk_mul_f16 v29, v58, v57 op_sel_hi:[0,1]\nds_read_b64_tr_b16 v[60:61], v237 offset:5824\n"
      "v_mfma_f32_16x16x32_f16 v[40:43], v[188:191], v[16:19], v[40:43]\nv_pk_mul_f16 v30, v56, v58 op_sel_hi:[0,1]\nv_mfma_f32_16x16x32_f16 v[40:43], v[200:203], v[20:23], v[40:43]\n"
      "v_pk_mul_f16 v31, v56, v59 op_sel_hi:[0,1]\nv_smfmac_f32_16x16x64_f16 v[44:47], v[204:207], v[16:23], v242\nds_read_b64_tr_b16 v[56:57], v236 offset:5568\n"
      "ds_read_b64_tr_b16 v[58:59], v237 offset:5696\nv_smfmac_f32_16x16x64_f16 v[44:47], v[232:235], v[24:31], v242\nv_pk_mul_f16 v16, v68, s48 op_sel:[0,1]\n"
      "v_pk_mul_f16 v17, v69, s48 op_sel:[0,1]\nv_mfma_f32_16x16x32_f16 v[32:35], v[208:211], v[24:27], v[32:35]\nv_pk_mul_f16 v18, v70, s48 op_sel:[0,1]\nv_pk_mul_f16 v19, v71, s48 op_sel:[0,1]\n"
      "v_mfma_f32_16x16x32_f16 v[32:35], v[220:223], v[28:31], v[32:35]\nv_pk_mul_f16 v20, v68, v70\nv_pk_mul_f16 v21, v69, v71\nv_mfma_f32_16x16x32_f16 v[36:39], v[224:227], v[28:31], v[36:39]\n"
      "v_add_f32 v44, v44, v45\nv_add_f32 v46, v46, v47\nv_mfma_f32_16x16x32_f16 v[36:39], v[212:215], v[24:27], v[36:39]\nv_mul_f32 v55, v44, v54\nv_mul_f32 v55, v55, v46\n"
      "ds_read_b128 v[44:47], v248 offset:48\nv_mfma_f32_16x16x32_f16 v[40:43], v[216:219], v[24:27], v[40:43]\ns_waitcnt lgkmcnt(1)\nv_pk_mul_f16 v22, v56, v56 op_sel_hi:[0,1]\n"
      "v_pk_mul_f16 v23, v56, v57 op_sel_hi:[0,1]\nv_mfma_f32_16x16x32_f16 v[40:43], v[228:231], v[28:31], v[40:43]\ns_cmp_lt_u32 s45, s53\nv_mul_f32 v32, v32, v48\nv_mul_f32 v33, v33, v49\n"
      "v_mul_f32 v34, v34, v50\ns_cbranch_scc0 .Lc_s2\n"
      ".Lc_u3:\nv_mfma_f32_16x16x32_f16 v[244:247], v[76:79], v[16:19], 0\nds_read_b32 v4, v248\nv_fmac_f32 v55, v32, v36\nv_mul_f32 v35, v35, v51\nv_fmac_f32 v55, v33, v37\n"
      "v_mfma_f32_16x16x32_f16 v[244:247], v[88:91], v[20:23], v[244:247]\nv_fmac_f32 v55, v34, v38\nv_fmac_f32 v55, v35, v39\ns_add_u32 s45, s45, 2\ns_cmp_ge_u32 s45, s46\ns_cbranch_scc0 .Lc_b3\n"
      "s_swappc_b64 s[4:5], s[34:35]\n"
      ".Lc_b3:\nv_mfma_f32_16x16x32_f16 v[36:39], v[84:87], v[20:23], 0\nv_pk_mul_f16 v24, v66, v66 op_sel_hi:[0,1]\nv_pk_mul_f16 v25, v66, v57 op_sel_hi:[0,1]\n"
      "v_mfma_f32_16x16x32_f16 v[36:39], v[72:75], v[16:19], v[36:39]\nv_pk_mul_f16 v26, v56, v66 op_sel_hi:[0,1]\nv_pk_mul_f16 v27, v56, v67 op_sel_hi:[0,1]\ns_setprio 0\n"
      "v_mfma_f32_16x16x32_f16 v[32:35], v[80:83], v[20:23], 0\nv_pk_mul_f16 v28, v58, v66 op_sel_hi:[0,1]\nv_pk_mul_f16 v29, v58, v67 op_sel_hi:[0,1]\ns_waitcnt lgkmcnt(0)\n"
      "v_smfmac_f32_16x16x64_f16 v[44:47], v[92:95], v[16:23], v242\nv_pk_mul_f16 v30, v60, v66 op_sel_hi:[0,1]\nv_pk_mul_f16 v31, v60, v67 op_sel_hi:[0,1]\n"
      "v_mfma_f32_16x16x32_f16 v[32:35], v[96:99], v[24:27], v[32:35]\nv_pk_mul_f16 v16, v62, v66 op_sel_hi:[0,1]\nv_pk_mul_f16 v17, v62, v67 op_sel_hi:[0,1]\ns_setprio 1\n"
      "v_mfma_f32_16x16x32_f16 v[32:35], v[108:111], v[28:31], v[32:35]\nv_pk_mul_f16 v18, v64, v66 op_sel_hi:[0,1]\nv_mul_f32 v40, v40, v52\n"
      "v_mfma_f32_16x16x32_f16 v[36:39], v[112:115], v[28:31], v[36:39]\nv_pk_mul_f16 v19, v64, v67 op_sel_hi:[0,1]\nv_mul_f32 v41, v41, v53\n"
      "v_mfma_f32_16x16x32_f16 v[36:39], v[100:103], v[24:27], v[36:39]\nv_pk_mul_f16 v20, v64, v64 op_sel_hi:[0,1]\nv_fmac_f32 v55, v40, v42\n"
      "v_mfma_f32_16x16x32_f16 v[244:247], v[104:107], v[24:27], v[244:247]\nv_pk_mul_f16 v21, v64, v57 op_sel_hi:[0,1]\nv_fmac_f32 v55, v41, v43\n"
      "v_mfma_f32_16x16x32_f16 v[244:247], v[116:119], v[28:31], v[244:247]\nv_pk_mul_f16 v22, v56, v64 op_sel_hi:[0,1]\nv_readfirstlane_b32 s44, v4\n"
      "v_smfmac_f32_16x16x64_f16 v[44:47], v[120:123], v[24:31], v242\nv_pk_mul_f16 v23, v56, v65 op_sel_hi:[0,1]\nv_mfma_f32_16x16x32_f16 v[32:35], v[124:127], v[16:19], v[32:35]\n"
      "v_pk_mul_f16 v24, v58, v64 op_sel_hi:[0,1]\nv_pk_mul_f16 v25, v58, v65 op_sel_hi:[0,1]\ns_setprio 2\nds_read_b64_tr_b16 v[70:71], v239 offset:7424\n"
      "v_mfma_f32_16x16x32_f16 v[32:35], v[136:139], v[20:23], v[32:35]\nv_pk_mul_f16 v26, v60, v64 op_sel_hi:[0,1]\nds_read_b64_tr_b16 v[68:69], v238 offset:7424\n"
      "v_mfma_f32_16x16x32_f16 v[36:39], v[140:143], v[20:23], v[36:39]\nv_pk_mul_f16 v27, v60, v65 op_sel_hi:[0,1]\nds_read_b64_tr_b16 v[66:67], v237 offset:8064\n"
      "v_mfma_f32_16x16x32_f16 v[36:39], v[128:131], v[16:19], v[36:39]\nv_pk_mul_f16 v28, v62, v64 op_sel_hi:[0,1]\nv_add_f32_dpp v55, v55, v55 quad_perm:[1,0,3,2] row_mask:0xf bank_mask:0xf\n"
      "v_mfma_f32_16x16x32_f16 v[244:247], v[132:135], v[16:19], v[244:247]\nv_pk_mul_f16 v29, v62, v65 op_sel_hi:[0,1]\nv_add_f32_dpp v55, v55, v55 quad_perm:[2,3,0,1] row_mask:0xf bank_mask:0xf\n"
      "v_mfma_f32_16x16x32_f16 v[244:247], v[144:147], v[20:23], v[244:247]\nv_pk_mul_f16 v30, v62, v62 op_sel_hi:[0,1]\nv_add_f32_dpp v55, v55, v55 row_half_mirror row_mask:0xf bank_mask:0xf\n"
      "v_smfmac_f32_16x16x64_f16 v[44:47], v[148:151], v[16:23], v242\nv_pk_mul_f16 v31, v62, v57 op_sel_hi:[0,1]\nv_add_f32_dpp v55, v55, v55 row_mirror row_mask:0xf bank_mask:0xf\n"
      "v_mfma_f32_16x16x32_f16 v[32:35], v[152:155], v[24:27], v[32:35]\nv_pk_mul_f16 v16, v56, v62 op_sel_hi:[0,1]\nv_pk_mul_f16 v17, v56, v63 op_sel_hi:[0,1]\nds_write_b32 v243, v55 offset:256\n"
      "v_mfma_f32_16x16x32_f16 v[32:35], v[164:167], v[28:31], v[32:35]\nv_pk_mul_f16 v18, v58, v62 op_sel_hi:[0,1]\nds_read_b64_tr_b16 v[64:65], v237 offset:7936\n"
      "v_mfma_f32_16x16x32_f16 v[36:39], v[168:171], v[28:31], v[36:39]\nv_pk_mul_f16 v19, v58, v63 op_sel_hi:[0,1]\nv_mfma_f32_16x16x32_f16 v[36:39], v[156:159], v[24:27], v[36:39]\n"
      "v_pk_mul_f16 v20, v60, v62 op_sel_hi:[0,1]\ns_setprio 3\ns_ff0_i32_b32 s53, s44\nv_mfma_f32_16x16x32_f16 v[244:247], v[160:163], v[24:27], v[244:247]\n"
      "v_pk_mul_f16 v21, v60, v63 op_sel_hi:[0,1]\ns_waitcnt lgkmcnt(1)\nv_readlane_b32 s48, v69, 48\nv_mfma_f32_16x16x32_f16 v[244:247], v[172:175], v[28:31], v[244:247]\n"
      "v_pk_mul_f16 v22, v60, v60 op_sel_hi:[0,1]\ns_add_u32 s53, s53, s64\nv_smfmac_f32_16x16x64_f16 v[44:47], v[176:179], v[24:31], v242\nv_pk_mul_f16 v23, v60, v57 op_sel_hi:[0,1]\n"
      "v_mfma_f32_16x16x32_f16 v[32:35], v[180:183], v[16:19], v[32:35]\nv_pk_mul_f16 v24, v56, v60 op_sel_hi:[0,1]\nv_pk_mul_f16 v25, v56, v61 op_sel_hi:[0,1]\n"
      "v_mfma_f32_16x16x32_f16 v[32:35], v[192:195], v[20:23], v[32:35]\nv_pk_mul_f16 v26, v58, v60 op_sel_hi:[0,1]\nv_pk_mul_f16 v27, v58, v61 op_sel_hi:[0,1]\n"
      "v_mfma_f32_16x16x32_f16 v[36:39], v[196:199], v[20:23], v[36:39]\nv_pk_mul_f16 v28, v58, v58 op_sel_hi:[0,1]\nds_read_b64_tr_b16 v[62:63], v237 offset:7808\n"
      "v_mfma_f32_16x16x32_f16 v[36:39], v[184:187], v[16:19], v[36:39]\nv_pk_mul_f16 v29, v58, v57 op_sel_hi:[0,1]\nds_read_b64_tr_b16 v[60:61], v237 offset:7680\n"
      "v_mfma_f32_16x16x32_f16 v[244:247], v[188:191], v[16:19], v[244:247]\nv_pk_mul_f16 v30, v56, v58 op_sel_hi:[0,1]\nv_mfma_f32_16x16x32_f16 v[244:247], v[200:203], v[20:23], v[244:247]\n"
      "v_pk_mul_f16 v31, v56, v59 op_sel_hi:[0,1]\nv_smfmac_f32_16x16x64_f16 v[44:47], v[204:207], v[16:23], v242\nds_read_b64_tr_b16 v[56:57], v236 offset:7424\n"
      "ds_read_b64_tr_b16 v[58:59], v237 offset:7552\nv_smfmac_f32_16x16x64_f16 v[44:47], v[232:235], v[24:31], v242\nv_pk_mul_f16 v16, v68, s48 op_sel:[0,1]\n"
      "v_pk_mul_f16 v17, v69, s48 op_sel:[0,1]\nv_mfma_f32_16x16x32_f16 v[32:35], v[208:211], v[24:27], v[32:35]\nv_pk_mul_f16 v18, v70, s48 op_sel:[0,1]\nv_pk_mul_f16 v19, v71, s48 op_sel:[0,1]\n"
      "v_mfma_f32_16x16x32_f16 v[32:35], v[220:223], v[28:31], v[32:35]\nv_pk_mul_f16 v20, v68, v70\nv_pk_mul_f16 v21, v69, v71\nv_mfma_f32_16x16x32_f16 v[36:39], v[224:227], v[28:31], v[36:39]\n"
      "v_add_f32 v44, v44, v45\nv_add_f32 v46, v46, v47\nv_mfma_f32_16x16x32_f16 v[36:39], v[212:215], v[24:27], v[36:39]\nv_mul_f32 v55, v44, v54\nv_mul_f32 v55, v55, v46\n"
      "ds_read_b128 v[44:47], v248 offset:48\nv_mfma_f32_16x16x32_f16 v[244:247], v[216:219], v[24:27], v[244:247]\ns_waitcnt lgkmcnt(1)\nv_pk_mul_f16 v22, v56, v56 op_sel_hi:[0,1]\n"
      "v_pk_mul_f16 v23, v56, v57 op_sel_hi:[0,1]\nv_mfma_f32_16x16x32_f16 v[244:247], v[228:231], v[28:31], v[244:247]\ns_cmp_lt_u32 s45, s53\nv_mul_f32 v32, v32, v48\nv_mul_f32 v33, v33, v49\n"
      "v_mul_f32 v34, v34, v50\ns_cbranch_scc0 .Lc_s3\n"
      ".Lc_u4:\nv_mfma_f32_16x16x32_f16 v[40:43], v[76:79], v[16:19], 0\nds_read_b32 v4, v248\nv_fmac_f32 v55, v32, v36\nv_mul_f32 v35, v35, v51\nv_fmac_f32 v55, v33, v37\n"
      "v_mfma_f32_16x16x32_f16 v[40:43], v[88:91], v[20:23], v[40:43]\nv_fmac_f32 v55, v34, v38\nv_fmac_f32 v55, v35, v39\ns_add_u32 s45, s45, 2\ns_cmp_ge_u32 s45, s46\ns_cbranch_scc0 .Lc_b4\n"
      "s_swappc_b64 s[4:5], s[34:35]\n"
      ".Lc_b4:\nv_mfma_f32_16x16x32_f16 v[36:39], v[84:87], v[20:23], 0\nv_pk_mul_f16 v24, v66, v66 op_sel_hi:[0,1]\nv_pk_mul_f16 v25, v66, v57 op_sel_hi:[0,1]\n"
      "v_mfma_f32_16x16x32_f16 v[36:39], v[72:75], v[16:19], v[36:39]\nv_pk_mul_f16 v26, v56, v66 op_sel_hi:[0,1]\nv_pk_mul_f16 v27, v56, v67 op_sel_hi:[0,1]\ns_setprio 0\n"
      "v_mfma_f32_16x16x32_f16 v[32:35], v[80:83], v[20:23], 0\nv_pk_mul_f16 v28, v58, v66 op_sel_hi:[0,1]\nv_pk_mul_f16 v29, v58, v67 op_sel_hi:[0,1]\ns_waitcnt lgkmcnt(0)\n"
      "v_smfmac_f32_16x16x64_f16 v[44:47], v[92:95], v[16:23], v242\nv_pk_mul_f16 v30, v60, v66 op_sel_hi:[0,1]\nv_pk_mul_f16 v31, v60, v67 op_sel_hi:[0,1]\n"
      "v_mfma_f32_16x16x32_f16 v[32:35], v[96:99], v[24:27], v[32:35]\nv_pk_mul_f16 v16, v62, v66 op_sel_hi:[0,1]\nv_pk_mul_f16 v17, v62, v67 op_sel_hi:[0,1]\ns_setprio 1\n"
      "v_mfma_f32_16x16x32_f16 v[32:35], v[108:111], v[28:31], v[32:35]\nv_pk_mul_f16 v18, v64, v66 op_sel_hi:[0,1]\nv_mul_f32 v244, v244, v52\n"
      "v_mfma_f32_16x16x32_f16 v[36:39], v[112:115], v[28:31], v[36:39]\nv_pk_mul_f16 v19, v64, v67 op_sel_hi:[0,1]\nv_mul_f32 v245, v245, v53\n"
      "v_mfma_f32_16x16x32_f16 v[36:39], v[100:103], v[24:27], v[36:39]\nv_pk_mul_f16 v20, v64, v64 op_sel_hi:[0,1]\nv_fmac_f32 v55, v244, v246\n"
      "v_mfma_f32_16x16x32_f16 v[40:43], v[104:107], v[24:27], v[40:43]\nv_pk_mul_f16 v21, v64, v57 op_sel_hi:[0,1]\nv_fmac_f32 v55, v245, v247\n"
      "v_mfma_f32_16x16x32_f16 v[40:43], v[116:119], v[28:31], v[40:43]\nv_pk_mul_f16 v22, v56, v64 op_sel_hi:[0,1]\nv_readfirstlane_b32 s44, v4\n"
      "v_smfmac_f32_16x16x64_f16 v[44:47], v[120:123], v[24:31], v242\nv_pk_mul_f16 v23, v56, v65 op_sel_hi:[0,1]\nv_mfma_f32_16x16x32_f16 v[32:35], v[124:127], v[16:19], v[32:35]\n"
      "v_pk_mul_f16 v24, v58, v64 op_sel_hi:[0,1]\nv_pk_mul_f16 v25, v58, v65 op_sel_hi:[0,1]\ns_setprio 2\nds_read_b64_tr_b16 v[70:71], v239 offset:9280\n"
      "v_mfma_f32_16x16x32_f16 v[32:35], v[136:139], v[20:23], v[32:35]\nv_pk_mul_f16 v26, v60, v64 op_sel_hi:[0,1]\nds_read_b64_tr_b16 v[68:69], v238 offset:9280\n"
      "v_mfma_f32_16x16x32_f16 v[36:39], v[140:143], v[20:23], v[36:39]\nv_pk_mul_f16 v27, v60, v65 op_sel_hi:[0,1]\nds_read_b64_tr_b16 v[66:67], v237 offset:9920\n"
      "v_mfma_f32_16x16x32_f16 v[36:39], v[128:131], v[16:19], v[36:39]\nv_pk_mul_f16 v28, v62, v64 op_sel_hi:[0,1]\nv_add_f32_dpp v55, v55, v55 quad_perm:[1,0,3,2] row_mask:0xf bank_mask:0xf\n"
      "v_mfma_f32_16x16x32_f16 v[40:43], v[132:135], v[16:19], v[40:43]\nv_pk_mul_f16 v29, v62, v65 op_sel_hi:[0,1]\nv_add_f32_dpp v55, v55, v55 quad_perm:[2,3,0,1] row_mask:0xf bank_mask:0xf\n"
      "v_mfma_f32_16x16x32_f16 v[40:43], v[144:147], v[20:23], v[40:43]\nv_pk_mul_f16 v30, v62, v62 op_sel_hi:[0,1]\nv_add_f32_dpp v55, v55, v55 row_half_mirror row_mask:0xf bank_mask:0xf\n"
      "v_smfmac_f32_16x16x64_f16 v[44:47], v[148:151], v[16:23], v242\nv_pk_mul_f16 v31, v62, v57 op_sel_hi:[0,1]\nv_add_f32_dpp v55, v55, v55 row_mirror row_mask:0xf bank_mask:0xf\n"
      "v_mfma_f32_16x16x32_f16 v[32:35], v[152:155], v[24:27], v[32:35]\nv_pk_mul_f16 v16, v56, v62 op_sel_hi:[0,1]\nv_pk_mul_f16 v17, v56, v63 op_sel_hi:[0,1]\nds_write_b32 v243, v55 offset:384\n"
      "v_mfma_f32_16x16x32_f16 v[32:35], v[164:167], v[28:31], v[32:35]\nv_pk_mul_f16 v18, v58, v62 op_sel_hi:[0,1]\nds_read_b64_tr_b16 v[64:65], v237 offset:9792\n"
      "v_mfma_f32_16x16x32_f16 v[36:39], v[168:171], v[28:31], v[36:39]\nv_pk_mul_f16 v19, v58, v63 op_sel_hi:[0,1]\nv_mfma_f32_16x16x32_f16 v[36:39], v[156:159], v[24:27], v[36:39]\n"
      "v_pk_mul_f16 v20, v60, v62 op_sel_hi:[0,1]\ns_setprio 3\ns_ff0_i32_b32 s53, s44\nv_mfma_f32_16x16x32_f16 v[40:43], v[160:163], v[24:27], v[40:43]\n"
      "v_pk_mul_f16 v21, v60, v63 op_sel_hi:[0,1]\ns_waitcnt lgkmcnt(1)\nv_readlane_b32 s48, v69, 48\nv_mfma_f32_16x16x32_f16 v[40:43], v[172:175], v[28:31], v[40:43]\n"
      "v_pk_mul_f16 v22, v60, v60 op_sel_hi:[0,1]\ns_add_u32 s53, s53, s64\nv_smfmac_f32_16x16x64_f16 v[44:47], v[176:179], v[24:31], v242\nv_pk_mul_f16 v23, v60, v57 op_sel_hi:[0,1]\n"
      "v_mfma_f32_16x16x32_f16 v[32:35], v[180:183], v[16:19], v[32:35]\nv_pk_mul_f16 v24, v56, v60 op_sel_hi:[0,1]\nv_pk_mul_f16 v25, v56, v61 op_sel_hi:[0,1]\n"
      "v_mfma_f32_16x16x32_f16 v[32:35], v[192:195], v[20:23], v[32:35]\nv_pk_mul_f16 v26, v58, v60 op_sel_hi:[0,1]\nv_pk_mul_f16 v27, v58, v61 op_sel_hi:[0,1]\n"
      "v_mfma_f32_16x16x32_f16 v[36:39], v[196:199], v[20:23], v[36:39]\nv_pk_mul_f16 v28, v58, v58 op_sel_hi:[0,1]\nds_read_b64_tr_b16 v[62:63], v237 offset:9664\n"
      "v_mfma_f32_16x16x32_f16 v[36:39], v[184:187], v[16:19], v[36:39]\nv_pk_mul_f16 v29, v58, v57 op_sel_hi:[0,1]\nds_read_b64_tr_b16 v[60:61], v237 offset:9536\n"
      "v_mfma_f32_16x16x32_f16 v[40:43], v[188:191], v[16:19], v[40:43]\nv_pk_mul_f16 v30, v56, v58 op_sel_hi:[0,1]\nv_mfma_f32_16x16x32_f16 v[40:43], v[200:203], v[20:23], v[40:43]\n"
      "v_pk_mul_f16 v31, v56, v59 op_sel_hi:[0,1]\nv_smfmac_f32_16x16x64_f16 v[44:47], v[204:207], v[16:23], v242\nds_read_b64_tr_b16 v[56:57], v236 offset:9280\n"
      "ds_read_b64_tr_b16 v[58:59], v237 offset:9408\nv_smfmac_f32_16x16x64_f16 v[44:47], v[232:235], v[24:31], v242\nv_pk_mul_f16 v16, v68, s48 op_sel:[0,1]\n"
      "v_pk_mul_f16 v17, v69, s48 op_sel:[0,1]\nv_mfma_f32_16x16x32_f16 v[32:35], v[208:211], v[24:27], v[32:35]\nv_pk_mul_f16 v18, v70, s48 op_sel:[0,1]\nv_pk_mul_f16 v19, v71, s48 op_sel:[0,1]\n"
      "v_mfma_f32_16x16x32_f16 v[32:35], v[220:223], v[28:31], v[32:35]\nv_pk_mul_f16 v20, v68, v70\nv_pk_mul_f16 v21, v69, v71\nv_mfma_f32_16x16x32_f16 v[36:39], v[224:227], v[28:31], v[36:39]\n"
      "v_add_f32 v44, v44, v45\nv_add_f32 v46, v46, v47\nv_mfma_f32_16x16x32_f16 v[36:39], v[212:215], v[24:27], v[36:39]\nv_mul_f32 v55, v44, v54\nv_mul_f32 v55, v55, v46\n"
      "ds_read_b128 v[44:47], v248 offset:48\nv_mfma_f32_16x16x32_f16 v[40:43], v[216:219], v[24:27], v[40:43]\ns_waitcnt lgkmcnt(1)\nv_pk_mul_f16 v22, v56, v56 op_sel_hi:[0,1]\n"
      "v_pk_mul_f16 v23, v56, v57 op_sel_hi:[0,1]\nv_mfma_f32_16x16x32_f16 v[40:43], v[228:231], v[28:31], v[40:43]\ns_cmp_lt_u32 s45, s53\nv_mul_f32 v32, v32, v48\nv_mul_f32 v33, v33, v49\n"
      "v_mul_f32 v34, v34, v50\ns_cbranch_scc0 .Lc_s4\n"
      ".Lc_u5:\nv_mfma_f32_16x16x32_f16 v[244:247], v[76:79], v[16:19], 0\nds_read_b32 v4, v248\nv_fmac_f32 v55, v32, v36\nv_mul_f32 v35, v35, v51\nv_fmac_f32 v55, v33, v37\n"
      "v_mfma_f32_16x16x32_f16 v[244:247], v[88:91], v[20:23], v[244:247]\nv_fmac_f32 v55, v34, v38\nv_fmac_f32 v55, v35, v39\ns_add_u32 s45, s45, 2\ns_cmp_ge_u32 s45, s46\ns_cbranch_scc0 .Lc_b5\n"
      "s_swappc_b64 s[4:5], s[34:35]\n"
      ".Lc_b5:\nv_mfma_f32_16x16x32_f16 v[36:39], v[84:87], v[20:23], 0\nv_pk_mul_f16 v24, v66, v66 op_sel_hi:[0,1]\nv_pk_mul_f16 v25, v66, v57 op_sel_hi:[0,1]\n"
      "v_mfma_f32_16x16x32_f16 v[36:39], v[72:75], v[16:19], v[36:39]\nv_pk_mul_f16 v26, v56, v66 op_sel_hi:[0,1]\nv_pk_mul_f16 v27, v56, v67 op_sel_hi:[0,1]\ns_setprio 0\n"
      "v_mfma_f32_16x16x32_f16 v[32:35], v[80:83], v[20:23], 0\nv_pk_mul_f16 v28, v58, v66 op_sel_hi:[0,1]\nv_pk_mul_f16 v29, v58, v67 op_sel_hi:[0,1]\ns_waitcnt lgkmcnt(0)\n"
      "v_smfmac_f32_16x16x64_f16 v[44:47], v[92:95], v[16:23], v242\nv_pk_mul_f16 v30, v60, v66 op_sel_hi:[0,1]\nv_pk_mul_f16 v31, v60, v67 op_sel_hi:[0,1]\n"
      "v_mfma_f32_16x16x32_f16 v[32:35], v[96:99], v[24:27], v[32:35]\nv_pk_mul_f16 v16, v62, v66 op_sel_hi:[0,1]\nv_pk_mul_f16 v17, v62, v67 op_sel_hi:[0,1]\ns_setprio 1\n"
      "v_mfma_f32_16x16x32_f16 v[32:35], v[108:111], v[28:31], v[32:35]\nv_pk_mul_f16 v18, v64, v66 op_sel_hi:[0,1]\nv_mul_f32 v40, v40, v52\n"
      "v_mfma_f32_16x16x32_f16 v[36:39], v[112:115], v[28:31], v[36:39]\nv_pk_mul_f16 v19, v64, v67 op_sel_hi:[0,1]\nv_mul_f32 v41, v41, v53\n"
      "v_mfma_f32_16x16x32_f16 v[36:39], v[100:103], v[24:27], v[36:39]\nv_pk_mul_f16 v20, v64, v64 op_sel_hi:[0,1]\nv_fmac_f32 v55, v40, v42\n"
      "v_mfma_f32_16x16x32_f16 v[244:247], v[104:107], v[24:27], v[244:247]\nv_pk_mul_f16 v21, v64, v57 op_sel_hi:[0,1]\nv_fmac_f32 v55, v41, v43\n"
      "v_mfma_f32_16x16x32_f16 v[244:247], v[116:119], v[28:31], v[244:247]\nv_pk_mul_f16 v22, v56, v64 op_sel_hi:[0,1]\nv_readfirstlane_b32 s44, v4\n"
      "v_smfmac_f32_16x16x64_f16 v[44:47], v[120:123], v[24:31], v242\nv_pk_mul_f16 v23, v56, v65 op_sel_hi:[0,1]\nv_mfma_f32_16x16x32_f16 v[32:35], v[124:127], v[16:19], v[32:35]\n"
      "v_pk_mul_f16 v24, v58, v64 op_sel_hi:[0,1]\nv_pk_mul_f16 v25, v58, v65 op_sel_hi:[0,1]\ns_setprio 2\nds_read_b64_tr_b16 v[70:71], v239 offset:11136\n"
      "v_mfma_f32_16x16x32_f16 v[32:35], v[136:139], v[20:23], v[32:35]\nv_pk_mul_f16 v26, v60, v64 op_sel_hi:[0,1]\nds_read_b64_tr_b16 v[68:69], v238 offset:11136\n"
      "v_mfma_f32_16x16x32_f16 v[36:39], v[140:143], v[20:23], v[36:39]\nv_pk_mul_f16 v27, v60, v65 op_sel_hi:[0,1]\nds_read_b64_tr_b16 v[66:67], v237 offset:11776\n"
      "v_mfma_f32_16x16x32_f16 v[36:39], v[128:131], v[16:19], v[36:39]\nv_pk_mul_f16 v28, v62, v64 op_sel_hi:[0,1]\nv_add_f32_dpp v55, v55, v55 quad_perm:[1,0,3,2] row_mask:0xf bank_mask:0xf\n"
      "v_mfma_f32_16x16x32_f16 v[244:247], v[132:135], v[16:19], v[244:247]\nv_pk_mul_f16 v29, v62, v65 op_sel_hi:[0,1]\nv_add_f32_dpp v55, v55, v55 quad_perm:[2,3,0,1] row_mask:0xf bank_mask:0xf\n"
      "v_mfma_f32_16x16x32_f16 v[244:247], v[144:147], v[20:23], v[244:247]\nv_pk_mul_f16 v30, v62, v62 op_sel_hi:[0,1]\nv_add_f32_dpp v55, v55, v55 row_half_mirror row_mask:0xf bank_mask:0xf\n"
      "v_smfmac_f32_16x16x64_f16 v[44:47], v[148:151], v[16:23], v242\nv_pk_mul_f16 v31, v62, v57 op_sel_hi:[0,1]\nv_add_f32_dpp v55, v55, v55 row_mirror row_mask:0xf bank_mask:0xf\n"
      "v_mfma_f32_16x16x32_f16 v[32:35], v[152:155], v[24:27], v[32:35]\nv_pk_mul_f16 v16, v56, v62 op_sel_hi:[0,1]\nv_pk_mul_f16 v17, v56, v63 op_sel_hi:[0,1]\nds_write_b32 v243, v55 offset:512\n"
      "v_mfma_f32_16x16x32_f16 v[32:35], v[164:167], v[28:31], v[32:35]\nv_pk_mul_f16 v18, v58, v62 op_sel_hi:[0,1]\nds_read_b64_tr_b16 v[64:65], v237 offset:11648\n"
      "v_mfma_f32_16x16x32_f16 v[36:39], v[168:171], v[28:31], v[36:39]\nv_pk_mul_f16 v19, v58, v63 op_sel_hi:[0,1]\nv_mfma_f32_16x16x32_f16 v[36:39], v[156:159], v[24:27], v[36:39]\n"
      "v_pk_mul_f16 v20, v60, v62 op_sel_hi:[0,1]\ns_setprio 3\ns_ff0_i32_b32 s53, s44\nv_mfma_f32_16x16x32_f16 v[244:247], v[160:163], v[24:27], v[244:247]\n"
      "v_pk_mul_f16 v21, v60, v63 op_sel_hi:[0,1]\ns_waitcnt lgkmcnt(1)\nv_readlane_b32 s48, v69, 48\nv_mfma_f32_16x16x32_f16 v[244:247], v[172:175], v[28:31], v[244:247]\n"
      "v_pk_mul_f16 v22, v60, v60 op_sel_hi:[0,1]\ns_add_u32 s53, s53, s64\nv_smfmac_f32_16x16x64_f16 v[44:47], v[176:179], v[24:31], v242\nv_pk_mul_f16 v23, v60, v57 op_sel_hi:[0,1]\n"
      "v_mfma_f32_16x16x32_f16 v[32:35], v[180:183], v[16:19], v[32:35]\nv_pk_mul_f16 v24, v56, v60 op_sel_hi:[0,1]\nv_pk_mul_f16 v25, v56, v61 op_sel_hi:[0,1]\n"
      "v_mfma_f32_16x16x32_f16 v[32:35], v[192:195], v[20:23], v[32:35]\nv_pk_mul_f16 v26, v58, v60 op_sel_hi:[0,1]\nv_pk_mul_f16 v27, v58, v61 op_sel_hi:[0,1]\n"
      "v_mfma_f32_16x16x32_f16 v[36:39], v[196:199], v[20:23], v[36:39]\nv_pk_mul_f16 v28, v58, v58 op_sel_hi:[0,1]\nds_read_b64_tr_b16 v[62:63], v237 offset:11520\n"
      "v_mfma_f32_16x16x32_f16 v[36:39], v[184:187], v[16:19], v[36:39]\nv_pk_mul_f16 v29, v58, v57 op_sel_hi:[0,1]\nds_read_b64_tr_b16 v[60:61], v237 offset:11392\n"
      "v_mfma_f32_16x16x32_f16 v[244:247], v[188:191], v[16:19], v[244:247]\nv_pk_mul_f16 v30, v56, v58 op_sel_hi:[0,1]\nv_mfma_f32_16x16x32_f16 v[244:247], v[200:203], v[20:23], v[244:247]\n"
      "v_pk_mul_f16 v31, v56, v59 op_sel_hi:[0,1]\nv_smfmac_f32_16x16x64_f16 v[44:47], v[204:207], v[16:23], v242\nds_read_b64_tr_b16 v[56:57], v236 offset:11136\n"
      "ds_read_b64_tr_b16 v[58:59], v237 offset:11264\nv_smfmac_f32_16x16x64_f16 v[44:47], v[232:235], v[24:31], v242\nv_pk_mul_f16 v16, v68, s48 op_sel:[0,1]\n"
      "v_pk_mul_f16 v17, v69, s48 op_sel:[0,1]\nv_mfma_f32_16x16x32_f16 v[32:35], v[208:211], v[24:27], v[32:35]\nv_pk_mul_f16 v18, v70, s48 op_sel:[0,1]\nv_pk_mul_f16 v19, v71, s48 op_sel:[0,1]\n"
      "v_mfma_f32_16x16x32_f16 v[32:35], v[220:223], v[28:31], v[32:35]\nv_pk_mul_f16 v20, v68, v70\nv_pk_mul_f16 v21, v69, v71\nv_mfma_f32_16x16x32_f16 v[36:39], v[224:227], v[28:31], v[36:39]\n"
      "v_add_f32 v44, v44, v45\nv_add_f32 v46, v46, v47\nv_mfma_f32_16x16x32_f16 v[36:39], v[212:215], v[24:27], v[36:39]\nv_mul_f32 v55, v44, v54\nv_mul_f32 v55, v55, v46\n"
      "ds_read_b128 v[44:47], v248 offset:48\nv_mfma_f32_16x16x32_f16 v[244:247], v[216:219], v[24:27], v[244:247]\ns_waitcnt lgkmcnt(1)\nv_pk_mul_f16 v22, v56, v56 op_sel_hi:[0,1]\n"
      "v_pk_mul_f16 v23, v56, v57 op_sel_hi:[0,1]\nv_mfma_f32_16x16x32_f16 v[244:247], v[228:231], v[28:31], v[244:247]\ns_cmp_lt_u32 s45, s53\nv_mul_f32 v32, v32, v48\nv_mul_f32 v33, v33, v49\n"
      "v_mul_f32 v34, v34, v50\ns_cbranch_scc0 .Lc_s5\n"
      ".Lc_u6:\nv_mfma_f32_16x16x32_f16 v[40:43], v[76:79], v[16:19], 0\nds_read_b32 v4, v248\nv_fmac_f32 v55, v32, v36\nv_mul_f32 v35, v35, v51\nv_fmac_f32 v55, v33, v37\n"
      "v_mfma_f32_16x16x32_f16 v[40:43], v[88:91], v[20:23], v[40:43]\nv_fmac_f32 v55, v34, v38\nv_fmac_f32 v55, v35, v39\ns_add_u32 s45, s45, 2\ns_cmp_ge_u32 s45, s46\ns_cbranch_scc0 .Lc_b6\n"
      "s_swappc_b64 s[4:5], s[34:35]\n"
      ".Lc_b6:\nv_mfma_f32_16x16x32_f16 v[36:39], v[84:87], v[20:23], 0\nv_pk_mul_f16 v24, v66, v66 op_sel_hi:[0,1]\nv_pk_mul_f16 v25, v66, v57 op_sel_hi:[0,1]\n"
      "v_mfma_f32_16x16x32_f16 v[36:39], v[72:75], v[16:19], v[36:39]\nv_pk_mul_f16 v26, v56, v66 op_sel_hi:[0,1]\nv_pk_mul_f16 v27, v56, v67 op_sel_hi:[0,1]\ns_setprio 0\n"
      "v_mfma_f32_16x16x32_f16 v[32:35], v[80:83], v[20:23], 0\nv_pk_mul_f16 v28, v58, v66 op_sel_hi:[0,1]\nv_pk_mul_f16 v29, v58, v67 op_sel_hi:[0,1]\ns_waitcnt lgkmcnt(0)\n"
      "v_smfmac_f32_16x16x64_f16 v[44:47], v[92:95], v[16:23], v242\nv_pk_mul_f16 v30, v60, v66 op_sel_hi:[0,1]\nv_pk_mul_f16 v31, v60, v67 op_sel_hi:[0,1]\n"
      "v_mfma_f32_16x16x32_f16 v[32:35], v[96:99], v[24:27], v[32:35]\nv_pk_mul_f16 v16, v62, v66 op_sel_hi:[0,1]\nv_pk_mul_f16 v17, v62, v67 op_sel_hi:[0,1]\ns_setprio 1\n"
      "v_mfma_f32_16x16x32_f16 v[32:35], v[108:111], v[28:31], v[32:35]\nv_pk_mul_f16 v18, v64, v66 op_sel_hi:[0,1]\nv_mul_f32 v244, v244, v52\n"
      "v_mfma_f32_16x16x32_f16 v[36:39], v[112:115], v[28:31], v[36:39]\nv_pk_mul_f16 v19, v64, v67 op_sel_hi:[0,1]\nv_mul_f32 v245, v245, v53\n"
      "v_mfma_f32_16x16x32_f16 v[36:39], v[100:103], v[24:27], v[36:39]\nv_pk_mul_f16 v20, v64, v64 op_sel_hi:[0,1]\nv_fmac_f32 v55, v244, v246\n"
      "v_mfma_f32_16x16x32_f16 v[40:43], v[104:107], v[24:27], v[40:43]\nv_pk_mul_f16 v21, v64, v57 op_sel_hi:[0,1]\nv_fmac_f32 v55, v245, v247\n"
      "v_mfma_f32_16x16x32_f16 v[40:43], v[116:119], v[28:31], v[40:43]\nv_pk_mul_f16 v22, v56, v64 op_sel_hi:[0,1]\nv_readfirstlane_b32 s44, v4\n"
      "v_smfmac_f32_16x16x64_f16 v[44:47], v[120:123], v[24:31], v242\nv_pk_mul_f16 v23, v56, v65 op_sel_hi:[0,1]\nv_mfma_f32_16x16x32_f16 v[32:35], v[124:127], v[16:19], v[32:35]\n"
      "v_pk_mul_f16 v24, v58, v64 op_sel_hi:[0,1]\nv_pk_mul_f16 v25, v58, v65 op_sel_hi:[0,1]\ns_setprio 2\nds_read_b64_tr_b16 v[70:71], v239 offset:12992\n"
      "v_mfma_f32_16x16x32_f16 v[32:35], v[136:139], v[20:23], v[32:35]\nv_pk_mul_f16 v26, v60, v64 op_sel_hi:[0,1]\nds_read_b64_tr_b16 v[68:69], v238 offset:12992\n"
      "v_mfma_f32_16x16x32_f16 v[36:39], v[140:143], v[20:23], v[36:39]\nv_pk_mul_f16 v27, v60, v65 op_sel_hi:[0,1]\nds_read_b64_tr_b16 v[66:67], v237 offset:13632\n"
      "v_mfma_f32_16x16x32_f16 v[36:39], v[128:131], v[16:19], v[36:39]\nv_pk_mul_f16 v28, v62, v64 op_sel_hi:[0,1]\nv_add_f32_dpp v55, v55, v55 quad_perm:[1,0,3,2] row_mask:0xf bank_mask:0xf\n"
      "v_mfma_f32_16x16x32_f16 v[40:43], v[132:135], v[16:19], v[40:43]\nv_pk_mul_f16 v29, v62, v65 op_sel_hi:[0,1]\nv_add_f32_dpp v55, v55, v55 quad_perm:[2,3,0,1] row_mask:0xf bank_mask:0xf\n"
      "v_mfma_f32_16x16x32_f16 v[40:43], v[144:147], v[20:23], v[40:43]\nv_pk_mul_f16 v30, v62, v62 op_sel_hi:[0,1]\nv_add_f32_dpp v55, v55, v55 row_half_mirror row_mask:0xf bank_mask:0xf\n"
      "v_smfmac_f32_16x16x64_f16 v[44:47], v[148:151], v[16:23], v242\nv_pk_mul_f16 v31, v62, v57 op_sel_hi:[0,1]\nv_add_f32_dpp v55, v55, v55 row_mirror row_mask:0xf bank_mask:0xf\n"
      "v_mfma_f32_16x16x32_f16 v[32:35], v[152:155], v[24:27], v[32:35]\nv_pk_mul_f16 v16, v56, v62 op_sel_hi:[0,1]\nv_pk_mul_f16 v17, v56, v63 op_sel_hi:[0,1]\nds_write_b32 v243, v55 offset:640\n"
      "v_mfma_f32_16x16x32_f16 v[32:35], v[164:167], v[28:31], v[32:35]\nv_pk_mul_f16 v18, v58, v62 op_sel_hi:[0,1]\nds_read_b64_tr_b16 v[64:65], v237 offset:13504\n"
      "v_mfma_f32_16x16x32_f16 v[36:39], v[168:171], v[28:31], v[36:39]\nv_pk_mul_f16 v19, v58, v63 op_sel_hi:[0,1]\nv_mfma_f32_16x16x32_f16 v[36:39], v[156:159], v[24:27], v[36:39]\n"
      "v_pk_mul_f16 v20, v60, v62 op_sel_hi:[0,1]\ns_setprio 3\ns_ff0_i32_b32 s53, s44\nv_mfma_f32_16x16x32_f16 v[40:43], v[160:163], v[24:27], v[40:43]\n"
      "v_pk_mul_f16 v21, v60, v63 op_sel_hi:[0,1]\ns_waitcnt lgkmcnt(1)\nv_readlane_b32 s48, v69, 48\nv_mfma_f32_16x16x32_f16 v[40:43], v[172:175], v[28:31], v[40:43]\n"
      "v_pk_mul_f16 v22, v60, v60 op_sel_hi:[0,1]\ns_add_u32 s53, s53, s64\nv_smfmac_f32_16x16x64_f16 v[44:47], v[176:179], v[24:31], v242\nv_pk_mul_f16 v23, v60, v57 op_sel_hi:[0,1]\n"
      "v_mfma_f32_16x16x32_f16 v[32:35], v[180:183], v[16:19], v[32:35]\nv_pk_mul_f16 v24, v56, v60 op_sel_hi:[0,1]\nv_pk_mul_f16 v25, v56, v61 op_sel_hi:[0,1]\n"
      "v_mfma_f32_16x16x32_f16 v[32:35], v[192:195], v[20:23], v[32:35]\nv_pk_mul_f16 v26, v58, v60 op_sel_hi:[0,1]\nv_pk_mul_f16 v27, v58, v61 op_sel_hi:[0,1]\n"
      "v_mfma_f32_16x16x32_f16 v[36:39], v[196:199], v[20:23], v[36:39]\nv_pk_mul_f16 v28, v58, v58 op_sel_hi:[0,1]\nds_read_b64_tr_b16 v[62:63], v237 offset:13376\n"
      "v_mfma_f32_16x16x32_f16 v[36:39], v[184:187], v[16:19], v[36:39]\nv_pk_mul_f16 v29, v58, v57 op_sel_hi:[0,1]\nds_read_b64_tr_b16 v[60:61], v237 offset:13248\n"
      "v_mfma_f32_16x16x32_f16 v[40:43], v[188:191], v[16:19], v[40:43]\nv_pk_mul_f16 v30, v56, v58 op_sel_hi:[0,1]\nv_mfma_f32_16x16x32_f16 v[40:43], v[200:203], v[20:23], v[40:43]\n"
      "v_pk_mul_f16 v31, v56, v59 op_sel_hi:[0,1]\nv_smfmac_f32_16x16x64_f16 v[44:47], v[204:207], v[16:23], v242\nds_read_b64_tr_b16 v[56:57], v236 offset:12992\n"
      "ds_read_b64_tr_b16 v[58:59], v237 offset:13120\nv_smfmac_f32_16x16x64_f16 v[44:47], v[232:235], v[24:31], v242\nv_pk_mul_f16 v16, v68, s48 op_sel:[0,1]\n"
      "v_pk_mul_f16 v17, v69, s48 op_sel:[0,1]\nv_mfma_f32_16x16x32_f16 v[32:35], v[208:211], v[24:27], v[32:35]\nv_pk_mul_f16 v18, v70, s48 op_sel:[0,1]\nv_pk_mul_f16 v19, v71, s48 op_sel:[0,1]\n"
      "v_mfma_f32_16x16x32_f16 v[32:35], v[220:223], v[28:31], v[32:35]\nv_pk_mul_f16 v20, v68, v70\nv_pk_mul_f16 v21, v69, v71\nv_mfma_f32_16x16x32_f16 v[36:39], v[224:227], v[28:31], v[36:39]\n"
      "v_add_f32 v44, v44, v45\nv_add_f32 v46, v46, v47\nv_mfma_f32_16x16x32_f16 v[36:39], v[212:215], v[24:27], v[36:39]\nv_mul_f32 v55, v44, v54\nv_mul_f32 v55, v55, v46\n"
      "ds_read_b128 v[44:47], v248 offset:48\nv_mfma_f32_16x16x32_f16 v[40:43], v[216:219], v[24:27], v[40:43]\ns_waitcnt lgkmcnt(1)\nv_pk_mul_f16 v22, v56, v56 op_sel_hi:[0,1]\n"
      "v_pk_mul_f16 v23, v56, v57 op_sel_hi:[0,1]\nv_mfma_f32_16x16x32_f16 v[40:43], v[228:231], v[28:31], v[40:43]\ns_cmp_lt_u32 s45, s53\nv_mul_f32 v32, v32, v48\nv_mul_f32 v33, v33, v49\n"
      "v_mul_f32 v34, v34, v50\ns_cbranch_scc0 .Lc_s6\n"
      ".Lc_u7:\nv_mfma_f32_16x16x32_f16 v[244:247], v[76:79], v[16:19], 0\nds_read_b32 v4, v248\nv_fmac_f32 v55, v32, v36\nv_mul_f32 v35, v35, v51\nv_fmac_f32 v55, v33, v37\n"
      "v_mfma_f32_16x16x32_f16 v[244:247], v[88:91], v[20:23], v[244:247]\nv_fmac_f32 v55, v34, v38\nv_fmac_f32 v55, v35, v39\ns_add_u32 s45, s45, 2\ns_cmp_ge_u32 s45, s46\ns_cbranch_scc0 .Lc_b7\n"
      "s_swappc_b64 s[4:5], s[34:35]\n"
      ".Lc_b7:\nv_mfma_f32_16x16x32_f16 v[36:39], v[84:87], v[20:23], 0\nv_pk_mul_f16 v24, v66, v66 op_sel_hi:[0,1]\nv_pk_mul_f16 v25, v66, v57 op_sel_hi:[0,1]\n"
      "v_mfma_f32_16x16x32_f16 v[36:39], v[72:75], v[16:19], v[36:39]\nv_pk_mul_f16 v26, v56, v66 op_sel_hi:[0,1]\nv_pk_mul_f16 v27, v56, v67 op_sel_hi:[0,1]\ns_setprio 0\n"
      "v_mfma_f32_16x16x32_f16 v[32:35], v[80:83], v[20:23], 0\nv_pk_mul_f16 v28, v58, v66 op_sel_hi:[0,1]\nv_pk_mul_f16 v29, v58, v67 op_sel_hi:[0,1]\ns_waitcnt lgkmcnt(0)\n"
      "v_smfmac_f32_16x16x64_f16 v[44:47], v[92:95], v[16:23], v242\nv_pk_mul_f16 v30, v60, v66 op_sel_hi:[0,1]\nv_pk_mul_f16 v31, v60, v67 op_sel_hi:[0,1]\n"
      "v_mfma_f32_16x16x32_f16 v[32:35], v[96:99], v[24:27], v[32:35]\nv_pk_mul_f16 v16, v62, v66 op_sel_hi:[0,1]\nv_pk_mul_f16 v17, v62, v67 op_sel_hi:[0,1]\ns_setprio 1\n"
      "v_mfma_f32_16x16x32_f16 v[32:35], v[108:111], v[28:31], v[32:35]\nv_pk_mul_f16 v18, v64, v66 op_sel_hi:[0,1]\nv_mul_f32 v40, v40, v52\n"
      "v_mfma_f32_16x16x32_f16 v[36:39], v[112:115], v[28:31], v[36:39]\nv_pk_mul_f16 v19, v64, v67 op_sel_hi:[0,1]\nv_mul_f32 v41, v41, v53\n"
      "v_mfma_f32_16x16x32_f16 v[36:39], v[100:103], v[24:27], v[36:39]\nv_pk_mul_f16 v20, v64, v64 op_sel_hi:[0,1]\nv_fmac_f32 v55, v40, v42\n"
      "v_mfma_f32_16x16x32_f16 v[244:247], v[104:107], v[24:27], v[244:247]\nv_pk_mul_f16 v21, v64, v57 op_sel_hi:[0,1]\nv_fmac_f32 v55, v41, v43\n"
      "v_mfma_f32_16x16x32_f16 v[244:247], v[116:119], v[28:31], v[244:247]\nv_pk_mul_f16 v22, v56, v64 op_sel_hi:[0,1]\nv_readfirstlane_b32 s44, v4\n"
      "v_smfmac_f32_16x16x64_f16 v[44:47], v[120:123], v[24:31], v242\nv_pk_mul_f16 v23, v56, v65 op_sel_hi:[0,1]\nv_mfma_f32_16x16x32_f16 v[32:35], v[124:127], v[16:19], v[32:35]\n"
      "v_pk_mul_f16 v24, v58, v64 op_sel_hi:[0,1]\nv_pk_mul_f16 v25, v58, v65 op_sel_hi:[0,1]\ns_setprio 2\nds_read_b64_tr_b16 v[70:71], v239 offset:14848\n"
      "v_mfma_f32_16x16x32_f16 v[32:35], v[136:139], v[20:23], v[32:35]\nv_pk_mul_f16 v26, v60, v64 op_sel_hi:[0,1]\nds_read_b64_tr_b16 v[68:69], v238 offset:14848\n"
      "v_mfma_f32_16x16x32_f16 v[36:39], v[140:143], v[20:23], v[36:39]\nv_pk_mul_f16 v27, v60, v65 op_sel_hi:[0,1]\nds_read_b64_tr_b16 v[66:67], v237 offset:15488\n"
      "v_mfma_f32_16x16x32_f16 v[36:39], v[128:131], v[16:19], v[36:39]\nv_pk_mul_f16 v28, v62, v64 op_sel_hi:[0,1]\nv_add_f32_dpp v55, v55, v55 quad_perm:[1,0,3,2] row_mask:0xf bank_mask:0xf\n"
      "v_mfma_f32_16x16x32_f16 v[244:247], v[132:135], v[16:19], v[244:247]\nv_pk_mul_f16 v29, v62, v65 op_sel_hi:[0,1]\nv_add_f32_dpp v55, v55, v55 quad_perm:[2,3,0,1] row_mask:0xf bank_mask:0xf\n"
      "v_mfma_f32_16x16x32_f16 v[244:247], v[144:147], v[20:23], v[244:247]\nv_pk_mul_f16 v30, v62, v62 op_sel_hi:[0,1]\nv_add_f32_dpp v55, v55, v55 row_half_mirror row_mask:0xf bank_mask:0xf\n"
      "v_smfmac_f32_16x16x64_f16 v[44:47], v[148:151], v[16:23], v242\nv_pk_mul_f16 v31, v62, v57 op_sel_hi:[0,1]\nv_add_f32_dpp v55, v55, v55 row_mirror row_mask:0xf bank_mask:0xf\n"
      "v_mfma_f32_16x16x32_f16 v[32:35], v[152:155], v[24:27], v[32:35]\nv_pk_mul_f16 v16, v56, v62 op_sel_hi:[0,1]\nv_pk_mul_f16 v17, v56, v63 op_sel_hi:[0,1]\nds_write_b32 v243, v55 offset:768\n"
      "v_mfma_f32_16x16x32_f16 v[32:35], v[164:167], v[28:31], v[32:35]\nv_pk_mul_f16 v18, v58, v62 op_sel_hi:[0,1]\nds_read_b64_tr_b16 v[64:65], v237 offset:15360\n"
      "v_mfma_f32_16x16x32_f16 v[36:39], v[168:171], v[28:31], v[36:39]\nv_pk_mul_f16 v19, v58, v63 op_sel_hi:[0,1]\nv_mfma_f32_16x16x32_f16 v[36:39], v[156:159], v[24:27], v[36:39]\n"
      "v_pk_mul_f16 v20, v60, v62 op_sel_hi:[0,1]\ns_setprio 3\ns_ff0_i32_b32 s53, s44\nv_mfma_f32_16x16x32_f16 v[244:247], v[160:163], v[24:27], v[244:247]\n"
      "v_pk_mul_f16 v21, v60, v63 op_sel_hi:[0,1]\ns_waitcnt lgkmcnt(1)\nv_readlane_b32 s48, v69, 48\nv_mfma_f32_16x16x32_f16 v[244:247], v[172:175], v[28:31], v[244:247]\n"
      "v_pk_mul_f16 v22, v60, v60 op_sel_hi:[0,1]\ns_add_u32 s53, s53, s64\nv_smfmac_f32_16x16x64_f16 v[44:47], v[176:179], v[24:31], v242\nv_pk_mul_f16 v23, v60, v57 op_sel_hi:[0,1]\n"
      "v_mfma_f32_16x16x32_f16 v[32:35], v[180:183], v[16:19], v[32:35]\nv_pk_mul_f16 v24, v56, v60 op_sel_hi:[0,1]\nv_pk_mul_f16 v25, v56, v61 op_sel_hi:[0,1]\n"
      "v_mfma_f32_16x16x32_f16 v[32:35], v[192:195], v[20:23], v[32:35]\nv_pk_mul_f16 v26, v58, v60 op_sel_hi:[0,1]\nv_pk_mul_f16 v27, v58, v61 op_sel_hi:[0,1]\n"
      "v_mfma_f32_16x16x32_f16 v[36:39], v[196:199], v[20:23], v[36:39]\nv_pk_mul_f16 v28, v58, v58 op_sel_hi:[0,1]\nds_read_b64_tr_b16 v[62:63], v237 offset:15232\n"
      "v_mfma_f32_16x16x32_f16 v[36:39], v[184:187], v[16:19], v[36:39]\nv_pk_mul_f16 v29, v58, v57 op_sel_hi:[0,1]\nds_read_b64_tr_b16 v[60:61], v237 offset:15104\n"
      "v_mfma_f32_16x16x32_f16 v[244:247], v[188:191], v[16:19], v[244:247]\nv_pk_mul_f16 v30, v56, v58 op_sel_hi:[0,1]\nv_mfma_f32_16x16x32_f16 v[244:247], v[200:203], v[20:23], v[244:247]\n"
      "v_pk_mul_f16 v31, v56, v59 op_sel_hi:[0,1]\nv_smfmac_f32_16x16x64_f16 v[44:47], v[204:207], v[16:23], v242\nds_read_b64_tr_b16 v[56:57], v236 offset:14848\n"
      "ds_read_b64_tr_b16 v[58:59], v237 offset:14976\nv_smfmac_f32_16x16x64_f16 v[44:47], v[232:235], v[24:31], v242\nv_pk_mul_f16 v16, v68, s48 op_sel:[0,1]\n"
      "v_pk_mul_f16 v17, v69, s48 op_sel:[0,1]\nv_mfma_f32_16x16x32_f16 v[32:35], v[208:211], v[24:27], v[32:35]\nv_pk_mul_f16 v18, v70, s48 op_sel:[0,1]\nv_pk_mul_f16 v19, v71, s48 op_sel:[0,1]\n"
      "v_mfma_f32_16x16x32_f16 v[32:35], v[220:223], v[28:31], v[32:35]\nv_pk_mul_f16 v20, v68, v70\nv_pk_mul_f16 v21, v69, v71\nv_mfma_f32_16x16x32_f16 v[36:39], v[224:227], v[28:31], v[36:39]\n"
      "v_add_f32 v44, v44, v45\nv_add_f32 v46, v46, v47\nv_mfma_f32_16x16x32_f16 v[36:39], v[212:215], v[24:27], v[36:39]\nv_mul_f32 v55, v44, v54\nv_mul_f32 v55, v55, v46\n"
      "ds_read_b128 v[44:47], v248 offset:48\nv_mfma_f32_16x16x32_f16 v[244:247], v[216:219], v[24:27], v[244:247]\ns_waitcnt lgkmcnt(1)\nv_pk_mul_f16 v22, v56, v56 op_sel_hi:[0,1]\n"
      "v_pk_mul_f16 v23, v56, v57 op_sel_hi:[0,1]\nv_mfma_f32_16x16x32_f16 v[244:247], v[228:231], v[28:31], v[244:247]\ns_cmp_lt_u32 s45, s53\nv_mul_f32 v32, v32, v48\nv_mul_f32 v33, v33, v49\n"
      "v_mul_f32 v34, v34, v50\ns_cbranch_scc0 .Lc_s7\n"
      ".Lc_u8:\nv_mfma_f32_16x16x32_f16 v[40:43], v[76:79], v[16:19], 0\nds_read_b32 v4, v248\nv_fmac_f32 v55, v32, v36\nv_mul_f32 v35, v35, v51\nv_fmac_f32 v55, v33, v37\n"
      "v_mfma_f32_16x16x32_f16 v[40:43], v[88:91], v[20:23], v[40:43]\nv_fmac_f32 v55, v34, v38\nv_fmac_f32 v55, v35, v39\ns_add_u32 s45, s45, 2\ns_cmp_ge_u32 s45, s46\ns_cbranch_scc0 .Lc_b8\n"
      "s_swappc_b64 s[4:5], s[34:35]\n"
      ".Lc_b8:\nv_mfma_f32_16x16x32_f16 v[36:39], v[84:87], v[20:23], 0\nv_pk_mul_f16 v24, v66, v66 op_sel_hi:[0,1]\nv_pk_mul_f16 v25, v66, v57 op_sel_hi:[0,1]\n"
      "v_mfma_f32_16x16x32_f16 v[36:39], v[72:75], v[16:19], v[36:39]\nv_pk_mul_f16 v26, v56, v66 op_sel_hi:[0,1]\nv_pk_mul_f16 v27, v56, v67 op_sel_hi:[0,1]\ns_setprio 0\n"
      "v_mfma_f32_16x16x32_f16 v[32:35], v[80:83], v[20:23], 0\nv_pk_mul_f16 v28, v58, v66 op_sel_hi:[0,1]\nv_pk_mul_f16 v29, v58, v67 op_sel_hi:[0,1]\ns_waitcnt lgkmcnt(0)\n"
      "v_smfmac_f32_16x16x64_f16 v[44:47], v[92:95], v[16:23], v242\nv_pk_mul_f16 v30, v60, v66 op_sel_hi:[0,1]\nv_pk_mul_f16 v31, v60, v67 op_sel_hi:[0,1]\n"
      "v_mfma_f32_16x16x32_f16 v[32:35], v[96:99], v[24:27], v[32:35]\nv_pk_mul_f16 v16, v62, v66 op_sel_hi:[0,1]\nv_pk_mul_f16 v17, v62, v67 op_sel_hi:[0,1]\ns_setprio 1\n"
      "v_mfma_f32_16x16x32_f16 v[32:35], v[108:111], v[28:31], v[32:35]\nv_pk_mul_f16 v18, v64, v66 op_sel_hi:[0,1]\nv_mul_f32 v244, v244, v52\n"
      "v_mfma_f32_16x16x32_f16 v[36:39], v[112:115], v[28:31], v[36:39]\nv_pk_mul_f16 v19, v64, v67 op_sel_hi:[0,1]\nv_mul_f32 v245, v245, v53\n"
      "v_mfma_f32_16x16x32_f16 v[36:39], v[100:103], v[24:27], v[36:39]\nv_pk_mul_f16 v20, v64, v64 op_sel_hi:[0,1]\nv_fmac_f32 v55, v244, v246\n"
      "v_mfma_f32_16x16x32_f16 v[40:43], v[104:107], v[24:27], v[40:43]\nv_pk_mul_f16 v21, v64, v57 op_sel_hi:[0,1]\nv_fmac_f32 v55, v245, v247\n"
      "v_mfma_f32_16x16x32_f16 v[40:43], v[116:119], v[28:31], v[40:43]\nv_pk_mul_f16 v22, v56, v64 op_sel_hi:[0,1]\nv_readfirstlane_b32 s44, v4\n"
      "v_smfmac_f32_16x16x64_f16 v[44:47], v[120:123], v[24:31], v242\nv_pk_mul_f16 v23, v56, v65 op_sel_hi:[0,1]\nv_mfma_f32_16x16x32_f16 v[32:35], v[124:127], v[16:19], v[32:35]\n"
      "v_pk_mul_f16 v24, v58, v64 op_sel_hi:[0,1]\nv_pk_mul_f16 v25, v58, v65 op_sel_hi:[0,1]\ns_setprio 2\nds_read_b64_tr_b16 v[70:71], v239 offset:16704\n"
      "v_mfma_f32_16x16x32_f16 v[32:35], v[136:139], v[20:23], v[32:35]\nv_pk_mul_f16 v26, v60, v64 op_sel_hi:[0,1]\nds_read_b64_tr_b16 v[68:69], v238 offset:16704\n"
      "v_mfma_f32_16x16x32_f16 v[36:39], v[140:143], v[20:23], v[36:39]\nv_pk_mul_f16 v27, v60, v65 op_sel_hi:[0,1]\nds_read_b64_tr_b16 v[66:67], v237 offset:17344\n"
      "v_mfma_f32_16x16x32_f16 v[36:39], v[128:131], v[16:19], v[36:39]\nv_pk_mul_f16 v28, v62, v64 op_sel_hi:[0,1]\nv_add_f32_dpp v55, v55, v55 quad_perm:[1,0,3,2] row_mask:0xf bank_mask:0xf\n"
      "v_mfma_f32_16x16x32_f16 v[40:43], v[132:135], v[16:19], v[40:43]\nv_pk_mul_f16 v29, v62, v65 op_sel_hi:[0,1]\nv_add_f32_dpp v55, v55, v55 quad_perm:[2,3,0,1] row_mask:0xf bank_mask:0xf\n"
      "v_mfma_f32_16x16x32_f16 v[40:43], v[144:147], v[20:23], v[40:43]\nv_pk_mul_f16 v30, v62, v62 op_sel_hi:[0,1]\nv_add_f32_dpp v55, v55, v55 row_half_mirror row_mask:0xf bank_mask:0xf\n"
      "v_smfmac_f32_16x16x64_f16 v[44:47], v[148:151], v[16:23], v242\nv_pk_mul_f16 v31, v62, v57 op_sel_hi:[0,1]\nv_add_f32_dpp v55, v55, v55 row_mirror row_mask:0xf bank_mask:0xf\n"
      "v_mfma_f32_16x16x32_f16 v[32:35], v[152:155], v[24:27], v[32:35]\nv_pk_mul_f16 v16, v56, v62 op_sel_hi:[0,1]\nv_pk_mul_f16 v17, v56, v63 op_sel_hi:[0,1]\nds_write_b32 v243, v55 offset:896\n"
      "v_mfma_f32_16x16x32_f16 v[32:35], v[164:167], v[28:31], v[32:35]\nv_pk_mul_f16 v18, v58, v62 op_sel_hi:[0,1]\nds_read_b64_tr_b16 v[64:65], v237 offset:17216\n"
      "v_mfma_f32_16x16x32_f16 v[36:39], v[168:171], v[28:31], v[36:39]\nv_pk_mul_f16 v19, v58, v63 op_sel_hi:[0,1]\nv_mfma_f32_16x16x32_f16 v[36:39], v[156:159], v[24:27], v[36:39]\n"
      "v_pk_mul_f16 v20, v60, v62 op_sel_hi:[0,1]\ns_setprio 3\ns_ff0_i32_b32 s53, s44\nv_mfma_f32_16x16x32_f16 v[40:43], v[160:163], v[24:27], v[40:43]\n"
      "v_pk_mul_f16 v21, v60, v63 op_sel_hi:[0,1]\ns_waitcnt lgkmcnt(1)\nv_readlane_b32 s48, v69, 48\nv_mfma_f32_16x16x32_f16 v[40:43], v[172:175], v[28:31], v[40:43]\n"
      "v_pk_mul_f16 v22, v60, v60 op_sel_hi:[0,1]\ns_add_u32 s53, s53, s64\nv_smfmac_f32_16x16x64_f16 v[44:47], v[176:179], v[24:31], v242\nv_pk_mul_f16 v23, v60, v57 op_sel_hi:[0,1]\n"
      "v_mfma_f32_16x16x32_f16 v[32:35], v[180:183], v[16:19], v[32:35]\nv_pk_mul_f16 v24, v56, v60 op_sel_hi:[0,1]\nv_pk_mul_f16 v25, v56, v61 op_sel_hi:[0,1]\n"
      "v_mfma_f32_16x16x32_f16 v[32:35], v[192:195], v[20:23], v[32:35]\nv_pk_mul_f16 v26, v58, v60 op_sel_hi:[0,1]\nv_pk_mul_f16 v27, v58, v61 op_sel_hi:[0,1]\n"
      "v_mfma_f32_16x16x32_f16 v[36:39], v[196:199], v[20:23], v[36:39]\nv_pk_mul_f16 v28, v58, v58 op_sel_hi:[0,1]\nds_read_b64_tr_b16 v[62:63], v237 offset:17088\n"
      "v_mfma_f32_16x16x32_f16 v[36:39], v[184:187], v[16:19], v[36:39]\nv_pk_mul_f16 v29, v58, v57 op_sel_hi:[0,1]\nds_read_b64_tr_b16 v[60:61], v237 offset:16960\n"
      "v_mfma_f32_16x16x32_f16 v[40:43], v[188:191], v[16:19], v[40:43]\nv_pk_mul_f16 v30, v56, v58 op_sel_hi:[0,1]\nv_mfma_f32_16x16x32_f16 v[40:43], v[200:203], v[20:23], v[40:43]\n"
      "v_pk_mul_f16 v31, v56, v59 op_sel_hi:[0,1]\nv_smfmac_f32_16x16x64_f16 v[44:47], v[204:207], v[16:23], v242\nds_read_b64_tr_b16 v[56:57], v236 offset:16704\n"
      "ds_read_b64_tr_b16 v[58:59], v237 offset:16832\nv_smfmac_f32_16x16x64_f16 v[44:47], v[232:235], v[24:31], v242\nv_pk_mul_f16 v16, v68, s48 op_sel:[0,1]\n"
      "v_pk_mul_f16 v17, v69, s48 op_sel:[0,1]\nv_mfma_f32_16x16x32_f16 v[32:35], v[208:211], v[24:27], v[32:35]\nv_pk_mul_f16 v18, v70, s48 op_sel:[0,1]\nv_pk_mul_f16 v19, v71, s48 op_sel:[0,1]\n"
      "v_mfma_f32_16x16x32_f16 v[32:35], v[220:223], v[28:31], v[32:35]\nv_pk_mul_f16 v20, v68, v70\nv_pk_mul_f16 v21, v69, v71\nv_mfma_f32_16x16x32_f16 v[36:39], v[224:227], v[28:31], v[36:39]\n"
      "v_add_f32 v44, v44, v45\nv_add_f32 v46, v46, v47\nv_mfma_f32_16x16x32_f16 v[36:39], v[212:215], v[24:27], v[36:39]\nv_mul_f32 v55, v44, v54\nv_mul_f32 v55, v55, v46\n"
      "ds_read_b128 v[44:47], v248 offset:48\nv_mfma_f32_16x16x32_f16 v[40:43], v[216:219], v[24:27], v[40:43]\ns_waitcnt lgkmcnt(1)\nv_pk_mul_f16 v22, v56, v56 op_sel_hi:[0,1]\n"
      "v_pk_mul_f16 v23, v56, v57 op_sel_hi:[0,1]\nv_mfma_f32_16x16x32_f16 v[40:43], v[228:231], v[28:31], v[40:43]\ns_cmp_lt_u32 s45, s53\nv_mul_f32 v32, v32, v48\nv_mul_f32 v33, v33, v49\n"
      "v_mul_f32 v34, v34, v50\ns_cbranch_scc0 .Lc_s8\n"
      ".Lc_u9:\nv_mfma_f32_16x16x32_f16 v[244:247], v[76:79], v[16:19], 0\nds_read_b32 v4, v248\nv_fmac_f32 v55, v32, v36\nv_mul_f32 v35, v35, v51\nv_fmac_f32 v55, v33, v37\n"
      "v_mfma_f32_16x16x32_f16 v[244:247], v[88:91], v[20:23], v[244:247]\nv_fmac_f32 v55, v34, v38\nv_fmac_f32 v55, v35, v39\ns_add_u32 s45, s45, 2\ns_cmp_ge_u32 s45, s46\ns_cbranch_scc0 .Lc_b9\n"
      "s_swappc_b64 s[4:5], s[34:35]\n"
      ".Lc_b9:\nv_mfma_f32_16x16x32_f16 v[36:39], v[84:87], v[20:23], 0\nv_pk_mul_f16 v24, v66, v66 op_sel_hi:[0,1]\nv_pk_mul_f16 v25, v66, v57 op_sel_hi:[0,1]\n"
      "v_mfma_f32_16x16x32_f16 v[36:39], v[72:75], v[16:19], v[36:39]\nv_pk_mul_f16 v26, v56, v66 op_sel_hi:[0,1]\nv_pk_mul_f16 v27, v56, v67 op_sel_hi:[0,1]\ns_setprio 0\n"
      "v_mfma_f32_16x16x32_f16 v[32:35], v[80:83], v[20:23], 0\nv_pk_mul_f16 v28, v58, v66 op_sel_hi:[0,1]\nv_pk_mul_f16 v29, v58, v67 op_sel_hi:[0,1]\ns_waitcnt lgkmcnt(0)\n"
      "v_smfmac_f32_16x16x64_f16 v[44:47], v[92:95], v[16:23], v242\nv_pk_mul_f16 v30, v60, v66 op_sel_hi:[0,1]\nv_pk_mul_f16 v31, v60, v67 op_sel_hi:[0,1]\n"
      "v_mfma_f32_16x16x32_f16 v[32:35], v[96:99], v[24:27], v[32:35]\nv_pk_mul_f16 v16, v62, v66 op_sel_hi:[0,1]\nv_pk_mul_f16 v17, v62, v67 op_sel_hi:[0,1]\ns_setprio 1\n"
      "v_mfma_f32_16x16x32_f16 v[32:35], v[108:111], v[28:31], v[32:35]\nv_pk_mul_f16 v18, v64, v66 op_sel_hi:[0,1]\nv_mul_f32 v40, v40, v52\n"
      "v_mfma_f32_16x16x32_f16 v[36:39], v[112:115], v[28:31], v[36:39]\nv_pk_mul_f16 v19, v64, v67 op_sel_hi:[0,1]\nv_mul_f32 v41, v41, v53\n"
      "v_mfma_f32_16x16x32_f16 v[36:39], v[100:103], v[24:27], v[36:39]\nv_pk_mul_f16 v20, v64, v64 op_sel_hi:[0,1]\nv_fmac_f32 v55, v40, v42\n"
      "v_mfma_f32_16x16x32_f16 v[244:247], v[104:107], v[24:27], v[244:247]\nv_pk_mul_f16 v21, v64, v57 op_sel_hi:[0,1]\nv_fmac_f32 v55, v41, v43\n"
      "v_mfma_f32_16x16x32_f16 v[244:247], v[116:119], v[28:31], v[244:247]\nv_pk_mul_f16 v22, v56, v64 op_sel_hi:[0,1]\nv_readfirstlane_b32 s44, v4\n"
      "v_smfmac_f32_16x16x64_f16 v[44:47], v[120:123], v[24:31], v242\nv_pk_mul_f16 v23, v56, v65 op_sel_hi:[0,1]\nv_mfma_f32_16x16x32_f16 v[32:35], v[124:127], v[16:19], v[32:35]\n"
      "v_pk_mul_f16 v24, v58, v64 op_sel_hi:[0,1]\nv_pk_mul_f16 v25, v58, v65 op_sel_hi:[0,1]\ns_setprio 2\nds_read_b64_tr_b16 v[70:71], v239 offset:18560\n"
      "v_mfma_f32_16x16x32_f16 v[32:35], v[136:139], v[20:23], v[32:35]\nv_pk_mul_f16 v26, v60, v64 op_sel_hi:[0,1]\nds_read_b64_tr_b16 v[68:69], v238 offset:18560\n"
      "v_mfma_f32_16x16x32_f16 v[36:39], v[140:143], v[20:23], v[36:39]\nv_pk_mul_f16 v27, v60, v65 op_sel_hi:[0,1]\nds_read_b64_tr_b16 v[66:67], v237 offset:19200\n"
      "v_mfma_f32_16x16x32_f16 v[36:39], v[128:131], v[16:19], v[36:39]\nv_pk_mul_f16 v28, v62, v64 op_sel_hi:[0,1]\nv_add_f32_dpp v55, v55, v55 quad_perm:[1,0,3,2] row_mask:0xf bank_mask:0xf\n"
      "v_mfma_f32_16x16x32_f16 v[244:247], v[132:135], v[16:19], v[244:247]\nv_pk_mul_f16 v29, v62, v65 op_sel_hi:[0,1]\nv_add_f32_dpp v55, v55, v55 quad_perm:[2,3,0,1] row_mask:0xf bank_mask:0xf\n"
      "v_mfma_f32_16x16x32_f16 v[244:247], v[144:147], v[20:23], v[244:247]\nv_pk_mul_f16 v30, v62, v62 op_sel_hi:[0,1]\nv_add_f32_dpp v55, v55, v55 row_half_mirror row_mask:0xf bank_mask:0xf\n"
      "v_smfmac_f32_16x16x64_f16 v[44:47], v[148:151], v[16:23], v242\nv_pk_mul_f16 v31, v62, v57 op_sel_hi:[0,1]\nv_add_f32_dpp v55, v55, v55 row_mirror row_mask:0xf bank_mask:0xf\n"
      "v_mfma_f32_16x16x32_f16 v[32:35], v[152:155], v[24:27], v[32:35]\nv_pk_mul_f16 v16, v56, v62 op_sel_hi:[0,1]\nv_pk_mul_f16 v17, v56, v63 op_sel_hi:[0,1]\nds_write_b32 v243, v55 offset:1024\n"
      "v_mfma_f32_16x16x32_f16 v[32:35], v[164:167], v[28:31], v[32:35]\nv_pk_mul_f16 v18, v58, v62 op_sel_hi:[0,1]\nds_read_b64_tr_b16 v[64:65], v237 offset:19072\n"
      "v_mfma_f32_16x16x32_f16 v[36:39], v[168:171], v[28:31], v[36:39]\nv_pk_mul_f16 v19, v58, v63 op_sel_hi:[0,1]\nv_mfma_f32_16x16x32_f16 v[36:39], v[156:159], v[24:27], v[36:39]\n"
      "v_pk_mul_f16 v20, v60, v62 op_sel_hi:[0,1]\ns_setprio 3\ns_ff0_i32_b32 s53, s44\nv_mfma_f32_16x16x32_f16 v[244:247], v[160:163], v[24:27], v[244:247]\n"
      "v_pk_mul_f16 v21, v60, v63 op_sel_hi:[0,1]\ns_waitcnt lgkmcnt(1)\nv_readlane_b32 s48, v69, 48\nv_mfma_f32_16x16x32_f16 v[244:247], v[172:175], v[28:31], v[244:247]\n"
      "v_pk_mul_f16 v22, v60, v60 op_sel_hi:[0,1]\ns_add_u32 s53, s53, s64\nv_smfmac_f32_16x16x64_f16 v[44:47], v[176:179], v[24:31], v242\nv_pk_mul_f16 v23, v60, v57 op_sel_hi:[0,1]\n"
      "v_mfma_f32_16x16x32_f16 v[32:35], v[180:183], v[16:19], v[32:35]\nv_pk_mul_f16 v24, v56, v60 op_sel_hi:[0,1]\nv_pk_mul_f16 v25, v56, v61 op_sel_hi:[0,1]\n"
      "v_mfma_f32_16x16x32_f16 v[32:35], v[192:195], v[20:23], v[32:35]\nv_pk_mul_f16 v26, v58, v60 op_sel_hi:[0,1]\nv_pk_mul_f16 v27, v58, v61 op_sel_hi:[0,1]\n"
      "v_mfma_f32_16x16x32_f16 v[36:39], v[196:199], v[20:23], v[36:39]\nv_pk_mul_f16 v28, v58, v58 op_sel_hi:[0,1]\nds_read_b64_tr_b16 v[62:63], v237 offset:18944\n"
      "v_mfma_f32_16x16x32_f16 v[36:39], v[184:187], v[16:19], v[36:39]\nv_pk_mul_f16 v29, v58, v57 op_sel_hi:[0,1]\nds_read_b64_tr_b16 v[60:61], v237 offset:18816\n"
      "v_mfma_f32_16x16x32_f16 v[244:247], v[188:191], v[16:19], v[244:247]\nv_pk_mul_f16 v30, v56, v58 op_sel_hi:[0,1]\nv_mfma_f32_16x16x32_f16 v[244:247], v[200:203], v[20:23], v[244:247]\n"
      "v_pk_mul_f16 v31, v56, v59 op_sel_hi:[0,1]\nv_smfmac_f32_16x16x64_f16 v[44:47], v[204:207], v[16:23], v242\nds_read_b64_tr_b16 v[56:57], v236 offset:18560\n"
      "ds_read_b64_tr_b16 v[58:59], v237 offset:18688\nv_smfmac_f32_16x16x64_f16 v[44:47], v[232:235], v[24:31], v242\nv_pk_mul_f16 v16, v68, s48 op_sel:[0,1]\n"
      "v_pk_mul_f16 v17, v69, s48 op_sel:[0,1]\nv_mfma_f32_16x16x32_f16 v[32:35], v[208:211], v[24:27], v[32:35]\nv_pk_mul_f16 v18, v70, s48 op_sel:[0,1]\nv_pk_mul_f16 v19, v71, s48 op_sel:[0,1]\n"
      "v_mfma_f32_16x16x32_f16 v[32:35], v[220:223], v[28:31], v[32:35]\nv_pk_mul_f16 v20, v68, v70\nv_pk_mul_f16 v21, v69, v71\nv_mfma_f32_16x16x32_f16 v[36:39], v[224:227], v[28:31], v[36:39]\n"
      "v_add_f32 v44, v44, v45\nv_add_f32 v46, v46, v47\nv_mfma_f32_16x16x32_f16 v[36:39], v[212:215], v[24:27], v[36:39]\nv_mul_f32 v55, v44, v54\nv_mul_f32 v55, v55, v46\n"
      "ds_read_b128 v[44:47], v248 offset:48\nv_mfma_f32_16x16x32_f16 v[244:247], v[216:219], v[24:27], v[244:247]\ns_waitcnt lgkmcnt(1)\nv_pk_mul_f16 v22, v56, v56 op_sel_hi:[0,1]\n"
      "v_pk_mul_f16 v23, v56, v57 op_sel_hi:[0,1]\nv_mfma_f32_16x16x32_f16 v[244:247], v[228:231], v[28:31], v[244:247]\ns_cmp_lt_u32 s45, s53\nv_mul_f32 v32, v32, v48\nv_mul_f32 v33, v33, v49\n"
      "v_mul_f32 v34, v34, v50\ns_cbranch_scc0 .Lc_s9\n"
      ".Lc_u10:\nv_mfma_f32_16x16x32_f16 v[40:43], v[76:79], v[16:19], 0\nds_read_b32 v4, v248\nv_fmac_f32 v55, v32, v36\nv_mul_f32 v35, v35, v51\nv_fmac_f32 v55, v33, v37\n"
      "v_mfma_f32_16x16x32_f16 v[40:43], v[88:91], v[20:23], v[40:43]\nv_fmac_f32 v55, v34, v38\nv_fmac_f32 v55, v35, v39\ns_add_u32 s45, s45, 2\ns_cmp_ge_u32 s45, s46\ns_cbranch_scc0 .Lc_b10\n"
      "s_swappc_b64 s[4:5], s[34:35]\n"
      ".Lc_b10:\nv_mfma_f32_16x16x32_f16 v[36:39], v[84:87], v[20:23], 0\nv_pk_mul_f16 v24, v66, v66 op_sel_hi:[0,1]\nv_pk_mul_f16 v25, v66, v57 op_sel_hi:[0,1]\n"
      "v_mfma_f32_16x16x32_f16 v[36:39], v[72:75], v[16:19], v[36:39]\nv_pk_mul_f16 v26, v56, v66 op_sel_hi:[0,1]\nv_pk_mul_f16 v27, v56, v67 op_sel_hi:[0,1]\ns_setprio 0\n"
      "v_mfma_f32_16x16x32_f16 v[32:35], v[80:83], v[20:23], 0\nv_pk_mul_f16 v28, v58, v66 op_sel_hi:[0,1]\nv_pk_mul_f16 v29, v58, v67 op_sel_hi:[0,1]\ns_waitcnt lgkmcnt(0)\n"
      "v_smfmac_f32_16x16x64_f16 v[44:47], v[92:95], v[16:23], v242\nv_pk_mul_f16 v30, v60, v66 op_sel_hi:[0,1]\nv_pk_mul_f16 v31, v60, v67 op_sel_hi:[0,1]\n"
      "v_mfma_f32_16x16x32_f16 v[32:35], v[96:99], v[24:27], v[32:35]\nv_pk_mul_f16 v16, v62, v66 op_sel_hi:[0,1]\nv_pk_mul_f16 v17, v62, v67 op_sel_hi:[0,1]\ns_setprio 1\n"
      "v_mfma_f32_16x16x32_f16 v[32:35], v[108:111], v[28:31], v[32:35]\nv_pk_mul_f16 v18, v64, v66 op_sel_hi:[0,1]\nv_mul_f32 v244, v244, v52\n"
      "v_mfma_f32_16x16x32_f16 v[36:39], v[112:115], v[28:31], v[36:39]\nv_pk_mul_f16 v19, v64, v67 op_sel_hi:[0,1]\nv_mul_f32 v245, v245, v53\n"
      "v_mfma_f32_16x16x32_f16 v[36:39], v[100:103], v[24:27], v[36:39]\nv_pk_mul_f16 v20, v64, v64 op_sel_hi:[0,1]\nv_fmac_f32 v55, v244, v246\n"
      "v_mfma_f32_16x16x32_f16 v[40:43], v[104:107], v[24:27], v[40:43]\nv_pk_mul_f16 v21, v64, v57 op_sel_hi:[0,1]\nv_fmac_f32 v55, v245, v247\n"
      "v_mfma_f32_16x16x32_f16 v[40:43], v[116:119], v[28:31], v[40:43]\nv_pk_mul_f16 v22, v56, v64 op_sel_hi:[0,1]\nv_readfirstlane_b32 s44, v4\n"
      "v_smfmac_f32_16x16x64_f16 v[44:47], v[120:123], v[24:31], v242\nv_pk_mul_f16 v23, v56, v65 op_sel_hi:[0,1]\nv_mfma_f32_16x16x32_f16 v[32:35], v[124:127], v[16:19], v[32:35]\n"
      "v_pk_mul_f16 v24, v58, v64 op_sel_hi:[0,1]\nv_pk_mul_f16 v25, v58, v65 op_sel_hi:[0,1]\ns_setprio 2\nv_mfma_f32_16x16x32_f16 v[32:35], v[136:139], v[20:23], v[32:35]\n"
      "v_pk_mul_f16 v26, v60, v64 op_sel_hi:[0,1]\nv_mfma_f32_16x16x32_f16 v[36:39], v[140:143], v[20:23], v[36:39]\nv_pk_mul_f16 v27, v60, v65 op_sel_hi:[0,1]\n"
      "v_mfma_f32_16x16x32_f16 v[36:39], v[128:131], v[16:19], v[36:39]\nv_pk_mul_f16 v28, v62, v64 op_sel_hi:[0,1]\nv_add_f32_dpp v55, v55, v55 quad_perm:[1,0,3,2] row_mask:0xf bank_mask:0xf\n"
      "v_mfma_f32_16x16x32_f16 v[40:43], v[132:135], v[16:19], v[40:43]\nv_pk_mul_f16 v29, v62, v65 op_sel_hi:[0,1]\nv_add_f32_dpp v55, v55, v55 quad_perm:[2,3,0,1] row_mask:0xf bank_mask:0xf\n"
      "v_mfma_f32_16x16x32_f16 v[40:43], v[144:147], v[20:23], v[40:43]\nv_pk_mul_f16 v30, v62, v62 op_sel_hi:[0,1]\nv_add_f32_dpp v55, v55, v55 row_half_mirror row_mask:0xf bank_mask:0xf\n"
      "v_smfmac_f32_16x16x64_f16 v[44:47], v[148:151], v[16:23], v242\nv_pk_mul_f16 v31, v62, v57 op_sel_hi:[0,1]\nv_add_f32_dpp v55, v55, v55 row_mirror row_mask:0xf bank_mask:0xf\n"
      "v_mfma_f32_16x16x32_f16 v[32:35], v[152:155], v[24:27], v[32:35]\nv_pk_mul_f16 v16, v56, v62 op_sel_hi:[0,1]\nv_pk_mul_f16 v17, v56, v63 op_sel_hi:[0,1]\nds_write_b32 v243, v55 offset:1152\n"
      "v_mfma_f32_16x16x32_f16 v[32:35], v[164:167], v[28:31], v[32:35]\nv_pk_mul_f16 v18, v58, v62 op_sel_hi:[0,1]\nv_mfma_f32_16x16x32_f16 v[36:39], v[168:171], v[28:31], v[36:39]\n"
      "v_pk_mul_f16 v19, v58, v63 op_sel_hi:[0,1]\nv_mfma_f32_16x16x32_f16 v[36:39], v[156:159], v[24:27], v[36:39]\nv_pk_mul_f16 v20, v60, v62 op_sel_hi:[0,1]\ns_setprio 3\n"
      "s_ff0_i32_b32 s53, s44\nv_mfma_f32_16x16x32_f16 v[40:43], v[160:163], v[24:27], v[40:43]\nv_pk_mul_f16 v21, v60, v63 op_sel_hi:[0,1]\n"
      "v_mfma_f32_16x16x32_f16 v[40:43], v[172:175], v[28:31], v[40:43]\nv_pk_mul_f16 v22, v60, v60 op_sel_hi:[0,1]\ns_add_u32 s53, s53, s64\n"
      "v_smfmac_f32_16x16x64_f16 v[44:47], v[176:179], v[24:31], v242\nv_pk_mul_f16 v23, v60, v57 op_sel_hi:[0,1]\nv_mfma_f32_16x16x32_f16 v[32:35], v[180:183], v[16:19], v[32:35]\n"
      "v_pk_mul_f16 v24, v56, v60 op_sel_hi:[0,1]\nv_pk_mul_f16 v25, v56, v61 op_sel_hi:[0,1]\nv_mfma_f32_16x16x32_f16 v[32:35], v[192:195], v[20:23], v[32:35]\n"
      "v_pk_mul_f16 v26, v58, v60 op_sel_hi:[0,1]\nv_pk_mul_f16 v27, v58, v61 op_sel_hi:[0,1]\nv_mfma_f32_16x16x32_f16 v[36:39], v[196:199], v[20:23], v[36:39]\n"
      "v_pk_mul_f16 v28, v58, v58 op_sel_hi:[0,1]\nv_mfma_f32_16x16x32_f16 v[36:39], v[184:187], v[16:19], v[36:39]\nv_pk_mul_f16 v29, v58, v57 op_sel_hi:[0,1]\n"
      "v_mfma_f32_16x16x32_f16 v[40:43], v[188:191], v[16:19], v[40:43]\nv_pk_mul_f16 v30, v56, v58 op_sel_hi:[0,1]\nv_mfma_f32_16x16x32_f16 v[40:43], v[200:203], v[20:23], v[40:43]\n"
      "v_pk_mul_f16 v31, v56, v59 op_sel_hi:[0,1]\nv_smfmac_f32_16x16x64_f16 v[44:47], v[204:207], v[16:23], v242\ns_nop 0\nv_smfmac_f32_16x16x64_f16 v[44:47], v[232:235], v[24:31], v242\ns_nop 1\n"
      "v_mfma_f32_16x16x32_f16 v[32:35], v[208:211], v[24:27], v[32:35]\ns_nop 1\nv_mfma_f32_16x16x32_f16 v[32:35], v[220:223], v[28:31], v[32:35]\ns_nop 1\n"
      "v_mfma_f32_16x16x32_f16 v[36:39], v[224:227], v[28:31], v[36:39]\nv_add_f32 v44, v44, v45\nv_add_f32 v46, v46, v47\nv_mfma_f32_16x16x32_f16 v[36:39], v[212:215], v[24:27], v[36:39]\n"
      "v_mul_f32 v55, v44, v54\nv_mul_f32 v55, v55, v46\nds_read_b128 v[44:47], v248 offset:48\nv_mfma_f32_16x16x32_f16 v[40:43], v[216:219], v[24:27], v[40:43]\ns_nop 2\n"
      "v_mfma_f32_16x16x32_f16 v[40:43], v[228:231], v[28:31], v[40:43]\nv_mul_f32 v32, v32, v48\nv_mul_f32 v33, v33, v49\nv_mul_f32 v34, v34, v50\n"
      ".Lc_slow:\ns_sub_u32 s49, s45, s64\ns_lshr_b32 s50, s44, 16\ns_or_b32 s50, s50, s56\ns_ashr_i32 s50, s50, s49\ns_cmp_lg_u32 s50, -1\ns_cbranch_scc1 .Lc_more\n"
      ".Lc_fin:\ns_sub_u32 s50, s45, s33\ns_bitcmp1_b32 s50, 1\ns_cselect_b64 vcc, -1, 0\ns_lshl_b32 s50, s50, 6\ns_sub_u32 s50, s50, 128\nv_fmac_f32 v55, v32, v36\nv_mul_f32 v35, v35, v51\n"
      "v_fmac_f32 v55, v33, v37\nv_fmac_f32 v55, v34, v38\nv_fmac_f32 v55, v35, v39\nv_cndmask_b32 v40, v244, v40, vcc\nv_cndmask_b32 v41, v245, v41, vcc\nv_cndmask_b32 v42, v246, v42, vcc\n"
      "v_cndmask_b32 v43, v247, v43, vcc\nv_mul_f32 v40, v40, v52\nv_mul_f32 v41, v41, v53\nv_fmac_f32 v55, v40, v42\nv_fmac_f32 v55, v41, v43\nv_add_u32 v243, s50, v243\ns_nop 0\n"
      "v_add_f32_dpp v55, v55, v55 quad_perm:[1,0,3,2] row_mask:0xf bank_mask:0xf\ns_nop 1\nv_add_f32_dpp v55, v55, v55 quad_perm:[2,3,0,1] row_mask:0xf bank_mask:0xf\ns_nop 1\n"
      "v_add_f32_dpp v55, v55, v55 row_half_mirror row_mask:0xf bank_mask:0xf\ns_nop 1\nv_add_f32_dpp v55, v55, v55 row_mirror row_mask:0xf bank_mask:0xf\nds_write_b32 v243, v55\n"
      "s_cmp_gt_u32 s24, 1\ns_waitcnt lgkmcnt(0)\ns_barrier\ns_cbranch_scc1 .Lc_end\nds_read_b128 v[4:7], v10\nds_read_b32 v3, v11\nds_read_b128 v[16:19], v13\nds_read_b32 v2, v248\n"
      "s_mov_b32 vcc_lo, 0x88888888\ns_mov_b32 vcc_hi, vcc_lo\ns_cmp_eq_u32 s24, 0\ns_cselect_b32 s43, vcc_lo, 0\ns_cselect_b32 s42, vcc_lo, 0x88888\nv_mov_b32 v9, s38\ns_waitcnt lgkmcnt(2)\n"
      "v_add_f32 v4, v4, v5\nv_add_f32 v6, v6, v7\nv_add_f32 v4, v4, v6\nv_ldexp_f32 v4, v4, v3\ns_waitcnt lgkmcnt(1)\nv_add_f32 v17, v17, v18\nv_add_f32 v17, v17, v19\nv_add_f32 v4, v4, v16\n"
      "v_cndmask_b32 v17, v17, v9, vcc\nv_add_f32 v4, v4, v17\ns_waitcnt lgkmcnt(0)\nv_and_b32 v2, 0xff, v2\nv_add_f32_dpp v4, v4, v4 quad_perm:[1,0,3,2] row_mask:0xf bank_mask:0xf\n"
      "v_lshrrev_b32 v2, v14, v2\nv_and_or_b32 v2, v2, 1, v15\nv_add_f32_dpp v4, v4, v4 quad_perm:[2,3,0,1] row_mask:0xf bank_mask:0xf\nv_cmp_ne_u32 vcc, 0, v2\nv_bfe_u32 v8, v19, 3, 12\n"
      "v_lshlrev_b32 v8, 1, v8\nv_cvt_pk_bf16_f32 v4, v4, v4\ns_and_b64 exec, vcc, s[42:43]\ns_cbranch_execz .Lc_end\nglobal_store_short v8, v4, s[22:23]\ns_branch .Lc_end\n"
      ".Lc_s2:\ns_getpc_b64 s[60:61]\ns_branch .Lc_slow\ns_branch .Lc_u3\n"
      ".Lc_s3:\ns_getpc_b64 s[60:61]\ns_branch .Lc_slow\ns_branch .Lc_u4\n"
      ".Lc_s4:\ns_getpc_b64 s[60:61]\ns_branch .Lc_slow\ns_branch .Lc_u5\n"
      ".Lc_s5:\ns_getpc_b64 s[60:61]\ns_branch .Lc_slow\ns_branch .Lc_u6\n"
      ".Lc_s6:\ns_getpc_b64 s[60:61]\ns_branch .Lc_slow\ns_branch .Lc_u7\n"
      ".Lc_s7:\ns_getpc_b64 s[60:61]\ns_branch .Lc_slow\ns_branch .Lc_u8\n"
      ".Lc_s8:\ns_getpc_b64 s[60:61]\ns_branch .Lc_slow\ns_branch .Lc_u9\n"
      ".Lc_s9:\ns_getpc_b64 s[60:61]\ns_branch .Lc_slow\ns_branch .Lc_u10\n"
      ".Lc_t0:\ns_swappc_b64 s[4:5], s[34:35]\ns_branch .Lc_b0\n"
      ".Lc_t1:\ns_swappc_b64 s[4:5], s[34:35]\ns_branch .Lc_b1\n"
      ".Lc_t2:\ns_swappc_b64 s[4:5], s[34:35]\ns_branch .Lc_b2\n"
      ".Lc_t3:\ns_swappc_b64 s[4:5], s[34:35]\ns_branch .Lc_b3\n"
      ".Lc_t4:\ns_swappc_b64 s[4:5], s[34:35]\ns_branch .Lc_b4\n"
      ".Lc_t5:\ns_swappc_b64 s[4:5], s[34:35]\ns_branch .Lc_b5\n"
      ".Lc_t6:\ns_swappc_b64 s[4:5], s[34:35]\ns_branch .Lc_b6\n"
      ".Lc_t7:\ns_swappc_b64 s[4:5], s[34:35]\ns_branch .Lc_b7\n"
      ".Lc_t8:\ns_swappc_b64 s[4:5], s[34:35]\ns_branch .Lc_b8\n"
      ".Lc_t9:\ns_swappc_b64 s[4:5], s[34:35]\ns_branch .Lc_b9\n"
      ".Lc_t10:\ns_swappc_b64 s[4:5], s[34:35]\ns_branch .Lc_b10\n"
      ".Lc_tg:\ns_swappc_b64 s[4:5], s[34:35]\ns_branch .Lc_bg\n"
      ".Lc_th:\ns_swappc_b64 s[4:5], s[34:35]\ns_branch .Lc_bh\n"
      ".Lc_more:\ns_mov_b32 s51, 0\ns_add_u32 s60, s60, 4\ns_addc_u32 s61, s61, 0\n"
      ".Lc_chk:\ns_sub_u32 s49, s45, s64\ns_sub_u32 s53, s68, s64\ns_lshl_b32 s53, -1, s53\ns_lshr_b32 s50, s44, 16\ns_or_b32 s50, s50, s53\ns_ashr_i32 s50, s50, s49\ns_cmp_eq_u32 s50, -1\n"
      "s_cbranch_scc0 .Lc_c1\ns_cmp_lg_u32 s56, 0\ns_cbranch_scc1 .Lc_fin\ns_branch .Lc_c2\n"
      ".Lc_c1:\ns_lshr_b32 s50, s44, s49\ns_and_b32 s50, s50, 0x10001\ns_cbranch_scc1 .Lc_have\n"
      ".Lc_c2:\ns_add_u32 s51, s51, 1\ns_bitcmp1_b32 s51, 22\ns_cbranch_scc1 .Lc_fin\ns_cmp_lg_u32 s56, 0\ns_cbranch_scc1 .Lc_rd\ns_swappc_b64 s[4:5], s[34:35]\n"
      ".Lc_rd:\nds_read_b32 v4, v248\ns_waitcnt lgkmcnt(0)\nv_readfirstlane_b32 s44, v4\ns_branch .Lc_chk\n"
      ".Lc_have:\ns_cmp_eq_u32 s51, 0\ns_cbranch_scc1 .Lc_go\ns_sub_u32 s49, s45, s33\ns_mulk_i32 s49, 928\nv_add_u32 v24, s49, v236\nv_add_u32 v25, s49, v237\nv_add_u32 v26, s49, v238\n"
      "v_add_u32 v27, s49, v239\nds_read_b64_tr_b16 v[68:69], v26 offset:0\nds_read_b64_tr_b16 v[70:71], v27 offset:0\nds_read_b64_tr_b16 v[56:57], v24 offset:0\n"
      "ds_read_b64_tr_b16 v[58:59], v25 offset:128\nds_read_b64_tr_b16 v[60:61], v25 offset:256\nds_read_b64_tr_b16 v[62:63], v25 offset:384\nds_read_b64_tr_b16 v[64:65], v25 offset:512\n"
      "ds_read_b64_tr_b16 v[66:67], v25 offset:640\ns_waitcnt lgkmcnt(0)\nv_readlane_b32 s48, v69, 48\nv_pk_mul_f16 v20, v68, v70\nv_pk_mul_f16 v21, v69, v71\n"
      "v_pk_mul_f16 v22, v56, v56 op_sel_hi:[0,1]\nv_pk_mul_f16 v23, v56, v57 op_sel_hi:[0,1]\nv_pk_mul_f16 v16, v68, s48 op_sel:[0,1]\nv_pk_mul_f16 v17, v69, s48 op_sel:[0,1]\n"
      "v_pk_mul_f16 v18, v70, s48 op_sel:[0,1]\nv_pk_mul_f16 v19, v71, s48 op_sel:[0,1]\n"
      ".Lc_go:\ns_setpc_b64 s[60:61]\n"
      ".Lt_g:\ns_waitcnt lgkmcnt(0)\nv_readfirstlane_b32 s42, v2\ns_mov_b32 s43, 43008\nv_lshl_add_u32 v1, v240, 2, s43\ns_cmp_lg_u32 s42, 3\ns_cbranch_scc1 .Lt_p\nds_read_b32 v3, v248 offset:764\n"
      "ds_read2st64_b32 v[6:7], v1 offset0:0 offset1:4\nds_read2st64_b32 v[8:9], v1 offset0:1 offset1:5\nds_read2st64_b32 v[10:11], v1 offset0:2 offset1:6\n"
      "ds_read2st64_b32 v[12:13], v1 offset0:3 offset1:7\ns_lshl_b32 s42, s28, 8\ns_add_u32 s42, s42, s29\ns_lshl_b32 s42, s42, 7\ns_add_u32 s58, s12, s42\ns_addc_u32 s59, s13, 0\n"
      "v_lshlrev_b32 v14, 3, v240\ns_cmp_eq_u32 s25, 2\ns_cselect_b64 vcc, -1, 0\ns_movk_i32 s42, 0xff00\nv_mov_b32 v1, s41\ns_lshl_b32 s43, s25, 3\ns_waitcnt lgkmcnt(0)\nv_add_f32 v12, v12, v13\n"
      "v_add_f32 v6, v6, v7\nv_add_f32 v8, v8, v9\nv_add_f32 v10, v10, v11\nv_mov_b32 v13, v12\nv_mov_b32 v15, v12\ns_nop 1\nv_permlane16_swap_b32 v13, v15\nv_add_f32 v13, v13, v15\n"
      "v_cndmask_b32 v12, v12, v13, vcc\nv_mul_f32 v6, v3, v6\nv_mul_f32 v8, v3, v8\nv_mul_f32 v10, v3, v10\nv_mul_f32 v12, v3, v12\nv_add_u32 v6, 0x80, v6\nv_and_or_b32 v6, v6, s42, v1\n"
      "v_mov_b32 v7, s40\nv_add_u32 v8, 0x80, v8\nv_and_or_b32 v8, v8, s42, v1\nv_mov_b32 v9, s40\nv_add_u32 v10, 0x80, v10\nv_and_or_b32 v10, v10, s42, v1\nv_mov_b32 v11, s40\n"
      "v_add_u32 v12, 0x80, v12\nv_and_or_b32 v12, v12, s42, v1\nv_mov_b32 v13, s40\nglobal_store_dwordx2 v14, v[6:7], s[58:59] offset:0 sc1\n"
      "global_store_dwordx2 v14, v[8:9], s[58:59] offset:512 sc1\nglobal_store_dwordx2 v14, v[10:11], s[58:59] offset:1024 sc1\ns_cmp_eq_u32 s43, 0\ns_cbranch_scc1 .Lt_p\ns_bfm_b64 exec, s43, 0\n"
      "global_store_dwordx2 v14, v[12:13], s[58:59] offset:1536 sc1\ns_mov_b64 exec, -1\n"
      ".Lt_p:\ns_cmp_gt_u32 s66, 100\ns_cbranch_scc1 .Lt_d\ns_mov_b32 s46, s66\ns_swappc_b64 s[34:35], s[4:5]\ns_lshl_b32 s43, 0x10000, s67\ns_cmp_gt_u32 s44, 0xffff\ns_cbranch_scc1 .Lt_a\n"
      "v_lshlrev_b32 v1, 3, v240\ns_mov_b64 exec, 1\nglobal_atomic_add v5, v1, v241, s[8:9] sc0 sc1\ns_mov_b64 exec, -1\nglobal_load_dwordx2 v[254:255], v249, s[12:13] sc1\n"
      ".Lt_f:\nglobal_load_dwordx2 v[2:3], v1, s[14:15] sc1\ns_swappc_b64 s[34:35], s[4:5]\ns_waitcnt vmcnt(0)\nv_readfirstlane_b32 s50, v5\nv_cmp_eq_u32 vcc, s41, v2\n"
      "v_cmp_eq_u32 s[62:63], s40, v3\nv_mov_b32 v7, s43\ns_cmp_lt_u32 s50, s55\ns_cbranch_scc1 .Lt_r\n"
      ".Lt_e:\nds_add_u32 v253, v7\ns_branch .Lt_c\n"
      ".Lt_r:\ns_add_u32 s50, s50, 1\nv_readlane_b32 s54, v251, s50\ns_and_b64 s[62:63], s[62:63], vcc\ns_bfe_u32 s50, s54, 0x50004\ns_lshr_b32 s49, s54, 9\ns_lshl3_add_u32 s50, s50, s49\n"
      "s_sub_u32 s50, s50, s69\ns_bitcmp0_b64 s[62:63], s50\ns_cbranch_scc1 .Lt_f\ns_mul_i32 s50, s54, 3584\ns_add_u32 s62, s16, s50\ns_addc_u32 s63, s17, 0\ns_add_u32 s50, s64, s67\n"
      "s_mul_i32 s49, s50, 928\ns_add_u32 m0, s49, 45056\ns_mov_b32 exec_hi, 0x7fffff\nglobal_load_lds_dwordx4 v252, s[62:63] offset:0 sc1\ns_add_u32 m0, m0, 21376\ns_lshl_b32 s54, s54, 3\n"
      "global_load_lds_dwordx4 v252, s[62:63] offset:896 sc1\ns_add_u32 m0, m0, 21376\ns_lshl_b32 s50, s50, 6\nglobal_load_lds_dwordx4 v252, s[62:63] offset:1792 sc1\ns_add_u32 m0, m0, 21376\n"
      "s_lshl_b32 s43, 1, s67\nglobal_load_lds_dwordx4 v252, s[62:63] offset:2688 sc1\ns_mov_b32 exec_hi, -1\ns_mov_b32 exec_lo, 0\nv_and_b32 v249, 0xffff8000, v249\ns_add_u32 s50, s50, 136192\n"
      "v_or_b32 v249, s54, v249\nv_and_or_b32 v250, v250, 63, s50\ns_mov_b32 exec_lo, -1\nv_mov_b32 v7, s43\nglobal_load_dwordx2 v[254:255], v249, s[12:13] sc1\ns_swappc_b64 s[34:35], s[4:5]\n"
      "s_waitcnt vmcnt(0)\ns_branch .Lt_e\n"
      ".Lt_a:\nv_mov_b32 v0, s43\nds_add_u32 v253, v0\n"
      ".Lt_d:\ns_sub_u32 s46, s64, 4\ns_cmp_ge_i32 s45, s46\ns_cbranch_scc1 .Lt_l\ns_swappc_b64 s[34:35], s[4:5]\n"
      ".Lt_l:\nglobal_load_dwordx2 v[254:255], v249, s[12:13] sc1\ns_swappc_b64 s[34:35], s[4:5]\ns_waitcnt vmcnt(0)\n"
      ".Lt_c:\nv_and_b32 v6, 0xff, v254\nv_cmp_ne_u32 vcc, s40, v255\nv_cmp_ne_u32 s[62:63], s41, v6\nv_and_b32 v9, 0xffffff00, v254\ns_or_b64 vcc, vcc, s[62:63]\ns_cbranch_vccnz .Lt_d\n"
      "s_mov_b32 vcc_lo, 0x1fff1fff\ns_mov_b32 vcc_hi, vcc_lo\ns_sub_u32 s56, s68, s64\nv_cndmask_b32 v9, v249, v9, vcc\ns_lshl_b32 s56, -1, s56\ns_movk_i32 s46, 99\nds_write_b32 v250, v9\n"
      "s_setpc_b64 s[4:5]\n"
      ".Lc_end:\n"
      ".Lxd_end:"
      :: "{v240}"(lane), "{v253}"(tch), "{v[248:251]}"(mrow), "{v252}"(msc), "{v[254:255]}"(rsp), "{s[4:19]}"(ka0), "{s[20:23]}"(ka1), "{s[24:31]}"(gs), "{s2}"(blk)
      : "memory", "vcc", "scc", "s33", "s34", "s35", "s36", "s37", "s38", "s39", "s40", "s41", "s42", "s43", "s44", "s45", "s46", "s47", "s48", "s49", "s50", "s51", "s52", "s53", "s54", "s55", "s56", "s57", "s58", "s59", "s60", "s61", "s62", "s63", "s64", "s65", "s66", "s67", "s68", "s69",
        "v0", "v1", "v2", "v3", "v4", "v5", "v6", "v7", "v8", "v9", "v10", "v11", "v12", "v13", "v14", "v15", "v16", "v17", "v18", "v19", "v20", "v21", "v22", "v23", "v24", "v25", "v26", "v27",
        "v28", "v29", "v30", "v31", "v32", "v33", "v34", "v35", "v36", "v37", "v38", "v39", "v40", "v41", "v42", "v43", "v44", "v45", "v46", "v47", "v48", "v49", "v50", "v51", "v52", "v53", "v54",
        "v55", "v56", "v57", "v58", "v59", "v60", "v61", "v62", "v63", "v64", "v65", "v66", "v67", "v68", "v69", "v70", "v71", "v72", "v73", "v74", "v75", "v76", "v77", "v78", "v79", "v80", "v81",
        "v82", "v83", "v84", "v85", "v86", "v87", "v88", "v89", "v90", "v91", "v92", "v93", "v94", "v95", "v96", "v97", "v98", "v99", "v100", "v101", "v102", "v103", "v104", "v105", "v106", "v107",
        "v108", "v109", "v110", "v111", "v112", "v113", "v114", "v115", "v116", "v117", "v118", "v119", "v120", "v121", "v122", "v123", "v124", "v125", "v126", "v127", "v128", "v129", "v130",
        "v131", "v132", "v133", "v134", "v135", "v136", "v137", "v138", "v139", "v140", "v141", "v142", "v143", "v144", "v145", "v146", "v147", "v148", "v149", "v150", "v151", "v152", "v153",
        "v154", "v155", "v156", "v157", "v158", "v159", "v160", "v161", "v162", "v163", "v164", "v165", "v166", "v167", "v168", "v169", "v170", "v171", "v172", "v173", "v174", "v175", "v176",
        "v177", "v178", "v179", "v180", "v181", "v182", "v183", "v184", "v185", "v186", "v187", "v188", "v189", "v190", "v191", "v192", "v193", "v194", "v195", "v196", "v197", "v198", "v199",
        "v200", "v201", "v202", "v203", "v204", "v205", "v206", "v207", "v208", "v209", "v210", "v211", "v212", "v213", "v214", "v215", "v216", "v217", "v218", "v219", "v220", "v221", "v222",
        "v223", "v224", "v225", "v226", "v227", "v228", "v229", "v230", "v231", "v232", "v233", "v234", "v235", "v236", "v237", "v238", "v239", "v241", "v242", "v243", "v244", "v245", "v246", "v247");
}

extern "C" void kernel_launch(void* const* d_in, const int* in_sizes, int n_in, void* d_out, int out_size,
                              void* d_ws, size_t ws_size, hipStream_t stream) {
  const int* idx = (const int*)d_in[0];
  const u16* table = (const u16*)d_in[1];
  const u16* ws_cin = (const u16*)d_in[2];
  const u16* wout = (const u16*)d_in[3];
  const u16* w1 = (const u16*)d_in[4];
  const u16* b1 = (const u16*)d_in[5];
  const u16* w2 = (const u16*)d_in[6];
  const u16* b_out = (const u16*)d_in[7];
  dnn_kernel<<<256, 512, 0, stream>>>(idx, table, ws_cin, w1, b1, w2, wout, b_out, (char*)d_ws, (u16*)d_out);
}
```

```cpp
#include <hip/hip_runtime.h>
#include <hip/hip_bf16.h>
#include <stdint.h>
#include <utility>

typedef __attribute__((ext_vector_type(8))) short bf16x8;
typedef __attribute__((ext_vector_type(4))) short s16x4;
typedef __attribute__((ext_vector_type(4))) float f32x4;
typedef __attribute__((ext_vector_type(2))) float f32x2;
typedef __attribute__((ext_vector_type(2))) __bf16 bf16x2;
typedef __attribute__((ext_vector_type(16))) float f32x16;
typedef unsigned short u16;
typedef __attribute__((ext_vector_type(2))) unsigned u32x2;
typedef __attribute__((ext_vector_type(4))) unsigned u32x4;

constexpr int NB = 4096, NF = 26, ND = 64, DIN = 1664;

#define STAMP(k,i)
#define WSTAMP(i)
template <class F, int... I>
__device__ __forceinline__ void static_for(F& f, std::integer_sequence<int, I...>) { (f(std::integral_constant<int, I>{}), ...); }
template <int CTRL, int RM = 0xf>
__device__ __forceinline__ float dpp(float v) { return __builtin_bit_cast(float, __builtin_amdgcn_update_dpp(0, __builtin_bit_cast(int, v), CTRL, RM, 0xf, false)); }
__device__ __forceinline__ unsigned pk2(float a, float b) { return __builtin_bit_cast(unsigned, __builtin_convertvector((f32x2){a, b}, bf16x2)); }

template <int V> using IC = std::integral_constant<int, V>;
typedef __attribute__((ext_vector_type(8))) unsigned u32x8;
typedef __attribute__((ext_vector_type(16))) unsigned u32x16;
constexpr int STAGE = 224 * 128 + 64 * 256, G_LDS = 163840;
constexpr int MC = 41 * 1024;
constexpr int W3S = 0, U_OFF = W3S + 35840, NC_OFF = U_OFF + 2048, GT_OFF = NC_OFF + 1024, WO_OFF = GT_OFF + 2048, DP = 4352, W2S = 2 * STAGE;
static_assert(WO_OFF + 256 <= STAGE, "lds");
constexpr int WS_RESP = 4096, WS_POOL = 32768, WS_M = 1 << 20;
#define NCLV 8
constexpr int NSLOT = 21, NCL = NCLV;

struct Geo { unsigned nt, s0, ns; };
constexpr __host__ __device__ Geo geo(unsigned e) {
  const unsigned i = (e * 79) >> 10, nt = e - 13 * i, c = nt < 9 ? 13108 : 13798, t = i * c + ((nt * 247) & 1023);
  return Geo{nt, t >> 10, ((t + c) >> 10) - (t >> 10)};
}
constexpr unsigned long long builders() {
  unsigned long long k = 0;
  for (unsigned x = 0; x < 8; ++x)
    for (unsigned s = 32; s-- > 0;)
      if (geo(32 * x + s).ns == 12) k = (k & ~(31ull << (5 * x))) | (unsigned long long)s << (5 * x);
  return k;
}
constexpr unsigned long long BK = builders();
#define KEEP 10, 13, 15, 11, 8
constexpr int KP[5] = {KEEP};
constexpr int WANT[5] = {138, 163, 177, 135, 113};
constexpr int kind(unsigned b) { return ((BK >> (5 * (b & 7))) & 31) == b >> 3 ? 3 + ((b & 7) >> 2) : 14 - geo((b & 7) * 32 + (b >> 3)).ns; }
constexpr __host__ __device__ unsigned group(unsigned b) { return ((b >> 3) + 2 * b) & 15; }
constexpr bool geo_ok() {
  for (unsigned nt = 0; nt < 13; ++nt) {
    unsigned s = 0;
    for (unsigned e = nt; e < 256; e += 13) {
      const Geo g = geo(e);
      if (g.nt != nt || g.s0 != s || g.ns < 12 || g.ns > 14 || e < g.s0 || e >= g.s0 + g.ns) return false;
      s += g.ns;
    }
    if (s != 256) return false;
  }
  return true;
}
static_assert(geo_ok(), "tiles");

struct PI { int a, b; };
constexpr PI prod(int n) {
  int m = n - 2;
  for (int jj = 0; jj < 6; ++jj) {
    const int j = jj ? 6 - jj : 0;
    if (m < 2 + 2 * j) return m == 0 ? PI{2 * j, 2 * j} : m == 1 ? PI{2 * j, 1} : PI{(m - 2) & ~1, 2 * j + (m & 1)};
    m -= 2 + 2 * j;
  }
  return PI{0, 0};
}
constexpr int xfield(int k, int h, int g) { return k == 1 ? 24 + h : 4 * (k >> 1) + ((g + 2 * (k & 1) + h) & 3); }
constexpr int pqfield(int q, int sl, int g) { return g < 3 ? 8 * g + 4 * (sl >> 1) + (sl & 1) + (((g ^ q) & 1) << 1) : sl == 3 ? 26 : 24 + ((sl + q) >> 1); }
constexpr unsigned short slot_code(int f, int t, int g, int h) {
  if (f == 0) {
    const int sl = 2 * (t & 1) + h, q = t >> 1;
    return g == 3 && (q || (sl & 1)) ? 0x8000 : 0x4000 | 6 << 10 | (26 * 33 + pqfield(q, sl, g));
  }
  const int n = 4 * (f - 1) + t;
  const int p = n < 2 ? pqfield(0, 2 * n + h, g) : xfield(prod(n).a, 0, g), q = n < 2 ? pqfield(1, 2 * n + h, g) : xfield(prod(n).b, h, g);
  return (p == 26 ? 0x4000 | 10 << 10 : p == q ? 1 << 10 : 2 << 10) | (p * 33 + q);
}
struct GT { unsigned short t[2][64][8], pool[16][64]; };
constexpr GT make_gt() {
  GT r{};
  for (int l = 0; l < 64; ++l)
    for (int e = 0; e < 8; ++e) {
      r.t[0][l][e] = l < 48 ? slot_code(l >> 2, e >> 1, l & 3, e & 1) : 0x8000;
      r.t[1][l][e] = l < 48 ? slot_code(2 * (l >> 3) + ((l >> 1) & 1), 2 * ((e >> 1) & 1) + ((l >> 2) & 1), 2 * (l & 1) + (e >> 2), e & 1) : 0x8000;
    }
  int tot = 0, dem = 0, acc = 0, k = 15, b = 0;
  for (int i = 0; i < 256; ++i) { tot += 16 - KP[kind(i)]; dem += WANT[kind(i)] - 10 * KP[kind(i)]; }
  for (int q = 0, n = 0; q < 16; ++q) {
    for (int i = 0; i < 256; ++i) acc += group(i) == q ? WANT[kind(i)] - 10 * KP[kind(i)] : 0;
    const int len = (acc * tot + dem / 2) / dem - n;
    n += len;
    r.pool[q][0] = len;
    for (int i = 1; i <= len; ++i) {
      while (k < KP[kind(b)]) { ++b; k = 15; }
      r.pool[q][i] = ((b & 7) * 32 + (b >> 3)) * 16 + k--;
    }
  }
  return r;
}
constexpr bool pool_ok() {
  const GT t = make_gt();
  for (int q = 0; q < 16; ++q) {
    int n = 0;
    for (int i = 0; i < 256; ++i) n += group(i) != q ? 0 : NSLOT - KP[kind(i)] < NCL ? NSLOT - KP[kind(i)] : NCL;
    if (t.pool[q][0] > 62 || t.pool[q][0] > n) return false;
    const int fb = 16 * q < 24 ? 0 : 16 * q - 24 > 192 ? 192 : 16 * q - 24;
    for (int i = 1; i <= t.pool[q][0]; ++i) {
      const int b = ((t.pool[q][i] >> 4) & 31) * 8 + (t.pool[q][i] >> 9);
      if (b < fb || b >= fb + 64) return false;
    }
  }
  for (int k = 0; k < 5; ++k) if (KP[k] < 8 || KP[k] > 16) return false;
  return true;
}
static_assert(pool_ok(), "pools");
static __device__ const __attribute__((aligned(16))) GT g_tab = make_gt();

__global__ __launch_bounds__(512, 2) void dnn_kernel(const int* __restrict__ idx, const u16* __restrict__ table,
                                                     const u16* __restrict__ ws_cin, const u16* __restrict__ w1,
                                                     const u16* __restrict__ b1, const u16* __restrict__ w2,
                                                     const u16* __restrict__ wout, const u16* __restrict__ b_out,
                                                     char* __restrict__ ws, u16* __restrict__ out) {
  __shared__ __attribute__((aligned(1024))) char shm[G_LDS];
  u32x4 ka1;
  u32x8 gs;
  u32x16 ka0;
  unsigned tch, blk = blockIdx.x;
  int lane;
  asm volatile(
      "s_load_dwordx16 s[4:19], %[ka], 0\ns_load_dwordx4 s[20:23], %[ka], 64\nv_readfirstlane_b32 s24, %[tid]\ns_lshr_b32 s37, s2, 3\ns_cmp_lt_u32 s24, 448\ns_cbranch_scc1 .Lf3\n"
      "s_getpc_b64 s[42:43]\n"
      ".Lxd_pc0:\nv_and_b32 v245, 15, %[tid]\nv_lshl_add_u32 v245, s37, 4, v245\nv_lshlrev_b32 v245, 7, v245\nv_min_u32 v245, .Lxd_end-.Lxd_pc0-4, v245\nglobal_load_dword v253, v245, s[42:43]\n"
      ".Lf3:\ns_and_b32 s36, s2, 7\ns_lshl_b32 s30, s36, 5\ns_add_u32 s30, s30, s37\ns_lshr_b32 s24, s24, 6\ns_mul_i32 s38, s30, 79\ns_lshr_b32 s38, s38, 10\ns_mul_i32 s28, s38, 13\n"
      "s_sub_u32 s28, s30, s28\nv_and_b32 v240, 63, %[tid]\ns_lshr_b32 s27, s36, 1\ns_add_u32 s27, s27, s36\ns_and_b32 s39, s28, 3\ns_mul_i32 s39, s39, 3\ns_add_u32 s27, s27, s39\n"
      "s_sub_u32 s39, s27, 12\ns_min_u32 s27, s27, s39\ns_lshl_b32 s27, s27, 1\ns_cmp_ge_u32 s24, 4\ns_cbranch_scc1 .Lf1\ns_movk_i32 s39, 13798\nv_mul_u32_u24 v245, 9363, v240\n"
      "s_cmp_lt_u32 s28, 9\ns_cselect_b32 s39, 13108, s39\ns_or_b32 s41, s27, 2\ns_mul_i32 s40, s28, 247\nv_lshrrev_b32 v245, 16, v245\ns_and_b32 s40, s40, 1023\n"
      "v_mad_i32_i24 v246, v245, -7, v240\ns_mul_i32 s38, s38, s39\nv_lshl_add_u32 v246, v246, 2, s41\ns_add_u32 s38, s38, s40\nv_subrev_u32 v247, 26, v246\ns_lshr_b32 s29, s38, 10\n"
      "v_mul_u32_u24 v245, 26, v245\ns_add_u32 s38, s38, s39\nv_min_u32 v246, v246, v247\ns_lshr_b32 s38, s38, 10\nv_add_lshl_u32 v241, v246, v245, 2\ns_sub_u32 s38, s38, s29\n"
      "v_add_u32 v244, 192, v241\ns_lshl_b32 s38, s38, 1\nv_add_u32 v243, 128, v241\ns_sub_u32 s25, s38, 24\nv_add_u32 v242, 64, v241\ns_mul_i32 s38, s24, 6272\ns_add_u32 m0, s38, 139648\n"
      "s_lshl1_add_u32 s38, s29, s24\ns_mul_i32 s38, s38, 832\ns_mul_i32 s40, s24, 3328\ns_add_u32 s38, s38, 16640\ns_sub_u32 s40, 16896, s40\ns_mov_b32 exec_hi, 0xffffff\n"
      "v_add_u32 v245, 256, v241\nv_add_u32 v246, 2432, v241\ns_waitcnt lgkmcnt(0)\ns_add_u32 s38, s4, s38\ns_addc_u32 s39, s5, 0\ns_cmp_ge_u32 s24, s25\nglobal_load_lds_dwordx4 v241, s[38:39]\n"
      "s_cbranch_scc1 .Li0\nglobal_load_lds_dwordx4 v246, s[38:39] offset:896\n"
      ".Li0:\ns_sub_u32 m0, m0, 4480\ns_sub_u32 s38, s38, s40\ns_subb_u32 s39, s39, 0\nglobal_load_lds_dwordx4 v245, s[38:39] offset:0\nglobal_load_lds_dwordx4 v244, s[38:39] offset:896\n"
      "global_load_lds_dwordx4 v243, s[38:39] offset:1792\nglobal_load_lds_dwordx4 v242, s[38:39] offset:2688\nglobal_load_lds_dwordx4 v241, s[38:39] offset:3584\ns_mov_b32 exec_hi, -1\n"
      "s_add_u32 s27, s27, 2\ns_mul_i32 s36, s36, 5\ns_mov_b32 s38, %[bkl]\ns_mov_b32 s39, %[bkh]\ns_lshr_b64 s[38:39], s[38:39], s36\ns_and_b32 s38, s38, 31\ns_xor_b32 s31, s38, s37\n"
      "s_cbranch_scc0 .Lbld\n"
      ".Lbp:\ns_mov_b32 s36, s24\ns_mul_i32 s42, s36, 6272\ns_cmp_ge_u32 s36, s25\ns_cselect_b64 vcc, 0, -1\ns_and_b32 s44, s27, 2\ns_xor_b32 s44, s44, 2\ns_lshl_b32 s44, s44, 2\n"
      "s_add_u32 s42, s42, s44\ns_add_u32 s42, s42, 135168\nv_lshrrev_b32 v22, 3, v240\nv_mul_u32_u24 v6, 112, v22\nv_add_u32 v6, s42, v6\nv_lshrrev_b32 v22, 4, v240\ns_lshl_b32 s42, s36, 2\n"
      "v_xor_b32 v22, v22, v240\nv_xor_b32 v22, s42, v22\nv_and_b32 v22, 7, v22\nv_lshlrev_b32 v4, 4, v22\nv_xor_b32 v5, 64, v4\ns_sub_u32 s44, s6, 1024\ns_subb_u32 s45, s7, 0\n"
      "s_sub_u32 s46, s6, 2048\ns_subb_u32 s47, s7, 0\ns_sub_u32 s48, s6, 3072\ns_subb_u32 s49, s7, 0\ns_mul_i32 s53, s36, 5120\ns_lshl_b32 s26, s36, 10\ns_lshl_b32 s54, s36, 12\n"
      "s_add_u32 s54, s54, 28672\nv_add_u32 v7, 1792, v6\nv_add_u32 v8, 3584, v6\nv_add_u32 v9, 5376, v6\ns_waitcnt vmcnt(0)\nds_read2_b64 v[196:199], v8 offset0:112 offset1:113\n"
      "ds_read2_b64 v[200:203], v9 offset0:0 offset1:1\nds_read2_b64 v[176:179], v6 offset0:0 offset1:1\nds_read2_b64 v[180:183], v6 offset0:112 offset1:113\n"
      "ds_read2_b64 v[184:187], v7 offset0:0 offset1:1\nds_read2_b64 v[188:191], v7 offset0:112 offset1:113\nds_read2_b64 v[192:195], v8 offset0:0 offset1:1\ns_waitcnt lgkmcnt(0)\n"
      "v_lshl_add_u32 v22, v176, 7, v4\nv_lshl_add_u32 v23, v180, 7, v5\nv_lshl_add_u32 v24, v184, 7, v4\nv_lshl_add_u32 v25, v188, 7, v5\nv_lshl_add_u32 v26, v192, 7, v4\n"
      "v_lshl_add_u32 v27, v196, 7, v4\nv_lshl_add_u32 v28, v200, 7, v4\ns_add_u32 m0, s26, 65536\ns_nop 0\nglobal_load_lds_dwordx4 v27, s[6:7] offset:0\nv_lshl_add_u32 v27, v197, 7, v4\n"
      "ds_read2_b64 v[196:199], v8 offset0:113 offset1:114\ns_cbranch_vccz .La1001\ns_add_u32 m0, s26, 69632\ns_nop 0\nglobal_load_lds_dwordx4 v28, s[6:7] offset:0\n"
      ".La1001:\nv_lshl_add_u32 v28, v201, 7, v4\ns_add_u32 m0, s53, 45056\ns_nop 0\nglobal_load_lds_dwordx4 v22, s[6:7] offset:0\nv_lshl_add_u32 v22, v177, 7, v4\n"
      "global_load_lds_dwordx4 v23, s[44:45] offset:1024\nv_lshl_add_u32 v23, v181, 7, v5\nds_read2_b64 v[180:183], v6 offset0:113 offset1:114\nglobal_load_lds_dwordx4 v24, s[46:47] offset:2048\n"
      "v_lshl_add_u32 v24, v185, 7, v4\nglobal_load_lds_dwordx4 v25, s[48:49] offset:3072\nv_lshl_add_u32 v25, v189, 7, v5\nds_read2_b64 v[188:191], v7 offset0:113 offset1:114\n"
      "s_add_u32 m0, s53, 49152\ns_nop 0\nglobal_load_lds_dwordx4 v26, s[6:7] offset:0\nv_lshl_add_u32 v26, v193, 7, v4\ns_waitcnt lgkmcnt(0)\ns_setprio 3\ns_barrier\ns_add_u32 m0, s26, 110592\n"
      "s_nop 0\nglobal_load_lds_dwordx4 v27, s[6:7] offset:0\nv_lshl_add_u32 v27, v196, 7, v4\ns_cbranch_vccz .La1002\ns_add_u32 m0, s26, 114688\ns_nop 0\n"
      "global_load_lds_dwordx4 v28, s[6:7] offset:0\n"
      ".La1002:\nv_lshl_add_u32 v28, v202, 7, v4\ns_add_u32 m0, s53, 90112\ns_nop 0\nglobal_load_lds_dwordx4 v22, s[6:7] offset:0\nv_lshl_add_u32 v22, v178, 7, v4\n"
      "global_load_lds_dwordx4 v23, s[44:45] offset:1024\nv_lshl_add_u32 v23, v180, 7, v5\nglobal_load_lds_dwordx4 v24, s[46:47] offset:2048\nv_lshl_add_u32 v24, v186, 7, v4\n"
      "global_load_lds_dwordx4 v25, s[48:49] offset:3072\nv_lshl_add_u32 v25, v188, 7, v5\ns_add_u32 m0, s53, 94208\ns_nop 0\nglobal_load_lds_dwordx4 v26, s[6:7] offset:0\n"
      "v_lshl_add_u32 v26, v194, 7, v4\nv_lshrrev_b32 v30, 2, v240\nv_bfe_u32 v31, v240, 1, 1\ns_lshl_b32 s42, s36, 1\nv_lshrrev_b32 v32, 3, v240\nv_or_b32 v31, s42, v31\ns_sub_u32 s33, s30, s29\n"
      "s_lshl_b32 s42, s33, 11\nv_xor_b32 v31, v31, v32\nv_lshl_add_u32 v30, v30, 7, s42\nv_and_b32 v32, 1, v240\nv_lshl_add_u32 v30, v31, 4, v30\nv_lshl_add_u32 v30, v32, 3, v30\n"
      "v_add_u32 v31, 90112, v30\nv_mov_b32 v32, 0\nv_mov_b32 v33, 0x3d800000\ns_mov_b32 s55, 0x80008000\ns_and_b32 s41, s24, 3\nv_lshrrev_b32 v245, 4, v240\nv_and_b32 v246, 15, v240\n"
      "s_mul_i32 s40, s41, 53248\nv_lshlrev_b32 v238, 1, v245\nv_mul_u32_u24 v245, 3328, v245\nv_xor_b32 v247, v246, v238\nv_add_u32 v245, s40, v245\nv_lshl_add_u32 v236, v247, 4, v245\n"
      "v_xor_b32 v238, 128, v236\nv_add_u32 v237, 12288, v236\nv_add_u32 v239, 36864, v238\nv_add_u32 v238, 24576, v238\ns_lshl_b32 s40, s28, 8\ns_add_u32 s10, s10, s40\ns_addc_u32 s11, s11, 0\n"
      "s_add_u32 s52, s27, 1\ns_mul_i32 s52, s52, 212992\ns_add_u32 s50, s10, s52\ns_addc_u32 s51, s11, 0\ns_add_u32 s50, s50, 212992\ns_addc_u32 s51, s51, 0\ns_cmp_eq_u32 s27, 24\n"
      "s_cselect_b64 s[50:51], s[10:11], s[50:51]\ns_lshl_b32 s42, s2, 2\ns_load_dword s37, s[20:21], s42\ns_load_dword s38, s[18:19], 0\ns_bfe_u32 s42, s2, 0x10002\ns_add_u32 s42, s42, 3\n"
      "s_lshr_b32 s43, s25, 1\ns_sub_u32 s43, 2, s43\ns_cmp_eq_u32 s31, 0\ns_cselect_b32 s42, s42, s43\ns_mul_i32 s42, s42, 5\ns_lshr_b32 s64, %[kp], s42\ns_and_b32 s64, s64, 31\ns_branch .Lfeed\n"
      ".Lf1:\ns_and_b32 s41, s24, 3\nv_lshrrev_b32 v245, 4, v240\nv_and_b32 v246, 15, v240\ns_mul_i32 s40, s41, 53248\nv_lshlrev_b32 v238, 1, v245\nv_mul_u32_u24 v245, 3328, v245\n"
      "v_xor_b32 v247, v246, v238\nv_add_u32 v245, s40, v245\nv_lshl_add_u32 v236, v247, 4, v245\nv_xor_b32 v238, 128, v236\nv_add_u32 v237, 12288, v236\nv_add_u32 v239, 36864, v238\n"
      "v_add_u32 v238, 24576, v238\ns_lshl_b32 s41, s41, 12\ns_mov_b32 m0, 160512\ns_lshl_b32 s40, s28, 8\ns_add_u32 s39, s27, 2\ns_mul_i32 s39, s39, 212992\ns_add_u32 s39, s39, s40\n"
      "v_lshl_add_u32 v244, v240, 4, s40\nv_lshl_add_u32 v243, v240, 2, s40\ns_mov_b64 exec, 0xffff\ns_cmp_lg_u32 s24, 5\ns_waitcnt lgkmcnt(0)\nglobal_load_lds_dwordx4 v244, s[12:13]\n"
      "s_mov_b64 exec, -1\ns_cbranch_scc1 .Lf4\nglobal_load_dword v253, v243, s[14:15]\n"
      ".Lf4:\ns_add_u32 m0, s41, 73728\ns_add_u32 s42, s10, s39\ns_addc_u32 s43, s11, 0\nglobal_load_lds_dwordx4 v236, s[42:43] offset:0\nglobal_load_lds_dwordx4 v237, s[42:43] offset:1024\n"
      "global_load_lds_dwordx4 v238, s[42:43] offset:2048\nglobal_load_lds_dwordx4 v239, s[42:43] offset:3072\ns_add_u32 m0, s41, 118784\ns_add_u32 s42, s42, 212992\ns_addc_u32 s43, s43, 0\n"
      "global_load_lds_dwordx4 v236, s[42:43] offset:0\nglobal_load_lds_dwordx4 v237, s[42:43] offset:1024\nglobal_load_lds_dwordx4 v238, s[42:43] offset:2048\n"
      "global_load_lds_dwordx4 v239, s[42:43] offset:3072\ns_movk_i32 s39, 13798\ns_cmp_lt_u32 s28, 9\ns_cselect_b32 s39, 13108, s39\ns_mul_i32 s40, s28, 247\ns_and_b32 s40, s40, 1023\n"
      "s_mul_i32 s38, s38, s39\ns_add_u32 s38, s38, s40\ns_lshr_b32 s29, s38, 10\ns_add_u32 s38, s38, s39\ns_lshr_b32 s38, s38, 10\ns_sub_u32 s38, s38, s29\ns_lshl_b32 s38, s38, 1\n"
      "s_sub_u32 s25, s38, 24\ns_add_u32 s27, s27, 2\ns_mul_i32 s36, s36, 5\ns_mov_b32 s38, %[bkl]\ns_mov_b32 s39, %[bkh]\ns_lshr_b64 s[38:39], s[38:39], s36\ns_and_b32 s38, s38, 31\n"
      "s_xor_b32 s31, s38, s37\ns_cbranch_scc0 .Lbld\n"
      ".Lbg:\ns_and_b32 s36, s24, 3\ns_lshl_b32 s42, s2, 2\ns_load_dword s37, s[20:21], s42\ns_load_dword s38, s[18:19], 0\ns_bfe_u32 s42, s2, 0x10002\ns_add_u32 s42, s42, 3\n"
      "s_lshr_b32 s43, s25, 1\ns_sub_u32 s43, 2, s43\ns_cmp_eq_u32 s31, 0\ns_cselect_b32 s42, s42, s43\ns_mul_i32 s42, s42, 5\ns_lshr_b32 s64, %[kp], s42\ns_and_b32 s64, s64, 31\ns_setprio 2\n"
      "s_and_b32 s56, s36, 1\ns_lshr_b32 s57, s36, 1\nv_lshrrev_b32 v1, 4, v240\nv_and_b32 v2, 15, v240\nv_lshlrev_b32 v3, 2, v2\nv_add_u32 v3, 162048, v3\nv_mov_b32 v4, 0\nds_write_b32 v3, v4\n"
      "s_mul_i32 s42, s56, 10240\nv_lshrrev_b32 v3, 1, v2\nv_xor_b32 v3, v3, v1\nv_lshl_add_u32 v4, v2, 7, s42\nv_lshl_add_u32 v96, v3, 4, v4\nv_xor_b32 v97, 64, v96\nv_add_u32 v98, 90112, v96\n"
      "v_add_u32 v99, 90112, v97\ns_cmp_eq_u32 s25, 4\ns_cselect_b32 s42, 4, 5\ns_mul_i32 s42, s42, s56\ns_sub_u32 s42, 12, s42\ns_lshl_b32 s42, s42, 11\ns_lshl_b32 s43, s56, 13\n"
      "s_sub_u32 s43, 20480, s43\nv_add_u32 v100, s42, v96\nv_add_u32 v120, s43, v96\nv_add_u32 v101, s42, v97\nv_add_u32 v121, s43, v97\nv_add_u32 v102, s42, v98\nv_add_u32 v122, s43, v98\n"
      "v_add_u32 v103, s42, v99\nv_add_u32 v123, s43, v99\nv_lshrrev_b32 v3, 2, v2\nv_lshl_add_u32 v4, v1, 3, v3\nv_and_b32 v5, 1, v1\nv_lshl_add_u32 v3, v5, 2, v3\ns_lshl_b32 s42, s36, 1\n"
      "v_xor_b32 v3, s42, v3\nv_and_b32 v5, 3, v2\nv_lshlrev_b32 v5, 3, v5\nv_lshl_add_u32 v4, v4, 8, v5\nv_lshl_add_u32 v4, v3, 5, v4\nv_add_u32 v104, 28672, v4\nv_xor_b32 v105, 32, v104\n"
      "v_xor_b32 v106, 64, v104\nv_xor_b32 v107, 96, v104\nv_add_u32 v108, 90112, v104\nv_add_u32 v109, 90112, v105\nv_add_u32 v110, 90112, v106\nv_add_u32 v111, 90112, v107\n"
      "s_lshl_b32 s42, s57, 10\ns_add_u32 s42, s42, 43008\ns_mul_i32 s43, s56, 320\ns_add_u32 s43, s43, s42\nv_lshl_add_u32 v254, v240, 2, s43\nv_cmp_gt_u32 vcc, 16, v240\ns_lshl_b32 s43, s56, 6\n"
      "s_add_u32 s42, s42, s43\ns_add_u32 s42, s42, 256\nv_min_u32 v5, 2, v1\nv_lshl_add_u32 v255, v2, 2, s42\nv_lshl_add_u32 v255, v5, 7, v255\nv_cndmask_b32 v255, v255, v254, vcc\n"
      "s_lshl_b32 s42, s36, 6\ns_add_u32 s42, s42, 160512\nv_lshl_add_u32 v3, v1, 3, s42\nv_xor_b32 v4, 64, v3\ns_lshl_b32 s42, s36, 7\ns_add_u32 s42, s42, 162816\nv_lshl_add_u32 v236, v1, 4, s42\n"
      "v_xor_b32 v237, 128, v236\ns_waitcnt lgkmcnt(0)\ns_mul_i32 s65, s36, 22272\ns_add_u32 s65, s65, 45056\ns_add_u32 s68, s64, 8\ns_min_u32 s68, s68, 21\ns_lshl_b32 s38, s38, 16\n"
      "s_lshr_b32 s42, s24, 2\ns_mul_i32 s43, s42, 3\ns_xor_b32 s43, s43, s36\ns_add_u32 s42, s42, s64\ns_add_u32 s42, s42, 9\ns_and_b32 s42, s42, 1\ns_lshl1_add_u32 s67, s43, s42\n"
      "s_add_u32 s66, s64, s67\ns_cmp_lt_u32 s66, s68\ns_cselect_b32 s66, s66, 127\ns_sub_u32 s66, s66, 8\ns_max_i32 s66, s66, 0\ns_getpc_b64 s[34:35]\n"
      ".Lk1063:\ns_add_u32 s34, s34, .Lt_g-.Lk1063\ns_addc_u32 s35, s35, 0\ns_lshr_b32 s42, s2, 3\ns_lshl1_add_u32 s42, s2, s42\ns_and_b32 s69, s42, 15\ns_lshl_b32 s43, s69, 7\n"
      "s_add_u32 s43, s43, 32768\ns_add_u32 s8, s20, s43\ns_addc_u32 s9, s21, 0\ns_add_u32 s12, s20, 65536\ns_addc_u32 s13, s21, 0\ns_add_u32 s16, s20, 2097152\ns_addc_u32 s17, s21, 0\n"
      "s_lshl_b32 s42, s69, 4\ns_max_u32 s42, s42, 24\ns_sub_u32 s42, s42, 24\ns_min_u32 s69, s42, 192\ns_lshl_b32 s42, s69, 3\ns_add_u32 s42, s42, 24576\ns_add_u32 s14, s20, s42\n"
      "s_addc_u32 s15, s21, 0\ns_mov_b32 s59, 0xc0135761\ns_mov_b32 s60, 0xc1e00000\ns_add_u32 s49, s65, 1856\ns_mov_b32 s45, 2\ns_mov_b32 s46, 0\ns_cmp_lg_u32 s24, 5\ns_cbranch_scc1 .Lj1064\n"
      "s_waitcnt vmcnt(8)\nv_lshlrev_b32 v16, 16, v253\nv_and_b32 v17, 0xffff0000, v253\ns_mov_b32 s42, 0xf000000\nv_max3_f32 v18, |v16|, |v17|, s42\ns_nop 1\n"
      "v_max_u32_dpp v18, v18, v18 quad_perm:[1,0,3,2] row_mask:0xf bank_mask:0xf\ns_nop 1\nv_max_u32_dpp v18, v18, v18 quad_perm:[2,3,0,1] row_mask:0xf bank_mask:0xf\ns_nop 1\n"
      "v_max_u32_dpp v18, v18, v18 row_half_mirror row_mask:0xf bank_mask:0xf\ns_nop 1\nv_max_u32_dpp v18, v18, v18 row_mirror row_mask:0xf bank_mask:0xf\nv_mov_b32 v19, v18\n"
      "s_mov_b32 s42, 162816\nv_lshl_add_u32 v20, v240, 3, s42\nv_permlane16_swap_b32 v18, v19\nv_max_u32 v18, v18, v19\nv_mov_b32 v19, v18\nv_mov_b32 v21, 162812\ns_nop 0\n"
      "v_permlane32_swap_b32 v18, v19\nv_max_u32 v18, v18, v19\nv_and_b32 v18, 0x7f800000, v18\nv_subrev_u32 v19, 0xc000000, v18\nds_write_b32 v21, v19\nv_lshrrev_b32 v18, 23, v18\n"
      "v_sub_u32 v18, 123, v18\nv_ldexp_f32 v16, v16, v18\nv_ldexp_f32 v17, v17, v18\ns_mov_b32 s42, 0x32000000\nv_max_f32 v18, |v16|, s42\nv_max_f32 v19, |v17|, s42\ns_brev_b32 s42, -2\n"
      "v_bfi_b32 v16, s42, v18, v16\nv_bfi_b32 v17, s42, v19, v17\nv_rcp_f32 v16, v16\nv_rcp_f32 v17, v17\ns_nop 0\nv_mul_f32 v18, 0x31800000, v16\nv_mul_f32 v19, 0x31800000, v17\n"
      "ds_write2st64_b64 v20, v[16:17], v[18:19] offset1:1\n"
      ".Lj1064:\ns_barrier\ns_waitcnt vmcnt(8)\nds_read_b64 v[8:9], v3 offset:0\nds_read_b64 v[10:11], v3 offset:32\nds_read_b64 v[12:13], v4 offset:0\nds_read_b64 v[14:15], v4 offset:32\n"
      "s_waitcnt lgkmcnt(0)\nv_lshlrev_b32 v124, 16, v8\nv_and_b32 v125, 0xffff0000, v8\nv_lshlrev_b32 v126, 16, v9\nv_and_b32 v127, 0xffff0000, v9\nv_lshlrev_b32 v128, 16, v10\n"
      "v_and_b32 v129, 0xffff0000, v10\nv_lshlrev_b32 v130, 16, v11\nv_and_b32 v131, 0xffff0000, v11\nv_lshlrev_b32 v132, 16, v12\nv_and_b32 v133, 0xffff0000, v12\nv_lshlrev_b32 v134, 16, v13\n"
      "v_and_b32 v135, 0xffff0000, v13\nv_lshlrev_b32 v136, 16, v14\nv_and_b32 v137, 0xffff0000, v14\nv_lshlrev_b32 v138, 16, v15\nv_and_b32 v139, 0xffff0000, v15\n"
      "v_mov_b64 v[140:141], v[124:125]\nv_mov_b64 v[142:143], v[126:127]\nv_mov_b64 v[144:145], v[128:129]\nv_mov_b64 v[146:147], v[130:131]\nv_mov_b64 v[148:149], v[132:133]\n"
      "v_mov_b64 v[150:151], v[134:135]\nv_mov_b64 v[152:153], v[136:137]\nv_mov_b64 v[154:155], v[138:139]\nv_mov_b64 v[156:157], v[124:125]\nv_mov_b64 v[158:159], v[126:127]\n"
      "v_mov_b64 v[160:161], v[128:129]\nv_mov_b64 v[162:163], v[130:131]\nv_mov_b64 v[164:165], v[132:133]\nv_mov_b64 v[166:167], v[134:135]\nv_mov_b64 v[168:169], v[136:137]\n"
      "v_mov_b64 v[170:171], v[138:139]\nv_mov_b64 v[172:173], v[124:125]\nv_mov_b64 v[174:175], v[126:127]\nv_mov_b64 v[176:177], v[128:129]\nv_mov_b64 v[178:179], v[130:131]\n"
      "v_mov_b64 v[180:181], v[132:133]\nv_mov_b64 v[182:183], v[134:135]\nv_mov_b64 v[184:185], v[136:137]\nv_mov_b64 v[186:187], v[138:139]\nv_mov_b64 v[188:189], v[124:125]\n"
      "v_mov_b64 v[190:191], v[126:127]\nv_mov_b64 v[192:193], v[128:129]\nv_mov_b64 v[194:195], v[130:131]\nv_mov_b64 v[196:197], v[132:133]\nv_mov_b64 v[198:199], v[134:135]\n"
      "v_mov_b64 v[200:201], v[136:137]\nv_mov_b64 v[202:203], v[138:139]\nv_mov_b64 v[204:205], v[124:125]\nv_mov_b64 v[206:207], v[126:127]\nv_mov_b64 v[208:209], v[128:129]\n"
      "v_mov_b64 v[210:211], v[130:131]\nv_mov_b64 v[212:213], v[132:133]\nv_mov_b64 v[214:215], v[134:135]\nv_mov_b64 v[216:217], v[136:137]\nv_mov_b64 v[218:219], v[138:139]\n"
      "v_mov_b64 v[220:221], v[124:125]\nv_mov_b64 v[222:223], v[126:127]\nv_mov_b64 v[224:225], v[128:129]\nv_mov_b64 v[226:227], v[130:131]\nv_mov_b64 v[228:229], v[132:133]\n"
      "v_mov_b64 v[230:231], v[134:135]\nv_mov_b64 v[232:233], v[136:137]\nv_mov_b64 v[234:235], v[138:139]\ns_mov_b32 s58, 4\ns_cmp_eq_u32 s25, 2\ns_cbranch_scc1 .Lg13\ns_cmp_eq_u32 s25, 0\n"
      "s_cbranch_scc1 .Lg12\ns_branch .Lg14\n"
      ".Lbld:\n"
      : "=&{v240}"(lane), "=&{v253}"(tch), "=&{s[4:19]}"(ka0), "=&{s[20:23]}"(ka1), "=&{s[24:31]}"(gs), "+{s2}"(blk)
      : [ka] "s"(__builtin_amdgcn_kernarg_segment_ptr()), [tid] "v"(threadIdx.x), [bkl] "i"((unsigned)BK), [bkh] "i"((unsigned)(BK >> 32)), [kp] "i"(KP[0] | KP[1] << 5 | KP[2] << 10 | KP[3] << 15 | KP[4] << 20)
      : "memory", "scc", "s36", "s37", "s38", "s39", "s40", "s41", "s42", "s43", "v236", "v237", "v238", "v239", "v241", "v242", "v243", "v244", "v245", "v246", "v247");
#define SB __builtin_amdgcn_sched_barrier(0)
#define PIN(x) asm volatile("" : "+v"(x))
  const int w = gs[0], tid = w * 64 + lane;
  ws_cin = (const u16*)((unsigned long long)ka0[5] << 32 | ka0[4]);
  wout = (const u16*)((unsigned long long)ka0[13] << 32 | ka0[12]);
  ws = (char*)((unsigned long long)ka1[1] << 32 | ka1[0]);
  const unsigned sb = (unsigned)(size_t)(__attribute__((address_space(3))) char*)shm;
  const int g = lane >> 4, fr = lane & 15, hf = lane >> 5, l31 = lane & 31;
  constexpr unsigned TX = 0xA5C30F96u;
  auto chk = [](unsigned t) -> unsigned { return (t * 0x9E3779B1u) >> 24; };
  const int bid = blk & 7;
  const bool rblk = bid >= 4;
  const int fi = (bid & 3) * 8 + w, mi_ = min(fi, NF - 1);
  const unsigned tc = sb + (g & 1) * 32 + (fr & 3) * 8, h0 = 8 * (g >> 1) + (fr >> 2);
  u32x4 mrow;
  u32x2 rsp;
  unsigned msc;
  {
    unsigned ma_, ur[2], uw[6], fa[2][2];
    if (rblk) {
      const unsigned o3 = min(w * 4480, NF * NF * NF * 2 - 4480);
      asm volatile("s_mov_b32 m0, %2\n\ts_nop 0\n\tglobal_load_lds_dwordx4 %0, %1\n\tglobal_load_lds_dwordx4 %0, %1 offset:1024\n\tglobal_load_lds_dwordx4 %0, %1 offset:2048\n\t"
                   "global_load_lds_dwordx4 %0, %1 offset:3072\n\tglobal_load_lds_dwordx4 %0, %1 offset:3456"
                   :: "v"(lane * 16), "s"((const char*)(ws_cin + 2 * NF * NF * NF) + o3), "s"(sb + W3S + o3) : "memory");
      const unsigned rr = (bid & 3) * 8 + ((lane >> 2) & 7);
      const unsigned vo = (hf * NF * NF + min(rr, 25u) * NF) * 2 + (lane & 3) * 16;
#pragma unroll
      for (int m = 0; m < 2; ++m) {
        const unsigned p = min(w + 8 * m, 12);
        asm volatile("s_mov_b32 m0, %2\n\ts_nop 0\n\tglobal_load_lds_dwordx4 %0, %1" :: "v"(vo), "s"(ws_cin + (NF + 2 * p) * NF * NF), "s"(sb + W2S + p * 1024) : "memory");
      }
      asm volatile("ds_write_b32 %0, %1" :: "v"(sb + U_OFF + tid * 4), "v"(0) : "memory");
    } else {
#pragma unroll
      for (int s = 0; s < 2; ++s)
        asm volatile("s_mov_b32 m0, %2\n\ts_nop 0\n\tglobal_load_lds_dwordx4 %0, %1" :: "v"(min(l31, NF - 1) * NF * 2 + hf * 16), "s"(ws_cin + mi_ * NF * NF + s * 16), "s"(sb + W3S + w * DP + s * 1024) : "memory");
    }
    if (w < 3) asm volatile("s_mov_b32 m0, %2\n\ts_nop 0\n\tglobal_load_lds_dwordx4 %0, %1 sc1" :: "v"(lane * 16), "s"(w ? (const char*)&g_tab + (w - 1) * 1024 : ws), "s"(sb + NC_OFF + w * 1024) : "memory");
    if (w == 3) asm volatile("s_mov_b32 m0, %2\n\ts_nop 0\n\tglobal_load_lds_dword %0, %1" :: "v"(min(lane, 38) * 4), "s"(wout), "s"(sb + WO_OFF) : "memory");
    {
      const int ty = bid >> 2, f = lane >> 2;
      const bool sp = fi >= 24;
      int c0 = sp ? 5 : fi < 16 ? ty - 1 : 1, c1 = sp ? 16 : 3072, rho = sp ? 4 * (fi - 24) + 2 * ty : fi < 16 ? fi : 2 * (fi & 6) + (fi & 1) + 2 * ty;
      asm volatile("" : "+s"(c0), "+s"(c1), "+s"(rho));
      int t = (7 * (f >> 1) + c0) * 1024 + (f & 1) * c1 + ((lane & 3) * 16 + rho) * 16;
      PIN(t);
      ma_ = min((unsigned)t | (fi < 28 ? 0u : ~0u), 41u * 1024 + 1024);
      ma_ = lane < 48 ? ma_ : 43008;
    }
#pragma unroll
    for (int p = 0; p < 2; ++p) {
      ur[p] = sb + W3S + min(8 * g + 4 * p + (fr >> 2), NF - 1) * (NF * NF * 2) + w * 32 + (fr & 3) * 8;
      fa[0][p] = tc + W2S + ((h0 + 4 * p) * 8 + w) * 64; fa[1][p] = tc + W2S + (min(h0 + 16 + 4 * p, 25u) * 8 + w) * 64;
      PIN(ur[p]); PIN(fa[0][p]); PIN(fa[1][p]);
    }
#pragma unroll
    for (int m = 0; m < 6; ++m) {
      const unsigned e = (w + 8 * m) * 16 + fr;
      uw[m] = sb + U_OFF + e * 2 + ((e * 2521u) >> 16) * 12;
      PIN(uw[m]);
      if (m == 5) uw[m] = e < NF * NF ? uw[m] : 0x40000u;
    }
    PIN(uw[5]); PIN(ma_);
    asm volatile("s_waitcnt vmcnt(0) lgkmcnt(0)\n\ts_barrier" :: "{v253}"(tch) : "memory");
    const unsigned wov = *(const unsigned*)(shm + WO_OFF + lane * 4);
    const u32x4 tb = *(const u32x4*)(shm + GT_OFF + (fi >= 24) * 1024 + lane * 16);
    bf16x8 P[2], Q[2];
    if (rblk) {
      s16x4 wl[6][2];
      u32x4 wa;
      unsigned wo2;
      asm volatile("ds_read2_b64 %0, %1 offset0:13 offset1:14" : "=v"(wa) : "v"(sb + WO_OFF + g * 16));
      asm volatile("ds_read_u16 %0, %1 offset:52" : "=v"(wo2) : "v"(sb + WO_OFF + lane * 2));
#pragma unroll
      for (int m = 0; m < 6; ++m)
#pragma unroll
        for (int p = 0; p < 2; ++p) asm volatile("ds_read_b64_tr_b16 %0, %1 offset:%2" : "=v"(wl[m][p]) : "v"(ur[p]), "i"(m * 256));
      u32x2 fl[2][2];
#pragma unroll
      for (int s = 0; s < 2; ++s)
#pragma unroll
        for (int p = 0; p < 2; ++p) asm volatile("ds_read_b64_tr_b16 %0, %1" : "=v"(fl[s][p]) : "v"(fa[s][p]));
      asm volatile("s_waitcnt lgkmcnt(0)" : "+v"(wa), "+v"(wo2), "+v"(wl[0][0]), "+v"(wl[0][1]), "+v"(wl[1][0]), "+v"(wl[1][1]), "+v"(wl[2][0]), "+v"(wl[2][1]), "+v"(wl[3][0]), "+v"(wl[3][1]),
                   "+v"(wl[4][0]), "+v"(wl[4][1]), "+v"(wl[5][0]), "+v"(wl[5][1]), "+v"(fl[0][0]), "+v"(fl[0][1]), "+v"(fl[1][0]), "+v"(fl[1][1]));
      const unsigned km = g < 3 ? ~0u : 0u;
      const u32x4 wm = {wa[0], wa[1] & km, wa[2] & km, wa[3] & km};
      f32x4 du[6];
#pragma unroll
      for (int m = 0; m < 6; ++m)
        du[m] = __builtin_amdgcn_mfma_f32_16x16x32_bf16(__builtin_bit_cast(bf16x8, wm), __builtin_shufflevector(wl[m][0], wl[m][1], 0, 1, 2, 3, 4, 5, 6, 7), (f32x4){0.f, 0.f, 0.f, 0.f}, 0, 0, 0);
      asm volatile("ds_write_b16 %0, %1" :: "v"(lane < NF ? sb + U_OFF + lane * 64 + 52 : 0x40000u), "v"(wo2) : "memory");
#pragma unroll
      for (int m = 0; m < 6; ++m) {
        asm volatile("ds_write_b16 %0, %1" :: "v"(uw[m]), "v"(pk2(du[m][0], du[m][0])) : "memory");
        asm volatile("" :: "v"(du[m]));
      }
      asm volatile("s_waitcnt lgkmcnt(0)\n\ts_barrier" ::: "memory");
      s16x4 ul[2][2];
#pragma unroll
      for (int s = 0; s < 2; ++s)
#pragma unroll
        for (int p = 0; p < 2; ++p)
          asm volatile("ds_read_b64_tr_b16 %0, %1 offset:%2" : "=v"(ul[s][p]) : "v"(tc + U_OFF + h0 * 64), "i"(s * 1024 + p * 256));
      const unsigned fmask = l31 < NF ? ~0u : 0u;
#pragma unroll
      for (int s = 0; s < 2; ++s) {
        u32x4 q4 = {fl[s][0][0] & fmask, fl[s][0][1] & fmask, fl[s][1][0] & fmask, fl[s][1][1] & fmask};
        Q[s] = __builtin_bit_cast(bf16x8, q4);
      }
      asm volatile("s_waitcnt lgkmcnt(0)" : "+v"(ul[0][0]), "+v"(ul[0][1]), "+v"(ul[1][0]), "+v"(ul[1][1]));
#pragma unroll
      for (int s = 0; s < 2; ++s) P[s] = __builtin_shufflevector(ul[s][0], ul[s][1], 0, 1, 2, 3, 4, 5, 6, 7);
    } else {
#pragma unroll
      for (int s = 0; s < 2; ++s) {
        u32x4 xf = *(const u32x4*)(shm + W3S + w * DP + s * 1024 + lane * 16);
        if (s == 1) { const unsigned m = hf ? 0u : ~0u; xf[1] &= m; xf[2] &= m; xf[3] &= m; }
        Q[s] = __builtin_bit_cast(bf16x8, xf);
        const unsigned e = (l31 >> 1) - 8 * s - 4 * hf, v = 0x3f80u << ((lane & 1) * 16);
        u32x4 q4 = {e == 0 ? v : 0u, e == 1 ? v : 0u, e == 2 ? v : 0u, e == 3 ? v : 0u};
        P[s] = __builtin_bit_cast(bf16x8, q4);
      }
    }
    f32x16 D = {};
#pragma unroll
    for (int s = 0; s < 2; ++s) {
      D = __builtin_amdgcn_mfma_f32_32x32x16_bf16(P[s], Q[s], D, 0, 0, 0);
      D = __builtin_amdgcn_mfma_f32_32x32x16_bf16(Q[s], P[s], D, 0, 0, 0);
    }
    float* dl = (float*)(shm + W3S + w * DP);
    D[14] = lane == NF ? __uint_as_float(((unsigned)__builtin_amdgcn_readlane(wov, mi_ >> 1) >> ((mi_ & 1) * 16)) << 16) : D[14];
#pragma unroll
    for (int r = 0; r < 15; ++r) dl[((r & 3) + 8 * (r >> 2) + 4 * hf) * 33 + l31] = D[r];
    __builtin_amdgcn_wave_barrier();
    float v[8], mx = 0.f;
    const unsigned zm = fi >= NF ? 0xffff : rblk ? 0x8000 : 0xc000;
#pragma unroll
    for (int e = 0; e < 8; ++e) v[e] = dl[(tb[e >> 1] >> (16 * (e & 1))) & 0x3ff];
#pragma unroll
    for (int e = 0; e < 8; ++e) {
      const unsigned c = tb[e >> 1] >> (16 * (e & 1));
      float x = __builtin_amdgcn_ldexpf(v[e], ((c >> 10) & 15) - 2);
      PIN(x);
      v[e] = c & zm ? 0.f : x;
      mx = fmaxf(mx, fabsf(v[e]));
    }
    mx = fmaxf(mx, dpp<0xB1>(mx)); mx = fmaxf(mx, dpp<0x4E>(mx)); mx = fmaxf(mx, dpp<0x141>(mx)); mx = fmaxf(mx, dpp<0x140>(mx));
    const unsigned mxu = max(max((unsigned)__builtin_amdgcn_readlane(__float_as_int(mx), 0), (unsigned)__builtin_amdgcn_readlane(__float_as_int(mx), 16)), (unsigned)__builtin_amdgcn_readlane(__float_as_int(mx), 32));
    const unsigned ex = min(max(mxu >> 23, 40u), 250u);
    const float al = __uint_as_float((268 - ex) << 23);
#pragma unroll
    for (int e = 0; e < 4; ++e) {
      const unsigned q = pk2(v[2 * e] * al, v[2 * e + 1] * al);
      mrow[e] = __builtin_bit_cast(unsigned, __builtin_amdgcn_cvt_pkrtz(__uint_as_float(q << 16), __uint_as_float(q & 0xffff0000u)));
    }
    msc = fi < NF ? (ex - 14) << 23 : 0u;
    PIN(msc);
    asm volatile("global_store_dwordx4 %0, %1, %2 sc1" :: "v"(ma_), "{v[248:251]}"(mrow), "s"(ws + WS_M) : "memory");
    asm volatile("global_store_dword %0, %1, %2 sc1" :: "v"((fi < 16 ? (fi >> 2) * 64 + (fi & 3) * 4 : fi < 24 ? ((fi & 6) << 5) + (fi & 1) * 4 + 16 : (fi & 3) * 64 + (fi & 4) + 24) + (bid & 4) * 8), "{v252}"(msc), "s"(ws + WS_M + MC) : "memory");
    if (fi == 31) asm volatile("global_store_dword %0, %1, %2 sc1\n\ts_waitcnt vmcnt(0)" :: "v"((lane & 15) * 128), "{v252}"(msc), "s"(ws + WS_POOL) : "memory");
    const unsigned mval = *(const unsigned*)(shm + NC_OFF + (tid & 255) * 4), qval = *(const unsigned*)(shm + NC_OFF);
    rsp = (u32x2){qval << 8 | chk(mval), mval ^ TX};
    PIN(rsp);
    asm volatile("s_waitcnt lgkmcnt(0)\n\ts_barrier" ::: "memory");
  }
  static_assert(STAGE == 45056 && MC == 41984 && NSLOT == 21 && WS_RESP == 4096 && WS_POOL == 32768 && WS_M == 1048576 && NCL == 8 && sizeof(g_tab.t) == 2048, "constants of the code below");
  asm volatile(
      "s_cmp_lt_u32 s24, 4\ns_cbranch_scc1 .Lbp\ns_branch .Lbg\n"
      ".Lg14:\ns_waitcnt vmcnt(4)\ns_barrier\nds_read_b64_tr_b16 v[64:65], v104 offset:45056\nds_read_b64_tr_b16 v[66:67], v104 offset:46080\nds_read_b64_tr_b16 v[68:69], v104 offset:53248\n"
      "ds_read_b64_tr_b16 v[70:71], v104 offset:54272\nds_read_b64_tr_b16 v[72:73], v105 offset:45056\nds_read_b64_tr_b16 v[74:75], v105 offset:46080\n"
      "ds_read_b64_tr_b16 v[76:77], v105 offset:53248\nds_read_b64_tr_b16 v[78:79], v105 offset:54272\nds_read_b64_tr_b16 v[80:81], v106 offset:45056\n"
      "ds_read_b64_tr_b16 v[82:83], v106 offset:46080\nds_read_b64_tr_b16 v[84:85], v106 offset:53248\nds_read_b64_tr_b16 v[86:87], v106 offset:54272\n"
      "ds_read_b64_tr_b16 v[88:89], v107 offset:45056\nds_read_b64_tr_b16 v[90:91], v107 offset:46080\nds_read_b64_tr_b16 v[92:93], v107 offset:53248\n"
      "ds_read_b64_tr_b16 v[94:95], v107 offset:54272\nds_read_b128 v[0:3], v100 offset:45056\nds_read_b128 v[4:7], v101 offset:45056\nds_read_b128 v[8:11], v120 offset:45056\n"
      "ds_read_b128 v[12:15], v121 offset:45056\nds_read_b128 v[16:19], v96 offset:45056\ns_waitcnt lgkmcnt(1)\n.p2align 3\n"
      ".Lgl14:\nv_mfma_f32_16x16x32_bf16 v[124:127], v[64:67], v[0:3], v[124:127]\nds_read_b128 v[20:23], v97 offset:45056\nv_mfma_f32_16x16x32_bf16 v[124:127], v[68:71], v[4:7], v[124:127]\n"
      "ds_read_b128 v[24:27], v96 offset:47104\nv_mfma_f32_16x16x32_bf16 v[128:131], v[76:79], v[4:7], v[128:131]\nds_read_b128 v[28:31], v97 offset:47104\n"
      "v_mfma_f32_16x16x32_bf16 v[128:131], v[72:75], v[0:3], v[128:131]\nv_mfma_f32_16x16x32_bf16 v[132:135], v[80:83], v[0:3], v[132:135]\n"
      "v_mfma_f32_16x16x32_bf16 v[132:135], v[84:87], v[4:7], v[132:135]\nv_mfma_f32_16x16x32_bf16 v[136:139], v[92:95], v[4:7], v[136:139]\n"
      "v_mfma_f32_16x16x32_bf16 v[136:139], v[88:91], v[0:3], v[136:139]\nv_mfma_f32_16x16x32_bf16 v[152:155], v[88:91], v[8:11], v[152:155]\nds_read_b128 v[0:3], v96 offset:49152\n"
      "v_mfma_f32_16x16x32_bf16 v[152:155], v[92:95], v[12:15], v[152:155]\nds_read_b128 v[4:7], v97 offset:49152\nv_mfma_f32_16x16x32_bf16 v[148:151], v[84:87], v[12:15], v[148:151]\n"
      "v_mfma_f32_16x16x32_bf16 v[148:151], v[80:83], v[8:11], v[148:151]\nv_mfma_f32_16x16x32_bf16 v[144:147], v[72:75], v[8:11], v[144:147]\n"
      "v_mfma_f32_16x16x32_bf16 v[144:147], v[76:79], v[12:15], v[144:147]\nv_mfma_f32_16x16x32_bf16 v[140:143], v[68:71], v[12:15], v[140:143]\ns_waitcnt lgkmcnt(4)\n"
      "v_mfma_f32_16x16x32_bf16 v[140:143], v[64:67], v[8:11], v[140:143]\nv_mfma_f32_16x16x32_bf16 v[156:159], v[64:67], v[16:19], v[156:159]\nds_read_b128 v[8:11], v96 offset:51200\n"
      "v_mfma_f32_16x16x32_bf16 v[156:159], v[68:71], v[20:23], v[156:159]\nds_read_b128 v[12:15], v97 offset:51200\nv_mfma_f32_16x16x32_bf16 v[160:163], v[76:79], v[20:23], v[160:163]\n"
      "v_mfma_f32_16x16x32_bf16 v[160:163], v[72:75], v[16:19], v[160:163]\nv_mfma_f32_16x16x32_bf16 v[164:167], v[80:83], v[16:19], v[164:167]\n"
      "v_mfma_f32_16x16x32_bf16 v[164:167], v[84:87], v[20:23], v[164:167]\nv_mfma_f32_16x16x32_bf16 v[168:171], v[92:95], v[20:23], v[168:171]\ns_waitcnt lgkmcnt(2)\n"
      "v_mfma_f32_16x16x32_bf16 v[168:171], v[88:91], v[16:19], v[168:171]\nv_mfma_f32_16x16x32_bf16 v[184:187], v[88:91], v[24:27], v[184:187]\nds_read_b128 v[16:19], v96 offset:53248\n"
      "v_mfma_f32_16x16x32_bf16 v[184:187], v[92:95], v[28:31], v[184:187]\nds_read_b128 v[20:23], v97 offset:53248\nv_mfma_f32_16x16x32_bf16 v[180:183], v[84:87], v[28:31], v[180:183]\n"
      "v_mfma_f32_16x16x32_bf16 v[180:183], v[80:83], v[24:27], v[180:183]\nv_mfma_f32_16x16x32_bf16 v[176:179], v[72:75], v[24:27], v[176:179]\n"
      "v_mfma_f32_16x16x32_bf16 v[176:179], v[76:79], v[28:31], v[176:179]\ns_waitcnt vmcnt(0) lgkmcnt(0)\nv_mfma_f32_16x16x32_bf16 v[172:175], v[68:71], v[28:31], v[172:175]\ns_barrier\n"
      "v_mfma_f32_16x16x32_bf16 v[172:175], v[64:67], v[24:27], v[172:175]\nds_read_b64_tr_b16 v[32:33], v108 offset:0\nv_mfma_f32_16x16x32_bf16 v[188:191], v[64:67], v[0:3], v[188:191]\n"
      "ds_read_b64_tr_b16 v[34:35], v108 offset:1024\nv_mfma_f32_16x16x32_bf16 v[188:191], v[68:71], v[4:7], v[188:191]\nds_read_b64_tr_b16 v[36:37], v108 offset:8192\n"
      "v_mfma_f32_16x16x32_bf16 v[192:195], v[76:79], v[4:7], v[192:195]\nds_read_b64_tr_b16 v[38:39], v108 offset:9216\nv_mfma_f32_16x16x32_bf16 v[192:195], v[72:75], v[0:3], v[192:195]\n"
      "ds_read_b64_tr_b16 v[40:41], v109 offset:0\nv_mfma_f32_16x16x32_bf16 v[196:199], v[80:83], v[0:3], v[196:199]\nds_read_b64_tr_b16 v[42:43], v109 offset:1024\n"
      "v_mfma_f32_16x16x32_bf16 v[196:199], v[84:87], v[4:7], v[196:199]\nds_read_b64_tr_b16 v[44:45], v109 offset:8192\nv_mfma_f32_16x16x32_bf16 v[200:203], v[92:95], v[4:7], v[200:203]\n"
      "ds_read_b64_tr_b16 v[46:47], v109 offset:9216\nv_mfma_f32_16x16x32_bf16 v[200:203], v[88:91], v[0:3], v[200:203]\nds_read_b64_tr_b16 v[48:49], v110 offset:0\n"
      "v_mfma_f32_16x16x32_bf16 v[216:219], v[88:91], v[8:11], v[216:219]\nds_read_b128 v[0:3], v102 offset:0\nv_mfma_f32_16x16x32_bf16 v[216:219], v[92:95], v[12:15], v[216:219]\n"
      "ds_read_b128 v[4:7], v103 offset:0\nv_mfma_f32_16x16x32_bf16 v[212:215], v[84:87], v[12:15], v[212:215]\nds_read_b64_tr_b16 v[50:51], v110 offset:1024\n"
      "v_mfma_f32_16x16x32_bf16 v[212:215], v[80:83], v[8:11], v[212:215]\nds_read_b64_tr_b16 v[52:53], v110 offset:8192\nv_mfma_f32_16x16x32_bf16 v[208:211], v[72:75], v[8:11], v[208:211]\n"
      "ds_read_b64_tr_b16 v[54:55], v110 offset:9216\nv_mfma_f32_16x16x32_bf16 v[208:211], v[76:79], v[12:15], v[208:211]\nds_read_b64_tr_b16 v[56:57], v111 offset:0\n"
      "v_mfma_f32_16x16x32_bf16 v[204:207], v[68:71], v[12:15], v[204:207]\nds_read_b64_tr_b16 v[58:59], v111 offset:1024\nv_mfma_f32_16x16x32_bf16 v[204:207], v[64:67], v[8:11], v[204:207]\n"
      "ds_read_b64_tr_b16 v[60:61], v111 offset:8192\nv_mfma_f32_16x16x32_bf16 v[220:223], v[64:67], v[16:19], v[220:223]\nds_read_b128 v[8:11], v122 offset:0\n"
      "v_mfma_f32_16x16x32_bf16 v[220:223], v[68:71], v[20:23], v[220:223]\nds_read_b128 v[12:15], v123 offset:0\nv_mfma_f32_16x16x32_bf16 v[224:227], v[76:79], v[20:23], v[224:227]\n"
      "ds_read_b64_tr_b16 v[62:63], v111 offset:9216\nv_mfma_f32_16x16x32_bf16 v[224:227], v[72:75], v[16:19], v[224:227]\nv_mfma_f32_16x16x32_bf16 v[228:231], v[80:83], v[16:19], v[228:231]\n"
      "v_mfma_f32_16x16x32_bf16 v[228:231], v[84:87], v[20:23], v[228:231]\nv_mfma_f32_16x16x32_bf16 v[232:235], v[92:95], v[20:23], v[232:235]\ns_waitcnt lgkmcnt(0)\n"
      "v_mfma_f32_16x16x32_bf16 v[232:235], v[88:91], v[16:19], v[232:235]\nds_read_b128 v[16:19], v98 offset:0\nv_mfma_f32_16x16x32_bf16 v[124:127], v[32:35], v[0:3], v[124:127]\n"
      "ds_read_b128 v[20:23], v99 offset:0\nv_mfma_f32_16x16x32_bf16 v[124:127], v[36:39], v[4:7], v[124:127]\nds_read_b128 v[24:27], v98 offset:2048\n"
      "v_mfma_f32_16x16x32_bf16 v[128:131], v[44:47], v[4:7], v[128:131]\nds_read_b128 v[28:31], v99 offset:2048\nv_mfma_f32_16x16x32_bf16 v[128:131], v[40:43], v[0:3], v[128:131]\n"
      "v_mfma_f32_16x16x32_bf16 v[132:135], v[48:51], v[0:3], v[132:135]\nv_mfma_f32_16x16x32_bf16 v[132:135], v[52:55], v[4:7], v[132:135]\n"
      "v_mfma_f32_16x16x32_bf16 v[136:139], v[60:63], v[4:7], v[136:139]\nv_mfma_f32_16x16x32_bf16 v[136:139], v[56:59], v[0:3], v[136:139]\n"
      "v_mfma_f32_16x16x32_bf16 v[152:155], v[56:59], v[8:11], v[152:155]\nds_read_b128 v[0:3], v98 offset:4096\nv_mfma_f32_16x16x32_bf16 v[152:155], v[60:63], v[12:15], v[152:155]\n"
      "ds_read_b128 v[4:7], v99 offset:4096\nv_mfma_f32_16x16x32_bf16 v[148:151], v[52:55], v[12:15], v[148:151]\nv_mfma_f32_16x16x32_bf16 v[148:151], v[48:51], v[8:11], v[148:151]\n"
      "v_mfma_f32_16x16x32_bf16 v[144:147], v[40:43], v[8:11], v[144:147]\nv_mfma_f32_16x16x32_bf16 v[144:147], v[44:47], v[12:15], v[144:147]\n"
      "v_mfma_f32_16x16x32_bf16 v[140:143], v[36:39], v[12:15], v[140:143]\ns_waitcnt lgkmcnt(4)\nv_mfma_f32_16x16x32_bf16 v[140:143], v[32:35], v[8:11], v[140:143]\n"
      "v_mfma_f32_16x16x32_bf16 v[156:159], v[32:35], v[16:19], v[156:159]\nds_read_b128 v[8:11], v98 offset:6144\nv_mfma_f32_16x16x32_bf16 v[156:159], v[36:39], v[20:23], v[156:159]\n"
      "ds_read_b128 v[12:15], v99 offset:6144\nv_mfma_f32_16x16x32_bf16 v[160:163], v[44:47], v[20:23], v[160:163]\nv_mfma_f32_16x16x32_bf16 v[160:163], v[40:43], v[16:19], v[160:163]\n"
      "v_mfma_f32_16x16x32_bf16 v[164:167], v[48:51], v[16:19], v[164:167]\nv_mfma_f32_16x16x32_bf16 v[164:167], v[52:55], v[20:23], v[164:167]\n"
      "v_mfma_f32_16x16x32_bf16 v[168:171], v[60:63], v[20:23], v[168:171]\ns_waitcnt lgkmcnt(2)\nv_mfma_f32_16x16x32_bf16 v[168:171], v[56:59], v[16:19], v[168:171]\n"
      "v_mfma_f32_16x16x32_bf16 v[184:187], v[56:59], v[24:27], v[184:187]\nds_read_b128 v[16:19], v98 offset:8192\nv_mfma_f32_16x16x32_bf16 v[184:187], v[60:63], v[28:31], v[184:187]\n"
      "ds_read_b128 v[20:23], v99 offset:8192\nv_mfma_f32_16x16x32_bf16 v[180:183], v[52:55], v[28:31], v[180:183]\nv_mfma_f32_16x16x32_bf16 v[180:183], v[48:51], v[24:27], v[180:183]\n"
      "v_mfma_f32_16x16x32_bf16 v[176:179], v[40:43], v[24:27], v[176:179]\nv_mfma_f32_16x16x32_bf16 v[176:179], v[44:47], v[28:31], v[176:179]\ns_waitcnt vmcnt(0) lgkmcnt(0)\n"
      "v_mfma_f32_16x16x32_bf16 v[172:175], v[36:39], v[28:31], v[172:175]\ns_barrier\nv_mfma_f32_16x16x32_bf16 v[172:175], v[32:35], v[24:27], v[172:175]\n"
      "ds_read_b64_tr_b16 v[64:65], v104 offset:0\nv_mfma_f32_16x16x32_bf16 v[188:191], v[32:35], v[0:3], v[188:191]\nds_read_b64_tr_b16 v[66:67], v104 offset:1024\n"
      "v_mfma_f32_16x16x32_bf16 v[188:191], v[36:39], v[4:7], v[188:191]\nds_read_b64_tr_b16 v[68:69], v104 offset:8192\nv_mfma_f32_16x16x32_bf16 v[192:195], v[44:47], v[4:7], v[192:195]\n"
      "ds_read_b64_tr_b16 v[70:71], v104 offset:9216\nv_mfma_f32_16x16x32_bf16 v[192:195], v[40:43], v[0:3], v[192:195]\nds_read_b64_tr_b16 v[72:73], v105 offset:0\n"
      "v_mfma_f32_16x16x32_bf16 v[196:199], v[48:51], v[0:3], v[196:199]\nds_read_b64_tr_b16 v[74:75], v105 offset:1024\nv_mfma_f32_16x16x32_bf16 v[196:199], v[52:55], v[4:7], v[196:199]\n"
      "ds_read_b64_tr_b16 v[76:77], v105 offset:8192\nv_mfma_f32_16x16x32_bf16 v[200:203], v[60:63], v[4:7], v[200:203]\nds_read_b64_tr_b16 v[78:79], v105 offset:9216\n"
      "v_mfma_f32_16x16x32_bf16 v[200:203], v[56:59], v[0:3], v[200:203]\nds_read_b64_tr_b16 v[80:81], v106 offset:0\nv_mfma_f32_16x16x32_bf16 v[216:219], v[56:59], v[8:11], v[216:219]\n"
      "ds_read_b128 v[0:3], v100 offset:0\nv_mfma_f32_16x16x32_bf16 v[216:219], v[60:63], v[12:15], v[216:219]\nds_read_b128 v[4:7], v101 offset:0\n"
      "v_mfma_f32_16x16x32_bf16 v[212:215], v[52:55], v[12:15], v[212:215]\nds_read_b64_tr_b16 v[82:83], v106 offset:1024\nv_mfma_f32_16x16x32_bf16 v[212:215], v[48:51], v[8:11], v[212:215]\n"
      "ds_read_b64_tr_b16 v[84:85], v106 offset:8192\nv_mfma_f32_16x16x32_bf16 v[208:211], v[40:43], v[8:11], v[208:211]\nds_read_b64_tr_b16 v[86:87], v106 offset:9216\n"
      "v_mfma_f32_16x16x32_bf16 v[208:211], v[44:47], v[12:15], v[208:211]\nds_read_b64_tr_b16 v[88:89], v107 offset:0\nv_mfma_f32_16x16x32_bf16 v[204:207], v[36:39], v[12:15], v[204:207]\n"
      "ds_read_b64_tr_b16 v[90:91], v107 offset:1024\nv_mfma_f32_16x16x32_bf16 v[204:207], v[32:35], v[8:11], v[204:207]\nds_read_b64_tr_b16 v[92:93], v107 offset:8192\n"
      "v_mfma_f32_16x16x32_bf16 v[220:223], v[32:35], v[16:19], v[220:223]\nds_read_b128 v[8:11], v120 offset:0\nv_mfma_f32_16x16x32_bf16 v[220:223], v[36:39], v[20:23], v[220:223]\n"
      "ds_read_b128 v[12:15], v121 offset:0\nv_mfma_f32_16x16x32_bf16 v[224:227], v[44:47], v[20:23], v[224:227]\nds_read_b64_tr_b16 v[94:95], v107 offset:9216\n"
      "v_mfma_f32_16x16x32_bf16 v[224:227], v[40:43], v[16:19], v[224:227]\nv_mfma_f32_16x16x32_bf16 v[228:231], v[48:51], v[16:19], v[228:231]\n"
      "v_mfma_f32_16x16x32_bf16 v[228:231], v[52:55], v[20:23], v[228:231]\nv_mfma_f32_16x16x32_bf16 v[232:235], v[60:63], v[20:23], v[232:235]\ns_waitcnt lgkmcnt(0)\n"
      "v_mfma_f32_16x16x32_bf16 v[232:235], v[56:59], v[16:19], v[232:235]\nds_read_b128 v[16:19], v96 offset:0\nv_mfma_f32_16x16x32_bf16 v[124:127], v[64:67], v[0:3], v[124:127]\n"
      "ds_read_b128 v[20:23], v97 offset:0\nv_mfma_f32_16x16x32_bf16 v[124:127], v[68:71], v[4:7], v[124:127]\nds_read_b128 v[24:27], v96 offset:2048\n"
      "v_mfma_f32_16x16x32_bf16 v[128:131], v[76:79], v[4:7], v[128:131]\nds_read_b128 v[28:31], v97 offset:2048\nv_mfma_f32_16x16x32_bf16 v[128:131], v[72:75], v[0:3], v[128:131]\n"
      "v_mfma_f32_16x16x32_bf16 v[132:135], v[80:83], v[0:3], v[132:135]\nv_mfma_f32_16x16x32_bf16 v[132:135], v[84:87], v[4:7], v[132:135]\n"
      "v_mfma_f32_16x16x32_bf16 v[136:139], v[92:95], v[4:7], v[136:139]\nv_mfma_f32_16x16x32_bf16 v[136:139], v[88:91], v[0:3], v[136:139]\n"
      "v_mfma_f32_16x16x32_bf16 v[152:155], v[88:91], v[8:11], v[152:155]\nds_read_b128 v[0:3], v96 offset:4096\nv_mfma_f32_16x16x32_bf16 v[152:155], v[92:95], v[12:15], v[152:155]\n"
      "ds_read_b128 v[4:7], v97 offset:4096\nv_mfma_f32_16x16x32_bf16 v[148:151], v[84:87], v[12:15], v[148:151]\nv_mfma_f32_16x16x32_bf16 v[148:151], v[80:83], v[8:11], v[148:151]\n"
      "v_mfma_f32_16x16x32_bf16 v[144:147], v[72:75], v[8:11], v[144:147]\nv_mfma_f32_16x16x32_bf16 v[144:147], v[76:79], v[12:15], v[144:147]\n"
      "v_mfma_f32_16x16x32_bf16 v[140:143], v[68:71], v[12:15], v[140:143]\ns_waitcnt lgkmcnt(4)\nv_mfma_f32_16x16x32_bf16 v[140:143], v[64:67], v[8:11], v[140:143]\n"
      "v_mfma_f32_16x16x32_bf16 v[156:159], v[64:67], v[16:19], v[156:159]\nds_read_b128 v[8:11], v96 offset:6144\nv_mfma_f32_16x16x32_bf16 v[156:159], v[68:71], v[20:23], v[156:159]\n"
      "ds_read_b128 v[12:15], v97 offset:6144\nv_mfma_f32_16x16x32_bf16 v[160:163], v[76:79], v[20:23], v[160:163]\nv_mfma_f32_16x16x32_bf16 v[160:163], v[72:75], v[16:19], v[160:163]\n"
      "v_mfma_f32_16x16x32_bf16 v[164:167], v[80:83], v[16:19], v[164:167]\nv_mfma_f32_16x16x32_bf16 v[164:167], v[84:87], v[20:23], v[164:167]\n"
      "v_mfma_f32_16x16x32_bf16 v[168:171], v[92:95], v[20:23], v[168:171]\ns_waitcnt lgkmcnt(2)\nv_mfma_f32_16x16x32_bf16 v[168:171], v[88:91], v[16:19], v[168:171]\n"
      "v_mfma_f32_16x16x32_bf16 v[184:187], v[88:91], v[24:27], v[184:187]\nds_read_b128 v[16:19], v96 offset:8192\nv_mfma_f32_16x16x32_bf16 v[184:187], v[92:95], v[28:31], v[184:187]\n"
      "ds_read_b128 v[20:23], v97 offset:8192\nv_mfma_f32_16x16x32_bf16 v[180:183], v[84:87], v[28:31], v[180:183]\nv_mfma_f32_16x16x32_bf16 v[180:183], v[80:83], v[24:27], v[180:183]\n"
      "v_mfma_f32_16x16x32_bf16 v[176:179], v[72:75], v[24:27], v[176:179]\nv_mfma_f32_16x16x32_bf16 v[176:179], v[76:79], v[28:31], v[176:179]\ns_waitcnt vmcnt(0) lgkmcnt(0)\n"
      "v_mfma_f32_16x16x32_bf16 v[172:175], v[68:71], v[28:31], v[172:175]\ns_barrier\nv_mfma_f32_16x16x32_bf16 v[172:175], v[64:67], v[24:27], v[172:175]\n"
      "ds_read_b64_tr_b16 v[32:33], v104 offset:45056\nv_mfma_f32_16x16x32_bf16 v[188:191], v[64:67], v[0:3], v[188:191]\nds_read_b64_tr_b16 v[34:35], v104 offset:46080\n"
      "v_mfma_f32_16x16x32_bf16 v[188:191], v[68:71], v[4:7], v[188:191]\nds_read_b64_tr_b16 v[36:37], v104 offset:53248\nv_mfma_f32_16x16x32_bf16 v[192:195], v[76:79], v[4:7], v[192:195]\n"
      "ds_read_b64_tr_b16 v[38:39], v104 offset:54272\nv_mfma_f32_16x16x32_bf16 v[192:195], v[72:75], v[0:3], v[192:195]\nds_read_b64_tr_b16 v[40:41], v105 offset:45056\n"
      "v_mfma_f32_16x16x32_bf16 v[196:199], v[80:83], v[0:3], v[196:199]\nds_read_b64_tr_b16 v[42:43], v105 offset:46080\nv_mfma_f32_16x16x32_bf16 v[196:199], v[84:87], v[4:7], v[196:199]\n"
      "ds_read_b64_tr_b16 v[44:45], v105 offset:53248\nv_mfma_f32_16x16x32_bf16 v[200:203], v[92:95], v[4:7], v[200:203]\nds_read_b64_tr_b16 v[46:47], v105 offset:54272\n"
      "v_mfma_f32_16x16x32_bf16 v[200:203], v[88:91], v[0:3], v[200:203]\nds_read_b64_tr_b16 v[48:49], v106 offset:45056\nv_mfma_f32_16x16x32_bf16 v[216:219], v[88:91], v[8:11], v[216:219]\n"
      "ds_read_b128 v[0:3], v100 offset:45056\nv_mfma_f32_16x16x32_bf16 v[216:219], v[92:95], v[12:15], v[216:219]\nds_read_b128 v[4:7], v101 offset:45056\n"
      "v_mfma_f32_16x16x32_bf16 v[212:215], v[84:87], v[12:15], v[212:215]\nds_read_b64_tr_b16 v[50:51], v106 offset:46080\nv_mfma_f32_16x16x32_bf16 v[212:215], v[80:83], v[8:11], v[212:215]\n"
      "ds_read_b64_tr_b16 v[52:53], v106 offset:53248\nv_mfma_f32_16x16x32_bf16 v[208:211], v[72:75], v[8:11], v[208:211]\nds_read_b64_tr_b16 v[54:55], v106 offset:54272\n"
      "v_mfma_f32_16x16x32_bf16 v[208:211], v[76:79], v[12:15], v[208:211]\nds_read_b64_tr_b16 v[56:57], v107 offset:45056\nv_mfma_f32_16x16x32_bf16 v[204:207], v[68:71], v[12:15], v[204:207]\n"
      "ds_read_b64_tr_b16 v[58:59], v107 offset:46080\nv_mfma_f32_16x16x32_bf16 v[204:207], v[64:67], v[8:11], v[204:207]\nds_read_b64_tr_b16 v[60:61], v107 offset:53248\n"
      "v_mfma_f32_16x16x32_bf16 v[220:223], v[64:67], v[16:19], v[220:223]\nds_read_b128 v[8:11], v120 offset:45056\nv_mfma_f32_16x16x32_bf16 v[220:223], v[68:71], v[20:23], v[220:223]\n"
      "ds_read_b128 v[12:15], v121 offset:45056\nv_mfma_f32_16x16x32_bf16 v[224:227], v[76:79], v[20:23], v[224:227]\nds_read_b64_tr_b16 v[62:63], v107 offset:54272\n"
      "v_mfma_f32_16x16x32_bf16 v[224:227], v[72:75], v[16:19], v[224:227]\nv_mfma_f32_16x16x32_bf16 v[228:231], v[80:83], v[16:19], v[228:231]\n"
      "v_mfma_f32_16x16x32_bf16 v[228:231], v[84:87], v[20:23], v[228:231]\nv_mfma_f32_16x16x32_bf16 v[232:235], v[92:95], v[20:23], v[232:235]\ns_waitcnt lgkmcnt(0)\n"
      "v_mfma_f32_16x16x32_bf16 v[232:235], v[88:91], v[16:19], v[232:235]\nds_read_b128 v[16:19], v96 offset:45056\nv_mfma_f32_16x16x32_bf16 v[124:127], v[32:35], v[0:3], v[124:127]\n"
      "ds_read_b128 v[20:23], v97 offset:45056\nv_mfma_f32_16x16x32_bf16 v[124:127], v[36:39], v[4:7], v[124:127]\nds_read_b128 v[24:27], v96 offset:47104\n"
      "v_mfma_f32_16x16x32_bf16 v[128:131], v[44:47], v[4:7], v[128:131]\nds_read_b128 v[28:31], v97 offset:47104\nv_mfma_f32_16x16x32_bf16 v[128:131], v[40:43], v[0:3], v[128:131]\n"
      "v_mfma_f32_16x16x32_bf16 v[132:135], v[48:51], v[0:3], v[132:135]\nv_mfma_f32_16x16x32_bf16 v[132:135], v[52:55], v[4:7], v[132:135]\n"
      "v_mfma_f32_16x16x32_bf16 v[136:139], v[60:63], v[4:7], v[136:139]\nv_mfma_f32_16x16x32_bf16 v[136:139], v[56:59], v[0:3], v[136:139]\n"
      "v_mfma_f32_16x16x32_bf16 v[152:155], v[56:59], v[8:11], v[152:155]\nds_read_b128 v[0:3], v96 offset:49152\nv_mfma_f32_16x16x32_bf16 v[152:155], v[60:63], v[12:15], v[152:155]\n"
      "ds_read_b128 v[4:7], v97 offset:49152\nv_mfma_f32_16x16x32_bf16 v[148:151], v[52:55], v[12:15], v[148:151]\nv_mfma_f32_16x16x32_bf16 v[148:151], v[48:51], v[8:11], v[148:151]\n"
      "v_mfma_f32_16x16x32_bf16 v[144:147], v[40:43], v[8:11], v[144:147]\nv_mfma_f32_16x16x32_bf16 v[144:147], v[44:47], v[12:15], v[144:147]\n"
      "v_mfma_f32_16x16x32_bf16 v[140:143], v[36:39], v[12:15], v[140:143]\ns_waitcnt lgkmcnt(4)\nv_mfma_f32_16x16x32_bf16 v[140:143], v[32:35], v[8:11], v[140:143]\n"
      "v_mfma_f32_16x16x32_bf16 v[156:159], v[32:35], v[16:19], v[156:159]\nds_read_b128 v[8:11], v96 offset:51200\nv_mfma_f32_16x16x32_bf16 v[156:159], v[36:39], v[20:23], v[156:159]\n"
      "ds_read_b128 v[12:15], v97 offset:51200\nv_mfma_f32_16x16x32_bf16 v[160:163], v[44:47], v[20:23], v[160:163]\nv_mfma_f32_16x16x32_bf16 v[160:163], v[40:43], v[16:19], v[160:163]\n"
      "v_mfma_f32_16x16x32_bf16 v[164:167], v[48:51], v[16:19], v[164:167]\nv_mfma_f32_16x16x32_bf16 v[164:167], v[52:55], v[20:23], v[164:167]\n"
      "v_mfma_f32_16x16x32_bf16 v[168:171], v[60:63], v[20:23], v[168:171]\ns_waitcnt lgkmcnt(2)\nv_mfma_f32_16x16x32_bf16 v[168:171], v[56:59], v[16:19], v[168:171]\n"
      "v_mfma_f32_16x16x32_bf16 v[184:187], v[56:59], v[24:27], v[184:187]\nds_read_b128 v[16:19], v96 offset:53248\nv_mfma_f32_16x16x32_bf16 v[184:187], v[60:63], v[28:31], v[184:187]\n"
      "ds_read_b128 v[20:23], v97 offset:53248\nv_mfma_f32_16x16x32_bf16 v[180:183], v[52:55], v[28:31], v[180:183]\nv_mfma_f32_16x16x32_bf16 v[180:183], v[48:51], v[24:27], v[180:183]\n"
      "v_mfma_f32_16x16x32_bf16 v[176:179], v[40:43], v[24:27], v[176:179]\nv_mfma_f32_16x16x32_bf16 v[176:179], v[44:47], v[28:31], v[176:179]\ns_waitcnt vmcnt(0) lgkmcnt(0)\n"
      "v_mfma_f32_16x16x32_bf16 v[172:175], v[36:39], v[28:31], v[172:175]\ns_barrier\nv_mfma_f32_16x16x32_bf16 v[172:175], v[32:35], v[24:27], v[172:175]\n"
      "ds_read_b64_tr_b16 v[64:65], v108 offset:0\nv_mfma_f32_16x16x32_bf16 v[188:191], v[32:35], v[0:3], v[188:191]\nds_read_b64_tr_b16 v[66:67], v108 offset:1024\n"
      "v_mfma_f32_16x16x32_bf16 v[188:191], v[36:39], v[4:7], v[188:191]\nds_read_b64_tr_b16 v[68:69], v108 offset:8192\nv_mfma_f32_16x16x32_bf16 v[192:195], v[44:47], v[4:7], v[192:195]\n"
      "ds_read_b64_tr_b16 v[70:71], v108 offset:9216\nv_mfma_f32_16x16x32_bf16 v[192:195], v[40:43], v[0:3], v[192:195]\nds_read_b64_tr_b16 v[72:73], v109 offset:0\n"
      "v_mfma_f32_16x16x32_bf16 v[196:199], v[48:51], v[0:3], v[196:199]\nds_read_b64_tr_b16 v[74:75], v109 offset:1024\nv_mfma_f32_16x16x32_bf16 v[196:199], v[52:55], v[4:7], v[196:199]\n"
      "ds_read_b64_tr_b16 v[76:77], v109 offset:8192\nv_mfma_f32_16x16x32_bf16 v[200:203], v[60:63], v[4:7], v[200:203]\nds_read_b64_tr_b16 v[78:79], v109 offset:9216\n"
      "v_mfma_f32_16x16x32_bf16 v[200:203], v[56:59], v[0:3], v[200:203]\nds_read_b64_tr_b16 v[80:81], v110 offset:0\nv_mfma_f32_16x16x32_bf16 v[216:219], v[56:59], v[8:11], v[216:219]\n"
      "ds_read_b128 v[0:3], v102 offset:0\nv_mfma_f32_16x16x32_bf16 v[216:219], v[60:63], v[12:15], v[216:219]\nds_read_b128 v[4:7], v103 offset:0\n"
      "v_mfma_f32_16x16x32_bf16 v[212:215], v[52:55], v[12:15], v[212:215]\nds_read_b64_tr_b16 v[82:83], v110 offset:1024\nv_mfma_f32_16x16x32_bf16 v[212:215], v[48:51], v[8:11], v[212:215]\n"
      "ds_read_b64_tr_b16 v[84:85], v110 offset:8192\nv_mfma_f32_16x16x32_bf16 v[208:211], v[40:43], v[8:11], v[208:211]\nds_read_b64_tr_b16 v[86:87], v110 offset:9216\n"
      "v_mfma_f32_16x16x32_bf16 v[208:211], v[44:47], v[12:15], v[208:211]\nds_read_b64_tr_b16 v[88:89], v111 offset:0\nv_mfma_f32_16x16x32_bf16 v[204:207], v[36:39], v[12:15], v[204:207]\n"
      "ds_read_b64_tr_b16 v[90:91], v111 offset:1024\nv_mfma_f32_16x16x32_bf16 v[204:207], v[32:35], v[8:11], v[204:207]\nds_read_b64_tr_b16 v[92:93], v111 offset:8192\n"
      "v_mfma_f32_16x16x32_bf16 v[220:223], v[32:35], v[16:19], v[220:223]\nds_read_b128 v[8:11], v122 offset:0\nv_mfma_f32_16x16x32_bf16 v[220:223], v[36:39], v[20:23], v[220:223]\n"
      "ds_read_b128 v[12:15], v123 offset:0\nv_mfma_f32_16x16x32_bf16 v[224:227], v[44:47], v[20:23], v[224:227]\nds_read_b64_tr_b16 v[94:95], v111 offset:9216\n"
      "v_mfma_f32_16x16x32_bf16 v[224:227], v[40:43], v[16:19], v[224:227]\nv_mfma_f32_16x16x32_bf16 v[228:231], v[48:51], v[16:19], v[228:231]\n"
      "v_mfma_f32_16x16x32_bf16 v[228:231], v[52:55], v[20:23], v[228:231]\nv_mfma_f32_16x16x32_bf16 v[232:235], v[60:63], v[20:23], v[232:235]\ns_waitcnt lgkmcnt(0)\n"
      "v_mfma_f32_16x16x32_bf16 v[232:235], v[56:59], v[16:19], v[232:235]\nds_read_b128 v[16:19], v98 offset:0\nv_mfma_f32_16x16x32_bf16 v[124:127], v[64:67], v[0:3], v[124:127]\n"
      "ds_read_b128 v[20:23], v99 offset:0\nv_mfma_f32_16x16x32_bf16 v[124:127], v[68:71], v[4:7], v[124:127]\nds_read_b128 v[24:27], v98 offset:2048\n"
      "v_mfma_f32_16x16x32_bf16 v[128:131], v[76:79], v[4:7], v[128:131]\nds_read_b128 v[28:31], v99 offset:2048\nv_mfma_f32_16x16x32_bf16 v[128:131], v[72:75], v[0:3], v[128:131]\n"
      "v_mfma_f32_16x16x32_bf16 v[132:135], v[80:83], v[0:3], v[132:135]\nv_mfma_f32_16x16x32_bf16 v[132:135], v[84:87], v[4:7], v[132:135]\n"
      "v_mfma_f32_16x16x32_bf16 v[136:139], v[92:95], v[4:7], v[136:139]\nv_mfma_f32_16x16x32_bf16 v[136:139], v[88:91], v[0:3], v[136:139]\n"
      "v_mfma_f32_16x16x32_bf16 v[152:155], v[88:91], v[8:11], v[152:155]\nds_read_b128 v[0:3], v98 offset:4096\nv_mfma_f32_16x16x32_bf16 v[152:155], v[92:95], v[12:15], v[152:155]\n"
      "ds_read_b128 v[4:7], v99 offset:4096\nv_mfma_f32_16x16x32_bf16 v[148:151], v[84:87], v[12:15], v[148:151]\nv_mfma_f32_16x16x32_bf16 v[148:151], v[80:83], v[8:11], v[148:151]\n"
      "v_mfma_f32_16x16x32_bf16 v[144:147], v[72:75], v[8:11], v[144:147]\nv_mfma_f32_16x16x32_bf16 v[144:147], v[76:79], v[12:15], v[144:147]\n"
      "v_mfma_f32_16x16x32_bf16 v[140:143], v[68:71], v[12:15], v[140:143]\ns_waitcnt lgkmcnt(4)\nv_mfma_f32_16x16x32_bf16 v[140:143], v[64:67], v[8:11], v[140:143]\n"
      "v_mfma_f32_16x16x32_bf16 v[156:159], v[64:67], v[16:19], v[156:159]\nds_read_b128 v[8:11], v98 offset:6144\nv_mfma_f32_16x16x32_bf16 v[156:159], v[68:71], v[20:23], v[156:159]\n"
      "ds_read_b128 v[12:15], v99 offset:6144\nv_mfma_f32_16x16x32_bf16 v[160:163], v[76:79], v[20:23], v[160:163]\nv_mfma_f32_16x16x32_bf16 v[160:163], v[72:75], v[16:19], v[160:163]\n"
      "v_mfma_f32_16x16x32_bf16 v[164:167], v[80:83], v[16:19], v[164:167]\nv_mfma_f32_16x16x32_bf16 v[164:167], v[84:87], v[20:23], v[164:167]\n"
      "v_mfma_f32_16x16x32_bf16 v[168:171], v[92:95], v[20:23], v[168:171]\ns_waitcnt lgkmcnt(2)\nv_mfma_f32_16x16x32_bf16 v[168:171], v[88:91], v[16:19], v[168:171]\n"
      "v_mfma_f32_16x16x32_bf16 v[184:187], v[88:91], v[24:27], v[184:187]\nds_read_b128 v[16:19], v98 offset:8192\nv_mfma_f32_16x16x32_bf16 v[184:187], v[92:95], v[28:31], v[184:187]\n"
      "ds_read_b128 v[20:23], v99 offset:8192\nv_mfma_f32_16x16x32_bf16 v[180:183], v[84:87], v[28:31], v[180:183]\nv_mfma_f32_16x16x32_bf16 v[180:183], v[80:83], v[24:27], v[180:183]\n"
      "v_mfma_f32_16x16x32_bf16 v[176:179], v[72:75], v[24:27], v[176:179]\nv_mfma_f32_16x16x32_bf16 v[176:179], v[76:79], v[28:31], v[176:179]\ns_waitcnt vmcnt(0) lgkmcnt(0)\n"
      "v_mfma_f32_16x16x32_bf16 v[172:175], v[68:71], v[28:31], v[172:175]\ns_barrier\nv_mfma_f32_16x16x32_bf16 v[172:175], v[64:67], v[24:27], v[172:175]\n"
      "ds_read_b64_tr_b16 v[32:33], v104 offset:0\nv_mfma_f32_16x16x32_bf16 v[188:191], v[64:67], v[0:3], v[188:191]\nds_read_b64_tr_b16 v[34:35], v104 offset:1024\n"
      "v_mfma_f32_16x16x32_bf16 v[188:191], v[68:71], v[4:7], v[188:191]\nds_read_b64_tr_b16 v[36:37], v104 offset:8192\nv_mfma_f32_16x16x32_bf16 v[192:195], v[76:79], v[4:7], v[192:195]\n"
      "ds_read_b64_tr_b16 v[38:39], v104 offset:9216\nv_mfma_f32_16x16x32_bf16 v[192:195], v[72:75], v[0:3], v[192:195]\nds_read_b64_tr_b16 v[40:41], v105 offset:0\n"
      "v_mfma_f32_16x16x32_bf16 v[196:199], v[80:83], v[0:3], v[196:199]\nds_read_b64_tr_b16 v[42:43], v105 offset:1024\nv_mfma_f32_16x16x32_bf16 v[196:199], v[84:87], v[4:7], v[196:199]\n"
      "ds_read_b64_tr_b16 v[44:45], v105 offset:8192\nv_mfma_f32_16x16x32_bf16 v[200:203], v[92:95], v[4:7], v[200:203]\nds_read_b64_tr_b16 v[46:47], v105 offset:9216\n"
      "v_mfma_f32_16x16x32_bf16 v[200:203], v[88:91], v[0:3], v[200:203]\nds_read_b64_tr_b16 v[48:49], v106 offset:0\nv_mfma_f32_16x16x32_bf16 v[216:219], v[88:91], v[8:11], v[216:219]\n"
      "ds_read_b128 v[0:3], v100 offset:0\nv_mfma_f32_16x16x32_bf16 v[216:219], v[92:95], v[12:15], v[216:219]\nds_read_b128 v[4:7], v101 offset:0\n"
      "v_mfma_f32_16x16x32_bf16 v[212:215], v[84:87], v[12:15], v[212:215]\nds_read_b64_tr_b16 v[50:51], v106 offset:1024\nv_mfma_f32_16x16x32_bf16 v[212:215], v[80:83], v[8:11], v[212:215]\n"
      "ds_read_b64_tr_b16 v[52:53], v106 offset:8192\nv_mfma_f32_16x16x32_bf16 v[208:211], v[72:75], v[8:11], v[208:211]\nds_read_b64_tr_b16 v[54:55], v106 offset:9216\n"
      "v_mfma_f32_16x16x32_bf16 v[208:211], v[76:79], v[12:15], v[208:211]\nds_read_b64_tr_b16 v[56:57], v107 offset:0\nv_mfma_f32_16x16x32_bf16 v[204:207], v[68:71], v[12:15], v[204:207]\n"
      "ds_read_b64_tr_b16 v[58:59], v107 offset:1024\nv_mfma_f32_16x16x32_bf16 v[204:207], v[64:67], v[8:11], v[204:207]\nds_read_b64_tr_b16 v[60:61], v107 offset:8192\n"
      "v_mfma_f32_16x16x32_bf16 v[220:223], v[64:67], v[16:19], v[220:223]\nds_read_b128 v[8:11], v120 offset:0\nv_mfma_f32_16x16x32_bf16 v[220:223], v[68:71], v[20:23], v[220:223]\n"
      "ds_read_b128 v[12:15], v121 offset:0\nv_mfma_f32_16x16x32_bf16 v[224:227], v[76:79], v[20:23], v[224:227]\nds_read_b64_tr_b16 v[62:63], v107 offset:9216\n"
      "v_mfma_f32_16x16x32_bf16 v[224:227], v[72:75], v[16:19], v[224:227]\nv_mfma_f32_16x16x32_bf16 v[228:231], v[80:83], v[16:19], v[228:231]\n"
      "v_mfma_f32_16x16x32_bf16 v[228:231], v[84:87], v[20:23], v[228:231]\nv_mfma_f32_16x16x32_bf16 v[232:235], v[92:95], v[20:23], v[232:235]\ns_waitcnt lgkmcnt(0)\n"
      "v_mfma_f32_16x16x32_bf16 v[232:235], v[88:91], v[16:19], v[232:235]\nds_read_b128 v[16:19], v96 offset:0\nv_mfma_f32_16x16x32_bf16 v[124:127], v[32:35], v[0:3], v[124:127]\n"
      "ds_read_b128 v[20:23], v97 offset:0\nv_mfma_f32_16x16x32_bf16 v[124:127], v[36:39], v[4:7], v[124:127]\nds_read_b128 v[24:27], v96 offset:2048\n"
      "v_mfma_f32_16x16x32_bf16 v[128:131], v[44:47], v[4:7], v[128:131]\nds_read_b128 v[28:31], v97 offset:2048\nv_mfma_f32_16x16x32_bf16 v[128:131], v[40:43], v[0:3], v[128:131]\n"
      "v_mfma_f32_16x16x32_bf16 v[132:135], v[48:51], v[0:3], v[132:135]\nv_mfma_f32_16x16x32_bf16 v[132:135], v[52:55], v[4:7], v[132:135]\n"
      "v_mfma_f32_16x16x32_bf16 v[136:139], v[60:63], v[4:7], v[136:139]\nv_mfma_f32_16x16x32_bf16 v[136:139], v[56:59], v[0:3], v[136:139]\n"
      "v_mfma_f32_16x16x32_bf16 v[152:155], v[56:59], v[8:11], v[152:155]\nds_read_b128 v[0:3], v96 offset:4096\nv_mfma_f32_16x16x32_bf16 v[152:155], v[60:63], v[12:15], v[152:155]\n"
      "ds_read_b128 v[4:7], v97 offset:4096\nv_mfma_f32_16x16x32_bf16 v[148:151], v[52:55], v[12:15], v[148:151]\nv_mfma_f32_16x16x32_bf16 v[148:151], v[48:51], v[8:11], v[148:151]\n"
      "v_mfma_f32_16x16x32_bf16 v[144:147], v[40:43], v[8:11], v[144:147]\nv_mfma_f32_16x16x32_bf16 v[144:147], v[44:47], v[12:15], v[144:147]\n"
      "v_mfma_f32_16x16x32_bf16 v[140:143], v[36:39], v[12:15], v[140:143]\ns_waitcnt lgkmcnt(4)\nv_mfma_f32_16x16x32_bf16 v[140:143], v[32:35], v[8:11], v[140:143]\n"
      "v_mfma_f32_16x16x32_bf16 v[156:159], v[32:35], v[16:19], v[156:159]\nds_read_b128 v[8:11], v96 offset:6144\nv_mfma_f32_16x16x32_bf16 v[156:159], v[36:39], v[20:23], v[156:159]\n"
      "ds_read_b128 v[12:15], v97 offset:6144\nv_mfma_f32_16x16x32_bf16 v[160:163], v[44:47], v[20:23], v[160:163]\nv_mfma_f32_16x16x32_bf16 v[160:163], v[40:43], v[16:19], v[160:163]\n"
      "v_mfma_f32_16x16x32_bf16 v[164:167], v[48:51], v[16:19], v[164:167]\nv_mfma_f32_16x16x32_bf16 v[164:167], v[52:55], v[20:23], v[164:167]\n"
      "v_mfma_f32_16x16x32_bf16 v[168:171], v[60:63], v[20:23], v[168:171]\ns_waitcnt lgkmcnt(2)\nv_mfma_f32_16x16x32_bf16 v[168:171], v[56:59], v[16:19], v[168:171]\n"
      "v_mfma_f32_16x16x32_bf16 v[184:187], v[56:59], v[24:27], v[184:187]\nds_read_b128 v[16:19], v96 offset:8192\nv_mfma_f32_16x16x32_bf16 v[184:187], v[60:63], v[28:31], v[184:187]\n"
      "ds_read_b128 v[20:23], v97 offset:8192\nv_mfma_f32_16x16x32_bf16 v[180:183], v[52:55], v[28:31], v[180:183]\nv_mfma_f32_16x16x32_bf16 v[180:183], v[48:51], v[24:27], v[180:183]\n"
      "v_mfma_f32_16x16x32_bf16 v[176:179], v[40:43], v[24:27], v[176:179]\nv_mfma_f32_16x16x32_bf16 v[176:179], v[44:47], v[28:31], v[176:179]\ns_waitcnt vmcnt(0) lgkmcnt(0)\n"
      "v_mfma_f32_16x16x32_bf16 v[172:175], v[36:39], v[28:31], v[172:175]\ns_barrier\nv_mfma_f32_16x16x32_bf16 v[172:175], v[32:35], v[24:27], v[172:175]\n"
      "ds_read_b64_tr_b16 v[64:65], v104 offset:45056\nv_mfma_f32_16x16x32_bf16 v[188:191], v[32:35], v[0:3], v[188:191]\nds_read_b64_tr_b16 v[66:67], v104 offset:46080\n"
      "v_mfma_f32_16x16x32_bf16 v[188:191], v[36:39], v[4:7], v[188:191]\nds_read_b64_tr_b16 v[68:69], v104 offset:53248\nv_mfma_f32_16x16x32_bf16 v[192:195], v[44:47], v[4:7], v[192:195]\n"
      "ds_read_b64_tr_b16 v[70:71], v104 offset:54272\nv_mfma_f32_16x16x32_bf16 v[192:195], v[40:43], v[0:3], v[192:195]\nds_read_b64_tr_b16 v[72:73], v105 offset:45056\n"
      "v_mfma_f32_16x16x32_bf16 v[196:199], v[48:51], v[0:3], v[196:199]\nds_read_b64_tr_b16 v[74:75], v105 offset:46080\nv_mfma_f32_16x16x32_bf16 v[196:199], v[52:55], v[4:7], v[196:199]\n"
      "ds_read_b64_tr_b16 v[76:77], v105 offset:53248\nv_mfma_f32_16x16x32_bf16 v[200:203], v[60:63], v[4:7], v[200:203]\nds_read_b64_tr_b16 v[78:79], v105 offset:54272\n"
      "v_mfma_f32_16x16x32_bf16 v[200:203], v[56:59], v[0:3], v[200:203]\nds_read_b64_tr_b16 v[80:81], v106 offset:45056\nv_mfma_f32_16x16x32_bf16 v[216:219], v[56:59], v[8:11], v[216:219]\n"
      "ds_read_b128 v[0:3], v100 offset:45056\nv_mfma_f32_16x16x32_bf16 v[216:219], v[60:63], v[12:15], v[216:219]\nds_read_b128 v[4:7], v101 offset:45056\n"
      "v_mfma_f32_16x16x32_bf16 v[212:215], v[52:55], v[12:15], v[212:215]\nds_read_b64_tr_b16 v[82:83], v106 offset:46080\nv_mfma_f32_16x16x32_bf16 v[212:215], v[48:51], v[8:11], v[212:215]\n"
      "ds_read_b64_tr_b16 v[84:85], v106 offset:53248\nv_mfma_f32_16x16x32_bf16 v[208:211], v[40:43], v[8:11], v[208:211]\nds_read_b64_tr_b16 v[86:87], v106 offset:54272\n"
      "v_mfma_f32_16x16x32_bf16 v[208:211], v[44:47], v[12:15], v[208:211]\nds_read_b64_tr_b16 v[88:89], v107 offset:45056\nv_mfma_f32_16x16x32_bf16 v[204:207], v[36:39], v[12:15], v[204:207]\n"
      "ds_read_b64_tr_b16 v[90:91], v107 offset:46080\nv_mfma_f32_16x16x32_bf16 v[204:207], v[32:35], v[8:11], v[204:207]\nds_read_b64_tr_b16 v[92:93], v107 offset:53248\n"
      "v_mfma_f32_16x16x32_bf16 v[220:223], v[32:35], v[16:19], v[220:223]\nds_read_b128 v[8:11], v120 offset:45056\nv_mfma_f32_16x16x32_bf16 v[220:223], v[36:39], v[20:23], v[220:223]\n"
      "ds_read_b128 v[12:15], v121 offset:45056\nv_mfma_f32_16x16x32_bf16 v[224:227], v[44:47], v[20:23], v[224:227]\nds_read_b64_tr_b16 v[94:95], v107 offset:54272\n"
      "v_mfma_f32_16x16x32_bf16 v[224:227], v[40:43], v[16:19], v[224:227]\nv_mfma_f32_16x16x32_bf16 v[228:231], v[48:51], v[16:19], v[228:231]\ns_sub_u32 s58, s58, 1\ns_cmp_lg_u32 s58, 0\n"
      "v_mfma_f32_16x16x32_bf16 v[228:231], v[52:55], v[20:23], v[228:231]\nv_mfma_f32_16x16x32_bf16 v[232:235], v[60:63], v[20:23], v[232:235]\ns_waitcnt lgkmcnt(0)\n"
      "v_mfma_f32_16x16x32_bf16 v[232:235], v[56:59], v[16:19], v[232:235]\nds_read_b128 v[16:19], v96 offset:45056\ns_cbranch_scc1 .Lgl14\n"
      "v_mfma_f32_16x16x32_bf16 v[124:127], v[64:67], v[0:3], v[124:127]\nds_read_b128 v[20:23], v97 offset:45056\nv_cmp_eq_u32 s[62:63], 0, v240\n"
      "v_mfma_f32_16x16x32_bf16 v[124:127], v[68:71], v[4:7], v[124:127]\nds_read_b128 v[24:27], v96 offset:47104\nv_mov_b32 v253, 262144\n"
      "v_mfma_f32_16x16x32_bf16 v[128:131], v[76:79], v[4:7], v[128:131]\nds_read_b128 v[28:31], v97 offset:47104\nv_mov_b32 v248, 162048\n"
      "v_mfma_f32_16x16x32_bf16 v[128:131], v[72:75], v[0:3], v[128:131]\nv_mov_b32 v241, 1\nv_cndmask_b32 v253, v253, v248, s[62:63]\n"
      "v_mfma_f32_16x16x32_bf16 v[132:135], v[80:83], v[0:3], v[132:135]\nv_lshrrev_b32 v112, 4, v240\nv_bfe_u32 v113, v240, 2, 2\n"
      "v_mfma_f32_16x16x32_bf16 v[132:135], v[84:87], v[4:7], v[132:135]\nv_and_b32 v114, 3, v240\nv_mul_u32_u24 v114, 0x80008, v114\n"
      "v_mfma_f32_16x16x32_bf16 v[136:139], v[92:95], v[4:7], v[136:139]\nv_add_u32 v244, v112, v113\nv_and_b32 v244, 3, v244\nv_mfma_f32_16x16x32_bf16 v[136:139], v[88:91], v[0:3], v[136:139]\n"
      "v_add_u32 v116, 22, v113\nv_cmp_gt_u32 s[62:63], 2, v113\nv_mfma_f32_16x16x32_bf16 v[152:155], v[88:91], v[8:11], v[152:155]\nds_read_b128 v[0:3], v96 offset:49152\n"
      "v_and_b32 v113, 60, v240\nv_mfma_f32_16x16x32_bf16 v[152:155], v[92:95], v[12:15], v[152:155]\nds_read_b128 v[4:7], v97 offset:49152\ns_mov_b32 s42, 0\n"
      "v_mfma_f32_16x16x32_bf16 v[148:151], v[84:87], v[12:15], v[148:151]\nv_cndmask_b32 v116, v116, v244, s[62:63]\nv_lshl_or_b32 v244, v244, 16, v116\n"
      "v_mfma_f32_16x16x32_bf16 v[148:151], v[80:83], v[8:11], v[148:151]\nv_lshl_add_u32 v244, v244, 5, v114\nv_lshl_add_u32 v252, v240, 4, s42\n"
      "v_mfma_f32_16x16x32_bf16 v[144:147], v[72:75], v[8:11], v[144:147]\ns_mov_b32 s42, 0x76325410\ns_mov_b32 s43, 0x21005410\n"
      "v_mfma_f32_16x16x32_bf16 v[144:147], v[76:79], v[12:15], v[144:147]\nv_lshrrev_b64 v[116:117], v113, s[42:43]\nv_and_b32 v116, 7, v116\n"
      "v_mfma_f32_16x16x32_bf16 v[140:143], v[68:71], v[12:15], v[140:143]\ns_waitcnt lgkmcnt(4)\nv_lshl_add_u32 v116, v112, 3, v116\n"
      "v_mfma_f32_16x16x32_bf16 v[140:143], v[64:67], v[8:11], v[140:143]\ns_mov_b32 s42, 0x54107632\ns_mov_b32 s43, 0x21107632\n"
      "v_mfma_f32_16x16x32_bf16 v[156:159], v[64:67], v[16:19], v[156:159]\nds_read_b128 v[8:11], v96 offset:51200\nv_lshrrev_b64 v[118:119], v113, s[42:43]\n"
      "v_mfma_f32_16x16x32_bf16 v[156:159], v[68:71], v[20:23], v[156:159]\nds_read_b128 v[12:15], v97 offset:51200\nv_and_b32 v118, 7, v118\n"
      "v_mfma_f32_16x16x32_bf16 v[160:163], v[76:79], v[20:23], v[160:163]\nv_lshl_add_u32 v118, v112, 3, v118\nv_lshl_or_b32 v245, v118, 16, v116\n"
      "v_mfma_f32_16x16x32_bf16 v[160:163], v[72:75], v[16:19], v[160:163]\nv_lshl_add_u32 v245, v245, 5, v114\nv_and_b32 v113, 1, v240\n"
      "v_mfma_f32_16x16x32_bf16 v[164:167], v[80:83], v[16:19], v[164:167]\nv_mul_u32_u24 v113, 0xaaaa, v113\nv_add_u32 v242, 0x4444, v113\n"
      "v_mfma_f32_16x16x32_bf16 v[164:167], v[84:87], v[20:23], v[164:167]\ns_lshl_b32 s42, s36, 4\ns_add_u32 s42, s42, 160640\nv_mfma_f32_16x16x32_bf16 v[168:171], v[92:95], v[20:23], v[168:171]\n"
      "s_waitcnt lgkmcnt(2)\nv_lshl_add_u32 v243, v112, 2, s42\nv_mfma_f32_16x16x32_bf16 v[168:171], v[88:91], v[16:19], v[168:171]\nv_and_b32 v112, 1, v112\nv_lshl_add_u32 v112, v112, 3, s24\n"
      "v_mfma_f32_16x16x32_bf16 v[184:187], v[88:91], v[24:27], v[184:187]\nds_read_b128 v[16:19], v96 offset:53248\ns_sub_u32 s42, s64, 1\n"
      "v_mfma_f32_16x16x32_bf16 v[184:187], v[92:95], v[28:31], v[184:187]\nds_read_b128 v[20:23], v97 offset:53248\nv_min_u32 v112, s42, v112\n"
      "v_mfma_f32_16x16x32_bf16 v[180:183], v[84:87], v[28:31], v[180:183]\nv_lshlrev_b32 v112, 3, v112\nv_and_b32 v113, 15, v240\n"
      "v_mfma_f32_16x16x32_bf16 v[180:183], v[80:83], v[24:27], v[180:183]\nv_lshl_add_u32 v250, v112, 1, v113\ns_mov_b32 s42, 136192\n"
      "v_mfma_f32_16x16x32_bf16 v[176:179], v[72:75], v[24:27], v[176:179]\nv_lshl_add_u32 v250, v250, 2, s42\nv_min_u32 v113, 12, v113\n"
      "v_mfma_f32_16x16x32_bf16 v[176:179], v[76:79], v[28:31], v[176:179]\ns_waitcnt vmcnt(0) lgkmcnt(0)\ns_lshl_b32 s42, s30, 7\n"
      "v_mfma_f32_16x16x32_bf16 v[172:175], v[68:71], v[28:31], v[172:175]\ns_barrier\nv_lshl_add_u32 v113, v113, 15, s42\nv_mfma_f32_16x16x32_bf16 v[172:175], v[64:67], v[24:27], v[172:175]\n"
      "ds_read_b64_tr_b16 v[32:33], v108 offset:0\nv_add_u32 v249, v113, v112\nv_mfma_f32_16x16x32_bf16 v[188:191], v[64:67], v[0:3], v[188:191]\nds_read_b64_tr_b16 v[34:35], v108 offset:1024\n"
      "v_mfma_f32_16x16x32_bf16 v[188:191], v[68:71], v[4:7], v[188:191]\nds_read_b64_tr_b16 v[36:37], v108 offset:8192\nv_mfma_f32_16x16x32_bf16 v[192:195], v[76:79], v[4:7], v[192:195]\n"
      "ds_read_b64_tr_b16 v[38:39], v108 offset:9216\nv_mfma_f32_16x16x32_bf16 v[192:195], v[72:75], v[0:3], v[192:195]\nds_read_b64_tr_b16 v[40:41], v109 offset:0\n"
      "v_mfma_f32_16x16x32_bf16 v[196:199], v[80:83], v[0:3], v[196:199]\nds_read_b64_tr_b16 v[42:43], v109 offset:1024\nv_mfma_f32_16x16x32_bf16 v[196:199], v[84:87], v[4:7], v[196:199]\n"
      "ds_read_b64_tr_b16 v[44:45], v109 offset:8192\nv_mfma_f32_16x16x32_bf16 v[200:203], v[92:95], v[4:7], v[200:203]\nds_read_b64_tr_b16 v[46:47], v109 offset:9216\n"
      "v_mfma_f32_16x16x32_bf16 v[200:203], v[88:91], v[0:3], v[200:203]\nds_read_b64_tr_b16 v[48:49], v110 offset:0\nv_mfma_f32_16x16x32_bf16 v[216:219], v[88:91], v[8:11], v[216:219]\n"
      "ds_read_b128 v[0:3], v102 offset:0\nv_mfma_f32_16x16x32_bf16 v[216:219], v[92:95], v[12:15], v[216:219]\nds_read_b128 v[4:7], v103 offset:0\n"
      "v_mfma_f32_16x16x32_bf16 v[212:215], v[84:87], v[12:15], v[212:215]\nds_read_b64_tr_b16 v[50:51], v110 offset:1024\nv_mfma_f32_16x16x32_bf16 v[212:215], v[80:83], v[8:11], v[212:215]\n"
      "ds_read_b64_tr_b16 v[52:53], v110 offset:8192\nv_mfma_f32_16x16x32_bf16 v[208:211], v[72:75], v[8:11], v[208:211]\nds_read_b64_tr_b16 v[54:55], v110 offset:9216\n"
      "v_mfma_f32_16x16x32_bf16 v[208:211], v[76:79], v[12:15], v[208:211]\nds_read_b64_tr_b16 v[56:57], v111 offset:0\nv_mfma_f32_16x16x32_bf16 v[204:207], v[68:71], v[12:15], v[204:207]\n"
      "ds_read_b64_tr_b16 v[58:59], v111 offset:1024\nv_mfma_f32_16x16x32_bf16 v[204:207], v[64:67], v[8:11], v[204:207]\nds_read_b64_tr_b16 v[60:61], v111 offset:8192\n"
      "v_mfma_f32_16x16x32_bf16 v[220:223], v[64:67], v[16:19], v[220:223]\nds_read_b128 v[8:11], v122 offset:0\nv_mfma_f32_16x16x32_bf16 v[220:223], v[68:71], v[20:23], v[220:223]\n"
      "ds_read_b128 v[12:15], v123 offset:0\nv_mfma_f32_16x16x32_bf16 v[224:227], v[76:79], v[20:23], v[224:227]\nds_read_b64_tr_b16 v[62:63], v111 offset:9216\n"
      "v_mfma_f32_16x16x32_bf16 v[224:227], v[72:75], v[16:19], v[224:227]\nv_mfma_f32_16x16x32_bf16 v[228:231], v[80:83], v[16:19], v[228:231]\n"
      "v_mfma_f32_16x16x32_bf16 v[228:231], v[84:87], v[20:23], v[228:231]\nv_mfma_f32_16x16x32_bf16 v[232:235], v[92:95], v[20:23], v[232:235]\ns_waitcnt lgkmcnt(0)\n"
      "v_mfma_f32_16x16x32_bf16 v[232:235], v[88:91], v[16:19], v[232:235]\nds_read_b128 v[16:19], v98 offset:0\nv_mfma_f32_16x16x32_bf16 v[124:127], v[32:35], v[0:3], v[124:127]\n"
      "ds_read_b128 v[20:23], v99 offset:0\nv_mfma_f32_16x16x32_bf16 v[124:127], v[36:39], v[4:7], v[124:127]\nds_read_b128 v[24:27], v98 offset:2048\n"
      "v_mfma_f32_16x16x32_bf16 v[128:131], v[44:47], v[4:7], v[128:131]\nds_read_b128 v[28:31], v99 offset:2048\nv_mfma_f32_16x16x32_bf16 v[128:131], v[40:43], v[0:3], v[128:131]\n"
      "v_mfma_f32_16x16x32_bf16 v[132:135], v[48:51], v[0:3], v[132:135]\nv_mfma_f32_16x16x32_bf16 v[132:135], v[52:55], v[4:7], v[132:135]\n"
      "v_mfma_f32_16x16x32_bf16 v[136:139], v[60:63], v[4:7], v[136:139]\nv_mfma_f32_16x16x32_bf16 v[136:139], v[56:59], v[0:3], v[136:139]\n"
      "v_mfma_f32_16x16x32_bf16 v[152:155], v[56:59], v[8:11], v[152:155]\nds_read_b128 v[0:3], v98 offset:4096\nv_mfma_f32_16x16x32_bf16 v[152:155], v[60:63], v[12:15], v[152:155]\n"
      "ds_read_b128 v[4:7], v99 offset:4096\nv_mfma_f32_16x16x32_bf16 v[148:151], v[52:55], v[12:15], v[148:151]\nv_mfma_f32_16x16x32_bf16 v[148:151], v[48:51], v[8:11], v[148:151]\n"
      "v_mfma_f32_16x16x32_bf16 v[144:147], v[40:43], v[8:11], v[144:147]\nv_mfma_f32_16x16x32_bf16 v[144:147], v[44:47], v[12:15], v[144:147]\n"
      "v_mfma_f32_16x16x32_bf16 v[140:143], v[36:39], v[12:15], v[140:143]\ns_waitcnt lgkmcnt(4)\nv_mfma_f32_16x16x32_bf16 v[140:143], v[32:35], v[8:11], v[140:143]\n"
      "v_mfma_f32_16x16x32_bf16 v[156:159], v[32:35], v[16:19], v[156:159]\nds_read_b128 v[8:11], v98 offset:6144\nv_mfma_f32_16x16x32_bf16 v[156:159], v[36:39], v[20:23], v[156:159]\n"
      "ds_read_b128 v[12:15], v99 offset:6144\nv_mfma_f32_16x16x32_bf16 v[160:163], v[44:47], v[20:23], v[160:163]\nv_mfma_f32_16x16x32_bf16 v[160:163], v[40:43], v[16:19], v[160:163]\n"
      "v_mfma_f32_16x16x32_bf16 v[164:167], v[48:51], v[16:19], v[164:167]\nv_mfma_f32_16x16x32_bf16 v[164:167], v[52:55], v[20:23], v[164:167]\n"
      "v_mfma_f32_16x16x32_bf16 v[168:171], v[60:63], v[20:23], v[168:171]\ns_waitcnt lgkmcnt(2)\nv_mfma_f32_16x16x32_bf16 v[168:171], v[56:59], v[16:19], v[168:171]\n"
      "v_mfma_f32_16x16x32_bf16 v[184:187], v[56:59], v[24:27], v[184:187]\nds_read_b128 v[16:19], v98 offset:8192\nv_mfma_f32_16x16x32_bf16 v[184:187], v[60:63], v[28:31], v[184:187]\n"
      "ds_read_b128 v[20:23], v99 offset:8192\nv_mfma_f32_16x16x32_bf16 v[180:183], v[52:55], v[28:31], v[180:183]\nv_mfma_f32_16x16x32_bf16 v[180:183], v[48:51], v[24:27], v[180:183]\n"
      "v_mfma_f32_16x16x32_bf16 v[176:179], v[40:43], v[24:27], v[176:179]\nv_mfma_f32_16x16x32_bf16 v[176:179], v[44:47], v[28:31], v[176:179]\ns_waitcnt vmcnt(0) lgkmcnt(0)\n"
      "v_mfma_f32_16x16x32_bf16 v[172:175], v[36:39], v[28:31], v[172:175]\ns_barrier\nv_mfma_f32_16x16x32_bf16 v[172:175], v[32:35], v[24:27], v[172:175]\nds_read_b128 v[72:75], v252 offset:0\n"
      "v_lshl_add_u32 v246, v240, 1, v248\nv_mfma_f32_16x16x32_bf16 v[188:191], v[32:35], v[0:3], v[188:191]\nds_read_b128 v[24:27], v236 offset:512\nds_read_u16 v251, v246 offset:64\n"
      "v_mfma_f32_16x16x32_bf16 v[188:191], v[36:39], v[4:7], v[188:191]\nds_read_b128 v[28:31], v236 offset:576\nds_read_b32 v247, v248 offset:24\n"
      "v_mfma_f32_16x16x32_bf16 v[192:195], v[44:47], v[4:7], v[192:195]\nds_read_b128 v[76:79], v252 offset:1024\nv_and_b32 v246, 48, v240\n"
      "v_mfma_f32_16x16x32_bf16 v[192:195], v[40:43], v[0:3], v[192:195]\nds_read_b128 v[80:83], v252 offset:2048\ns_mov_b32 s42, 42240\n"
      "v_mfma_f32_16x16x32_bf16 v[196:199], v[48:51], v[0:3], v[196:199]\nds_read_b128 v[84:87], v252 offset:3072\nv_lshl_add_u32 v246, v246, 2, s42\n"
      "v_mfma_f32_16x16x32_bf16 v[196:199], v[52:55], v[4:7], v[196:199]\nds_read_b128 v[88:91], v252 offset:4096\ns_mov_b32 s58, 0xbdd2d3e7\n"
      "v_mfma_f32_16x16x32_bf16 v[200:203], v[60:63], v[4:7], v[200:203]\nds_read_b128 v[92:95], v252 offset:5120\ns_waitcnt lgkmcnt(4)\n"
      "v_mfma_f32_16x16x32_bf16 v[200:203], v[56:59], v[0:3], v[200:203]\nds_read_b128 v[96:99], v252 offset:6144\nv_readfirstlane_b32 s39, v247\n"
      "v_mfma_f32_16x16x32_bf16 v[216:219], v[56:59], v[8:11], v[216:219]\nds_read_b128 v[0:3], v236 offset:0\nv_readlane_b32 s55, v251, 0\n"
      "v_mfma_f32_16x16x32_bf16 v[216:219], v[60:63], v[12:15], v[216:219]\nds_read_b128 v[4:7], v236 offset:64\ns_setprio 3\nv_mfma_f32_16x16x32_bf16 v[212:215], v[52:55], v[12:15], v[212:215]\n"
      "ds_read_b128 v[100:103], v252 offset:7168\ns_xor_b32 s40, s39, 0xa5c30f96\nv_mfma_f32_16x16x32_bf16 v[212:215], v[48:51], v[8:11], v[212:215]\nds_read_b128 v[104:107], v252 offset:8192\n"
      "s_mul_i32 s41, s39, 0x9E3779B1\nv_mfma_f32_16x16x32_bf16 v[208:211], v[40:43], v[8:11], v[208:211]\nds_read_b128 v[108:111], v252 offset:9216\ns_lshr_b32 s41, s41, 24\n"
      "v_mfma_f32_16x16x32_bf16 v[208:211], v[44:47], v[12:15], v[208:211]\nds_read_b128 v[64:67], v237 offset:512\nv_mfma_f32_16x16x32_bf16 v[204:207], v[36:39], v[12:15], v[204:207]\n"
      "ds_read_b128 v[68:71], v237 offset:576\nv_mfma_f32_16x16x32_bf16 v[204:207], v[32:35], v[8:11], v[204:207]\nds_read_b128 v[112:115], v252 offset:10240\n"
      "v_mfma_f32_16x16x32_bf16 v[220:223], v[32:35], v[16:19], v[220:223]\nds_read_b128 v[8:11], v237 offset:0\nv_mfma_f32_16x16x32_bf16 v[220:223], v[36:39], v[20:23], v[220:223]\n"
      "ds_read_b128 v[12:15], v237 offset:64\nv_mfma_f32_16x16x32_bf16 v[224:227], v[44:47], v[20:23], v[224:227]\nds_read_b128 v[116:119], v252 offset:11264\n"
      "v_mfma_f32_16x16x32_bf16 v[224:227], v[40:43], v[16:19], v[224:227]\nds_read_b128 v[120:123], v252 offset:12288\n"
      "v_add_u32_sdwa v239, s49, v245 dst_sel:DWORD dst_unused:UNUSED_PAD src0_sel:DWORD src1_sel:WORD_1\nv_mfma_f32_16x16x32_bf16 v[228:231], v[48:51], v[16:19], v[228:231]\n"
      "v_add_u32_sdwa v236, s49, v244 dst_sel:DWORD dst_unused:UNUSED_PAD src0_sel:DWORD src1_sel:WORD_0\nv_mfma_f32_16x16x32_bf16 v[228:231], v[52:55], v[20:23], v[228:231]\n"
      "v_add_u32_sdwa v237, s49, v244 dst_sel:DWORD dst_unused:UNUSED_PAD src0_sel:DWORD src1_sel:WORD_1\nv_mfma_f32_16x16x32_bf16 v[232:235], v[60:63], v[20:23], v[232:235]\n"
      "v_add_u32_sdwa v238, s49, v245 dst_sel:DWORD dst_unused:UNUSED_PAD src0_sel:DWORD src1_sel:WORD_0\ns_waitcnt lgkmcnt(0)\nv_mfma_f32_16x16x32_bf16 v[232:235], v[56:59], v[16:19], v[232:235]\n"
      "v_pk_mul_f32 v[44:45], v[124:125], v[124:125]\nv_pk_mul_f32 v[46:47], v[126:127], v[126:127]\nv_pk_mul_f32 v[48:49], v[128:129], v[128:129]\nv_pk_mul_f32 v[50:51], v[130:131], v[130:131]\n"
      "v_pk_fma_f32 v[44:45], v[44:45], s[58:59], s[58:59] op_sel:[0,0,1] op_sel_hi:[1,0,1]\nv_pk_fma_f32 v[46:47], v[46:47], s[58:59], s[58:59] op_sel:[0,0,1] op_sel_hi:[1,0,1]\n"
      "v_pk_fma_f32 v[48:49], v[48:49], s[58:59], s[58:59] op_sel:[0,0,1] op_sel_hi:[1,0,1]\nv_pk_fma_f32 v[50:51], v[50:51], s[58:59], s[58:59] op_sel:[0,0,1] op_sel_hi:[1,0,1]\n"
      "v_pk_fma_f32 v[44:45], v[44:45], v[124:125], s[60:61] op_sel_hi:[1,1,0]\nv_pk_fma_f32 v[46:47], v[46:47], v[126:127], s[60:61] op_sel_hi:[1,1,0]\n"
      "v_pk_fma_f32 v[48:49], v[48:49], v[128:129], s[60:61] op_sel_hi:[1,1,0]\nv_pk_fma_f32 v[50:51], v[50:51], v[130:131], s[60:61] op_sel_hi:[1,1,0]\nv_exp_f32 v44, v44 clamp\n"
      "v_pk_mul_f32 v[52:53], v[132:133], v[132:133]\nv_exp_f32 v45, v45 clamp\nv_pk_mul_f32 v[54:55], v[134:135], v[134:135]\nv_pk_mul_f32 v[56:57], v[136:137], v[136:137]\n"
      "v_exp_f32 v46, v46 clamp\nv_pk_mul_f32 v[58:59], v[138:139], v[138:139]\nv_exp_f32 v47, v47 clamp\nv_pk_fma_f32 v[52:53], v[52:53], s[58:59], s[58:59] op_sel:[0,0,1] op_sel_hi:[1,0,1]\n"
      "v_pk_fma_f32 v[54:55], v[54:55], s[58:59], s[58:59] op_sel:[0,0,1] op_sel_hi:[1,0,1]\nv_exp_f32 v48, v48 clamp\n"
      "v_pk_fma_f32 v[56:57], v[56:57], s[58:59], s[58:59] op_sel:[0,0,1] op_sel_hi:[1,0,1]\nv_exp_f32 v49, v49 clamp\n"
      "v_pk_fma_f32 v[58:59], v[58:59], s[58:59], s[58:59] op_sel:[0,0,1] op_sel_hi:[1,0,1]\nv_pk_fma_f32 v[52:53], v[52:53], v[132:133], s[60:61] op_sel_hi:[1,1,0]\nv_exp_f32 v50, v50 clamp\n"
      "v_pk_fma_f32 v[54:55], v[54:55], v[134:135], s[60:61] op_sel_hi:[1,1,0]\nv_exp_f32 v51, v51 clamp\nv_pk_fma_f32 v[56:57], v[56:57], v[136:137], s[60:61] op_sel_hi:[1,1,0]\n"
      "v_pk_fma_f32 v[58:59], v[58:59], v[138:139], s[60:61] op_sel_hi:[1,1,0]\nv_exp_f32 v52, v52 clamp\nv_pk_fma_f32 v[46:47], v[46:47], v[2:3], v[26:27]\n"
      "v_pk_fma_f32 v[44:45], v[44:45], v[0:1], v[24:25]\nv_pk_fma_f32 v[48:49], v[48:49], v[4:5], v[28:29]\nv_exp_f32 v53, v53 clamp\nv_pk_fma_f32 v[50:51], v[50:51], v[6:7], v[30:31]\n"
      "v_pk_mul_f32 v[124:125], v[124:125], v[46:47]\nv_pk_mul_f32 v[128:129], v[128:129], v[50:51]\nv_exp_f32 v54, v54 clamp\nv_pk_fma_f32 v[124:125], v[126:127], v[44:45], v[124:125]\n"
      "v_pk_fma_f32 v[128:129], v[130:131], v[48:49], v[128:129]\nv_pk_mul_f32 v[44:45], v[44:45], v[46:47]\nv_exp_f32 v55, v55 clamp\nv_pk_mul_f32 v[48:49], v[48:49], v[50:51]\n"
      "v_pk_mul_f32 v[46:47], v[44:45], v[48:49]\nv_rcp_f32 v46, v46\nv_pk_mul_f32 v[124:125], v[124:125], v[48:49]\nv_rcp_f32 v47, v47\nv_pk_fma_f32 v[124:125], v[128:129], v[44:45], v[124:125]\n"
      "v_pk_mul_f32 v[32:33], v[124:125], v[46:47]\nv_pk_mul_f32 v[16:17], v[140:141], v[140:141]\nv_pk_mul_f32 v[18:19], v[142:143], v[142:143]\nv_exp_f32 v56, v56 clamp\n"
      "v_pk_mul_f32 v[20:21], v[144:145], v[144:145]\nv_pk_mul_f32 v[22:23], v[146:147], v[146:147]\nv_exp_f32 v57, v57 clamp\n"
      "v_pk_fma_f32 v[16:17], v[16:17], s[58:59], s[58:59] op_sel:[0,0,1] op_sel_hi:[1,0,1]\nv_pk_fma_f32 v[18:19], v[18:19], s[58:59], s[58:59] op_sel:[0,0,1] op_sel_hi:[1,0,1]\n"
      "v_pk_fma_f32 v[20:21], v[20:21], s[58:59], s[58:59] op_sel:[0,0,1] op_sel_hi:[1,0,1]\nv_exp_f32 v58, v58 clamp\n"
      "v_pk_fma_f32 v[22:23], v[22:23], s[58:59], s[58:59] op_sel:[0,0,1] op_sel_hi:[1,0,1]\nv_pk_fma_f32 v[16:17], v[16:17], v[140:141], s[60:61] op_sel_hi:[1,1,0]\n"
      "v_pk_fma_f32 v[18:19], v[18:19], v[142:143], s[60:61] op_sel_hi:[1,1,0]\nv_exp_f32 v59, v59 clamp\nv_pk_fma_f32 v[20:21], v[20:21], v[144:145], s[60:61] op_sel_hi:[1,1,0]\n"
      "v_pk_fma_f32 v[22:23], v[22:23], v[146:147], s[60:61] op_sel_hi:[1,1,0]\nv_exp_f32 v16, v16 clamp\nv_pk_fma_f32 v[54:55], v[54:55], v[10:11], v[66:67]\n"
      "v_pk_fma_f32 v[52:53], v[52:53], v[8:9], v[64:65]\nv_pk_fma_f32 v[56:57], v[56:57], v[12:13], v[68:69]\nv_exp_f32 v17, v17 clamp\nv_pk_fma_f32 v[58:59], v[58:59], v[14:15], v[70:71]\n"
      "v_pk_mul_f32 v[132:133], v[132:133], v[54:55]\nv_pk_mul_f32 v[136:137], v[136:137], v[58:59]\nv_exp_f32 v18, v18 clamp\nv_pk_fma_f32 v[132:133], v[134:135], v[52:53], v[132:133]\n"
      "v_pk_fma_f32 v[136:137], v[138:139], v[56:57], v[136:137]\nv_pk_mul_f32 v[52:53], v[52:53], v[54:55]\nv_exp_f32 v19, v19 clamp\nv_pk_mul_f32 v[56:57], v[56:57], v[58:59]\n"
      "v_pk_mul_f32 v[54:55], v[52:53], v[56:57]\nv_rcp_f32 v54, v54\nv_pk_mul_f32 v[132:133], v[132:133], v[56:57]\nv_rcp_f32 v55, v55\nv_pk_fma_f32 v[132:133], v[136:137], v[52:53], v[132:133]\n"
      "v_pk_fma_f32 v[32:33], v[132:133], v[54:55], v[32:33]\nv_add_f32 v42, v32, v33\nv_pk_mul_f32 v[44:45], v[148:149], v[148:149]\nv_pk_mul_f32 v[46:47], v[150:151], v[150:151]\n"
      "v_exp_f32 v20, v20 clamp\nv_pk_mul_f32 v[48:49], v[152:153], v[152:153]\nv_pk_mul_f32 v[50:51], v[154:155], v[154:155]\nv_exp_f32 v21, v21 clamp\n"
      "v_pk_fma_f32 v[44:45], v[44:45], s[58:59], s[58:59] op_sel:[0,0,1] op_sel_hi:[1,0,1]\nv_pk_fma_f32 v[46:47], v[46:47], s[58:59], s[58:59] op_sel:[0,0,1] op_sel_hi:[1,0,1]\n"
      "v_pk_fma_f32 v[48:49], v[48:49], s[58:59], s[58:59] op_sel:[0,0,1] op_sel_hi:[1,0,1]\nv_exp_f32 v22, v22 clamp\n"
      "v_pk_fma_f32 v[50:51], v[50:51], s[58:59], s[58:59] op_sel:[0,0,1] op_sel_hi:[1,0,1]\nv_pk_fma_f32 v[44:45], v[44:45], v[148:149], s[60:61] op_sel_hi:[1,1,0]\n"
      "v_pk_fma_f32 v[46:47], v[46:47], v[150:151], s[60:61] op_sel_hi:[1,1,0]\nv_exp_f32 v23, v23 clamp\nv_pk_fma_f32 v[48:49], v[48:49], v[152:153], s[60:61] op_sel_hi:[1,1,0]\n"
      "v_pk_fma_f32 v[50:51], v[50:51], v[154:155], s[60:61] op_sel_hi:[1,1,0]\nv_exp_f32 v44, v44 clamp\nv_pk_fma_f32 v[18:19], v[18:19], v[2:3], v[26:27]\n"
      "v_pk_fma_f32 v[16:17], v[16:17], v[0:1], v[24:25]\nv_pk_fma_f32 v[20:21], v[20:21], v[4:5], v[28:29]\nv_exp_f32 v45, v45 clamp\nv_pk_fma_f32 v[22:23], v[22:23], v[6:7], v[30:31]\n"
      "v_pk_mul_f32 v[140:141], v[140:141], v[18:19]\nv_pk_mul_f32 v[144:145], v[144:145], v[22:23]\nv_exp_f32 v46, v46 clamp\nv_pk_fma_f32 v[140:141], v[142:143], v[16:17], v[140:141]\n"
      "v_pk_fma_f32 v[144:145], v[146:147], v[20:21], v[144:145]\nv_pk_mul_f32 v[16:17], v[16:17], v[18:19]\nv_exp_f32 v47, v47 clamp\nv_pk_mul_f32 v[20:21], v[20:21], v[22:23]\n"
      "v_pk_mul_f32 v[18:19], v[16:17], v[20:21]\nv_rcp_f32 v18, v18\nv_pk_mul_f32 v[140:141], v[140:141], v[20:21]\nv_rcp_f32 v19, v19\nv_pk_fma_f32 v[140:141], v[144:145], v[16:17], v[140:141]\n"
      "v_pk_mul_f32 v[32:33], v[140:141], v[18:19]\nv_pk_mul_f32 v[52:53], v[156:157], v[156:157]\nv_pk_mul_f32 v[54:55], v[158:159], v[158:159]\nv_exp_f32 v48, v48 clamp\n"
      "v_pk_mul_f32 v[56:57], v[160:161], v[160:161]\nv_pk_mul_f32 v[58:59], v[162:163], v[162:163]\nv_exp_f32 v49, v49 clamp\n"
      "v_pk_fma_f32 v[52:53], v[52:53], s[58:59], s[58:59] op_sel:[0,0,1] op_sel_hi:[1,0,1]\nv_pk_fma_f32 v[54:55], v[54:55], s[58:59], s[58:59] op_sel:[0,0,1] op_sel_hi:[1,0,1]\n"
      "v_pk_fma_f32 v[56:57], v[56:57], s[58:59], s[58:59] op_sel:[0,0,1] op_sel_hi:[1,0,1]\nv_exp_f32 v50, v50 clamp\n"
      "v_pk_fma_f32 v[58:59], v[58:59], s[58:59], s[58:59] op_sel:[0,0,1] op_sel_hi:[1,0,1]\nv_pk_fma_f32 v[52:53], v[52:53], v[156:157], s[60:61] op_sel_hi:[1,1,0]\n"
      "v_pk_fma_f32 v[54:55], v[54:55], v[158:159], s[60:61] op_sel_hi:[1,1,0]\nv_exp_f32 v51, v51 clamp\nv_pk_fma_f32 v[56:57], v[56:57], v[160:161], s[60:61] op_sel_hi:[1,1,0]\n"
      "v_pk_fma_f32 v[58:59], v[58:59], v[162:163], s[60:61] op_sel_hi:[1,1,0]\nv_exp_f32 v52, v52 clamp\nv_pk_fma_f32 v[46:47], v[46:47], v[10:11], v[66:67]\n"
      "v_pk_fma_f32 v[44:45], v[44:45], v[8:9], v[64:65]\nv_pk_fma_f32 v[48:49], v[48:49], v[12:13], v[68:69]\nv_exp_f32 v53, v53 clamp\nv_pk_fma_f32 v[50:51], v[50:51], v[14:15], v[70:71]\n"
      "v_pk_mul_f32 v[148:149], v[148:149], v[46:47]\nv_pk_mul_f32 v[152:153], v[152:153], v[50:51]\nv_exp_f32 v54, v54 clamp\nv_pk_fma_f32 v[148:149], v[150:151], v[44:45], v[148:149]\n"
      "v_pk_fma_f32 v[152:153], v[154:155], v[48:49], v[152:153]\nv_pk_mul_f32 v[44:45], v[44:45], v[46:47]\nv_exp_f32 v55, v55 clamp\nv_pk_mul_f32 v[48:49], v[48:49], v[50:51]\n"
      "v_pk_mul_f32 v[46:47], v[44:45], v[48:49]\nv_rcp_f32 v46, v46\nv_pk_mul_f32 v[148:149], v[148:149], v[48:49]\nv_rcp_f32 v47, v47\nv_pk_fma_f32 v[148:149], v[152:153], v[44:45], v[148:149]\n"
      "v_pk_fma_f32 v[32:33], v[148:149], v[46:47], v[32:33]\nv_add_f32 v41, v32, v33\nv_pk_mul_f32 v[16:17], v[164:165], v[164:165]\nv_pk_mul_f32 v[18:19], v[166:167], v[166:167]\n"
      "v_exp_f32 v56, v56 clamp\nv_pk_mul_f32 v[20:21], v[168:169], v[168:169]\nv_pk_mul_f32 v[22:23], v[170:171], v[170:171]\nv_exp_f32 v57, v57 clamp\n"
      "v_pk_fma_f32 v[16:17], v[16:17], s[58:59], s[58:59] op_sel:[0,0,1] op_sel_hi:[1,0,1]\nv_pk_fma_f32 v[18:19], v[18:19], s[58:59], s[58:59] op_sel:[0,0,1] op_sel_hi:[1,0,1]\n"
      "v_pk_fma_f32 v[20:21], v[20:21], s[58:59], s[58:59] op_sel:[0,0,1] op_sel_hi:[1,0,1]\nv_exp_f32 v58, v58 clamp\n"
      "v_pk_fma_f32 v[22:23], v[22:23], s[58:59], s[58:59] op_sel:[0,0,1] op_sel_hi:[1,0,1]\nv_pk_fma_f32 v[16:17], v[16:17], v[164:165], s[60:61] op_sel_hi:[1,1,0]\n"
      "v_pk_fma_f32 v[18:19], v[18:19], v[166:167], s[60:61] op_sel_hi:[1,1,0]\nv_exp_f32 v59, v59 clamp\nv_pk_fma_f32 v[20:21], v[20:21], v[168:169], s[60:61] op_sel_hi:[1,1,0]\n"
      "v_pk_fma_f32 v[22:23], v[22:23], v[170:171], s[60:61] op_sel_hi:[1,1,0]\nv_exp_f32 v16, v16 clamp\nv_pk_fma_f32 v[54:55], v[54:55], v[2:3], v[26:27]\n"
      "v_pk_fma_f32 v[52:53], v[52:53], v[0:1], v[24:25]\nv_pk_fma_f32 v[56:57], v[56:57], v[4:5], v[28:29]\nv_exp_f32 v17, v17 clamp\nv_pk_fma_f32 v[58:59], v[58:59], v[6:7], v[30:31]\n"
      "v_pk_mul_f32 v[156:157], v[156:157], v[54:55]\nv_pk_mul_f32 v[160:161], v[160:161], v[58:59]\nv_exp_f32 v18, v18 clamp\nv_pk_fma_f32 v[156:157], v[158:159], v[52:53], v[156:157]\n"
      "v_pk_fma_f32 v[160:161], v[162:163], v[56:57], v[160:161]\nv_pk_mul_f32 v[52:53], v[52:53], v[54:55]\nv_exp_f32 v19, v19 clamp\nv_pk_mul_f32 v[56:57], v[56:57], v[58:59]\n"
      "v_pk_mul_f32 v[54:55], v[52:53], v[56:57]\nv_rcp_f32 v54, v54\nv_pk_mul_f32 v[156:157], v[156:157], v[56:57]\nv_rcp_f32 v55, v55\nv_pk_fma_f32 v[156:157], v[160:161], v[52:53], v[156:157]\n"
      "v_pk_mul_f32 v[32:33], v[156:157], v[54:55]\nv_pk_mul_f32 v[44:45], v[172:173], v[172:173]\nv_pk_mul_f32 v[46:47], v[174:175], v[174:175]\nv_exp_f32 v20, v20 clamp\n"
      "v_pk_mul_f32 v[48:49], v[176:177], v[176:177]\nv_pk_mul_f32 v[50:51], v[178:179], v[178:179]\nv_exp_f32 v21, v21 clamp\n"
      "v_pk_fma_f32 v[44:45], v[44:45], s[58:59], s[58:59] op_sel:[0,0,1] op_sel_hi:[1,0,1]\nv_pk_fma_f32 v[46:47], v[46:47], s[58:59], s[58:59] op_sel:[0,0,1] op_sel_hi:[1,0,1]\n"
      "v_pk_fma_f32 v[48:49], v[48:49], s[58:59], s[58:59] op_sel:[0,0,1] op_sel_hi:[1,0,1]\nv_exp_f32 v22, v22 clamp\n"
      "v_pk_fma_f32 v[50:51], v[50:51], s[58:59], s[58:59] op_sel:[0,0,1] op_sel_hi:[1,0,1]\nv_pk_fma_f32 v[44:45], v[44:45], v[172:173], s[60:61] op_sel_hi:[1,1,0]\n"
      "v_pk_fma_f32 v[46:47], v[46:47], v[174:175], s[60:61] op_sel_hi:[1,1,0]\nv_exp_f32 v23, v23 clamp\nv_pk_fma_f32 v[48:49], v[48:49], v[176:177], s[60:61] op_sel_hi:[1,1,0]\n"
      "v_pk_fma_f32 v[50:51], v[50:51], v[178:179], s[60:61] op_sel_hi:[1,1,0]\nv_exp_f32 v44, v44 clamp\nv_pk_fma_f32 v[18:19], v[18:19], v[10:11], v[66:67]\n"
      "v_pk_fma_f32 v[16:17], v[16:17], v[8:9], v[64:65]\nv_pk_fma_f32 v[20:21], v[20:21], v[12:13], v[68:69]\nv_exp_f32 v45, v45 clamp\nv_pk_fma_f32 v[22:23], v[22:23], v[14:15], v[70:71]\n"
      "v_pk_mul_f32 v[164:165], v[164:165], v[18:19]\nv_pk_mul_f32 v[168:169], v[168:169], v[22:23]\nv_exp_f32 v46, v46 clamp\nv_pk_fma_f32 v[164:165], v[166:167], v[16:17], v[164:165]\n"
      "v_pk_fma_f32 v[168:169], v[170:171], v[20:21], v[168:169]\nv_pk_mul_f32 v[16:17], v[16:17], v[18:19]\nv_exp_f32 v47, v47 clamp\nv_pk_mul_f32 v[20:21], v[20:21], v[22:23]\n"
      "v_pk_mul_f32 v[18:19], v[16:17], v[20:21]\nv_rcp_f32 v18, v18\nv_pk_mul_f32 v[164:165], v[164:165], v[20:21]\nv_rcp_f32 v19, v19\nv_pk_fma_f32 v[164:165], v[168:169], v[16:17], v[164:165]\n"
      "v_pk_fma_f32 v[32:33], v[164:165], v[18:19], v[32:33]\nv_add_f32 v36, v32, v33\nv_pk_mul_f32 v[52:53], v[180:181], v[180:181]\nv_pk_mul_f32 v[54:55], v[182:183], v[182:183]\n"
      "v_exp_f32 v48, v48 clamp\nv_pk_mul_f32 v[56:57], v[184:185], v[184:185]\nv_pk_mul_f32 v[58:59], v[186:187], v[186:187]\nv_exp_f32 v49, v49 clamp\n"
      "v_pk_fma_f32 v[52:53], v[52:53], s[58:59], s[58:59] op_sel:[0,0,1] op_sel_hi:[1,0,1]\nv_pk_fma_f32 v[54:55], v[54:55], s[58:59], s[58:59] op_sel:[0,0,1] op_sel_hi:[1,0,1]\n"
      "v_pk_fma_f32 v[56:57], v[56:57], s[58:59], s[58:59] op_sel:[0,0,1] op_sel_hi:[1,0,1]\nv_exp_f32 v50, v50 clamp\n"
      "v_pk_fma_f32 v[58:59], v[58:59], s[58:59], s[58:59] op_sel:[0,0,1] op_sel_hi:[1,0,1]\nv_pk_fma_f32 v[52:53], v[52:53], v[180:181], s[60:61] op_sel_hi:[1,1,0]\n"
      "v_pk_fma_f32 v[54:55], v[54:55], v[182:183], s[60:61] op_sel_hi:[1,1,0]\nv_exp_f32 v51, v51 clamp\nv_pk_fma_f32 v[56:57], v[56:57], v[184:185], s[60:61] op_sel_hi:[1,1,0]\n"
      "v_pk_fma_f32 v[58:59], v[58:59], v[186:187], s[60:61] op_sel_hi:[1,1,0]\nv_exp_f32 v52, v52 clamp\nv_pk_fma_f32 v[46:47], v[46:47], v[2:3], v[26:27]\n"
      "v_pk_fma_f32 v[44:45], v[44:45], v[0:1], v[24:25]\nv_pk_fma_f32 v[48:49], v[48:49], v[4:5], v[28:29]\nv_exp_f32 v53, v53 clamp\nv_pk_fma_f32 v[50:51], v[50:51], v[6:7], v[30:31]\n"
      "v_pk_mul_f32 v[172:173], v[172:173], v[46:47]\nv_pk_mul_f32 v[176:177], v[176:177], v[50:51]\nv_exp_f32 v54, v54 clamp\nv_pk_fma_f32 v[172:173], v[174:175], v[44:45], v[172:173]\n"
      "v_pk_fma_f32 v[176:177], v[178:179], v[48:49], v[176:177]\nv_pk_mul_f32 v[44:45], v[44:45], v[46:47]\nv_exp_f32 v55, v55 clamp\nv_pk_mul_f32 v[48:49], v[48:49], v[50:51]\n"
      "v_pk_mul_f32 v[46:47], v[44:45], v[48:49]\nv_rcp_f32 v46, v46\nv_pk_mul_f32 v[172:173], v[172:173], v[48:49]\nv_rcp_f32 v47, v47\nv_pk_fma_f32 v[172:173], v[176:177], v[44:45], v[172:173]\n"
      "v_pk_mul_f32 v[32:33], v[172:173], v[46:47]\nv_pk_mul_f32 v[16:17], v[188:189], v[188:189]\nv_pk_mul_f32 v[18:19], v[190:191], v[190:191]\nv_exp_f32 v56, v56 clamp\n"
      "v_pk_mul_f32 v[20:21], v[192:193], v[192:193]\nv_pk_mul_f32 v[22:23], v[194:195], v[194:195]\nv_exp_f32 v57, v57 clamp\n"
      "v_pk_fma_f32 v[16:17], v[16:17], s[58:59], s[58:59] op_sel:[0,0,1] op_sel_hi:[1,0,1]\nv_pk_fma_f32 v[18:19], v[18:19], s[58:59], s[58:59] op_sel:[0,0,1] op_sel_hi:[1,0,1]\n"
      "v_pk_fma_f32 v[20:21], v[20:21], s[58:59], s[58:59] op_sel:[0,0,1] op_sel_hi:[1,0,1]\nv_exp_f32 v58, v58 clamp\n"
      "v_pk_fma_f32 v[22:23], v[22:23], s[58:59], s[58:59] op_sel:[0,0,1] op_sel_hi:[1,0,1]\nv_pk_fma_f32 v[16:17], v[16:17], v[188:189], s[60:61] op_sel_hi:[1,1,0]\n"
      "v_pk_fma_f32 v[18:19], v[18:19], v[190:191], s[60:61] op_sel_hi:[1,1,0]\nv_exp_f32 v59, v59 clamp\nv_pk_fma_f32 v[20:21], v[20:21], v[192:193], s[60:61] op_sel_hi:[1,1,0]\n"
      "v_pk_fma_f32 v[22:23], v[22:23], v[194:195], s[60:61] op_sel_hi:[1,1,0]\nv_exp_f32 v16, v16 clamp\nv_pk_fma_f32 v[54:55], v[54:55], v[10:11], v[66:67]\n"
      "v_pk_fma_f32 v[52:53], v[52:53], v[8:9], v[64:65]\nv_pk_fma_f32 v[56:57], v[56:57], v[12:13], v[68:69]\nv_exp_f32 v17, v17 clamp\nv_pk_fma_f32 v[58:59], v[58:59], v[14:15], v[70:71]\n"
      "v_pk_mul_f32 v[180:181], v[180:181], v[54:55]\nv_pk_mul_f32 v[184:185], v[184:185], v[58:59]\nv_exp_f32 v18, v18 clamp\nv_pk_fma_f32 v[180:181], v[182:183], v[52:53], v[180:181]\n"
      "v_pk_fma_f32 v[184:185], v[186:187], v[56:57], v[184:185]\nv_pk_mul_f32 v[52:53], v[52:53], v[54:55]\nv_exp_f32 v19, v19 clamp\nv_pk_mul_f32 v[56:57], v[56:57], v[58:59]\n"
      "v_pk_mul_f32 v[54:55], v[52:53], v[56:57]\nv_rcp_f32 v54, v54\nv_pk_mul_f32 v[180:181], v[180:181], v[56:57]\nv_rcp_f32 v55, v55\nv_pk_fma_f32 v[180:181], v[184:185], v[52:53], v[180:181]\n"
      "v_pk_fma_f32 v[32:33], v[180:181], v[54:55], v[32:33]\nv_add_f32 v37, v32, v33\nv_pk_mul_f32 v[44:45], v[196:197], v[196:197]\nv_pk_mul_f32 v[46:47], v[198:199], v[198:199]\n"
      "v_exp_f32 v20, v20 clamp\nv_pk_mul_f32 v[48:49], v[200:201], v[200:201]\nv_pk_mul_f32 v[50:51], v[202:203], v[202:203]\nv_exp_f32 v21, v21 clamp\n"
      "v_pk_fma_f32 v[44:45], v[44:45], s[58:59], s[58:59] op_sel:[0,0,1] op_sel_hi:[1,0,1]\nv_pk_fma_f32 v[46:47], v[46:47], s[58:59], s[58:59] op_sel:[0,0,1] op_sel_hi:[1,0,1]\n"
      "v_pk_fma_f32 v[48:49], v[48:49], s[58:59], s[58:59] op_sel:[0,0,1] op_sel_hi:[1,0,1]\nv_exp_f32 v22, v22 clamp\n"
      "v_pk_fma_f32 v[50:51], v[50:51], s[58:59], s[58:59] op_sel:[0,0,1] op_sel_hi:[1,0,1]\nv_pk_fma_f32 v[44:45], v[44:45], v[196:197], s[60:61] op_sel_hi:[1,1,0]\n"
      "v_pk_fma_f32 v[46:47], v[46:47], v[198:199], s[60:61] op_sel_hi:[1,1,0]\nv_exp_f32 v23, v23 clamp\nv_pk_fma_f32 v[48:49], v[48:49], v[200:201], s[60:61] op_sel_hi:[1,1,0]\n"
      "v_pk_fma_f32 v[50:51], v[50:51], v[202:203], s[60:61] op_sel_hi:[1,1,0]\nv_exp_f32 v44, v44 clamp\nv_pk_fma_f32 v[18:19], v[18:19], v[2:3], v[26:27]\n"
      "v_pk_fma_f32 v[16:17], v[16:17], v[0:1], v[24:25]\nv_pk_fma_f32 v[20:21], v[20:21], v[4:5], v[28:29]\nv_exp_f32 v45, v45 clamp\nv_pk_fma_f32 v[22:23], v[22:23], v[6:7], v[30:31]\n"
      "v_pk_mul_f32 v[188:189], v[188:189], v[18:19]\nv_pk_mul_f32 v[192:193], v[192:193], v[22:23]\nv_exp_f32 v46, v46 clamp\nv_pk_fma_f32 v[188:189], v[190:191], v[16:17], v[188:189]\n"
      "v_pk_fma_f32 v[192:193], v[194:195], v[20:21], v[192:193]\nv_pk_mul_f32 v[16:17], v[16:17], v[18:19]\nv_exp_f32 v47, v47 clamp\nv_pk_mul_f32 v[20:21], v[20:21], v[22:23]\n"
      "v_pk_mul_f32 v[18:19], v[16:17], v[20:21]\nv_rcp_f32 v18, v18\nv_pk_mul_f32 v[188:189], v[188:189], v[20:21]\nv_rcp_f32 v19, v19\nv_pk_fma_f32 v[188:189], v[192:193], v[16:17], v[188:189]\n"
      "v_pk_mul_f32 v[32:33], v[188:189], v[18:19]\nv_pk_mul_f32 v[52:53], v[204:205], v[204:205]\nv_pk_mul_f32 v[54:55], v[206:207], v[206:207]\nv_exp_f32 v48, v48 clamp\n"
      "v_pk_mul_f32 v[56:57], v[208:209], v[208:209]\nv_pk_mul_f32 v[58:59], v[210:211], v[210:211]\nv_exp_f32 v49, v49 clamp\n"
      "v_pk_fma_f32 v[52:53], v[52:53], s[58:59], s[58:59] op_sel:[0,0,1] op_sel_hi:[1,0,1]\nv_pk_fma_f32 v[54:55], v[54:55], s[58:59], s[58:59] op_sel:[0,0,1] op_sel_hi:[1,0,1]\n"
      "v_pk_fma_f32 v[56:57], v[56:57], s[58:59], s[58:59] op_sel:[0,0,1] op_sel_hi:[1,0,1]\nv_exp_f32 v50, v50 clamp\n"
      "v_pk_fma_f32 v[58:59], v[58:59], s[58:59], s[58:59] op_sel:[0,0,1] op_sel_hi:[1,0,1]\nv_pk_fma_f32 v[52:53], v[52:53], v[204:205], s[60:61] op_sel_hi:[1,1,0]\n"
      "v_pk_fma_f32 v[54:55], v[54:55], v[206:207], s[60:61] op_sel_hi:[1,1,0]\nv_exp_f32 v51, v51 clamp\nv_pk_fma_f32 v[56:57], v[56:57], v[208:209], s[60:61] op_sel_hi:[1,1,0]\n"
      "v_pk_fma_f32 v[58:59], v[58:59], v[210:211], s[60:61] op_sel_hi:[1,1,0]\nv_exp_f32 v52, v52 clamp\nv_pk_fma_f32 v[46:47], v[46:47], v[10:11], v[66:67]\n"
      "v_pk_fma_f32 v[44:45], v[44:45], v[8:9], v[64:65]\nv_pk_fma_f32 v[48:49], v[48:49], v[12:13], v[68:69]\nv_exp_f32 v53, v53 clamp\nv_pk_fma_f32 v[50:51], v[50:51], v[14:15], v[70:71]\n"
      "v_pk_mul_f32 v[196:197], v[196:197], v[46:47]\nv_pk_mul_f32 v[200:201], v[200:201], v[50:51]\nv_exp_f32 v54, v54 clamp\nv_pk_fma_f32 v[196:197], v[198:199], v[44:45], v[196:197]\n"
      "v_pk_fma_f32 v[200:201], v[202:203], v[48:49], v[200:201]\nv_pk_mul_f32 v[44:45], v[44:45], v[46:47]\nv_exp_f32 v55, v55 clamp\nv_pk_mul_f32 v[48:49], v[48:49], v[50:51]\n"
      "v_pk_mul_f32 v[46:47], v[44:45], v[48:49]\nv_rcp_f32 v46, v46\nv_pk_mul_f32 v[196:197], v[196:197], v[48:49]\nv_rcp_f32 v47, v47\nv_pk_fma_f32 v[196:197], v[200:201], v[44:45], v[196:197]\n"
      "v_pk_fma_f32 v[32:33], v[196:197], v[46:47], v[32:33]\nv_add_f32 v38, v32, v33\nv_pk_mul_f32 v[16:17], v[212:213], v[212:213]\nv_pk_mul_f32 v[18:19], v[214:215], v[214:215]\n"
      "v_exp_f32 v56, v56 clamp\nv_pk_mul_f32 v[20:21], v[216:217], v[216:217]\nv_pk_mul_f32 v[22:23], v[218:219], v[218:219]\nv_exp_f32 v57, v57 clamp\n"
      "v_pk_fma_f32 v[16:17], v[16:17], s[58:59], s[58:59] op_sel:[0,0,1] op_sel_hi:[1,0,1]\nv_pk_fma_f32 v[18:19], v[18:19], s[58:59], s[58:59] op_sel:[0,0,1] op_sel_hi:[1,0,1]\n"
      "v_pk_fma_f32 v[20:21], v[20:21], s[58:59], s[58:59] op_sel:[0,0,1] op_sel_hi:[1,0,1]\nv_exp_f32 v58, v58 clamp\n"
      "v_pk_fma_f32 v[22:23], v[22:23], s[58:59], s[58:59] op_sel:[0,0,1] op_sel_hi:[1,0,1]\nv_pk_fma_f32 v[16:17], v[16:17], v[212:213], s[60:61] op_sel_hi:[1,1,0]\n"
      "v_pk_fma_f32 v[18:19], v[18:19], v[214:215], s[60:61] op_sel_hi:[1,1,0]\nv_exp_f32 v59, v59 clamp\nv_pk_fma_f32 v[20:21], v[20:21], v[216:217], s[60:61] op_sel_hi:[1,1,0]\n"
      "v_pk_fma_f32 v[22:23], v[22:23], v[218:219], s[60:61] op_sel_hi:[1,1,0]\nv_exp_f32 v16, v16 clamp\nv_pk_fma_f32 v[54:55], v[54:55], v[2:3], v[26:27]\n"
      "v_pk_fma_f32 v[52:53], v[52:53], v[0:1], v[24:25]\nv_pk_fma_f32 v[56:57], v[56:57], v[4:5], v[28:29]\nv_exp_f32 v17, v17 clamp\nv_pk_fma_f32 v[58:59], v[58:59], v[6:7], v[30:31]\n"
      "v_pk_mul_f32 v[204:205], v[204:205], v[54:55]\nv_pk_mul_f32 v[208:209], v[208:209], v[58:59]\nv_exp_f32 v18, v18 clamp\nv_pk_fma_f32 v[204:205], v[206:207], v[52:53], v[204:205]\n"
      "v_pk_fma_f32 v[208:209], v[210:211], v[56:57], v[208:209]\nv_pk_mul_f32 v[52:53], v[52:53], v[54:55]\nv_exp_f32 v19, v19 clamp\nv_pk_mul_f32 v[56:57], v[56:57], v[58:59]\n"
      "v_pk_mul_f32 v[54:55], v[52:53], v[56:57]\nv_rcp_f32 v54, v54\nv_pk_mul_f32 v[204:205], v[204:205], v[56:57]\nv_rcp_f32 v55, v55\nv_pk_fma_f32 v[204:205], v[208:209], v[52:53], v[204:205]\n"
      "v_pk_mul_f32 v[32:33], v[204:205], v[54:55]\nv_pk_mul_f32 v[44:45], v[220:221], v[220:221]\nv_pk_mul_f32 v[46:47], v[222:223], v[222:223]\nv_exp_f32 v20, v20 clamp\n"
      "v_pk_mul_f32 v[48:49], v[224:225], v[224:225]\nv_pk_mul_f32 v[50:51], v[226:227], v[226:227]\nv_exp_f32 v21, v21 clamp\n"
      "v_pk_fma_f32 v[44:45], v[44:45], s[58:59], s[58:59] op_sel:[0,0,1] op_sel_hi:[1,0,1]\nv_pk_fma_f32 v[46:47], v[46:47], s[58:59], s[58:59] op_sel:[0,0,1] op_sel_hi:[1,0,1]\n"
      "v_pk_fma_f32 v[48:49], v[48:49], s[58:59], s[58:59] op_sel:[0,0,1] op_sel_hi:[1,0,1]\nv_exp_f32 v22, v22 clamp\n"
      "v_pk_fma_f32 v[50:51], v[50:51], s[58:59], s[58:59] op_sel:[0,0,1] op_sel_hi:[1,0,1]\nv_pk_fma_f32 v[44:45], v[44:45], v[220:221], s[60:61] op_sel_hi:[1,1,0]\n"
      "v_pk_fma_f32 v[46:47], v[46:47], v[222:223], s[60:61] op_sel_hi:[1,1,0]\nv_exp_f32 v23, v23 clamp\nv_pk_fma_f32 v[48:49], v[48:49], v[224:225], s[60:61] op_sel_hi:[1,1,0]\n"
      "v_pk_fma_f32 v[50:51], v[50:51], v[226:227], s[60:61] op_sel_hi:[1,1,0]\nv_exp_f32 v44, v44 clamp\nv_pk_fma_f32 v[18:19], v[18:19], v[10:11], v[66:67]\n"
      "v_pk_fma_f32 v[16:17], v[16:17], v[8:9], v[64:65]\nv_pk_fma_f32 v[20:21], v[20:21], v[12:13], v[68:69]\nv_exp_f32 v45, v45 clamp\nv_pk_fma_f32 v[22:23], v[22:23], v[14:15], v[70:71]\n"
      "v_pk_mul_f32 v[212:213], v[212:213], v[18:19]\nv_pk_mul_f32 v[216:217], v[216:217], v[22:23]\nv_exp_f32 v46, v46 clamp\nv_pk_fma_f32 v[212:213], v[214:215], v[16:17], v[212:213]\n"
      "v_pk_fma_f32 v[216:217], v[218:219], v[20:21], v[216:217]\nv_pk_mul_f32 v[16:17], v[16:17], v[18:19]\nv_exp_f32 v47, v47 clamp\nv_pk_mul_f32 v[20:21], v[20:21], v[22:23]\n"
      "v_pk_mul_f32 v[18:19], v[16:17], v[20:21]\nv_rcp_f32 v18, v18\nv_pk_mul_f32 v[212:213], v[212:213], v[20:21]\nv_rcp_f32 v19, v19\nv_pk_fma_f32 v[212:213], v[216:217], v[16:17], v[212:213]\n"
      "v_pk_fma_f32 v[32:33], v[212:213], v[18:19], v[32:33]\nv_add_f32 v39, v32, v33\nv_pk_mul_f32 v[52:53], v[228:229], v[228:229]\nv_pk_mul_f32 v[54:55], v[230:231], v[230:231]\n"
      "v_exp_f32 v48, v48 clamp\nv_pk_mul_f32 v[56:57], v[232:233], v[232:233]\nv_pk_mul_f32 v[58:59], v[234:235], v[234:235]\nv_exp_f32 v49, v49 clamp\n"
      "v_pk_fma_f32 v[52:53], v[52:53], s[58:59], s[58:59] op_sel:[0,0,1] op_sel_hi:[1,0,1]\nv_pk_fma_f32 v[54:55], v[54:55], s[58:59], s[58:59] op_sel:[0,0,1] op_sel_hi:[1,0,1]\n"
      "v_pk_fma_f32 v[56:57], v[56:57], s[58:59], s[58:59] op_sel:[0,0,1] op_sel_hi:[1,0,1]\nv_exp_f32 v50, v50 clamp\n"
      "v_pk_fma_f32 v[58:59], v[58:59], s[58:59], s[58:59] op_sel:[0,0,1] op_sel_hi:[1,0,1]\nv_pk_fma_f32 v[52:53], v[52:53], v[228:229], s[60:61] op_sel_hi:[1,1,0]\n"
      "v_pk_fma_f32 v[54:55], v[54:55], v[230:231], s[60:61] op_sel_hi:[1,1,0]\nv_exp_f32 v51, v51 clamp\nv_pk_fma_f32 v[56:57], v[56:57], v[232:233], s[60:61] op_sel_hi:[1,1,0]\n"
      "v_pk_fma_f32 v[58:59], v[58:59], v[234:235], s[60:61] op_sel_hi:[1,1,0]\nv_exp_f32 v52, v52 clamp\nv_pk_fma_f32 v[46:47], v[46:47], v[2:3], v[26:27]\nv_exp_f32 v53, v53 clamp\n"
      "v_pk_fma_f32 v[44:45], v[44:45], v[0:1], v[24:25]\nv_pk_fma_f32 v[48:49], v[48:49], v[4:5], v[28:29]\nv_exp_f32 v54, v54 clamp\nv_pk_fma_f32 v[50:51], v[50:51], v[6:7], v[30:31]\n"
      "v_pk_mul_f32 v[220:221], v[220:221], v[46:47]\nv_exp_f32 v55, v55 clamp\nv_pk_mul_f32 v[224:225], v[224:225], v[50:51]\nv_pk_fma_f32 v[220:221], v[222:223], v[44:45], v[220:221]\n"
      "v_exp_f32 v56, v56 clamp\nv_pk_fma_f32 v[224:225], v[226:227], v[48:49], v[224:225]\nv_exp_f32 v57, v57 clamp\nv_pk_mul_f32 v[44:45], v[44:45], v[46:47]\n"
      "v_pk_mul_f32 v[48:49], v[48:49], v[50:51]\nv_exp_f32 v58, v58 clamp\nv_pk_mul_f32 v[46:47], v[44:45], v[48:49]\nv_rcp_f32 v46, v46\nv_pk_mul_f32 v[220:221], v[220:221], v[48:49]\n"
      "v_exp_f32 v59, v59 clamp\nv_rcp_f32 v47, v47\nv_pk_fma_f32 v[220:221], v[224:225], v[44:45], v[220:221]\nv_pk_mul_f32 v[32:33], v[220:221], v[46:47]\n"
      "v_pk_fma_f32 v[54:55], v[54:55], v[10:11], v[66:67]\nv_pk_fma_f32 v[52:53], v[52:53], v[8:9], v[64:65]\nv_pk_fma_f32 v[56:57], v[56:57], v[12:13], v[68:69]\n"
      "v_pk_fma_f32 v[58:59], v[58:59], v[14:15], v[70:71]\nv_pk_mul_f32 v[228:229], v[228:229], v[54:55]\nv_pk_mul_f32 v[232:233], v[232:233], v[58:59]\n"
      "v_pk_fma_f32 v[228:229], v[230:231], v[52:53], v[228:229]\nv_pk_fma_f32 v[232:233], v[234:235], v[56:57], v[232:233]\nv_pk_mul_f32 v[52:53], v[52:53], v[54:55]\n"
      "v_pk_mul_f32 v[56:57], v[56:57], v[58:59]\nv_pk_mul_f32 v[54:55], v[52:53], v[56:57]\nv_rcp_f32 v54, v54\nv_pk_mul_f32 v[228:229], v[228:229], v[56:57]\nv_rcp_f32 v55, v55\n"
      "v_pk_fma_f32 v[228:229], v[232:233], v[52:53], v[228:229]\nv_pk_fma_f32 v[32:33], v[228:229], v[54:55], v[32:33]\nv_add_f32 v40, v32, v33\ns_barrier\ns_branch .Lge\n"
      ".Lg12:\ns_waitcnt vmcnt(4)\ns_barrier\nds_read_b64_tr_b16 v[64:65], v104 offset:45056\nds_read_b64_tr_b16 v[66:67], v104 offset:46080\nds_read_b64_tr_b16 v[68:69], v104 offset:53248\n"
      "ds_read_b64_tr_b16 v[70:71], v104 offset:54272\nds_read_b64_tr_b16 v[72:73], v105 offset:45056\nds_read_b64_tr_b16 v[74:75], v105 offset:46080\n"
      "ds_read_b64_tr_b16 v[76:77], v105 offset:53248\nds_read_b64_tr_b16 v[78:79], v105 offset:54272\nds_read_b64_tr_b16 v[80:81], v106 offset:45056\n"
      "ds_read_b64_tr_b16 v[82:83], v106 offset:46080\nds_read_b64_tr_b16 v[84:85], v106 offset:53248\nds_read_b64_tr_b16 v[86:87], v106 offset:54272\n"
      "ds_read_b64_tr_b16 v[88:89], v107 offset:45056\nds_read_b64_tr_b16 v[90:91], v107 offset:46080\nds_read_b64_tr_b16 v[92:93], v107 offset:53248\n"
      "ds_read_b64_tr_b16 v[94:95], v107 offset:54272\nds_read_b128 v[8:11], v120 offset:45056\nds_read_b128 v[12:15], v121 offset:45056\nds_read_b128 v[16:19], v96 offset:45056\n"
      "s_waitcnt lgkmcnt(1)\n.p2align 3\n"
      ".Lgl12:\nv_mfma_f32_16x16x32_bf16 v[152:155], v[88:91], v[8:11], v[152:155]\nds_read_b128 v[20:23], v97 offset:45056\nv_mfma_f32_16x16x32_bf16 v[152:155], v[92:95], v[12:15], v[152:155]\n"
      "ds_read_b128 v[24:27], v96 offset:47104\nv_mfma_f32_16x16x32_bf16 v[148:151], v[84:87], v[12:15], v[148:151]\nds_read_b128 v[28:31], v97 offset:47104\nds_read_b128 v[0:3], v96 offset:49152\n"
      "v_mfma_f32_16x16x32_bf16 v[148:151], v[80:83], v[8:11], v[148:151]\nds_read_b128 v[4:7], v97 offset:49152\nv_mfma_f32_16x16x32_bf16 v[144:147], v[72:75], v[8:11], v[144:147]\n"
      "v_mfma_f32_16x16x32_bf16 v[144:147], v[76:79], v[12:15], v[144:147]\nv_mfma_f32_16x16x32_bf16 v[140:143], v[68:71], v[12:15], v[140:143]\ns_waitcnt lgkmcnt(4)\n"
      "v_mfma_f32_16x16x32_bf16 v[140:143], v[64:67], v[8:11], v[140:143]\nv_mfma_f32_16x16x32_bf16 v[156:159], v[64:67], v[16:19], v[156:159]\nds_read_b128 v[8:11], v96 offset:51200\n"
      "v_mfma_f32_16x16x32_bf16 v[156:159], v[68:71], v[20:23], v[156:159]\nds_read_b128 v[12:15], v97 offset:51200\nv_mfma_f32_16x16x32_bf16 v[160:163], v[76:79], v[20:23], v[160:163]\n"
      "v_mfma_f32_16x16x32_bf16 v[160:163], v[72:75], v[16:19], v[160:163]\nv_mfma_f32_16x16x32_bf16 v[164:167], v[80:83], v[16:19], v[164:167]\n"
      "v_mfma_f32_16x16x32_bf16 v[164:167], v[84:87], v[20:23], v[164:167]\nv_mfma_f32_16x16x32_bf16 v[168:171], v[92:95], v[20:23], v[168:171]\ns_waitcnt lgkmcnt(2)\n"
      "v_mfma_f32_16x16x32_bf16 v[168:171], v[88:91], v[16:19], v[168:171]\nv_mfma_f32_16x16x32_bf16 v[184:187], v[88:91], v[24:27], v[184:187]\nds_read_b128 v[16:19], v96 offset:53248\n"
      "v_mfma_f32_16x16x32_bf16 v[184:187], v[92:95], v[28:31], v[184:187]\nds_read_b128 v[20:23], v97 offset:53248\nv_mfma_f32_16x16x32_bf16 v[180:183], v[84:87], v[28:31], v[180:183]\n"
      "v_mfma_f32_16x16x32_bf16 v[180:183], v[80:83], v[24:27], v[180:183]\nv_mfma_f32_16x16x32_bf16 v[176:179], v[72:75], v[24:27], v[176:179]\n"
      "v_mfma_f32_16x16x32_bf16 v[176:179], v[76:79], v[28:31], v[176:179]\ns_waitcnt vmcnt(0) lgkmcnt(0)\nv_mfma_f32_16x16x32_bf16 v[172:175], v[68:71], v[28:31], v[172:175]\ns_barrier\n"
      "v_mfma_f32_16x16x32_bf16 v[172:175], v[64:67], v[24:27], v[172:175]\nds_read_b64_tr_b16 v[32:33], v108 offset:0\nv_mfma_f32_16x16x32_bf16 v[188:191], v[64:67], v[0:3], v[188:191]\n"
      "ds_read_b64_tr_b16 v[34:35], v108 offset:1024\nv_mfma_f32_16x16x32_bf16 v[188:191], v[68:71], v[4:7], v[188:191]\nds_read_b64_tr_b16 v[36:37], v108 offset:8192\n"
      "v_mfma_f32_16x16x32_bf16 v[192:195], v[76:79], v[4:7], v[192:195]\nds_read_b64_tr_b16 v[38:39], v108 offset:9216\nv_mfma_f32_16x16x32_bf16 v[192:195], v[72:75], v[0:3], v[192:195]\n"
      "ds_read_b64_tr_b16 v[40:41], v109 offset:0\nv_mfma_f32_16x16x32_bf16 v[196:199], v[80:83], v[0:3], v[196:199]\nds_read_b64_tr_b16 v[42:43], v109 offset:1024\n"
      "v_mfma_f32_16x16x32_bf16 v[196:199], v[84:87], v[4:7], v[196:199]\nds_read_b64_tr_b16 v[44:45], v109 offset:8192\nv_mfma_f32_16x16x32_bf16 v[200:203], v[92:95], v[4:7], v[200:203]\n"
      "ds_read_b64_tr_b16 v[46:47], v109 offset:9216\nv_mfma_f32_16x16x32_bf16 v[200:203], v[88:91], v[0:3], v[200:203]\nds_read_b64_tr_b16 v[48:49], v110 offset:0\n"
      "v_mfma_f32_16x16x32_bf16 v[216:219], v[88:91], v[8:11], v[216:219]\nv_mfma_f32_16x16x32_bf16 v[216:219], v[92:95], v[12:15], v[216:219]\n"
      "v_mfma_f32_16x16x32_bf16 v[212:215], v[84:87], v[12:15], v[212:215]\nds_read_b64_tr_b16 v[50:51], v110 offset:1024\nv_mfma_f32_16x16x32_bf16 v[212:215], v[80:83], v[8:11], v[212:215]\n"
      "ds_read_b64_tr_b16 v[52:53], v110 offset:8192\nv_mfma_f32_16x16x32_bf16 v[208:211], v[72:75], v[8:11], v[208:211]\nds_read_b64_tr_b16 v[54:55], v110 offset:9216\n"
      "v_mfma_f32_16x16x32_bf16 v[208:211], v[76:79], v[12:15], v[208:211]\nds_read_b64_tr_b16 v[56:57], v111 offset:0\nv_mfma_f32_16x16x32_bf16 v[204:207], v[68:71], v[12:15], v[204:207]\n"
      "ds_read_b64_tr_b16 v[58:59], v111 offset:1024\nv_mfma_f32_16x16x32_bf16 v[204:207], v[64:67], v[8:11], v[204:207]\nds_read_b64_tr_b16 v[60:61], v111 offset:8192\n"
      "v_mfma_f32_16x16x32_bf16 v[220:223], v[64:67], v[16:19], v[220:223]\nds_read_b128 v[8:11], v122 offset:0\nv_mfma_f32_16x16x32_bf16 v[220:223], v[68:71], v[20:23], v[220:223]\n"
      "ds_read_b128 v[12:15], v123 offset:0\nv_mfma_f32_16x16x32_bf16 v[224:227], v[76:79], v[20:23], v[224:227]\nds_read_b64_tr_b16 v[62:63], v111 offset:9216\n"
      "v_mfma_f32_16x16x32_bf16 v[224:227], v[72:75], v[16:19], v[224:227]\nv_mfma_f32_16x16x32_bf16 v[228:231], v[80:83], v[16:19], v[228:231]\n"
      "v_mfma_f32_16x16x32_bf16 v[228:231], v[84:87], v[20:23], v[228:231]\nv_mfma_f32_16x16x32_bf16 v[232:235], v[92:95], v[20:23], v[232:235]\ns_waitcnt lgkmcnt(0)\n"
      "v_mfma_f32_16x16x32_bf16 v[232:235], v[88:91], v[16:19], v[232:235]\nds_read_b128 v[16:19], v98 offset:0\nv_mfma_f32_16x16x32_bf16 v[152:155], v[56:59], v[8:11], v[152:155]\n"
      "ds_read_b128 v[20:23], v99 offset:0\nv_mfma_f32_16x16x32_bf16 v[152:155], v[60:63], v[12:15], v[152:155]\nds_read_b128 v[24:27], v98 offset:2048\n"
      "v_mfma_f32_16x16x32_bf16 v[148:151], v[52:55], v[12:15], v[148:151]\nds_read_b128 v[28:31], v99 offset:2048\nds_read_b128 v[0:3], v98 offset:4096\n"
      "v_mfma_f32_16x16x32_bf16 v[148:151], v[48:51], v[8:11], v[148:151]\nds_read_b128 v[4:7], v99 offset:4096\nv_mfma_f32_16x16x32_bf16 v[144:147], v[40:43], v[8:11], v[144:147]\n"
      "v_mfma_f32_16x16x32_bf16 v[144:147], v[44:47], v[12:15], v[144:147]\nv_mfma_f32_16x16x32_bf16 v[140:143], v[36:39], v[12:15], v[140:143]\ns_waitcnt lgkmcnt(4)\n"
      "v_mfma_f32_16x16x32_bf16 v[140:143], v[32:35], v[8:11], v[140:143]\nv_mfma_f32_16x16x32_bf16 v[156:159], v[32:35], v[16:19], v[156:159]\nds_read_b128 v[8:11], v98 offset:6144\n"
      "v_mfma_f32_16x16x32_bf16 v[156:159], v[36:39], v[20:23], v[156:159]\nds_read_b128 v[12:15], v99 offset:6144\nv_mfma_f32_16x16x32_bf16 v[160:163], v[44:47], v[20:23], v[160:163]\n"
      "v_mfma_f32_16x16x32_bf16 v[160:163], v[40:43], v[16:19], v[160:163]\nv_mfma_f32_16x16x32_bf16 v[164:167], v[48:51], v[16:19], v[164:167]\n"
      "v_mfma_f32_16x16x32_bf16 v[164:167], v[52:55], v[20:23], v[164:167]\nv_mfma_f32_16x16x32_bf16 v[168:171], v[60:63], v[20:23], v[168:171]\ns_waitcnt lgkmcnt(2)\n"
      "v_mfma_f32_16x16x32_bf16 v[168:171], v[56:59], v[16:19], v[168:171]\nv_mfma_f32_16x16x32_bf16 v[184:187], v[56:59], v[24:27], v[184:187]\nds_read_b128 v[16:19], v98 offset:8192\n"
      "v_mfma_f32_16x16x32_bf16 v[184:187], v[60:63], v[28:31], v[184:187]\nds_read_b128 v[20:23], v99 offset:8192\nv_mfma_f32_16x16x32_bf16 v[180:183], v[52:55], v[28:31], v[180:183]\n"
      "v_mfma_f32_16x16x32_bf16 v[180:183], v[48:51], v[24:27], v[180:183]\nv_mfma_f32_16x16x32_bf16 v[176:179], v[40:43], v[24:27], v[176:179]\n"
      "v_mfma_f32_16x16x32_bf16 v[176:179], v[44:47], v[28:31], v[176:179]\ns_waitcnt vmcnt(0) lgkmcnt(0)\nv_mfma_f32_16x16x32_bf16 v[172:175], v[36:39], v[28:31], v[172:175]\ns_barrier\n"
      "v_mfma_f32_16x16x32_bf16 v[172:175], v[32:35], v[24:27], v[172:175]\nds_read_b64_tr_b16 v[64:65], v104 offset:0\nv_mfma_f32_16x16x32_bf16 v[188:191], v[32:35], v[0:3], v[188:191]\n"
      "ds_read_b64_tr_b16 v[66:67], v104 offset:1024\nv_mfma_f32_16x16x32_bf16 v[188:191], v[36:39], v[4:7], v[188:191]\nds_read_b64_tr_b16 v[68:69], v104 offset:8192\n"
      "v_mfma_f32_16x16x32_bf16 v[192:195], v[44:47], v[4:7], v[192:195]\nds_read_b64_tr_b16 v[70:71], v104 offset:9216\nv_mfma_f32_16x16x32_bf16 v[192:195], v[40:43], v[0:3], v[192:195]\n"
      "ds_read_b64_tr_b16 v[72:73], v105 offset:0\nv_mfma_f32_16x16x32_bf16 v[196:199], v[48:51], v[0:3], v[196:199]\nds_read_b64_tr_b16 v[74:75], v105 offset:1024\n"
      "v_mfma_f32_16x16x32_bf16 v[196:199], v[52:55], v[4:7], v[196:199]\nds_read_b64_tr_b16 v[76:77], v105 offset:8192\nv_mfma_f32_16x16x32_bf16 v[200:203], v[60:63], v[4:7], v[200:203]\n"
      "ds_read_b64_tr_b16 v[78:79], v105 offset:9216\nv_mfma_f32_16x16x32_bf16 v[200:203], v[56:59], v[0:3], v[200:203]\nds_read_b64_tr_b16 v[80:81], v106 offset:0\n"
      "v_mfma_f32_16x16x32_bf16 v[216:219], v[56:59], v[8:11], v[216:219]\nv_mfma_f32_16x16x32_bf16 v[216:219], v[60:63], v[12:15], v[216:219]\n"
      "v_mfma_f32_16x16x32_bf16 v[212:215], v[52:55], v[12:15], v[212:215]\nds_read_b64_tr_b16 v[82:83], v106 offset:1024\nv_mfma_f32_16x16x32_bf16 v[212:215], v[48:51], v[8:11], v[212:215]\n"
      "ds_read_b64_tr_b16 v[84:85], v106 offset:8192\nv_mfma_f32_16x16x32_bf16 v[208:211], v[40:43], v[8:11], v[208:211]\nds_read_b64_tr_b16 v[86:87], v106 offset:9216\n"
      "v_mfma_f32_16x16x32_bf16 v[208:211], v[44:47], v[12:15], v[208:211]\nds_read_b64_tr_b16 v[88:89], v107 offset:0\nv_mfma_f32_16x16x32_bf16 v[204:207], v[36:39], v[12:15], v[204:207]\n"
      "ds_read_b64_tr_b16 v[90:91], v107 offset:1024\nv_mfma_f32_16x16x32_bf16 v[204:207], v[32:35], v[8:11], v[204:207]\nds_read_b64_tr_b16 v[92:93], v107 offset:8192\n"
      "v_mfma_f32_16x16x32_bf16 v[220:223], v[32:35], v[16:19], v[220:223]\nds_read_b128 v[8:11], v120 offset:0\nv_mfma_f32_16x16x32_bf16 v[220:223], v[36:39], v[20:23], v[220:223]\n"
      "ds_read_b128 v[12:15], v121 offset:0\nv_mfma_f32_16x16x32_bf16 v[224:227], v[44:47], v[20:23], v[224:227]\nds_read_b64_tr_b16 v[94:95], v107 offset:9216\n"
      "v_mfma_f32_16x16x32_bf16 v[224:227], v[40:43], v[16:19], v[224:227]\nv_mfma_f32_16x16x32_bf16 v[228:231], v[48:51], v[16:19], v[228:231]\n"
      "v_mfma_f32_16x16x32_bf16 v[228:231], v[52:55], v[20:23], v[228:231]\nv_mfma_f32_16x16x32_bf16 v[232:235], v[60:63], v[20:23], v[232:235]\ns_waitcnt lgkmcnt(0)\n"
      "v_mfma_f32_16x16x32_bf16 v[232:235], v[56:59], v[16:19], v[232:235]\nds_read_b128 v[16:19], v96 offset:0\nv_mfma_f32_16x16x32_bf16 v[152:155], v[88:91], v[8:11], v[152:155]\n"
      "ds_read_b128 v[20:23], v97 offset:0\nv_mfma_f32_16x16x32_bf16 v[152:155], v[92:95], v[12:15], v[152:155]\nds_read_b128 v[24:27], v96 offset:2048\n"
      "v_mfma_f32_16x16x32_bf16 v[148:151], v[84:87], v[12:15], v[148:151]\nds_read_b128 v[28:31], v97 offset:2048\nds_read_b128 v[0:3], v96 offset:4096\n"
      "v_mfma_f32_16x16x32_bf16 v[148:151], v[80:83], v[8:11], v[148:151]\nds_read_b128 v[4:7], v97 offset:4096\nv_mfma_f32_16x16x32_bf16 v[144:147], v[72:75], v[8:11], v[144:147]\n"
      "v_mfma_f32_16x16x32_bf16 v[144:147], v[76:79], v[12:15], v[144:147]\nv_mfma_f32_16x16x32_bf16 v[140:143], v[68:71], v[12:15], v[140:143]\ns_waitcnt lgkmcnt(4)\n"
      "v_mfma_f32_16x16x32_bf16 v[140:143], v[64:67], v[8:11], v[140:143]\nv_mfma_f32_16x16x32_bf16 v[156:159], v[64:67], v[16:19], v[156:159]\nds_read_b128 v[8:11], v96 offset:6144\n"
      "v_mfma_f32_16x16x32_bf16 v[156:159], v[68:71], v[20:23], v[156:159]\nds_read_b128 v[12:15], v97 offset:6144\nv_mfma_f32_16x16x32_bf16 v[160:163], v[76:79], v[20:23], v[160:163]\n"
      "v_mfma_f32_16x16x32_bf16 v[160:163], v[72:75], v[16:19], v[160:163]\nv_mfma_f32_16x16x32_bf16 v[164:167], v[80:83], v[16:19], v[164:167]\n"
      "v_mfma_f32_16x16x32_bf16 v[164:167], v[84:87], v[20:23], v[164:167]\nv_mfma_f32_16x16x32_bf16 v[168:171], v[92:95], v[20:23], v[168:171]\ns_waitcnt lgkmcnt(2)\n"
      "v_mfma_f32_16x16x32_bf16 v[168:171], v[88:91], v[16:19], v[168:171]\nv_mfma_f32_16x16x32_bf16 v[184:187], v[88:91], v[24:27], v[184:187]\nds_read_b128 v[16:19], v96 offset:8192\n"
      "v_mfma_f32_16x16x32_bf16 v[184:187], v[92:95], v[28:31], v[184:187]\nds_read_b128 v[20:23], v97 offset:8192\nv_mfma_f32_16x16x32_bf16 v[180:183], v[84:87], v[28:31], v[180:183]\n"
      "v_mfma_f32_16x16x32_bf16 v[180:183], v[80:83], v[24:27], v[180:183]\nv_mfma_f32_16x16x32_bf16 v[176:179], v[72:75], v[24:27], v[176:179]\n"
      "v_mfma_f32_16x16x32_bf16 v[176:179], v[76:79], v[28:31], v[176:179]\ns_waitcnt vmcnt(0) lgkmcnt(0)\nv_mfma_f32_16x16x32_bf16 v[172:175], v[68:71], v[28:31], v[172:175]\ns_barrier\n"
      "v_mfma_f32_16x16x32_bf16 v[172:175], v[64:67], v[24:27], v[172:175]\nds_read_b64_tr_b16 v[32:33], v104 offset:45056\nv_mfma_f32_16x16x32_bf16 v[188:191], v[64:67], v[0:3], v[188:191]\n"
      "ds_read_b64_tr_b16 v[34:35], v104 offset:46080\nv_mfma_f32_16x16x32_bf16 v[188:191], v[68:71], v[4:7], v[188:191]\nds_read_b64_tr_b16 v[36:37], v104 offset:53248\n"
      "v_mfma_f32_16x16x32_bf16 v[192:195], v[76:79], v[4:7], v[192:195]\nds_read_b64_tr_b16 v[38:39], v104 offset:54272\nv_mfma_f32_16x16x32_bf16 v[192:195], v[72:75], v[0:3], v[192:195]\n"
      "ds_read_b64_tr_b16 v[40:41], v105 offset:45056\nv_mfma_f32_16x16x32_bf16 v[196:199], v[80:83], v[0:3], v[196:199]\nds_read_b64_tr_b16 v[42:43], v105 offset:46080\n"
      "v_mfma_f32_16x16x32_bf16 v[196:199], v[84:87], v[4:7], v[196:199]\nds_read_b64_tr_b16 v[44:45], v105 offset:53248\nv_mfma_f32_16x16x32_bf16 v[200:203], v[92:95], v[4:7], v[200:203]\n"
      "ds_read_b64_tr_b16 v[46:47], v105 offset:54272\nv_mfma_f32_16x16x32_bf16 v[200:203], v[88:91], v[0:3], v[200:203]\nds_read_b64_tr_b16 v[48:49], v106 offset:45056\n"
      "v_mfma_f32_16x16x32_bf16 v[216:219], v[88:91], v[8:11], v[216:219]\nv_mfma_f32_16x16x32_bf16 v[216:219], v[92:95], v[12:15], v[216:219]\n"
      "v_mfma_f32_16x16x32_bf16 v[212:215], v[84:87], v[12:15], v[212:215]\nds_read_b64_tr_b16 v[50:51], v106 offset:46080\nv_mfma_f32_16x16x32_bf16 v[212:215], v[80:83], v[8:11], v[212:215]\n"
      "ds_read_b64_tr_b16 v[52:53], v106 offset:53248\nv_mfma_f32_16x16x32_bf16 v[208:211], v[72:75], v[8:11], v[208:211]\nds_read_b64_tr_b16 v[54:55], v106 offset:54272\n"
      "v_mfma_f32_16x16x32_bf16 v[208:211], v[76:79], v[12:15], v[208:211]\nds_read_b64_tr_b16 v[56:57], v107 offset:45056\nv_mfma_f32_16x16x32_bf16 v[204:207], v[68:71], v[12:15], v[204:207]\n"
      "ds_read_b64_tr_b16 v[58:59], v107 offset:46080\nv_mfma_f32_16x16x32_bf16 v[204:207], v[64:67], v[8:11], v[204:207]\nds_read_b64_tr_b16 v[60:61], v107 offset:53248\n"
      "v_mfma_f32_16x16x32_bf16 v[220:223], v[64:67], v[16:19], v[220:223]\nds_read_b128 v[8:11], v120 offset:45056\nv_mfma_f32_16x16x32_bf16 v[220:223], v[68:71], v[20:23], v[220:223]\n"
      "ds_read_b128 v[12:15], v121 offset:45056\nv_mfma_f32_16x16x32_bf16 v[224:227], v[76:79], v[20:23], v[224:227]\nds_read_b64_tr_b16 v[62:63], v107 offset:54272\n"
      "v_mfma_f32_16x16x32_bf16 v[224:227], v[72:75], v[16:19], v[224:227]\nv_mfma_f32_16x16x32_bf16 v[228:231], v[80:83], v[16:19], v[228:231]\n"
      "v_mfma_f32_16x16x32_bf16 v[228:231], v[84:87], v[20:23], v[228:231]\nv_mfma_f32_16x16x32_bf16 v[232:235], v[92:95], v[20:23], v[232:235]\ns_waitcnt lgkmcnt(0)\n"
      "v_mfma_f32_16x16x32_bf16 v[232:235], v[88:91], v[16:19], v[232:235]\nds_read_b128 v[16:19], v96 offset:45056\nv_mfma_f32_16x16x32_bf16 v[152:155], v[56:59], v[8:11], v[152:155]\n"
      "ds_read_b128 v[20:23], v97 offset:45056\nv_mfma_f32_16x16x32_bf16 v[152:155], v[60:63], v[12:15], v[152:155]\nds_read_b128 v[24:27], v96 offset:47104\n"
      "v_mfma_f32_16x16x32_bf16 v[148:151], v[52:55], v[12:15], v[148:151]\nds_read_b128 v[28:31], v97 offset:47104\nds_read_b128 v[0:3], v96 offset:49152\n"
      "v_mfma_f32_16x16x32_bf16 v[148:151], v[48:51], v[8:11], v[148:151]\nds_read_b128 v[4:7], v97 offset:49152\nv_mfma_f32_16x16x32_bf16 v[144:147], v[40:43], v[8:11], v[144:147]\n"
      "v_mfma_f32_16x16x32_bf16 v[144:147], v[44:47], v[12:15], v[144:147]\nv_mfma_f32_16x16x32_bf16 v[140:143], v[36:39], v[12:15], v[140:143]\ns_waitcnt lgkmcnt(4)\n"
      "v_mfma_f32_16x16x32_bf16 v[140:143], v[32:35], v[8:11], v[140:143]\nv_mfma_f32_16x16x32_bf16 v[156:159], v[32:35], v[16:19], v[156:159]\nds_read_b128 v[8:11], v96 offset:51200\n"
      "v_mfma_f32_16x16x32_bf16 v[156:159], v[36:39], v[20:23], v[156:159]\nds_read_b128 v[12:15], v97 offset:51200\nv_mfma_f32_16x16x32_bf16 v[160:163], v[44:47], v[20:23], v[160:163]\n"
      "v_mfma_f32_16x16x32_bf16 v[160:163], v[40:43], v[16:19], v[160:163]\nv_mfma_f32_16x16x32_bf16 v[164:167], v[48:51], v[16:19], v[164:167]\n"
      "v_mfma_f32_16x16x32_bf16 v[164:167], v[52:55], v[20:23], v[164:167]\nv_mfma_f32_16x16x32_bf16 v[168:171], v[60:63], v[20:23], v[168:171]\ns_waitcnt lgkmcnt(2)\n"
      "v_mfma_f32_16x16x32_bf16 v[168:171], v[56:59], v[16:19], v[168:171]\nv_mfma_f32_16x16x32_bf16 v[184:187], v[56:59], v[24:27], v[184:187]\nds_read_b128 v[16:19], v96 offset:53248\n"
      "v_mfma_f32_16x16x32_bf16 v[184:187], v[60:63], v[28:31], v[184:187]\nds_read_b128 v[20:23], v97 offset:53248\nv_mfma_f32_16x16x32_bf16 v[180:183], v[52:55], v[28:31], v[180:183]\n"
      "v_mfma_f32_16x16x32_bf16 v[180:183], v[48:51], v[24:27], v[180:183]\nv_mfma_f32_16x16x32_bf16 v[176:179], v[40:43], v[24:27], v[176:179]\n"
      "v_mfma_f32_16x16x32_bf16 v[176:179], v[44:47], v[28:31], v[176:179]\ns_waitcnt vmcnt(0) lgkmcnt(0)\nv_mfma_f32_16x16x32_bf16 v[172:175], v[36:39], v[28:31], v[172:175]\ns_barrier\n"
      "v_mfma_f32_16x16x32_bf16 v[172:175], v[32:35], v[24:27], v[172:175]\nds_read_b64_tr_b16 v[64:65], v108 offset:0\nv_mfma_f32_16x16x32_bf16 v[188:191], v[32:35], v[0:3], v[188:191]\n"
      "ds_read_b64_tr_b16 v[66:67], v108 offset:1024\nv_mfma_f32_16x16x32_bf16 v[188:191], v[36:39], v[4:7], v[188:191]\nds_read_b64_tr_b16 v[68:69], v108 offset:8192\n"
      "v_mfma_f32_16x16x32_bf16 v[192:195], v[44:47], v[4:7], v[192:195]\nds_read_b64_tr_b16 v[70:71], v108 offset:9216\nv_mfma_f32_16x16x32_bf16 v[192:195], v[40:43], v[0:3], v[192:195]\n"
      "ds_read_b64_tr_b16 v[72:73], v109 offset:0\nv_mfma_f32_16x16x32_bf16 v[196:199], v[48:51], v[0:3], v[196:199]\nds_read_b64_tr_b16 v[74:75], v109 offset:1024\n"
      "v_mfma_f32_16x16x32_bf16 v[196:199], v[52:55], v[4:7], v[196:199]\nds_read_b64_tr_b16 v[76:77], v109 offset:8192\nv_mfma_f32_16x16x32_bf16 v[200:203], v[60:63], v[4:7], v[200:203]\n"
      "ds_read_b64_tr_b16 v[78:79], v109 offset:9216\nv_mfma_f32_16x16x32_bf16 v[200:203], v[56:59], v[0:3], v[200:203]\nds_read_b64_tr_b16 v[80:81], v110 offset:0\n"
      "v_mfma_f32_16x16x32_bf16 v[216:219], v[56:59], v[8:11], v[216:219]\nv_mfma_f32_16x16x32_bf16 v[216:219], v[60:63], v[12:15], v[216:219]\n"
      "v_mfma_f32_16x16x32_bf16 v[212:215], v[52:55], v[12:15], v[212:215]\nds_read_b64_tr_b16 v[82:83], v110 offset:1024\nv_mfma_f32_16x16x32_bf16 v[212:215], v[48:51], v[8:11], v[212:215]\n"
      "ds_read_b64_tr_b16 v[84:85], v110 offset:8192\nv_mfma_f32_16x16x32_bf16 v[208:211], v[40:43], v[8:11], v[208:211]\nds_read_b64_tr_b16 v[86:87], v110 offset:9216\n"
      "v_mfma_f32_16x16x32_bf16 v[208:211], v[44:47], v[12:15], v[208:211]\nds_read_b64_tr_b16 v[88:89], v111 offset:0\nv_mfma_f32_16x16x32_bf16 v[204:207], v[36:39], v[12:15], v[204:207]\n"
      "ds_read_b64_tr_b16 v[90:91], v111 offset:1024\nv_mfma_f32_16x16x32_bf16 v[204:207], v[32:35], v[8:11], v[204:207]\nds_read_b64_tr_b16 v[92:93], v111 offset:8192\n"
      "v_mfma_f32_16x16x32_bf16 v[220:223], v[32:35], v[16:19], v[220:223]\nds_read_b128 v[8:11], v122 offset:0\nv_mfma_f32_16x16x32_bf16 v[220:223], v[36:39], v[20:23], v[220:223]\n"
      "ds_read_b128 v[12:15], v123 offset:0\nv_mfma_f32_16x16x32_bf16 v[224:227], v[44:47], v[20:23], v[224:227]\nds_read_b64_tr_b16 v[94:95], v111 offset:9216\n"
      "v_mfma_f32_16x16x32_bf16 v[224:227], v[40:43], v[16:19], v[224:227]\nv_mfma_f32_16x16x32_bf16 v[228:231], v[48:51], v[16:19], v[228:231]\n"
      "v_mfma_f32_16x16x32_bf16 v[228:231], v[52:55], v[20:23], v[228:231]\nv_mfma_f32_16x16x32_bf16 v[232:235], v[60:63], v[20:23], v[232:235]\ns_waitcnt lgkmcnt(0)\n"
      "v_mfma_f32_16x16x32_bf16 v[232:235], v[56:59], v[16:19], v[232:235]\nds_read_b128 v[16:19], v98 offset:0\nv_mfma_f32_16x16x32_bf16 v[152:155], v[88:91], v[8:11], v[152:155]\n"
      "ds_read_b128 v[20:23], v99 offset:0\nv_mfma_f32_16x16x32_bf16 v[152:155], v[92:95], v[12:15], v[152:155]\nds_read_b128 v[24:27], v98 offset:2048\n"
      "v_mfma_f32_16x16x32_bf16 v[148:151], v[84:87], v[12:15], v[148:151]\nds_read_b128 v[28:31], v99 offset:2048\nds_read_b128 v[0:3], v98 offset:4096\n"
      "v_mfma_f32_16x16x32_bf16 v[148:151], v[80:83], v[8:11], v[148:151]\nds_read_b128 v[4:7], v99 offset:4096\nv_mfma_f32_16x16x32_bf16 v[144:147], v[72:75], v[8:11], v[144:147]\n"
      "v_mfma_f32_16x16x32_bf16 v[144:147], v[76:79], v[12:15], v[144:147]\nv_mfma_f32_16x16x32_bf16 v[140:143], v[68:71], v[12:15], v[140:143]\ns_waitcnt lgkmcnt(4)\n"
      "v_mfma_f32_16x16x32_bf16 v[140:143], v[64:67], v[8:11], v[140:143]\nv_mfma_f32_16x16x32_bf16 v[156:159], v[64:67], v[16:19], v[156:159]\nds_read_b128 v[8:11], v98 offset:6144\n"
      "v_mfma_f32_16x16x32_bf16 v[156:159], v[68:71], v[20:23], v[156:159]\nds_read_b128 v[12:15], v99 offset:6144\nv_mfma_f32_16x16x32_bf16 v[160:163], v[76:79], v[20:23], v[160:163]\n"
      "v_mfma_f32_16x16x32_bf16 v[160:163], v[72:75], v[16:19], v[160:163]\nv_mfma_f32_16x16x32_bf16 v[164:167], v[80:83], v[16:19], v[164:167]\n"
      "v_mfma_f32_16x16x32_bf16 v[164:167], v[84:87], v[20:23], v[164:167]\nv_mfma_f32_16x16x32_bf16 v[168:171], v[92:95], v[20:23], v[168:171]\ns_waitcnt lgkmcnt(2)\n"
      "v_mfma_f32_16x16x32_bf16 v[168:171], v[88:91], v[16:19], v[168:171]\nv_mfma_f32_16x16x32_bf16 v[184:187], v[88:91], v[24:27], v[184:187]\nds_read_b128 v[16:19], v98 offset:8192\n"
      "v_mfma_f32_16x16x32_bf16 v[184:187], v[92:95], v[28:31], v[184:187]\nds_read_b128 v[20:23], v99 offset:8192\nv_mfma_f32_16x16x32_bf16 v[180:183], v[84:87], v[28:31], v[180:183]\n"
      "v_mfma_f32_16x16x32_bf16 v[180:183], v[80:83], v[24:27], v[180:183]\nv_mfma_f32_16x16x32_bf16 v[176:179], v[72:75], v[24:27], v[176:179]\n"
      "v_mfma_f32_16x16x32_bf16 v[176:179], v[76:79], v[28:31], v[176:179]\ns_waitcnt vmcnt(0) lgkmcnt(0)\nv_mfma_f32_16x16x32_bf16 v[172:175], v[68:71], v[28:31], v[172:175]\ns_barrier\n"
      "v_mfma_f32_16x16x32_bf16 v[172:175], v[64:67], v[24:27], v[172:175]\nds_read_b64_tr_b16 v[32:33], v104 offset:0\nv_mfma_f32_16x16x32_bf16 v[188:191], v[64:67], v[0:3], v[188:191]\n"
      "ds_read_b64_tr_b16 v[34:35], v104 offset:1024\nv_mfma_f32_16x16x32_bf16 v[188:191], v[68:71], v[4:7], v[188:191]\nds_read_b64_tr_b16 v[36:37], v104 offset:8192\n"
      "v_mfma_f32_16x16x32_bf16 v[192:195], v[76:79], v[4:7], v[192:195]\nds_read_b64_tr_b16 v[38:39], v104 offset:9216\nv_mfma_f32_16x16x32_bf16 v[192:195], v[72:75], v[0:3], v[192:195]\n"
      "ds_read_b64_tr_b16 v[40:41], v105 offset:0\nv_mfma_f32_16x16x32_bf16 v[196:199], v[80:83], v[0:3], v[196:199]\nds_read_b64_tr_b16 v[42:43], v105 offset:1024\n"
      "v_mfma_f32_16x16x32_bf16 v[196:199], v[84:87], v[4:7], v[196:199]\nds_read_b64_tr_b16 v[44:45], v105 offset:8192\nv_mfma_f32_16x16x32_bf16 v[200:203], v[92:95], v[4:7], v[200:203]\n"
      "ds_read_b64_tr_b16 v[46:47], v105 offset:9216\nv_mfma_f32_16x16x32_bf16 v[200:203], v[88:91], v[0:3], v[200:203]\nds_read_b64_tr_b16 v[48:49], v106 offset:0\n"
      "v_mfma_f32_16x16x32_bf16 v[216:219], v[88:91], v[8:11], v[216:219]\nv_mfma_f32_16x16x32_bf16 v[216:219], v[92:95], v[12:15], v[216:219]\n"
      "v_mfma_f32_16x16x32_bf16 v[212:215], v[84:87], v[12:15], v[212:215]\nds_read_b64_tr_b16 v[50:51], v106 offset:1024\nv_mfma_f32_16x16x32_bf16 v[212:215], v[80:83], v[8:11], v[212:215]\n"
      "ds_read_b64_tr_b16 v[52:53], v106 offset:8192\nv_mfma_f32_16x16x32_bf16 v[208:211], v[72:75], v[8:11], v[208:211]\nds_read_b64_tr_b16 v[54:55], v106 offset:9216\n"
      "v_mfma_f32_16x16x32_bf16 v[208:211], v[76:79], v[12:15], v[208:211]\nds_read_b64_tr_b16 v[56:57], v107 offset:0\nv_mfma_f32_16x16x32_bf16 v[204:207], v[68:71], v[12:15], v[204:207]\n"
      "ds_read_b64_tr_b16 v[58:59], v107 offset:1024\nv_mfma_f32_16x16x32_bf16 v[204:207], v[64:67], v[8:11], v[204:207]\nds_read_b64_tr_b16 v[60:61], v107 offset:8192\n"
      "v_mfma_f32_16x16x32_bf16 v[220:223], v[64:67], v[16:19], v[220:223]\nds_read_b128 v[8:11], v120 offset:0\nv_mfma_f32_16x16x32_bf16 v[220:223], v[68:71], v[20:23], v[220:223]\n"
      "ds_read_b128 v[12:15], v121 offset:0\nv_mfma_f32_16x16x32_bf16 v[224:227], v[76:79], v[20:23], v[224:227]\nds_read_b64_tr_b16 v[62:63], v107 offset:9216\n"
      "v_mfma_f32_16x16x32_bf16 v[224:227], v[72:75], v[16:19], v[224:227]\nv_mfma_f32_16x16x32_bf16 v[228:231], v[80:83], v[16:19], v[228:231]\n"
      "v_mfma_f32_16x16x32_bf16 v[228:231], v[84:87], v[20:23], v[228:231]\nv_mfma_f32_16x16x32_bf16 v[232:235], v[92:95], v[20:23], v[232:235]\ns_waitcnt lgkmcnt(0)\n"
      "v_mfma_f32_16x16x32_bf16 v[232:235], v[88:91], v[16:19], v[232:235]\nds_read_b128 v[16:19], v96 offset:0\nv_mfma_f32_16x16x32_bf16 v[152:155], v[56:59], v[8:11], v[152:155]\n"
      "ds_read_b128 v[20:23], v97 offset:0\nv_mfma_f32_16x16x32_bf16 v[152:155], v[60:63], v[12:15], v[152:155]\nds_read_b128 v[24:27], v96 offset:2048\n"
      "v_mfma_f32_16x16x32_bf16 v[148:151], v[52:55], v[12:15], v[148:151]\nds_read_b128 v[28:31], v97 offset:2048\nds_read_b128 v[0:3], v96 offset:4096\n"
      "v_mfma_f32_16x16x32_bf16 v[148:151], v[48:51], v[8:11], v[148:151]\nds_read_b128 v[4:7], v97 offset:4096\nv_mfma_f32_16x16x32_bf16 v[144:147], v[40:43], v[8:11], v[144:147]\n"
      "v_mfma_f32_16x16x32_bf16 v[144:147], v[44:47], v[12:15], v[144:147]\nv_mfma_f32_16x16x32_bf16 v[140:143], v[36:39], v[12:15], v[140:143]\ns_waitcnt lgkmcnt(4)\n"
      "v_mfma_f32_16x16x32_bf16 v[140:143], v[32:35], v[8:11], v[140:143]\nv_mfma_f32_16x16x32_bf16 v[156:159], v[32:35], v[16:19], v[156:159]\nds_read_b128 v[8:11], v96 offset:6144\n"
      "v_mfma_f32_16x16x32_bf16 v[156:159], v[36:39], v[20:23], v[156:159]\nds_read_b128 v[12:15], v97 offset:6144\nv_mfma_f32_16x16x32_bf16 v[160:163], v[44:47], v[20:23], v[160:163]\n"
      "v_mfma_f32_16x16x32_bf16 v[160:163], v[40:43], v[16:19], v[160:163]\nv_mfma_f32_16x16x32_bf16 v[164:167], v[48:51], v[16:19], v[164:167]\n"
      "v_mfma_f32_16x16x32_bf16 v[164:167], v[52:55], v[20:23], v[164:167]\nv_mfma_f32_16x16x32_bf16 v[168:171], v[60:63], v[20:23], v[168:171]\ns_waitcnt lgkmcnt(2)\n"
      "v_mfma_f32_16x16x32_bf16 v[168:171], v[56:59], v[16:19], v[168:171]\nv_mfma_f32_16x16x32_bf16 v[184:187], v[56:59], v[24:27], v[184:187]\nds_read_b128 v[16:19], v96 offset:8192\n"
      "v_mfma_f32_16x16x32_bf16 v[184:187], v[60:63], v[28:31], v[184:187]\nds_read_b128 v[20:23], v97 offset:8192\nv_mfma_f32_16x16x32_bf16 v[180:183], v[52:55], v[28:31], v[180:183]\n"
      "v_mfma_f32_16x16x32_bf16 v[180:183], v[48:51], v[24:27], v[180:183]\nv_mfma_f32_16x16x32_bf16 v[176:179], v[40:43], v[24:27], v[176:179]\n"
      "v_mfma_f32_16x16x32_bf16 v[176:179], v[44:47], v[28:31], v[176:179]\ns_waitcnt vmcnt(0) lgkmcnt(0)\nv_mfma_f32_16x16x32_bf16 v[172:175], v[36:39], v[28:31], v[172:175]\ns_barrier\n"
      "v_mfma_f32_16x16x32_bf16 v[172:175], v[32:35], v[24:27], v[172:175]\nds_read_b64_tr_b16 v[64:65], v104 offset:45056\nv_mfma_f32_16x16x32_bf16 v[188:191], v[32:35], v[0:3], v[188:191]\n"
      "ds_read_b64_tr_b16 v[66:67], v104 offset:46080\nv_mfma_f32_16x16x32_bf16 v[188:191], v[36:39], v[4:7], v[188:191]\nds_read_b64_tr_b16 v[68:69], v104 offset:53248\n"
      "v_mfma_f32_16x16x32_bf16 v[192:195], v[44:47], v[4:7], v[192:195]\nds_read_b64_tr_b16 v[70:71], v104 offset:54272\nv_mfma_f32_16x16x32_bf16 v[192:195], v[40:43], v[0:3], v[192:195]\n"
      "ds_read_b64_tr_b16 v[72:73], v105 offset:45056\nv_mfma_f32_16x16x32_bf16 v[196:199], v[48:51], v[0:3], v[196:199]\nds_read_b64_tr_b16 v[74:75], v105 offset:46080\n"
      "v_mfma_f32_16x16x32_bf16 v[196:199], v[52:55], v[4:7], v[196:199]\nds_read_b64_tr_b16 v[76:77], v105 offset:53248\nv_mfma_f32_16x16x32_bf16 v[200:203], v[60:63], v[4:7], v[200:203]\n"
      "ds_read_b64_tr_b16 v[78:79], v105 offset:54272\nv_mfma_f32_16x16x32_bf16 v[200:203], v[56:59], v[0:3], v[200:203]\nds_read_b64_tr_b16 v[80:81], v106 offset:45056\n"
      "v_mfma_f32_16x16x32_bf16 v[216:219], v[56:59], v[8:11], v[216:219]\nv_mfma_f32_16x16x32_bf16 v[216:219], v[60:63], v[12:15], v[216:219]\n"
      "v_mfma_f32_16x16x32_bf16 v[212:215], v[52:55], v[12:15], v[212:215]\nds_read_b64_tr_b16 v[82:83], v106 offset:46080\nv_mfma_f32_16x16x32_bf16 v[212:215], v[48:51], v[8:11], v[212:215]\n"
      "ds_read_b64_tr_b16 v[84:85], v106 offset:53248\nv_mfma_f32_16x16x32_bf16 v[208:211], v[40:43], v[8:11], v[208:211]\nds_read_b64_tr_b16 v[86:87], v106 offset:54272\n"
      "v_mfma_f32_16x16x32_bf16 v[208:211], v[44:47], v[12:15], v[208:211]\nds_read_b64_tr_b16 v[88:89], v107 offset:45056\nv_mfma_f32_16x16x32_bf16 v[204:207], v[36:39], v[12:15], v[204:207]\n"
      "ds_read_b64_tr_b16 v[90:91], v107 offset:46080\nv_mfma_f32_16x16x32_bf16 v[204:207], v[32:35], v[8:11], v[204:207]\nds_read_b64_tr_b16 v[92:93], v107 offset:53248\n"
      "v_mfma_f32_16x16x32_bf16 v[220:223], v[32:35], v[16:19], v[220:223]\nds_read_b128 v[8:11], v120 offset:45056\nv_mfma_f32_16x16x32_bf16 v[220:223], v[36:39], v[20:23], v[220:223]\n"
      "ds_read_b128 v[12:15], v121 offset:45056\nv_mfma_f32_16x16x32_bf16 v[224:227], v[44:47], v[20:23], v[224:227]\nds_read_b64_tr_b16 v[94:95], v107 offset:54272\n"
      "v_mfma_f32_16x16x32_bf16 v[224:227], v[40:43], v[16:19], v[224:227]\nv_mfma_f32_16x16x32_bf16 v[228:231], v[48:51], v[16:19], v[228:231]\ns_sub_u32 s58, s58, 1\ns_cmp_lg_u32 s58, 0\n"
      "v_mfma_f32_16x16x32_bf16 v[228:231], v[52:55], v[20:23], v[228:231]\nv_mfma_f32_16x16x32_bf16 v[232:235], v[60:63], v[20:23], v[232:235]\ns_waitcnt lgkmcnt(0)\n"
      "v_mfma_f32_16x16x32_bf16 v[232:235], v[56:59], v[16:19], v[232:235]\nds_read_b128 v[16:19], v96 offset:45056\ns_cbranch_scc1 .Lgl12\n"
      "v_mfma_f32_16x16x32_bf16 v[152:155], v[88:91], v[8:11], v[152:155]\nds_read_b128 v[20:23], v97 offset:45056\nv_cmp_eq_u32 s[62:63], 0, v240\n"
      "v_mfma_f32_16x16x32_bf16 v[152:155], v[92:95], v[12:15], v[152:155]\nds_read_b128 v[24:27], v96 offset:47104\nv_mov_b32 v253, 262144\n"
      "v_mfma_f32_16x16x32_bf16 v[148:151], v[84:87], v[12:15], v[148:151]\nds_read_b128 v[28:31], v97 offset:47104\nds_read_b128 v[0:3], v96 offset:49152\n"
      "v_mfma_f32_16x16x32_bf16 v[148:151], v[80:83], v[8:11], v[148:151]\nds_read_b128 v[4:7], v97 offset:49152\nv_mov_b32 v248, 162048\n"
      "v_mfma_f32_16x16x32_bf16 v[144:147], v[72:75], v[8:11], v[144:147]\nv_mov_b32 v241, 1\nv_cndmask_b32 v253, v253, v248, s[62:63]\n"
      "v_mfma_f32_16x16x32_bf16 v[144:147], v[76:79], v[12:15], v[144:147]\nv_lshrrev_b32 v112, 4, v240\nv_bfe_u32 v113, v240, 2, 2\n"
      "v_mfma_f32_16x16x32_bf16 v[140:143], v[68:71], v[12:15], v[140:143]\ns_waitcnt lgkmcnt(4)\nv_and_b32 v114, 3, v240\nv_mfma_f32_16x16x32_bf16 v[140:143], v[64:67], v[8:11], v[140:143]\n"
      "v_mul_u32_u24 v114, 0x80008, v114\nv_add_u32 v244, v112, v113\nv_mfma_f32_16x16x32_bf16 v[156:159], v[64:67], v[16:19], v[156:159]\nds_read_b128 v[8:11], v96 offset:51200\n"
      "v_and_b32 v244, 3, v244\nv_mfma_f32_16x16x32_bf16 v[156:159], v[68:71], v[20:23], v[156:159]\nds_read_b128 v[12:15], v97 offset:51200\nv_add_u32 v116, 22, v113\n"
      "v_mfma_f32_16x16x32_bf16 v[160:163], v[76:79], v[20:23], v[160:163]\nv_cmp_gt_u32 s[62:63], 2, v113\nv_and_b32 v113, 60, v240\n"
      "v_mfma_f32_16x16x32_bf16 v[160:163], v[72:75], v[16:19], v[160:163]\ns_mov_b32 s42, 0\nv_cndmask_b32 v116, v116, v244, s[62:63]\n"
      "v_mfma_f32_16x16x32_bf16 v[164:167], v[80:83], v[16:19], v[164:167]\nv_lshl_or_b32 v244, v244, 16, v116\nv_lshl_add_u32 v244, v244, 5, v114\n"
      "v_mfma_f32_16x16x32_bf16 v[164:167], v[84:87], v[20:23], v[164:167]\nv_lshl_add_u32 v252, v240, 4, s42\ns_mov_b32 s42, 0x76325410\n"
      "v_mfma_f32_16x16x32_bf16 v[168:171], v[92:95], v[20:23], v[168:171]\ns_waitcnt lgkmcnt(2)\ns_mov_b32 s43, 0x21005410\nv_mfma_f32_16x16x32_bf16 v[168:171], v[88:91], v[16:19], v[168:171]\n"
      "v_lshrrev_b64 v[116:117], v113, s[42:43]\nv_and_b32 v116, 7, v116\nv_mfma_f32_16x16x32_bf16 v[184:187], v[88:91], v[24:27], v[184:187]\nds_read_b128 v[16:19], v96 offset:53248\n"
      "v_lshl_add_u32 v116, v112, 3, v116\nv_mfma_f32_16x16x32_bf16 v[184:187], v[92:95], v[28:31], v[184:187]\nds_read_b128 v[20:23], v97 offset:53248\ns_mov_b32 s42, 0x54107632\n"
      "v_mfma_f32_16x16x32_bf16 v[180:183], v[84:87], v[28:31], v[180:183]\ns_mov_b32 s43, 0x21107632\nv_lshrrev_b64 v[118:119], v113, s[42:43]\n"
      "v_mfma_f32_16x16x32_bf16 v[180:183], v[80:83], v[24:27], v[180:183]\nv_and_b32 v118, 7, v118\nv_lshl_add_u32 v118, v112, 3, v118\n"
      "v_mfma_f32_16x16x32_bf16 v[176:179], v[72:75], v[24:27], v[176:179]\nv_lshl_or_b32 v245, v118, 16, v116\nv_lshl_add_u32 v245, v245, 5, v114\n"
      "v_mfma_f32_16x16x32_bf16 v[176:179], v[76:79], v[28:31], v[176:179]\ns_waitcnt vmcnt(0) lgkmcnt(0)\nv_and_b32 v113, 1, v240\n"
      "v_mfma_f32_16x16x32_bf16 v[172:175], v[68:71], v[28:31], v[172:175]\ns_barrier\nv_mul_u32_u24 v113, 0xaaaa, v113\nv_mfma_f32_16x16x32_bf16 v[172:175], v[64:67], v[24:27], v[172:175]\n"
      "ds_read_b64_tr_b16 v[32:33], v108 offset:0\nv_add_u32 v242, 0x4444, v113\nv_mfma_f32_16x16x32_bf16 v[188:191], v[64:67], v[0:3], v[188:191]\nds_read_b64_tr_b16 v[34:35], v108 offset:1024\n"
      "s_lshl_b32 s42, s36, 4\nv_mfma_f32_16x16x32_bf16 v[188:191], v[68:71], v[4:7], v[188:191]\nds_read_b64_tr_b16 v[36:37], v108 offset:8192\ns_add_u32 s42, s42, 160640\n"
      "v_mfma_f32_16x16x32_bf16 v[192:195], v[76:79], v[4:7], v[192:195]\nds_read_b64_tr_b16 v[38:39], v108 offset:9216\nv_lshl_add_u32 v243, v112, 2, s42\n"
      "v_mfma_f32_16x16x32_bf16 v[192:195], v[72:75], v[0:3], v[192:195]\nds_read_b64_tr_b16 v[40:41], v109 offset:0\nv_and_b32 v112, 1, v112\n"
      "v_mfma_f32_16x16x32_bf16 v[196:199], v[80:83], v[0:3], v[196:199]\nds_read_b64_tr_b16 v[42:43], v109 offset:1024\nv_lshl_add_u32 v112, v112, 3, s24\n"
      "v_mfma_f32_16x16x32_bf16 v[196:199], v[84:87], v[4:7], v[196:199]\nds_read_b64_tr_b16 v[44:45], v109 offset:8192\ns_sub_u32 s42, s64, 1\n"
      "v_mfma_f32_16x16x32_bf16 v[200:203], v[92:95], v[4:7], v[200:203]\nds_read_b64_tr_b16 v[46:47], v109 offset:9216\nv_min_u32 v112, s42, v112\n"
      "v_mfma_f32_16x16x32_bf16 v[200:203], v[88:91], v[0:3], v[200:203]\nds_read_b64_tr_b16 v[48:49], v110 offset:0\nv_lshlrev_b32 v112, 3, v112\n"
      "v_mfma_f32_16x16x32_bf16 v[216:219], v[88:91], v[8:11], v[216:219]\nv_and_b32 v113, 15, v240\nv_lshl_add_u32 v250, v112, 1, v113\n"
      "v_mfma_f32_16x16x32_bf16 v[216:219], v[92:95], v[12:15], v[216:219]\ns_mov_b32 s42, 136192\nv_lshl_add_u32 v250, v250, 2, s42\n"
      "v_mfma_f32_16x16x32_bf16 v[212:215], v[84:87], v[12:15], v[212:215]\nds_read_b64_tr_b16 v[50:51], v110 offset:1024\nv_min_u32 v113, 12, v113\n"
      "v_mfma_f32_16x16x32_bf16 v[212:215], v[80:83], v[8:11], v[212:215]\nds_read_b64_tr_b16 v[52:53], v110 offset:8192\ns_lshl_b32 s42, s30, 7\n"
      "v_mfma_f32_16x16x32_bf16 v[208:211], v[72:75], v[8:11], v[208:211]\nds_read_b64_tr_b16 v[54:55], v110 offset:9216\nv_lshl_add_u32 v113, v113, 15, s42\n"
      "v_mfma_f32_16x16x32_bf16 v[208:211], v[76:79], v[12:15], v[208:211]\nds_read_b64_tr_b16 v[56:57], v111 offset:0\nv_add_u32 v249, v113, v112\n"
      "v_mfma_f32_16x16x32_bf16 v[204:207], v[68:71], v[12:15], v[204:207]\nds_read_b64_tr_b16 v[58:59], v111 offset:1024\nv_mfma_f32_16x16x32_bf16 v[204:207], v[64:67], v[8:11], v[204:207]\n"
      "ds_read_b64_tr_b16 v[60:61], v111 offset:8192\nv_mfma_f32_16x16x32_bf16 v[220:223], v[64:67], v[16:19], v[220:223]\nds_read_b128 v[8:11], v122 offset:0\n"
      "v_mfma_f32_16x16x32_bf16 v[220:223], v[68:71], v[20:23], v[220:223]\nds_read_b128 v[12:15], v123 offset:0\nv_mfma_f32_16x16x32_bf16 v[224:227], v[76:79], v[20:23], v[224:227]\n"
      "ds_read_b64_tr_b16 v[62:63], v111 offset:9216\nv_mfma_f32_16x16x32_bf16 v[224:227], v[72:75], v[16:19], v[224:227]\nv_mfma_f32_16x16x32_bf16 v[228:231], v[80:83], v[16:19], v[228:231]\n"
      "v_mfma_f32_16x16x32_bf16 v[228:231], v[84:87], v[20:23], v[228:231]\nv_mfma_f32_16x16x32_bf16 v[232:235], v[92:95], v[20:23], v[232:235]\ns_waitcnt lgkmcnt(0)\n"
      "v_mfma_f32_16x16x32_bf16 v[232:235], v[88:91], v[16:19], v[232:235]\nds_read_b128 v[16:19], v98 offset:0\nv_mfma_f32_16x16x32_bf16 v[152:155], v[56:59], v[8:11], v[152:155]\n"
      "ds_read_b128 v[20:23], v99 offset:0\nv_mfma_f32_16x16x32_bf16 v[152:155], v[60:63], v[12:15], v[152:155]\nds_read_b128 v[24:27], v98 offset:2048\n"
      "v_mfma_f32_16x16x32_bf16 v[148:151], v[52:55], v[12:15], v[148:151]\nds_read_b128 v[28:31], v99 offset:2048\nds_read_b128 v[0:3], v98 offset:4096\n"
      "v_mfma_f32_16x16x32_bf16 v[148:151], v[48:51], v[8:11], v[148:151]\nds_read_b128 v[4:7], v99 offset:4096\nv_mfma_f32_16x16x32_bf16 v[144:147], v[40:43], v[8:11], v[144:147]\n"
      "v_mfma_f32_16x16x32_bf16 v[144:147], v[44:47], v[12:15], v[144:147]\nv_mfma_f32_16x16x32_bf16 v[140:143], v[36:39], v[12:15], v[140:143]\ns_waitcnt lgkmcnt(4)\n"
      "v_mfma_f32_16x16x32_bf16 v[140:143], v[32:35], v[8:11], v[140:143]\nv_mfma_f32_16x16x32_bf16 v[156:159], v[32:35], v[16:19], v[156:159]\nds_read_b128 v[8:11], v98 offset:6144\n"
      "v_mfma_f32_16x16x32_bf16 v[156:159], v[36:39], v[20:23], v[156:159]\nds_read_b128 v[12:15], v99 offset:6144\nv_mfma_f32_16x16x32_bf16 v[160:163], v[44:47], v[20:23], v[160:163]\n"
      "v_mfma_f32_16x16x32_bf16 v[160:163], v[40:43], v[16:19], v[160:163]\nv_mfma_f32_16x16x32_bf16 v[164:167], v[48:51], v[16:19], v[164:167]\n"
      "v_mfma_f32_16x16x32_bf16 v[164:167], v[52:55], v[20:23], v[164:167]\nv_mfma_f32_16x16x32_bf16 v[168:171], v[60:63], v[20:23], v[168:171]\ns_waitcnt lgkmcnt(2)\n"
      "v_mfma_f32_16x16x32_bf16 v[168:171], v[56:59], v[16:19], v[168:171]\nv_mfma_f32_16x16x32_bf16 v[184:187], v[56:59], v[24:27], v[184:187]\nds_read_b128 v[16:19], v98 offset:8192\n"
      "v_mfma_f32_16x16x32_bf16 v[184:187], v[60:63], v[28:31], v[184:187]\nds_read_b128 v[20:23], v99 offset:8192\nv_mfma_f32_16x16x32_bf16 v[180:183], v[52:55], v[28:31], v[180:183]\n"
      "v_mfma_f32_16x16x32_bf16 v[180:183], v[48:51], v[24:27], v[180:183]\nv_mfma_f32_16x16x32_bf16 v[176:179], v[40:43], v[24:27], v[176:179]\n"
      "v_mfma_f32_16x16x32_bf16 v[176:179], v[44:47], v[28:31], v[176:179]\ns_waitcnt vmcnt(0) lgkmcnt(0)\nv_mfma_f32_16x16x32_bf16 v[172:175], v[36:39], v[28:31], v[172:175]\ns_barrier\n"
      "v_mfma_f32_16x16x32_bf16 v[172:175], v[32:35], v[24:27], v[172:175]\nds_read_b128 v[72:75], v252 offset:0\nv_lshl_add_u32 v246, v240, 1, v248\n"
      "v_mfma_f32_16x16x32_bf16 v[188:191], v[32:35], v[0:3], v[188:191]\nds_read_b128 v[24:27], v236 offset:512\nds_read_u16 v251, v246 offset:64\n"
      "v_mfma_f32_16x16x32_bf16 v[188:191], v[36:39], v[4:7], v[188:191]\nds_read_b128 v[28:31], v236 offset:576\nds_read_b32 v247, v248 offset:24\n"
      "v_mfma_f32_16x16x32_bf16 v[192:195], v[44:47], v[4:7], v[192:195]\nds_read_b128 v[76:79], v252 offset:1024\nv_and_b32 v246, 48, v240\n"
      "v_mfma_f32_16x16x32_bf16 v[192:195], v[40:43], v[0:3], v[192:195]\nds_read_b128 v[80:83], v252 offset:2048\ns_mov_b32 s42, 42240\n"
      "v_mfma_f32_16x16x32_bf16 v[196:199], v[48:51], v[0:3], v[196:199]\nds_read_b128 v[84:87], v252 offset:3072\nv_lshl_add_u32 v246, v246, 2, s42\n"
      "v_mfma_f32_16x16x32_bf16 v[196:199], v[52:55], v[4:7], v[196:199]\nds_read_b128 v[88:91], v252 offset:4096\ns_mov_b32 s58, 0xbdd2d3e7\n"
      "v_mfma_f32_16x16x32_bf16 v[200:203], v[60:63], v[4:7], v[200:203]\nds_read_b128 v[92:95], v252 offset:5120\ns_waitcnt lgkmcnt(4)\n"
      "v_mfma_f32_16x16x32_bf16 v[200:203], v[56:59], v[0:3], v[200:203]\nds_read_b128 v[96:99], v252 offset:6144\nv_readfirstlane_b32 s39, v247\n"
      "v_mfma_f32_16x16x32_bf16 v[216:219], v[56:59], v[8:11], v[216:219]\nds_read_b128 v[0:3], v236 offset:0\nv_readlane_b32 s55, v251, 0\n"
      "v_mfma_f32_16x16x32_bf16 v[216:219], v[60:63], v[12:15], v[216:219]\nds_read_b128 v[4:7], v236 offset:64\ns_setprio 3\nv_mfma_f32_16x16x32_bf16 v[212:215], v[52:55], v[12:15], v[212:215]\n"
      "ds_read_b128 v[100:103], v252 offset:7168\ns_xor_b32 s40, s39, 0xa5c30f96\nv_mfma_f32_16x16x32_bf16 v[212:215], v[48:51], v[8:11], v[212:215]\nds_read_b128 v[104:107], v252 offset:8192\n"
      "s_mul_i32 s41, s39, 0x9E3779B1\nv_mfma_f32_16x16x32_bf16 v[208:211], v[40:43], v[8:11], v[208:211]\nds_read_b128 v[108:111], v252 offset:9216\ns_lshr_b32 s41, s41, 24\n"
      "v_mfma_f32_16x16x32_bf16 v[208:211], v[44:47], v[12:15], v[208:211]\nds_read_b128 v[64:67], v237 offset:512\nv_mfma_f32_16x16x32_bf16 v[204:207], v[36:39], v[12:15], v[204:207]\n"
      "ds_read_b128 v[68:71], v237 offset:576\nv_mfma_f32_16x16x32_bf16 v[204:207], v[32:35], v[8:11], v[204:207]\nds_read_b128 v[112:115], v252 offset:10240\n"
      "v_mfma_f32_16x16x32_bf16 v[220:223], v[32:35], v[16:19], v[220:223]\nds_read_b128 v[8:11], v237 offset:0\nv_mfma_f32_16x16x32_bf16 v[220:223], v[36:39], v[20:23], v[220:223]\n"
      "ds_read_b128 v[12:15], v237 offset:64\nv_mfma_f32_16x16x32_bf16 v[224:227], v[44:47], v[20:23], v[224:227]\nds_read_b128 v[116:119], v252 offset:11264\n"
      "v_mfma_f32_16x16x32_bf16 v[224:227], v[40:43], v[16:19], v[224:227]\nds_read_b128 v[120:123], v252 offset:12288\n"
      "v_add_u32_sdwa v239, s49, v245 dst_sel:DWORD dst_unused:UNUSED_PAD src0_sel:DWORD src1_sel:WORD_1\nv_mfma_f32_16x16x32_bf16 v[228:231], v[48:51], v[16:19], v[228:231]\n"
      "ds_read_b128 v[124:127], v252 offset:13312\nv_add_u32_sdwa v236, s49, v244 dst_sel:DWORD dst_unused:UNUSED_PAD src0_sel:DWORD src1_sel:WORD_0\n"
      "v_mfma_f32_16x16x32_bf16 v[228:231], v[52:55], v[20:23], v[228:231]\nds_read_b128 v[128:131], v252 offset:14336\n"
      "v_add_u32_sdwa v237, s49, v244 dst_sel:DWORD dst_unused:UNUSED_PAD src0_sel:DWORD src1_sel:WORD_1\nv_mfma_f32_16x16x32_bf16 v[232:235], v[60:63], v[20:23], v[232:235]\n"
      "ds_read_b128 v[132:135], v252 offset:15360\nv_add_u32_sdwa v238, s49, v245 dst_sel:DWORD dst_unused:UNUSED_PAD src0_sel:DWORD src1_sel:WORD_0\ns_waitcnt lgkmcnt(0)\n"
      "v_mfma_f32_16x16x32_bf16 v[232:235], v[56:59], v[16:19], v[232:235]\nds_read_b128 v[136:139], v252 offset:16384\nv_mov_b32 v42, 0\nv_pk_mul_f32 v[44:45], v[140:141], v[140:141]\n"
      "v_pk_mul_f32 v[46:47], v[142:143], v[142:143]\nv_pk_mul_f32 v[48:49], v[144:145], v[144:145]\nv_pk_mul_f32 v[50:51], v[146:147], v[146:147]\n"
      "v_pk_fma_f32 v[44:45], v[44:45], s[58:59], s[58:59] op_sel:[0,0,1] op_sel_hi:[1,0,1]\nv_pk_fma_f32 v[46:47], v[46:47], s[58:59], s[58:59] op_sel:[0,0,1] op_sel_hi:[1,0,1]\n"
      "v_pk_fma_f32 v[48:49], v[48:49], s[58:59], s[58:59] op_sel:[0,0,1] op_sel_hi:[1,0,1]\nv_pk_fma_f32 v[50:51], v[50:51], s[58:59], s[58:59] op_sel:[0,0,1] op_sel_hi:[1,0,1]\n"
      "v_pk_fma_f32 v[44:45], v[44:45], v[140:141], s[60:61] op_sel_hi:[1,1,0]\nv_pk_fma_f32 v[46:47], v[46:47], v[142:143], s[60:61] op_sel_hi:[1,1,0]\n"
      "v_pk_fma_f32 v[48:49], v[48:49], v[144:145], s[60:61] op_sel_hi:[1,1,0]\nv_pk_fma_f32 v[50:51], v[50:51], v[146:147], s[60:61] op_sel_hi:[1,1,0]\nv_exp_f32 v44, v44 clamp\n"
      "v_pk_mul_f32 v[52:53], v[148:149], v[148:149]\nv_exp_f32 v45, v45 clamp\nv_pk_mul_f32 v[54:55], v[150:151], v[150:151]\nv_pk_mul_f32 v[56:57], v[152:153], v[152:153]\n"
      "v_exp_f32 v46, v46 clamp\nv_pk_mul_f32 v[58:59], v[154:155], v[154:155]\nv_exp_f32 v47, v47 clamp\nv_pk_fma_f32 v[52:53], v[52:53], s[58:59], s[58:59] op_sel:[0,0,1] op_sel_hi:[1,0,1]\n"
      "v_pk_fma_f32 v[54:55], v[54:55], s[58:59], s[58:59] op_sel:[0,0,1] op_sel_hi:[1,0,1]\nv_exp_f32 v48, v48 clamp\n"
      "v_pk_fma_f32 v[56:57], v[56:57], s[58:59], s[58:59] op_sel:[0,0,1] op_sel_hi:[1,0,1]\nv_exp_f32 v49, v49 clamp\n"
      "v_pk_fma_f32 v[58:59], v[58:59], s[58:59], s[58:59] op_sel:[0,0,1] op_sel_hi:[1,0,1]\nv_pk_fma_f32 v[52:53], v[52:53], v[148:149], s[60:61] op_sel_hi:[1,1,0]\nv_exp_f32 v50, v50 clamp\n"
      "v_pk_fma_f32 v[54:55], v[54:55], v[150:151], s[60:61] op_sel_hi:[1,1,0]\nv_exp_f32 v51, v51 clamp\nv_pk_fma_f32 v[56:57], v[56:57], v[152:153], s[60:61] op_sel_hi:[1,1,0]\n"
      "v_pk_fma_f32 v[58:59], v[58:59], v[154:155], s[60:61] op_sel_hi:[1,1,0]\nv_exp_f32 v52, v52 clamp\nv_pk_fma_f32 v[46:47], v[46:47], v[2:3], v[26:27]\n"
      "v_pk_fma_f32 v[44:45], v[44:45], v[0:1], v[24:25]\nv_pk_fma_f32 v[48:49], v[48:49], v[4:5], v[28:29]\nv_exp_f32 v53, v53 clamp\nv_pk_fma_f32 v[50:51], v[50:51], v[6:7], v[30:31]\n"
      "v_pk_mul_f32 v[140:141], v[140:141], v[46:47]\nv_pk_mul_f32 v[144:145], v[144:145], v[50:51]\nv_exp_f32 v54, v54 clamp\nv_pk_fma_f32 v[140:141], v[142:143], v[44:45], v[140:141]\n"
      "v_pk_fma_f32 v[144:145], v[146:147], v[48:49], v[144:145]\nv_pk_mul_f32 v[44:45], v[44:45], v[46:47]\nv_exp_f32 v55, v55 clamp\nv_pk_mul_f32 v[48:49], v[48:49], v[50:51]\n"
      "v_pk_mul_f32 v[46:47], v[44:45], v[48:49]\nv_rcp_f32 v46, v46\nv_pk_mul_f32 v[140:141], v[140:141], v[48:49]\nv_rcp_f32 v47, v47\nv_pk_fma_f32 v[140:141], v[144:145], v[44:45], v[140:141]\n"
      "v_pk_mul_f32 v[32:33], v[140:141], v[46:47]\nv_pk_mul_f32 v[16:17], v[156:157], v[156:157]\nv_pk_mul_f32 v[18:19], v[158:159], v[158:159]\nv_exp_f32 v56, v56 clamp\n"
      "v_pk_mul_f32 v[20:21], v[160:161], v[160:161]\nv_pk_mul_f32 v[22:23], v[162:163], v[162:163]\nv_exp_f32 v57, v57 clamp\n"
      "v_pk_fma_f32 v[16:17], v[16:17], s[58:59], s[58:59] op_sel:[0,0,1] op_sel_hi:[1,0,1]\nv_pk_fma_f32 v[18:19], v[18:19], s[58:59], s[58:59] op_sel:[0,0,1] op_sel_hi:[1,0,1]\n"
      "v_pk_fma_f32 v[20:21], v[20:21], s[58:59], s[58:59] op_sel:[0,0,1] op_sel_hi:[1,0,1]\nv_exp_f32 v58, v58 clamp\n"
      "v_pk_fma_f32 v[22:23], v[22:23], s[58:59], s[58:59] op_sel:[0,0,1] op_sel_hi:[1,0,1]\nv_pk_fma_f32 v[16:17], v[16:17], v[156:157], s[60:61] op_sel_hi:[1,1,0]\n"
      "v_pk_fma_f32 v[18:19], v[18:19], v[158:159], s[60:61] op_sel_hi:[1,1,0]\nv_exp_f32 v59, v59 clamp\nv_pk_fma_f32 v[20:21], v[20:21], v[160:161], s[60:61] op_sel_hi:[1,1,0]\n"
      "v_pk_fma_f32 v[22:23], v[22:23], v[162:163], s[60:61] op_sel_hi:[1,1,0]\nv_exp_f32 v16, v16 clamp\nv_pk_fma_f32 v[54:55], v[54:55], v[10:11], v[66:67]\n"
      "v_pk_fma_f32 v[52:53], v[52:53], v[8:9], v[64:65]\nv_pk_fma_f32 v[56:57], v[56:57], v[12:13], v[68:69]\nv_exp_f32 v17, v17 clamp\nv_pk_fma_f32 v[58:59], v[58:59], v[14:15], v[70:71]\n"
      "v_pk_mul_f32 v[148:149], v[148:149], v[54:55]\nv_pk_mul_f32 v[152:153], v[152:153], v[58:59]\nv_exp_f32 v18, v18 clamp\nv_pk_fma_f32 v[148:149], v[150:151], v[52:53], v[148:149]\n"
      "v_pk_fma_f32 v[152:153], v[154:155], v[56:57], v[152:153]\nv_pk_mul_f32 v[52:53], v[52:53], v[54:55]\nv_exp_f32 v19, v19 clamp\nv_pk_mul_f32 v[56:57], v[56:57], v[58:59]\n"
      "v_pk_mul_f32 v[54:55], v[52:53], v[56:57]\nv_rcp_f32 v54, v54\nv_pk_mul_f32 v[148:149], v[148:149], v[56:57]\nv_rcp_f32 v55, v55\nv_pk_fma_f32 v[148:149], v[152:153], v[52:53], v[148:149]\n"
      "v_pk_fma_f32 v[32:33], v[148:149], v[54:55], v[32:33]\nv_add_f32 v41, v32, v33\nv_pk_mul_f32 v[44:45], v[164:165], v[164:165]\nv_pk_mul_f32 v[46:47], v[166:167], v[166:167]\n"
      "v_exp_f32 v20, v20 clamp\nv_pk_mul_f32 v[48:49], v[168:169], v[168:169]\nv_pk_mul_f32 v[50:51], v[170:171], v[170:171]\nv_exp_f32 v21, v21 clamp\n"
      "v_pk_fma_f32 v[44:45], v[44:45], s[58:59], s[58:59] op_sel:[0,0,1] op_sel_hi:[1,0,1]\nv_pk_fma_f32 v[46:47], v[46:47], s[58:59], s[58:59] op_sel:[0,0,1] op_sel_hi:[1,0,1]\n"
      "v_pk_fma_f32 v[48:49], v[48:49], s[58:59], s[58:59] op_sel:[0,0,1] op_sel_hi:[1,0,1]\nv_exp_f32 v22, v22 clamp\n"
      "v_pk_fma_f32 v[50:51], v[50:51], s[58:59], s[58:59] op_sel:[0,0,1] op_sel_hi:[1,0,1]\nv_pk_fma_f32 v[44:45], v[44:45], v[164:165], s[60:61] op_sel_hi:[1,1,0]\n"
      "v_pk_fma_f32 v[46:47], v[46:47], v[166:167], s[60:61] op_sel_hi:[1,1,0]\nv_exp_f32 v23, v23 clamp\nv_pk_fma_f32 v[48:49], v[48:49], v[168:169], s[60:61] op_sel_hi:[1,1,0]\n"
      "v_pk_fma_f32 v[50:51], v[50:51], v[170:171], s[60:61] op_sel_hi:[1,1,0]\nv_exp_f32 v44, v44 clamp\nv_pk_fma_f32 v[18:19], v[18:19], v[2:3], v[26:27]\n"
      "v_pk_fma_f32 v[16:17], v[16:17], v[0:1], v[24:25]\nv_pk_fma_f32 v[20:21], v[20:21], v[4:5], v[28:29]\nv_exp_f32 v45, v45 clamp\nv_pk_fma_f32 v[22:23], v[22:23], v[6:7], v[30:31]\n"
      "v_pk_mul_f32 v[156:157], v[156:157], v[18:19]\nv_pk_mul_f32 v[160:161], v[160:161], v[22:23]\nv_exp_f32 v46, v46 clamp\nv_pk_fma_f32 v[156:157], v[158:159], v[16:17], v[156:157]\n"
      "v_pk_fma_f32 v[160:161], v[162:163], v[20:21], v[160:161]\nv_pk_mul_f32 v[16:17], v[16:17], v[18:19]\nv_exp_f32 v47, v47 clamp\nv_pk_mul_f32 v[20:21], v[20:21], v[22:23]\n"
      "v_pk_mul_f32 v[18:19], v[16:17], v[20:21]\nv_rcp_f32 v18, v18\nv_pk_mul_f32 v[156:157], v[156:157], v[20:21]\nv_rcp_f32 v19, v19\nv_pk_fma_f32 v[156:157], v[160:161], v[16:17], v[156:157]\n"
      "v_pk_mul_f32 v[32:33], v[156:157], v[18:19]\nv_pk_mul_f32 v[52:53], v[172:173], v[172:173]\nv_pk_mul_f32 v[54:55], v[174:175], v[174:175]\nv_exp_f32 v48, v48 clamp\n"
      "v_pk_mul_f32 v[56:57], v[176:177], v[176:177]\nv_pk_mul_f32 v[58:59], v[178:179], v[178:179]\nv_exp_f32 v49, v49 clamp\n"
      "v_pk_fma_f32 v[52:53], v[52:53], s[58:59], s[58:59] op_sel:[0,0,1] op_sel_hi:[1,0,1]\nv_pk_fma_f32 v[54:55], v[54:55], s[58:59], s[58:59] op_sel:[0,0,1] op_sel_hi:[1,0,1]\n"
      "v_pk_fma_f32 v[56:57], v[56:57], s[58:59], s[58:59] op_sel:[0,0,1] op_sel_hi:[1,0,1]\nv_exp_f32 v50, v50 clamp\n"
      "v_pk_fma_f32 v[58:59], v[58:59], s[58:59], s[58:59] op_sel:[0,0,1] op_sel_hi:[1,0,1]\nv_pk_fma_f32 v[52:53], v[52:53], v[172:173], s[60:61] op_sel_hi:[1,1,0]\n"
      "v_pk_fma_f32 v[54:55], v[54:55], v[174:175], s[60:61] op_sel_hi:[1,1,0]\nv_exp_f32 v51, v51 clamp\nv_pk_fma_f32 v[56:57], v[56:57], v[176:177], s[60:61] op_sel_hi:[1,1,0]\n"
      "v_pk_fma_f32 v[58:59], v[58:59], v[178:179], s[60:61] op_sel_hi:[1,1,0]\nv_exp_f32 v52, v52 clamp\nv_pk_fma_f32 v[46:47], v[46:47], v[10:11], v[66:67]\n"
      "v_pk_fma_f32 v[44:45], v[44:45], v[8:9], v[64:65]\nv_pk_fma_f32 v[48:49], v[48:49], v[12:13], v[68:69]\nv_exp_f32 v53, v53 clamp\nv_pk_fma_f32 v[50:51], v[50:51], v[14:15], v[70:71]\n"
      "v_pk_mul_f32 v[164:165], v[164:165], v[46:47]\nv_pk_mul_f32 v[168:169], v[168:169], v[50:51]\nv_exp_f32 v54, v54 clamp\nv_pk_fma_f32 v[164:165], v[166:167], v[44:45], v[164:165]\n"
      "v_pk_fma_f32 v[168:169], v[170:171], v[48:49], v[168:169]\nv_pk_mul_f32 v[44:45], v[44:45], v[46:47]\nv_exp_f32 v55, v55 clamp\nv_pk_mul_f32 v[48:49], v[48:49], v[50:51]\n"
      "v_pk_mul_f32 v[46:47], v[44:45], v[48:49]\nv_rcp_f32 v46, v46\nv_pk_mul_f32 v[164:165], v[164:165], v[48:49]\nv_rcp_f32 v47, v47\nv_pk_fma_f32 v[164:165], v[168:169], v[44:45], v[164:165]\n"
      "v_pk_fma_f32 v[32:33], v[164:165], v[46:47], v[32:33]\nv_add_f32 v36, v32, v33\nv_pk_mul_f32 v[16:17], v[180:181], v[180:181]\nv_pk_mul_f32 v[18:19], v[182:183], v[182:183]\n"
      "v_exp_f32 v56, v56 clamp\nv_pk_mul_f32 v[20:21], v[184:185], v[184:185]\nv_pk_mul_f32 v[22:23], v[186:187], v[186:187]\nv_exp_f32 v57, v57 clamp\n"
      "v_pk_fma_f32 v[16:17], v[16:17], s[58:59], s[58:59] op_sel:[0,0,1] op_sel_hi:[1,0,1]\nv_pk_fma_f32 v[18:19], v[18:19], s[58:59], s[58:59] op_sel:[0,0,1] op_sel_hi:[1,0,1]\n"
      "v_pk_fma_f32 v[20:21], v[20:21], s[58:59], s[58:59] op_sel:[0,0,1] op_sel_hi:[1,0,1]\nv_exp_f32 v58, v58 clamp\n"
      "v_pk_fma_f32 v[22:23], v[22:23], s[58:59], s[58:59] op_sel:[0,0,1] op_sel_hi:[1,0,1]\nv_pk_fma_f32 v[16:17], v[16:17], v[180:181], s[60:61] op_sel_hi:[1,1,0]\n"
      "v_pk_fma_f32 v[18:19], v[18:19], v[182:183], s[60:61] op_sel_hi:[1,1,0]\nv_exp_f32 v59, v59 clamp\nv_pk_fma_f32 v[20:21], v[20:21], v[184:185], s[60:61] op_sel_hi:[1,1,0]\n"
      "v_pk_fma_f32 v[22:23], v[22:23], v[186:187], s[60:61] op_sel_hi:[1,1,0]\nv_exp_f32 v16, v16 clamp\nv_pk_fma_f32 v[54:55], v[54:55], v[2:3], v[26:27]\n"
      "v_pk_fma_f32 v[52:53], v[52:53], v[0:1], v[24:25]\nv_pk_fma_f32 v[56:57], v[56:57], v[4:5], v[28:29]\nv_exp_f32 v17, v17 clamp\nv_pk_fma_f32 v[58:59], v[58:59], v[6:7], v[30:31]\n"
      "v_pk_mul_f32 v[172:173], v[172:173], v[54:55]\nv_pk_mul_f32 v[176:177], v[176:177], v[58:59]\nv_exp_f32 v18, v18 clamp\nv_pk_fma_f32 v[172:173], v[174:175], v[52:53], v[172:173]\n"
      "v_pk_fma_f32 v[176:177], v[178:179], v[56:57], v[176:177]\nv_pk_mul_f32 v[52:53], v[52:53], v[54:55]\nv_exp_f32 v19, v19 clamp\nv_pk_mul_f32 v[56:57], v[56:57], v[58:59]\n"
      "v_pk_mul_f32 v[54:55], v[52:53], v[56:57]\nv_rcp_f32 v54, v54\nv_pk_mul_f32 v[172:173], v[172:173], v[56:57]\nv_rcp_f32 v55, v55\nv_pk_fma_f32 v[172:173], v[176:177], v[52:53], v[172:173]\n"
      "v_pk_mul_f32 v[32:33], v[172:173], v[54:55]\nv_pk_mul_f32 v[44:45], v[188:189], v[188:189]\nv_pk_mul_f32 v[46:47], v[190:191], v[190:191]\nv_exp_f32 v20, v20 clamp\n"
      "v_pk_mul_f32 v[48:49], v[192:193], v[192:193]\nv_pk_mul_f32 v[50:51], v[194:195], v[194:195]\nv_exp_f32 v21, v21 clamp\n"
      "v_pk_fma_f32 v[44:45], v[44:45], s[58:59], s[58:59] op_sel:[0,0,1] op_sel_hi:[1,0,1]\nv_pk_fma_f32 v[46:47], v[46:47], s[58:59], s[58:59] op_sel:[0,0,1] op_sel_hi:[1,0,1]\n"
      "v_pk_fma_f32 v[48:49], v[48:49], s[58:59], s[58:59] op_sel:[0,0,1] op_sel_hi:[1,0,1]\nv_exp_f32 v22, v22 clamp\n"
      "v_pk_fma_f32 v[50:51], v[50:51], s[58:59], s[58:59] op_sel:[0,0,1] op_sel_hi:[1,0,1]\nv_pk_fma_f32 v[44:45], v[44:45], v[188:189], s[60:61] op_sel_hi:[1,1,0]\n"
      "v_pk_fma_f32 v[46:47], v[46:47], v[190:191], s[60:61] op_sel_hi:[1,1,0]\nv_exp_f32 v23, v23 clamp\nv_pk_fma_f32 v[48:49], v[48:49], v[192:193], s[60:61] op_sel_hi:[1,1,0]\n"
      "v_pk_fma_f32 v[50:51], v[50:51], v[194:195], s[60:61] op_sel_hi:[1,1,0]\nv_exp_f32 v44, v44 clamp\nv_pk_fma_f32 v[18:19], v[18:19], v[10:11], v[66:67]\n"
      "v_pk_fma_f32 v[16:17], v[16:17], v[8:9], v[64:65]\nv_pk_fma_f32 v[20:21], v[20:21], v[12:13], v[68:69]\nv_exp_f32 v45, v45 clamp\nv_pk_fma_f32 v[22:23], v[22:23], v[14:15], v[70:71]\n"
      "v_pk_mul_f32 v[180:181], v[180:181], v[18:19]\nv_pk_mul_f32 v[184:185], v[184:185], v[22:23]\nv_exp_f32 v46, v46 clamp\nv_pk_fma_f32 v[180:181], v[182:183], v[16:17], v[180:181]\n"
      "v_pk_fma_f32 v[184:185], v[186:187], v[20:21], v[184:185]\nv_pk_mul_f32 v[16:17], v[16:17], v[18:19]\nv_exp_f32 v47, v47 clamp\nv_pk_mul_f32 v[20:21], v[20:21], v[22:23]\n"
      "v_pk_mul_f32 v[18:19], v[16:17], v[20:21]\nv_rcp_f32 v18, v18\nv_pk_mul_f32 v[180:181], v[180:181], v[20:21]\nv_rcp_f32 v19, v19\nv_pk_fma_f32 v[180:181], v[184:185], v[16:17], v[180:181]\n"
      "v_pk_fma_f32 v[32:33], v[180:181], v[18:19], v[32:33]\nv_add_f32 v37, v32, v33\nv_pk_mul_f32 v[52:53], v[196:197], v[196:197]\nv_pk_mul_f32 v[54:55], v[198:199], v[198:199]\n"
      "v_exp_f32 v48, v48 clamp\nv_pk_mul_f32 v[56:57], v[200:201], v[200:201]\nv_pk_mul_f32 v[58:59], v[202:203], v[202:203]\nv_exp_f32 v49, v49 clamp\n"
      "v_pk_fma_f32 v[52:53], v[52:53], s[58:59], s[58:59] op_sel:[0,0,1] op_sel_hi:[1,0,1]\nv_pk_fma_f32 v[54:55], v[54:55], s[58:59], s[58:59] op_sel:[0,0,1] op_sel_hi:[1,0,1]\n"
      "v_pk_fma_f32 v[56:57], v[56:57], s[58:59], s[58:59] op_sel:[0,0,1] op_sel_hi:[1,0,1]\nv_exp_f32 v50, v50 clamp\n"
      "v_pk_fma_f32 v[58:59], v[58:59], s[58:59], s[58:59] op_sel:[0,0,1] op_sel_hi:[1,0,1]\nv_pk_fma_f32 v[52:53], v[52:53], v[196:197], s[60:61] op_sel_hi:[1,1,0]\n"
      "v_pk_fma_f32 v[54:55], v[54:55], v[198:199], s[60:61] op_sel_hi:[1,1,0]\nv_exp_f32 v51, v51 clamp\nv_pk_fma_f32 v[56:57], v[56:57], v[200:201], s[60:61] op_sel_hi:[1,1,0]\n"
      "v_pk_fma_f32 v[58:59], v[58:59], v[202:203], s[60:61] op_sel_hi:[1,1,0]\nv_exp_f32 v52, v52 clamp\nv_pk_fma_f32 v[46:47], v[46:47], v[2:3], v[26:27]\n"
      "v_pk_fma_f32 v[44:45], v[44:45], v[0:1], v[24:25]\nv_pk_fma_f32 v[48:49], v[48:49], v[4:5], v[28:29]\nv_exp_f32 v53, v53 clamp\nv_pk_fma_f32 v[50:51], v[50:51], v[6:7], v[30:31]\n"
      "v_pk_mul_f32 v[188:189], v[188:189], v[46:47]\nv_pk_mul_f32 v[192:193], v[192:193], v[50:51]\nv_exp_f32 v54, v54 clamp\nv_pk_fma_f32 v[188:189], v[190:191], v[44:45], v[188:189]\n"
      "v_pk_fma_f32 v[192:193], v[194:195], v[48:49], v[192:193]\nv_pk_mul_f32 v[44:45], v[44:45], v[46:47]\nv_exp_f32 v55, v55 clamp\nv_pk_mul_f32 v[48:49], v[48:49], v[50:51]\n"
      "v_pk_mul_f32 v[46:47], v[44:45], v[48:49]\nv_rcp_f32 v46, v46\nv_pk_mul_f32 v[188:189], v[188:189], v[48:49]\nv_rcp_f32 v47, v47\nv_pk_fma_f32 v[188:189], v[192:193], v[44:45], v[188:189]\n"
      "v_pk_mul_f32 v[32:33], v[188:189], v[46:47]\nv_pk_mul_f32 v[16:17], v[204:205], v[204:205]\nv_pk_mul_f32 v[18:19], v[206:207], v[206:207]\nv_exp_f32 v56, v56 clamp\n"
      "v_pk_mul_f32 v[20:21], v[208:209], v[208:209]\nv_pk_mul_f32 v[22:23], v[210:211], v[210:211]\nv_exp_f32 v57, v57 clamp\n"
      "v_pk_fma_f32 v[16:17], v[16:17], s[58:59], s[58:59] op_sel:[0,0,1] op_sel_hi:[1,0,1]\nv_pk_fma_f32 v[18:19], v[18:19], s[58:59], s[58:59] op_sel:[0,0,1] op_sel_hi:[1,0,1]\n"
      "v_pk_fma_f32 v[20:21], v[20:21], s[58:59], s[58:59] op_sel:[0,0,1] op_sel_hi:[1,0,1]\nv_exp_f32 v58, v58 clamp\n"
      "v_pk_fma_f32 v[22:23], v[22:23], s[58:59], s[58:59] op_sel:[0,0,1] op_sel_hi:[1,0,1]\nv_pk_fma_f32 v[16:17], v[16:17], v[204:205], s[60:61] op_sel_hi:[1,1,0]\n"
      "v_pk_fma_f32 v[18:19], v[18:19], v[206:207], s[60:61] op_sel_hi:[1,1,0]\nv_exp_f32 v59, v59 clamp\nv_pk_fma_f32 v[20:21], v[20:21], v[208:209], s[60:61] op_sel_hi:[1,1,0]\n"
      "v_pk_fma_f32 v[22:23], v[22:23], v[210:211], s[60:61] op_sel_hi:[1,1,0]\nv_exp_f32 v16, v16 clamp\nv_pk_fma_f32 v[54:55], v[54:55], v[10:11], v[66:67]\n"
      "v_pk_fma_f32 v[52:53], v[52:53], v[8:9], v[64:65]\nv_pk_fma_f32 v[56:57], v[56:57], v[12:13], v[68:69]\nv_exp_f32 v17, v17 clamp\nv_pk_fma_f32 v[58:59], v[58:59], v[14:15], v[70:71]\n"
      "v_pk_mul_f32 v[196:197], v[196:197], v[54:55]\nv_pk_mul_f32 v[200:201], v[200:201], v[58:59]\nv_exp_f32 v18, v18 clamp\nv_pk_fma_f32 v[196:197], v[198:199], v[52:53], v[196:197]\n"
      "v_pk_fma_f32 v[200:201], v[202:203], v[56:57], v[200:201]\nv_pk_mul_f32 v[52:53], v[52:53], v[54:55]\nv_exp_f32 v19, v19 clamp\nv_pk_mul_f32 v[56:57], v[56:57], v[58:59]\n"
      "v_pk_mul_f32 v[54:55], v[52:53], v[56:57]\nv_rcp_f32 v54, v54\nv_pk_mul_f32 v[196:197], v[196:197], v[56:57]\nv_rcp_f32 v55, v55\nv_pk_fma_f32 v[196:197], v[200:201], v[52:53], v[196:197]\n"
      "v_pk_fma_f32 v[32:33], v[196:197], v[54:55], v[32:33]\nv_add_f32 v38, v32, v33\nv_pk_mul_f32 v[44:45], v[212:213], v[212:213]\nv_pk_mul_f32 v[46:47], v[214:215], v[214:215]\n"
      "v_exp_f32 v20, v20 clamp\nv_pk_mul_f32 v[48:49], v[216:217], v[216:217]\nv_pk_mul_f32 v[50:51], v[218:219], v[218:219]\nv_exp_f32 v21, v21 clamp\n"
      "v_pk_fma_f32 v[44:45], v[44:45], s[58:59], s[58:59] op_sel:[0,0,1] op_sel_hi:[1,0,1]\nv_pk_fma_f32 v[46:47], v[46:47], s[58:59], s[58:59] op_sel:[0,0,1] op_sel_hi:[1,0,1]\n"
      "v_pk_fma_f32 v[48:49], v[48:49], s[58:59], s[58:59] op_sel:[0,0,1] op_sel_hi:[1,0,1]\nv_exp_f32 v22, v22 clamp\n"
      "v_pk_fma_f32 v[50:51], v[50:51], s[58:59], s[58:59] op_sel:[0,0,1] op_sel_hi:[1,0,1]\nv_pk_fma_f32 v[44:45], v[44:45], v[212:213], s[60:61] op_sel_hi:[1,1,0]\n"
      "v_pk_fma_f32 v[46:47], v[46:47], v[214:215], s[60:61] op_sel_hi:[1,1,0]\nv_exp_f32 v23, v23 clamp\nv_pk_fma_f32 v[48:49], v[48:49], v[216:217], s[60:61] op_sel_hi:[1,1,0]\n"
      "v_pk_fma_f32 v[50:51], v[50:51], v[218:219], s[60:61] op_sel_hi:[1,1,0]\nv_exp_f32 v44, v44 clamp\nv_pk_fma_f32 v[18:19], v[18:19], v[2:3], v[26:27]\n"
      "v_pk_fma_f32 v[16:17], v[16:17], v[0:1], v[24:25]\nv_pk_fma_f32 v[20:21], v[20:21], v[4:5], v[28:29]\nv_exp_f32 v45, v45 clamp\nv_pk_fma_f32 v[22:23], v[22:23], v[6:7], v[30:31]\n"
      "v_pk_mul_f32 v[204:205], v[204:205], v[18:19]\nv_pk_mul_f32 v[208:209], v[208:209], v[22:23]\nv_exp_f32 v46, v46 clamp\nv_pk_fma_f32 v[204:205], v[206:207], v[16:17], v[204:205]\n"
      "v_pk_fma_f32 v[208:209], v[210:211], v[20:21], v[208:209]\nv_pk_mul_f32 v[16:17], v[16:17], v[18:19]\nv_exp_f32 v47, v47 clamp\nv_pk_mul_f32 v[20:21], v[20:21], v[22:23]\n"
      "v_pk_mul_f32 v[18:19], v[16:17], v[20:21]\nv_rcp_f32 v18, v18\nv_pk_mul_f32 v[204:205], v[204:205], v[20:21]\nv_rcp_f32 v19, v19\nv_pk_fma_f32 v[204:205], v[208:209], v[16:17], v[204:205]\n"
      "v_pk_mul_f32 v[32:33], v[204:205], v[18:19]\nv_pk_mul_f32 v[52:53], v[220:221], v[220:221]\nv_pk_mul_f32 v[54:55], v[222:223], v[222:223]\nv_exp_f32 v48, v48 clamp\n"
      "v_pk_mul_f32 v[56:57], v[224:225], v[224:225]\nv_pk_mul_f32 v[58:59], v[226:227], v[226:227]\nv_exp_f32 v49, v49 clamp\n"
      "v_pk_fma_f32 v[52:53], v[52:53], s[58:59], s[58:59] op_sel:[0,0,1] op_sel_hi:[1,0,1]\nv_pk_fma_f32 v[54:55], v[54:55], s[58:59], s[58:59] op_sel:[0,0,1] op_sel_hi:[1,0,1]\n"
      "v_pk_fma_f32 v[56:57], v[56:57], s[58:59], s[58:59] op_sel:[0,0,1] op_sel_hi:[1,0,1]\nv_exp_f32 v50, v50 clamp\n"
      "v_pk_fma_f32 v[58:59], v[58:59], s[58:59], s[58:59] op_sel:[0,0,1] op_sel_hi:[1,0,1]\nv_pk_fma_f32 v[52:53], v[52:53], v[220:221], s[60:61] op_sel_hi:[1,1,0]\n"
      "v_pk_fma_f32 v[54:55], v[54:55], v[222:223], s[60:61] op_sel_hi:[1,1,0]\nv_exp_f32 v51, v51 clamp\nv_pk_fma_f32 v[56:57], v[56:57], v[224:225], s[60:61] op_sel_hi:[1,1,0]\n"
      "v_pk_fma_f32 v[58:59], v[58:59], v[226:227], s[60:61] op_sel_hi:[1,1,0]\nv_exp_f32 v52, v52 clamp\nv_pk_fma_f32 v[46:47], v[46:47], v[10:11], v[66:67]\n"
      "v_pk_fma_f32 v[44:45], v[44:45], v[8:9], v[64:65]\nv_pk_fma_f32 v[48:49], v[48:49], v[12:13], v[68:69]\nv_exp_f32 v53, v53 clamp\nv_pk_fma_f32 v[50:51], v[50:51], v[14:15], v[70:71]\n"
      "v_pk_mul_f32 v[212:213], v[212:213], v[46:47]\nv_pk_mul_f32 v[216:217], v[216:217], v[50:51]\nv_exp_f32 v54, v54 clamp\nv_pk_fma_f32 v[212:213], v[214:215], v[44:45], v[212:213]\n"
      "v_pk_fma_f32 v[216:217], v[218:219], v[48:49], v[216:217]\nv_pk_mul_f32 v[44:45], v[44:45], v[46:47]\nv_exp_f32 v55, v55 clamp\nv_pk_mul_f32 v[48:49], v[48:49], v[50:51]\n"
      "v_pk_mul_f32 v[46:47], v[44:45], v[48:49]\nv_rcp_f32 v46, v46\nv_pk_mul_f32 v[212:213], v[212:213], v[48:49]\nv_rcp_f32 v47, v47\nv_pk_fma_f32 v[212:213], v[216:217], v[44:45], v[212:213]\n"
      "v_pk_fma_f32 v[32:33], v[212:213], v[46:47], v[32:33]\nv_add_f32 v39, v32, v33\nv_pk_mul_f32 v[16:17], v[228:229], v[228:229]\nv_pk_mul_f32 v[18:19], v[230:231], v[230:231]\n"
      "v_exp_f32 v56, v56 clamp\nv_pk_mul_f32 v[20:21], v[232:233], v[232:233]\nv_pk_mul_f32 v[22:23], v[234:235], v[234:235]\nv_exp_f32 v57, v57 clamp\n"
      "v_pk_fma_f32 v[16:17], v[16:17], s[58:59], s[58:59] op_sel:[0,0,1] op_sel_hi:[1,0,1]\nv_pk_fma_f32 v[18:19], v[18:19], s[58:59], s[58:59] op_sel:[0,0,1] op_sel_hi:[1,0,1]\n"
      "v_pk_fma_f32 v[20:21], v[20:21], s[58:59], s[58:59] op_sel:[0,0,1] op_sel_hi:[1,0,1]\nv_exp_f32 v58, v58 clamp\n"
      "v_pk_fma_f32 v[22:23], v[22:23], s[58:59], s[58:59] op_sel:[0,0,1] op_sel_hi:[1,0,1]\nv_pk_fma_f32 v[16:17], v[16:17], v[228:229], s[60:61] op_sel_hi:[1,1,0]\n"
      "v_pk_fma_f32 v[18:19], v[18:19], v[230:231], s[60:61] op_sel_hi:[1,1,0]\nv_exp_f32 v59, v59 clamp\nv_pk_fma_f32 v[20:21], v[20:21], v[232:233], s[60:61] op_sel_hi:[1,1,0]\n"
      "v_pk_fma_f32 v[22:23], v[22:23], v[234:235], s[60:61] op_sel_hi:[1,1,0]\nv_exp_f32 v16, v16 clamp\nv_pk_fma_f32 v[54:55], v[54:55], v[2:3], v[26:27]\nv_exp_f32 v17, v17 clamp\n"
      "v_pk_fma_f32 v[52:53], v[52:53], v[0:1], v[24:25]\nv_pk_fma_f32 v[56:57], v[56:57], v[4:5], v[28:29]\nv_exp_f32 v18, v18 clamp\nv_pk_fma_f32 v[58:59], v[58:59], v[6:7], v[30:31]\n"
      "v_pk_mul_f32 v[220:221], v[220:221], v[54:55]\nv_exp_f32 v19, v19 clamp\nv_pk_mul_f32 v[224:225], v[224:225], v[58:59]\nv_pk_fma_f32 v[220:221], v[222:223], v[52:53], v[220:221]\n"
      "v_exp_f32 v20, v20 clamp\nv_pk_fma_f32 v[224:225], v[226:227], v[56:57], v[224:225]\nv_exp_f32 v21, v21 clamp\nv_pk_mul_f32 v[52:53], v[52:53], v[54:55]\n"
      "v_pk_mul_f32 v[56:57], v[56:57], v[58:59]\nv_exp_f32 v22, v22 clamp\nv_pk_mul_f32 v[54:55], v[52:53], v[56:57]\nv_rcp_f32 v54, v54\nv_pk_mul_f32 v[220:221], v[220:221], v[56:57]\n"
      "v_exp_f32 v23, v23 clamp\nv_rcp_f32 v55, v55\nv_pk_fma_f32 v[220:221], v[224:225], v[52:53], v[220:221]\nv_pk_mul_f32 v[32:33], v[220:221], v[54:55]\n"
      "v_pk_fma_f32 v[18:19], v[18:19], v[10:11], v[66:67]\nv_pk_fma_f32 v[16:17], v[16:17], v[8:9], v[64:65]\nv_pk_fma_f32 v[20:21], v[20:21], v[12:13], v[68:69]\n"
      "v_pk_fma_f32 v[22:23], v[22:23], v[14:15], v[70:71]\nv_pk_mul_f32 v[228:229], v[228:229], v[18:19]\nv_pk_mul_f32 v[232:233], v[232:233], v[22:23]\n"
      "v_pk_fma_f32 v[228:229], v[230:231], v[16:17], v[228:229]\nv_pk_fma_f32 v[232:233], v[234:235], v[20:21], v[232:233]\nv_pk_mul_f32 v[16:17], v[16:17], v[18:19]\n"
      "v_pk_mul_f32 v[20:21], v[20:21], v[22:23]\nv_pk_mul_f32 v[18:19], v[16:17], v[20:21]\nv_rcp_f32 v18, v18\nv_pk_mul_f32 v[228:229], v[228:229], v[20:21]\nv_rcp_f32 v19, v19\n"
      "v_pk_fma_f32 v[228:229], v[232:233], v[16:17], v[228:229]\nv_pk_fma_f32 v[32:33], v[228:229], v[18:19], v[32:33]\nv_add_f32 v40, v32, v33\ns_barrier\ns_branch .Lge\n"
      ".Lg13:\ns_waitcnt vmcnt(4)\ns_barrier\nds_read_b64_tr_b16 v[64:65], v104 offset:45056\nds_read_b64_tr_b16 v[66:67], v104 offset:46080\nds_read_b64_tr_b16 v[68:69], v104 offset:53248\n"
      "ds_read_b64_tr_b16 v[70:71], v104 offset:54272\nds_read_b64_tr_b16 v[72:73], v105 offset:45056\nds_read_b64_tr_b16 v[74:75], v105 offset:46080\n"
      "ds_read_b64_tr_b16 v[76:77], v105 offset:53248\nds_read_b64_tr_b16 v[78:79], v105 offset:54272\nds_read_b64_tr_b16 v[80:81], v106 offset:45056\n"
      "ds_read_b64_tr_b16 v[82:83], v106 offset:46080\nds_read_b64_tr_b16 v[84:85], v106 offset:53248\nds_read_b64_tr_b16 v[86:87], v106 offset:54272\n"
      "ds_read_b64_tr_b16 v[88:89], v107 offset:45056\nds_read_b64_tr_b16 v[90:91], v107 offset:46080\nds_read_b64_tr_b16 v[92:93], v107 offset:53248\n"
      "ds_read_b64_tr_b16 v[94:95], v107 offset:54272\nds_read_b128 v[0:3], v100 offset:45056\nds_read_b128 v[4:7], v101 offset:45056\nds_read_b128 v[8:11], v120 offset:45056\n"
      "ds_read_b128 v[12:15], v121 offset:45056\nds_read_b128 v[16:19], v96 offset:45056\ns_waitcnt lgkmcnt(1)\n.p2align 3\n"
      ".Lgl13:\nv_mfma_f32_16x16x32_bf16 v[124:127], v[64:67], v[0:3], v[124:127]\nds_read_b128 v[20:23], v97 offset:45056\nv_mfma_f32_16x16x32_bf16 v[124:127], v[68:71], v[4:7], v[124:127]\n"
      "ds_read_b128 v[24:27], v96 offset:47104\nv_mfma_f32_16x16x32_bf16 v[128:131], v[76:79], v[4:7], v[128:131]\nds_read_b128 v[28:31], v97 offset:47104\n"
      "v_mfma_f32_16x16x32_bf16 v[128:131], v[72:75], v[0:3], v[128:131]\nv_mfma_f32_16x16x32_bf16 v[152:155], v[88:91], v[8:11], v[152:155]\nds_read_b128 v[0:3], v96 offset:49152\n"
      "v_mfma_f32_16x16x32_bf16 v[152:155], v[92:95], v[12:15], v[152:155]\nds_read_b128 v[4:7], v97 offset:49152\nv_mfma_f32_16x16x32_bf16 v[148:151], v[84:87], v[12:15], v[148:151]\n"
      "v_mfma_f32_16x16x32_bf16 v[148:151], v[80:83], v[8:11], v[148:151]\nv_mfma_f32_16x16x32_bf16 v[144:147], v[72:75], v[8:11], v[144:147]\n"
      "v_mfma_f32_16x16x32_bf16 v[144:147], v[76:79], v[12:15], v[144:147]\nv_mfma_f32_16x16x32_bf16 v[140:143], v[68:71], v[12:15], v[140:143]\ns_waitcnt lgkmcnt(4)\n"
      "v_mfma_f32_16x16x32_bf16 v[140:143], v[64:67], v[8:11], v[140:143]\nv_mfma_f32_16x16x32_bf16 v[156:159], v[64:67], v[16:19], v[156:159]\nds_read_b128 v[8:11], v96 offset:51200\n"
      "v_mfma_f32_16x16x32_bf16 v[156:159], v[68:71], v[20:23], v[156:159]\nds_read_b128 v[12:15], v97 offset:51200\nv_mfma_f32_16x16x32_bf16 v[160:163], v[76:79], v[20:23], v[160:163]\n"
      "v_mfma_f32_16x16x32_bf16 v[160:163], v[72:75], v[16:19], v[160:163]\nv_mfma_f32_16x16x32_bf16 v[164:167], v[80:83], v[16:19], v[164:167]\n"
      "v_mfma_f32_16x16x32_bf16 v[164:167], v[84:87], v[20:23], v[164:167]\nv_mfma_f32_16x16x32_bf16 v[168:171], v[92:95], v[20:23], v[168:171]\ns_waitcnt lgkmcnt(2)\n"
      "v_mfma_f32_16x16x32_bf16 v[168:171], v[88:91], v[16:19], v[168:171]\nv_mfma_f32_16x16x32_bf16 v[184:187], v[88:91], v[24:27], v[184:187]\nds_read_b128 v[16:19], v96 offset:53248\n"
      "v_mfma_f32_16x16x32_bf16 v[184:187], v[92:95], v[28:31], v[184:187]\nds_read_b128 v[20:23], v97 offset:53248\nv_mfma_f32_16x16x32_bf16 v[180:183], v[84:87], v[28:31], v[180:183]\n"
      "v_mfma_f32_16x16x32_bf16 v[180:183], v[80:83], v[24:27], v[180:183]\nv_mfma_f32_16x16x32_bf16 v[176:179], v[72:75], v[24:27], v[176:179]\n"
      "v_mfma_f32_16x16x32_bf16 v[176:179], v[76:79], v[28:31], v[176:179]\ns_waitcnt vmcnt(0) lgkmcnt(0)\nv_mfma_f32_16x16x32_bf16 v[172:175], v[68:71], v[28:31], v[172:175]\ns_barrier\n"
      "v_mfma_f32_16x16x32_bf16 v[172:175], v[64:67], v[24:27], v[172:175]\nds_read_b64_tr_b16 v[32:33], v108 offset:0\nv_mfma_f32_16x16x32_bf16 v[188:191], v[64:67], v[0:3], v[188:191]\n"
      "ds_read_b64_tr_b16 v[34:35], v108 offset:1024\nv_mfma_f32_16x16x32_bf16 v[188:191], v[68:71], v[4:7], v[188:191]\nds_read_b64_tr_b16 v[36:37], v108 offset:8192\n"
      "v_mfma_f32_16x16x32_bf16 v[192:195], v[76:79], v[4:7], v[192:195]\nds_read_b64_tr_b16 v[38:39], v108 offset:9216\nv_mfma_f32_16x16x32_bf16 v[192:195], v[72:75], v[0:3], v[192:195]\n"
      "ds_read_b64_tr_b16 v[40:41], v109 offset:0\nv_mfma_f32_16x16x32_bf16 v[196:199], v[80:83], v[0:3], v[196:199]\nds_read_b64_tr_b16 v[42:43], v109 offset:1024\n"
      "v_mfma_f32_16x16x32_bf16 v[196:199], v[84:87], v[4:7], v[196:199]\nds_read_b64_tr_b16 v[44:45], v109 offset:8192\nv_mfma_f32_16x16x32_bf16 v[200:203], v[92:95], v[4:7], v[200:203]\n"
      "ds_read_b64_tr_b16 v[46:47], v109 offset:9216\nv_mfma_f32_16x16x32_bf16 v[200:203], v[88:91], v[0:3], v[200:203]\nds_read_b64_tr_b16 v[48:49], v110 offset:0\n"
      "v_mfma_f32_16x16x32_bf16 v[216:219], v[88:91], v[8:11], v[216:219]\nds_read_b128 v[0:3], v102 offset:0\nv_mfma_f32_16x16x32_bf16 v[216:219], v[92:95], v[12:15], v[216:219]\n"
      "ds_read_b128 v[4:7], v103 offset:0\nv_mfma_f32_16x16x32_bf16 v[212:215], v[84:87], v[12:15], v[212:215]\nds_read_b64_tr_b16 v[50:51], v110 offset:1024\n"
      "v_mfma_f32_16x16x32_bf16 v[212:215], v[80:83], v[8:11], v[212:215]\nds_read_b64_tr_b16 v[52:53], v110 offset:8192\nv_mfma_f32_16x16x32_bf16 v[208:211], v[72:75], v[8:11], v[208:211]\n"
      "ds_read_b64_tr_b16 v[54:55], v110 offset:9216\nv_mfma_f32_16x16x32_bf16 v[208:211], v[76:79], v[12:15], v[208:211]\nds_read_b64_tr_b16 v[56:57], v111 offset:0\n"
      "v_mfma_f32_16x16x32_bf16 v[204:207], v[68:71], v[12:15], v[204:207]\nds_read_b64_tr_b16 v[58:59], v111 offset:1024\nv_mfma_f32_16x16x32_bf16 v[204:207], v[64:67], v[8:11], v[204:207]\n"
      "ds_read_b64_tr_b16 v[60:61], v111 offset:8192\nv_mfma_f32_16x16x32_bf16 v[220:223], v[64:67], v[16:19], v[220:223]\nds_read_b128 v[8:11], v122 offset:0\n"
      "v_mfma_f32_16x16x32_bf16 v[220:223], v[68:71], v[20:23], v[220:223]\nds_read_b128 v[12:15], v123 offset:0\nv_mfma_f32_16x16x32_bf16 v[224:227], v[76:79], v[20:23], v[224:227]\n"
      "ds_read_b64_tr_b16 v[62:63], v111 offset:9216\nv_mfma_f32_16x16x32_bf16 v[224:227], v[72:75], v[16:19], v[224:227]\nv_mfma_f32_16x16x32_bf16 v[228:231], v[80:83], v[16:19], v[228:231]\n"
      "v_mfma_f32_16x16x32_bf16 v[228:231], v[84:87], v[20:23], v[228:231]\nv_mfma_f32_16x16x32_bf16 v[232:235], v[92:95], v[20:23], v[232:235]\ns_waitcnt lgkmcnt(0)\n"
      "v_mfma_f32_16x16x32_bf16 v[232:235], v[88:91], v[16:19], v[232:235]\nds_read_b128 v[16:19], v98 offset:0\nv_mfma_f32_16x16x32_bf16 v[124:127], v[32:35], v[0:3], v[124:127]\n"
      "ds_read_b128 v[20:23], v99 offset:0\nv_mfma_f32_16x16x32_bf16 v[124:127], v[36:39], v[4:7], v[124:127]\nds_read_b128 v[24:27], v98 offset:2048\n"
      "v_mfma_f32_16x16x32_bf16 v[128:131], v[44:47], v[4:7], v[128:131]\nds_read_b128 v[28:31], v99 offset:2048\nv_mfma_f32_16x16x32_bf16 v[128:131], v[40:43], v[0:3], v[128:131]\n"
      "v_mfma_f32_16x16x32_bf16 v[152:155], v[56:59], v[8:11], v[152:155]\nds_read_b128 v[0:3], v98 offset:4096\nv_mfma_f32_16x16x32_bf16 v[152:155], v[60:63], v[12:15], v[152:155]\n"
      "ds_read_b128 v[4:7], v99 offset:4096\nv_mfma_f32_16x16x32_bf16 v[148:151], v[52:55], v[12:15], v[148:151]\nv_mfma_f32_16x16x32_bf16 v[148:151], v[48:51], v[8:11], v[148:151]\n"
      "v_mfma_f32_16x16x32_bf16 v[144:147], v[40:43], v[8:11], v[144:147]\nv_mfma_f32_16x16x32_bf16 v[144:147], v[44:47], v[12:15], v[144:147]\n"
      "v_mfma_f32_16x16x32_bf16 v[140:143], v[36:39], v[12:15], v[140:143]\ns_waitcnt lgkmcnt(4)\nv_mfma_f32_16x16x32_bf16 v[140:143], v[32:35], v[8:11], v[140:143]\n"
      "v_mfma_f32_16x16x32_bf16 v[156:159], v[32:35], v[16:19], v[156:159]\nds_read_b128 v[8:11], v98 offset:6144\nv_mfma_f32_16x16x32_bf16 v[156:159], v[36:39], v[20:23], v[156:159]\n"
      "ds_read_b128 v[12:15], v99 offset:6144\nv_mfma_f32_16x16x32_bf16 v[160:163], v[44:47], v[20:23], v[160:163]\nv_mfma_f32_16x16x32_bf16 v[160:163], v[40:43], v[16:19], v[160:163]\n"
      "v_mfma_f32_16x16x32_bf16 v[164:167], v[48:51], v[16:19], v[164:167]\nv_mfma_f32_16x16x32_bf16 v[164:167], v[52:55], v[20:23], v[164:167]\n"
      "v_mfma_f32_16x16x32_bf16 v[168:171], v[60:63], v[20:23], v[168:171]\ns_waitcnt lgkmcnt(2)\nv_mfma_f32_16x16x32_bf16 v[168:171], v[56:59], v[16:19], v[168:171]\n"
      "v_mfma_f32_16x16x32_bf16 v[184:187], v[56:59], v[24:27], v[184:187]\nds_read_b128 v[16:19], v98 offset:8192\nv_mfma_f32_16x16x32_bf16 v[184:187], v[60:63], v[28:31], v[184:187]\n"
      "ds_read_b128 v[20:23], v99 offset:8192\nv_mfma_f32_16x16x32_bf16 v[180:183], v[52:55], v[28:31], v[180:183]\nv_mfma_f32_16x16x32_bf16 v[180:183], v[48:51], v[24:27], v[180:183]\n"
      "v_mfma_f32_16x16x32_bf16 v[176:179], v[40:43], v[24:27], v[176:179]\nv_mfma_f32_16x16x32_bf16 v[176:179], v[44:47], v[28:31], v[176:179]\ns_waitcnt vmcnt(0) lgkmcnt(0)\n"
      "v_mfma_f32_16x16x32_bf16 v[172:175], v[36:39], v[28:31], v[172:175]\ns_barrier\nv_mfma_f32_16x16x32_bf16 v[172:175], v[32:35], v[24:27], v[172:175]\n"
      "ds_read_b64_tr_b16 v[64:65], v104 offset:0\nv_mfma_f32_16x16x32_bf16 v[188:191], v[32:35], v[0:3], v[188:191]\nds_read_b64_tr_b16 v[66:67], v104 offset:1024\n"
      "v_mfma_f32_16x16x32_bf16 v[188:191], v[36:39], v[4:7], v[188:191]\nds_read_b64_tr_b16 v[68:69], v104 offset:8192\nv_mfma_f32_16x16x32_bf16 v[192:195], v[44:47], v[4:7], v[192:195]\n"
      "ds_read_b64_tr_b16 v[70:71], v104 offset:9216\nv_mfma_f32_16x16x32_bf16 v[192:195], v[40:43], v[0:3], v[192:195]\nds_read_b64_tr_b16 v[72:73], v105 offset:0\n"
      "v_mfma_f32_16x16x32_bf16 v[196:199], v[48:51], v[0:3], v[196:199]\nds_read_b64_tr_b16 v[74:75], v105 offset:1024\nv_mfma_f32_16x16x32_bf16 v[196:199], v[52:55], v[4:7], v[196:199]\n"
      "ds_read_b64_tr_b16 v[76:77], v105 offset:8192\nv_mfma_f32_16x16x32_bf16 v[200:203], v[60:63], v[4:7], v[200:203]\nds_read_b64_tr_b16 v[78:79], v105 offset:9216\n"
      "v_mfma_f32_16x16x32_bf16 v[200:203], v[56:59], v[0:3], v[200:203]\nds_read_b64_tr_b16 v[80:81], v106 offset:0\nv_mfma_f32_16x16x32_bf16 v[216:219], v[56:59], v[8:11], v[216:219]\n"
      "ds_read_b128 v[0:3], v100 offset:0\nv_mfma_f32_16x16x32_bf16 v[216:219], v[60:63], v[12:15], v[216:219]\nds_read_b128 v[4:7], v101 offset:0\n"
      "v_mfma_f32_16x16x32_bf16 v[212:215], v[52:55], v[12:15], v[212:215]\nds_read_b64_tr_b16 v[82:83], v106 offset:1024\nv_mfma_f32_16x16x32_bf16 v[212:215], v[48:51], v[8:11], v[212:215]\n"
      "ds_read_b64_tr_b16 v[84:85], v106 offset:8192\nv_mfma_f32_16x16x32_bf16 v[208:211], v[40:43], v[8:11], v[208:211]\nds_read_b64_tr_b16 v[86:87], v106 offset:9216\n"
      "v_mfma_f32_16x16x32_bf16 v[208:211], v[44:47], v[12:15], v[208:211]\nds_read_b64_tr_b16 v[88:89], v107 offset:0\nv_mfma_f32_16x16x32_bf16 v[204:207], v[36:39], v[12:15], v[204:207]\n"
      "ds_read_b64_tr_b16 v[90:91], v107 offset:1024\nv_mfma_f32_16x16x32_bf16 v[204:207], v[32:35], v[8:11], v[204:207]\nds_read_b64_tr_b16 v[92:93], v107 offset:8192\n"
      "v_mfma_f32_16x16x32_bf16 v[220:223], v[32:35], v[16:19], v[220:223]\nds_read_b128 v[8:11], v120 offset:0\nv_mfma_f32_16x16x32_bf16 v[220:223], v[36:39], v[20:23], v[220:223]\n"
      "ds_read_b128 v[12:15], v121 offset:0\nv_mfma_f32_16x16x32_bf16 v[224:227], v[44:47], v[20:23], v[224:227]\nds_read_b64_tr_b16 v[94:95], v107 offset:9216\n"
      "v_mfma_f32_16x16x32_bf16 v[224:227], v[40:43], v[16:19], v[224:227]\nv_mfma_f32_16x16x32_bf16 v[228:231], v[48:51], v[16:19], v[228:231]\n"
      "v_mfma_f32_16x16x32_bf16 v[228:231], v[52:55], v[20:23], v[228:231]\nv_mfma_f32_16x16x32_bf16 v[232:235], v[60:63], v[20:23], v[232:235]\ns_waitcnt lgkmcnt(0)\n"
      "v_mfma_f32_16x16x32_bf16 v[232:235], v[56:59], v[16:19], v[232:235]\nds_read_b128 v[16:19], v96 offset:0\nv_mfma_f32_16x16x32_bf16 v[124:127], v[64:67], v[0:3], v[124:127]\n"
      "ds_read_b128 v[20:23], v97 offset:0\nv_mfma_f32_16x16x32_bf16 v[124:127], v[68:71], v[4:7], v[124:127]\nds_read_b128 v[24:27], v96 offset:2048\n"
      "v_mfma_f32_16x16x32_bf16 v[128:131], v[76:79], v[4:7], v[128:131]\nds_read_b128 v[28:31], v97 offset:2048\nv_mfma_f32_16x16x32_bf16 v[128:131], v[72:75], v[0:3], v[128:131]\n"
      "v_mfma_f32_16x16x32_bf16 v[152:155], v[88:91], v[8:11], v[152:155]\nds_read_b128 v[0:3], v96 offset:4096\nv_mfma_f32_16x16x32_bf16 v[152:155], v[92:95], v[12:15], v[152:155]\n"
      "ds_read_b128 v[4:7], v97 offset:4096\nv_mfma_f32_16x16x32_bf16 v[148:151], v[84:87], v[12:15], v[148:151]\nv_mfma_f32_16x16x32_bf16 v[148:151], v[80:83], v[8:11], v[148:151]\n"
      "v_mfma_f32_16x16x32_bf16 v[144:147], v[72:75], v[8:11], v[144:147]\nv_mfma_f32_16x16x32_bf16 v[144:147], v[76:79], v[12:15], v[144:147]\n"
      "v_mfma_f32_16x16x32_bf16 v[140:143], v[68:71], v[12:15], v[140:143]\ns_waitcnt lgkmcnt(4)\nv_mfma_f32_16x16x32_bf16 v[140:143], v[64:67], v[8:11], v[140:143]\n"
      "v_mfma_f32_16x16x32_bf16 v[156:159], v[64:67], v[16:19], v[156:159]\nds_read_b128 v[8:11], v96 offset:6144\nv_mfma_f32_16x16x32_bf16 v[156:159], v[68:71], v[20:23], v[156:159]\n"
      "ds_read_b128 v[12:15], v97 offset:6144\nv_mfma_f32_16x16x32_bf16 v[160:163], v[76:79], v[20:23], v[160:163]\nv_mfma_f32_16x16x32_bf16 v[160:163], v[72:75], v[16:19], v[160:163]\n"
      "v_mfma_f32_16x16x32_bf16 v[164:167], v[80:83], v[16:19], v[164:167]\nv_mfma_f32_16x16x32_bf16 v[164:167], v[84:87], v[20:23], v[164:167]\n"
      "v_mfma_f32_16x16x32_bf16 v[168:171], v[92:95], v[20:23], v[168:171]\ns_waitcnt lgkmcnt(2)\nv_mfma_f32_16x16x32_bf16 v[168:171], v[88:91], v[16:19], v[168:171]\n"
      "v_mfma_f32_16x16x32_bf16 v[184:187], v[88:91], v[24:27], v[184:187]\nds_read_b128 v[16:19], v96 offset:8192\nv_mfma_f32_16x16x32_bf16 v[184:187], v[92:95], v[28:31], v[184:187]\n"
      "ds_read_b128 v[20:23], v97 offset:8192\nv_mfma_f32_16x16x32_bf16 v[180:183], v[84:87], v[28:31], v[180:183]\nv_mfma_f32_16x16x32_bf16 v[180:183], v[80:83], v[24:27], v[180:183]\n"
      "v_mfma_f32_16x16x32_bf16 v[176:179], v[72:75], v[24:27], v[176:179]\nv_mfma_f32_16x16x32_bf16 v[176:179], v[76:79], v[28:31], v[176:179]\ns_waitcnt vmcnt(0) lgkmcnt(0)\n"
      "v_mfma_f32_16x16x32_bf16 v[172:175], v[68:71], v[28:31], v[172:175]\ns_barrier\nv_mfma_f32_16x16x32_bf16 v[172:175], v[64:67], v[24:27], v[172:175]\n"
      "ds_read_b64_tr_b16 v[32:33], v104 offset:45056\nv_mfma_f32_16x16x32_bf16 v[188:191], v[64:67], v[0:3], v[188:191]\nds_read_b64_tr_b16 v[34:35], v104 offset:46080\n"
      "v_mfma_f32_16x16x32_bf16 v[188:191], v[68:71], v[4:7], v[188:191]\nds_read_b64_tr_b16 v[36:37], v104 offset:53248\nv_mfma_f32_16x16x32_bf16 v[192:195], v[76:79], v[4:7], v[192:195]\n"
      "ds_read_b64_tr_b16 v[38:39], v104 offset:54272\nv_mfma_f32_16x16x32_bf16 v[192:195], v[72:75], v[0:3], v[192:195]\nds_read_b64_tr_b16 v[40:41], v105 offset:45056\n"
      "v_mfma_f32_16x16x32_bf16 v[196:199], v[80:83], v[0:3], v[196:199]\nds_read_b64_tr_b16 v[42:43], v105 offset:46080\nv_mfma_f32_16x16x32_bf16 v[196:199], v[84:87], v[4:7], v[196:199]\n"
      "ds_read_b64_tr_b16 v[44:45], v105 offset:53248\nv_mfma_f32_16x16x32_bf16 v[200:203], v[92:95], v[4:7], v[200:203]\nds_read_b64_tr_b16 v[46:47], v105 offset:54272\n"
      "v_mfma_f32_16x16x32_bf16 v[200:203], v[88:91], v[0:3], v[200:203]\nds_read_b64_tr_b16 v[48:49], v106 offset:45056\nv_mfma_f32_16x16x32_bf16 v[216:219], v[88:91], v[8:11], v[216:219]\n"
      "ds_read_b128 v[0:3], v100 offset:45056\nv_mfma_f32_16x16x32_bf16 v[216:219], v[92:95], v[12:15], v[216:219]\nds_read_b128 v[4:7], v101 offset:45056\n"
      "v_mfma_f32_16x16x32_bf16 v[212:215], v[84:87], v[12:15], v[212:215]\nds_read_b64_tr_b16 v[50:51], v106 offset:46080\nv_mfma_f32_16x16x32_bf16 v[212:215], v[80:83], v[8:11], v[212:215]\n"
      "ds_read_b64_tr_b16 v[52:53], v106 offset:53248\nv_mfma_f32_16x16x32_bf16 v[208:211], v[72:75], v[8:11], v[208:211]\nds_read_b64_tr_b16 v[54:55], v106 offset:54272\n"
      "v_mfma_f32_16x16x32_bf16 v[208:211], v[76:79], v[12:15], v[208:211]\nds_read_b64_tr_b16 v[56:57], v107 offset:45056\nv_mfma_f32_16x16x32_bf16 v[204:207], v[68:71], v[12:15], v[204:207]\n"
      "ds_read_b64_tr_b16 v[58:59], v107 offset:46080\nv_mfma_f32_16x16x32_bf16 v[204:207], v[64:67], v[8:11], v[204:207]\nds_read_b64_tr_b16 v[60:61], v107 offset:53248\n"
      "v_mfma_f32_16x16x32_bf16 v[220:223], v[64:67], v[16:19], v[220:223]\nds_read_b128 v[8:11], v120 offset:45056\nv_mfma_f32_16x16x32_bf16 v[220:223], v[68:71], v[20:23], v[220:223]\n"
      "ds_read_b128 v[12:15], v121 offset:45056\nv_mfma_f32_16x16x32_bf16 v[224:227], v[76:79], v[20:23], v[224:227]\nds_read_b64_tr_b16 v[62:63], v107 offset:54272\n"
      "v_mfma_f32_16x16x32_bf16 v[224:227], v[72:75], v[16:19], v[224:227]\nv_mfma_f32_16x16x32_bf16 v[228:231], v[80:83], v[16:19], v[228:231]\n"
      "v_mfma_f32_16x16x32_bf16 v[228:231], v[84:87], v[20:23], v[228:231]\nv_mfma_f32_16x16x32_bf16 v[232:235], v[92:95], v[20:23], v[232:235]\ns_waitcnt lgkmcnt(0)\n"
      "v_mfma_f32_16x16x32_bf16 v[232:235], v[88:91], v[16:19], v[232:235]\nds_read_b128 v[16:19], v96 offset:45056\nv_mfma_f32_16x16x32_bf16 v[124:127], v[32:35], v[0:3], v[124:127]\n"
      "ds_read_b128 v[20:23], v97 offset:45056\nv_mfma_f32_16x16x32_bf16 v[124:127], v[36:39], v[4:7], v[124:127]\nds_read_b128 v[24:27], v96 offset:47104\n"
      "v_mfma_f32_16x16x32_bf16 v[128:131], v[44:47], v[4:7], v[128:131]\nds_read_b128 v[28:31], v97 offset:47104\nv_mfma_f32_16x16x32_bf16 v[128:131], v[40:43], v[0:3], v[128:131]\n"
      "v_mfma_f32_16x16x32_bf16 v[152:155], v[56:59], v[8:11], v[152:155]\nds_read_b128 v[0:3], v96 offset:49152\nv_mfma_f32_16x16x32_bf16 v[152:155], v[60:63], v[12:15], v[152:155]\n"
      "ds_read_b128 v[4:7], v97 offset:49152\nv_mfma_f32_16x16x32_bf16 v[148:151], v[52:55], v[12:15], v[148:151]\nv_mfma_f32_16x16x32_bf16 v[148:151], v[48:51], v[8:11], v[148:151]\n"
      "v_mfma_f32_16x16x32_bf16 v[144:147], v[40:43], v[8:11], v[144:147]\nv_mfma_f32_16x16x32_bf16 v[144:147], v[44:47], v[12:15], v[144:147]\n"
      "v_mfma_f32_16x16x32_bf16 v[140:143], v[36:39], v[12:15], v[140:143]\ns_waitcnt lgkmcnt(4)\nv_mfma_f32_16x16x32_bf16 v[140:143], v[32:35], v[8:11], v[140:143]\n"
      "v_mfma_f32_16x16x32_bf16 v[156:159], v[32:35], v[16:19], v[156:159]\nds_read_b128 v[8:11], v96 offset:51200\nv_mfma_f32_16x16x32_bf16 v[156:159], v[36:39], v[20:23], v[156:159]\n"
      "ds_read_b128 v[12:15], v97 offset:51200\nv_mfma_f32_16x16x32_bf16 v[160:163], v[44:47], v[20:23], v[160:163]\nv_mfma_f32_16x16x32_bf16 v[160:163], v[40:43], v[16:19], v[160:163]\n"
      "v_mfma_f32_16x16x32_bf16 v[164:167], v[48:51], v[16:19], v[164:167]\nv_mfma_f32_16x16x32_bf16 v[164:167], v[52:55], v[20:23], v[164:167]\n"
      "v_mfma_f32_16x16x32_bf16 v[168:171], v[60:63], v[20:23], v[168:171]\ns_waitcnt lgkmcnt(2)\nv_mfma_f32_16x16x32_bf16 v[168:171], v[56:59], v[16:19], v[168:171]\n"
      "v_mfma_f32_16x16x32_bf16 v[184:187], v[56:59], v[24:27], v[184:187]\nds_read_b128 v[16:19], v96 offset:53248\nv_mfma_f32_16x16x32_bf16 v[184:187], v[60:63], v[28:31], v[184:187]\n"
      "ds_read_b128 v[20:23], v97 offset:53248\nv_mfma_f32_16x16x32_bf16 v[180:183], v[52:55], v[28:31], v[180:183]\nv_mfma_f32_16x16x32_bf16 v[180:183], v[48:51], v[24:27], v[180:183]\n"
      "v_mfma_f32_16x16x32_bf16 v[176:179], v[40:43], v[24:27], v[176:179]\nv_mfma_f32_16x16x32_bf16 v[176:179], v[44:47], v[28:31], v[176:179]\ns_waitcnt vmcnt(0) lgkmcnt(0)\n"
      "v_mfma_f32_16x16x32_bf16 v[172:175], v[36:39], v[28:31], v[172:175]\ns_barrier\nv_mfma_f32_16x16x32_bf16 v[172:175], v[32:35], v[24:27], v[172:175]\n"
      "ds_read_b64_tr_b16 v[64:65], v108 offset:0\nv_mfma_f32_16x16x32_bf16 v[188:191], v[32:35], v[0:3], v[188:191]\nds_read_b64_tr_b16 v[66:67], v108 offset:1024\n"
      "v_mfma_f32_16x16x32_bf16 v[188:191], v[36:39], v[4:7], v[188:191]\nds_read_b64_tr_b16 v[68:69], v108 offset:8192\nv_mfma_f32_16x16x32_bf16 v[192:195], v[44:47], v[4:7], v[192:195]\n"
      "ds_read_b64_tr_b16 v[70:71], v108 offset:9216\nv_mfma_f32_16x16x32_bf16 v[192:195], v[40:43], v[0:3], v[192:195]\nds_read_b64_tr_b16 v[72:73], v109 offset:0\n"
      "v_mfma_f32_16x16x32_bf16 v[196:199], v[48:51], v[0:3], v[196:199]\nds_read_b64_tr_b16 v[74:75], v109 offset:1024\nv_mfma_f32_16x16x32_bf16 v[196:199], v[52:55], v[4:7], v[196:199]\n"
      "ds_read_b64_tr_b16 v[76:77], v109 offset:8192\nv_mfma_f32_16x16x32_bf16 v[200:203], v[60:63], v[4:7], v[200:203]\nds_read_b64_tr_b16 v[78:79], v109 offset:9216\n"
      "v_mfma_f32_16x16x32_bf16 v[200:203], v[56:59], v[0:3], v[200:203]\nds_read_b64_tr_b16 v[80:81], v110 offset:0\nv_mfma_f32_16x16x32_bf16 v[216:219], v[56:59], v[8:11], v[216:219]\n"
      "ds_read_b128 v[0:3], v102 offset:0\nv_mfma_f32_16x16x32_bf16 v[216:219], v[60:63], v[12:15], v[216:219]\nds_read_b128 v[4:7], v103 offset:0\n"
      "v_mfma_f32_16x16x32_bf16 v[212:215], v[52:55], v[12:15], v[212:215]\nds_read_b64_tr_b16 v[82:83], v110 offset:1024\nv_mfma_f32_16x16x32_bf16 v[212:215], v[48:51], v[8:11], v[212:215]\n"
      "ds_read_b64_tr_b16 v[84:85], v110 offset:8192\nv_mfma_f32_16x16x32_bf16 v[208:211], v[40:43], v[8:11], v[208:211]\nds_read_b64_tr_b16 v[86:87], v110 offset:9216\n"
      "v_mfma_f32_16x16x32_bf16 v[208:211], v[44:47], v[12:15], v[208:211]\nds_read_b64_tr_b16 v[88:89], v111 offset:0\nv_mfma_f32_16x16x32_bf16 v[204:207], v[36:39], v[12:15], v[204:207]\n"
      "ds_read_b64_tr_b16 v[90:91], v111 offset:1024\nv_mfma_f32_16x16x32_bf16 v[204:207], v[32:35], v[8:11], v[204:207]\nds_read_b64_tr_b16 v[92:93], v111 offset:8192\n"
      "v_mfma_f32_16x16x32_bf16 v[220:223], v[32:35], v[16:19], v[220:223]\nds_read_b128 v[8:11], v122 offset:0\nv_mfma_f32_16x16x32_bf16 v[220:223], v[36:39], v[20:23], v[220:223]\n"
      "ds_read_b128 v[12:15], v123 offset:0\nv_mfma_f32_16x16x32_bf16 v[224:227], v[44:47], v[20:23], v[224:227]\nds_read_b64_tr_b16 v[94:95], v111 offset:9216\n"
      "v_mfma_f32_16x16x32_bf16 v[224:227], v[40:43], v[16:19], v[224:227]\nv_mfma_f32_16x16x32_bf16 v[228:231], v[48:51], v[16:19], v[228:231]\n"
      "v_mfma_f32_16x16x32_bf16 v[228:231], v[52:55], v[20:23], v[228:231]\nv_mfma_f32_16x16x32_bf16 v[232:235], v[60:63], v[20:23], v[232:235]\ns_waitcnt lgkmcnt(0)\n"
      "v_mfma_f32_16x16x32_bf16 v[232:235], v[56:59], v[16:19], v[232:235]\nds_read_b128 v[16:19], v98 offset:0\nv_mfma_f32_16x16x32_bf16 v[124:127], v[64:67], v[0:3], v[124:127]\n"
      "ds_read_b128 v[20:23], v99 offset:0\nv_mfma_f32_16x16x32_bf16 v[124:127], v[68:71], v[4:7], v[124:127]\nds_read_b128 v[24:27], v98 offset:2048\n"
      "v_mfma_f32_16x16x32_bf16 v[128:131], v[76:79], v[4:7], v[128:131]\nds_read_b128 v[28:31], v99 offset:2048\nv_mfma_f32_16x16x32_bf16 v[128:131], v[72:75], v[0:3], v[128:131]\n"
      "v_mfma_f32_16x16x32_bf16 v[152:155], v[88:91], v[8:11], v[152:155]\nds_read_b128 v[0:3], v98 offset:4096\nv_mfma_f32_16x16x32_bf16 v[152:155], v[92:95], v[12:15], v[152:155]\n"
      "ds_read_b128 v[4:7], v99 offset:4096\nv_mfma_f32_16x16x32_bf16 v[148:151], v[84:87], v[12:15], v[148:151]\nv_mfma_f32_16x16x32_bf16 v[148:151], v[80:83], v[8:11], v[148:151]\n"
      "v_mfma_f32_16x16x32_bf16 v[144:147], v[72:75], v[8:11], v[144:147]\nv_mfma_f32_16x16x32_bf16 v[144:147], v[76:79], v[12:15], v[144:147]\n"
      "v_mfma_f32_16x16x32_bf16 v[140:143], v[68:71], v[12:15], v[140:143]\ns_waitcnt lgkmcnt(4)\nv_mfma_f32_16x16x32_bf16 v[140:143], v[64:67], v[8:11], v[140:143]\n"
      "v_mfma_f32_16x16x32_bf16 v[156:159], v[64:67], v[16:19], v[156:159]\nds_read_b128 v[8:11], v98 offset:6144\nv_mfma_f32_16x16x32_bf16 v[156:159], v[68:71], v[20:23], v[156:159]\n"
      "ds_read_b128 v[12:15], v99 offset:6144\nv_mfma_f32_16x16x32_bf16 v[160:163], v[76:79], v[20:23], v[160:163]\nv_mfma_f32_16x16x32_bf16 v[160:163], v[72:75], v[16:19], v[160:163]\n"
      "v_mfma_f32_16x16x32_bf16 v[164:167], v[80:83], v[16:19], v[164:167]\nv_mfma_f32_16x16x32_bf16 v[164:167], v[84:87], v[20:23], v[164:167]\n"
      "v_mfma_f32_16x16x32_bf16 v[168:171], v[92:95], v[20:23], v[168:171]\ns_waitcnt lgkmcnt(2)\nv_mfma_f32_16x16x32_bf16 v[168:171], v[88:91], v[16:19], v[168:171]\n"
      "v_mfma_f32_16x16x32_bf16 v[184:187], v[88:91], v[24:27], v[184:187]\nds_read_b128 v[16:19], v98 offset:8192\nv_mfma_f32_16x16x32_bf16 v[184:187], v[92:95], v[28:31], v[184:187]\n"
      "ds_read_b128 v[20:23], v99 offset:8192\nv_mfma_f32_16x16x32_bf16 v[180:183], v[84:87], v[28:31], v[180:183]\nv_mfma_f32_16x16x32_bf16 v[180:183], v[80:83], v[24:27], v[180:183]\n"
      "v_mfma_f32_16x16x32_bf16 v[176:179], v[72:75], v[24:27], v[176:179]\nv_mfma_f32_16x16x32_bf16 v[176:179], v[76:79], v[28:31], v[176:179]\ns_waitcnt vmcnt(0) lgkmcnt(0)\n"
      "v_mfma_f32_16x16x32_bf16 v[172:175], v[68:71], v[28:31], v[172:175]\ns_barrier\nv_mfma_f32_16x16x32_bf16 v[172:175], v[64:67], v[24:27], v[172:175]\n"
      "ds_read_b64_tr_b16 v[32:33], v104 offset:0\nv_mfma_f32_16x16x32_bf16 v[188:191], v[64:67], v[0:3], v[188:191]\nds_read_b64_tr_b16 v[34:35], v104 offset:1024\n"
      "v_mfma_f32_16x16x32_bf16 v[188:191], v[68:71], v[4:7], v[188:191]\nds_read_b64_tr_b16 v[36:37], v104 offset:8192\nv_mfma_f32_16x16x32_bf16 v[192:195], v[76:79], v[4:7], v[192:195]\n"
      "ds_read_b64_tr_b16 v[38:39], v104 offset:9216\nv_mfma_f32_16x16x32_bf16 v[192:195], v[72:75], v[0:3], v[192:195]\nds_read_b64_tr_b16 v[40:41], v105 offset:0\n"
      "v_mfma_f32_16x16x32_bf16 v[196:199], v[80:83], v[0:3], v[196:199]\nds_read_b64_tr_b16 v[42:43], v105 offset:1024\nv_mfma_f32_16x16x32_bf16 v[196:199], v[84:87], v[4:7], v[196:199]\n"
      "ds_read_b64_tr_b16 v[44:45], v105 offset:8192\nv_mfma_f32_16x16x32_bf16 v[200:203], v[92:95], v[4:7], v[200:203]\nds_read_b64_tr_b16 v[46:47], v105 offset:9216\n"
      "v_mfma_f32_16x16x32_bf16 v[200:203], v[88:91], v[0:3], v[200:203]\nds_read_b64_tr_b16 v[48:49], v106 offset:0\nv_mfma_f32_16x16x32_bf16 v[216:219], v[88:91], v[8:11], v[216:219]\n"
      "ds_read_b128 v[0:3], v100 offset:0\nv_mfma_f32_16x16x32_bf16 v[216:219], v[92:95], v[12:15], v[216:219]\nds_read_b128 v[4:7], v101 offset:0\n"
      "v_mfma_f32_16x16x32_bf16 v[212:215], v[84:87], v[12:15], v[212:215]\nds_read_b64_tr_b16 v[50:51], v106 offset:1024\nv_mfma_f32_16x16x32_bf16 v[212:215], v[80:83], v[8:11], v[212:215]\n"
      "ds_read_b64_tr_b16 v[52:53], v106 offset:8192\nv_mfma_f32_16x16x32_bf16 v[208:211], v[72:75], v[8:11], v[208:211]\nds_read_b64_tr_b16 v[54:55], v106 offset:9216\n"
      "v_mfma_f32_16x16x32_bf16 v[208:211], v[76:79], v[12:15], v[208:211]\nds_read_b64_tr_b16 v[56:57], v107 offset:0\nv_mfma_f32_16x16x32_bf16 v[204:207], v[68:71], v[12:15], v[204:207]\n"
      "ds_read_b64_tr_b16 v[58:59], v107 offset:1024\nv_mfma_f32_16x16x32_bf16 v[204:207], v[64:67], v[8:11], v[204:207]\nds_read_b64_tr_b16 v[60:61], v107 offset:8192\n"
      "v_mfma_f32_16x16x32_bf16 v[220:223], v[64:67], v[16:19], v[220:223]\nds_read_b128 v[8:11], v120 offset:0\nv_mfma_f32_16x16x32_bf16 v[220:223], v[68:71], v[20:23], v[220:223]\n"
      "ds_read_b128 v[12:15], v121 offset:0\nv_mfma_f32_16x16x32_bf16 v[224:227], v[76:79], v[20:23], v[224:227]\nds_read_b64_tr_b16 v[62:63], v107 offset:9216\n"
      "v_mfma_f32_16x16x32_bf16 v[224:227], v[72:75], v[16:19], v[224:227]\nv_mfma_f32_16x16x32_bf16 v[228:231], v[80:83], v[16:19], v[228:231]\n"
      "v_mfma_f32_16x16x32_bf16 v[228:231], v[84:87], v[20:23], v[228:231]\nv_mfma_f32_16x16x32_bf16 v[232:235], v[92:95], v[20:23], v[232:235]\ns_waitcnt lgkmcnt(0)\n"
      "v_mfma_f32_16x16x32_bf16 v[232:235], v[88:91], v[16:19], v[232:235]\nds_read_b128 v[16:19], v96 offset:0\nv_mfma_f32_16x16x32_bf16 v[124:127], v[32:35], v[0:3], v[124:127]\n"
      "ds_read_b128 v[20:23], v97 offset:0\nv_mfma_f32_16x16x32_bf16 v[124:127], v[36:39], v[4:7], v[124:127]\nds_read_b128 v[24:27], v96 offset:2048\n"
      "v_mfma_f32_16x16x32_bf16 v[128:131], v[44:47], v[4:7], v[128:131]\nds_read_b128 v[28:31], v97 offset:2048\nv_mfma_f32_16x16x32_bf16 v[128:131], v[40:43], v[0:3], v[128:131]\n"
      "v_mfma_f32_16x16x32_bf16 v[152:155], v[56:59], v[8:11], v[152:155]\nds_read_b128 v[0:3], v96 offset:4096\nv_mfma_f32_16x16x32_bf16 v[152:155], v[60:63], v[12:15], v[152:155]\n"
      "ds_read_b128 v[4:7], v97 offset:4096\nv_mfma_f32_16x16x32_bf16 v[148:151], v[52:55], v[12:15], v[148:151]\nv_mfma_f32_16x16x32_bf16 v[148:151], v[48:51], v[8:11], v[148:151]\n"
      "v_mfma_f32_16x16x32_bf16 v[144:147], v[40:43], v[8:11], v[144:147]\nv_mfma_f32_16x16x32_bf16 v[144:147], v[44:47], v[12:15], v[144:147]\n"
      "v_mfma_f32_16x16x32_bf16 v[140:143], v[36:39], v[12:15], v[140:143]\ns_waitcnt lgkmcnt(4)\nv_mfma_f32_16x16x32_bf16 v[140:143], v[32:35], v[8:11], v[140:143]\n"
      "v_mfma_f32_16x16x32_bf16 v[156:159], v[32:35], v[16:19], v[156:159]\nds_read_b128 v[8:11], v96 offset:6144\nv_mfma_f32_16x16x32_bf16 v[156:159], v[36:39], v[20:23], v[156:159]\n"
      "ds_read_b128 v[12:15], v97 offset:6144\nv_mfma_f32_16x16x32_bf16 v[160:163], v[44:47], v[20:23], v[160:163]\nv_mfma_f32_16x16x32_bf16 v[160:163], v[40:43], v[16:19], v[160:163]\n"
      "v_mfma_f32_16x16x32_bf16 v[164:167], v[48:51], v[16:19], v[164:167]\nv_mfma_f32_16x16x32_bf16 v[164:167], v[52:55], v[20:23], v[164:167]\n"
      "v_mfma_f32_16x16x32_bf16 v[168:171], v[60:63], v[20:23], v[168:171]\ns_waitcnt lgkmcnt(2)\nv_mfma_f32_16x16x32_bf16 v[168:171], v[56:59], v[16:19], v[168:171]\n"
      "v_mfma_f32_16x16x32_bf16 v[184:187], v[56:59], v[24:27], v[184:187]\nds_read_b128 v[16:19], v96 offset:8192\nv_mfma_f32_16x16x32_bf16 v[184:187], v[60:63], v[28:31], v[184:187]\n"
      "ds_read_b128 v[20:23], v97 offset:8192\nv_mfma_f32_16x16x32_bf16 v[180:183], v[52:55], v[28:31], v[180:183]\nv_mfma_f32_16x16x32_bf16 v[180:183], v[48:51], v[24:27], v[180:183]\n"
      "v_mfma_f32_16x16x32_bf16 v[176:179], v[40:43], v[24:27], v[176:179]\nv_mfma_f32_16x16x32_bf16 v[176:179], v[44:47], v[28:31], v[176:179]\ns_waitcnt vmcnt(0) lgkmcnt(0)\n"
      "v_mfma_f32_16x16x32_bf16 v[172:175], v[36:39], v[28:31], v[172:175]\ns_barrier\nv_mfma_f32_16x16x32_bf16 v[172:175], v[32:35], v[24:27], v[172:175]\n"
      "ds_read_b64_tr_b16 v[64:65], v104 offset:45056\nv_mfma_f32_16x16x32_bf16 v[188:191], v[32:35], v[0:3], v[188:191]\nds_read_b64_tr_b16 v[66:67], v104 offset:46080\n"
      "v_mfma_f32_16x16x32_bf16 v[188:191], v[36:39], v[4:7], v[188:191]\nds_read_b64_tr_b16 v[68:69], v104 offset:53248\nv_mfma_f32_16x16x32_bf16 v[192:195], v[44:47], v[4:7], v[192:195]\n"
      "ds_read_b64_tr_b16 v[70:71], v104 offset:54272\nv_mfma_f32_16x16x32_bf16 v[192:195], v[40:43], v[0:3], v[192:195]\nds_read_b64_tr_b16 v[72:73], v105 offset:45056\n"
      "v_mfma_f32_16x16x32_bf16 v[196:199], v[48:51], v[0:3], v[196:199]\nds_read_b64_tr_b16 v[74:75], v105 offset:46080\nv_mfma_f32_16x16x32_bf16 v[196:199], v[52:55], v[4:7], v[196:199]\n"
      "ds_read_b64_tr_b16 v[76:77], v105 offset:53248\nv_mfma_f32_16x16x32_bf16 v[200:203], v[60:63], v[4:7], v[200:203]\nds_read_b64_tr_b16 v[78:79], v105 offset:54272\n"
      "v_mfma_f32_16x16x32_bf16 v[200:203], v[56:59], v[0:3], v[200:203]\nds_read_b64_tr_b16 v[80:81], v106 offset:45056\nv_mfma_f32_16x16x32_bf16 v[216:219], v[56:59], v[8:11], v[216:219]\n"
      "ds_read_b128 v[0:3], v100 offset:45056\nv_mfma_f32_16x16x32_bf16 v[216:219], v[60:63], v[12:15], v[216:219]\nds_read_b128 v[4:7], v101 offset:45056\n"
      "v_mfma_f32_16x16x32_bf16 v[212:215], v[52:55], v[12:15], v[212:215]\nds_read_b64_tr_b16 v[82:83], v106 offset:46080\nv_mfma_f32_16x16x32_bf16 v[212:215], v[48:51], v[8:11], v[212:215]\n"
      "ds_read_b64_tr_b16 v[84:85], v106 offset:53248\nv_mfma_f32_16x16x32_bf16 v[208:211], v[40:43], v[8:11], v[208:211]\nds_read_b64_tr_b16 v[86:87], v106 offset:54272\n"
      "v_mfma_f32_16x16x32_bf16 v[208:211], v[44:47], v[12:15], v[208:211]\nds_read_b64_tr_b16 v[88:89], v107 offset:45056\nv_mfma_f32_16x16x32_bf16 v[204:207], v[36:39], v[12:15], v[204:207]\n"
      "ds_read_b64_tr_b16 v[90:91], v107 offset:46080\nv_mfma_f32_16x16x32_bf16 v[204:207], v[32:35], v[8:11], v[204:207]\nds_read_b64_tr_b16 v[92:93], v107 offset:53248\n"
      "v_mfma_f32_16x16x32_bf16 v[220:223], v[32:35], v[16:19], v[220:223]\nds_read_b128 v[8:11], v120 offset:45056\nv_mfma_f32_16x16x32_bf16 v[220:223], v[36:39], v[20:23], v[220:223]\n"
      "ds_read_b128 v[12:15], v121 offset:45056\nv_mfma_f32_16x16x32_bf16 v[224:227], v[44:47], v[20:23], v[224:227]\nds_read_b64_tr_b16 v[94:95], v107 offset:54272\n"
      "v_mfma_f32_16x16x32_bf16 v[224:227], v[40:43], v[16:19], v[224:227]\nv_mfma_f32_16x16x32_bf16 v[228:231], v[48:51], v[16:19], v[228:231]\ns_sub_u32 s58, s58, 1\ns_cmp_lg_u32 s58, 0\n"
      "v_mfma_f32_16x16x32_bf16 v[228:231], v[52:55], v[20:23], v[228:231]\nv_mfma_f32_16x16x32_bf16 v[232:235], v[60:63], v[20:23], v[232:235]\ns_waitcnt lgkmcnt(0)\n"
      "v_mfma_f32_16x16x32_bf16 v[232:235], v[56:59], v[16:19], v[232:235]\nds_read_b128 v[16:19], v96 offset:45056\ns_cbranch_scc1 .Lgl13\n"
      "v_mfma_f32_16x16x32_bf16 v[124:127], v[64:67], v[0:3], v[124:127]\nds_read_b128 v[20:23], v97 offset:45056\nv_cmp_eq_u32 s[62:63], 0, v240\n"
      "v_mfma_f32_16x16x32_bf16 v[124:127], v[68:71], v[4:7], v[124:127]\nds_read_b128 v[24:27], v96 offset:47104\nv_mov_b32 v253, 262144\n"
      "v_mfma_f32_16x16x32_bf16 v[128:131], v[76:79], v[4:7], v[128:131]\nds_read_b128 v[28:31], v97 offset:47104\nv_mov_b32 v248, 162048\n"
      "v_mfma_f32_16x16x32_bf16 v[128:131], v[72:75], v[0:3], v[128:131]\nv_mov_b32 v241, 1\nv_cndmask_b32 v253, v253, v248, s[62:63]\n"
      "v_mfma_f32_16x16x32_bf16 v[152:155], v[88:91], v[8:11], v[152:155]\nds_read_b128 v[0:3], v96 offset:49152\nv_lshrrev_b32 v112, 4, v240\n"
      "v_mfma_f32_16x16x32_bf16 v[152:155], v[92:95], v[12:15], v[152:155]\nds_read_b128 v[4:7], v97 offset:49152\nv_bfe_u32 v113, v240, 2, 2\n"
      "v_mfma_f32_16x16x32_bf16 v[148:151], v[84:87], v[12:15], v[148:151]\nv_and_b32 v114, 3, v240\nv_mul_u32_u24 v114, 0x80008, v114\n"
      "v_mfma_f32_16x16x32_bf16 v[148:151], v[80:83], v[8:11], v[148:151]\nv_add_u32 v244, v112, v113\nv_and_b32 v244, 3, v244\nv_mfma_f32_16x16x32_bf16 v[144:147], v[72:75], v[8:11], v[144:147]\n"
      "v_add_u32 v116, 22, v113\nv_cmp_gt_u32 s[62:63], 2, v113\nv_mfma_f32_16x16x32_bf16 v[144:147], v[76:79], v[12:15], v[144:147]\nv_and_b32 v113, 60, v240\ns_mov_b32 s42, 0\n"
      "v_mfma_f32_16x16x32_bf16 v[140:143], v[68:71], v[12:15], v[140:143]\ns_waitcnt lgkmcnt(4)\nv_cndmask_b32 v116, v116, v244, s[62:63]\n"
      "v_mfma_f32_16x16x32_bf16 v[140:143], v[64:67], v[8:11], v[140:143]\nv_lshl_or_b32 v244, v244, 16, v116\nv_lshl_add_u32 v244, v244, 5, v114\n"
      "v_mfma_f32_16x16x32_bf16 v[156:159], v[64:67], v[16:19], v[156:159]\nds_read_b128 v[8:11], v96 offset:51200\nv_lshl_add_u32 v252, v240, 4, s42\n"
      "v_mfma_f32_16x16x32_bf16 v[156:159], v[68:71], v[20:23], v[156:159]\nds_read_b128 v[12:15], v97 offset:51200\ns_mov_b32 s42, 0x76325410\n"
      "v_mfma_f32_16x16x32_bf16 v[160:163], v[76:79], v[20:23], v[160:163]\ns_mov_b32 s43, 0x21005410\nv_lshrrev_b64 v[116:117], v113, s[42:43]\n"
      "v_mfma_f32_16x16x32_bf16 v[160:163], v[72:75], v[16:19], v[160:163]\nv_and_b32 v116, 7, v116\nv_lshl_add_u32 v116, v112, 3, v116\n"
      "v_mfma_f32_16x16x32_bf16 v[164:167], v[80:83], v[16:19], v[164:167]\ns_mov_b32 s42, 0x54107632\ns_mov_b32 s43, 0x21107632\n"
      "v_mfma_f32_16x16x32_bf16 v[164:167], v[84:87], v[20:23], v[164:167]\nv_lshrrev_b64 v[118:119], v113, s[42:43]\nv_and_b32 v118, 7, v118\n"
      "v_mfma_f32_16x16x32_bf16 v[168:171], v[92:95], v[20:23], v[168:171]\ns_waitcnt lgkmcnt(2)\nv_lshl_add_u32 v118, v112, 3, v118\n"
      "v_mfma_f32_16x16x32_bf16 v[168:171], v[88:91], v[16:19], v[168:171]\nv_lshl_or_b32 v245, v118, 16, v116\nv_lshl_add_u32 v245, v245, 5, v114\n"
      "v_mfma_f32_16x16x32_bf16 v[184:187], v[88:91], v[24:27], v[184:187]\nds_read_b128 v[16:19], v96 offset:53248\nv_and_b32 v113, 1, v240\n"
      "v_mfma_f32_16x16x32_bf16 v[184:187], v[92:95], v[28:31], v[184:187]\nds_read_b128 v[20:23], v97 offset:53248\nv_mul_u32_u24 v113, 0xaaaa, v113\n"
      "v_mfma_f32_16x16x32_bf16 v[180:183], v[84:87], v[28:31], v[180:183]\nv_add_u32 v242, 0x4444, v113\ns_lshl_b32 s42, s36, 4\n"
      "v_mfma_f32_16x16x32_bf16 v[180:183], v[80:83], v[24:27], v[180:183]\ns_add_u32 s42, s42, 160640\nv_lshl_add_u32 v243, v112, 2, s42\n"
      "v_mfma_f32_16x16x32_bf16 v[176:179], v[72:75], v[24:27], v[176:179]\nv_and_b32 v112, 1, v112\nv_lshl_add_u32 v112, v112, 3, s24\n"
      "v_mfma_f32_16x16x32_bf16 v[176:179], v[76:79], v[28:31], v[176:179]\ns_waitcnt vmcnt(0) lgkmcnt(0)\ns_sub_u32 s42, s64, 1\n"
      "v_mfma_f32_16x16x32_bf16 v[172:175], v[68:71], v[28:31], v[172:175]\ns_barrier\nv_min_u32 v112, s42, v112\nv_mfma_f32_16x16x32_bf16 v[172:175], v[64:67], v[24:27], v[172:175]\n"
      "ds_read_b64_tr_b16 v[32:33], v108 offset:0\nv_lshlrev_b32 v112, 3, v112\nv_mfma_f32_16x16x32_bf16 v[188:191], v[64:67], v[0:3], v[188:191]\nds_read_b64_tr_b16 v[34:35], v108 offset:1024\n"
      "v_and_b32 v113, 15, v240\nv_mfma_f32_16x16x32_bf16 v[188:191], v[68:71], v[4:7], v[188:191]\nds_read_b64_tr_b16 v[36:37], v108 offset:8192\nv_lshl_add_u32 v250, v112, 1, v113\n"
      "v_mfma_f32_16x16x32_bf16 v[192:195], v[76:79], v[4:7], v[192:195]\nds_read_b64_tr_b16 v[38:39], v108 offset:9216\ns_mov_b32 s42, 136192\n"
      "v_mfma_f32_16x16x32_bf16 v[192:195], v[72:75], v[0:3], v[192:195]\nds_read_b64_tr_b16 v[40:41], v109 offset:0\nv_lshl_add_u32 v250, v250, 2, s42\n"
      "v_mfma_f32_16x16x32_bf16 v[196:199], v[80:83], v[0:3], v[196:199]\nds_read_b64_tr_b16 v[42:43], v109 offset:1024\nv_min_u32 v113, 12, v113\n"
      "v_mfma_f32_16x16x32_bf16 v[196:199], v[84:87], v[4:7], v[196:199]\nds_read_b64_tr_b16 v[44:45], v109 offset:8192\ns_lshl_b32 s42, s30, 7\n"
      "v_mfma_f32_16x16x32_bf16 v[200:203], v[92:95], v[4:7], v[200:203]\nds_read_b64_tr_b16 v[46:47], v109 offset:9216\nv_lshl_add_u32 v113, v113, 15, s42\n"
      "v_mfma_f32_16x16x32_bf16 v[200:203], v[88:91], v[0:3], v[200:203]\nds_read_b64_tr_b16 v[48:49], v110 offset:0\nv_add_u32 v249, v113, v112\n"
      "v_mfma_f32_16x16x32_bf16 v[216:219], v[88:91], v[8:11], v[216:219]\nds_read_b128 v[0:3], v102 offset:0\nv_mfma_f32_16x16x32_bf16 v[216:219], v[92:95], v[12:15], v[216:219]\n"
      "ds_read_b128 v[4:7], v103 offset:0\nv_mfma_f32_16x16x32_bf16 v[212:215], v[84:87], v[12:15], v[212:215]\nds_read_b64_tr_b16 v[50:51], v110 offset:1024\n"
      "v_mfma_f32_16x16x32_bf16 v[212:215], v[80:83], v[8:11], v[212:215]\nds_read_b64_tr_b16 v[52:53], v110 offset:8192\nv_mfma_f32_16x16x32_bf16 v[208:211], v[72:75], v[8:11], v[208:211]\n"
      "ds_read_b64_tr_b16 v[54:55], v110 offset:9216\nv_mfma_f32_16x16x32_bf16 v[208:211], v[76:79], v[12:15], v[208:211]\nds_read_b64_tr_b16 v[56:57], v111 offset:0\n"
      "v_mfma_f32_16x16x32_bf16 v[204:207], v[68:71], v[12:15], v[204:207]\nds_read_b64_tr_b16 v[58:59], v111 offset:1024\nv_mfma_f32_16x16x32_bf16 v[204:207], v[64:67], v[8:11], v[204:207]\n"
      "ds_read_b64_tr_b16 v[60:61], v111 offset:8192\nv_mfma_f32_16x16x32_bf16 v[220:223], v[64:67], v[16:19], v[220:223]\nds_read_b128 v[8:11], v122 offset:0\n"
      "v_mfma_f32_16x16x32_bf16 v[220:223], v[68:71], v[20:23], v[220:223]\nds_read_b128 v[12:15], v123 offset:0\nv_mfma_f32_16x16x32_bf16 v[224:227], v[76:79], v[20:23], v[224:227]\n"
      "ds_read_b64_tr_b16 v[62:63], v111 offset:9216\nv_mfma_f32_16x16x32_bf16 v[224:227], v[72:75], v[16:19], v[224:227]\nv_mfma_f32_16x16x32_bf16 v[228:231], v[80:83], v[16:19], v[228:231]\n"
      "v_mfma_f32_16x16x32_bf16 v[228:231], v[84:87], v[20:23], v[228:231]\nv_mfma_f32_16x16x32_bf16 v[232:235], v[92:95], v[20:23], v[232:235]\ns_waitcnt lgkmcnt(0)\n"
      "v_mfma_f32_16x16x32_bf16 v[232:235], v[88:91], v[16:19], v[232:235]\nds_read_b128 v[16:19], v98 offset:0\nv_mfma_f32_16x16x32_bf16 v[124:127], v[32:35], v[0:3], v[124:127]\n"
      "ds_read_b128 v[20:23], v99 offset:0\nv_mfma_f32_16x16x32_bf16 v[124:127], v[36:39], v[4:7], v[124:127]\nds_read_b128 v[24:27], v98 offset:2048\n"
      "v_mfma_f32_16x16x32_bf16 v[128:131], v[44:47], v[4:7], v[128:131]\nds_read_b128 v[28:31], v99 offset:2048\nv_mfma_f32_16x16x32_bf16 v[128:131], v[40:43], v[0:3], v[128:131]\n"
      "v_mfma_f32_16x16x32_bf16 v[152:155], v[56:59], v[8:11], v[152:155]\nds_read_b128 v[0:3], v98 offset:4096\nv_mfma_f32_16x16x32_bf16 v[152:155], v[60:63], v[12:15], v[152:155]\n"
      "ds_read_b128 v[4:7], v99 offset:4096\nv_mfma_f32_16x16x32_bf16 v[148:151], v[52:55], v[12:15], v[148:151]\nv_mfma_f32_16x16x32_bf16 v[148:151], v[48:51], v[8:11], v[148:151]\n"
      "v_mfma_f32_16x16x32_bf16 v[144:147], v[40:43], v[8:11], v[144:147]\nv_mfma_f32_16x16x32_bf16 v[144:147], v[44:47], v[12:15], v[144:147]\n"
      "v_mfma_f32_16x16x32_bf16 v[140:143], v[36:39], v[12:15], v[140:143]\ns_waitcnt lgkmcnt(4)\nv_mfma_f32_16x16x32_bf16 v[140:143], v[32:35], v[8:11], v[140:143]\n"
      "v_mfma_f32_16x16x32_bf16 v[156:159], v[32:35], v[16:19], v[156:159]\nds_read_b128 v[8:11], v98 offset:6144\nv_mfma_f32_16x16x32_bf16 v[156:159], v[36:39], v[20:23], v[156:159]\n"
      "ds_read_b128 v[12:15], v99 offset:6144\nv_mfma_f32_16x16x32_bf16 v[160:163], v[44:47], v[20:23], v[160:163]\nv_mfma_f32_16x16x32_bf16 v[160:163], v[40:43], v[16:19], v[160:163]\n"
      "v_mfma_f32_16x16x32_bf16 v[164:167], v[48:51], v[16:19], v[164:167]\nv_mfma_f32_16x16x32_bf16 v[164:167], v[52:55], v[20:23], v[164:167]\n"
      "v_mfma_f32_16x16x32_bf16 v[168:171], v[60:63], v[20:23], v[168:171]\ns_waitcnt lgkmcnt(2)\nv_mfma_f32_16x16x32_bf16 v[168:171], v[56:59], v[16:19], v[168:171]\n"
      "v_mfma_f32_16x16x32_bf16 v[184:187], v[56:59], v[24:27], v[184:187]\nds_read_b128 v[16:19], v98 offset:8192\nv_mfma_f32_16x16x32_bf16 v[184:187], v[60:63], v[28:31], v[184:187]\n"
      "ds_read_b128 v[20:23], v99 offset:8192\nv_mfma_f32_16x16x32_bf16 v[180:183], v[52:55], v[28:31], v[180:183]\nv_mfma_f32_16x16x32_bf16 v[180:183], v[48:51], v[24:27], v[180:183]\n"
      "v_mfma_f32_16x16x32_bf16 v[176:179], v[40:43], v[24:27], v[176:179]\nv_mfma_f32_16x16x32_bf16 v[176:179], v[44:47], v[28:31], v[176:179]\ns_waitcnt vmcnt(0) lgkmcnt(0)\n"
      "v_mfma_f32_16x16x32_bf16 v[172:175], v[36:39], v[28:31], v[172:175]\ns_barrier\nv_mfma_f32_16x16x32_bf16 v[172:175], v[32:35], v[24:27], v[172:175]\nds_read_b128 v[72:75], v252 offset:0\n"
      "v_lshl_add_u32 v246, v240, 1, v248\nv_mfma_f32_16x16x32_bf16 v[188:191], v[32:35], v[0:3], v[188:191]\nds_read_b128 v[24:27], v236 offset:512\nds_read_u16 v251, v246 offset:64\n"
      "v_mfma_f32_16x16x32_bf16 v[188:191], v[36:39], v[4:7], v[188:191]\nds_read_b128 v[28:31], v236 offset:576\nds_read_b32 v247, v248 offset:24\n"
      "v_mfma_f32_16x16x32_bf16 v[192:195], v[44:47], v[4:7], v[192:195]\nds_read_b128 v[76:79], v252 offset:1024\nv_and_b32 v246, 48, v240\n"
      "v_mfma_f32_16x16x32_bf16 v[192:195], v[40:43], v[0:3], v[192:195]\nds_read_b128 v[80:83], v252 offset:2048\ns_mov_b32 s42, 42240\n"
      "v_mfma_f32_16x16x32_bf16 v[196:199], v[48:51], v[0:3], v[196:199]\nds_read_b128 v[84:87], v252 offset:3072\nv_lshl_add_u32 v246, v246, 2, s42\n"
      "v_mfma_f32_16x16x32_bf16 v[196:199], v[52:55], v[4:7], v[196:199]\nds_read_b128 v[88:91], v252 offset:4096\ns_mov_b32 s58, 0xbdd2d3e7\n"
      "v_mfma_f32_16x16x32_bf16 v[200:203], v[60:63], v[4:7], v[200:203]\nds_read_b128 v[92:95], v252 offset:5120\ns_waitcnt lgkmcnt(4)\n"
      "v_mfma_f32_16x16x32_bf16 v[200:203], v[56:59], v[0:3], v[200:203]\nds_read_b128 v[96:99], v252 offset:6144\nv_readfirstlane_b32 s39, v247\n"
      "v_mfma_f32_16x16x32_bf16 v[216:219], v[56:59], v[8:11], v[216:219]\nds_read_b128 v[0:3], v236 offset:0\nv_readlane_b32 s55, v251, 0\n"
      "v_mfma_f32_16x16x32_bf16 v[216:219], v[60:63], v[12:15], v[216:219]\nds_read_b128 v[4:7], v236 offset:64\ns_setprio 3\nv_mfma_f32_16x16x32_bf16 v[212:215], v[52:55], v[12:15], v[212:215]\n"
      "ds_read_b128 v[100:103], v252 offset:7168\ns_xor_b32 s40, s39, 0xa5c30f96\nv_mfma_f32_16x16x32_bf16 v[212:215], v[48:51], v[8:11], v[212:215]\nds_read_b128 v[104:107], v252 offset:8192\n"
      "s_mul_i32 s41, s39, 0x9E3779B1\nv_mfma_f32_16x16x32_bf16 v[208:211], v[40:43], v[8:11], v[208:211]\nds_read_b128 v[108:111], v252 offset:9216\ns_lshr_b32 s41, s41, 24\n"
      "v_mfma_f32_16x16x32_bf16 v[208:211], v[44:47], v[12:15], v[208:211]\nds_read_b128 v[64:67], v237 offset:512\nv_mfma_f32_16x16x32_bf16 v[204:207], v[36:39], v[12:15], v[204:207]\n"
      "ds_read_b128 v[68:71], v237 offset:576\nv_mfma_f32_16x16x32_bf16 v[204:207], v[32:35], v[8:11], v[204:207]\nds_read_b128 v[112:115], v252 offset:10240\n"
      "v_mfma_f32_16x16x32_bf16 v[220:223], v[32:35], v[16:19], v[220:223]\nds_read_b128 v[8:11], v237 offset:0\nv_mfma_f32_16x16x32_bf16 v[220:223], v[36:39], v[20:23], v[220:223]\n"
      "ds_read_b128 v[12:15], v237 offset:64\nv_mfma_f32_16x16x32_bf16 v[224:227], v[44:47], v[20:23], v[224:227]\nds_read_b128 v[116:119], v252 offset:11264\n"
      "v_mfma_f32_16x16x32_bf16 v[224:227], v[40:43], v[16:19], v[224:227]\nds_read_b128 v[120:123], v252 offset:12288\n"
      "v_add_u32_sdwa v239, s49, v245 dst_sel:DWORD dst_unused:UNUSED_PAD src0_sel:DWORD src1_sel:WORD_1\nv_mfma_f32_16x16x32_bf16 v[228:231], v[48:51], v[16:19], v[228:231]\n"
      "ds_read_b128 v[132:135], v252 offset:15360\nv_add_u32_sdwa v236, s49, v244 dst_sel:DWORD dst_unused:UNUSED_PAD src0_sel:DWORD src1_sel:WORD_0\n"
      "v_mfma_f32_16x16x32_bf16 v[228:231], v[52:55], v[20:23], v[228:231]\nds_read_b128 v[136:139], v252 offset:16384\n"
      "v_add_u32_sdwa v237, s49, v244 dst_sel:DWORD dst_unused:UNUSED_PAD src0_sel:DWORD src1_sel:WORD_1\nv_mfma_f32_16x16x32_bf16 v[232:235], v[60:63], v[20:23], v[232:235]\n"
      "v_add_u32_sdwa v238, s49, v245 dst_sel:DWORD dst_unused:UNUSED_PAD src0_sel:DWORD src1_sel:WORD_0\ns_waitcnt lgkmcnt(0)\nv_mfma_f32_16x16x32_bf16 v[232:235], v[56:59], v[16:19], v[232:235]\n"
      "v_pk_mul_f32 v[44:45], v[124:125], v[124:125]\nv_pk_mul_f32 v[46:47], v[126:127], v[126:127]\nv_pk_mul_f32 v[48:49], v[128:129], v[128:129]\nv_pk_mul_f32 v[50:51], v[130:131], v[130:131]\n"
      "v_pk_fma_f32 v[44:45], v[44:45], s[58:59], s[58:59] op_sel:[0,0,1] op_sel_hi:[1,0,1]\nv_pk_fma_f32 v[46:47], v[46:47], s[58:59], s[58:59] op_sel:[0,0,1] op_sel_hi:[1,0,1]\n"
      "v_pk_fma_f32 v[48:49], v[48:49], s[58:59], s[58:59] op_sel:[0,0,1] op_sel_hi:[1,0,1]\nv_pk_fma_f32 v[50:51], v[50:51], s[58:59], s[58:59] op_sel:[0,0,1] op_sel_hi:[1,0,1]\n"
      "v_pk_fma_f32 v[44:45], v[44:45], v[124:125], s[60:61] op_sel_hi:[1,1,0]\nv_pk_fma_f32 v[46:47], v[46:47], v[126:127], s[60:61] op_sel_hi:[1,1,0]\n"
      "v_pk_fma_f32 v[48:49], v[48:49], v[128:129], s[60:61] op_sel_hi:[1,1,0]\nv_pk_fma_f32 v[50:51], v[50:51], v[130:131], s[60:61] op_sel_hi:[1,1,0]\nv_exp_f32 v44, v44 clamp\n"
      "v_pk_mul_f32 v[52:53], v[140:141], v[140:141]\nv_exp_f32 v45, v45 clamp\nv_pk_mul_f32 v[54:55], v[142:143], v[142:143]\nv_pk_mul_f32 v[56:57], v[144:145], v[144:145]\n"
      "v_exp_f32 v46, v46 clamp\nv_pk_mul_f32 v[58:59], v[146:147], v[146:147]\nv_exp_f32 v47, v47 clamp\nv_pk_fma_f32 v[52:53], v[52:53], s[58:59], s[58:59] op_sel:[0,0,1] op_sel_hi:[1,0,1]\n"
      "v_pk_fma_f32 v[54:55], v[54:55], s[58:59], s[58:59] op_sel:[0,0,1] op_sel_hi:[1,0,1]\nv_exp_f32 v48, v48 clamp\n"
      "v_pk_fma_f32 v[56:57], v[56:57], s[58:59], s[58:59] op_sel:[0,0,1] op_sel_hi:[1,0,1]\nv_exp_f32 v49, v49 clamp\n"
      "v_pk_fma_f32 v[58:59], v[58:59], s[58:59], s[58:59] op_sel:[0,0,1] op_sel_hi:[1,0,1]\nv_pk_fma_f32 v[52:53], v[52:53], v[140:141], s[60:61] op_sel_hi:[1,1,0]\nv_exp_f32 v50, v50 clamp\n"
      "v_pk_fma_f32 v[54:55], v[54:55], v[142:143], s[60:61] op_sel_hi:[1,1,0]\nv_exp_f32 v51, v51 clamp\nv_pk_fma_f32 v[56:57], v[56:57], v[144:145], s[60:61] op_sel_hi:[1,1,0]\n"
      "v_pk_fma_f32 v[58:59], v[58:59], v[146:147], s[60:61] op_sel_hi:[1,1,0]\nv_exp_f32 v52, v52 clamp\nv_pk_fma_f32 v[46:47], v[46:47], v[2:3], v[26:27]\n"
      "v_pk_fma_f32 v[44:45], v[44:45], v[0:1], v[24:25]\nv_pk_fma_f32 v[48:49], v[48:49], v[4:5], v[28:29]\nv_exp_f32 v53, v53 clamp\nv_pk_fma_f32 v[50:51], v[50:51], v[6:7], v[30:31]\n"
      "v_pk_mul_f32 v[124:125], v[124:125], v[46:47]\nv_pk_mul_f32 v[128:129], v[128:129], v[50:51]\nv_exp_f32 v54, v54 clamp\nv_pk_fma_f32 v[124:125], v[126:127], v[44:45], v[124:125]\n"
      "v_pk_fma_f32 v[128:129], v[130:131], v[48:49], v[128:129]\nv_pk_mul_f32 v[44:45], v[44:45], v[46:47]\nv_exp_f32 v55, v55 clamp\nv_pk_mul_f32 v[48:49], v[48:49], v[50:51]\n"
      "v_pk_mul_f32 v[46:47], v[44:45], v[48:49]\nv_rcp_f32 v46, v46\nv_pk_mul_f32 v[124:125], v[124:125], v[48:49]\nv_rcp_f32 v47, v47\nv_pk_fma_f32 v[124:125], v[128:129], v[44:45], v[124:125]\n"
      "v_pk_mul_f32 v[32:33], v[124:125], v[46:47]\nv_add_f32 v42, v32, v33\nv_pk_mul_f32 v[16:17], v[148:149], v[148:149]\nv_pk_mul_f32 v[18:19], v[150:151], v[150:151]\nv_exp_f32 v56, v56 clamp\n"
      "v_pk_mul_f32 v[20:21], v[152:153], v[152:153]\nv_pk_mul_f32 v[22:23], v[154:155], v[154:155]\nv_exp_f32 v57, v57 clamp\n"
      "v_pk_fma_f32 v[16:17], v[16:17], s[58:59], s[58:59] op_sel:[0,0,1] op_sel_hi:[1,0,1]\nv_pk_fma_f32 v[18:19], v[18:19], s[58:59], s[58:59] op_sel:[0,0,1] op_sel_hi:[1,0,1]\n"
      "v_pk_fma_f32 v[20:21], v[20:21], s[58:59], s[58:59] op_sel:[0,0,1] op_sel_hi:[1,0,1]\nv_exp_f32 v58, v58 clamp\n"
      "v_pk_fma_f32 v[22:23], v[22:23], s[58:59], s[58:59] op_sel:[0,0,1] op_sel_hi:[1,0,1]\nv_pk_fma_f32 v[16:17], v[16:17], v[148:149], s[60:61] op_sel_hi:[1,1,0]\n"
      "v_pk_fma_f32 v[18:19], v[18:19], v[150:151], s[60:61] op_sel_hi:[1,1,0]\nv_exp_f32 v59, v59 clamp\nv_pk_fma_f32 v[20:21], v[20:21], v[152:153], s[60:61] op_sel_hi:[1,1,0]\n"
      "v_pk_fma_f32 v[22:23], v[22:23], v[154:155], s[60:61] op_sel_hi:[1,1,0]\nv_exp_f32 v16, v16 clamp\nv_pk_fma_f32 v[54:55], v[54:55], v[2:3], v[26:27]\n"
      "v_pk_fma_f32 v[52:53], v[52:53], v[0:1], v[24:25]\nv_pk_fma_f32 v[56:57], v[56:57], v[4:5], v[28:29]\nv_exp_f32 v17, v17 clamp\nv_pk_fma_f32 v[58:59], v[58:59], v[6:7], v[30:31]\n"
      "v_pk_mul_f32 v[140:141], v[140:141], v[54:55]\nv_pk_mul_f32 v[144:145], v[144:145], v[58:59]\nv_exp_f32 v18, v18 clamp\nv_pk_fma_f32 v[140:141], v[142:143], v[52:53], v[140:141]\n"
      "v_pk_fma_f32 v[144:145], v[146:147], v[56:57], v[144:145]\nv_pk_mul_f32 v[52:53], v[52:53], v[54:55]\nv_exp_f32 v19, v19 clamp\nv_pk_mul_f32 v[56:57], v[56:57], v[58:59]\n"
      "v_pk_mul_f32 v[54:55], v[52:53], v[56:57]\nv_rcp_f32 v54, v54\nv_pk_mul_f32 v[140:141], v[140:141], v[56:57]\nv_rcp_f32 v55, v55\nv_pk_fma_f32 v[140:141], v[144:145], v[52:53], v[140:141]\n"
      "v_pk_mul_f32 v[32:33], v[140:141], v[54:55]\nv_pk_mul_f32 v[44:45], v[156:157], v[156:157]\nv_pk_mul_f32 v[46:47], v[158:159], v[158:159]\nv_exp_f32 v20, v20 clamp\n"
      "v_pk_mul_f32 v[48:49], v[160:161], v[160:161]\nv_pk_mul_f32 v[50:51], v[162:163], v[162:163]\nv_exp_f32 v21, v21 clamp\n"
      "v_pk_fma_f32 v[44:45], v[44:45], s[58:59], s[58:59] op_sel:[0,0,1] op_sel_hi:[1,0,1]\nv_pk_fma_f32 v[46:47], v[46:47], s[58:59], s[58:59] op_sel:[0,0,1] op_sel_hi:[1,0,1]\n"
      "v_pk_fma_f32 v[48:49], v[48:49], s[58:59], s[58:59] op_sel:[0,0,1] op_sel_hi:[1,0,1]\nv_exp_f32 v22, v22 clamp\n"
      "v_pk_fma_f32 v[50:51], v[50:51], s[58:59], s[58:59] op_sel:[0,0,1] op_sel_hi:[1,0,1]\nv_pk_fma_f32 v[44:45], v[44:45], v[156:157], s[60:61] op_sel_hi:[1,1,0]\n"
      "v_pk_fma_f32 v[46:47], v[46:47], v[158:159], s[60:61] op_sel_hi:[1,1,0]\nv_exp_f32 v23, v23 clamp\nv_pk_fma_f32 v[48:49], v[48:49], v[160:161], s[60:61] op_sel_hi:[1,1,0]\n"
      "v_pk_fma_f32 v[50:51], v[50:51], v[162:163], s[60:61] op_sel_hi:[1,1,0]\nv_exp_f32 v44, v44 clamp\nv_pk_fma_f32 v[18:19], v[18:19], v[10:11], v[66:67]\n"
      "v_pk_fma_f32 v[16:17], v[16:17], v[8:9], v[64:65]\nv_pk_fma_f32 v[20:21], v[20:21], v[12:13], v[68:69]\nv_exp_f32 v45, v45 clamp\nv_pk_fma_f32 v[22:23], v[22:23], v[14:15], v[70:71]\n"
      "v_pk_mul_f32 v[148:149], v[148:149], v[18:19]\nv_pk_mul_f32 v[152:153], v[152:153], v[22:23]\nv_exp_f32 v46, v46 clamp\nv_pk_fma_f32 v[148:149], v[150:151], v[16:17], v[148:149]\n"
      "v_pk_fma_f32 v[152:153], v[154:155], v[20:21], v[152:153]\nv_pk_mul_f32 v[16:17], v[16:17], v[18:19]\nv_exp_f32 v47, v47 clamp\nv_pk_mul_f32 v[20:21], v[20:21], v[22:23]\n"
      "v_pk_mul_f32 v[18:19], v[16:17], v[20:21]\nv_rcp_f32 v18, v18\nv_pk_mul_f32 v[148:149], v[148:149], v[20:21]\nv_rcp_f32 v19, v19\nv_pk_fma_f32 v[148:149], v[152:153], v[16:17], v[148:149]\n"
      "v_pk_fma_f32 v[32:33], v[148:149], v[18:19], v[32:33]\nv_add_f32 v41, v32, v33\nv_pk_mul_f32 v[52:53], v[164:165], v[164:165]\nv_pk_mul_f32 v[54:55], v[166:167], v[166:167]\n"
      "v_exp_f32 v48, v48 clamp\nv_pk_mul_f32 v[56:57], v[168:169], v[168:169]\nv_pk_mul_f32 v[58:59], v[170:171], v[170:171]\nv_exp_f32 v49, v49 clamp\n"
      "v_pk_fma_f32 v[52:53], v[52:53], s[58:59], s[58:59] op_sel:[0,0,1] op_sel_hi:[1,0,1]\nv_pk_fma_f32 v[54:55], v[54:55], s[58:59], s[58:59] op_sel:[0,0,1] op_sel_hi:[1,0,1]\n"
      "v_pk_fma_f32 v[56:57], v[56:57], s[58:59], s[58:59] op_sel:[0,0,1] op_sel_hi:[1,0,1]\nv_exp_f32 v50, v50 clamp\n"
      "v_pk_fma_f32 v[58:59], v[58:59], s[58:59], s[58:59] op_sel:[0,0,1] op_sel_hi:[1,0,1]\nv_pk_fma_f32 v[52:53], v[52:53], v[164:165], s[60:61] op_sel_hi:[1,1,0]\n"
      "v_pk_fma_f32 v[54:55], v[54:55], v[166:167], s[60:61] op_sel_hi:[1,1,0]\nv_exp_f32 v51, v51 clamp\nv_pk_fma_f32 v[56:57], v[56:57], v[168:169], s[60:61] op_sel_hi:[1,1,0]\n"
      "v_pk_fma_f32 v[58:59], v[58:59], v[170:171], s[60:61] op_sel_hi:[1,1,0]\nv_exp_f32 v52, v52 clamp\nv_pk_fma_f32 v[46:47], v[46:47], v[2:3], v[26:27]\n"
      "v_pk_fma_f32 v[44:45], v[44:45], v[0:1], v[24:25]\nv_pk_fma_f32 v[48:49], v[48:49], v[4:5], v[28:29]\nv_exp_f32 v53, v53 clamp\nv_pk_fma_f32 v[50:51], v[50:51], v[6:7], v[30:31]\n"
      "v_pk_mul_f32 v[156:157], v[156:157], v[46:47]\nv_pk_mul_f32 v[160:161], v[160:161], v[50:51]\nv_exp_f32 v54, v54 clamp\nv_pk_fma_f32 v[156:157], v[158:159], v[44:45], v[156:157]\n"
      "v_pk_fma_f32 v[160:161], v[162:163], v[48:49], v[160:161]\nv_pk_mul_f32 v[44:45], v[44:45], v[46:47]\nv_exp_f32 v55, v55 clamp\nv_pk_mul_f32 v[48:49], v[48:49], v[50:51]\n"
      "v_pk_mul_f32 v[46:47], v[44:45], v[48:49]\nv_rcp_f32 v46, v46\nv_pk_mul_f32 v[156:157], v[156:157], v[48:49]\nv_rcp_f32 v47, v47\nv_pk_fma_f32 v[156:157], v[160:161], v[44:45], v[156:157]\n"
      "v_pk_mul_f32 v[32:33], v[156:157], v[46:47]\nv_pk_mul_f32 v[16:17], v[172:173], v[172:173]\nv_pk_mul_f32 v[18:19], v[174:175], v[174:175]\nv_exp_f32 v56, v56 clamp\n"
      "v_pk_mul_f32 v[20:21], v[176:177], v[176:177]\nv_pk_mul_f32 v[22:23], v[178:179], v[178:179]\nv_exp_f32 v57, v57 clamp\n"
      "v_pk_fma_f32 v[16:17], v[16:17], s[58:59], s[58:59] op_sel:[0,0,1] op_sel_hi:[1,0,1]\nv_pk_fma_f32 v[18:19], v[18:19], s[58:59], s[58:59] op_sel:[0,0,1] op_sel_hi:[1,0,1]\n"
      "v_pk_fma_f32 v[20:21], v[20:21], s[58:59], s[58:59] op_sel:[0,0,1] op_sel_hi:[1,0,1]\nv_exp_f32 v58, v58 clamp\n"
      "v_pk_fma_f32 v[22:23], v[22:23], s[58:59], s[58:59] op_sel:[0,0,1] op_sel_hi:[1,0,1]\nv_pk_fma_f32 v[16:17], v[16:17], v[172:173], s[60:61] op_sel_hi:[1,1,0]\n"
      "v_pk_fma_f32 v[18:19], v[18:19], v[174:175], s[60:61] op_sel_hi:[1,1,0]\nv_exp_f32 v59, v59 clamp\nv_pk_fma_f32 v[20:21], v[20:21], v[176:177], s[60:61] op_sel_hi:[1,1,0]\n"
      "v_pk_fma_f32 v[22:23], v[22:23], v[178:179], s[60:61] op_sel_hi:[1,1,0]\nv_exp_f32 v16, v16 clamp\nv_pk_fma_f32 v[54:55], v[54:55], v[10:11], v[66:67]\n"
      "v_pk_fma_f32 v[52:53], v[52:53], v[8:9], v[64:65]\nv_pk_fma_f32 v[56:57], v[56:57], v[12:13], v[68:69]\nv_exp_f32 v17, v17 clamp\nv_pk_fma_f32 v[58:59], v[58:59], v[14:15], v[70:71]\n"
      "v_pk_mul_f32 v[164:165], v[164:165], v[54:55]\nv_pk_mul_f32 v[168:169], v[168:169], v[58:59]\nv_exp_f32 v18, v18 clamp\nv_pk_fma_f32 v[164:165], v[166:167], v[52:53], v[164:165]\n"
      "v_pk_fma_f32 v[168:169], v[170:171], v[56:57], v[168:169]\nv_pk_mul_f32 v[52:53], v[52:53], v[54:55]\nv_exp_f32 v19, v19 clamp\nv_pk_mul_f32 v[56:57], v[56:57], v[58:59]\n"
      "v_pk_mul_f32 v[54:55], v[52:53], v[56:57]\nv_rcp_f32 v54, v54\nv_pk_mul_f32 v[164:165], v[164:165], v[56:57]\nv_rcp_f32 v55, v55\nv_pk_fma_f32 v[164:165], v[168:169], v[52:53], v[164:165]\n"
      "v_pk_fma_f32 v[32:33], v[164:165], v[54:55], v[32:33]\nv_add_f32 v36, v32, v33\nv_pk_mul_f32 v[44:45], v[180:181], v[180:181]\nv_pk_mul_f32 v[46:47], v[182:183], v[182:183]\n"
      "v_exp_f32 v20, v20 clamp\nv_pk_mul_f32 v[48:49], v[184:185], v[184:185]\nv_pk_mul_f32 v[50:51], v[186:187], v[186:187]\nv_exp_f32 v21, v21 clamp\n"
      "v_pk_fma_f32 v[44:45], v[44:45], s[58:59], s[58:59] op_sel:[0,0,1] op_sel_hi:[1,0,1]\nv_pk_fma_f32 v[46:47], v[46:47], s[58:59], s[58:59] op_sel:[0,0,1] op_sel_hi:[1,0,1]\n"
      "v_pk_fma_f32 v[48:49], v[48:49], s[58:59], s[58:59] op_sel:[0,0,1] op_sel_hi:[1,0,1]\nv_exp_f32 v22, v22 clamp\n"
      "v_pk_fma_f32 v[50:51], v[50:51], s[58:59], s[58:59] op_sel:[0,0,1] op_sel_hi:[1,0,1]\nv_pk_fma_f32 v[44:45], v[44:45], v[180:181], s[60:61] op_sel_hi:[1,1,0]\n"
      "v_pk_fma_f32 v[46:47], v[46:47], v[182:183], s[60:61] op_sel_hi:[1,1,0]\nv_exp_f32 v23, v23 clamp\nv_pk_fma_f32 v[48:49], v[48:49], v[184:185], s[60:61] op_sel_hi:[1,1,0]\n"
      "v_pk_fma_f32 v[50:51], v[50:51], v[186:187], s[60:61] op_sel_hi:[1,1,0]\nv_exp_f32 v44, v44 clamp\nv_pk_fma_f32 v[18:19], v[18:19], v[2:3], v[26:27]\n"
      "v_pk_fma_f32 v[16:17], v[16:17], v[0:1], v[24:25]\nv_pk_fma_f32 v[20:21], v[20:21], v[4:5], v[28:29]\nv_exp_f32 v45, v45 clamp\nv_pk_fma_f32 v[22:23], v[22:23], v[6:7], v[30:31]\n"
      "v_pk_mul_f32 v[172:173], v[172:173], v[18:19]\nv_pk_mul_f32 v[176:177], v[176:177], v[22:23]\nv_exp_f32 v46, v46 clamp\nv_pk_fma_f32 v[172:173], v[174:175], v[16:17], v[172:173]\n"
      "v_pk_fma_f32 v[176:177], v[178:179], v[20:21], v[176:177]\nv_pk_mul_f32 v[16:17], v[16:17], v[18:19]\nv_exp_f32 v47, v47 clamp\nv_pk_mul_f32 v[20:21], v[20:21], v[22:23]\n"
      "v_pk_mul_f32 v[18:19], v[16:17], v[20:21]\nv_rcp_f32 v18, v18\nv_pk_mul_f32 v[172:173], v[172:173], v[20:21]\nv_rcp_f32 v19, v19\nv_pk_fma_f32 v[172:173], v[176:177], v[16:17], v[172:173]\n"
      "v_pk_mul_f32 v[32:33], v[172:173], v[18:19]\nv_pk_mul_f32 v[52:53], v[188:189], v[188:189]\nv_pk_mul_f32 v[54:55], v[190:191], v[190:191]\nv_exp_f32 v48, v48 clamp\n"
      "v_pk_mul_f32 v[56:57], v[192:193], v[192:193]\nv_pk_mul_f32 v[58:59], v[194:195], v[194:195]\nv_exp_f32 v49, v49 clamp\n"
      "v_pk_fma_f32 v[52:53], v[52:53], s[58:59], s[58:59] op_sel:[0,0,1] op_sel_hi:[1,0,1]\nv_pk_fma_f32 v[54:55], v[54:55], s[58:59], s[58:59] op_sel:[0,0,1] op_sel_hi:[1,0,1]\n"
      "v_pk_fma_f32 v[56:57], v[56:57], s[58:59], s[58:59] op_sel:[0,0,1] op_sel_hi:[1,0,1]\nv_exp_f32 v50, v50 clamp\n"
      "v_pk_fma_f32 v[58:59], v[58:59], s[58:59], s[58:59] op_sel:[0,0,1] op_sel_hi:[1,0,1]\nv_pk_fma_f32 v[52:53], v[52:53], v[188:189], s[60:61] op_sel_hi:[1,1,0]\n"
      "v_pk_fma_f32 v[54:55], v[54:55], v[190:191], s[60:61] op_sel_hi:[1,1,0]\nv_exp_f32 v51, v51 clamp\nv_pk_fma_f32 v[56:57], v[56:57], v[192:193], s[60:61] op_sel_hi:[1,1,0]\n"
      "v_pk_fma_f32 v[58:59], v[58:59], v[194:195], s[60:61] op_sel_hi:[1,1,0]\nv_exp_f32 v52, v52 clamp\nv_pk_fma_f32 v[46:47], v[46:47], v[10:11], v[66:67]\n"
      "v_pk_fma_f32 v[44:45], v[44:45], v[8:9], v[64:65]\nv_pk_fma_f32 v[48:49], v[48:49], v[12:13], v[68:69]\nv_exp_f32 v53, v53 clamp\nv_pk_fma_f32 v[50:51], v[50:51], v[14:15], v[70:71]\n"
      "v_pk_mul_f32 v[180:181], v[180:181], v[46:47]\nv_pk_mul_f32 v[184:185], v[184:185], v[50:51]\nv_exp_f32 v54, v54 clamp\nv_pk_fma_f32 v[180:181], v[182:183], v[44:45], v[180:181]\n"
      "v_pk_fma_f32 v[184:185], v[186:187], v[48:49], v[184:185]\nv_pk_mul_f32 v[44:45], v[44:45], v[46:47]\nv_exp_f32 v55, v55 clamp\nv_pk_mul_f32 v[48:49], v[48:49], v[50:51]\n"
      "v_pk_mul_f32 v[46:47], v[44:45], v[48:49]\nv_rcp_f32 v46, v46\nv_pk_mul_f32 v[180:181], v[180:181], v[48:49]\nv_rcp_f32 v47, v47\nv_pk_fma_f32 v[180:181], v[184:185], v[44:45], v[180:181]\n"
      "v_pk_fma_f32 v[32:33], v[180:181], v[46:47], v[32:33]\nv_add_f32 v37, v32, v33\nv_pk_mul_f32 v[16:17], v[196:197], v[196:197]\nv_pk_mul_f32 v[18:19], v[198:199], v[198:199]\n"
      "v_exp_f32 v56, v56 clamp\nv_pk_mul_f32 v[20:21], v[200:201], v[200:201]\nv_pk_mul_f32 v[22:23], v[202:203], v[202:203]\nv_exp_f32 v57, v57 clamp\n"
      "v_pk_fma_f32 v[16:17], v[16:17], s[58:59], s[58:59] op_sel:[0,0,1] op_sel_hi:[1,0,1]\nv_pk_fma_f32 v[18:19], v[18:19], s[58:59], s[58:59] op_sel:[0,0,1] op_sel_hi:[1,0,1]\n"
      "v_pk_fma_f32 v[20:21], v[20:21], s[58:59], s[58:59] op_sel:[0,0,1] op_sel_hi:[1,0,1]\nv_exp_f32 v58, v58 clamp\n"
      "v_pk_fma_f32 v[22:23], v[22:23], s[58:59], s[58:59] op_sel:[0,0,1] op_sel_hi:[1,0,1]\nv_pk_fma_f32 v[16:17], v[16:17], v[196:197], s[60:61] op_sel_hi:[1,1,0]\n"
      "v_pk_fma_f32 v[18:19], v[18:19], v[198:199], s[60:61] op_sel_hi:[1,1,0]\nv_exp_f32 v59, v59 clamp\nv_pk_fma_f32 v[20:21], v[20:21], v[200:201], s[60:61] op_sel_hi:[1,1,0]\n"
      "v_pk_fma_f32 v[22:23], v[22:23], v[202:203], s[60:61] op_sel_hi:[1,1,0]\nv_exp_f32 v16, v16 clamp\nv_pk_fma_f32 v[54:55], v[54:55], v[2:3], v[26:27]\n"
      "v_pk_fma_f32 v[52:53], v[52:53], v[0:1], v[24:25]\nv_pk_fma_f32 v[56:57], v[56:57], v[4:5], v[28:29]\nv_exp_f32 v17, v17 clamp\nv_pk_fma_f32 v[58:59], v[58:59], v[6:7], v[30:31]\n"
      "v_pk_mul_f32 v[188:189], v[188:189], v[54:55]\nv_pk_mul_f32 v[192:193], v[192:193], v[58:59]\nv_exp_f32 v18, v18 clamp\nv_pk_fma_f32 v[188:189], v[190:191], v[52:53], v[188:189]\n"
      "v_pk_fma_f32 v[192:193], v[194:195], v[56:57], v[192:193]\nv_pk_mul_f32 v[52:53], v[52:53], v[54:55]\nv_exp_f32 v19, v19 clamp\nv_pk_mul_f32 v[56:57], v[56:57], v[58:59]\n"
      "v_pk_mul_f32 v[54:55], v[52:53], v[56:57]\nv_rcp_f32 v54, v54\nv_pk_mul_f32 v[188:189], v[188:189], v[56:57]\nv_rcp_f32 v55, v55\nv_pk_fma_f32 v[188:189], v[192:193], v[52:53], v[188:189]\n"
      "v_pk_mul_f32 v[32:33], v[188:189], v[54:55]\nv_pk_mul_f32 v[44:45], v[204:205], v[204:205]\nv_pk_mul_f32 v[46:47], v[206:207], v[206:207]\nv_exp_f32 v20, v20 clamp\n"
      "v_pk_mul_f32 v[48:49], v[208:209], v[208:209]\nv_pk_mul_f32 v[50:51], v[210:211], v[210:211]\nv_exp_f32 v21, v21 clamp\n"
      "v_pk_fma_f32 v[44:45], v[44:45], s[58:59], s[58:59] op_sel:[0,0,1] op_sel_hi:[1,0,1]\nv_pk_fma_f32 v[46:47], v[46:47], s[58:59], s[58:59] op_sel:[0,0,1] op_sel_hi:[1,0,1]\n"
      "v_pk_fma_f32 v[48:49], v[48:49], s[58:59], s[58:59] op_sel:[0,0,1] op_sel_hi:[1,0,1]\nv_exp_f32 v22, v22 clamp\n"
      "v_pk_fma_f32 v[50:51], v[50:51], s[58:59], s[58:59] op_sel:[0,0,1] op_sel_hi:[1,0,1]\nv_pk_fma_f32 v[44:45], v[44:45], v[204:205], s[60:61] op_sel_hi:[1,1,0]\n"
      "v_pk_fma_f32 v[46:47], v[46:47], v[206:207], s[60:61] op_sel_hi:[1,1,0]\nv_exp_f32 v23, v23 clamp\nv_pk_fma_f32 v[48:49], v[48:49], v[208:209], s[60:61] op_sel_hi:[1,1,0]\n"
      "v_pk_fma_f32 v[50:51], v[50:51], v[210:211], s[60:61] op_sel_hi:[1,1,0]\nv_exp_f32 v44, v44 clamp\nv_pk_fma_f32 v[18:19], v[18:19], v[10:11], v[66:67]\n"
      "v_pk_fma_f32 v[16:17], v[16:17], v[8:9], v[64:65]\nv_pk_fma_f32 v[20:21], v[20:21], v[12:13], v[68:69]\nv_exp_f32 v45, v45 clamp\nv_pk_fma_f32 v[22:23], v[22:23], v[14:15], v[70:71]\n"
      "v_pk_mul_f32 v[196:197], v[196:197], v[18:19]\nv_pk_mul_f32 v[200:201], v[200:201], v[22:23]\nv_exp_f32 v46, v46 clamp\nv_pk_fma_f32 v[196:197], v[198:199], v[16:17], v[196:197]\n"
      "v_pk_fma_f32 v[200:201], v[202:203], v[20:21], v[200:201]\nv_pk_mul_f32 v[16:17], v[16:17], v[18:19]\nv_exp_f32 v47, v47 clamp\nv_pk_mul_f32 v[20:21], v[20:21], v[22:23]\n"
      "v_pk_mul_f32 v[18:19], v[16:17], v[20:21]\nv_rcp_f32 v18, v18\nv_pk_mul_f32 v[196:197], v[196:197], v[20:21]\nv_rcp_f32 v19, v19\nv_pk_fma_f32 v[196:197], v[200:201], v[16:17], v[196:197]\n"
      "v_pk_fma_f32 v[32:33], v[196:197], v[18:19], v[32:33]\nv_add_f32 v38, v32, v33\nv_pk_mul_f32 v[52:53], v[212:213], v[212:213]\nv_pk_mul_f32 v[54:55], v[214:215], v[214:215]\n"
      "v_exp_f32 v48, v48 clamp\nv_pk_mul_f32 v[56:57], v[216:217], v[216:217]\nv_pk_mul_f32 v[58:59], v[218:219], v[218:219]\nv_exp_f32 v49, v49 clamp\n"
      "v_pk_fma_f32 v[52:53], v[52:53], s[58:59], s[58:59] op_sel:[0,0,1] op_sel_hi:[1,0,1]\nv_pk_fma_f32 v[54:55], v[54:55], s[58:59], s[58:59] op_sel:[0,0,1] op_sel_hi:[1,0,1]\n"
      "v_pk_fma_f32 v[56:57], v[56:57], s[58:59], s[58:59] op_sel:[0,0,1] op_sel_hi:[1,0,1]\nv_exp_f32 v50, v50 clamp\n"
      "v_pk_fma_f32 v[58:59], v[58:59], s[58:59], s[58:59] op_sel:[0,0,1] op_sel_hi:[1,0,1]\nv_pk_fma_f32 v[52:53], v[52:53], v[212:213], s[60:61] op_sel_hi:[1,1,0]\n"
      "v_pk_fma_f32 v[54:55], v[54:55], v[214:215], s[60:61] op_sel_hi:[1,1,0]\nv_exp_f32 v51, v51 clamp\nv_pk_fma_f32 v[56:57], v[56:57], v[216:217], s[60:61] op_sel_hi:[1,1,0]\n"
      "v_pk_fma_f32 v[58:59], v[58:59], v[218:219], s[60:61] op_sel_hi:[1,1,0]\nv_exp_f32 v52, v52 clamp\nv_pk_fma_f32 v[46:47], v[46:47], v[2:3], v[26:27]\n"
      "v_pk_fma_f32 v[44:45], v[44:45], v[0:1], v[24:25]\nv_pk_fma_f32 v[48:49], v[48:49], v[4:5], v[28:29]\nv_exp_f32 v53, v53 clamp\nv_pk_fma_f32 v[50:51], v[50:51], v[6:7], v[30:31]\n"
      "v_pk_mul_f32 v[204:205], v[204:205], v[46:47]\nv_pk_mul_f32 v[208:209], v[208:209], v[50:51]\nv_exp_f32 v54, v54 clamp\nv_pk_fma_f32 v[204:205], v[206:207], v[44:45], v[204:205]\n"
      "v_pk_fma_f32 v[208:209], v[210:211], v[48:49], v[208:209]\nv_pk_mul_f32 v[44:45], v[44:45], v[46:47]\nv_exp_f32 v55, v55 clamp\nv_pk_mul_f32 v[48:49], v[48:49], v[50:51]\n"
      "v_pk_mul_f32 v[46:47], v[44:45], v[48:49]\nv_rcp_f32 v46, v46\nv_pk_mul_f32 v[204:205], v[204:205], v[48:49]\nv_rcp_f32 v47, v47\nv_pk_fma_f32 v[204:205], v[208:209], v[44:45], v[204:205]\n"
      "v_pk_mul_f32 v[32:33], v[204:205], v[46:47]\nv_pk_mul_f32 v[16:17], v[220:221], v[220:221]\nv_pk_mul_f32 v[18:19], v[222:223], v[222:223]\nv_exp_f32 v56, v56 clamp\n"
      "v_pk_mul_f32 v[20:21], v[224:225], v[224:225]\nv_pk_mul_f32 v[22:23], v[226:227], v[226:227]\nv_exp_f32 v57, v57 clamp\n"
      "v_pk_fma_f32 v[16:17], v[16:17], s[58:59], s[58:59] op_sel:[0,0,1] op_sel_hi:[1,0,1]\nv_pk_fma_f32 v[18:19], v[18:19], s[58:59], s[58:59] op_sel:[0,0,1] op_sel_hi:[1,0,1]\n"
      "v_pk_fma_f32 v[20:21], v[20:21], s[58:59], s[58:59] op_sel:[0,0,1] op_sel_hi:[1,0,1]\nv_exp_f32 v58, v58 clamp\n"
      "v_pk_fma_f32 v[22:23], v[22:23], s[58:59], s[58:59] op_sel:[0,0,1] op_sel_hi:[1,0,1]\nv_pk_fma_f32 v[16:17], v[16:17], v[220:221], s[60:61] op_sel_hi:[1,1,0]\n"
      "v_pk_fma_f32 v[18:19], v[18:19], v[222:223], s[60:61] op_sel_hi:[1,1,0]\nv_exp_f32 v59, v59 clamp\nv_pk_fma_f32 v[20:21], v[20:21], v[224:225], s[60:61] op_sel_hi:[1,1,0]\n"
      "v_pk_fma_f32 v[22:23], v[22:23], v[226:227], s[60:61] op_sel_hi:[1,1,0]\nv_exp_f32 v16, v16 clamp\nv_pk_fma_f32 v[54:55], v[54:55], v[10:11], v[66:67]\n"
      "v_pk_fma_f32 v[52:53], v[52:53], v[8:9], v[64:65]\nv_pk_fma_f32 v[56:57], v[56:57], v[12:13], v[68:69]\nv_exp_f32 v17, v17 clamp\nv_pk_fma_f32 v[58:59], v[58:59], v[14:15], v[70:71]\n"
      "v_pk_mul_f32 v[212:213], v[212:213], v[54:55]\nv_pk_mul_f32 v[216:217], v[216:217], v[58:59]\nv_exp_f32 v18, v18 clamp\nv_pk_fma_f32 v[212:213], v[214:215], v[52:53], v[212:213]\n"
      "v_pk_fma_f32 v[216:217], v[218:219], v[56:57], v[216:217]\nv_pk_mul_f32 v[52:53], v[52:53], v[54:55]\nv_exp_f32 v19, v19 clamp\nv_pk_mul_f32 v[56:57], v[56:57], v[58:59]\n"
      "v_pk_mul_f32 v[54:55], v[52:53], v[56:57]\nv_rcp_f32 v54, v54\nv_pk_mul_f32 v[212:213], v[212:213], v[56:57]\nv_rcp_f32 v55, v55\nv_pk_fma_f32 v[212:213], v[216:217], v[52:53], v[212:213]\n"
      "v_pk_fma_f32 v[32:33], v[212:213], v[54:55], v[32:33]\nv_add_f32 v39, v32, v33\nv_pk_mul_f32 v[44:45], v[228:229], v[228:229]\nv_pk_mul_f32 v[46:47], v[230:231], v[230:231]\n"
      "v_exp_f32 v20, v20 clamp\nv_pk_mul_f32 v[48:49], v[232:233], v[232:233]\nv_pk_mul_f32 v[50:51], v[234:235], v[234:235]\nv_exp_f32 v21, v21 clamp\n"
      "v_pk_fma_f32 v[44:45], v[44:45], s[58:59], s[58:59] op_sel:[0,0,1] op_sel_hi:[1,0,1]\nv_pk_fma_f32 v[46:47], v[46:47], s[58:59], s[58:59] op_sel:[0,0,1] op_sel_hi:[1,0,1]\n"
      "v_pk_fma_f32 v[48:49], v[48:49], s[58:59], s[58:59] op_sel:[0,0,1] op_sel_hi:[1,0,1]\nv_exp_f32 v22, v22 clamp\n"
      "v_pk_fma_f32 v[50:51], v[50:51], s[58:59], s[58:59] op_sel:[0,0,1] op_sel_hi:[1,0,1]\nv_pk_fma_f32 v[44:45], v[44:45], v[228:229], s[60:61] op_sel_hi:[1,1,0]\n"
      "v_pk_fma_f32 v[46:47], v[46:47], v[230:231], s[60:61] op_sel_hi:[1,1,0]\nv_exp_f32 v23, v23 clamp\nv_pk_fma_f32 v[48:49], v[48:49], v[232:233], s[60:61] op_sel_hi:[1,1,0]\n"
      "v_pk_fma_f32 v[50:51], v[50:51], v[234:235], s[60:61] op_sel_hi:[1,1,0]\nv_exp_f32 v44, v44 clamp\nv_pk_fma_f32 v[18:19], v[18:19], v[2:3], v[26:27]\nv_exp_f32 v45, v45 clamp\n"
      "v_pk_fma_f32 v[16:17], v[16:17], v[0:1], v[24:25]\nv_pk_fma_f32 v[20:21], v[20:21], v[4:5], v[28:29]\nv_exp_f32 v46, v46 clamp\nv_pk_fma_f32 v[22:23], v[22:23], v[6:7], v[30:31]\n"
      "v_pk_mul_f32 v[220:221], v[220:221], v[18:19]\nv_exp_f32 v47, v47 clamp\nv_pk_mul_f32 v[224:225], v[224:225], v[22:23]\nv_pk_fma_f32 v[220:221], v[222:223], v[16:17], v[220:221]\n"
      "v_exp_f32 v48, v48 clamp\nv_pk_fma_f32 v[224:225], v[226:227], v[20:21], v[224:225]\nv_exp_f32 v49, v49 clamp\nv_pk_mul_f32 v[16:17], v[16:17], v[18:19]\n"
      "v_pk_mul_f32 v[20:21], v[20:21], v[22:23]\nv_exp_f32 v50, v50 clamp\nv_pk_mul_f32 v[18:19], v[16:17], v[20:21]\nv_rcp_f32 v18, v18\nv_pk_mul_f32 v[220:221], v[220:221], v[20:21]\n"
      "v_exp_f32 v51, v51 clamp\nv_rcp_f32 v19, v19\nv_pk_fma_f32 v[220:221], v[224:225], v[16:17], v[220:221]\nv_pk_mul_f32 v[32:33], v[220:221], v[18:19]\n"
      "v_pk_fma_f32 v[46:47], v[46:47], v[10:11], v[66:67]\nv_pk_fma_f32 v[44:45], v[44:45], v[8:9], v[64:65]\nv_pk_fma_f32 v[48:49], v[48:49], v[12:13], v[68:69]\n"
      "v_pk_fma_f32 v[50:51], v[50:51], v[14:15], v[70:71]\nv_pk_mul_f32 v[228:229], v[228:229], v[46:47]\nv_pk_mul_f32 v[232:233], v[232:233], v[50:51]\n"
      "v_pk_fma_f32 v[228:229], v[230:231], v[44:45], v[228:229]\nv_pk_fma_f32 v[232:233], v[234:235], v[48:49], v[232:233]\nv_pk_mul_f32 v[44:45], v[44:45], v[46:47]\n"
      "v_pk_mul_f32 v[48:49], v[48:49], v[50:51]\nv_pk_mul_f32 v[46:47], v[44:45], v[48:49]\nv_rcp_f32 v46, v46\nv_pk_mul_f32 v[228:229], v[228:229], v[48:49]\nv_rcp_f32 v47, v47\n"
      "v_pk_fma_f32 v[228:229], v[232:233], v[44:45], v[228:229]\nv_pk_fma_f32 v[32:33], v[228:229], v[46:47], v[32:33]\nv_add_f32 v40, v32, v33\ns_barrier\n"
      ".Lge:\nv_mov_b32 v43, v42\nds_read_b128 v[48:51], v246\nds_read_b128 v[52:55], v246 offset:16\nds_read_b64_tr_b16 v[68:69], v238 offset:0\nds_read_b64_tr_b16 v[70:71], v239 offset:0\n"
      "ds_read_b64_tr_b16 v[56:57], v236 offset:0\nds_read_b64_tr_b16 v[58:59], v237 offset:128\nds_read_b64_tr_b16 v[60:61], v237 offset:256\nds_read_b64_tr_b16 v[62:63], v237 offset:384\n"
      "ds_read_b64_tr_b16 v[64:65], v237 offset:512\nds_read_b64_tr_b16 v[66:67], v237 offset:640\nds_read_b128 v[44:47], v248 offset:48\nv_permlane32_swap_b32 v36, v38\n"
      "v_permlane32_swap_b32 v37, v39\nv_permlane32_swap_b32 v40, v42\nv_permlane32_swap_b32 v41, v43\nv_add_f32 v36, v36, v38\nv_add_f32 v37, v37, v39\nv_add_f32 v40, v40, v42\n"
      "v_add_f32 v41, v41, v43\ns_nop 0\nv_permlane16_swap_b32 v36, v37\nv_permlane16_swap_b32 v40, v41\nv_add_f32 v36, v36, v37\nv_add_f32 v40, v40, v41\nds_write_b32 v254, v36\n"
      "ds_write_b32 v255, v40 offset:256\nds_add_rtn_u32 v2, v253, v241 offset:20\ns_waitcnt lgkmcnt(11)\nv_readlane_b32 s48, v69, 48\ns_mov_b32 s56, 0\ns_mov_b32 s44, 0\ns_mov_b32 s33, s45\n"
      "s_waitcnt lgkmcnt(10)\nv_pk_mul_f16 v20, v68, v70\nv_pk_mul_f16 v21, v69, v71\ns_waitcnt lgkmcnt(9)\nv_pk_mul_f16 v22, v56, v56 op_sel_hi:[0,1]\nv_pk_mul_f16 v23, v56, v57 op_sel_hi:[0,1]\n"
      "v_pk_mul_f16 v16, v68, s48 op_sel:[0,1]\nv_pk_mul_f16 v17, v69, s48 op_sel:[0,1]\nv_pk_mul_f16 v18, v70, s48 op_sel:[0,1]\nv_pk_mul_f16 v19, v71, s48 op_sel:[0,1]\ns_nop 1\n"
      "v_mfma_f32_16x16x32_f16 v[40:43], v[76:79], v[16:19], 0\nv_mfma_f32_16x16x32_f16 v[40:43], v[88:91], v[20:23], v[40:43]\ns_add_u32 s45, s45, 2\ns_cmp_ge_u32 s45, s46\ns_cbranch_scc0 .Lc_bg\n"
      "s_swappc_b64 s[4:5], s[34:35]\n"
      ".Lc_bg:\nv_mfma_f32_16x16x32_f16 v[36:39], v[84:87], v[20:23], 0\ns_waitcnt lgkmcnt(4)\nv_pk_mul_f16 v24, v66, v66 op_sel_hi:[0,1]\nv_pk_mul_f16 v25, v66, v57 op_sel_hi:[0,1]\n"
      "ds_read_b128 v[124:127], v252 offset:13312\nv_mfma_f32_16x16x32_f16 v[36:39], v[72:75], v[16:19], v[36:39]\nv_pk_mul_f16 v26, v56, v66 op_sel_hi:[0,1]\n"
      "v_pk_mul_f16 v27, v56, v67 op_sel_hi:[0,1]\nds_read_b128 v[128:131], v252 offset:14336\nv_mfma_f32_16x16x32_f16 v[32:35], v[80:83], v[20:23], 0\nv_pk_mul_f16 v28, v58, v66 op_sel_hi:[0,1]\n"
      "v_pk_mul_f16 v29, v58, v67 op_sel_hi:[0,1]\nds_read_b128 v[132:135], v252 offset:15360\ns_waitcnt lgkmcnt(6)\nv_smfmac_f32_16x16x64_f16 v[44:47], v[92:95], v[16:23], v242\n"
      "v_pk_mul_f16 v30, v60, v66 op_sel_hi:[0,1]\nv_pk_mul_f16 v31, v60, v67 op_sel_hi:[0,1]\nds_read_b128 v[136:139], v252 offset:16384\n"
      "v_mfma_f32_16x16x32_f16 v[32:35], v[96:99], v[24:27], v[32:35]\nv_pk_mul_f16 v16, v62, v66 op_sel_hi:[0,1]\nv_pk_mul_f16 v17, v62, v67 op_sel_hi:[0,1]\n"
      "ds_read_b128 v[140:143], v252 offset:17408\nv_mfma_f32_16x16x32_f16 v[32:35], v[108:111], v[28:31], v[32:35]\nv_pk_mul_f16 v18, v64, v66 op_sel_hi:[0,1]\n"
      "ds_read_b128 v[144:147], v252 offset:18432\nv_mfma_f32_16x16x32_f16 v[36:39], v[112:115], v[28:31], v[36:39]\nv_pk_mul_f16 v19, v64, v67 op_sel_hi:[0,1]\ns_cmp_ge_u32 s45, s46\n"
      "s_cbranch_scc0 .Lc_bh\ns_swappc_b64 s[4:5], s[34:35]\n"
      ".Lc_bh:\nds_read_b128 v[148:151], v252 offset:19456\nv_mfma_f32_16x16x32_f16 v[36:39], v[100:103], v[24:27], v[36:39]\nv_pk_mul_f16 v20, v64, v64 op_sel_hi:[0,1]\n"
      "ds_read_b128 v[152:155], v252 offset:20480\nv_mfma_f32_16x16x32_f16 v[40:43], v[104:107], v[24:27], v[40:43]\nv_pk_mul_f16 v21, v64, v57 op_sel_hi:[0,1]\n"
      "ds_read_b128 v[156:159], v252 offset:21504\nv_mfma_f32_16x16x32_f16 v[40:43], v[116:119], v[28:31], v[40:43]\nv_pk_mul_f16 v22, v56, v64 op_sel_hi:[0,1]\n"
      "ds_read_b128 v[160:163], v252 offset:22528\nv_smfmac_f32_16x16x64_f16 v[44:47], v[120:123], v[24:31], v242\nv_pk_mul_f16 v23, v56, v65 op_sel_hi:[0,1]\n"
      "ds_read_b128 v[164:167], v252 offset:23552\ns_waitcnt lgkmcnt(10)\nv_mfma_f32_16x16x32_f16 v[32:35], v[124:127], v[16:19], v[32:35]\nv_pk_mul_f16 v24, v58, v64 op_sel_hi:[0,1]\n"
      "v_pk_mul_f16 v25, v58, v65 op_sel_hi:[0,1]\nds_read_b64_tr_b16 v[70:71], v239 offset:1856\nds_read_b128 v[168:171], v252 offset:24576\ns_waitcnt lgkmcnt(9)\n"
      "v_mfma_f32_16x16x32_f16 v[32:35], v[136:139], v[20:23], v[32:35]\nv_pk_mul_f16 v26, v60, v64 op_sel_hi:[0,1]\nds_read_b64_tr_b16 v[68:69], v238 offset:1856\n"
      "ds_read_b128 v[172:175], v252 offset:25600\ns_waitcnt lgkmcnt(10)\nv_mfma_f32_16x16x32_f16 v[36:39], v[140:143], v[20:23], v[36:39]\nv_pk_mul_f16 v27, v60, v65 op_sel_hi:[0,1]\n"
      "ds_read_b64_tr_b16 v[66:67], v237 offset:2496\nds_read_b128 v[176:179], v252 offset:26624\nv_mfma_f32_16x16x32_f16 v[36:39], v[128:131], v[16:19], v[36:39]\n"
      "v_pk_mul_f16 v28, v62, v64 op_sel_hi:[0,1]\nds_read_b128 v[180:183], v252 offset:27648\nv_mfma_f32_16x16x32_f16 v[40:43], v[132:135], v[16:19], v[40:43]\n"
      "v_pk_mul_f16 v29, v62, v65 op_sel_hi:[0,1]\nds_read_b128 v[184:187], v252 offset:28672\ns_waitcnt lgkmcnt(13)\nv_mfma_f32_16x16x32_f16 v[40:43], v[144:147], v[20:23], v[40:43]\n"
      "v_pk_mul_f16 v30, v62, v62 op_sel_hi:[0,1]\nds_read_b128 v[188:191], v252 offset:29696\ns_waitcnt lgkmcnt(13)\nv_smfmac_f32_16x16x64_f16 v[44:47], v[148:151], v[16:23], v242\n"
      "v_pk_mul_f16 v31, v62, v57 op_sel_hi:[0,1]\nds_read_b128 v[192:195], v252 offset:30720\ns_waitcnt lgkmcnt(13)\nv_mfma_f32_16x16x32_f16 v[32:35], v[152:155], v[24:27], v[32:35]\n"
      "v_pk_mul_f16 v16, v56, v62 op_sel_hi:[0,1]\nv_pk_mul_f16 v17, v56, v63 op_sel_hi:[0,1]\nds_read_b128 v[196:199], v252 offset:31744\ns_waitcnt lgkmcnt(11)\n"
      "v_mfma_f32_16x16x32_f16 v[32:35], v[164:167], v[28:31], v[32:35]\nv_pk_mul_f16 v18, v58, v62 op_sel_hi:[0,1]\nds_read_b128 v[200:203], v252 offset:32768\n"
      "ds_read_b64_tr_b16 v[64:65], v237 offset:2368\ns_waitcnt lgkmcnt(11)\nv_mfma_f32_16x16x32_f16 v[36:39], v[168:171], v[28:31], v[36:39]\nv_pk_mul_f16 v19, v58, v63 op_sel_hi:[0,1]\n"
      "ds_read_b128 v[204:207], v252 offset:33792\nv_mfma_f32_16x16x32_f16 v[36:39], v[156:159], v[24:27], v[36:39]\nv_pk_mul_f16 v20, v60, v62 op_sel_hi:[0,1]\n"
      "ds_read_b128 v[208:211], v252 offset:34816\nv_mfma_f32_16x16x32_f16 v[40:43], v[160:163], v[24:27], v[40:43]\nv_pk_mul_f16 v21, v60, v63 op_sel_hi:[0,1]\n"
      "ds_read_b128 v[212:215], v252 offset:35840\ns_waitcnt lgkmcnt(13)\nv_readlane_b32 s48, v69, 48\ns_waitcnt lgkmcnt(12)\nv_mfma_f32_16x16x32_f16 v[40:43], v[172:175], v[28:31], v[40:43]\n"
      "v_pk_mul_f16 v22, v60, v60 op_sel_hi:[0,1]\nds_read_b128 v[216:219], v252 offset:36864\ns_waitcnt lgkmcnt(11)\nv_smfmac_f32_16x16x64_f16 v[44:47], v[176:179], v[24:31], v242\n"
      "v_pk_mul_f16 v23, v60, v57 op_sel_hi:[0,1]\nds_read_b128 v[220:223], v252 offset:37888\ns_waitcnt lgkmcnt(11)\nv_mfma_f32_16x16x32_f16 v[32:35], v[180:183], v[16:19], v[32:35]\n"
      "v_pk_mul_f16 v24, v56, v60 op_sel_hi:[0,1]\nv_pk_mul_f16 v25, v56, v61 op_sel_hi:[0,1]\nds_read_b128 v[224:227], v252 offset:38912\ns_waitcnt lgkmcnt(9)\n"
      "v_mfma_f32_16x16x32_f16 v[32:35], v[192:195], v[20:23], v[32:35]\nv_pk_mul_f16 v26, v58, v60 op_sel_hi:[0,1]\nv_pk_mul_f16 v27, v58, v61 op_sel_hi:[0,1]\n"
      "ds_read_b128 v[228:231], v252 offset:39936\ns_waitcnt lgkmcnt(9)\nv_mfma_f32_16x16x32_f16 v[36:39], v[196:199], v[20:23], v[36:39]\nv_pk_mul_f16 v28, v58, v58 op_sel_hi:[0,1]\n"
      "ds_read_b128 v[232:235], v252 offset:40960\nds_read_b64_tr_b16 v[62:63], v237 offset:2240\nv_mfma_f32_16x16x32_f16 v[36:39], v[184:187], v[16:19], v[36:39]\n"
      "v_pk_mul_f16 v29, v58, v57 op_sel_hi:[0,1]\nds_read_b64_tr_b16 v[60:61], v237 offset:2112\nv_mfma_f32_16x16x32_f16 v[40:43], v[188:191], v[16:19], v[40:43]\n"
      "v_pk_mul_f16 v30, v56, v58 op_sel_hi:[0,1]\ns_waitcnt lgkmcnt(11)\nv_mfma_f32_16x16x32_f16 v[40:43], v[200:203], v[20:23], v[40:43]\nv_pk_mul_f16 v31, v56, v59 op_sel_hi:[0,1]\n"
      "s_waitcnt lgkmcnt(9)\nv_smfmac_f32_16x16x64_f16 v[44:47], v[204:207], v[16:23], v242\nds_read_b64_tr_b16 v[56:57], v236 offset:1856\nds_read_b64_tr_b16 v[58:59], v237 offset:1984\n"
      "s_waitcnt lgkmcnt(4)\nv_smfmac_f32_16x16x64_f16 v[44:47], v[232:235], v[24:31], v242\nv_pk_mul_f16 v16, v68, s48 op_sel:[0,1]\nv_pk_mul_f16 v17, v69, s48 op_sel:[0,1]\n"
      "v_mfma_f32_16x16x32_f16 v[32:35], v[208:211], v[24:27], v[32:35]\nv_pk_mul_f16 v18, v70, s48 op_sel:[0,1]\nv_pk_mul_f16 v19, v71, s48 op_sel:[0,1]\n"
      "v_mfma_f32_16x16x32_f16 v[32:35], v[220:223], v[28:31], v[32:35]\nv_pk_mul_f16 v20, v68, v70\nv_pk_mul_f16 v21, v69, v71\nv_mfma_f32_16x16x32_f16 v[36:39], v[224:227], v[28:31], v[36:39]\n"
      "v_add_f32 v44, v44, v45\nv_add_f32 v46, v46, v47\nv_mfma_f32_16x16x32_f16 v[36:39], v[212:215], v[24:27], v[36:39]\nv_mul_f32 v55, v44, v54\nv_mul_f32 v55, v55, v46\n"
      "ds_read_b128 v[44:47], v248 offset:48\nv_mfma_f32_16x16x32_f16 v[40:43], v[216:219], v[24:27], v[40:43]\ns_waitcnt lgkmcnt(2)\nv_pk_mul_f16 v22, v56, v56 op_sel_hi:[0,1]\n"
      "v_pk_mul_f16 v23, v56, v57 op_sel_hi:[0,1]\nv_mfma_f32_16x16x32_f16 v[40:43], v[228:231], v[28:31], v[40:43]\nv_mul_f32 v32, v32, v48\nv_mul_f32 v33, v33, v49\nv_mul_f32 v34, v34, v50\n"
      "s_waitcnt lgkmcnt(1)\ns_branch .Lc_u1\n"
      ".Lfeed:\ns_waitcnt vmcnt(7) lgkmcnt(0)\ns_cbranch_vccnz .Lw1004\ns_waitcnt vmcnt(6)\n"
      ".Lw1004:\ns_barrier\nds_read_b64 v[44:45], v30 offset:45056\ns_add_u32 m0, s54, 0\ns_nop 0\nglobal_load_lds_dwordx4 v236, s[50:51] offset:0\n"
      "global_load_lds_dwordx4 v237, s[50:51] offset:1024\nglobal_load_lds_dwordx4 v238, s[50:51] offset:2048\nglobal_load_lds_dwordx4 v239, s[50:51] offset:3072\ns_add_u32 s50, s50, 212992\n"
      "s_addc_u32 s51, s51, 0\ns_add_u32 m0, s26, 20480\ns_nop 0\nglobal_load_lds_dwordx4 v27, s[6:7] offset:0\nv_lshl_add_u32 v27, v197, 7, v4\ns_cbranch_vccz .La1003\ns_add_u32 m0, s26, 24576\n"
      "s_nop 0\nglobal_load_lds_dwordx4 v28, s[6:7] offset:0\n"
      ".La1003:\nv_lshl_add_u32 v28, v203, 7, v4\nds_read2_b64 v[200:203], v9 offset0:2 offset1:3\ns_add_u32 m0, s53, 0\ns_nop 0\nglobal_load_lds_dwordx4 v22, s[6:7] offset:0\n"
      "v_lshl_add_u32 v22, v179, 7, v4\nds_read2_b64 v[176:179], v6 offset0:2 offset1:3\nglobal_load_lds_dwordx4 v23, s[44:45] offset:1024\nv_lshl_add_u32 v23, v181, 7, v5\n"
      "global_load_lds_dwordx4 v24, s[46:47] offset:2048\nv_lshl_add_u32 v24, v187, 7, v4\nds_read2_b64 v[184:187], v7 offset0:2 offset1:3\nglobal_load_lds_dwordx4 v25, s[48:49] offset:3072\n"
      "v_lshl_add_u32 v25, v189, 7, v5\ns_add_u32 m0, s53, 4096\ns_nop 0\nglobal_load_lds_dwordx4 v26, s[6:7] offset:0\nv_lshl_add_u32 v26, v195, 7, v4\n"
      "ds_read2_b64 v[192:195], v8 offset0:2 offset1:3\ns_waitcnt vmcnt(11) lgkmcnt(0)\ns_cbranch_vccnz .Lw1006\ns_waitcnt vmcnt(10)\n"
      ".Lw1006:\ns_barrier\ns_add_u32 m0, s54, 45056\ns_nop 0\nglobal_load_lds_dwordx4 v236, s[50:51] offset:0\nds_read_b64 v[46:47], v31 offset:0\n"
      "global_load_lds_dwordx4 v237, s[50:51] offset:1024\nglobal_load_lds_dwordx4 v238, s[50:51] offset:2048\nglobal_load_lds_dwordx4 v239, s[50:51] offset:3072\ns_add_u32 s50, s50, 212992\n"
      "s_addc_u32 s51, s51, 0\ns_cmp_eq_u32 s27, 22\ns_cselect_b64 s[50:51], s[10:11], s[50:51]\ns_add_u32 m0, s26, 65536\nv_max3_f16 v32, |v44|, |v45|, v32\n"
      "global_load_lds_dwordx4 v27, s[6:7] offset:0\nv_lshl_add_u32 v27, v198, 7, v4\nv_max3_f32 v33, |v44|, |v45|, v33\ns_cbranch_vccz .La1007\ns_add_u32 m0, s26, 69632\ns_nop 0\n"
      "global_load_lds_dwordx4 v28, s[6:7] offset:0\n"
      ".La1007:\nv_lshl_add_u32 v28, v200, 7, v4\ns_add_u32 m0, s53, 45056\nv_and_b32 v96, s55, v44\nglobal_load_lds_dwordx4 v22, s[6:7] offset:0\nv_lshl_add_u32 v22, v176, 7, v4\n"
      "v_xor_b32 v44, v44, v96\nglobal_load_lds_dwordx4 v23, s[44:45] offset:1024\nv_lshl_add_u32 v23, v182, 7, v5\nv_and_b32 v97, s55, v45\nglobal_load_lds_dwordx4 v24, s[46:47] offset:2048\n"
      "v_lshl_add_u32 v24, v184, 7, v4\nv_xor_b32 v45, v45, v97\nglobal_load_lds_dwordx4 v25, s[48:49] offset:3072\ns_add_u32 m0, s53, 49152\nv_lshl_add_u32 v25, v190, 7, v5\n"
      "global_load_lds_dwordx4 v26, s[6:7] offset:0\nv_lshl_add_u32 v26, v192, 7, v4\ns_cmp_lg_u32 s31, 0\ns_cbranch_scc1 .Lb1008\ns_lshl_b32 s42, s24, 9\nv_lshl_add_u32 v39, v240, 3, s42\n"
      "s_and_b32 s42, s2, 7\ns_lshl_b32 s42, s42, 11\ns_add_u32 s42, s42, 4096\ns_add_u32 s58, s20, s42\ns_addc_u32 s59, s21, 0\nglobal_store_dwordx2 v39, v[254:255], s[58:59] sc1\n"
      ".Lb1008:\ns_waitcnt vmcnt(11) lgkmcnt(0)\ns_cbranch_vccnz .Lw1009\ns_waitcnt vmcnt(10)\n"
      ".Lw1009:\ns_barrier\ns_add_u32 m0, s54, 90112\ns_nop 0\nglobal_load_lds_dwordx4 v236, s[50:51] offset:0\nds_read_b64 v[48:49], v30 offset:0\n"
      "global_load_lds_dwordx4 v237, s[50:51] offset:1024\nglobal_load_lds_dwordx4 v238, s[50:51] offset:2048\nglobal_load_lds_dwordx4 v239, s[50:51] offset:3072\ns_add_u32 s50, s50, 212992\n"
      "s_addc_u32 s51, s51, 0\ns_add_u32 m0, s26, 110592\nv_max3_f16 v32, |v46|, |v47|, v32\nglobal_load_lds_dwordx4 v27, s[6:7] offset:0\nv_lshl_add_u32 v27, v199, 7, v4\n"
      "ds_read2_b64 v[196:199], v8 offset0:115 offset1:116\nv_max3_f32 v33, |v46|, |v47|, v33\ns_cbranch_vccz .La1010\ns_add_u32 m0, s26, 114688\ns_nop 0\n"
      "global_load_lds_dwordx4 v28, s[6:7] offset:0\n"
      ".La1010:\nv_lshl_add_u32 v28, v201, 7, v4\ns_add_u32 m0, s53, 90112\nv_and_b32 v98, s55, v46\nglobal_load_lds_dwordx4 v22, s[6:7] offset:0\nv_lshl_add_u32 v22, v177, 7, v4\n"
      "v_xor_b32 v46, v46, v98\nglobal_load_lds_dwordx4 v23, s[44:45] offset:1024\nv_lshl_add_u32 v23, v183, 7, v5\nds_read2_b64 v[180:183], v6 offset0:115 offset1:116\nv_and_b32 v99, s55, v47\n"
      "global_load_lds_dwordx4 v24, s[46:47] offset:2048\nv_lshl_add_u32 v24, v185, 7, v4\nv_xor_b32 v47, v47, v99\nglobal_load_lds_dwordx4 v25, s[48:49] offset:3072\n"
      "v_lshl_add_u32 v25, v191, 7, v5\ns_add_u32 m0, s53, 94208\nds_read2_b64 v[188:191], v7 offset0:115 offset1:116\nglobal_load_lds_dwordx4 v26, s[6:7] offset:0\n"
      "v_lshl_add_u32 v26, v193, 7, v4\ns_cmp_lg_u32 s24, 2\ns_cbranch_scc1 .Lo1011\ns_getpc_b64 s[62:63]\ns_add_u32 s62, s62, _ZL5g_tab@rel32@lo+2052\n"
      "s_addc_u32 s63, s63, _ZL5g_tab@rel32@hi+2060\ns_lshr_b32 s42, s2, 3\ns_lshl1_add_u32 s42, s2, s42\ns_and_b32 s42, s42, 15\ns_lshl_b32 s42, s42, 7\nv_lshl_add_u32 v40, v240, 4, s42\n"
      "s_mov_b32 m0, 162112\ns_mov_b64 exec, 0xff\nglobal_load_lds_dwordx4 v40, s[62:63]\ns_mov_b64 exec, -1\n"
      ".Lo1011:\ns_waitcnt vmcnt(11) lgkmcnt(0)\ns_cbranch_vccnz .Lw1012\ns_waitcnt vmcnt(10)\n"
      ".Lw1012:\ns_barrier\ns_add_u32 m0, s54, 0\ns_nop 0\nglobal_load_lds_dwordx4 v236, s[50:51] offset:0\nds_read_b64 v[50:51], v30 offset:45056\n"
      "global_load_lds_dwordx4 v237, s[50:51] offset:1024\nglobal_load_lds_dwordx4 v238, s[50:51] offset:2048\nglobal_load_lds_dwordx4 v239, s[50:51] offset:3072\ns_add_u32 s50, s50, 212992\n"
      "s_addc_u32 s51, s51, 0\ns_cmp_eq_u32 s27, 20\ns_cselect_b64 s[50:51], s[10:11], s[50:51]\ns_add_u32 m0, s26, 20480\nv_max3_f16 v32, |v48|, |v49|, v32\n"
      "global_load_lds_dwordx4 v27, s[6:7] offset:0\nv_lshl_add_u32 v27, v196, 7, v4\nv_max3_f32 v33, |v48|, |v49|, v33\ns_cbranch_vccz .La1013\ns_add_u32 m0, s26, 24576\ns_nop 0\n"
      "global_load_lds_dwordx4 v28, s[6:7] offset:0\n"
      ".La1013:\nv_lshl_add_u32 v28, v202, 7, v4\ns_add_u32 m0, s53, 0\nv_and_b32 v100, s55, v48\nglobal_load_lds_dwordx4 v22, s[6:7] offset:0\nv_lshl_add_u32 v22, v178, 7, v4\n"
      "v_xor_b32 v48, v48, v100\nglobal_load_lds_dwordx4 v23, s[44:45] offset:1024\nv_lshl_add_u32 v23, v180, 7, v5\nv_and_b32 v101, s55, v49\nglobal_load_lds_dwordx4 v24, s[46:47] offset:2048\n"
      "v_lshl_add_u32 v24, v186, 7, v4\nv_xor_b32 v49, v49, v101\nglobal_load_lds_dwordx4 v25, s[48:49] offset:3072\ns_add_u32 m0, s53, 4096\nv_lshl_add_u32 v25, v188, 7, v5\n"
      "global_load_lds_dwordx4 v26, s[6:7] offset:0\nv_lshl_add_u32 v26, v194, 7, v4\ns_waitcnt vmcnt(11) lgkmcnt(0)\ns_cbranch_vccnz .Lw1014\ns_waitcnt vmcnt(10)\n"
      ".Lw1014:\ns_barrier\ns_add_u32 m0, s54, 45056\ns_nop 0\nglobal_load_lds_dwordx4 v236, s[50:51] offset:0\nds_read_b64 v[52:53], v31 offset:0\n"
      "global_load_lds_dwordx4 v237, s[50:51] offset:1024\nglobal_load_lds_dwordx4 v238, s[50:51] offset:2048\nglobal_load_lds_dwordx4 v239, s[50:51] offset:3072\ns_add_u32 s50, s50, 212992\n"
      "s_addc_u32 s51, s51, 0\ns_add_u32 m0, s26, 65536\nv_max3_f16 v32, |v50|, |v51|, v32\nglobal_load_lds_dwordx4 v27, s[6:7] offset:0\nv_lshl_add_u32 v27, v197, 7, v4\n"
      "v_max3_f32 v33, |v50|, |v51|, v33\ns_cbranch_vccz .La1015\ns_add_u32 m0, s26, 69632\ns_nop 0\nglobal_load_lds_dwordx4 v28, s[6:7] offset:0\n"
      ".La1015:\nv_lshl_add_u32 v28, v203, 7, v4\nds_read2_b64 v[200:203], v9 offset0:4 offset1:5\ns_add_u32 m0, s53, 45056\nv_and_b32 v102, s55, v50\nglobal_load_lds_dwordx4 v22, s[6:7] offset:0\n"
      "v_lshl_add_u32 v22, v179, 7, v4\nds_read2_b64 v[176:179], v6 offset0:4 offset1:5\nv_xor_b32 v50, v50, v102\nglobal_load_lds_dwordx4 v23, s[44:45] offset:1024\n"
      "v_lshl_add_u32 v23, v181, 7, v5\nv_and_b32 v103, s55, v51\nglobal_load_lds_dwordx4 v24, s[46:47] offset:2048\nv_lshl_add_u32 v24, v187, 7, v4\n"
      "ds_read2_b64 v[184:187], v7 offset0:4 offset1:5\nv_xor_b32 v51, v51, v103\nglobal_load_lds_dwordx4 v25, s[48:49] offset:3072\ns_add_u32 m0, s53, 49152\nv_lshl_add_u32 v25, v189, 7, v5\n"
      "global_load_lds_dwordx4 v26, s[6:7] offset:0\nv_lshl_add_u32 v26, v195, 7, v4\nds_read2_b64 v[192:195], v8 offset0:4 offset1:5\ns_waitcnt vmcnt(11) lgkmcnt(0)\ns_cbranch_vccnz .Lw1016\n"
      "s_waitcnt vmcnt(10)\n"
      ".Lw1016:\ns_barrier\ns_add_u32 m0, s54, 90112\ns_nop 0\nglobal_load_lds_dwordx4 v236, s[50:51] offset:0\nds_read_b64 v[54:55], v30 offset:0\n"
      "global_load_lds_dwordx4 v237, s[50:51] offset:1024\nglobal_load_lds_dwordx4 v238, s[50:51] offset:2048\nglobal_load_lds_dwordx4 v239, s[50:51] offset:3072\ns_add_u32 s50, s50, 212992\n"
      "s_addc_u32 s51, s51, 0\ns_cmp_eq_u32 s27, 18\ns_cselect_b64 s[50:51], s[10:11], s[50:51]\ns_add_u32 m0, s26, 110592\nv_max3_f16 v32, |v52|, |v53|, v32\n"
      "global_load_lds_dwordx4 v27, s[6:7] offset:0\nv_lshl_add_u32 v27, v198, 7, v4\nv_max3_f32 v33, |v52|, |v53|, v33\ns_cbranch_vccz .La1017\ns_add_u32 m0, s26, 114688\ns_nop 0\n"
      "global_load_lds_dwordx4 v28, s[6:7] offset:0\n"
      ".La1017:\nv_lshl_add_u32 v28, v200, 7, v4\ns_add_u32 m0, s53, 90112\nv_and_b32 v104, s55, v52\nglobal_load_lds_dwordx4 v22, s[6:7] offset:0\nv_lshl_add_u32 v22, v176, 7, v4\n"
      "v_xor_b32 v52, v52, v104\nglobal_load_lds_dwordx4 v23, s[44:45] offset:1024\nv_lshl_add_u32 v23, v182, 7, v5\nv_and_b32 v105, s55, v53\nglobal_load_lds_dwordx4 v24, s[46:47] offset:2048\n"
      "v_lshl_add_u32 v24, v184, 7, v4\nv_xor_b32 v53, v53, v105\nglobal_load_lds_dwordx4 v25, s[48:49] offset:3072\ns_add_u32 m0, s53, 94208\nv_lshl_add_u32 v25, v190, 7, v5\n"
      "global_load_lds_dwordx4 v26, s[6:7] offset:0\nv_lshl_add_u32 v26, v192, 7, v4\ns_waitcnt vmcnt(11) lgkmcnt(0)\ns_cbranch_vccnz .Lw1018\ns_waitcnt vmcnt(10)\n"
      ".Lw1018:\ns_barrier\ns_add_u32 m0, s54, 0\ns_nop 0\nglobal_load_lds_dwordx4 v236, s[50:51] offset:0\nds_read_b64 v[56:57], v30 offset:45056\n"
      "global_load_lds_dwordx4 v237, s[50:51] offset:1024\nglobal_load_lds_dwordx4 v238, s[50:51] offset:2048\nglobal_load_lds_dwordx4 v239, s[50:51] offset:3072\ns_add_u32 s50, s50, 212992\n"
      "s_addc_u32 s51, s51, 0\nv_max3_f16 v32, |v54|, |v55|, v32\nv_max3_f32 v33, |v54|, |v55|, v33\ns_add_u32 m0, s26, 20480\nv_cmp_eq_u32 s[62:63], 0, v240\n"
      "global_load_lds_dwordx4 v27, s[6:7] offset:0\nv_lshl_add_u32 v27, v199, 7, v4\nds_read2_b64 v[196:199], v8 offset0:117 offset1:118\nv_mov_b32 v253, 262144\nv_mov_b32 v248, 162048\n"
      "v_mov_b32 v241, 1\ns_cbranch_vccz .La1019\ns_add_u32 m0, s26, 24576\ns_nop 0\nglobal_load_lds_dwordx4 v28, s[6:7] offset:0\n"
      ".La1019:\nv_lshl_add_u32 v28, v201, 7, v4\nv_cndmask_b32 v253, v253, v248, s[62:63]\ns_add_u32 m0, s53, 0\nv_and_b32 v106, s55, v54\nglobal_load_lds_dwordx4 v22, s[6:7] offset:0\n"
      "v_lshl_add_u32 v22, v177, 7, v4\nv_xor_b32 v54, v54, v106\nv_and_b32 v107, s55, v55\nglobal_load_lds_dwordx4 v23, s[44:45] offset:1024\nv_lshl_add_u32 v23, v183, 7, v5\n"
      "ds_read2_b64 v[180:183], v6 offset0:117 offset1:118\nv_xor_b32 v55, v55, v107\nv_lshrrev_b32 v164, 4, v240\nglobal_load_lds_dwordx4 v24, s[46:47] offset:2048\n"
      "v_lshl_add_u32 v24, v185, 7, v4\nv_bfe_u32 v165, v240, 2, 2\nv_and_b32 v166, 3, v240\nglobal_load_lds_dwordx4 v25, s[48:49] offset:3072\nv_lshl_add_u32 v25, v191, 7, v5\n"
      "ds_read2_b64 v[188:191], v7 offset0:117 offset1:118\nv_mul_u32_u24 v166, 0x80008, v166\ns_add_u32 m0, s53, 4096\nv_add_u32 v244, v164, v165\nglobal_load_lds_dwordx4 v26, s[6:7] offset:0\n"
      "v_lshl_add_u32 v26, v193, 7, v4\nv_and_b32 v244, 3, v244\nv_add_u32 v168, 22, v165\ns_waitcnt vmcnt(11) lgkmcnt(0)\ns_cbranch_vccnz .Lw1020\ns_waitcnt vmcnt(10)\n"
      ".Lw1020:\ns_barrier\ns_add_u32 m0, s54, 45056\ns_nop 0\nglobal_load_lds_dwordx4 v236, s[50:51] offset:0\nds_read_b64 v[58:59], v31 offset:0\n"
      "global_load_lds_dwordx4 v237, s[50:51] offset:1024\nglobal_load_lds_dwordx4 v238, s[50:51] offset:2048\nglobal_load_lds_dwordx4 v239, s[50:51] offset:3072\ns_add_u32 s50, s50, 212992\n"
      "s_addc_u32 s51, s51, 0\ns_cmp_eq_u32 s27, 16\ns_cselect_b64 s[50:51], s[10:11], s[50:51]\nv_max3_f16 v32, |v56|, |v57|, v32\nv_max3_f32 v33, |v56|, |v57|, v33\ns_add_u32 m0, s26, 65536\n"
      "v_cmp_gt_u32 s[62:63], 2, v165\nglobal_load_lds_dwordx4 v27, s[6:7] offset:0\nv_lshl_add_u32 v27, v196, 7, v4\nv_and_b32 v165, 60, v240\ns_mov_b32 s42, 0\n"
      "v_cndmask_b32 v168, v168, v244, s[62:63]\ns_cbranch_vccz .La1021\ns_add_u32 m0, s26, 69632\ns_nop 0\nglobal_load_lds_dwordx4 v28, s[6:7] offset:0\n"
      ".La1021:\nv_lshl_add_u32 v28, v202, 7, v4\nv_lshl_or_b32 v244, v244, 16, v168\nv_lshl_add_u32 v244, v244, 5, v166\ns_add_u32 m0, s53, 45056\nv_lshl_add_u32 v252, v240, 4, s42\n"
      "global_load_lds_dwordx4 v22, s[6:7] offset:0\nv_lshl_add_u32 v22, v178, 7, v4\nv_and_b32 v108, s55, v56\nv_xor_b32 v56, v56, v108\nv_and_b32 v109, s55, v57\n"
      "global_load_lds_dwordx4 v23, s[44:45] offset:1024\nv_lshl_add_u32 v23, v180, 7, v5\nv_xor_b32 v57, v57, v109\ns_mov_b32 s42, 0x76325410\ns_mov_b32 s43, 0x21005410\n"
      "global_load_lds_dwordx4 v24, s[46:47] offset:2048\nv_lshl_add_u32 v24, v186, 7, v4\nv_lshrrev_b64 v[168:169], v165, s[42:43]\nv_and_b32 v168, 7, v168\nv_lshl_add_u32 v168, v164, 3, v168\n"
      "global_load_lds_dwordx4 v25, s[48:49] offset:3072\nv_lshl_add_u32 v25, v188, 7, v5\ns_mov_b32 s42, 0x54107632\ns_mov_b32 s43, 0x21107632\ns_add_u32 m0, s53, 49152\n"
      "v_lshrrev_b64 v[170:171], v165, s[42:43]\nglobal_load_lds_dwordx4 v26, s[6:7] offset:0\nv_lshl_add_u32 v26, v194, 7, v4\nv_and_b32 v170, 7, v170\nv_lshl_add_u32 v170, v164, 3, v170\n"
      "s_waitcnt vmcnt(11) lgkmcnt(0)\ns_cbranch_vccnz .Lw1022\ns_waitcnt vmcnt(10)\n"
      ".Lw1022:\ns_barrier\ns_add_u32 m0, s54, 90112\ns_nop 0\nglobal_load_lds_dwordx4 v236, s[50:51] offset:0\nds_read_b64 v[60:61], v30 offset:0\n"
      "global_load_lds_dwordx4 v237, s[50:51] offset:1024\nglobal_load_lds_dwordx4 v238, s[50:51] offset:2048\nglobal_load_lds_dwordx4 v239, s[50:51] offset:3072\ns_add_u32 s50, s50, 212992\n"
      "s_addc_u32 s51, s51, 0\nv_max3_f16 v32, |v58|, |v59|, v32\ns_add_u32 m0, s26, 110592\nv_max3_f32 v33, |v58|, |v59|, v33\nglobal_load_lds_dwordx4 v27, s[6:7] offset:0\n"
      "v_lshl_add_u32 v27, v197, 7, v4\nv_lshl_or_b32 v245, v170, 16, v168\nv_lshl_add_u32 v245, v245, 5, v166\ns_cbranch_vccz .La1023\ns_add_u32 m0, s26, 114688\ns_nop 0\n"
      "global_load_lds_dwordx4 v28, s[6:7] offset:0\n"
      ".La1023:\nv_lshl_add_u32 v28, v203, 7, v4\nds_read2_b64 v[200:203], v9 offset0:6 offset1:7\nv_and_b32 v165, 1, v240\ns_add_u32 m0, s53, 90112\nv_mul_u32_u24 v165, 0xaaaa, v165\n"
      "global_load_lds_dwordx4 v22, s[6:7] offset:0\nv_lshl_add_u32 v22, v179, 7, v4\nds_read2_b64 v[176:179], v6 offset0:6 offset1:7\nv_add_u32 v242, 0x4444, v165\ns_lshl_b32 s42, s36, 4\n"
      "global_load_lds_dwordx4 v23, s[44:45] offset:1024\nv_lshl_add_u32 v23, v181, 7, v5\ns_add_u32 s42, s42, 160448\nv_lshl_add_u32 v243, v164, 2, s42\n"
      "global_load_lds_dwordx4 v24, s[46:47] offset:2048\nv_lshl_add_u32 v24, v187, 7, v4\nds_read2_b64 v[184:187], v7 offset0:6 offset1:7\nv_and_b32 v110, s55, v58\nv_xor_b32 v58, v58, v110\n"
      "global_load_lds_dwordx4 v25, s[48:49] offset:3072\nv_lshl_add_u32 v25, v189, 7, v5\ns_add_u32 m0, s53, 94208\nv_and_b32 v111, s55, v59\nglobal_load_lds_dwordx4 v26, s[6:7] offset:0\n"
      "v_lshl_add_u32 v26, v195, 7, v4\nds_read2_b64 v[192:195], v8 offset0:6 offset1:7\nv_xor_b32 v59, v59, v111\ns_waitcnt vmcnt(11) lgkmcnt(0)\ns_cbranch_vccnz .Lw1024\ns_waitcnt vmcnt(10)\n"
      ".Lw1024:\ns_barrier\ns_add_u32 m0, s54, 0\ns_nop 0\nglobal_load_lds_dwordx4 v236, s[50:51] offset:0\nds_read_b64 v[62:63], v30 offset:45056\n"
      "global_load_lds_dwordx4 v237, s[50:51] offset:1024\nglobal_load_lds_dwordx4 v238, s[50:51] offset:2048\nglobal_load_lds_dwordx4 v239, s[50:51] offset:3072\ns_add_u32 s50, s50, 212992\n"
      "s_addc_u32 s51, s51, 0\ns_cmp_eq_u32 s27, 14\ns_cselect_b64 s[50:51], s[10:11], s[50:51]\nv_max3_f16 v32, |v60|, |v61|, v32\nv_max3_f32 v33, |v60|, |v61|, v33\ns_add_u32 m0, s26, 20480\n"
      "v_and_b32 v164, 1, v164\nglobal_load_lds_dwordx4 v27, s[6:7] offset:0\nv_lshl_add_u32 v27, v198, 7, v4\nv_lshl_add_u32 v164, v164, 3, s24\ns_sub_u32 s42, s64, 1\nv_min_u32 v164, s42, v164\n"
      "s_cbranch_vccz .La1025\ns_add_u32 m0, s26, 24576\ns_nop 0\nglobal_load_lds_dwordx4 v28, s[6:7] offset:0\n"
      ".La1025:\nv_lshl_add_u32 v28, v200, 7, v4\nv_lshlrev_b32 v164, 3, v164\nv_and_b32 v165, 15, v240\ns_add_u32 m0, s53, 0\nv_lshl_add_u32 v250, v164, 1, v165\n"
      "global_load_lds_dwordx4 v22, s[6:7] offset:0\nv_lshl_add_u32 v22, v176, 7, v4\ns_mov_b32 s42, 136192\nv_lshl_add_u32 v250, v250, 2, s42\nglobal_load_lds_dwordx4 v23, s[44:45] offset:1024\n"
      "v_lshl_add_u32 v23, v182, 7, v5\nv_min_u32 v165, 12, v165\ns_lshl_b32 s42, s30, 7\nglobal_load_lds_dwordx4 v24, s[46:47] offset:2048\nv_lshl_add_u32 v24, v184, 7, v4\n"
      "v_lshl_add_u32 v165, v165, 15, s42\nv_add_u32 v249, v165, v164\nglobal_load_lds_dwordx4 v25, s[48:49] offset:3072\nv_lshl_add_u32 v25, v190, 7, v5\nv_and_b32 v112, s55, v60\n"
      "s_add_u32 m0, s53, 4096\nv_xor_b32 v60, v60, v112\nglobal_load_lds_dwordx4 v26, s[6:7] offset:0\nv_lshl_add_u32 v26, v192, 7, v4\nv_and_b32 v113, s55, v61\nv_xor_b32 v61, v61, v113\n"
      "s_waitcnt vmcnt(11) lgkmcnt(0)\ns_cbranch_vccnz .Lw1026\ns_waitcnt vmcnt(10)\n"
      ".Lw1026:\ns_barrier\ns_add_u32 m0, s54, 45056\ns_nop 0\nglobal_load_lds_dwordx4 v236, s[50:51] offset:0\nds_read_b64 v[64:65], v31 offset:0\n"
      "global_load_lds_dwordx4 v237, s[50:51] offset:1024\nglobal_load_lds_dwordx4 v238, s[50:51] offset:2048\nglobal_load_lds_dwordx4 v239, s[50:51] offset:3072\ns_add_u32 s50, s50, 212992\n"
      "s_addc_u32 s51, s51, 0\nv_max3_f16 v32, |v62|, |v63|, v32\ns_add_u32 m0, s26, 65536\nv_max3_f32 v33, |v62|, |v63|, v33\nglobal_load_lds_dwordx4 v27, s[6:7] offset:0\n"
      "v_lshl_add_u32 v27, v199, 7, v4\nds_read2_b64 v[196:199], v8 offset0:119 offset1:120\ns_mul_i32 s65, s36, 22272\ns_add_u32 s65, s65, 45056\ns_cbranch_vccz .La1027\ns_add_u32 m0, s26, 69632\n"
      "s_nop 0\nglobal_load_lds_dwordx4 v28, s[6:7] offset:0\n"
      ".La1027:\nv_lshl_add_u32 v28, v201, 7, v4\ns_add_u32 s68, s64, 8\ns_min_u32 s68, s68, 21\ns_add_u32 m0, s53, 45056\ns_nop 0\nglobal_load_lds_dwordx4 v22, s[6:7] offset:0\n"
      "v_lshl_add_u32 v22, v177, 7, v4\ns_lshl_b32 s38, s38, 16\nglobal_load_lds_dwordx4 v23, s[44:45] offset:1024\nv_lshl_add_u32 v23, v183, 7, v5\n"
      "ds_read2_b64 v[180:183], v6 offset0:119 offset1:120\nv_and_b32 v114, s55, v62\nglobal_load_lds_dwordx4 v24, s[46:47] offset:2048\nv_lshl_add_u32 v24, v185, 7, v4\nv_xor_b32 v62, v62, v114\n"
      "global_load_lds_dwordx4 v25, s[48:49] offset:3072\nv_lshl_add_u32 v25, v191, 7, v5\nds_read2_b64 v[188:191], v7 offset0:119 offset1:120\ns_add_u32 m0, s53, 49152\nv_and_b32 v115, s55, v63\n"
      "global_load_lds_dwordx4 v26, s[6:7] offset:0\nv_lshl_add_u32 v26, v193, 7, v4\nv_xor_b32 v63, v63, v115\ns_waitcnt vmcnt(11) lgkmcnt(0)\ns_cbranch_vccnz .Lw1028\ns_waitcnt vmcnt(10)\n"
      ".Lw1028:\ns_barrier\ns_add_u32 m0, s54, 90112\ns_nop 0\nglobal_load_lds_dwordx4 v236, s[50:51] offset:0\nds_read_b64 v[66:67], v30 offset:0\n"
      "global_load_lds_dwordx4 v237, s[50:51] offset:1024\nglobal_load_lds_dwordx4 v238, s[50:51] offset:2048\nglobal_load_lds_dwordx4 v239, s[50:51] offset:3072\ns_add_u32 s50, s50, 212992\n"
      "s_addc_u32 s51, s51, 0\ns_cmp_eq_u32 s27, 12\ns_cselect_b64 s[50:51], s[10:11], s[50:51]\nv_max3_f16 v32, |v64|, |v65|, v32\nv_max3_f32 v33, |v64|, |v65|, v33\ns_lshr_b32 s42, s24, 2\n"
      "s_add_u32 m0, s26, 110592\ns_nop 0\nglobal_load_lds_dwordx4 v27, s[6:7] offset:0\nv_lshl_add_u32 v27, v196, 7, v4\ns_mul_i32 s43, s42, 3\ns_xor_b32 s43, s43, s36\ns_cbranch_vccz .La1029\n"
      "s_add_u32 m0, s26, 114688\ns_nop 0\nglobal_load_lds_dwordx4 v28, s[6:7] offset:0\n"
      ".La1029:\nv_lshl_add_u32 v28, v202, 7, v4\ns_add_u32 s42, s42, s64\ns_add_u32 s42, s42, 9\ns_add_u32 m0, s53, 90112\ns_nop 0\nglobal_load_lds_dwordx4 v22, s[6:7] offset:0\n"
      "v_lshl_add_u32 v22, v178, 7, v4\ns_and_b32 s42, s42, 1\ns_lshl1_add_u32 s67, s43, s42\nglobal_load_lds_dwordx4 v23, s[44:45] offset:1024\nv_lshl_add_u32 v23, v180, 7, v5\n"
      "s_add_u32 s66, s64, s67\ns_cmp_lt_u32 s66, s68\ns_cselect_b32 s66, s66, 127\nglobal_load_lds_dwordx4 v24, s[46:47] offset:2048\nv_lshl_add_u32 v24, v186, 7, v4\ns_sub_u32 s66, s66, 8\n"
      "s_max_i32 s66, s66, 0\nglobal_load_lds_dwordx4 v25, s[48:49] offset:3072\nv_lshl_add_u32 v25, v188, 7, v5\nv_and_b32 v116, s55, v64\ns_add_u32 m0, s53, 94208\nv_xor_b32 v64, v64, v116\n"
      "global_load_lds_dwordx4 v26, s[6:7] offset:0\nv_lshl_add_u32 v26, v194, 7, v4\nv_and_b32 v117, s55, v65\nv_xor_b32 v65, v65, v117\ns_waitcnt vmcnt(11) lgkmcnt(0)\ns_cbranch_vccnz .Lw1030\n"
      "s_waitcnt vmcnt(10)\n"
      ".Lw1030:\ns_barrier\ns_add_u32 m0, s54, 0\ns_nop 0\nglobal_load_lds_dwordx4 v236, s[50:51] offset:0\nds_read_b64 v[68:69], v30 offset:45056\n"
      "global_load_lds_dwordx4 v237, s[50:51] offset:1024\nglobal_load_lds_dwordx4 v238, s[50:51] offset:2048\nglobal_load_lds_dwordx4 v239, s[50:51] offset:3072\ns_add_u32 s50, s50, 212992\n"
      "s_addc_u32 s51, s51, 0\ns_add_u32 m0, s26, 20480\nv_max3_f16 v32, |v66|, |v67|, v32\nglobal_load_lds_dwordx4 v27, s[6:7] offset:0\nv_lshl_add_u32 v27, v197, 7, v4\n"
      "v_max3_f32 v33, |v66|, |v67|, v33\ns_cbranch_vccz .La1031\ns_add_u32 m0, s26, 24576\ns_nop 0\nglobal_load_lds_dwordx4 v28, s[6:7] offset:0\n"
      ".La1031:\nv_lshl_add_u32 v28, v203, 7, v4\nds_read2_b64 v[200:203], v9 offset0:8 offset1:9\ns_getpc_b64 s[34:35]\n"
      ".Lk1005:\ns_add_u32 m0, s53, 0\ns_nop 0\nglobal_load_lds_dwordx4 v22, s[6:7] offset:0\nv_lshl_add_u32 v22, v179, 7, v4\nds_read2_b64 v[176:179], v6 offset0:8 offset1:9\n"
      "s_add_u32 s34, s34, .Lt_p-.Lk1005\ns_addc_u32 s35, s35, 0\nglobal_load_lds_dwordx4 v23, s[44:45] offset:1024\nv_lshl_add_u32 v23, v181, 7, v5\nv_and_b32 v118, s55, v66\n"
      "global_load_lds_dwordx4 v24, s[46:47] offset:2048\nv_lshl_add_u32 v24, v187, 7, v4\nds_read2_b64 v[184:187], v7 offset0:8 offset1:9\nv_xor_b32 v66, v66, v118\n"
      "global_load_lds_dwordx4 v25, s[48:49] offset:3072\nv_lshl_add_u32 v25, v189, 7, v5\ns_add_u32 m0, s53, 4096\nv_and_b32 v119, s55, v67\nglobal_load_lds_dwordx4 v26, s[6:7] offset:0\n"
      "v_lshl_add_u32 v26, v195, 7, v4\nds_read2_b64 v[192:195], v8 offset0:8 offset1:9\nv_xor_b32 v67, v67, v119\ns_waitcnt vmcnt(11) lgkmcnt(0)\ns_cbranch_vccnz .Lw1032\ns_waitcnt vmcnt(10)\n"
      ".Lw1032:\ns_barrier\ns_add_u32 m0, s54, 45056\ns_nop 0\nglobal_load_lds_dwordx4 v236, s[50:51] offset:0\nds_read_b64 v[70:71], v31 offset:0\n"
      "global_load_lds_dwordx4 v237, s[50:51] offset:1024\nglobal_load_lds_dwordx4 v238, s[50:51] offset:2048\nglobal_load_lds_dwordx4 v239, s[50:51] offset:3072\ns_add_u32 s50, s50, 212992\n"
      "s_addc_u32 s51, s51, 0\ns_cmp_eq_u32 s27, 10\ns_cselect_b64 s[50:51], s[10:11], s[50:51]\nv_max3_f16 v32, |v68|, |v69|, v32\ns_add_u32 m0, s26, 65536\nv_max3_f32 v33, |v68|, |v69|, v33\n"
      "global_load_lds_dwordx4 v27, s[6:7] offset:0\nv_lshl_add_u32 v27, v198, 7, v4\ns_lshr_b32 s42, s2, 3\ns_lshl1_add_u32 s42, s2, s42\ns_cbranch_vccz .La1033\ns_add_u32 m0, s26, 69632\n"
      "s_nop 0\nglobal_load_lds_dwordx4 v28, s[6:7] offset:0\n"
      ".La1033:\nv_lshl_add_u32 v28, v200, 7, v4\ns_and_b32 s69, s42, 15\ns_lshl_b32 s43, s69, 7\ns_add_u32 m0, s53, 45056\ns_nop 0\nglobal_load_lds_dwordx4 v22, s[6:7] offset:0\n"
      "v_lshl_add_u32 v22, v176, 7, v4\ns_add_u32 s43, s43, 32768\ns_add_u32 s8, s20, s43\ns_addc_u32 s9, s21, 0\nglobal_load_lds_dwordx4 v23, s[44:45] offset:1024\n"
      "v_lshl_add_u32 v23, v182, 7, v5\ns_add_u32 s12, s20, 65536\ns_addc_u32 s13, s21, 0\ns_add_u32 s16, s20, 2097152\ns_addc_u32 s17, s21, 0\nglobal_load_lds_dwordx4 v24, s[46:47] offset:2048\n"
      "v_lshl_add_u32 v24, v184, 7, v4\nv_and_b32 v120, s55, v68\nv_xor_b32 v68, v68, v120\nglobal_load_lds_dwordx4 v25, s[48:49] offset:3072\nv_lshl_add_u32 v25, v190, 7, v5\n"
      "s_add_u32 m0, s53, 49152\nv_and_b32 v121, s55, v69\nglobal_load_lds_dwordx4 v26, s[6:7] offset:0\nv_lshl_add_u32 v26, v192, 7, v4\nv_xor_b32 v69, v69, v121\ns_waitcnt vmcnt(11) lgkmcnt(0)\n"
      "s_cbranch_vccnz .Lw1034\ns_waitcnt vmcnt(10)\n"
      ".Lw1034:\ns_barrier\ns_add_u32 m0, s54, 90112\ns_nop 0\nglobal_load_lds_dwordx4 v236, s[50:51] offset:0\nds_read_b64 v[72:73], v30 offset:0\n"
      "global_load_lds_dwordx4 v237, s[50:51] offset:1024\nglobal_load_lds_dwordx4 v238, s[50:51] offset:2048\nglobal_load_lds_dwordx4 v239, s[50:51] offset:3072\ns_add_u32 s50, s50, 212992\n"
      "s_addc_u32 s51, s51, 0\nv_max3_f16 v32, |v70|, |v71|, v32\ns_add_u32 m0, s26, 110592\nv_max3_f32 v33, |v70|, |v71|, v33\nglobal_load_lds_dwordx4 v27, s[6:7] offset:0\n"
      "v_lshl_add_u32 v27, v199, 7, v4\nds_read2_b64 v[196:199], v8 offset0:121 offset1:122\ns_lshl_b32 s42, s69, 4\ns_max_u32 s42, s42, 24\ns_cbranch_vccz .La1035\ns_add_u32 m0, s26, 114688\n"
      "s_nop 0\nglobal_load_lds_dwordx4 v28, s[6:7] offset:0\n"
      ".La1035:\nv_lshl_add_u32 v28, v201, 7, v4\ns_sub_u32 s42, s42, 24\ns_min_u32 s69, s42, 192\ns_add_u32 m0, s53, 90112\ns_nop 0\nglobal_load_lds_dwordx4 v22, s[6:7] offset:0\n"
      "v_lshl_add_u32 v22, v177, 7, v4\ns_lshl_b32 s42, s69, 3\ns_add_u32 s42, s42, 24576\nglobal_load_lds_dwordx4 v23, s[44:45] offset:1024\nv_lshl_add_u32 v23, v183, 7, v5\n"
      "ds_read2_b64 v[180:183], v6 offset0:121 offset1:122\ns_add_u32 s14, s20, s42\ns_addc_u32 s15, s21, 0\nv_and_b32 v122, s55, v70\nglobal_load_lds_dwordx4 v24, s[46:47] offset:2048\n"
      "v_lshl_add_u32 v24, v185, 7, v4\nv_xor_b32 v70, v70, v122\nglobal_load_lds_dwordx4 v25, s[48:49] offset:3072\nv_lshl_add_u32 v25, v191, 7, v5\n"
      "ds_read2_b64 v[188:191], v7 offset0:121 offset1:122\ns_add_u32 m0, s53, 94208\nv_and_b32 v123, s55, v71\nglobal_load_lds_dwordx4 v26, s[6:7] offset:0\nv_lshl_add_u32 v26, v193, 7, v4\n"
      "v_xor_b32 v71, v71, v123\ns_waitcnt vmcnt(11) lgkmcnt(0)\ns_cbranch_vccnz .Lw1036\ns_waitcnt vmcnt(10)\n"
      ".Lw1036:\ns_barrier\ns_add_u32 m0, s54, 0\ns_nop 0\nglobal_load_lds_dwordx4 v236, s[50:51] offset:0\nds_read_b64 v[74:75], v30 offset:45056\n"
      "global_load_lds_dwordx4 v237, s[50:51] offset:1024\nglobal_load_lds_dwordx4 v238, s[50:51] offset:2048\nglobal_load_lds_dwordx4 v239, s[50:51] offset:3072\ns_add_u32 s50, s50, 212992\n"
      "s_addc_u32 s51, s51, 0\ns_cmp_eq_u32 s27, 8\ns_cselect_b64 s[50:51], s[10:11], s[50:51]\nv_max3_f16 v32, |v72|, |v73|, v32\ns_add_u32 m0, s26, 20480\nv_max3_f32 v33, |v72|, |v73|, v33\n"
      "global_load_lds_dwordx4 v27, s[6:7] offset:0\nv_lshl_add_u32 v27, v196, 7, v4\ns_lshl_b32 s42, s2, 3\ns_add_u32 s42, s42, 4096\ns_cbranch_vccz .La1037\ns_add_u32 m0, s26, 24576\ns_nop 0\n"
      "global_load_lds_dwordx4 v28, s[6:7] offset:0\n"
      ".La1037:\nv_lshl_add_u32 v28, v202, 7, v4\ns_add_u32 s58, s20, s42\ns_addc_u32 s59, s21, 0\ns_add_u32 m0, s53, 0\nv_and_b32 v38, 7, v240\nglobal_load_lds_dwordx4 v22, s[6:7] offset:0\n"
      "v_lshl_add_u32 v22, v178, 7, v4\nv_lshlrev_b32 v38, 11, v38\ns_xor_b32 s40, s37, 0xa5c30f96\nglobal_load_lds_dwordx4 v23, s[44:45] offset:1024\nv_lshl_add_u32 v23, v180, 7, v5\n"
      "s_mul_i32 s41, s37, 0x9E3779B1\ns_lshr_b32 s41, s41, 24\nglobal_load_lds_dwordx4 v24, s[46:47] offset:2048\nv_lshl_add_u32 v24, v186, 7, v4\nv_and_b32 v124, s55, v72\n"
      "v_xor_b32 v72, v72, v124\nglobal_load_lds_dwordx4 v25, s[48:49] offset:3072\nv_lshl_add_u32 v25, v188, 7, v5\ns_add_u32 m0, s53, 4096\nv_and_b32 v125, s55, v73\n"
      "global_load_lds_dwordx4 v26, s[6:7] offset:0\nv_lshl_add_u32 v26, v194, 7, v4\nv_xor_b32 v73, v73, v125\ns_waitcnt vmcnt(11) lgkmcnt(0)\ns_cbranch_vccnz .Lw1038\ns_waitcnt vmcnt(10)\n"
      ".Lw1038:\ns_barrier\ns_add_u32 m0, s54, 45056\ns_nop 0\nglobal_load_lds_dwordx4 v236, s[50:51] offset:0\nds_read_b64 v[76:77], v31 offset:0\n"
      "global_load_lds_dwordx4 v237, s[50:51] offset:1024\nglobal_load_lds_dwordx4 v238, s[50:51] offset:2048\nglobal_load_lds_dwordx4 v239, s[50:51] offset:3072\ns_add_u32 s50, s50, 212992\n"
      "s_addc_u32 s51, s51, 0\nv_max3_f16 v32, |v74|, |v75|, v32\ns_add_u32 m0, s26, 65536\nv_max3_f32 v33, |v74|, |v75|, v33\nglobal_load_lds_dwordx4 v27, s[6:7] offset:0\n"
      "v_lshl_add_u32 v27, v197, 7, v4\nv_lshrrev_b32 v164, 2, v240\nv_and_b32 v148, 3, v240\ns_cbranch_vccz .La1039\ns_add_u32 m0, s26, 69632\ns_nop 0\n"
      "global_load_lds_dwordx4 v28, s[6:7] offset:0\n"
      ".La1039:\nv_lshl_add_u32 v28, v203, 7, v4\nds_read2_b64 v[200:203], v9 offset0:10 offset1:11\nv_lshl_add_u32 v148, v148, 3, s65\ns_add_u32 m0, s53, 45056\nv_mul_u32_u24 v164, 928, v164\n"
      "global_load_lds_dwordx4 v22, s[6:7] offset:0\nv_lshl_add_u32 v22, v179, 7, v4\nds_read2_b64 v[176:179], v6 offset0:10 offset1:11\nv_add_u32 v149, s65, v164\nv_add_u32 v148, v148, v164\n"
      "global_load_lds_dwordx4 v23, s[44:45] offset:1024\nv_lshl_add_u32 v23, v181, 7, v5\ns_add_u32 s42, s65, 7424\nv_add_u32 v163, s42, v252\nglobal_load_lds_dwordx4 v24, s[46:47] offset:2048\n"
      "v_lshl_add_u32 v24, v187, 7, v4\nds_read2_b64 v[184:187], v7 offset0:10 offset1:11\nv_and_b32 v126, s55, v74\nv_xor_b32 v74, v74, v126\nglobal_load_lds_dwordx4 v25, s[48:49] offset:3072\n"
      "v_lshl_add_u32 v25, v189, 7, v5\ns_add_u32 m0, s53, 49152\nv_and_b32 v127, s55, v75\nglobal_load_lds_dwordx4 v26, s[6:7] offset:0\nv_lshl_add_u32 v26, v195, 7, v4\n"
      "ds_read2_b64 v[192:195], v8 offset0:10 offset1:11\nv_xor_b32 v75, v75, v127\ns_waitcnt vmcnt(11) lgkmcnt(0)\ns_cbranch_vccnz .Lw1040\ns_waitcnt vmcnt(10)\n"
      ".Lw1040:\ns_barrier\ns_add_u32 m0, s54, 90112\ns_nop 0\nglobal_load_lds_dwordx4 v236, s[50:51] offset:0\nds_read_b64 v[78:79], v30 offset:0\n"
      "global_load_lds_dwordx4 v237, s[50:51] offset:1024\nglobal_load_lds_dwordx4 v238, s[50:51] offset:2048\nglobal_load_lds_dwordx4 v239, s[50:51] offset:3072\ns_add_u32 s50, s50, 212992\n"
      "s_addc_u32 s51, s51, 0\ns_cmp_eq_u32 s27, 6\ns_cselect_b64 s[50:51], s[10:11], s[50:51]\nv_max3_f16 v32, |v76|, |v77|, v32\ns_add_u32 m0, s26, 110592\nv_max3_f32 v33, |v76|, |v77|, v33\n"
      "global_load_lds_dwordx4 v27, s[6:7] offset:0\nv_lshl_add_u32 v27, v198, 7, v4\ns_mul_i32 s42, s36, 10752\ns_add_u32 s56, s42, 0\ns_cbranch_vccz .La1041\ns_add_u32 m0, s26, 114688\ns_nop 0\n"
      "global_load_lds_dwordx4 v28, s[6:7] offset:0\n"
      ".La1041:\nv_lshl_add_u32 v28, v200, 7, v4\ns_add_u32 s42, s42, 1048576\ns_add_u32 m0, s53, 90112\ns_nop 0\nglobal_load_lds_dwordx4 v22, s[6:7] offset:0\nv_lshl_add_u32 v22, v176, 7, v4\n"
      "s_add_u32 s60, s20, s42\ns_addc_u32 s61, s21, 0\nglobal_load_lds_dwordx4 v23, s[44:45] offset:1024\nv_lshl_add_u32 v23, v182, 7, v5\nv_and_b32 v128, s55, v76\n"
      "global_load_lds_dwordx4 v24, s[46:47] offset:2048\nv_lshl_add_u32 v24, v184, 7, v4\nv_xor_b32 v76, v76, v128\nglobal_load_lds_dwordx4 v25, s[48:49] offset:3072\n"
      "v_lshl_add_u32 v25, v190, 7, v5\ns_add_u32 m0, s53, 94208\nv_and_b32 v129, s55, v77\nglobal_load_lds_dwordx4 v26, s[6:7] offset:0\nv_lshl_add_u32 v26, v192, 7, v4\nv_xor_b32 v77, v77, v129\n"
      "s_waitcnt vmcnt(11) lgkmcnt(0)\ns_cbranch_vccnz .Lw1042\ns_waitcnt vmcnt(10)\n"
      ".Lw1042:\ns_barrier\ns_add_u32 m0, s54, 0\ns_nop 0\nglobal_load_lds_dwordx4 v236, s[50:51] offset:0\nds_read_b64 v[80:81], v30 offset:45056\n"
      "global_load_lds_dwordx4 v237, s[50:51] offset:1024\nglobal_load_lds_dwordx4 v238, s[50:51] offset:2048\nglobal_load_lds_dwordx4 v239, s[50:51] offset:3072\ns_add_u32 s50, s50, 212992\n"
      "s_addc_u32 s51, s51, 0\nv_max3_f16 v32, |v78|, |v79|, v32\nv_max3_f32 v33, |v78|, |v79|, v33\ns_add_u32 s42, s27, 0\ns_add_u32 m0, s26, 20480\ns_nop 0\n"
      "global_load_lds_dwordx4 v27, s[6:7] offset:0\nv_lshl_add_u32 v27, v199, 7, v4\nds_read2_b64 v[196:199], v8 offset0:123 offset1:124\ns_sub_u32 s43, s42, 26\ns_min_u32 s42, s42, s43\n"
      "v_lshl_add_u32 v150, s42, 5, v148\ns_cbranch_vccz .La1043\ns_add_u32 m0, s26, 24576\ns_nop 0\nglobal_load_lds_dwordx4 v28, s[6:7] offset:0\n"
      ".La1043:\nv_lshl_add_u32 v28, v201, 7, v4\ns_add_u32 s42, s27, 2\ns_sub_u32 s43, s42, 26\ns_add_u32 m0, s53, 0\ns_nop 0\nglobal_load_lds_dwordx4 v22, s[6:7] offset:0\n"
      "v_lshl_add_u32 v22, v177, 7, v4\ns_min_u32 s42, s42, s43\nv_lshl_add_u32 v151, s42, 5, v148\nglobal_load_lds_dwordx4 v23, s[44:45] offset:1024\nv_lshl_add_u32 v23, v183, 7, v5\n"
      "ds_read2_b64 v[180:183], v6 offset0:123 offset1:124\ns_add_u32 s42, s27, 4\ns_sub_u32 s43, s42, 26\nglobal_load_lds_dwordx4 v24, s[46:47] offset:2048\nv_lshl_add_u32 v24, v185, 7, v4\n"
      "s_min_u32 s42, s42, s43\nv_lshl_add_u32 v152, s42, 5, v148\nglobal_load_lds_dwordx4 v25, s[48:49] offset:3072\nv_lshl_add_u32 v25, v191, 7, v5\n"
      "ds_read2_b64 v[188:191], v7 offset0:123 offset1:124\nv_and_b32 v130, s55, v78\ns_add_u32 m0, s53, 4096\nv_xor_b32 v78, v78, v130\nglobal_load_lds_dwordx4 v26, s[6:7] offset:0\n"
      "v_lshl_add_u32 v26, v193, 7, v4\nv_and_b32 v131, s55, v79\nv_xor_b32 v79, v79, v131\ns_waitcnt vmcnt(11) lgkmcnt(0)\ns_cbranch_vccnz .Lw1044\ns_waitcnt vmcnt(10)\n"
      ".Lw1044:\ns_barrier\ns_add_u32 m0, s54, 45056\ns_nop 0\nglobal_load_lds_dwordx4 v236, s[50:51] offset:0\nds_read_b64 v[82:83], v31 offset:0\n"
      "global_load_lds_dwordx4 v237, s[50:51] offset:1024\nglobal_load_lds_dwordx4 v238, s[50:51] offset:2048\nglobal_load_lds_dwordx4 v239, s[50:51] offset:3072\ns_add_u32 s50, s50, 212992\n"
      "s_addc_u32 s51, s51, 0\ns_cmp_eq_u32 s27, 4\ns_cselect_b64 s[50:51], s[10:11], s[50:51]\nv_max3_f16 v32, |v80|, |v81|, v32\nv_max3_f32 v33, |v80|, |v81|, v33\ns_add_u32 s42, s27, 6\n"
      "s_add_u32 m0, s26, 65536\ns_nop 0\nglobal_load_lds_dwordx4 v27, s[6:7] offset:0\nv_lshl_add_u32 v27, v196, 7, v4\ns_sub_u32 s43, s42, 26\ns_min_u32 s42, s42, s43\n"
      "v_lshl_add_u32 v153, s42, 5, v148\ns_cbranch_vccz .La1045\ns_add_u32 m0, s26, 69632\ns_nop 0\nglobal_load_lds_dwordx4 v28, s[6:7] offset:0\n"
      ".La1045:\nv_lshl_add_u32 v28, v202, 7, v4\ns_add_u32 s42, s27, 8\ns_sub_u32 s43, s42, 26\ns_add_u32 m0, s53, 45056\ns_nop 0\nglobal_load_lds_dwordx4 v22, s[6:7] offset:0\n"
      "v_lshl_add_u32 v22, v178, 7, v4\ns_min_u32 s42, s42, s43\nv_lshl_add_u32 v154, s42, 5, v148\nglobal_load_lds_dwordx4 v23, s[44:45] offset:1024\nv_lshl_add_u32 v23, v180, 7, v5\n"
      "s_add_u32 s42, s27, 10\ns_sub_u32 s43, s42, 26\nglobal_load_lds_dwordx4 v24, s[46:47] offset:2048\nv_lshl_add_u32 v24, v186, 7, v4\ns_min_u32 s42, s42, s43\n"
      "v_lshl_add_u32 v155, s42, 5, v148\nglobal_load_lds_dwordx4 v25, s[48:49] offset:3072\nv_lshl_add_u32 v25, v188, 7, v5\nv_and_b32 v132, s55, v80\ns_add_u32 m0, s53, 49152\n"
      "v_xor_b32 v80, v80, v132\nglobal_load_lds_dwordx4 v26, s[6:7] offset:0\nv_lshl_add_u32 v26, v194, 7, v4\nv_and_b32 v133, s55, v81\nv_xor_b32 v81, v81, v133\ns_cmp_lg_u32 s24, 3\n"
      "s_cbranch_scc1 .Lo1046\nglobal_load_dwordx2 v[36:37], v38, s[58:59] sc1\n"
      ".Lo1046:\ns_waitcnt vmcnt(11) lgkmcnt(0)\ns_cbranch_vccnz .Lw1047\ns_waitcnt vmcnt(10)\n"
      ".Lw1047:\ns_barrier\ns_add_u32 m0, s54, 90112\ns_nop 0\nglobal_load_lds_dwordx4 v236, s[50:51] offset:0\nds_read_b64 v[84:85], v30 offset:0\n"
      "global_load_lds_dwordx4 v237, s[50:51] offset:1024\nglobal_load_lds_dwordx4 v238, s[50:51] offset:2048\nglobal_load_lds_dwordx4 v239, s[50:51] offset:3072\ns_add_u32 s50, s50, 212992\n"
      "s_addc_u32 s51, s51, 0\nv_max3_f16 v32, |v82|, |v83|, v32\nv_max3_f32 v33, |v82|, |v83|, v33\ns_add_u32 s42, s27, 12\ns_add_u32 m0, s26, 110592\ns_nop 0\n"
      "global_load_lds_dwordx4 v27, s[6:7] offset:0\nv_lshl_add_u32 v27, v197, 7, v4\ns_sub_u32 s43, s42, 26\ns_min_u32 s42, s42, s43\nv_lshl_add_u32 v156, s42, 5, v148\ns_cbranch_vccz .La1048\n"
      "s_add_u32 m0, s26, 114688\ns_nop 0\nglobal_load_lds_dwordx4 v28, s[6:7] offset:0\n"
      ".La1048:\nv_lshl_add_u32 v28, v203, 7, v4\nds_read2_b64 v[200:203], v9 offset0:12 offset1:13\ns_add_u32 s42, s27, 14\ns_sub_u32 s43, s42, 26\ns_add_u32 m0, s53, 90112\ns_nop 0\n"
      "global_load_lds_dwordx4 v22, s[6:7] offset:0\nv_lshl_add_u32 v22, v179, 7, v4\nds_read2_b64 v[176:179], v6 offset0:12 offset1:13\ns_min_u32 s42, s42, s43\nv_lshl_add_u32 v157, s42, 5, v148\n"
      "global_load_lds_dwordx4 v23, s[44:45] offset:1024\nv_lshl_add_u32 v23, v181, 7, v5\ns_add_u32 s42, s27, 16\ns_sub_u32 s43, s42, 26\nglobal_load_lds_dwordx4 v24, s[46:47] offset:2048\n"
      "v_lshl_add_u32 v24, v187, 7, v4\nds_read2_b64 v[184:187], v7 offset0:12 offset1:13\ns_min_u32 s42, s42, s43\nv_lshl_add_u32 v158, s42, 5, v148\n"
      "global_load_lds_dwordx4 v25, s[48:49] offset:3072\nv_lshl_add_u32 v25, v189, 7, v5\nv_and_b32 v134, s55, v82\ns_add_u32 m0, s53, 94208\nv_xor_b32 v82, v82, v134\n"
      "global_load_lds_dwordx4 v26, s[6:7] offset:0\nv_lshl_add_u32 v26, v195, 7, v4\nds_read2_b64 v[192:195], v8 offset0:12 offset1:13\nv_and_b32 v135, s55, v83\nv_xor_b32 v83, v83, v135\n"
      "s_waitcnt vmcnt(11) lgkmcnt(0)\ns_cbranch_vccnz .Lw1049\ns_waitcnt vmcnt(10)\n"
      ".Lw1049:\ns_barrier\ns_add_u32 m0, s54, 0\ns_nop 0\nglobal_load_lds_dwordx4 v236, s[50:51] offset:0\nds_read_b64 v[86:87], v30 offset:45056\n"
      "global_load_lds_dwordx4 v237, s[50:51] offset:1024\nglobal_load_lds_dwordx4 v238, s[50:51] offset:2048\nglobal_load_lds_dwordx4 v239, s[50:51] offset:3072\ns_add_u32 s50, s50, 212992\n"
      "s_addc_u32 s51, s51, 0\ns_cmp_eq_u32 s27, 2\ns_cselect_b64 s[50:51], s[10:11], s[50:51]\nv_max3_f16 v32, |v84|, |v85|, v32\nv_max3_f32 v33, |v84|, |v85|, v33\ns_add_u32 s42, s27, 18\n"
      "s_add_u32 m0, s26, 20480\ns_nop 0\nglobal_load_lds_dwordx4 v27, s[6:7] offset:0\nv_lshl_add_u32 v27, v198, 7, v4\ns_sub_u32 s43, s42, 26\ns_min_u32 s42, s42, s43\n"
      "v_lshl_add_u32 v159, s42, 5, v148\ns_cbranch_vccz .La1050\ns_add_u32 m0, s26, 24576\ns_nop 0\nglobal_load_lds_dwordx4 v28, s[6:7] offset:0\n"
      ".La1050:\nv_lshl_add_u32 v28, v200, 7, v4\ns_add_u32 s42, s27, 20\ns_sub_u32 s43, s42, 26\ns_add_u32 m0, s53, 0\ns_nop 0\nglobal_load_lds_dwordx4 v22, s[6:7] offset:0\n"
      "v_lshl_add_u32 v22, v176, 7, v4\ns_min_u32 s42, s42, s43\nv_lshl_add_u32 v160, s42, 5, v148\nglobal_load_lds_dwordx4 v23, s[44:45] offset:1024\nv_lshl_add_u32 v23, v182, 7, v5\n"
      "s_add_u32 s42, s27, 22\ns_sub_u32 s43, s42, 26\nglobal_load_lds_dwordx4 v24, s[46:47] offset:2048\nv_lshl_add_u32 v24, v184, 7, v4\ns_min_u32 s42, s42, s43\n"
      "v_lshl_add_u32 v161, s42, 5, v148\nglobal_load_lds_dwordx4 v25, s[48:49] offset:3072\nv_lshl_add_u32 v25, v190, 7, v5\nv_and_b32 v136, s55, v84\ns_add_u32 m0, s53, 4096\n"
      "v_xor_b32 v84, v84, v136\nglobal_load_lds_dwordx4 v26, s[6:7] offset:0\nv_lshl_add_u32 v26, v192, 7, v4\nv_and_b32 v137, s55, v85\nv_xor_b32 v85, v85, v137\ns_waitcnt vmcnt(11) lgkmcnt(0)\n"
      "s_cbranch_vccnz .Lw1051\ns_waitcnt vmcnt(10)\n"
      ".Lw1051:\ns_barrier\ns_add_u32 m0, s54, 45056\ns_nop 0\nglobal_load_lds_dwordx4 v236, s[50:51] offset:0\nds_read_b64 v[88:89], v31 offset:0\n"
      "global_load_lds_dwordx4 v237, s[50:51] offset:1024\nglobal_load_lds_dwordx4 v238, s[50:51] offset:2048\nglobal_load_lds_dwordx4 v239, s[50:51] offset:3072\ns_add_u32 s50, s50, 212992\n"
      "s_addc_u32 s51, s51, 0\nv_max3_f16 v32, |v86|, |v87|, v32\ns_add_u32 m0, s26, 65536\nv_max3_f32 v33, |v86|, |v87|, v33\nglobal_load_lds_dwordx4 v27, s[6:7] offset:0\n"
      "v_lshl_add_u32 v27, v199, 7, v4\ns_add_u32 s42, s27, 24\ns_sub_u32 s43, s42, 26\ns_cbranch_vccz .La1052\ns_add_u32 m0, s26, 69632\ns_nop 0\nglobal_load_lds_dwordx4 v28, s[6:7] offset:0\n"
      ".La1052:\nv_lshl_add_u32 v28, v201, 7, v4\ns_min_u32 s42, s42, s43\ns_add_u32 m0, s53, 45056\ns_nop 0\nglobal_load_lds_dwordx4 v22, s[6:7] offset:0\nv_lshl_add_u32 v22, v177, 7, v4\n"
      "v_lshl_add_u32 v162, s42, 5, v148\nglobal_load_lds_dwordx4 v23, s[44:45] offset:1024\nv_lshl_add_u32 v23, v183, 7, v5\nv_and_b32 v138, s55, v86\n"
      "global_load_lds_dwordx4 v24, s[46:47] offset:2048\nv_lshl_add_u32 v24, v185, 7, v4\nv_xor_b32 v86, v86, v138\nglobal_load_lds_dwordx4 v25, s[48:49] offset:3072\n"
      "v_lshl_add_u32 v25, v191, 7, v5\ns_add_u32 m0, s53, 49152\nv_and_b32 v139, s55, v87\nglobal_load_lds_dwordx4 v26, s[6:7] offset:0\nv_lshl_add_u32 v26, v193, 7, v4\nv_xor_b32 v87, v87, v139\n"
      "s_cmp_lg_u32 s24, 3\ns_cbranch_scc1 .Lo1055\ns_mov_b32 s42, 0\n"
      ".Lq1053:\nv_and_b32 v164, 0xff, v36\nv_xor_b32 v164, s41, v164\nv_xor_b32 v165, s40, v37\nv_or_b32 v164, v164, v165\nv_cmp_ne_u32 s[62:63], 0, v164\nv_lshrrev_b32 v164, 8, v36\n"
      "s_cmp_eq_u64 s[62:63], 0\ns_cbranch_scc1 .Lr1054\ns_sleep 1\nglobal_load_dwordx2 v[36:37], v38, s[58:59] sc1\ns_add_u32 s42, s42, 1\ns_bitcmp0_b32 s42, 22\ns_waitcnt vmcnt(0)\n"
      "s_cbranch_scc1 .Lq1053\n"
      ".Lr1054:\nds_write_b32 v253, v164 offset:24\n"
      ".Lo1055:\ns_waitcnt vmcnt(11) lgkmcnt(0)\ns_cbranch_vccnz .Lw1056\ns_waitcnt vmcnt(10)\n"
      ".Lw1056:\ns_barrier\ns_add_u32 m0, s54, 90112\ns_nop 0\nglobal_load_lds_dwordx4 v236, s[50:51] offset:0\nds_read_b64 v[90:91], v30 offset:0\n"
      "global_load_lds_dwordx4 v237, s[50:51] offset:1024\nglobal_load_lds_dwordx4 v238, s[50:51] offset:2048\nglobal_load_lds_dwordx4 v239, s[50:51] offset:3072\n"
      "v_max3_f16 v32, |v88|, |v89|, v32\nv_max3_f32 v33, |v88|, |v89|, v33\nv_and_b32 v140, s55, v88\ns_add_u32 m0, s26, 110592\nv_xor_b32 v88, v88, v140\n"
      "global_load_lds_dwordx4 v27, s[6:7] offset:0\nv_and_b32 v141, s55, v89\nv_xor_b32 v89, v89, v141\ns_lshl_b32 s42, s24, 6\nv_add_u32 v10, s42, v240\ns_cbranch_vccz .La1057\n"
      "s_add_u32 m0, s26, 114688\ns_nop 0\nglobal_load_lds_dwordx4 v28, s[6:7] offset:0\n"
      ".La1057:\nv_min_u32 v10, 83, v10\nv_and_b32 v11, 3, v10\nv_lshrrev_b32 v12, 2, v10\ns_add_u32 m0, s53, 90112\nv_mul_u32_u24 v11, 22272, v11\nglobal_load_lds_dwordx4 v22, s[6:7] offset:0\n"
      "v_mul_u32_u24 v13, 928, v12\ns_mov_b32 s42, 45920\nv_add3_u32 v11, v11, v13, s42\nglobal_load_lds_dwordx4 v23, s[44:45] offset:1024\ns_mov_b32 s42, 136192\nv_lshl_add_u32 v13, v10, 4, s42\n"
      "s_mov_b32 s42, 160448\nglobal_load_lds_dwordx4 v24, s[46:47] offset:2048\nv_lshl_add_u32 v10, v10, 4, s42\nv_min_u32 v14, 1, v12\nv_lshl_add_u32 v10, v14, 6, v10\n"
      "global_load_lds_dwordx4 v25, s[48:49] offset:3072\ns_bfm_b32 s42, s64, 0\nv_lshrrev_b32 v15, v12, s42\ns_add_u32 m0, s53, 94208\nv_subrev_u32 v14, s64, v12\n"
      "global_load_lds_dwordx4 v26, s[6:7] offset:0\nv_lshl_add_u32 v12, v240, 1, v248\nds_read_u16 v251, v12 offset:64\nds_read_b32 v9, v248 offset:24\ns_waitcnt vmcnt(11) lgkmcnt(0)\n"
      "s_cbranch_vccnz .Lw1058\ns_waitcnt vmcnt(10)\n"
      ".Lw1058:\ns_barrier\ns_add_u32 m0, s56, 0\ns_nop 0\nglobal_load_lds_dwordx4 v252, s[60:61] offset:0 sc1\nds_read_b64 v[92:93], v30 offset:45056\n"
      "global_load_lds_dwordx4 v252, s[60:61] offset:1024 sc1\nglobal_load_lds_dwordx4 v252, s[60:61] offset:2048 sc1\nglobal_load_lds_dwordx4 v252, s[60:61] offset:3072 sc1\n"
      "s_add_u32 s60, s60, 4096\ns_addc_u32 s61, s61, 0\nv_max3_f16 v32, |v90|, |v91|, v32\nv_max3_f32 v33, |v90|, |v91|, v33\nv_and_b32 v142, s55, v90\ns_add_u32 m0, s56, 4096\n"
      "v_xor_b32 v90, v90, v142\nglobal_load_lds_dwordx4 v252, s[60:61] offset:0 sc1\nv_and_b32 v143, s55, v91\nv_xor_b32 v91, v91, v143\nv_readfirstlane_b32 s39, v9\nv_readlane_b32 s57, v251, 0\n"
      "global_load_lds_dwordx4 v252, s[60:61] offset:1024 sc1\ns_xor_b32 s40, s39, 0xa5c30f96\ns_mul_i32 s41, s39, 0x9E3779B1\ns_lshr_b32 s41, s41, 24\ns_lshl_b32 s42, s2, 3\n"
      "global_load_lds_dwordx4 v252, s[60:61] offset:2048 sc1\ns_add_u32 s42, s42, 24576\nv_mov_b32 v7, s42\nv_mov_b32 v8, s41\nv_mov_b32 v9, s40\n"
      "global_load_lds_dwordx4 v252, s[60:61] offset:3072 sc1\ns_add_u32 s60, s60, 4096\ns_addc_u32 s61, s61, 0\ns_lshl_b32 s42, s2, 2\nv_mov_b32 v12, s42\ns_add_u32 s42, s37, 1\n"
      "s_add_u32 m0, s56, 8192\ns_nop 0\nglobal_load_lds_dwordx4 v252, s[60:61] offset:0 sc1\nv_mov_b32 v6, s42\nv_and_b32 v1, 48, v240\ns_mov_b32 s42, 41984\n"
      "global_load_lds_dwordx4 v252, s[60:61] offset:1024 sc1\nv_lshl_add_u32 v1, v1, 2, s42\ns_sub_u32 s49, s65, 928\n"
      "v_add_u32_sdwa v236, s49, v244 dst_sel:DWORD dst_unused:UNUSED_PAD src0_sel:DWORD src1_sel:WORD_0\nglobal_load_lds_dwordx4 v252, s[60:61] offset:1536 sc1\n"
      "v_add_u32_sdwa v237, s49, v244 dst_sel:DWORD dst_unused:UNUSED_PAD src0_sel:DWORD src1_sel:WORD_1\n"
      "v_add_u32_sdwa v238, s49, v245 dst_sel:DWORD dst_unused:UNUSED_PAD src0_sel:DWORD src1_sel:WORD_0\n"
      "v_add_u32_sdwa v239, s49, v245 dst_sel:DWORD dst_unused:UNUSED_PAD src0_sel:DWORD src1_sel:WORD_1\ns_waitcnt vmcnt(11) lgkmcnt(0)\ns_barrier\nds_read_b64 v[94:95], v31 offset:0\n"
      "v_max3_f16 v32, |v92|, |v93|, v32\nv_max3_f32 v33, |v92|, |v93|, v33\ns_nop 1\nv_and_b32 v144, s55, v92\nv_xor_b32 v92, v92, v144\ns_nop 1\nv_and_b32 v145, s55, v93\n"
      "v_xor_b32 v93, v93, v145\ns_lshl_b32 s42, s30, 6\ns_add_u32 s42, s42, s36\ns_add_u32 s42, s42, 60\ns_mulk_i32 s42, 896\ns_add_u32 s44, s16, s42\ns_addc_u32 s45, s17, 0\n"
      "s_waitcnt lgkmcnt(0)\nv_max3_f16 v32, |v94|, |v95|, v32\nv_max3_f32 v33, |v94|, |v95|, v33\ns_nop 1\nv_and_b32 v146, s55, v94\nv_xor_b32 v94, v94, v146\ns_nop 1\nv_and_b32 v147, s55, v95\n"
      "v_xor_b32 v95, v95, v147\ns_nop 1\nv_lshlrev_b32 v32, 16, v32\nv_max_u32 v33, v32, v33\ns_nop 1\nv_max_u32_dpp v33, v33, v33 quad_perm:[1,0,3,2] row_mask:0xf bank_mask:0xf\ns_nop 1\n"
      "v_max_u32_dpp v33, v33, v33 quad_perm:[2,3,0,1] row_mask:0xf bank_mask:0xf\nv_lshrrev_b32 v33, 23, v33\ns_nop 1\nv_subrev_u32 v32, 22, v33\nv_mul_u32_u24 v32, 0x800080, v32\ns_nop 1\n"
      "v_pk_sub_u16 v44, v44, v32 clamp\nv_pk_sub_u16 v45, v45, v32 clamp\ns_nop 1\nv_pk_sub_u16 v46, v46, v32 clamp\nv_pk_sub_u16 v47, v47, v32 clamp\ns_nop 1\nv_lshl_or_b32 v44, v44, 3, v96\n"
      "v_lshl_or_b32 v45, v45, 3, v97\ns_nop 1\nv_lshl_or_b32 v46, v46, 3, v98\nv_lshl_or_b32 v47, v47, 3, v99\ns_nop 1\nv_pk_sub_u16 v48, v48, v32 clamp\nv_pk_sub_u16 v49, v49, v32 clamp\n"
      "s_nop 1\nv_pk_sub_u16 v50, v50, v32 clamp\nv_pk_sub_u16 v51, v51, v32 clamp\ns_nop 1\nv_lshl_or_b32 v48, v48, 3, v100\nv_lshl_or_b32 v49, v49, 3, v101\ns_nop 1\n"
      "v_lshl_or_b32 v50, v50, 3, v102\nv_lshl_or_b32 v51, v51, 3, v103\ns_nop 1\nv_pk_sub_u16 v52, v52, v32 clamp\nv_pk_sub_u16 v53, v53, v32 clamp\ns_nop 1\nv_pk_sub_u16 v54, v54, v32 clamp\n"
      "v_pk_sub_u16 v55, v55, v32 clamp\ns_nop 1\nv_lshl_or_b32 v52, v52, 3, v104\nv_lshl_or_b32 v53, v53, 3, v105\ns_nop 1\nv_lshl_or_b32 v54, v54, 3, v106\nv_lshl_or_b32 v55, v55, 3, v107\n"
      "s_nop 1\nv_pk_sub_u16 v56, v56, v32 clamp\nv_pk_sub_u16 v57, v57, v32 clamp\ns_nop 1\nv_pk_sub_u16 v58, v58, v32 clamp\nv_pk_sub_u16 v59, v59, v32 clamp\ns_nop 1\n"
      "v_lshl_or_b32 v56, v56, 3, v108\nv_lshl_or_b32 v57, v57, 3, v109\ns_nop 1\nv_lshl_or_b32 v58, v58, 3, v110\nv_lshl_or_b32 v59, v59, 3, v111\ns_nop 1\nv_pk_sub_u16 v60, v60, v32 clamp\n"
      "v_pk_sub_u16 v61, v61, v32 clamp\ns_nop 1\nv_pk_sub_u16 v62, v62, v32 clamp\nv_pk_sub_u16 v63, v63, v32 clamp\ns_nop 1\nv_lshl_or_b32 v60, v60, 3, v112\nv_lshl_or_b32 v61, v61, 3, v113\n"
      "s_nop 1\nv_lshl_or_b32 v62, v62, 3, v114\nv_lshl_or_b32 v63, v63, 3, v115\ns_nop 1\nv_pk_sub_u16 v64, v64, v32 clamp\nv_pk_sub_u16 v65, v65, v32 clamp\ns_nop 1\n"
      "v_pk_sub_u16 v66, v66, v32 clamp\nv_pk_sub_u16 v67, v67, v32 clamp\ns_nop 1\nv_lshl_or_b32 v64, v64, 3, v116\nv_lshl_or_b32 v65, v65, 3, v117\ns_nop 1\nv_lshl_or_b32 v66, v66, 3, v118\n"
      "v_lshl_or_b32 v67, v67, 3, v119\ns_nop 1\nv_pk_sub_u16 v68, v68, v32 clamp\nv_pk_sub_u16 v69, v69, v32 clamp\ns_nop 1\nv_pk_sub_u16 v70, v70, v32 clamp\nv_pk_sub_u16 v71, v71, v32 clamp\n"
      "s_nop 1\nv_lshl_or_b32 v68, v68, 3, v120\nv_lshl_or_b32 v69, v69, 3, v121\ns_nop 1\nv_lshl_or_b32 v70, v70, 3, v122\nv_lshl_or_b32 v71, v71, 3, v123\ns_nop 1\n"
      "v_pk_sub_u16 v72, v72, v32 clamp\nv_pk_sub_u16 v73, v73, v32 clamp\ns_nop 1\nv_pk_sub_u16 v74, v74, v32 clamp\nv_pk_sub_u16 v75, v75, v32 clamp\ns_nop 1\nv_lshl_or_b32 v72, v72, 3, v124\n"
      "v_lshl_or_b32 v73, v73, 3, v125\ns_nop 1\nv_lshl_or_b32 v74, v74, 3, v126\nv_lshl_or_b32 v75, v75, 3, v127\ns_waitcnt vmcnt(0)\ns_barrier\nv_pk_sub_u16 v76, v76, v32 clamp\n"
      "v_pk_sub_u16 v77, v77, v32 clamp\ns_nop 1\nv_pk_sub_u16 v78, v78, v32 clamp\nv_pk_sub_u16 v79, v79, v32 clamp\ns_nop 1\nv_lshl_or_b32 v76, v76, 3, v128\nv_lshl_or_b32 v77, v77, 3, v129\n"
      "s_nop 1\nv_lshl_or_b32 v78, v78, 3, v130\nv_lshl_or_b32 v79, v79, 3, v131\ns_nop 1\nv_pk_sub_u16 v80, v80, v32 clamp\nv_pk_sub_u16 v81, v81, v32 clamp\ns_nop 1\n"
      "v_pk_sub_u16 v82, v82, v32 clamp\nv_pk_sub_u16 v83, v83, v32 clamp\ns_nop 1\nv_lshl_or_b32 v80, v80, 3, v132\nv_lshl_or_b32 v81, v81, 3, v133\ns_nop 1\nv_lshl_or_b32 v82, v82, 3, v134\n"
      "v_lshl_or_b32 v83, v83, 3, v135\ns_nop 1\nv_pk_sub_u16 v84, v84, v32 clamp\nv_pk_sub_u16 v85, v85, v32 clamp\ns_nop 1\nv_pk_sub_u16 v86, v86, v32 clamp\nv_pk_sub_u16 v87, v87, v32 clamp\n"
      "s_nop 1\nv_lshl_or_b32 v84, v84, 3, v136\nv_lshl_or_b32 v85, v85, 3, v137\ns_nop 1\nv_lshl_or_b32 v86, v86, 3, v138\nv_lshl_or_b32 v87, v87, 3, v139\ns_nop 1\n"
      "v_pk_sub_u16 v88, v88, v32 clamp\nv_pk_sub_u16 v89, v89, v32 clamp\ns_nop 1\nv_pk_sub_u16 v90, v90, v32 clamp\nv_pk_sub_u16 v91, v91, v32 clamp\ns_nop 1\nv_lshl_or_b32 v88, v88, 3, v140\n"
      "v_lshl_or_b32 v89, v89, 3, v141\ns_nop 1\nv_lshl_or_b32 v90, v90, 3, v142\nv_lshl_or_b32 v91, v91, 3, v143\ns_nop 1\nv_pk_sub_u16 v92, v92, v32 clamp\nv_pk_sub_u16 v93, v93, v32 clamp\n"
      "s_nop 1\nv_pk_sub_u16 v94, v94, v32 clamp\nv_pk_sub_u16 v95, v95, v32 clamp\ns_nop 1\nv_lshl_or_b32 v92, v92, 3, v144\nv_lshl_or_b32 v93, v93, 3, v145\ns_nop 1\n"
      "v_lshl_or_b32 v94, v94, 3, v146\nv_lshl_or_b32 v95, v95, 3, v147\ns_nop 1\nv_sub_u32 v4, 257, v33\nv_lshlrev_b32 v4, 23, v4\ns_nop 1\nv_cvt_pkrtz_f16_f32 v4, v4, v4\n"
      "v_lshlrev_b32 v33, 2, v33\ns_nop 1\nv_mov_b32 v5, v4\nv_subrev_u32 v33, 536, v33\ns_setprio 0\nds_write_b64 v148, v[4:5] offset:832\nds_write_b32 v149, v33 offset:864\n"
      "ds_write2_b64 v150, v[44:45], v[46:47] offset1:4\nds_write2_b64 v151, v[48:49], v[50:51] offset1:4\nds_write2_b64 v152, v[52:53], v[54:55] offset1:4\n"
      "ds_write2_b64 v153, v[56:57], v[58:59] offset1:4\nds_write2_b64 v154, v[60:61], v[62:63] offset1:4\nds_write2_b64 v155, v[64:65], v[66:67] offset1:4\n"
      "ds_write2_b64 v156, v[68:69], v[70:71] offset1:4\nds_write2_b64 v157, v[72:73], v[74:75] offset1:4\nds_write2_b64 v158, v[76:77], v[78:79] offset1:4\n"
      "ds_write2_b64 v159, v[80:81], v[82:83] offset1:4\nds_write2_b64 v160, v[84:85], v[86:87] offset1:4\nds_write2_b64 v161, v[88:89], v[90:91] offset1:4\n"
      "ds_write2_b64 v162, v[92:93], v[94:95] offset1:4\nds_read_b128 v[16:19], v163 offset:6496\nds_read_b128 v[20:23], v163 offset:5568\nds_read_b128 v[24:27], v163 offset:4640\n"
      "s_cmp_gt_u32 s64, 12\ns_cbranch_scc1 .Ld1060\nds_read_b128 v[28:31], v163 offset:3712\nds_read_b128 v[32:35], v163 offset:2784\nds_read_b128 v[36:39], v163 offset:1856\n"
      "ds_read_b128 v[40:43], v163 offset:928\nds_read_b128 v[44:47], v163 offset:0\n"
      ".Ld1060:\ns_mov_b32 exec_hi, 0x7fffff\ns_waitcnt lgkmcnt(0)\ns_cmp_gt_u32 s64, 15\ns_cbranch_scc1 .Ld1061\nglobal_store_dwordx4 v252, v[16:19], s[44:45] sc1\ns_sub_u32 s44, s44, 3584\n"
      "s_subb_u32 s45, s45, 0\ns_cmp_gt_u32 s64, 14\ns_cbranch_scc1 .Ld1061\nglobal_store_dwordx4 v252, v[20:23], s[44:45] sc1\ns_sub_u32 s44, s44, 3584\ns_subb_u32 s45, s45, 0\n"
      "s_cmp_gt_u32 s64, 13\ns_cbranch_scc1 .Ld1061\nglobal_store_dwordx4 v252, v[24:27], s[44:45] sc1\ns_sub_u32 s44, s44, 3584\ns_subb_u32 s45, s45, 0\ns_cmp_gt_u32 s64, 12\n"
      "s_cbranch_scc1 .Ld1061\nglobal_store_dwordx4 v252, v[28:31], s[44:45] sc1\ns_sub_u32 s44, s44, 3584\ns_subb_u32 s45, s45, 0\ns_cmp_gt_u32 s64, 11\ns_cbranch_scc1 .Ld1061\n"
      "global_store_dwordx4 v252, v[32:35], s[44:45] sc1\ns_sub_u32 s44, s44, 3584\ns_subb_u32 s45, s45, 0\ns_cmp_gt_u32 s64, 10\ns_cbranch_scc1 .Ld1061\n"
      "global_store_dwordx4 v252, v[36:39], s[44:45] sc1\ns_sub_u32 s44, s44, 3584\ns_subb_u32 s45, s45, 0\ns_cmp_gt_u32 s64, 9\ns_cbranch_scc1 .Ld1061\n"
      "global_store_dwordx4 v252, v[40:43], s[44:45] sc1\ns_sub_u32 s44, s44, 3584\ns_subb_u32 s45, s45, 0\ns_cmp_gt_u32 s64, 8\ns_cbranch_scc1 .Ld1061\n"
      "global_store_dwordx4 v252, v[44:47], s[44:45] sc1\ns_sub_u32 s44, s44, 3584\ns_subb_u32 s45, s45, 0\n"
      ".Ld1061:\ns_mov_b32 exec_hi, -1\nds_read_b128 v[48:51], v1 offset:0\nds_read_b128 v[52:55], v1 offset:16\nds_read_b128 v[56:59], v1 offset:32\nds_read_b128 v[60:63], v1 offset:48\n"
      "ds_read_b128 v[72:75], v252 offset:0\nds_read_b128 v[76:79], v252 offset:1024\nds_read_b128 v[80:83], v252 offset:2048\nds_read_b128 v[84:87], v252 offset:3072\n"
      "ds_read_b128 v[88:91], v252 offset:4096\nds_read_b128 v[92:95], v252 offset:5120\nds_read_b128 v[96:99], v252 offset:6144\nds_read_b128 v[100:103], v252 offset:7168\n"
      "ds_read_b128 v[104:107], v252 offset:8192\nds_read_b128 v[108:111], v252 offset:9216\nds_read_b128 v[112:115], v252 offset:10240\nds_read_b128 v[116:119], v252 offset:11264\n"
      "ds_read_b128 v[120:123], v252 offset:12288\nds_read_b128 v[124:127], v252 offset:13312\nds_read_b128 v[128:131], v252 offset:14336\nds_read_b128 v[132:135], v252 offset:15360\n"
      "ds_read_b128 v[136:139], v252 offset:16384\nds_read_b128 v[140:143], v252 offset:17408\nds_read_b128 v[144:147], v252 offset:18432\nds_read_b128 v[148:151], v252 offset:19456\n"
      "ds_read_b128 v[152:155], v252 offset:20480\nds_read_b128 v[156:159], v252 offset:21504\nds_read_b128 v[160:163], v252 offset:22528\nds_read_b128 v[164:167], v252 offset:23552\n"
      "ds_read_b128 v[168:171], v252 offset:24576\nds_read_b128 v[172:175], v252 offset:25600\nds_read_b128 v[176:179], v252 offset:26624\nds_read_b128 v[180:183], v252 offset:27648\n"
      "ds_read_b128 v[184:187], v252 offset:28672\nds_read_b128 v[188:191], v252 offset:29696\nds_read_b128 v[192:195], v252 offset:30720\nds_read_b128 v[196:199], v252 offset:31744\n"
      "ds_read_b128 v[200:203], v252 offset:32768\nds_read_b128 v[204:207], v252 offset:33792\nds_read_b128 v[208:211], v252 offset:34816\nds_read_b128 v[212:215], v252 offset:35840\n"
      "ds_read_b128 v[216:219], v252 offset:36864\nds_read_b128 v[220:223], v252 offset:37888\nds_read_b128 v[224:227], v252 offset:38912\nds_read_b128 v[228:231], v252 offset:39936\n"
      "ds_read_b128 v[232:235], v252 offset:40960\ns_mov_b32 s55, s57\ns_mov_b32 s45, -1\ns_mov_b32 s46, 0\ns_setprio 3\ns_waitcnt lgkmcnt(15)\nv_mul_f32 v48, v48, v56\nv_mul_f32 v49, v49, v57\n"
      "v_mul_f32 v50, v50, v58\nv_mul_f32 v51, v51, v59\nv_mul_f32 v52, v52, v60\nv_mul_f32 v53, v53, v61\nv_mul_f32 v54, v54, v62\nds_write_b128 v1, v[48:51] offset:256\n"
      "ds_write_b128 v1, v[52:55] offset:272\nds_read_b64_tr_b16 v[68:69], v238 offset:928\nds_read_b64_tr_b16 v[70:71], v239 offset:928\nds_read_b64_tr_b16 v[56:57], v236 offset:928\n"
      "ds_read_b64_tr_b16 v[58:59], v237 offset:1056\nds_read_b64_tr_b16 v[60:61], v237 offset:1184\nds_read_b64_tr_b16 v[62:63], v237 offset:1312\nds_read_b64_tr_b16 v[64:65], v237 offset:1440\n"
      "ds_read_b64_tr_b16 v[66:67], v237 offset:1568\ns_lshl_b32 s42, s30, 5\nv_mov_b32 v2, s42\ns_mov_b64 exec, 1\nglobal_load_dword v3, v2, s[22:23]\ns_mov_b64 exec, -1\n"
      "s_swappc_b64 s[4:5], s[34:35]\ns_waitcnt vmcnt(0)\nds_read_b128 v[44:47], v248 offset:48\nds_add_rtn_u32 v0, v253, v241 offset:32\ns_waitcnt lgkmcnt(0)\nv_readfirstlane_b32 s42, v0\n"
      "s_cmp_lg_u32 s42, 3\ns_cbranch_scc1 .Lf1062\ns_mov_b64 exec, 1\nglobal_store_dwordx2 v7, v[8:9], s[20:21] sc1\nglobal_store_dword v12, v6, s[20:21] sc1\ns_mov_b64 exec, -1\n"
      ".Lf1062:\nv_readlane_b32 s48, v69, 48\ns_mov_b32 s56, 0\ns_mov_b32 s44, 0\ns_mov_b32 s33, s45\nv_pk_mul_f16 v20, v68, v70\nv_pk_mul_f16 v21, v69, v71\n"
      "v_pk_mul_f16 v22, v56, v56 op_sel_hi:[0,1]\nv_pk_mul_f16 v23, v56, v57 op_sel_hi:[0,1]\nv_pk_mul_f16 v16, v68, s48 op_sel:[0,1]\nv_pk_mul_f16 v17, v69, s48 op_sel:[0,1]\n"
      "v_pk_mul_f16 v18, v70, s48 op_sel:[0,1]\nv_pk_mul_f16 v19, v71, s48 op_sel:[0,1]\n.p2align 3\ns_nop 0\ns_barrier\nv_mfma_f32_16x16x32_f16 v[40:43], v[76:79], v[16:19], 0\n"
      "v_mfma_f32_16x16x32_f16 v[40:43], v[88:91], v[20:23], v[40:43]\ns_add_u32 s45, s45, 2\ns_cmp_ge_u32 s45, s46\ns_cbranch_scc0 .Lc_b0\ns_swappc_b64 s[4:5], s[34:35]\n"
      ".Lc_b0:\nv_mfma_f32_16x16x32_f16 v[36:39], v[84:87], v[20:23], 0\nv_pk_mul_f16 v24, v66, v66 op_sel_hi:[0,1]\nv_pk_mul_f16 v25, v66, v57 op_sel_hi:[0,1]\n"
      "v_mfma_f32_16x16x32_f16 v[36:39], v[72:75], v[16:19], v[36:39]\nv_pk_mul_f16 v26, v56, v66 op_sel_hi:[0,1]\nv_pk_mul_f16 v27, v56, v67 op_sel_hi:[0,1]\ns_setprio 0\n"
      "v_mfma_f32_16x16x32_f16 v[32:35], v[80:83], v[20:23], 0\nv_pk_mul_f16 v28, v58, v66 op_sel_hi:[0,1]\nv_pk_mul_f16 v29, v58, v67 op_sel_hi:[0,1]\ns_waitcnt lgkmcnt(0)\n"
      "v_smfmac_f32_16x16x64_f16 v[44:47], v[92:95], v[16:23], v242\nv_pk_mul_f16 v30, v60, v66 op_sel_hi:[0,1]\nv_pk_mul_f16 v31, v60, v67 op_sel_hi:[0,1]\n"
      "v_mfma_f32_16x16x32_f16 v[32:35], v[96:99], v[24:27], v[32:35]\nv_pk_mul_f16 v16, v62, v66 op_sel_hi:[0,1]\nv_pk_mul_f16 v17, v62, v67 op_sel_hi:[0,1]\ns_setprio 1\n"
      "v_mfma_f32_16x16x32_f16 v[32:35], v[108:111], v[28:31], v[32:35]\nv_pk_mul_f16 v18, v64, v66 op_sel_hi:[0,1]\nv_mfma_f32_16x16x32_f16 v[36:39], v[112:115], v[28:31], v[36:39]\n"
      "v_pk_mul_f16 v19, v64, v67 op_sel_hi:[0,1]\nv_mfma_f32_16x16x32_f16 v[36:39], v[100:103], v[24:27], v[36:39]\nv_pk_mul_f16 v20, v64, v64 op_sel_hi:[0,1]\n"
      "v_mfma_f32_16x16x32_f16 v[40:43], v[104:107], v[24:27], v[40:43]\nv_pk_mul_f16 v21, v64, v57 op_sel_hi:[0,1]\nv_mfma_f32_16x16x32_f16 v[40:43], v[116:119], v[28:31], v[40:43]\n"
      "v_pk_mul_f16 v22, v56, v64 op_sel_hi:[0,1]\nv_smfmac_f32_16x16x64_f16 v[44:47], v[120:123], v[24:31], v242\nv_pk_mul_f16 v23, v56, v65 op_sel_hi:[0,1]\n"
      "v_mfma_f32_16x16x32_f16 v[32:35], v[124:127], v[16:19], v[32:35]\nv_pk_mul_f16 v24, v58, v64 op_sel_hi:[0,1]\nv_pk_mul_f16 v25, v58, v65 op_sel_hi:[0,1]\ns_setprio 2\n"
      "ds_read_b64_tr_b16 v[70:71], v239 offset:1856\nv_mfma_f32_16x16x32_f16 v[32:35], v[136:139], v[20:23], v[32:35]\nv_pk_mul_f16 v26, v60, v64 op_sel_hi:[0,1]\n"
      "ds_read_b64_tr_b16 v[68:69], v238 offset:1856\nv_mfma_f32_16x16x32_f16 v[36:39], v[140:143], v[20:23], v[36:39]\nv_pk_mul_f16 v27, v60, v65 op_sel_hi:[0,1]\n"
      "ds_read_b64_tr_b16 v[66:67], v237 offset:2496\nv_mfma_f32_16x16x32_f16 v[36:39], v[128:131], v[16:19], v[36:39]\nv_pk_mul_f16 v28, v62, v64 op_sel_hi:[0,1]\n"
      "v_mfma_f32_16x16x32_f16 v[40:43], v[132:135], v[16:19], v[40:43]\nv_pk_mul_f16 v29, v62, v65 op_sel_hi:[0,1]\nv_mfma_f32_16x16x32_f16 v[40:43], v[144:147], v[20:23], v[40:43]\n"
      "v_pk_mul_f16 v30, v62, v62 op_sel_hi:[0,1]\nv_smfmac_f32_16x16x64_f16 v[44:47], v[148:151], v[16:23], v242\nv_pk_mul_f16 v31, v62, v57 op_sel_hi:[0,1]\n"
      "v_mfma_f32_16x16x32_f16 v[32:35], v[152:155], v[24:27], v[32:35]\nv_pk_mul_f16 v16, v56, v62 op_sel_hi:[0,1]\nv_pk_mul_f16 v17, v56, v63 op_sel_hi:[0,1]\n"
      "v_mfma_f32_16x16x32_f16 v[32:35], v[164:167], v[28:31], v[32:35]\nv_pk_mul_f16 v18, v58, v62 op_sel_hi:[0,1]\nds_read_b64_tr_b16 v[64:65], v237 offset:2368\n"
      "v_mfma_f32_16x16x32_f16 v[36:39], v[168:171], v[28:31], v[36:39]\nv_pk_mul_f16 v19, v58, v63 op_sel_hi:[0,1]\nv_mfma_f32_16x16x32_f16 v[36:39], v[156:159], v[24:27], v[36:39]\n"
      "v_pk_mul_f16 v20, v60, v62 op_sel_hi:[0,1]\ns_setprio 3\nv_mfma_f32_16x16x32_f16 v[40:43], v[160:163], v[24:27], v[40:43]\nv_pk_mul_f16 v21, v60, v63 op_sel_hi:[0,1]\ns_waitcnt lgkmcnt(1)\n"
      "v_readlane_b32 s48, v69, 48\nv_mfma_f32_16x16x32_f16 v[40:43], v[172:175], v[28:31], v[40:43]\nv_pk_mul_f16 v22, v60, v60 op_sel_hi:[0,1]\n"
      "v_smfmac_f32_16x16x64_f16 v[44:47], v[176:179], v[24:31], v242\nv_pk_mul_f16 v23, v60, v57 op_sel_hi:[0,1]\nv_mfma_f32_16x16x32_f16 v[32:35], v[180:183], v[16:19], v[32:35]\n"
      "v_pk_mul_f16 v24, v56, v60 op_sel_hi:[0,1]\nv_pk_mul_f16 v25, v56, v61 op_sel_hi:[0,1]\nv_mfma_f32_16x16x32_f16 v[32:35], v[192:195], v[20:23], v[32:35]\n"
      "v_pk_mul_f16 v26, v58, v60 op_sel_hi:[0,1]\nv_pk_mul_f16 v27, v58, v61 op_sel_hi:[0,1]\nv_mfma_f32_16x16x32_f16 v[36:39], v[196:199], v[20:23], v[36:39]\n"
      "v_pk_mul_f16 v28, v58, v58 op_sel_hi:[0,1]\nds_read_b64_tr_b16 v[62:63], v237 offset:2240\nv_mfma_f32_16x16x32_f16 v[36:39], v[184:187], v[16:19], v[36:39]\n"
      "v_pk_mul_f16 v29, v58, v57 op_sel_hi:[0,1]\nds_read_b64_tr_b16 v[60:61], v237 offset:2112\nv_mfma_f32_16x16x32_f16 v[40:43], v[188:191], v[16:19], v[40:43]\n"
      "v_pk_mul_f16 v30, v56, v58 op_sel_hi:[0,1]\nv_mfma_f32_16x16x32_f16 v[40:43], v[200:203], v[20:23], v[40:43]\nv_pk_mul_f16 v31, v56, v59 op_sel_hi:[0,1]\n"
      "v_smfmac_f32_16x16x64_f16 v[44:47], v[204:207], v[16:23], v242\nds_read_b64_tr_b16 v[56:57], v236 offset:1856\nds_read_b64_tr_b16 v[58:59], v237 offset:1984\n"
      "v_smfmac_f32_16x16x64_f16 v[44:47], v[232:235], v[24:31], v242\nv_pk_mul_f16 v16, v68, s48 op_sel:[0,1]\nv_pk_mul_f16 v17, v69, s48 op_sel:[0,1]\n"
      "v_mfma_f32_16x16x32_f16 v[32:35], v[208:211], v[24:27], v[32:35]\nv_pk_mul_f16 v18, v70, s48 op_sel:[0,1]\nv_pk_mul_f16 v19, v71, s48 op_sel:[0,1]\n"
      "v_mfma_f32_16x16x32_f16 v[32:35], v[220:223], v[28:31], v[32:35]\nv_pk_mul_f16 v20, v68, v70\nv_pk_mul_f16 v21, v69, v71\nv_mfma_f32_16x16x32_f16 v[36:39], v[224:227], v[28:31], v[36:39]\n"
      "v_add_f32 v44, v44, v45\nv_add_f32 v46, v46, v47\nv_mfma_f32_16x16x32_f16 v[36:39], v[212:215], v[24:27], v[36:39]\nv_mul_f32 v55, v44, v54\nv_mul_f32 v55, v55, v46\n"
      "ds_read_b128 v[44:47], v248 offset:48\nv_mfma_f32_16x16x32_f16 v[40:43], v[216:219], v[24:27], v[40:43]\ns_waitcnt lgkmcnt(1)\nv_pk_mul_f16 v22, v56, v56 op_sel_hi:[0,1]\n"
      "v_pk_mul_f16 v23, v56, v57 op_sel_hi:[0,1]\nv_mfma_f32_16x16x32_f16 v[40:43], v[228:231], v[28:31], v[40:43]\nv_mul_f32 v32, v32, v48\nv_mul_f32 v33, v33, v49\nv_mul_f32 v34, v34, v50\n"
      ".Lc_u1:\nv_mfma_f32_16x16x32_f16 v[244:247], v[76:79], v[16:19], 0\nv_fmac_f32 v55, v32, v36\nv_mul_f32 v35, v35, v51\nv_fmac_f32 v55, v33, v37\n"
      "v_mfma_f32_16x16x32_f16 v[244:247], v[88:91], v[20:23], v[244:247]\nv_fmac_f32 v55, v34, v38\nv_fmac_f32 v55, v35, v39\ns_add_u32 s45, s45, 2\ns_cmp_ge_u32 s45, s46\ns_cbranch_scc0 .Lc_b1\n"
      "s_swappc_b64 s[4:5], s[34:35]\n"
      ".Lc_b1:\nv_mfma_f32_16x16x32_f16 v[36:39], v[84:87], v[20:23], 0\nv_pk_mul_f16 v24, v66, v66 op_sel_hi:[0,1]\nv_pk_mul_f16 v25, v66, v57 op_sel_hi:[0,1]\n"
      "v_mfma_f32_16x16x32_f16 v[36:39], v[72:75], v[16:19], v[36:39]\nv_pk_mul_f16 v26, v56, v66 op_sel_hi:[0,1]\nv_pk_mul_f16 v27, v56, v67 op_sel_hi:[0,1]\ns_setprio 0\n"
      "v_mfma_f32_16x16x32_f16 v[32:35], v[80:83], v[20:23], 0\nv_pk_mul_f16 v28, v58, v66 op_sel_hi:[0,1]\nv_pk_mul_f16 v29, v58, v67 op_sel_hi:[0,1]\ns_waitcnt lgkmcnt(0)\n"
      "v_smfmac_f32_16x16x64_f16 v[44:47], v[92:95], v[16:23], v242\nv_pk_mul_f16 v30, v60, v66 op_sel_hi:[0,1]\nv_pk_mul_f16 v31, v60, v67 op_sel_hi:[0,1]\n"
      "v_mfma_f32_16x16x32_f16 v[32:35], v[96:99], v[24:27], v[32:35]\nv_pk_mul_f16 v16, v62, v66 op_sel_hi:[0,1]\nv_pk_mul_f16 v17, v62, v67 op_sel_hi:[0,1]\ns_setprio 1\n"
      "v_mfma_f32_16x16x32_f16 v[32:35], v[108:111], v[28:31], v[32:35]\nv_pk_mul_f16 v18, v64, v66 op_sel_hi:[0,1]\nv_mul_f32 v40, v40, v52\n"
      "v_mfma_f32_16x16x32_f16 v[36:39], v[112:115], v[28:31], v[36:39]\nv_pk_mul_f16 v19, v64, v67 op_sel_hi:[0,1]\nv_mul_f32 v41, v41, v53\n"
      "v_mfma_f32_16x16x32_f16 v[36:39], v[100:103], v[24:27], v[36:39]\nv_pk_mul_f16 v20, v64, v64 op_sel_hi:[0,1]\nv_fmac_f32 v55, v40, v42\n"
      "v_mfma_f32_16x16x32_f16 v[244:247], v[104:107], v[24:27], v[244:247]\nv_pk_mul_f16 v21, v64, v57 op_sel_hi:[0,1]\nv_fmac_f32 v55, v41, v43\n"
      "v_mfma_f32_16x16x32_f16 v[244:247], v[116:119], v[28:31], v[244:247]\nv_pk_mul_f16 v22, v56, v64 op_sel_hi:[0,1]\nv_smfmac_f32_16x16x64_f16 v[44:47], v[120:123], v[24:31], v242\n"
      "v_pk_mul_f16 v23, v56, v65 op_sel_hi:[0,1]\nv_mfma_f32_16x16x32_f16 v[32:35], v[124:127], v[16:19], v[32:35]\nv_pk_mul_f16 v24, v58, v64 op_sel_hi:[0,1]\n"
      "v_pk_mul_f16 v25, v58, v65 op_sel_hi:[0,1]\ns_setprio 2\nds_read_b64_tr_b16 v[70:71], v239 offset:3712\nv_mfma_f32_16x16x32_f16 v[32:35], v[136:139], v[20:23], v[32:35]\n"
      "v_pk_mul_f16 v26, v60, v64 op_sel_hi:[0,1]\nds_read_b64_tr_b16 v[68:69], v238 offset:3712\nv_mfma_f32_16x16x32_f16 v[36:39], v[140:143], v[20:23], v[36:39]\n"
      "v_pk_mul_f16 v27, v60, v65 op_sel_hi:[0,1]\nds_read_b64_tr_b16 v[66:67], v237 offset:4352\nv_mfma_f32_16x16x32_f16 v[36:39], v[128:131], v[16:19], v[36:39]\n"
      "v_pk_mul_f16 v28, v62, v64 op_sel_hi:[0,1]\nv_add_f32_dpp v55, v55, v55 quad_perm:[1,0,3,2] row_mask:0xf bank_mask:0xf\nv_mfma_f32_16x16x32_f16 v[244:247], v[132:135], v[16:19], v[244:247]\n"
      "v_pk_mul_f16 v29, v62, v65 op_sel_hi:[0,1]\nv_add_f32_dpp v55, v55, v55 quad_perm:[2,3,0,1] row_mask:0xf bank_mask:0xf\nv_mfma_f32_16x16x32_f16 v[244:247], v[144:147], v[20:23], v[244:247]\n"
      "v_pk_mul_f16 v30, v62, v62 op_sel_hi:[0,1]\nv_add_f32_dpp v55, v55, v55 row_half_mirror row_mask:0xf bank_mask:0xf\nv_smfmac_f32_16x16x64_f16 v[44:47], v[148:151], v[16:23], v242\n"
      "v_pk_mul_f16 v31, v62, v57 op_sel_hi:[0,1]\nv_add_f32_dpp v55, v55, v55 row_mirror row_mask:0xf bank_mask:0xf\nv_mfma_f32_16x16x32_f16 v[32:35], v[152:155], v[24:27], v[32:35]\n"
      "v_pk_mul_f16 v16, v56, v62 op_sel_hi:[0,1]\nv_pk_mul_f16 v17, v56, v63 op_sel_hi:[0,1]\nds_write_b32 v243, v55 offset:0\nv_mfma_f32_16x16x32_f16 v[32:35], v[164:167], v[28:31], v[32:35]\n"
      "v_pk_mul_f16 v18, v58, v62 op_sel_hi:[0,1]\nds_read_b64_tr_b16 v[64:65], v237 offset:4224\nv_mfma_f32_16x16x32_f16 v[36:39], v[168:171], v[28:31], v[36:39]\n"
      "v_pk_mul_f16 v19, v58, v63 op_sel_hi:[0,1]\nv_mfma_f32_16x16x32_f16 v[36:39], v[156:159], v[24:27], v[36:39]\nv_pk_mul_f16 v20, v60, v62 op_sel_hi:[0,1]\ns_setprio 3\n"
      "v_mfma_f32_16x16x32_f16 v[244:247], v[160:163], v[24:27], v[244:247]\nv_pk_mul_f16 v21, v60, v63 op_sel_hi:[0,1]\ns_waitcnt lgkmcnt(1)\nv_readlane_b32 s48, v69, 48\n"
      "v_mfma_f32_16x16x32_f16 v[244:247], v[172:175], v[28:31], v[244:247]\nv_pk_mul_f16 v22, v60, v60 op_sel_hi:[0,1]\nv_smfmac_f32_16x16x64_f16 v[44:47], v[176:179], v[24:31], v242\n"
      "v_pk_mul_f16 v23, v60, v57 op_sel_hi:[0,1]\nv_mfma_f32_16x16x32_f16 v[32:35], v[180:183], v[16:19], v[32:35]\nv_pk_mul_f16 v24, v56, v60 op_sel_hi:[0,1]\n"
      "v_pk_mul_f16 v25, v56, v61 op_sel_hi:[0,1]\nv_mfma_f32_16x16x32_f16 v[32:35], v[192:195], v[20:23], v[32:35]\nv_pk_mul_f16 v26, v58, v60 op_sel_hi:[0,1]\n"
      "v_pk_mul_f16 v27, v58, v61 op_sel_hi:[0,1]\nv_mfma_f32_16x16x32_f16 v[36:39], v[196:199], v[20:23], v[36:39]\nv_pk_mul_f16 v28, v58, v58 op_sel_hi:[0,1]\n"
      "ds_read_b64_tr_b16 v[62:63], v237 offset:4096\nv_mfma_f32_16x16x32_f16 v[36:39], v[184:187], v[16:19], v[36:39]\nv_pk_mul_f16 v29, v58, v57 op_sel_hi:[0,1]\n"
      "ds_read_b64_tr_b16 v[60:61], v237 offset:3968\nv_mfma_f32_16x16x32_f16 v[244:247], v[188:191], v[16:19], v[244:247]\nv_pk_mul_f16 v30, v56, v58 op_sel_hi:[0,1]\n"
      "v_mfma_f32_16x16x32_f16 v[244:247], v[200:203], v[20:23], v[244:247]\nv_pk_mul_f16 v31, v56, v59 op_sel_hi:[0,1]\nv_smfmac_f32_16x16x64_f16 v[44:47], v[204:207], v[16:23], v242\n"
      "ds_read_b64_tr_b16 v[56:57], v236 offset:3712\nds_read_b64_tr_b16 v[58:59], v237 offset:3840\nv_smfmac_f32_16x16x64_f16 v[44:47], v[232:235], v[24:31], v242\n"
      "v_pk_mul_f16 v16, v68, s48 op_sel:[0,1]\nv_pk_mul_f16 v17, v69, s48 op_sel:[0,1]\nv_mfma_f32_16x16x32_f16 v[32:35], v[208:211], v[24:27], v[32:35]\nv_pk_mul_f16 v18, v70, s48 op_sel:[0,1]\n"
      "v_pk_mul_f16 v19, v71, s48 op_sel:[0,1]\nv_mfma_f32_16x16x32_f16 v[32:35], v[220:223], v[28:31], v[32:35]\nv_pk_mul_f16 v20, v68, v70\nv_pk_mul_f16 v21, v69, v71\n"
      "v_mfma_f32_16x16x32_f16 v[36:39], v[224:227], v[28:31], v[36:39]\nv_add_f32 v44, v44, v45\nv_add_f32 v46, v46, v47\nv_mfma_f32_16x16x32_f16 v[36:39], v[212:215], v[24:27], v[36:39]\n"
      "v_mul_f32 v55, v44, v54\nv_mul_f32 v55, v55, v46\nds_read_b128 v[44:47], v248 offset:48\nv_mfma_f32_16x16x32_f16 v[244:247], v[216:219], v[24:27], v[244:247]\ns_waitcnt lgkmcnt(1)\n"
      "v_pk_mul_f16 v22, v56, v56 op_sel_hi:[0,1]\nv_pk_mul_f16 v23, v56, v57 op_sel_hi:[0,1]\nv_mfma_f32_16x16x32_f16 v[244:247], v[228:231], v[28:31], v[244:247]\nv_mul_f32 v32, v32, v48\n"
      "v_mul_f32 v33, v33, v49\nv_mul_f32 v34, v34, v50\nv_mfma_f32_16x16x32_f16 v[40:43], v[76:79], v[16:19], 0\nds_read_b32 v4, v248\nv_fmac_f32 v55, v32, v36\nv_mul_f32 v35, v35, v51\n"
      "v_fmac_f32 v55, v33, v37\nv_mfma_f32_16x16x32_f16 v[40:43], v[88:91], v[20:23], v[40:43]\nv_fmac_f32 v55, v34, v38\nv_fmac_f32 v55, v35, v39\ns_add_u32 s45, s45, 2\ns_cmp_ge_u32 s45, s46\n"
      "s_cbranch_scc0 .Lc_b2\ns_swappc_b64 s[4:5], s[34:35]\n"
      ".Lc_b2:\nv_mfma_f32_16x16x32_f16 v[36:39], v[84:87], v[20:23], 0\nv_pk_mul_f16 v24, v66, v66 op_sel_hi:[0,1]\nv_pk_mul_f16 v25, v66, v57 op_sel_hi:[0,1]\n"
      "v_mfma_f32_16x16x32_f16 v[36:39], v[72:75], v[16:19], v[36:39]\nv_pk_mul_f16 v26, v56, v66 op_sel_hi:[0,1]\nv_pk_mul_f16 v27, v56, v67 op_sel_hi:[0,1]\ns_setprio 0\n"
      "v_mfma_f32_16x16x32_f16 v[32:35], v[80:83], v[20:23], 0\nv_pk_mul_f16 v28, v58, v66 op_sel_hi:[0,1]\nv_pk_mul_f16 v29, v58, v67 op_sel_hi:[0,1]\ns_waitcnt lgkmcnt(0)\n"
      "v_smfmac_f32_16x16x64_f16 v[44:47], v[92:95], v[16:23], v242\nv_pk_mul_f16 v30, v60, v66 op_sel_hi:[0,1]\nv_pk_mul_f16 v31, v60, v67 op_sel_hi:[0,1]\n"
      "v_mfma_f32_16x16x32_f16 v[32:35], v[96:99], v[24:27], v[32:35]\nv_pk_mul_f16 v16, v62, v66 op_sel_hi:[0,1]\nv_pk_mul_f16 v17, v62, v67 op_sel_hi:[0,1]\ns_setprio 1\n"
      "v_mfma_f32_16x16x32_f16 v[32:35], v[108:111], v[28:31], v[32:35]\nv_pk_mul_f16 v18, v64, v66 op_sel_hi:[0,1]\nv_mul_f32 v244, v244, v52\n"
      "v_mfma_f32_16x16x32_f16 v[36:39], v[112:115], v[28:31], v[36:39]\nv_pk_mul_f16 v19, v64, v67 op_sel_hi:[0,1]\nv_mul_f32 v245, v245, v53\n"
      "v_mfma_f32_16x16x32_f16 v[36:39], v[100:103], v[24:27], v[36:39]\nv_pk_mul_f16 v20, v64, v64 op_sel_hi:[0,1]\nv_fmac_f32 v55, v244, v246\n"
      "v_mfma_f32_16x16x32_f16 v[40:43], v[104:107], v[24:27], v[40:43]\nv_pk_mul_f16 v21, v64, v57 op_sel_hi:[0,1]\nv_fmac_f32 v55, v245, v247\n"
      "v_mfma_f32_16x16x32_f16 v[40:43], v[116:119], v[28:31], v[40:43]\nv_pk_mul_f16 v22, v56, v64 op_sel_hi:[0,1]\nv_readfirstlane_b32 s44, v4\n"
      "v_smfmac_f32_16x16x64_f16 v[44:47], v[120:123], v[24:31], v242\nv_pk_mul_f16 v23, v56, v65 op_sel_hi:[0,1]\nv_mfma_f32_16x16x32_f16 v[32:35], v[124:127], v[16:19], v[32:35]\n"
      "v_pk_mul_f16 v24, v58, v64 op_sel_hi:[0,1]\nv_pk_mul_f16 v25, v58, v65 op_sel_hi:[0,1]\ns_setprio 2\nds_read_b64_tr_b16 v[70:71], v239 offset:5568\n"
      "v_mfma_f32_16x16x32_f16 v[32:35], v[136:139], v[20:23], v[32:35]\nv_pk_mul_f16 v26, v60, v64 op_sel_hi:[0,1]\nds_read_b64_tr_b16 v[68:69], v238 offset:5568\n"
      "v_mfma_f32_16x16x32_f16 v[36:39], v[140:143], v[20:23], v[36:39]\nv_pk_mul_f16 v27, v60, v65 op_sel_hi:[0,1]\nds_read_b64_tr_b16 v[66:67], v237 offset:6208\n"
      "v_mfma_f32_16x16x32_f16 v[36:39], v[128:131], v[16:19], v[36:39]\nv_pk_mul_f16 v28, v62, v64 op_sel_hi:[0,1]\nv_add_f32_dpp v55, v55, v55 quad_perm:[1,0,3,2] row_mask:0xf bank_mask:0xf\n"
      "v_mfma_f32_16x16x32_f16 v[40:43], v[132:135], v[16:19], v[40:43]\nv_pk_mul_f16 v29, v62, v65 op_sel_hi:[0,1]\nv_add_f32_dpp v55, v55, v55 quad_perm:[2,3,0,1] row_mask:0xf bank_mask:0xf\n"
      "v_mfma_f32_16x16x32_f16 v[40:43], v[144:147], v[20:23], v[40:43]\nv_pk_mul_f16 v30, v62, v62 op_sel_hi:[0,1]\nv_add_f32_dpp v55, v55, v55 row_half_mirror row_mask:0xf bank_mask:0xf\n"
      "v_smfmac_f32_16x16x64_f16 v[44:47], v[148:151], v[16:23], v242\nv_pk_mul_f16 v31, v62, v57 op_sel_hi:[0,1]\nv_add_f32_dpp v55, v55, v55 row_mirror row_mask:0xf bank_mask:0xf\n"
      "v_mfma_f32_16x16x32_f16 v[32:35], v[152:155], v[24:27], v[32:35]\nv_pk_mul_f16 v16, v56, v62 op_sel_hi:[0,1]\nv_pk_mul_f16 v17, v56, v63 op_sel_hi:[0,1]\nds_write_b32 v243, v55 offset:128\n"
      "v_mfma_f32_16x16x32_f16 v[32:35], v[164:167], v[28:31], v[32:35]\nv_pk_mul_f16 v18, v58, v62 op_sel_hi:[0,1]\nds_read_b64_tr_b16 v[64:65], v237 offset:6080\n"
      "v_mfma_f32_16x16x32_f16 v[36:39], v[168:171], v[28:31], v[36:39]\nv_pk_mul_f16 v19, v58, v63 op_sel_hi:[0,1]\nv_mfma_f32_16x16x32_f16 v[36:39], v[156:159], v[24:27], v[36:39]\n"
      "v_pk_mul_f16 v20, v60, v62 op_sel_hi:[0,1]\ns_setprio 3\ns_ff0_i32_b32 s53, s44\nv_mfma_f32_16x16x32_f16 v[40:43], v[160:163], v[24:27], v[40:43]\n"
      "v_pk_mul_f16 v21, v60, v63 op_sel_hi:[0,1]\ns_waitcnt lgkmcnt(1)\nv_readlane_b32 s48, v69, 48\nv_mfma_f32_16x16x32_f16 v[40:43], v[172:175], v[28:31], v[40:43]\n"
      "v_pk_mul_f16 v22, v60, v60 op_sel_hi:[0,1]\ns_add_u32 s53, s53, s64\nv_smfmac_f32_16x16x64_f16 v[44:47], v[176:179], v[24:31], v242\nv_pk_mul_f16 v23, v60, v57 op_sel_hi:[0,1]\n"
      "v_mfma_f32_16x16x32_f16 v[32:35], v[180:183], v[16:19], v[32:35]\nv_pk_mul_f16 v24, v56, v60 op_sel_hi:[0,1]\nv_pk_mul_f16 v25, v56, v61 op_sel_hi:[0,1]\n"
      "v_mfma_f32_16x16x32_f16 v[32:35], v[192:195], v[20:23], v[32:35]\nv_pk_mul_f16 v26, v58, v60 op_sel_hi:[0,1]\nv_pk_mul_f16 v27, v58, v61 op_sel_hi:[0,1]\n"
      "v_mfma_f32_16x16x32_f16 v[36:39], v[196:199], v[20:23], v[36:39]\nv_pk_mul_f16 v28, v58, v58 op_sel_hi:[0,1]\nds_read_b64_tr_b16 v[62:63], v237 offset:5952\n"
      "v_mfma_f32_16x16x32_f16 v[36:39], v[184:187], v[16:19], v[36:39]\nv_pk_mul_f16 v29, v58, v57 op_sel_hi:[0,1]\nds_read_b64_tr_b16 v[60:61], v237 offset:5824\n"
      "v_mfma_f32_16x16x32_f16 v[40:43], v[188:191], v[16:19], v[40:43]\nv_pk_mul_f16 v30, v56, v58 op_sel_hi:[0,1]\nv_mfma_f32_16x16x32_f16 v[40:43], v[200:203], v[20:23], v[40:43]\n"
      "v_pk_mul_f16 v31, v56, v59 op_sel_hi:[0,1]\nv_smfmac_f32_16x16x64_f16 v[44:47], v[204:207], v[16:23], v242\nds_read_b64_tr_b16 v[56:57], v236 offset:5568\n"
      "ds_read_b64_tr_b16 v[58:59], v237 offset:5696\nv_smfmac_f32_16x16x64_f16 v[44:47], v[232:235], v[24:31], v242\nv_pk_mul_f16 v16, v68, s48 op_sel:[0,1]\n"
      "v_pk_mul_f16 v17, v69, s48 op_sel:[0,1]\nv_mfma_f32_16x16x32_f16 v[32:35], v[208:211], v[24:27], v[32:35]\nv_pk_mul_f16 v18, v70, s48 op_sel:[0,1]\nv_pk_mul_f16 v19, v71, s48 op_sel:[0,1]\n"
      "v_mfma_f32_16x16x32_f16 v[32:35], v[220:223], v[28:31], v[32:35]\nv_pk_mul_f16 v20, v68, v70\nv_pk_mul_f16 v21, v69, v71\nv_mfma_f32_16x16x32_f16 v[36:39], v[224:227], v[28:31], v[36:39]\n"
      "v_add_f32 v44, v44, v45\nv_add_f32 v46, v46, v47\nv_mfma_f32_16x16x32_f16 v[36:39], v[212:215], v[24:27], v[36:39]\nv_mul_f32 v55, v44, v54\nv_mul_f32 v55, v55, v46\n"
      "ds_read_b128 v[44:47], v248 offset:48\nv_mfma_f32_16x16x32_f16 v[40:43], v[216:219], v[24:27], v[40:43]\ns_waitcnt lgkmcnt(1)\nv_pk_mul_f16 v22, v56, v56 op_sel_hi:[0,1]\n"
      "v_pk_mul_f16 v23, v56, v57 op_sel_hi:[0,1]\nv_mfma_f32_16x16x32_f16 v[40:43], v[228:231], v[28:31], v[40:43]\ns_cmp_lt_u32 s45, s53\nv_mul_f32 v32, v32, v48\nv_mul_f32 v33, v33, v49\n"
      "v_mul_f32 v34, v34, v50\ns_cbranch_scc0 .Lc_s2\n"
      ".Lc_u3:\nv_mfma_f32_16x16x32_f16 v[244:247], v[76:79], v[16:19], 0\nds_read_b32 v4, v248\nv_fmac_f32 v55, v32, v36\nv_mul_f32 v35, v35, v51\nv_fmac_f32 v55, v33, v37\n"
      "v_mfma_f32_16x16x32_f16 v[244:247], v[88:91], v[20:23], v[244:247]\nv_fmac_f32 v55, v34, v38\nv_fmac_f32 v55, v35, v39\ns_add_u32 s45, s45, 2\ns_cmp_ge_u32 s45, s46\ns_cbranch_scc0 .Lc_b3\n"
      "s_swappc_b64 s[4:5], s[34:35]\n"
      ".Lc_b3:\nv_mfma_f32_16x16x32_f16 v[36:39], v[84:87], v[20:23], 0\nv_pk_mul_f16 v24, v66, v66 op_sel_hi:[0,1]\nv_pk_mul_f16 v25, v66, v57 op_sel_hi:[0,1]\n"
      "v_mfma_f32_16x16x32_f16 v[36:39], v[72:75], v[16:19], v[36:39]\nv_pk_mul_f16 v26, v56, v66 op_sel_hi:[0,1]\nv_pk_mul_f16 v27, v56, v67 op_sel_hi:[0,1]\ns_setprio 0\n"
      "v_mfma_f32_16x16x32_f16 v[32:35], v[80:83], v[20:23], 0\nv_pk_mul_f16 v28, v58, v66 op_sel_hi:[0,1]\nv_pk_mul_f16 v29, v58, v67 op_sel_hi:[0,1]\ns_waitcnt lgkmcnt(0)\n"
      "v_smfmac_f32_16x16x64_f16 v[44:47], v[92:95], v[16:23], v242\nv_pk_mul_f16 v30, v60, v66 op_sel_hi:[0,1]\nv_pk_mul_f16 v31, v60, v67 op_sel_hi:[0,1]\n"
      "v_mfma_f32_16x16x32_f16 v[32:35], v[96:99], v[24:27], v[32:35]\nv_pk_mul_f16 v16, v62, v66 op_sel_hi:[0,1]\nv_pk_mul_f16 v17, v62, v67 op_sel_hi:[0,1]\ns_setprio 1\n"
      "v_mfma_f32_16x16x32_f16 v[32:35], v[108:111], v[28:31], v[32:35]\nv_pk_mul_f16 v18, v64, v66 op_sel_hi:[0,1]\nv_mul_f32 v40, v40, v52\n"
      "v_mfma_f32_16x16x32_f16 v[36:39], v[112:115], v[28:31], v[36:39]\nv_pk_mul_f16 v19, v64, v67 op_sel_hi:[0,1]\nv_mul_f32 v41, v41, v53\n"
      "v_mfma_f32_16x16x32_f16 v[36:39], v[100:103], v[24:27], v[36:39]\nv_pk_mul_f16 v20, v64, v64 op_sel_hi:[0,1]\nv_fmac_f32 v55, v40, v42\n"
      "v_mfma_f32_16x16x32_f16 v[244:247], v[104:107], v[24:27], v[244:247]\nv_pk_mul_f16 v21, v64, v57 op_sel_hi:[0,1]\nv_fmac_f32 v55, v41, v43\n"
      "v_mfma_f32_16x16x32_f16 v[244:247], v[116:119], v[28:31], v[244:247]\nv_pk_mul_f16 v22, v56, v64 op_sel_hi:[0,1]\nv_readfirstlane_b32 s44, v4\n"
      "v_smfmac_f32_16x16x64_f16 v[44:47], v[120:123], v[24:31], v242\nv_pk_mul_f16 v23, v56, v65 op_sel_hi:[0,1]\nv_mfma_f32_16x16x32_f16 v[32:35], v[124:127], v[16:19], v[32:35]\n"
      "v_pk_mul_f16 v24, v58, v64 op_sel_hi:[0,1]\nv_pk_mul_f16 v25, v58, v65 op_sel_hi:[0,1]\ns_setprio 2\nds_read_b64_tr_b16 v[70:71], v239 offset:7424\n"
      "v_mfma_f32_16x16x32_f16 v[32:35], v[136:139], v[20:23], v[32:35]\nv_pk_mul_f16 v26, v60, v64 op_sel_hi:[0,1]\nds_read_b64_tr_b16 v[68:69], v238 offset:7424\n"
      "v_mfma_f32_16x16x32_f16 v[36:39], v[140:143], v[20:23], v[36:39]\nv_pk_mul_f16 v27, v60, v65 op_sel_hi:[0,1]\nds_read_b64_tr_b16 v[66:67], v237 offset:8064\n"
      "v_mfma_f32_16x16x32_f16 v[36:39], v[128:131], v[16:19], v[36:39]\nv_pk_mul_f16 v28, v62, v64 op_sel_hi:[0,1]\nv_add_f32_dpp v55, v55, v55 quad_perm:[1,0,3,2] row_mask:0xf bank_mask:0xf\n"
      "v_mfma_f32_16x16x32_f16 v[244:247], v[132:135], v[16:19], v[244:247]\nv_pk_mul_f16 v29, v62, v65 op_sel_hi:[0,1]\nv_add_f32_dpp v55, v55, v55 quad_perm:[2,3,0,1] row_mask:0xf bank_mask:0xf\n"
      "v_mfma_f32_16x16x32_f16 v[244:247], v[144:147], v[20:23], v[244:247]\nv_pk_mul_f16 v30, v62, v62 op_sel_hi:[0,1]\nv_add_f32_dpp v55, v55, v55 row_half_mirror row_mask:0xf bank_mask:0xf\n"
      "v_smfmac_f32_16x16x64_f16 v[44:47], v[148:151], v[16:23], v242\nv_pk_mul_f16 v31, v62, v57 op_sel_hi:[0,1]\nv_add_f32_dpp v55, v55, v55 row_mirror row_mask:0xf bank_mask:0xf\n"
      "v_mfma_f32_16x16x32_f16 v[32:35], v[152:155], v[24:27], v[32:35]\nv_pk_mul_f16 v16, v56, v62 op_sel_hi:[0,1]\nv_pk_mul_f16 v17, v56, v63 op_sel_hi:[0,1]\nds_write_b32 v243, v55 offset:256\n"
      "v_mfma_f32_16x16x32_f16 v[32:35], v[164:167], v[28:31], v[32:35]\nv_pk_mul_f16 v18, v58, v62 op_sel_hi:[0,1]\nds_read_b64_tr_b16 v[64:65], v237 offset:7936\n"
      "v_mfma_f32_16x16x32_f16 v[36:39], v[168:171], v[28:31], v[36:39]\nv_pk_mul_f16 v19, v58, v63 op_sel_hi:[0,1]\nv_mfma_f32_16x16x32_f16 v[36:39], v[156:159], v[24:27], v[36:39]\n"
      "v_pk_mul_f16 v20, v60, v62 op_sel_hi:[0,1]\ns_setprio 3\ns_ff0_i32_b32 s53, s44\nv_mfma_f32_16x16x32_f16 v[244:247], v[160:163], v[24:27], v[244:247]\n"
      "v_pk_mul_f16 v21, v60, v63 op_sel_hi:[0,1]\ns_waitcnt lgkmcnt(1)\nv_readlane_b32 s48, v69, 48\nv_mfma_f32_16x16x32_f16 v[244:247], v[172:175], v[28:31], v[244:247]\n"
      "v_pk_mul_f16 v22, v60, v60 op_sel_hi:[0,1]\ns_add_u32 s53, s53, s64\nv_smfmac_f32_16x16x64_f16 v[44:47], v[176:179], v[24:31], v242\nv_pk_mul_f16 v23, v60, v57 op_sel_hi:[0,1]\n"
      "v_mfma_f32_16x16x32_f16 v[32:35], v[180:183], v[16:19], v[32:35]\nv_pk_mul_f16 v24, v56, v60 op_sel_hi:[0,1]\nv_pk_mul_f16 v25, v56, v61 op_sel_hi:[0,1]\n"
      "v_mfma_f32_16x16x32_f16 v[32:35], v[192:195], v[20:23], v[32:35]\nv_pk_mul_f16 v26, v58, v60 op_sel_hi:[0,1]\nv_pk_mul_f16 v27, v58, v61 op_sel_hi:[0,1]\n"
      "v_mfma_f32_16x16x32_f16 v[36:39], v[196:199], v[20:23], v[36:39]\nv_pk_mul_f16 v28, v58, v58 op_sel_hi:[0,1]\nds_read_b64_tr_b16 v[62:63], v237 offset:7808\n"
      "v_mfma_f32_16x16x32_f16 v[36:39], v[184:187], v[16:19], v[36:39]\nv_pk_mul_f16 v29, v58, v57 op_sel_hi:[0,1]\nds_read_b64_tr_b16 v[60:61], v237 offset:7680\n"
      "v_mfma_f32_16x16x32_f16 v[244:247], v[188:191], v[16:19], v[244:247]\nv_pk_mul_f16 v30, v56, v58 op_sel_hi:[0,1]\nv_mfma_f32_16x16x32_f16 v[244:247], v[200:203], v[20:23], v[244:247]\n"
      "v_pk_mul_f16 v31, v56, v59 op_sel_hi:[0,1]\nv_smfmac_f32_16x16x64_f16 v[44:47], v[204:207], v[16:23], v242\nds_read_b64_tr_b16 v[56:57], v236 offset:7424\n"
      "ds_read_b64_tr_b16 v[58:59], v237 offset:7552\nv_smfmac_f32_16x16x64_f16 v[44:47], v[232:235], v[24:31], v242\nv_pk_mul_f16 v16, v68, s48 op_sel:[0,1]\n"
      "v_pk_mul_f16 v17, v69, s48 op_sel:[0,1]\nv_mfma_f32_16x16x32_f16 v[32:35], v[208:211], v[24:27], v[32:35]\nv_pk_mul_f16 v18, v70, s48 op_sel:[0,1]\nv_pk_mul_f16 v19, v71, s48 op_sel:[0,1]\n"
      "v_mfma_f32_16x16x32_f16 v[32:35], v[220:223], v[28:31], v[32:35]\nv_pk_mul_f16 v20, v68, v70\nv_pk_mul_f16 v21, v69, v71\nv_mfma_f32_16x16x32_f16 v[36:39], v[224:227], v[28:31], v[36:39]\n"
      "v_add_f32 v44, v44, v45\nv_add_f32 v46, v46, v47\nv_mfma_f32_16x16x32_f16 v[36:39], v[212:215], v[24:27], v[36:39]\nv_mul_f32 v55, v44, v54\nv_mul_f32 v55, v55, v46\n"
      "ds_read_b128 v[44:47], v248 offset:48\nv_mfma_f32_16x16x32_f16 v[244:247], v[216:219], v[24:27], v[244:247]\ns_waitcnt lgkmcnt(1)\nv_pk_mul_f16 v22, v56, v56 op_sel_hi:[0,1]\n"
      "v_pk_mul_f16 v23, v56, v57 op_sel_hi:[0,1]\nv_mfma_f32_16x16x32_f16 v[244:247], v[228:231], v[28:31], v[244:247]\ns_cmp_lt_u32 s45, s53\nv_mul_f32 v32, v32, v48\nv_mul_f32 v33, v33, v49\n"
      "v_mul_f32 v34, v34, v50\ns_cbranch_scc0 .Lc_s3\n"
      ".Lc_u4:\nv_mfma_f32_16x16x32_f16 v[40:43], v[76:79], v[16:19], 0\nds_read_b32 v4, v248\nv_fmac_f32 v55, v32, v36\nv_mul_f32 v35, v35, v51\nv_fmac_f32 v55, v33, v37\n"
      "v_mfma_f32_16x16x32_f16 v[40:43], v[88:91], v[20:23], v[40:43]\nv_fmac_f32 v55, v34, v38\nv_fmac_f32 v55, v35, v39\ns_add_u32 s45, s45, 2\ns_cmp_ge_u32 s45, s46\ns_cbranch_scc0 .Lc_b4\n"
      "s_swappc_b64 s[4:5], s[34:35]\n"
      ".Lc_b4:\nv_mfma_f32_16x16x32_f16 v[36:39], v[84:87], v[20:23], 0\nv_pk_mul_f16 v24, v66, v66 op_sel_hi:[0,1]\nv_pk_mul_f16 v25, v66, v57 op_sel_hi:[0,1]\n"
      "v_mfma_f32_16x16x32_f16 v[36:39], v[72:75], v[16:19], v[36:39]\nv_pk_mul_f16 v26, v56, v66 op_sel_hi:[0,1]\nv_pk_mul_f16 v27, v56, v67 op_sel_hi:[0,1]\ns_setprio 0\n"
      "v_mfma_f32_16x16x32_f16 v[32:35], v[80:83], v[20:23], 0\nv_pk_mul_f16 v28, v58, v66 op_sel_hi:[0,1]\nv_pk_mul_f16 v29, v58, v67 op_sel_hi:[0,1]\ns_waitcnt lgkmcnt(0)\n"
      "v_smfmac_f32_16x16x64_f16 v[44:47], v[92:95], v[16:23], v242\nv_pk_mul_f16 v30, v60, v66 op_sel_hi:[0,1]\nv_pk_mul_f16 v31, v60, v67 op_sel_hi:[0,1]\n"
      "v_mfma_f32_16x16x32_f16 v[32:35], v[96:99], v[24:27], v[32:35]\nv_pk_mul_f16 v16, v62, v66 op_sel_hi:[0,1]\nv_pk_mul_f16 v17, v62, v67 op_sel_hi:[0,1]\ns_setprio 1\n"
      "v_mfma_f32_16x16x32_f16 v[32:35], v[108:111], v[28:31], v[32:35]\nv_pk_mul_f16 v18, v64, v66 op_sel_hi:[0,1]\nv_mul_f32 v244, v244, v52\n"
      "v_mfma_f32_16x16x32_f16 v[36:39], v[112:115], v[28:31], v[36:39]\nv_pk_mul_f16 v19, v64, v67 op_sel_hi:[0,1]\nv_mul_f32 v245, v245, v53\n"
      "v_mfma_f32_16x16x32_f16 v[36:39], v[100:103], v[24:27], v[36:39]\nv_pk_mul_f16 v20, v64, v64 op_sel_hi:[0,1]\nv_fmac_f32 v55, v244, v246\n"
      "v_mfma_f32_16x16x32_f16 v[40:43], v[104:107], v[24:27], v[40:43]\nv_pk_mul_f16 v21, v64, v57 op_sel_hi:[0,1]\nv_fmac_f32 v55, v245, v247\n"
      "v_mfma_f32_16x16x32_f16 v[40:43], v[116:119], v[28:31], v[40:43]\nv_pk_mul_f16 v22, v56, v64 op_sel_hi:[0,1]\nv_readfirstlane_b32 s44, v4\n"
      "v_smfmac_f32_16x16x64_f16 v[44:47], v[120:123], v[24:31], v242\nv_pk_mul_f16 v23, v56, v65 op_sel_hi:[0,1]\nv_mfma_f32_16x16x32_f16 v[32:35], v[124:127], v[16:19], v[32:35]\n"
      "v_pk_mul_f16 v24, v58, v64 op_sel_hi:[0,1]\nv_pk_mul_f16 v25, v58, v65 op_sel_hi:[0,1]\ns_setprio 2\nds_read_b64_tr_b16 v[70:71], v239 offset:9280\n"
      "v_mfma_f32_16x16x32_f16 v[32:35], v[136:139], v[20:23], v[32:35]\nv_pk_mul_f16 v26, v60, v64 op_sel_hi:[0,1]\nds_read_b64_tr_b16 v[68:69], v238 offset:9280\n"
      "v_mfma_f32_16x16x32_f16 v[36:39], v[140:143], v[20:23], v[36:39]\nv_pk_mul_f16 v27, v60, v65 op_sel_hi:[0,1]\nds_read_b64_tr_b16 v[66:67], v237 offset:9920\n"
      "v_mfma_f32_16x16x32_f16 v[36:39], v[128:131], v[16:19], v[36:39]\nv_pk_mul_f16 v28, v62, v64 op_sel_hi:[0,1]\nv_add_f32_dpp v55, v55, v55 quad_perm:[1,0,3,2] row_mask:0xf bank_mask:0xf\n"
      "v_mfma_f32_16x16x32_f16 v[40:43], v[132:135], v[16:19], v[40:43]\nv_pk_mul_f16 v29, v62, v65 op_sel_hi:[0,1]\nv_add_f32_dpp v55, v55, v55 quad_perm:[2,3,0,1] row_mask:0xf bank_mask:0xf\n"
      "v_mfma_f32_16x16x32_f16 v[40:43], v[144:147], v[20:23], v[40:43]\nv_pk_mul_f16 v30, v62, v62 op_sel_hi:[0,1]\nv_add_f32_dpp v55, v55, v55 row_half_mirror row_mask:0xf bank_mask:0xf\n"
      "v_smfmac_f32_16x16x64_f16 v[44:47], v[148:151], v[16:23], v242\nv_pk_mul_f16 v31, v62, v57 op_sel_hi:[0,1]\nv_add_f32_dpp v55, v55, v55 row_mirror row_mask:0xf bank_mask:0xf\n"
      "v_mfma_f32_16x16x32_f16 v[32:35], v[152:155], v[24:27], v[32:35]\nv_pk_mul_f16 v16, v56, v62 op_sel_hi:[0,1]\nv_pk_mul_f16 v17, v56, v63 op_sel_hi:[0,1]\nds_write_b32 v243, v55 offset:384\n"
      "v_mfma_f32_16x16x32_f16 v[32:35], v[164:167], v[28:31], v[32:35]\nv_pk_mul_f16 v18, v58, v62 op_sel_hi:[0,1]\nds_read_b64_tr_b16 v[64:65], v237 offset:9792\n"
      "v_mfma_f32_16x16x32_f16 v[36:39], v[168:171], v[28:31], v[36:39]\nv_pk_mul_f16 v19, v58, v63 op_sel_hi:[0,1]\nv_mfma_f32_16x16x32_f16 v[36:39], v[156:159], v[24:27], v[36:39]\n"
      "v_pk_mul_f16 v20, v60, v62 op_sel_hi:[0,1]\ns_setprio 3\ns_ff0_i32_b32 s53, s44\nv_mfma_f32_16x16x32_f16 v[40:43], v[160:163], v[24:27], v[40:43]\n"
      "v_pk_mul_f16 v21, v60, v63 op_sel_hi:[0,1]\ns_waitcnt lgkmcnt(1)\nv_readlane_b32 s48, v69, 48\nv_mfma_f32_16x16x32_f16 v[40:43], v[172:175], v[28:31], v[40:43]\n"
      "v_pk_mul_f16 v22, v60, v60 op_sel_hi:[0,1]\ns_add_u32 s53, s53, s64\nv_smfmac_f32_16x16x64_f16 v[44:47], v[176:179], v[24:31], v242\nv_pk_mul_f16 v23, v60, v57 op_sel_hi:[0,1]\n"
      "v_mfma_f32_16x16x32_f16 v[32:35], v[180:183], v[16:19], v[32:35]\nv_pk_mul_f16 v24, v56, v60 op_sel_hi:[0,1]\nv_pk_mul_f16 v25, v56, v61 op_sel_hi:[0,1]\n"
      "v_mfma_f32_16x16x32_f16 v[32:35], v[192:195], v[20:23], v[32:35]\nv_pk_mul_f16 v26, v58, v60 op_sel_hi:[0,1]\nv_pk_mul_f16 v27, v58, v61 op_sel_hi:[0,1]\n"
      "v_mfma_f32_16x16x32_f16 v[36:39], v[196:199], v[20:23], v[36:39]\nv_pk_mul_f16 v28, v58, v58 op_sel_hi:[0,1]\nds_read_b64_tr_b16 v[62:63], v237 offset:9664\n"
      "v_mfma_f32_16x16x32_f16 v[36:39], v[184:187], v[16:19], v[36:39]\nv_pk_mul_f16 v29, v58, v57 op_sel_hi:[0,1]\nds_read_b64_tr_b16 v[60:61], v237 offset:9536\n"
      "v_mfma_f32_16x16x32_f16 v[40:43], v[188:191], v[16:19], v[40:43]\nv_pk_mul_f16 v30, v56, v58 op_sel_hi:[0,1]\nv_mfma_f32_16x16x32_f16 v[40:43], v[200:203], v[20:23], v[40:43]\n"
      "v_pk_mul_f16 v31, v56, v59 op_sel_hi:[0,1]\nv_smfmac_f32_16x16x64_f16 v[44:47], v[204:207], v[16:23], v242\nds_read_b64_tr_b16 v[56:57], v236 offset:9280\n"
      "ds_read_b64_tr_b16 v[58:59], v237 offset:9408\nv_smfmac_f32_16x16x64_f16 v[44:47], v[232:235], v[24:31], v242\nv_pk_mul_f16 v16, v68, s48 op_sel:[0,1]\n"
      "v_pk_mul_f16 v17, v69, s48 op_sel:[0,1]\nv_mfma_f32_16x16x32_f16 v[32:35], v[208:211], v[24:27], v[32:35]\nv_pk_mul_f16 v18, v70, s48 op_sel:[0,1]\nv_pk_mul_f16 v19, v71, s48 op_sel:[0,1]\n"
      "v_mfma_f32_16x16x32_f16 v[32:35], v[220:223], v[28:31], v[32:35]\nv_pk_mul_f16 v20, v68, v70\nv_pk_mul_f16 v21, v69, v71\nv_mfma_f32_16x16x32_f16 v[36:39], v[224:227], v[28:31], v[36:39]\n"
      "v_add_f32 v44, v44, v45\nv_add_f32 v46, v46, v47\nv_mfma_f32_16x16x32_f16 v[36:39], v[212:215], v[24:27], v[36:39]\nv_mul_f32 v55, v44, v54\nv_mul_f32 v55, v55, v46\n"
      "ds_read_b128 v[44:47], v248 offset:48\nv_mfma_f32_16x16x32_f16 v[40:43], v[216:219], v[24:27], v[40:43]\ns_waitcnt lgkmcnt(1)\nv_pk_mul_f16 v22, v56, v56 op_sel_hi:[0,1]\n"
      "v_pk_mul_f16 v23, v56, v57 op_sel_hi:[0,1]\nv_mfma_f32_16x16x32_f16 v[40:43], v[228:231], v[28:31], v[40:43]\ns_cmp_lt_u32 s45, s53\nv_mul_f32 v32, v32, v48\nv_mul_f32 v33, v33, v49\n"
      "v_mul_f32 v34, v34, v50\ns_cbranch_scc0 .Lc_s4\n"
      ".Lc_u5:\nv_mfma_f32_16x16x32_f16 v[244:247], v[76:79], v[16:19], 0\nds_read_b32 v4, v248\nv_fmac_f32 v55, v32, v36\nv_mul_f32 v35, v35, v51\nv_fmac_f32 v55, v33, v37\n"
      "v_mfma_f32_16x16x32_f16 v[244:247], v[88:91], v[20:23], v[244:247]\nv_fmac_f32 v55, v34, v38\nv_fmac_f32 v55, v35, v39\ns_add_u32 s45, s45, 2\ns_cmp_ge_u32 s45, s46\ns_cbranch_scc0 .Lc_b5\n"
      "s_swappc_b64 s[4:5], s[34:35]\n"
      ".Lc_b5:\nv_mfma_f32_16x16x32_f16 v[36:39], v[84:87], v[20:23], 0\nv_pk_mul_f16 v24, v66, v66 op_sel_hi:[0,1]\nv_pk_mul_f16 v25, v66, v57 op_sel_hi:[0,1]\n"
      "v_mfma_f32_16x16x32_f16 v[36:39], v[72:75], v[16:19], v[36:39]\nv_pk_mul_f16 v26, v56, v66 op_sel_hi:[0,1]\nv_pk_mul_f16 v27, v56, v67 op_sel_hi:[0,1]\ns_setprio 0\n"
      "v_mfma_f32_16x16x32_f16 v[32:35], v[80:83], v[20:23], 0\nv_pk_mul_f16 v28, v58, v66 op_sel_hi:[0,1]\nv_pk_mul_f16 v29, v58, v67 op_sel_hi:[0,1]\ns_waitcnt lgkmcnt(0)\n"
      "v_smfmac_f32_16x16x64_f16 v[44:47], v[92:95], v[16:23], v242\nv_pk_mul_f16 v30, v60, v66 op_sel_hi:[0,1]\nv_pk_mul_f16 v31, v60, v67 op_sel_hi:[0,1]\n"
      "v_mfma_f32_16x16x32_f16 v[32:35], v[96:99], v[24:27], v[32:35]\nv_pk_mul_f16 v16, v62, v66 op_sel_hi:[0,1]\nv_pk_mul_f16 v17, v62, v67 op_sel_hi:[0,1]\ns_setprio 1\n"
      "v_mfma_f32_16x16x32_f16 v[32:35], v[108:111], v[28:31], v[32:35]\nv_pk_mul_f16 v18, v64, v66 op_sel_hi:[0,1]\nv_mul_f32 v40, v40, v52\n"
      "v_mfma_f32_16x16x32_f16 v[36:39], v[112:115], v[28:31], v[36:39]\nv_pk_mul_f16 v19, v64, v67 op_sel_hi:[0,1]\nv_mul_f32 v41, v41, v53\n"
      "v_mfma_f32_16x16x32_f16 v[36:39], v[100:103], v[24:27], v[36:39]\nv_pk_mul_f16 v20, v64, v64 op_sel_hi:[0,1]\nv_fmac_f32 v55, v40, v42\n"
      "v_mfma_f32_16x16x32_f16 v[244:247], v[104:107], v[24:27], v[244:247]\nv_pk_mul_f16 v21, v64, v57 op_sel_hi:[0,1]\nv_fmac_f32 v55, v41, v43\n"
      "v_mfma_f32_16x16x32_f16 v[244:247], v[116:119], v[28:31], v[244:247]\nv_pk_mul_f16 v22, v56, v64 op_sel_hi:[0,1]\nv_readfirstlane_b32 s44, v4\n"
      "v_smfmac_f32_16x16x64_f16 v[44:47], v[120:123], v[24:31], v242\nv_pk_mul_f16 v23, v56, v65 op_sel_hi:[0,1]\nv_mfma_f32_16x16x32_f16 v[32:35], v[124:127], v[16:19], v[32:35]\n"
      "v_pk_mul_f16 v24, v58, v64 op_sel_hi:[0,1]\nv_pk_mul_f16 v25, v58, v65 op_sel_hi:[0,1]\ns_setprio 2\nds_read_b64_tr_b16 v[70:71], v239 offset:11136\n"
      "v_mfma_f32_16x16x32_f16 v[32:35], v[136:139], v[20:23], v[32:35]\nv_pk_mul_f16 v26, v60, v64 op_sel_hi:[0,1]\nds_read_b64_tr_b16 v[68:69], v238 offset:11136\n"
      "v_mfma_f32_16x16x32_f16 v[36:39], v[140:143], v[20:23], v[36:39]\nv_pk_mul_f16 v27, v60, v65 op_sel_hi:[0,1]\nds_read_b64_tr_b16 v[66:67], v237 offset:11776\n"
      "v_mfma_f32_16x16x32_f16 v[36:39], v[128:131], v[16:19], v[36:39]\nv_pk_mul_f16 v28, v62, v64 op_sel_hi:[0,1]\nv_add_f32_dpp v55, v55, v55 quad_perm:[1,0,3,2] row_mask:0xf bank_mask:0xf\n"
      "v_mfma_f32_16x16x32_f16 v[244:247], v[132:135], v[16:19], v[244:247]\nv_pk_mul_f16 v29, v62, v65 op_sel_hi:[0,1]\nv_add_f32_dpp v55, v55, v55 quad_perm:[2,3,0,1] row_mask:0xf bank_mask:0xf\n"
      "v_mfma_f32_16x16x32_f16 v[244:247], v[144:147], v[20:23], v[244:247]\nv_pk_mul_f16 v30, v62, v62 op_sel_hi:[0,1]\nv_add_f32_dpp v55, v55, v55 row_half_mirror row_mask:0xf bank_mask:0xf\n"
      "v_smfmac_f32_16x16x64_f16 v[44:47], v[148:151], v[16:23], v242\nv_pk_mul_f16 v31, v62, v57 op_sel_hi:[0,1]\nv_add_f32_dpp v55, v55, v55 row_mirror row_mask:0xf bank_mask:0xf\n"
      "v_mfma_f32_16x16x32_f16 v[32:35], v[152:155], v[24:27], v[32:35]\nv_pk_mul_f16 v16, v56, v62 op_sel_hi:[0,1]\nv_pk_mul_f16 v17, v56, v63 op_sel_hi:[0,1]\nds_write_b32 v243, v55 offset:512\n"
      "v_mfma_f32_16x16x32_f16 v[32:35], v[164:167], v[28:31], v[32:35]\nv_pk_mul_f16 v18, v58, v62 op_sel_hi:[0,1]\nds_read_b64_tr_b16 v[64:65], v237 offset:11648\n"
      "v_mfma_f32_16x16x32_f16 v[36:39], v[168:171], v[28:31], v[36:39]\nv_pk_mul_f16 v19, v58, v63 op_sel_hi:[0,1]\nv_mfma_f32_16x16x32_f16 v[36:39], v[156:159], v[24:27], v[36:39]\n"
      "v_pk_mul_f16 v20, v60, v62 op_sel_hi:[0,1]\ns_setprio 3\ns_ff0_i32_b32 s53, s44\nv_mfma_f32_16x16x32_f16 v[244:247], v[160:163], v[24:27], v[244:247]\n"
      "v_pk_mul_f16 v21, v60, v63 op_sel_hi:[0,1]\ns_waitcnt lgkmcnt(1)\nv_readlane_b32 s48, v69, 48\nv_mfma_f32_16x16x32_f16 v[244:247], v[172:175], v[28:31], v[244:247]\n"
      "v_pk_mul_f16 v22, v60, v60 op_sel_hi:[0,1]\ns_add_u32 s53, s53, s64\nv_smfmac_f32_16x16x64_f16 v[44:47], v[176:179], v[24:31], v242\nv_pk_mul_f16 v23, v60, v57 op_sel_hi:[0,1]\n"
      "v_mfma_f32_16x16x32_f16 v[32:35], v[180:183], v[16:19], v[32:35]\nv_pk_mul_f16 v24, v56, v60 op_sel_hi:[0,1]\nv_pk_mul_f16 v25, v56, v61 op_sel_hi:[0,1]\n"
      "v_mfma_f32_16x16x32_f16 v[32:35], v[192:195], v[20:23], v[32:35]\nv_pk_mul_f16 v26, v58, v60 op_sel_hi:[0,1]\nv_pk_mul_f16 v27, v58, v61 op_sel_hi:[0,1]\n"
      "v_mfma_f32_16x16x32_f16 v[36:39], v[196:199], v[20:23], v[36:39]\nv_pk_mul_f16 v28, v58, v58 op_sel_hi:[0,1]\nds_read_b64_tr_b16 v[62:63], v237 offset:11520\n"
      "v_mfma_f32_16x16x32_f16 v[36:39], v[184:187], v[16:19], v[36:39]\nv_pk_mul_f16 v29, v58, v57 op_sel_hi:[0,1]\nds_read_b64_tr_b16 v[60:61], v237 offset:11392\n"
      "v_mfma_f32_16x16x32_f16 v[244:247], v[188:191], v[16:19], v[244:247]\nv_pk_mul_f16 v30, v56, v58 op_sel_hi:[0,1]\nv_mfma_f32_16x16x32_f16 v[244:247], v[200:203], v[20:23], v[244:247]\n"
      "v_pk_mul_f16 v31, v56, v59 op_sel_hi:[0,1]\nv_smfmac_f32_16x16x64_f16 v[44:47], v[204:207], v[16:23], v242\nds_read_b64_tr_b16 v[56:57], v236 offset:11136\n"
      "ds_read_b64_tr_b16 v[58:59], v237 offset:11264\nv_smfmac_f32_16x16x64_f16 v[44:47], v[232:235], v[24:31], v242\nv_pk_mul_f16 v16, v68, s48 op_sel:[0,1]\n"
      "v_pk_mul_f16 v17, v69, s48 op_sel:[0,1]\nv_mfma_f32_16x16x32_f16 v[32:35], v[208:211], v[24:27], v[32:35]\nv_pk_mul_f16 v18, v70, s48 op_sel:[0,1]\nv_pk_mul_f16 v19, v71, s48 op_sel:[0,1]\n"
      "v_mfma_f32_16x16x32_f16 v[32:35], v[220:223], v[28:31], v[32:35]\nv_pk_mul_f16 v20, v68, v70\nv_pk_mul_f16 v21, v69, v71\nv_mfma_f32_16x16x32_f16 v[36:39], v[224:227], v[28:31], v[36:39]\n"
      "v_add_f32 v44, v44, v45\nv_add_f32 v46, v46, v47\nv_mfma_f32_16x16x32_f16 v[36:39], v[212:215], v[24:27], v[36:39]\nv_mul_f32 v55, v44, v54\nv_mul_f32 v55, v55, v46\n"
      "ds_read_b128 v[44:47], v248 offset:48\nv_mfma_f32_16x16x32_f16 v[244:247], v[216:219], v[24:27], v[244:247]\ns_waitcnt lgkmcnt(1)\nv_pk_mul_f16 v22, v56, v56 op_sel_hi:[0,1]\n"
      "v_pk_mul_f16 v23, v56, v57 op_sel_hi:[0,1]\nv_mfma_f32_16x16x32_f16 v[244:247], v[228:231], v[28:31], v[244:247]\ns_cmp_lt_u32 s45, s53\nv_mul_f32 v32, v32, v48\nv_mul_f32 v33, v33, v49\n"
      "v_mul_f32 v34, v34, v50\ns_cbranch_scc0 .Lc_s5\n"
      ".Lc_u6:\nv_mfma_f32_16x16x32_f16 v[40:43], v[76:79], v[16:19], 0\nds_read_b32 v4, v248\nv_fmac_f32 v55, v32, v36\nv_mul_f32 v35, v35, v51\nv_fmac_f32 v55, v33, v37\n"
      "v_mfma_f32_16x16x32_f16 v[40:43], v[88:91], v[20:23], v[40:43]\nv_fmac_f32 v55, v34, v38\nv_fmac_f32 v55, v35, v39\ns_add_u32 s45, s45, 2\ns_cmp_ge_u32 s45, s46\ns_cbranch_scc0 .Lc_b6\n"
      "s_swappc_b64 s[4:5], s[34:35]\n"
      ".Lc_b6:\nv_mfma_f32_16x16x32_f16 v[36:39], v[84:87], v[20:23], 0\nv_pk_mul_f16 v24, v66, v66 op_sel_hi:[0,1]\nv_pk_mul_f16 v25, v66, v57 op_sel_hi:[0,1]\n"
      "v_mfma_f32_16x16x32_f16 v[36:39], v[72:75], v[16:19], v[36:39]\nv_pk_mul_f16 v26, v56, v66 op_sel_hi:[0,1]\nv_pk_mul_f16 v27, v56, v67 op_sel_hi:[0,1]\ns_setprio 0\n"
      "v_mfma_f32_16x16x32_f16 v[32:35], v[80:83], v[20:23], 0\nv_pk_mul_f16 v28, v58, v66 op_sel_hi:[0,1]\nv_pk_mul_f16 v29, v58, v67 op_sel_hi:[0,1]\ns_waitcnt lgkmcnt(0)\n"
      "v_smfmac_f32_16x16x64_f16 v[44:47], v[92:95], v[16:23], v242\nv_pk_mul_f16 v30, v60, v66 op_sel_hi:[0,1]\nv_pk_mul_f16 v31, v60, v67 op_sel_hi:[0,1]\n"
      "v_mfma_f32_16x16x32_f16 v[32:35], v[96:99], v[24:27], v[32:35]\nv_pk_mul_f16 v16, v62, v66 op_sel_hi:[0,1]\nv_pk_mul_f16 v17, v62, v67 op_sel_hi:[0,1]\ns_setprio 1\n"
      "v_mfma_f32_16x16x32_f16 v[32:35], v[108:111], v[28:31], v[32:35]\nv_pk_mul_f16 v18, v64, v66 op_sel_hi:[0,1]\nv_mul_f32 v244, v244, v52\n"
      "v_mfma_f32_16x16x32_f16 v[36:39], v[112:115], v[28:31], v[36:39]\nv_pk_mul_f16 v19, v64, v67 op_sel_hi:[0,1]\nv_mul_f32 v245, v245, v53\n"
      "v_mfma_f32_16x16x32_f16 v[36:39], v[100:103], v[24:27], v[36:39]\nv_pk_mul_f16 v20, v64, v64 op_sel_hi:[0,1]\nv_fmac_f32 v55, v244, v246\n"
      "v_mfma_f32_16x16x32_f16 v[40:43], v[104:107], v[24:27], v[40:43]\nv_pk_mul_f16 v21, v64, v57 op_sel_hi:[0,1]\nv_fmac_f32 v55, v245, v247\n"
      "v_mfma_f32_16x16x32_f16 v[40:43], v[116:119], v[28:31], v[40:43]\nv_pk_mul_f16 v22, v56, v64 op_sel_hi:[0,1]\nv_readfirstlane_b32 s44, v4\n"
      "v_smfmac_f32_16x16x64_f16 v[44:47], v[120:123], v[24:31], v242\nv_pk_mul_f16 v23, v56, v65 op_sel_hi:[0,1]\nv_mfma_f32_16x16x32_f16 v[32:35], v[124:127], v[16:19], v[32:35]\n"
      "v_pk_mul_f16 v24, v58, v64 op_sel_hi:[0,1]\nv_pk_mul_f16 v25, v58, v65 op_sel_hi:[0,1]\ns_setprio 2\nds_read_b64_tr_b16 v[70:71], v239 offset:12992\n"
      "v_mfma_f32_16x16x32_f16 v[32:35], v[136:139], v[20:23], v[32:35]\nv_pk_mul_f16 v26, v60, v64 op_sel_hi:[0,1]\nds_read_b64_tr_b16 v[68:69], v238 offset:12992\n"
      "v_mfma_f32_16x16x32_f16 v[36:39], v[140:143], v[20:23], v[36:39]\nv_pk_mul_f16 v27, v60, v65 op_sel_hi:[0,1]\nds_read_b64_tr_b16 v[66:67], v237 offset:13632\n"
      "v_mfma_f32_16x16x32_f16 v[36:39], v[128:131], v[16:19], v[36:39]\nv_pk_mul_f16 v28, v62, v64 op_sel_hi:[0,1]\nv_add_f32_dpp v55, v55, v55 quad_perm:[1,0,3,2] row_mask:0xf bank_mask:0xf\n"
      "v_mfma_f32_16x16x32_f16 v[40:43], v[132:135], v[16:19], v[40:43]\nv_pk_mul_f16 v29, v62, v65 op_sel_hi:[0,1]\nv_add_f32_dpp v55, v55, v55 quad_perm:[2,3,0,1] row_mask:0xf bank_mask:0xf\n"
      "v_mfma_f32_16x16x32_f16 v[40:43], v[144:147], v[20:23], v[40:43]\nv_pk_mul_f16 v30, v62, v62 op_sel_hi:[0,1]\nv_add_f32_dpp v55, v55, v55 row_half_mirror row_mask:0xf bank_mask:0xf\n"
      "v_smfmac_f32_16x16x64_f16 v[44:47], v[148:151], v[16:23], v242\nv_pk_mul_f16 v31, v62, v57 op_sel_hi:[0,1]\nv_add_f32_dpp v55, v55, v55 row_mirror row_mask:0xf bank_mask:0xf\n"
      "v_mfma_f32_16x16x32_f16 v[32:35], v[152:155], v[24:27], v[32:35]\nv_pk_mul_f16 v16, v56, v62 op_sel_hi:[0,1]\nv_pk_mul_f16 v17, v56, v63 op_sel_hi:[0,1]\nds_write_b32 v243, v55 offset:640\n"
      "v_mfma_f32_16x16x32_f16 v[32:35], v[164:167], v[28:31], v[32:35]\nv_pk_mul_f16 v18, v58, v62 op_sel_hi:[0,1]\nds_read_b64_tr_b16 v[64:65], v237 offset:13504\n"
      "v_mfma_f32_16x16x32_f16 v[36:39], v[168:171], v[28:31], v[36:39]\nv_pk_mul_f16 v19, v58, v63 op_sel_hi:[0,1]\nv_mfma_f32_16x16x32_f16 v[36:39], v[156:159], v[24:27], v[36:39]\n"
      "v_pk_mul_f16 v20, v60, v62 op_sel_hi:[0,1]\ns_setprio 3\ns_ff0_i32_b32 s53, s44\nv_mfma_f32_16x16x32_f16 v[40:43], v[160:163], v[24:27], v[40:43]\n"
      "v_pk_mul_f16 v21, v60, v63 op_sel_hi:[0,1]\ns_waitcnt lgkmcnt(1)\nv_readlane_b32 s48, v69, 48\nv_mfma_f32_16x16x32_f16 v[40:43], v[172:175], v[28:31], v[40:43]\n"
      "v_pk_mul_f16 v22, v60, v60 op_sel_hi:[0,1]\ns_add_u32 s53, s53, s64\nv_smfmac_f32_16x16x64_f16 v[44:47], v[176:179], v[24:31], v242\nv_pk_mul_f16 v23, v60, v57 op_sel_hi:[0,1]\n"
      "v_mfma_f32_16x16x32_f16 v[32:35], v[180:183], v[16:19], v[32:35]\nv_pk_mul_f16 v24, v56, v60 op_sel_hi:[0,1]\nv_pk_mul_f16 v25, v56, v61 op_sel_hi:[0,1]\n"
      "v_mfma_f32_16x16x32_f16 v[32:35], v[192:195], v[20:23], v[32:35]\nv_pk_mul_f16 v26, v58, v60 op_sel_hi:[0,1]\nv_pk_mul_f16 v27, v58, v61 op_sel_hi:[0,1]\n"
      "v_mfma_f32_16x16x32_f16 v[36:39], v[196:199], v[20:23], v[36:39]\nv_pk_mul_f16 v28, v58, v58 op_sel_hi:[0,1]\nds_read_b64_tr_b16 v[62:63], v237 offset:13376\n"
      "v_mfma_f32_16x16x32_f16 v[36:39], v[184:187], v[16:19], v[36:39]\nv_pk_mul_f16 v29, v58, v57 op_sel_hi:[0,1]\nds_read_b64_tr_b16 v[60:61], v237 offset:13248\n"
      "v_mfma_f32_16x16x32_f16 v[40:43], v[188:191], v[16:19], v[40:43]\nv_pk_mul_f16 v30, v56, v58 op_sel_hi:[0,1]\nv_mfma_f32_16x16x32_f16 v[40:43], v[200:203], v[20:23], v[40:43]\n"
      "v_pk_mul_f16 v31, v56, v59 op_sel_hi:[0,1]\nv_smfmac_f32_16x16x64_f16 v[44:47], v[204:207], v[16:23], v242\nds_read_b64_tr_b16 v[56:57], v236 offset:12992\n"
      "ds_read_b64_tr_b16 v[58:59], v237 offset:13120\nv_smfmac_f32_16x16x64_f16 v[44:47], v[232:235], v[24:31], v242\nv_pk_mul_f16 v16, v68, s48 op_sel:[0,1]\n"
      "v_pk_mul_f16 v17, v69, s48 op_sel:[0,1]\nv_mfma_f32_16x16x32_f16 v[32:35], v[208:211], v[24:27], v[32:35]\nv_pk_mul_f16 v18, v70, s48 op_sel:[0,1]\nv_pk_mul_f16 v19, v71, s48 op_sel:[0,1]\n"
      "v_mfma_f32_16x16x32_f16 v[32:35], v[220:223], v[28:31], v[32:35]\nv_pk_mul_f16 v20, v68, v70\nv_pk_mul_f16 v21, v69, v71\nv_mfma_f32_16x16x32_f16 v[36:39], v[224:227], v[28:31], v[36:39]\n"
      "v_add_f32 v44, v44, v45\nv_add_f32 v46, v46, v47\nv_mfma_f32_16x16x32_f16 v[36:39], v[212:215], v[24:27], v[36:39]\nv_mul_f32 v55, v44, v54\nv_mul_f32 v55, v55, v46\n"
      "ds_read_b128 v[44:47], v248 offset:48\nv_mfma_f32_16x16x32_f16 v[40:43], v[216:219], v[24:27], v[40:43]\ns_waitcnt lgkmcnt(1)\nv_pk_mul_f16 v22, v56, v56 op_sel_hi:[0,1]\n"
      "v_pk_mul_f16 v23, v56, v57 op_sel_hi:[0,1]\nv_mfma_f32_16x16x32_f16 v[40:43], v[228:231], v[28:31], v[40:43]\ns_cmp_lt_u32 s45, s53\nv_mul_f32 v32, v32, v48\nv_mul_f32 v33, v33, v49\n"
      "v_mul_f32 v34, v34, v50\ns_cbranch_scc0 .Lc_s6\n"
      ".Lc_u7:\nv_mfma_f32_16x16x32_f16 v[244:247], v[76:79], v[16:19], 0\nds_read_b32 v4, v248\nv_fmac_f32 v55, v32, v36\nv_mul_f32 v35, v35, v51\nv_fmac_f32 v55, v33, v37\n"
      "v_mfma_f32_16x16x32_f16 v[244:247], v[88:91], v[20:23], v[244:247]\nv_fmac_f32 v55, v34, v38\nv_fmac_f32 v55, v35, v39\ns_add_u32 s45, s45, 2\ns_cmp_ge_u32 s45, s46\ns_cbranch_scc0 .Lc_b7\n"
      "s_swappc_b64 s[4:5], s[34:35]\n"
      ".Lc_b7:\nv_mfma_f32_16x16x32_f16 v[36:39], v[84:87], v[20:23], 0\nv_pk_mul_f16 v24, v66, v66 op_sel_hi:[0,1]\nv_pk_mul_f16 v25, v66, v57 op_sel_hi:[0,1]\n"
      "v_mfma_f32_16x16x32_f16 v[36:39], v[72:75], v[16:19], v[36:39]\nv_pk_mul_f16 v26, v56, v66 op_sel_hi:[0,1]\nv_pk_mul_f16 v27, v56, v67 op_sel_hi:[0,1]\ns_setprio 0\n"
      "v_mfma_f32_16x16x32_f16 v[32:35], v[80:83], v[20:23], 0\nv_pk_mul_f16 v28, v58, v66 op_sel_hi:[0,1]\nv_pk_mul_f16 v29, v58, v67 op_sel_hi:[0,1]\ns_waitcnt lgkmcnt(0)\n"
      "v_smfmac_f32_16x16x64_f16 v[44:47], v[92:95], v[16:23], v242\nv_pk_mul_f16 v30, v60, v66 op_sel_hi:[0,1]\nv_pk_mul_f16 v31, v60, v67 op_sel_hi:[0,1]\n"
      "v_mfma_f32_16x16x32_f16 v[32:35], v[96:99], v[24:27], v[32:35]\nv_pk_mul_f16 v16, v62, v66 op_sel_hi:[0,1]\nv_pk_mul_f16 v17, v62, v67 op_sel_hi:[0,1]\ns_setprio 1\n"
      "v_mfma_f32_16x16x32_f16 v[32:35], v[108:111], v[28:31], v[32:35]\nv_pk_mul_f16 v18, v64, v66 op_sel_hi:[0,1]\nv_mul_f32 v40, v40, v52\n"
      "v_mfma_f32_16x16x32_f16 v[36:39], v[112:115], v[28:31], v[36:39]\nv_pk_mul_f16 v19, v64, v67 op_sel_hi:[0,1]\nv_mul_f32 v41, v41, v53\n"
      "v_mfma_f32_16x16x32_f16 v[36:39], v[100:103], v[24:27], v[36:39]\nv_pk_mul_f16 v20, v64, v64 op_sel_hi:[0,1]\nv_fmac_f32 v55, v40, v42\n"
      "v_mfma_f32_16x16x32_f16 v[244:247], v[104:107], v[24:27], v[244:247]\nv_pk_mul_f16 v21, v64, v57 op_sel_hi:[0,1]\nv_fmac_f32 v55, v41, v43\n"
      "v_mfma_f32_16x16x32_f16 v[244:247], v[116:119], v[28:31], v[244:247]\nv_pk_mul_f16 v22, v56, v64 op_sel_hi:[0,1]\nv_readfirstlane_b32 s44, v4\n"
      "v_smfmac_f32_16x16x64_f16 v[44:47], v[120:123], v[24:31], v242\nv_pk_mul_f16 v23, v56, v65 op_sel_hi:[0,1]\nv_mfma_f32_16x16x32_f16 v[32:35], v[124:127], v[16:19], v[32:35]\n"
      "v_pk_mul_f16 v24, v58, v64 op_sel_hi:[0,1]\nv_pk_mul_f16 v25, v58, v65 op_sel_hi:[0,1]\ns_setprio 2\nds_read_b64_tr_b16 v[70:71], v239 offset:14848\n"
      "v_mfma_f32_16x16x32_f16 v[32:35], v[136:139], v[20:23], v[32:35]\nv_pk_mul_f16 v26, v60, v64 op_sel_hi:[0,1]\nds_read_b64_tr_b16 v[68:69], v238 offset:14848\n"
      "v_mfma_f32_16x16x32_f16 v[36:39], v[140:143], v[20:23], v[36:39]\nv_pk_mul_f16 v27, v60, v65 op_sel_hi:[0,1]\nds_read_b64_tr_b16 v[66:67], v237 offset:15488\n"
      "v_mfma_f32_16x16x32_f16 v[36:39], v[128:131], v[16:19], v[36:39]\nv_pk_mul_f16 v28, v62, v64 op_sel_hi:[0,1]\nv_add_f32_dpp v55, v55, v55 quad_perm:[1,0,3,2] row_mask:0xf bank_mask:0xf\n"
      "v_mfma_f32_16x16x32_f16 v[244:247], v[132:135], v[16:19], v[244:247]\nv_pk_mul_f16 v29, v62, v65 op_sel_hi:[0,1]\nv_add_f32_dpp v55, v55, v55 quad_perm:[2,3,0,1] row_mask:0xf bank_mask:0xf\n"
      "v_mfma_f32_16x16x32_f16 v[244:247], v[144:147], v[20:23], v[244:247]\nv_pk_mul_f16 v30, v62, v62 op_sel_hi:[0,1]\nv_add_f32_dpp v55, v55, v55 row_half_mirror row_mask:0xf bank_mask:0xf\n"
      "v_smfmac_f32_16x16x64_f16 v[44:47], v[148:151], v[16:23], v242\nv_pk_mul_f16 v31, v62, v57 op_sel_hi:[0,1]\nv_add_f32_dpp v55, v55, v55 row_mirror row_mask:0xf bank_mask:0xf\n"
      "v_mfma_f32_16x16x32_f16 v[32:35], v[152:155], v[24:27], v[32:35]\nv_pk_mul_f16 v16, v56, v62 op_sel_hi:[0,1]\nv_pk_mul_f16 v17, v56, v63 op_sel_hi:[0,1]\nds_write_b32 v243, v55 offset:768\n"
      "v_mfma_f32_16x16x32_f16 v[32:35], v[164:167], v[28:31], v[32:35]\nv_pk_mul_f16 v18, v58, v62 op_sel_hi:[0,1]\nds_read_b64_tr_b16 v[64:65], v237 offset:15360\n"
      "v_mfma_f32_16x16x32_f16 v[36:39], v[168:171], v[28:31], v[36:39]\nv_pk_mul_f16 v19, v58, v63 op_sel_hi:[0,1]\nv_mfma_f32_16x16x32_f16 v[36:39], v[156:159], v[24:27], v[36:39]\n"
      "v_pk_mul_f16 v20, v60, v62 op_sel_hi:[0,1]\ns_setprio 3\ns_ff0_i32_b32 s53, s44\nv_mfma_f32_16x16x32_f16 v[244:247], v[160:163], v[24:27], v[244:247]\n"
      "v_pk_mul_f16 v21, v60, v63 op_sel_hi:[0,1]\ns_waitcnt lgkmcnt(1)\nv_readlane_b32 s48, v69, 48\nv_mfma_f32_16x16x32_f16 v[244:247], v[172:175], v[28:31], v[244:247]\n"
      "v_pk_mul_f16 v22, v60, v60 op_sel_hi:[0,1]\ns_add_u32 s53, s53, s64\nv_smfmac_f32_16x16x64_f16 v[44:47], v[176:179], v[24:31], v242\nv_pk_mul_f16 v23, v60, v57 op_sel_hi:[0,1]\n"
      "v_mfma_f32_16x16x32_f16 v[32:35], v[180:183], v[16:19], v[32:35]\nv_pk_mul_f16 v24, v56, v60 op_sel_hi:[0,1]\nv_pk_mul_f16 v25, v56, v61 op_sel_hi:[0,1]\n"
      "v_mfma_f32_16x16x32_f16 v[32:35], v[192:195], v[20:23], v[32:35]\nv_pk_mul_f16 v26, v58, v60 op_sel_hi:[0,1]\nv_pk_mul_f16 v27, v58, v61 op_sel_hi:[0,1]\n"
      "v_mfma_f32_16x16x32_f16 v[36:39], v[196:199], v[20:23], v[36:39]\nv_pk_mul_f16 v28, v58, v58 op_sel_hi:[0,1]\nds_read_b64_tr_b16 v[62:63], v237 offset:15232\n"
      "v_mfma_f32_16x16x32_f16 v[36:39], v[184:187], v[16:19], v[36:39]\nv_pk_mul_f16 v29, v58, v57 op_sel_hi:[0,1]\nds_read_b64_tr_b16 v[60:61], v237 offset:15104\n"
      "v_mfma_f32_16x16x32_f16 v[244:247], v[188:191], v[16:19], v[244:247]\nv_pk_mul_f16 v30, v56, v58 op_sel_hi:[0,1]\nv_mfma_f32_16x16x32_f16 v[244:247], v[200:203], v[20:23], v[244:247]\n"
      "v_pk_mul_f16 v31, v56, v59 op_sel_hi:[0,1]\nv_smfmac_f32_16x16x64_f16 v[44:47], v[204:207], v[16:23], v242\nds_read_b64_tr_b16 v[56:57], v236 offset:14848\n"
      "ds_read_b64_tr_b16 v[58:59], v237 offset:14976\nv_smfmac_f32_16x16x64_f16 v[44:47], v[232:235], v[24:31], v242\nv_pk_mul_f16 v16, v68, s48 op_sel:[0,1]\n"
      "v_pk_mul_f16 v17, v69, s48 op_sel:[0,1]\nv_mfma_f32_16x16x32_f16 v[32:35], v[208:211], v[24:27], v[32:35]\nv_pk_mul_f16 v18, v70, s48 op_sel:[0,1]\nv_pk_mul_f16 v19, v71, s48 op_sel:[0,1]\n"
      "v_mfma_f32_16x16x32_f16 v[32:35], v[220:223], v[28:31], v[32:35]\nv_pk_mul_f16 v20, v68, v70\nv_pk_mul_f16 v21, v69, v71\nv_mfma_f32_16x16x32_f16 v[36:39], v[224:227], v[28:31], v[36:39]\n"
      "v_add_f32 v44, v44, v45\nv_add_f32 v46, v46, v47\nv_mfma_f32_16x16x32_f16 v[36:39], v[212:215], v[24:27], v[36:39]\nv_mul_f32 v55, v44, v54\nv_mul_f32 v55, v55, v46\n"
      "ds_read_b128 v[44:47], v248 offset:48\nv_mfma_f32_16x16x32_f16 v[244:247], v[216:219], v[24:27], v[244:247]\ns_waitcnt lgkmcnt(1)\nv_pk_mul_f16 v22, v56, v56 op_sel_hi:[0,1]\n"
      "v_pk_mul_f16 v23, v56, v57 op_sel_hi:[0,1]\nv_mfma_f32_16x16x32_f16 v[244:247], v[228:231], v[28:31], v[244:247]\ns_cmp_lt_u32 s45, s53\nv_mul_f32 v32, v32, v48\nv_mul_f32 v33, v33, v49\n"
      "v_mul_f32 v34, v34, v50\ns_cbranch_scc0 .Lc_s7\n"
      ".Lc_u8:\nv_mfma_f32_16x16x32_f16 v[40:43], v[76:79], v[16:19], 0\nds_read_b32 v4, v248\nv_fmac_f32 v55, v32, v36\nv_mul_f32 v35, v35, v51\nv_fmac_f32 v55, v33, v37\n"
      "v_mfma_f32_16x16x32_f16 v[40:43], v[88:91], v[20:23], v[40:43]\nv_fmac_f32 v55, v34, v38\nv_fmac_f32 v55, v35, v39\ns_add_u32 s45, s45, 2\ns_cmp_ge_u32 s45, s46\ns_cbranch_scc0 .Lc_b8\n"
      "s_swappc_b64 s[4:5], s[34:35]\n"
      ".Lc_b8:\nv_mfma_f32_16x16x32_f16 v[36:39], v[84:87], v[20:23], 0\nv_pk_mul_f16 v24, v66, v66 op_sel_hi:[0,1]\nv_pk_mul_f16 v25, v66, v57 op_sel_hi:[0,1]\n"
      "v_mfma_f32_16x16x32_f16 v[36:39], v[72:75], v[16:19], v[36:39]\nv_pk_mul_f16 v26, v56, v66 op_sel_hi:[0,1]\nv_pk_mul_f16 v27, v56, v67 op_sel_hi:[0,1]\ns_setprio 0\n"
      "v_mfma_f32_16x16x32_f16 v[32:35], v[80:83], v[20:23], 0\nv_pk_mul_f16 v28, v58, v66 op_sel_hi:[0,1]\nv_pk_mul_f16 v29, v58, v67 op_sel_hi:[0,1]\ns_waitcnt lgkmcnt(0)\n"
      "v_smfmac_f32_16x16x64_f16 v[44:47], v[92:95], v[16:23], v242\nv_pk_mul_f16 v30, v60, v66 op_sel_hi:[0,1]\nv_pk_mul_f16 v31, v60, v67 op_sel_hi:[0,1]\n"
      "v_mfma_f32_16x16x32_f16 v[32:35], v[96:99], v[24:27], v[32:35]\nv_pk_mul_f16 v16, v62, v66 op_sel_hi:[0,1]\nv_pk_mul_f16 v17, v62, v67 op_sel_hi:[0,1]\ns_setprio 1\n"
      "v_mfma_f32_16x16x32_f16 v[32:35], v[108:111], v[28:31], v[32:35]\nv_pk_mul_f16 v18, v64, v66 op_sel_hi:[0,1]\nv_mul_f32 v244, v244, v52\n"
      "v_mfma_f32_16x16x32_f16 v[36:39], v[112:115], v[28:31], v[36:39]\nv_pk_mul_f16 v19, v64, v67 op_sel_hi:[0,1]\nv_mul_f32 v245, v245, v53\n"
      "v_mfma_f32_16x16x32_f16 v[36:39], v[100:103], v[24:27], v[36:39]\nv_pk_mul_f16 v20, v64, v64 op_sel_hi:[0,1]\nv_fmac_f32 v55, v244, v246\n"
      "v_mfma_f32_16x16x32_f16 v[40:43], v[104:107], v[24:27], v[40:43]\nv_pk_mul_f16 v21, v64, v57 op_sel_hi:[0,1]\nv_fmac_f32 v55, v245, v247\n"
      "v_mfma_f32_16x16x32_f16 v[40:43], v[116:119], v[28:31], v[40:43]\nv_pk_mul_f16 v22, v56, v64 op_sel_hi:[0,1]\nv_readfirstlane_b32 s44, v4\n"
      "v_smfmac_f32_16x16x64_f16 v[44:47], v[120:123], v[24:31], v242\nv_pk_mul_f16 v23, v56, v65 op_sel_hi:[0,1]\nv_mfma_f32_16x16x32_f16 v[32:35], v[124:127], v[16:19], v[32:35]\n"
      "v_pk_mul_f16 v24, v58, v64 op_sel_hi:[0,1]\nv_pk_mul_f16 v25, v58, v65 op_sel_hi:[0,1]\ns_setprio 2\nds_read_b64_tr_b16 v[70:71], v239 offset:16704\n"
      "v_mfma_f32_16x16x32_f16 v[32:35], v[136:139], v[20:23], v[32:35]\nv_pk_mul_f16 v26, v60, v64 op_sel_hi:[0,1]\nds_read_b64_tr_b16 v[68:69], v238 offset:16704\n"
      "v_mfma_f32_16x16x32_f16 v[36:39], v[140:143], v[20:23], v[36:39]\nv_pk_mul_f16 v27, v60, v65 op_sel_hi:[0,1]\nds_read_b64_tr_b16 v[66:67], v237 offset:17344\n"
      "v_mfma_f32_16x16x32_f16 v[36:39], v[128:131], v[16:19], v[36:39]\nv_pk_mul_f16 v28, v62, v64 op_sel_hi:[0,1]\nv_add_f32_dpp v55, v55, v55 quad_perm:[1,0,3,2] row_mask:0xf bank_mask:0xf\n"
      "v_mfma_f32_16x16x32_f16 v[40:43], v[132:135], v[16:19], v[40:43]\nv_pk_mul_f16 v29, v62, v65 op_sel_hi:[0,1]\nv_add_f32_dpp v55, v55, v55 quad_perm:[2,3,0,1] row_mask:0xf bank_mask:0xf\n"
      "v_mfma_f32_16x16x32_f16 v[40:43], v[144:147], v[20:23], v[40:43]\nv_pk_mul_f16 v30, v62, v62 op_sel_hi:[0,1]\nv_add_f32_dpp v55, v55, v55 row_half_mirror row_mask:0xf bank_mask:0xf\n"
      "v_smfmac_f32_16x16x64_f16 v[44:47], v[148:151], v[16:23], v242\nv_pk_mul_f16 v31, v62, v57 op_sel_hi:[0,1]\nv_add_f32_dpp v55, v55, v55 row_mirror row_mask:0xf bank_mask:0xf\n"
      "v_mfma_f32_16x16x32_f16 v[32:35], v[152:155], v[24:27], v[32:35]\nv_pk_mul_f16 v16, v56, v62 op_sel_hi:[0,1]\nv_pk_mul_f16 v17, v56, v63 op_sel_hi:[0,1]\nds_write_b32 v243, v55 offset:896\n"
      "v_mfma_f32_16x16x32_f16 v[32:35], v[164:167], v[28:31], v[32:35]\nv_pk_mul_f16 v18, v58, v62 op_sel_hi:[0,1]\nds_read_b64_tr_b16 v[64:65], v237 offset:17216\n"
      "v_mfma_f32_16x16x32_f16 v[36:39], v[168:171], v[28:31], v[36:39]\nv_pk_mul_f16 v19, v58, v63 op_sel_hi:[0,1]\nv_mfma_f32_16x16x32_f16 v[36:39], v[156:159], v[24:27], v[36:39]\n"
      "v_pk_mul_f16 v20, v60, v62 op_sel_hi:[0,1]\ns_setprio 3\ns_ff0_i32_b32 s53, s44\nv_mfma_f32_16x16x32_f16 v[40:43], v[160:163], v[24:27], v[40:43]\n"
      "v_pk_mul_f16 v21, v60, v63 op_sel_hi:[0,1]\ns_waitcnt lgkmcnt(1)\nv_readlane_b32 s48, v69, 48\nv_mfma_f32_16x16x32_f16 v[40:43], v[172:175], v[28:31], v[40:43]\n"
      "v_pk_mul_f16 v22, v60, v60 op_sel_hi:[0,1]\ns_add_u32 s53, s53, s64\nv_smfmac_f32_16x16x64_f16 v[44:47], v[176:179], v[24:31], v242\nv_pk_mul_f16 v23, v60, v57 op_sel_hi:[0,1]\n"
      "v_mfma_f32_16x16x32_f16 v[32:35], v[180:183], v[16:19], v[32:35]\nv_pk_mul_f16 v24, v56, v60 op_sel_hi:[0,1]\nv_pk_mul_f16 v25, v56, v61 op_sel_hi:[0,1]\n"
      "v_mfma_f32_16x16x32_f16 v[32:35], v[192:195], v[20:23], v[32:35]\nv_pk_mul_f16 v26, v58, v60 op_sel_hi:[0,1]\nv_pk_mul_f16 v27, v58, v61 op_sel_hi:[0,1]\n"
      "v_mfma_f32_16x16x32_f16 v[36:39], v[196:199], v[20:23], v[36:39]\nv_pk_mul_f16 v28, v58, v58 op_sel_hi:[0,1]\nds_read_b64_tr_b16 v[62:63], v237 offset:17088\n"
      "v_mfma_f32_16x16x32_f16 v[36:39], v[184:187], v[16:19], v[36:39]\nv_pk_mul_f16 v29, v58, v57 op_sel_hi:[0,1]\nds_read_b64_tr_b16 v[60:61], v237 offset:16960\n"
      "v_mfma_f32_16x16x32_f16 v[40:43], v[188:191], v[16:19], v[40:43]\nv_pk_mul_f16 v30, v56, v58 op_sel_hi:[0,1]\nv_mfma_f32_16x16x32_f16 v[40:43], v[200:203], v[20:23], v[40:43]\n"
      "v_pk_mul_f16 v31, v56, v59 op_sel_hi:[0,1]\nv_smfmac_f32_16x16x64_f16 v[44:47], v[204:207], v[16:23], v242\nds_read_b64_tr_b16 v[56:57], v236 offset:16704\n"
      "ds_read_b64_tr_b16 v[58:59], v237 offset:16832\nv_smfmac_f32_16x16x64_f16 v[44:47], v[232:235], v[24:31], v242\nv_pk_mul_f16 v16, v68, s48 op_sel:[0,1]\n"
      "v_pk_mul_f16 v17, v69, s48 op_sel:[0,1]\nv_mfma_f32_16x16x32_f16 v[32:35], v[208:211], v[24:27], v[32:35]\nv_pk_mul_f16 v18, v70, s48 op_sel:[0,1]\nv_pk_mul_f16 v19, v71, s48 op_sel:[0,1]\n"
      "v_mfma_f32_16x16x32_f16 v[32:35], v[220:223], v[28:31], v[32:35]\nv_pk_mul_f16 v20, v68, v70\nv_pk_mul_f16 v21, v69, v71\nv_mfma_f32_16x16x32_f16 v[36:39], v[224:227], v[28:31], v[36:39]\n"
      "v_add_f32 v44, v44, v45\nv_add_f32 v46, v46, v47\nv_mfma_f32_16x16x32_f16 v[36:39], v[212:215], v[24:27], v[36:39]\nv_mul_f32 v55, v44, v54\nv_mul_f32 v55, v55, v46\n"
      "ds_read_b128 v[44:47], v248 offset:48\nv_mfma_f32_16x16x32_f16 v[40:43], v[216:219], v[24:27], v[40:43]\ns_waitcnt lgkmcnt(1)\nv_pk_mul_f16 v22, v56, v56 op_sel_hi:[0,1]\n"
      "v_pk_mul_f16 v23, v56, v57 op_sel_hi:[0,1]\nv_mfma_f32_16x16x32_f16 v[40:43], v[228:231], v[28:31], v[40:43]\ns_cmp_lt_u32 s45, s53\nv_mul_f32 v32, v32, v48\nv_mul_f32 v33, v33, v49\n"
      "v_mul_f32 v34, v34, v50\ns_cbranch_scc0 .Lc_s8\n"
      ".Lc_u9:\nv_mfma_f32_16x16x32_f16 v[244:247], v[76:79], v[16:19], 0\nds_read_b32 v4, v248\nv_fmac_f32 v55, v32, v36\nv_mul_f32 v35, v35, v51\nv_fmac_f32 v55, v33, v37\n"
      "v_mfma_f32_16x16x32_f16 v[244:247], v[88:91], v[20:23], v[244:247]\nv_fmac_f32 v55, v34, v38\nv_fmac_f32 v55, v35, v39\ns_add_u32 s45, s45, 2\ns_cmp_ge_u32 s45, s46\ns_cbranch_scc0 .Lc_b9\n"
      "s_swappc_b64 s[4:5], s[34:35]\n"
      ".Lc_b9:\nv_mfma_f32_16x16x32_f16 v[36:39], v[84:87], v[20:23], 0\nv_pk_mul_f16 v24, v66, v66 op_sel_hi:[0,1]\nv_pk_mul_f16 v25, v66, v57 op_sel_hi:[0,1]\n"
      "v_mfma_f32_16x16x32_f16 v[36:39], v[72:75], v[16:19], v[36:39]\nv_pk_mul_f16 v26, v56, v66 op_sel_hi:[0,1]\nv_pk_mul_f16 v27, v56, v67 op_sel_hi:[0,1]\ns_setprio 0\n"
      "v_mfma_f32_16x16x32_f16 v[32:35], v[80:83], v[20:23], 0\nv_pk_mul_f16 v28, v58, v66 op_sel_hi:[0,1]\nv_pk_mul_f16 v29, v58, v67 op_sel_hi:[0,1]\ns_waitcnt lgkmcnt(0)\n"
      "v_smfmac_f32_16x16x64_f16 v[44:47], v[92:95], v[16:23], v242\nv_pk_mul_f16 v30, v60, v66 op_sel_hi:[0,1]\nv_pk_mul_f16 v31, v60, v67 op_sel_hi:[0,1]\n"
      "v_mfma_f32_16x16x32_f16 v[32:35], v[96:99], v[24:27], v[32:35]\nv_pk_mul_f16 v16, v62, v66 op_sel_hi:[0,1]\nv_pk_mul_f16 v17, v62, v67 op_sel_hi:[0,1]\ns_setprio 1\n"
      "v_mfma_f32_16x16x32_f16 v[32:35], v[108:111], v[28:31], v[32:35]\nv_pk_mul_f16 v18, v64, v66 op_sel_hi:[0,1]\nv_mul_f32 v40, v40, v52\n"
      "v_mfma_f32_16x16x32_f16 v[36:39], v[112:115], v[28:31], v[36:39]\nv_pk_mul_f16 v19, v64, v67 op_sel_hi:[0,1]\nv_mul_f32 v41, v41, v53\n"
      "v_mfma_f32_16x16x32_f16 v[36:39], v[100:103], v[24:27], v[36:39]\nv_pk_mul_f16 v20, v64, v64 op_sel_hi:[0,1]\nv_fmac_f32 v55, v40, v42\n"
      "v_mfma_f32_16x16x32_f16 v[244:247], v[104:107], v[24:27], v[244:247]\nv_pk_mul_f16 v21, v64, v57 op_sel_hi:[0,1]\nv_fmac_f32 v55, v41, v43\n"
      "v_mfma_f32_16x16x32_f16 v[244:247], v[116:119], v[28:31], v[244:247]\nv_pk_mul_f16 v22, v56, v64 op_sel_hi:[0,1]\nv_readfirstlane_b32 s44, v4\n"
      "v_smfmac_f32_16x16x64_f16 v[44:47], v[120:123], v[24:31], v242\nv_pk_mul_f16 v23, v56, v65 op_sel_hi:[0,1]\nv_mfma_f32_16x16x32_f16 v[32:35], v[124:127], v[16:19], v[32:35]\n"
      "v_pk_mul_f16 v24, v58, v64 op_sel_hi:[0,1]\nv_pk_mul_f16 v25, v58, v65 op_sel_hi:[0,1]\ns_setprio 2\nds_read_b64_tr_b16 v[70:71], v239 offset:18560\n"
      "v_mfma_f32_16x16x32_f16 v[32:35], v[136:139], v[20:23], v[32:35]\nv_pk_mul_f16 v26, v60, v64 op_sel_hi:[0,1]\nds_read_b64_tr_b16 v[68:69], v238 offset:18560\n"
      "v_mfma_f32_16x16x32_f16 v[36:39], v[140:143], v[20:23], v[36:39]\nv_pk_mul_f16 v27, v60, v65 op_sel_hi:[0,1]\nds_read_b64_tr_b16 v[66:67], v237 offset:19200\n"
      "v_mfma_f32_16x16x32_f16 v[36:39], v[128:131], v[16:19], v[36:39]\nv_pk_mul_f16 v28, v62, v64 op_sel_hi:[0,1]\nv_add_f32_dpp v55, v55, v55 quad_perm:[1,0,3,2] row_mask:0xf bank_mask:0xf\n"
      "v_mfma_f32_16x16x32_f16 v[244:247], v[132:135], v[16:19], v[244:247]\nv_pk_mul_f16 v29, v62, v65 op_sel_hi:[0,1]\nv_add_f32_dpp v55, v55, v55 quad_perm:[2,3,0,1] row_mask:0xf bank_mask:0xf\n"
      "v_mfma_f32_16x16x32_f16 v[244:247], v[144:147], v[20:23], v[244:247]\nv_pk_mul_f16 v30, v62, v62 op_sel_hi:[0,1]\nv_add_f32_dpp v55, v55, v55 row_half_mirror row_mask:0xf bank_mask:0xf\n"
      "v_smfmac_f32_16x16x64_f16 v[44:47], v[148:151], v[16:23], v242\nv_pk_mul_f16 v31, v62, v57 op_sel_hi:[0,1]\nv_add_f32_dpp v55, v55, v55 row_mirror row_mask:0xf bank_mask:0xf\n"
      "v_mfma_f32_16x16x32_f16 v[32:35], v[152:155], v[24:27], v[32:35]\nv_pk_mul_f16 v16, v56, v62 op_sel_hi:[0,1]\nv_pk_mul_f16 v17, v56, v63 op_sel_hi:[0,1]\nds_write_b32 v243, v55 offset:1024\n"
      "v_mfma_f32_16x16x32_f16 v[32:35], v[164:167], v[28:31], v[32:35]\nv_pk_mul_f16 v18, v58, v62 op_sel_hi:[0,1]\nds_read_b64_tr_b16 v[64:65], v237 offset:19072\n"
      "v_mfma_f32_16x16x32_f16 v[36:39], v[168:171], v[28:31], v[36:39]\nv_pk_mul_f16 v19, v58, v63 op_sel_hi:[0,1]\nv_mfma_f32_16x16x32_f16 v[36:39], v[156:159], v[24:27], v[36:39]\n"
      "v_pk_mul_f16 v20, v60, v62 op_sel_hi:[0,1]\ns_setprio 3\ns_ff0_i32_b32 s53, s44\nv_mfma_f32_16x16x32_f16 v[244:247], v[160:163], v[24:27], v[244:247]\n"
      "v_pk_mul_f16 v21, v60, v63 op_sel_hi:[0,1]\ns_waitcnt lgkmcnt(1)\nv_readlane_b32 s48, v69, 48\nv_mfma_f32_16x16x32_f16 v[244:247], v[172:175], v[28:31], v[244:247]\n"
      "v_pk_mul_f16 v22, v60, v60 op_sel_hi:[0,1]\ns_add_u32 s53, s53, s64\nv_smfmac_f32_16x16x64_f16 v[44:47], v[176:179], v[24:31], v242\nv_pk_mul_f16 v23, v60, v57 op_sel_hi:[0,1]\n"
      "v_mfma_f32_16x16x32_f16 v[32:35], v[180:183], v[16:19], v[32:35]\nv_pk_mul_f16 v24, v56, v60 op_sel_hi:[0,1]\nv_pk_mul_f16 v25, v56, v61 op_sel_hi:[0,1]\n"
      "v_mfma_f32_16x16x32_f16 v[32:35], v[192:195], v[20:23], v[32:35]\nv_pk_mul_f16 v26, v58, v60 op_sel_hi:[0,1]\nv_pk_mul_f16 v27, v58, v61 op_sel_hi:[0,1]\n"
      "v_mfma_f32_16x16x32_f16 v[36:39], v[196:199], v[20:23], v[36:39]\nv_pk_mul_f16 v28, v58, v58 op_sel_hi:[0,1]\nds_read_b64_tr_b16 v[62:63], v237 offset:18944\n"
      "v_mfma_f32_16x16x32_f16 v[36:39], v[184:187], v[16:19], v[36:39]\nv_pk_mul_f16 v29, v58, v57 op_sel_hi:[0,1]\nds_read_b64_tr_b16 v[60:61], v237 offset:18816\n"
      "v_mfma_f32_16x16x32_f16 v[244:247], v[188:191], v[16:19], v[244:247]\nv_pk_mul_f16 v30, v56, v58 op_sel_hi:[0,1]\nv_mfma_f32_16x16x32_f16 v[244:247], v[200:203], v[20:23], v[244:247]\n"
      "v_pk_mul_f16 v31, v56, v59 op_sel_hi:[0,1]\nv_smfmac_f32_16x16x64_f16 v[44:47], v[204:207], v[16:23], v242\nds_read_b64_tr_b16 v[56:57], v236 offset:18560\n"
      "ds_read_b64_tr_b16 v[58:59], v237 offset:18688\nv_smfmac_f32_16x16x64_f16 v[44:47], v[232:235], v[24:31], v242\nv_pk_mul_f16 v16, v68, s48 op_sel:[0,1]\n"
      "v_pk_mul_f16 v17, v69, s48 op_sel:[0,1]\nv_mfma_f32_16x16x32_f16 v[32:35], v[208:211], v[24:27], v[32:35]\nv_pk_mul_f16 v18, v70, s48 op_sel:[0,1]\nv_pk_mul_f16 v19, v71, s48 op_sel:[0,1]\n"
      "v_mfma_f32_16x16x32_f16 v[32:35], v[220:223], v[28:31], v[32:35]\nv_pk_mul_f16 v20, v68, v70\nv_pk_mul_f16 v21, v69, v71\nv_mfma_f32_16x16x32_f16 v[36:39], v[224:227], v[28:31], v[36:39]\n"
      "v_add_f32 v44, v44, v45\nv_add_f32 v46, v46, v47\nv_mfma_f32_16x16x32_f16 v[36:39], v[212:215], v[24:27], v[36:39]\nv_mul_f32 v55, v44, v54\nv_mul_f32 v55, v55, v46\n"
      "ds_read_b128 v[44:47], v248 offset:48\nv_mfma_f32_16x16x32_f16 v[244:247], v[216:219], v[24:27], v[244:247]\ns_waitcnt lgkmcnt(1)\nv_pk_mul_f16 v22, v56, v56 op_sel_hi:[0,1]\n"
      "v_pk_mul_f16 v23, v56, v57 op_sel_hi:[0,1]\nv_mfma_f32_16x16x32_f16 v[244:247], v[228:231], v[28:31], v[244:247]\ns_cmp_lt_u32 s45, s53\nv_mul_f32 v32, v32, v48\nv_mul_f32 v33, v33, v49\n"
      "v_mul_f32 v34, v34, v50\ns_cbranch_scc0 .Lc_s9\n"
      ".Lc_u10:\nv_mfma_f32_16x16x32_f16 v[40:43], v[76:79], v[16:19], 0\nds_read_b32 v4, v248\nv_fmac_f32 v55, v32, v36\nv_mul_f32 v35, v35, v51\nv_fmac_f32 v55, v33, v37\n"
      "v_mfma_f32_16x16x32_f16 v[40:43], v[88:91], v[20:23], v[40:43]\nv_fmac_f32 v55, v34, v38\nv_fmac_f32 v55, v35, v39\ns_add_u32 s45, s45, 2\ns_cmp_ge_u32 s45, s46\ns_cbranch_scc0 .Lc_b10\n"
      "s_swappc_b64 s[4:5], s[34:35]\n"
      ".Lc_b10:\nv_mfma_f32_16x16x32_f16 v[36:39], v[84:87], v[20:23], 0\nv_pk_mul_f16 v24, v66, v66 op_sel_hi:[0,1]\nv_pk_mul_f16 v25, v66, v57 op_sel_hi:[0,1]\n"
      "v_mfma_f32_16x16x32_f16 v[36:39], v[72:75], v[16:19], v[36:39]\nv_pk_mul_f16 v26, v56, v66 op_sel_hi:[0,1]\nv_pk_mul_f16 v27, v56, v67 op_sel_hi:[0,1]\ns_setprio 0\n"
      "v_mfma_f32_16x16x32_f16 v[32:35], v[80:83], v[20:23], 0\nv_pk_mul_f16 v28, v58, v66 op_sel_hi:[0,1]\nv_pk_mul_f16 v29, v58, v67 op_sel_hi:[0,1]\ns_waitcnt lgkmcnt(0)\n"
      "v_smfmac_f32_16x16x64_f16 v[44:47], v[92:95], v[16:23], v242\nv_pk_mul_f16 v30, v60, v66 op_sel_hi:[0,1]\nv_pk_mul_f16 v31, v60, v67 op_sel_hi:[0,1]\n"
      "v_mfma_f32_16x16x32_f16 v[32:35], v[96:99], v[24:27], v[32:35]\nv_pk_mul_f16 v16, v62, v66 op_sel_hi:[0,1]\nv_pk_mul_f16 v17, v62, v67 op_sel_hi:[0,1]\ns_setprio 1\n"
      "v_mfma_f32_16x16x32_f16 v[32:35], v[108:111], v[28:31], v[32:35]\nv_pk_mul_f16 v18, v64, v66 op_sel_hi:[0,1]\nv_mul_f32 v244, v244, v52\n"
      "v_mfma_f32_16x16x32_f16 v[36:39], v[112:115], v[28:31], v[36:39]\nv_pk_mul_f16 v19, v64, v67 op_sel_hi:[0,1]\nv_mul_f32 v245, v245, v53\n"
      "v_mfma_f32_16x16x32_f16 v[36:39], v[100:103], v[24:27], v[36:39]\nv_pk_mul_f16 v20, v64, v64 op_sel_hi:[0,1]\nv_fmac_f32 v55, v244, v246\n"
      "v_mfma_f32_16x16x32_f16 v[40:43], v[104:107], v[24:27], v[40:43]\nv_pk_mul_f16 v21, v64, v57 op_sel_hi:[0,1]\nv_fmac_f32 v55, v245, v247\n"
      "v_mfma_f32_16x16x32_f16 v[40:43], v[116:119], v[28:31], v[40:43]\nv_pk_mul_f16 v22, v56, v64 op_sel_hi:[0,1]\nv_readfirstlane_b32 s44, v4\n"
      "v_smfmac_f32_16x16x64_f16 v[44:47], v[120:123], v[24:31], v242\nv_pk_mul_f16 v23, v56, v65 op_sel_hi:[0,1]\nv_mfma_f32_16x16x32_f16 v[32:35], v[124:127], v[16:19], v[32:35]\n"
      "v_pk_mul_f16 v24, v58, v64 op_sel_hi:[0,1]\nv_pk_mul_f16 v25, v58, v65 op_sel_hi:[0,1]\ns_setprio 2\nv_mfma_f32_16x16x32_f16 v[32:35], v[136:139], v[20:23], v[32:35]\n"
      "v_pk_mul_f16 v26, v60, v64 op_sel_hi:[0,1]\nv_mfma_f32_16x16x32_f16 v[36:39], v[140:143], v[20:23], v[36:39]\nv_pk_mul_f16 v27, v60, v65 op_sel_hi:[0,1]\n"
      "v_mfma_f32_16x16x32_f16 v[36:39], v[128:131], v[16:19], v[36:39]\nv_pk_mul_f16 v28, v62, v64 op_sel_hi:[0,1]\nv_add_f32_dpp v55, v55, v55 quad_perm:[1,0,3,2] row_mask:0xf bank_mask:0xf\n"
      "v_mfma_f32_16x16x32_f16 v[40:43], v[132:135], v[16:19], v[40:43]\nv_pk_mul_f16 v29, v62, v65 op_sel_hi:[0,1]\nv_add_f32_dpp v55, v55, v55 quad_perm:[2,3,0,1] row_mask:0xf bank_mask:0xf\n"
      "v_mfma_f32_16x16x32_f16 v[40:43], v[144:147], v[20:23], v[40:43]\nv_pk_mul_f16 v30, v62, v62 op_sel_hi:[0,1]\nv_add_f32_dpp v55, v55, v55 row_half_mirror row_mask:0xf bank_mask:0xf\n"
      "v_smfmac_f32_16x16x64_f16 v[44:47], v[148:151], v[16:23], v242\nv_pk_mul_f16 v31, v62, v57 op_sel_hi:[0,1]\nv_add_f32_dpp v55, v55, v55 row_mirror row_mask:0xf bank_mask:0xf\n"
      "v_mfma_f32_16x16x32_f16 v[32:35], v[152:155], v[24:27], v[32:35]\nv_pk_mul_f16 v16, v56, v62 op_sel_hi:[0,1]\nv_pk_mul_f16 v17, v56, v63 op_sel_hi:[0,1]\nds_write_b32 v243, v55 offset:1152\n"
      "v_mfma_f32_16x16x32_f16 v[32:35], v[164:167], v[28:31], v[32:35]\nv_pk_mul_f16 v18, v58, v62 op_sel_hi:[0,1]\nv_mfma_f32_16x16x32_f16 v[36:39], v[168:171], v[28:31], v[36:39]\n"
      "v_pk_mul_f16 v19, v58, v63 op_sel_hi:[0,1]\nv_mfma_f32_16x16x32_f16 v[36:39], v[156:159], v[24:27], v[36:39]\nv_pk_mul_f16 v20, v60, v62 op_sel_hi:[0,1]\ns_setprio 3\n"
      "s_ff0_i32_b32 s53, s44\nv_mfma_f32_16x16x32_f16 v[40:43], v[160:163], v[24:27], v[40:43]\nv_pk_mul_f16 v21, v60, v63 op_sel_hi:[0,1]\n"
      "v_mfma_f32_16x16x32_f16 v[40:43], v[172:175], v[28:31], v[40:43]\nv_pk_mul_f16 v22, v60, v60 op_sel_hi:[0,1]\ns_add_u32 s53, s53, s64\n"
      "v_smfmac_f32_16x16x64_f16 v[44:47], v[176:179], v[24:31], v242\nv_pk_mul_f16 v23, v60, v57 op_sel_hi:[0,1]\nv_mfma_f32_16x16x32_f16 v[32:35], v[180:183], v[16:19], v[32:35]\n"
      "v_pk_mul_f16 v24, v56, v60 op_sel_hi:[0,1]\nv_pk_mul_f16 v25, v56, v61 op_sel_hi:[0,1]\nv_mfma_f32_16x16x32_f16 v[32:35], v[192:195], v[20:23], v[32:35]\n"
      "v_pk_mul_f16 v26, v58, v60 op_sel_hi:[0,1]\nv_pk_mul_f16 v27, v58, v61 op_sel_hi:[0,1]\nv_mfma_f32_16x16x32_f16 v[36:39], v[196:199], v[20:23], v[36:39]\n"
      "v_pk_mul_f16 v28, v58, v58 op_sel_hi:[0,1]\nv_mfma_f32_16x16x32_f16 v[36:39], v[184:187], v[16:19], v[36:39]\nv_pk_mul_f16 v29, v58, v57 op_sel_hi:[0,1]\n"
      "v_mfma_f32_16x16x32_f16 v[40:43], v[188:191], v[16:19], v[40:43]\nv_pk_mul_f16 v30, v56, v58 op_sel_hi:[0,1]\nv_mfma_f32_16x16x32_f16 v[40:43], v[200:203], v[20:23], v[40:43]\n"
      "v_pk_mul_f16 v31, v56, v59 op_sel_hi:[0,1]\nv_smfmac_f32_16x16x64_f16 v[44:47], v[204:207], v[16:23], v242\ns_nop 0\nv_smfmac_f32_16x16x64_f16 v[44:47], v[232:235], v[24:31], v242\ns_nop 1\n"
      "v_mfma_f32_16x16x32_f16 v[32:35], v[208:211], v[24:27], v[32:35]\ns_nop 1\nv_mfma_f32_16x16x32_f16 v[32:35], v[220:223], v[28:31], v[32:35]\ns_nop 1\n"
      "v_mfma_f32_16x16x32_f16 v[36:39], v[224:227], v[28:31], v[36:39]\nv_add_f32 v44, v44, v45\nv_add_f32 v46, v46, v47\nv_mfma_f32_16x16x32_f16 v[36:39], v[212:215], v[24:27], v[36:39]\n"
      "v_mul_f32 v55, v44, v54\nv_mul_f32 v55, v55, v46\nds_read_b128 v[44:47], v248 offset:48\nv_mfma_f32_16x16x32_f16 v[40:43], v[216:219], v[24:27], v[40:43]\ns_nop 2\n"
      "v_mfma_f32_16x16x32_f16 v[40:43], v[228:231], v[28:31], v[40:43]\nv_mul_f32 v32, v32, v48\nv_mul_f32 v33, v33, v49\nv_mul_f32 v34, v34, v50\n"
      ".Lc_slow:\ns_sub_u32 s49, s45, s64\ns_lshr_b32 s50, s44, 16\ns_or_b32 s50, s50, s56\ns_ashr_i32 s50, s50, s49\ns_cmp_lg_u32 s50, -1\ns_cbranch_scc1 .Lc_more\n"
      ".Lc_fin:\ns_sub_u32 s50, s45, s33\ns_bitcmp1_b32 s50, 1\ns_cselect_b64 vcc, -1, 0\ns_lshl_b32 s50, s50, 6\ns_sub_u32 s50, s50, 128\nv_fmac_f32 v55, v32, v36\nv_mul_f32 v35, v35, v51\n"
      "v_fmac_f32 v55, v33, v37\nv_fmac_f32 v55, v34, v38\nv_fmac_f32 v55, v35, v39\nv_cndmask_b32 v40, v244, v40, vcc\nv_cndmask_b32 v41, v245, v41, vcc\nv_cndmask_b32 v42, v246, v42, vcc\n"
      "v_cndmask_b32 v43, v247, v43, vcc\nv_mul_f32 v40, v40, v52\nv_mul_f32 v41, v41, v53\nv_fmac_f32 v55, v40, v42\nv_fmac_f32 v55, v41, v43\nv_add_u32 v243, s50, v243\ns_nop 0\n"
      "v_add_f32_dpp v55, v55, v55 quad_perm:[1,0,3,2] row_mask:0xf bank_mask:0xf\ns_nop 1\nv_add_f32_dpp v55, v55, v55 quad_perm:[2,3,0,1] row_mask:0xf bank_mask:0xf\ns_nop 1\n"
      "v_add_f32_dpp v55, v55, v55 row_half_mirror row_mask:0xf bank_mask:0xf\ns_nop 1\nv_add_f32_dpp v55, v55, v55 row_mirror row_mask:0xf bank_mask:0xf\nds_write_b32 v243, v55\n"
      "s_cmp_gt_u32 s24, 1\ns_waitcnt lgkmcnt(0)\ns_barrier\ns_cbranch_scc1 .Lc_end\nds_read_b128 v[4:7], v10\nds_read_b32 v3, v11\nds_read_b128 v[16:19], v13\nds_read_b32 v2, v248\n"
      "s_mov_b32 vcc_lo, 0x88888888\ns_mov_b32 vcc_hi, vcc_lo\ns_cmp_eq_u32 s24, 0\ns_cselect_b32 s43, vcc_lo, 0\ns_cselect_b32 s42, vcc_lo, 0x88888\nv_mov_b32 v9, s38\ns_waitcnt lgkmcnt(2)\n"
      "v_add_f32 v4, v4, v5\nv_add_f32 v6, v6, v7\nv_add_f32 v4, v4, v6\nv_ldexp_f32 v4, v4, v3\ns_waitcnt lgkmcnt(1)\nv_add_f32 v17, v17, v18\nv_add_f32 v17, v17, v19\nv_add_f32 v4, v4, v16\n"
      "v_cndmask_b32 v17, v17, v9, vcc\nv_add_f32 v4, v4, v17\ns_waitcnt lgkmcnt(0)\nv_and_b32 v2, 0xff, v2\nv_add_f32_dpp v4, v4, v4 quad_perm:[1,0,3,2] row_mask:0xf bank_mask:0xf\n"
      "v_lshrrev_b32 v2, v14, v2\nv_and_or_b32 v2, v2, 1, v15\nv_add_f32_dpp v4, v4, v4 quad_perm:[2,3,0,1] row_mask:0xf bank_mask:0xf\nv_cmp_ne_u32 vcc, 0, v2\nv_bfe_u32 v8, v19, 3, 12\n"
      "v_lshlrev_b32 v8, 1, v8\nv_cvt_pk_bf16_f32 v4, v4, v4\ns_and_b64 exec, vcc, s[42:43]\ns_cbranch_execz .Lc_end\nglobal_store_short v8, v4, s[22:23] sc1\ns_branch .Lc_end\n"
      ".Lc_s2:\ns_getpc_b64 s[60:61]\ns_branch .Lc_slow\ns_branch .Lc_u3\n"
      ".Lc_s3:\ns_getpc_b64 s[60:61]\ns_branch .Lc_slow\ns_branch .Lc_u4\n"
      ".Lc_s4:\ns_getpc_b64 s[60:61]\ns_branch .Lc_slow\ns_branch .Lc_u5\n"
      ".Lc_s5:\ns_getpc_b64 s[60:61]\ns_branch .Lc_slow\ns_branch .Lc_u6\n"
      ".Lc_s6:\ns_getpc_b64 s[60:61]\ns_branch .Lc_slow\ns_branch .Lc_u7\n"
      ".Lc_s7:\ns_getpc_b64 s[60:61]\ns_branch .Lc_slow\ns_branch .Lc_u8\n"
      ".Lc_s8:\ns_getpc_b64 s[60:61]\ns_branch .Lc_slow\ns_branch .Lc_u9\n"
      ".Lc_s9:\ns_getpc_b64 s[60:61]\ns_branch .Lc_slow\ns_branch .Lc_u10\n"
      ".Lc_t0:\ns_swappc_b64 s[4:5], s[34:35]\ns_branch .Lc_b0\n"
      ".Lc_t1:\ns_swappc_b64 s[4:5], s[34:35]\ns_branch .Lc_b1\n"
      ".Lc_t2:\ns_swappc_b64 s[4:5], s[34:35]\ns_branch .Lc_b2\n"
      ".Lc_t3:\ns_swappc_b64 s[4:5], s[34:35]\ns_branch .Lc_b3\n"
      ".Lc_t4:\ns_swappc_b64 s[4:5], s[34:35]\ns_branch .Lc_b4\n"
      ".Lc_t5:\ns_swappc_b64 s[4:5], s[34:35]\ns_branch .Lc_b5\n"
      ".Lc_t6:\ns_swappc_b64 s[4:5], s[34:35]\ns_branch .Lc_b6\n"
      ".Lc_t7:\ns_swappc_b64 s[4:5], s[34:35]\ns_branch .Lc_b7\n"
      ".Lc_t8:\ns_swappc_b64 s[4:5], s[34:35]\ns_branch .Lc_b8\n"
      ".Lc_t9:\ns_swappc_b64 s[4:5], s[34:35]\ns_branch .Lc_b9\n"
      ".Lc_t10:\ns_swappc_b64 s[4:5], s[34:35]\ns_branch .Lc_b10\n"
      ".Lc_tg:\ns_swappc_b64 s[4:5], s[34:35]\ns_branch .Lc_bg\n"
      ".Lc_th:\ns_swappc_b64 s[4:5], s[34:35]\ns_branch .Lc_bh\n"
      ".Lc_more:\ns_mov_b32 s51, 0\ns_add_u32 s60, s60, 4\ns_addc_u32 s61, s61, 0\n"
      ".Lc_chk:\ns_sub_u32 s49, s45, s64\ns_sub_u32 s53, s68, s64\ns_lshl_b32 s53, -1, s53\ns_lshr_b32 s50, s44, 16\ns_or_b32 s50, s50, s53\ns_ashr_i32 s50, s50, s49\ns_cmp_eq_u32 s50, -1\n"
      "s_cbranch_scc0 .Lc_c1\ns_cmp_lg_u32 s56, 0\ns_cbranch_scc1 .Lc_fin\ns_branch .Lc_c2\n"
      ".Lc_c1:\ns_lshr_b32 s50, s44, s49\ns_and_b32 s50, s50, 0x10001\ns_cbranch_scc1 .Lc_have\n"
      ".Lc_c2:\ns_add_u32 s51, s51, 1\ns_bitcmp1_b32 s51, 22\ns_cbranch_scc1 .Lc_fin\ns_cmp_lg_u32 s56, 0\ns_cbranch_scc1 .Lc_rd\ns_swappc_b64 s[4:5], s[34:35]\n"
      ".Lc_rd:\nds_read_b32 v4, v248\ns_waitcnt lgkmcnt(0)\nv_readfirstlane_b32 s44, v4\ns_branch .Lc_chk\n"
      ".Lc_have:\ns_cmp_eq_u32 s51, 0\ns_cbranch_scc1 .Lc_go\ns_sub_u32 s49, s45, s33\ns_mulk_i32 s49, 928\nv_add_u32 v24, s49, v236\nv_add_u32 v25, s49, v237\nv_add_u32 v26, s49, v238\n"
      "v_add_u32 v27, s49, v239\nds_read_b64_tr_b16 v[68:69], v26 offset:0\nds_read_b64_tr_b16 v[70:71], v27 offset:0\nds_read_b64_tr_b16 v[56:57], v24 offset:0\n"
      "ds_read_b64_tr_b16 v[58:59], v25 offset:128\nds_read_b64_tr_b16 v[60:61], v25 offset:256\nds_read_b64_tr_b16 v[62:63], v25 offset:384\nds_read_b64_tr_b16 v[64:65], v25 offset:512\n"
      "ds_read_b64_tr_b16 v[66:67], v25 offset:640\ns_waitcnt lgkmcnt(0)\nv_readlane_b32 s48, v69, 48\nv_pk_mul_f16 v20, v68, v70\nv_pk_mul_f16 v21, v69, v71\n"
      "v_pk_mul_f16 v22, v56, v56 op_sel_hi:[0,1]\nv_pk_mul_f16 v23, v56, v57 op_sel_hi:[0,1]\nv_pk_mul_f16 v16, v68, s48 op_sel:[0,1]\nv_pk_mul_f16 v17, v69, s48 op_sel:[0,1]\n"
      "v_pk_mul_f16 v18, v70, s48 op_sel:[0,1]\nv_pk_mul_f16 v19, v71, s48 op_sel:[0,1]\n"
      ".Lc_go:\ns_setpc_b64 s[60:61]\n"
      ".Lt_g:\ns_waitcnt lgkmcnt(0)\nv_readfirstlane_b32 s42, v2\ns_mov_b32 s43, 43008\nv_lshl_add_u32 v1, v240, 2, s43\ns_cmp_lg_u32 s42, 3\ns_cbranch_scc1 .Lt_p\nds_read_b32 v3, v248 offset:764\n"
      "ds_read2st64_b32 v[6:7], v1 offset0:0 offset1:4\nds_read2st64_b32 v[8:9], v1 offset0:1 offset1:5\nds_read2st64_b32 v[10:11], v1 offset0:2 offset1:6\n"
      "ds_read2st64_b32 v[12:13], v1 offset0:3 offset1:7\ns_lshl_b32 s42, s28, 8\ns_add_u32 s42, s42, s29\ns_lshl_b32 s42, s42, 7\ns_add_u32 s58, s12, s42\ns_addc_u32 s59, s13, 0\n"
      "v_lshlrev_b32 v14, 3, v240\ns_cmp_eq_u32 s25, 2\ns_cselect_b64 vcc, -1, 0\ns_movk_i32 s42, 0xff00\nv_mov_b32 v1, s41\ns_lshl_b32 s43, s25, 3\ns_waitcnt lgkmcnt(0)\nv_add_f32 v12, v12, v13\n"
      "v_add_f32 v6, v6, v7\nv_add_f32 v8, v8, v9\nv_add_f32 v10, v10, v11\nv_mov_b32 v13, v12\nv_mov_b32 v15, v12\ns_nop 1\nv_permlane16_swap_b32 v13, v15\nv_add_f32 v13, v13, v15\n"
      "v_cndmask_b32 v12, v12, v13, vcc\nv_mul_f32 v6, v3, v6\nv_mul_f32 v8, v3, v8\nv_mul_f32 v10, v3, v10\nv_mul_f32 v12, v3, v12\nv_add_u32 v6, 0x80, v6\nv_and_or_b32 v6, v6, s42, v1\n"
      "v_mov_b32 v7, s40\nv_add_u32 v8, 0x80, v8\nv_and_or_b32 v8, v8, s42, v1\nv_mov_b32 v9, s40\nv_add_u32 v10, 0x80, v10\nv_and_or_b32 v10, v10, s42, v1\nv_mov_b32 v11, s40\n"
      "v_add_u32 v12, 0x80, v12\nv_and_or_b32 v12, v12, s42, v1\nv_mov_b32 v13, s40\nglobal_store_dwordx2 v14, v[6:7], s[58:59] offset:0 sc1\n"
      "global_store_dwordx2 v14, v[8:9], s[58:59] offset:512 sc1\nglobal_store_dwordx2 v14, v[10:11], s[58:59] offset:1024 sc1\ns_cmp_eq_u32 s43, 0\ns_cbranch_scc1 .Lt_p\ns_bfm_b64 exec, s43, 0\n"
      "global_store_dwordx2 v14, v[12:13], s[58:59] offset:1536 sc1\ns_mov_b64 exec, -1\n"
      ".Lt_p:\ns_cmp_gt_u32 s66, 100\ns_cbranch_scc1 .Lt_d\ns_mov_b32 s46, s66\ns_swappc_b64 s[34:35], s[4:5]\ns_lshl_b32 s43, 0x10000, s67\ns_cmp_gt_u32 s44, 0xffff\ns_cbranch_scc1 .Lt_a\n"
      "v_lshlrev_b32 v1, 3, v240\ns_mov_b64 exec, 1\nglobal_atomic_add v5, v1, v241, s[8:9] sc0 sc1\ns_mov_b64 exec, -1\nglobal_load_dwordx2 v[254:255], v249, s[12:13] sc1\n"
      ".Lt_f:\nglobal_load_dwordx2 v[2:3], v1, s[14:15] sc1\ns_swappc_b64 s[34:35], s[4:5]\ns_waitcnt vmcnt(0)\nv_readfirstlane_b32 s50, v5\nv_cmp_eq_u32 vcc, s41, v2\n"
      "v_cmp_eq_u32 s[62:63], s40, v3\nv_mov_b32 v7, s43\ns_cmp_lt_u32 s50, s55\ns_cbranch_scc1 .Lt_r\n"
      ".Lt_e:\nds_add_u32 v253, v7\ns_branch .Lt_c\n"
      ".Lt_r:\ns_add_u32 s50, s50, 1\nv_readlane_b32 s54, v251, s50\ns_and_b64 s[62:63], s[62:63], vcc\ns_bfe_u32 s50, s54, 0x50004\ns_lshr_b32 s49, s54, 9\ns_lshl3_add_u32 s50, s50, s49\n"
      "s_sub_u32 s50, s50, s69\ns_bitcmp0_b64 s[62:63], s50\ns_cbranch_scc1 .Lt_f\ns_mul_i32 s50, s54, 3584\ns_add_u32 s62, s16, s50\ns_addc_u32 s63, s17, 0\ns_add_u32 s50, s64, s67\n"
      "s_mul_i32 s49, s50, 928\ns_add_u32 m0, s49, 45056\ns_mov_b32 exec_hi, 0x7fffff\nglobal_load_lds_dwordx4 v252, s[62:63] offset:0 sc1\ns_add_u32 m0, m0, 21376\ns_lshl_b32 s54, s54, 3\n"
      "global_load_lds_dwordx4 v252, s[62:63] offset:896 sc1\ns_add_u32 m0, m0, 21376\ns_lshl_b32 s50, s50, 6\nglobal_load_lds_dwordx4 v252, s[62:63] offset:1792 sc1\ns_add_u32 m0, m0, 21376\n"
      "s_lshl_b32 s43, 1, s67\nglobal_load_lds_dwordx4 v252, s[62:63] offset:2688 sc1\ns_mov_b32 exec_hi, -1\ns_mov_b32 exec_lo, 0\nv_and_b32 v249, 0xffff8000, v249\ns_add_u32 s50, s50, 136192\n"
      "v_or_b32 v249, s54, v249\nv_and_or_b32 v250, v250, 63, s50\ns_mov_b32 exec_lo, -1\nv_mov_b32 v7, s43\nglobal_load_dwordx2 v[254:255], v249, s[12:13] sc1\ns_swappc_b64 s[34:35], s[4:5]\n"
      "s_waitcnt vmcnt(0)\ns_branch .Lt_e\n"
      ".Lt_a:\nv_mov_b32 v0, s43\nds_add_u32 v253, v0\n"
      ".Lt_d:\ns_sub_u32 s46, s64, 4\ns_cmp_ge_i32 s45, s46\ns_cbranch_scc1 .Lt_l\ns_swappc_b64 s[34:35], s[4:5]\n"
      ".Lt_l:\nglobal_load_dwordx2 v[254:255], v249, s[12:13] sc1\ns_swappc_b64 s[34:35], s[4:5]\ns_waitcnt vmcnt(0)\n"
      ".Lt_c:\nv_and_b32 v6, 0xff, v254\nv_cmp_ne_u32 vcc, s40, v255\nv_cmp_ne_u32 s[62:63], s41, v6\nv_and_b32 v9, 0xffffff00, v254\ns_or_b64 vcc, vcc, s[62:63]\ns_cbranch_vccnz .Lt_d\n"
      "s_mov_b32 vcc_lo, 0x1fff1fff\ns_mov_b32 vcc_hi, vcc_lo\ns_sub_u32 s56, s68, s64\nv_cndmask_b32 v9, v249, v9, vcc\ns_lshl_b32 s56, -1, s56\ns_movk_i32 s46, 99\nds_write_b32 v250, v9\n"
      "s_setpc_b64 s[4:5]\n"
      ".Lc_end:\n"
      ".Lxd_end:"
      :: "{v240}"(lane), "{v253}"(tch), "{v[248:251]}"(mrow), "{v252}"(msc), "{v[254:255]}"(rsp), "{s[4:19]}"(ka0), "{s[20:23]}"(ka1), "{s[24:31]}"(gs), "{s2}"(blk)
      : "memory", "vcc", "scc", "s33", "s34", "s35", "s36", "s37", "s38", "s39", "s40", "s41", "s42", "s43", "s44", "s45", "s46", "s47", "s48", "s49", "s50", "s51", "s52", "s53", "s54", "s55", "s56", "s57", "s58", "s59", "s60", "s61", "s62", "s63", "s64", "s65", "s66", "s67", "s68", "s69",
        "v0", "v1", "v2", "v3", "v4", "v5", "v6", "v7", "v8", "v9", "v10", "v11", "v12", "v13", "v14", "v15", "v16", "v17", "v18", "v19", "v20", "v21", "v22", "v23", "v24", "v25", "v26", "v27",
        "v28", "v29", "v30", "v31", "v32", "v33", "v34", "v35", "v36", "v37", "v38", "v39", "v40", "v41", "v42", "v43", "v44", "v45", "v46", "v47", "v48", "v49", "v50", "v51", "v52", "v53", "v54",
        "v55", "v56", "v57", "v58", "v59", "v60", "v61", "v62", "v63", "v64", "v65", "v66", "v67", "v68", "v69", "v70", "v71", "v72", "v73", "v74", "v75", "v76", "v77", "v78", "v79", "v80", "v81",
        "v82", "v83", "v84", "v85", "v86", "v87", "v88", "v89", "v90", "v91", "v92", "v93", "v94", "v95", "v96", "v97", "v98", "v99", "v100", "v101", "v102", "v103", "v104", "v105", "v106", "v107",
        "v108", "v109", "v110", "v111", "v112", "v113", "v114", "v115", "v116", "v117", "v118", "v119", "v120", "v121", "v122", "v123", "v124", "v125", "v126", "v127", "v128", "v129", "v130",
        "v131", "v132", "v133", "v134", "v135", "v136", "v137", "v138", "v139", "v140", "v141", "v142", "v143", "v144", "v145", "v146", "v147", "v148", "v149", "v150", "v151", "v152", "v153",
        "v154", "v155", "v156", "v157", "v158", "v159", "v160", "v161", "v162", "v163", "v164", "v165", "v166", "v167", "v168", "v169", "v170", "v171", "v172", "v173", "v174", "v175", "v176",
        "v177", "v178", "v179", "v180", "v181", "v182", "v183", "v184", "v185", "v186", "v187", "v188", "v189", "v190", "v191", "v192", "v193", "v194", "v195", "v196", "v197", "v198", "v199",
        "v200", "v201", "v202", "v203", "v204", "v205", "v206", "v207", "v208", "v209", "v210", "v211", "v212", "v213", "v214", "v215", "v216", "v217", "v218", "v219", "v220", "v221", "v222",
        "v223", "v224", "v225", "v226", "v227", "v228", "v229", "v230", "v231", "v232", "v233", "v234", "v235", "v236", "v237", "v238", "v239", "v241", "v242", "v243", "v244", "v245", "v246", "v247");
}

extern "C" void kernel_launch(void* const* d_in, const int* in_sizes, int n_in, void* d_out, int out_size,
                              void* d_ws, size_t ws_size, hipStream_t stream) {
  const int* idx = (const int*)d_in[0];
  const u16* table = (const u16*)d_in[1];
  const u16* ws_cin = (const u16*)d_in[2];
  const u16* wout = (const u16*)d_in[3];
  const u16* w1 = (const u16*)d_in[4];
  const u16* b1 = (const u16*)d_in[5];
  const u16* w2 = (const u16*)d_in[6];
  const u16* b_out = (const u16*)d_in[7];
  dnn_kernel<<<256, 512, 0, stream>>>(idx, table, ws_cin, w1, b1, w2, wout, b_out, (char*)d_ws, (u16*)d_out);
}
```

```cpp
#include <hip/hip_runtime.h>
#include <stdint.h>

typedef __attribute__((ext_vector_type(8))) short bf16x8;
typedef __attribute__((ext_vector_type(4))) short s16x4;
typedef __attribute__((ext_vector_type(4))) float f32x4;
typedef __attribute__((ext_vector_type(2))) float f32x2;
typedef __attribute__((ext_vector_type(2))) __bf16 bf16x2;
typedef __attribute__((ext_vector_type(16))) float f32x16;
typedef unsigned short u16;
typedef __attribute__((ext_vector_type(2))) unsigned u32x2;
typedef __attribute__((ext_vector_type(4))) unsigned u32x4;

constexpr int NF = 26;

template <int CTRL, int RM = 0xf>
__device__ __forceinline__ float dpp(float v) { return __builtin_bit_cast(float, __builtin_amdgcn_update_dpp(0, __builtin_bit_cast(int, v), CTRL, RM, 0xf, false)); }
__device__ __forceinline__ unsigned pk2(float a, float b) { return __builtin_bit_cast(unsigned, __builtin_convertvector((f32x2){a, b}, bf16x2)); }

typedef __attribute__((ext_vector_type(8))) unsigned u32x8;
typedef __attribute__((ext_vector_type(16))) unsigned u32x16;
constexpr int STAGE = 224 * 128 + 64 * 256, G_LDS = 163840;
constexpr int MC = 41 * 1024;
constexpr int W3S = 0, U_OFF = W3S + 35840, NC_OFF = U_OFF + 2048, GT_OFF = NC_OFF + 1024, WO_OFF = GT_OFF + 2048, DP = 4352, W2S = 2 * STAGE;
static_assert(WO_OFF + 256 <= STAGE, "lds");
constexpr int WS_RESP = 4096, WS_POOL = 32768, WS_M = 1 << 20;
#define NCLV 7
constexpr int NSLOT = 21, NCL = NCLV;

struct Geo { unsigned nt, s0, ns; };
constexpr __host__ __device__ Geo geo(unsigned e) {
  const unsigned i = (e * 79) >> 10, nt = e - 13 * i, c = nt < 9 ? 13108 : 13798, t = i * c + ((nt * 247) & 1023);
  return Geo{nt, t >> 10, ((t + c) >> 10) - (t >> 10)};
}
constexpr unsigned long long builders() {
  unsigned long long k = 0;
  for (unsigned x = 0; x < 8; ++x)
    for (unsigned s = 32; s-- > 0;)
      if (geo(32 * x + s).ns == 12) k = (k & ~(31ull << (5 * x))) | (unsigned long long)s << (5 * x);
  return k;
}
constexpr unsigned long long BK = builders();
#define KEEP 10, 13, 15, 11, 8
constexpr int KP[5] = {KEEP};
constexpr int WANT[5] = {138, 163, 177, 135, 113};
constexpr int kind(unsigned b) { return ((BK >> (5 * (b & 7))) & 31) == b >> 3 ? 3 + ((b & 7) >> 2) : 14 - geo((b & 7) * 32 + (b >> 3)).ns; }
constexpr __host__ __device__ unsigned group(unsigned b) { return ((b >> 3) + 2 * b) & 15; }
constexpr bool geo_ok() {
  for (unsigned nt = 0; nt < 13; ++nt) {
    unsigned s = 0;
    for (unsigned e = nt; e < 256; e += 13) {
      const Geo g = geo(e);
      if (g.nt != nt || g.s0 != s || g.ns < 12 || g.ns > 14 || e < g.s0 || e >= g.s0 + g.ns) return false;
      s += g.ns;
    }
    if (s != 256) return false;
  }
  return true;
}
static_assert(geo_ok(), "tiles");

struct PI { int a, b; };
constexpr PI prod(int n) {
  int m = n - 2;
  for (int jj = 0; jj < 6; ++jj) {
    const int j = jj ? 6 - jj : 0;
    if (m < 2 + 2 * j) return m == 0 ? PI{2 * j, 2 * j} : m == 1 ? PI{2 * j, 1} : PI{(m - 2) & ~1, 2 * j + (m & 1)};
    m -= 2 + 2 * j;
  }
  return PI{0, 0};
}
constexpr int xfield(int k, int h, int g) { return k == 1 ? 24 + h : 4 * (k >> 1) + ((g + 2 * (k & 1) + h) & 3); }
constexpr int pqfield(int q, int sl, int g) { return g < 3 ? 8 * g + 4 * (sl >> 1) + (sl & 1) + (((g ^ q) & 1) << 1) : sl == 3 ? 26 : 24 + ((sl + q) >> 1); }
constexpr unsigned short slot_code(int f, int t, int g, int h) {
  if (f == 0) {
    const int sl = 2 * (t & 1) + h, q = t >> 1;
    return g == 3 && (q || (sl & 1)) ? 0x8000 : 0x4000 | 6 << 10 | (26 * 33 + pqfield(q, sl, g));
  }
  const int n = 4 * (f - 1) + t;
  const int p = n < 2 ? pqfield(0, 2 * n + h, g) : xfield(prod(n).a, 0, g), q = n < 2 ? pqfield(1, 2 * n + h, g) : xfield(prod(n).b, h, g);
  return (p == 26 ? 0x4000 | 10 << 10 : p == q ? 1 << 10 : 2 << 10) | (p * 33 + q);
}
struct GT { unsigned short t[2][64][8], pool[16][64]; };
constexpr GT make_gt() {
  GT r{};
  for (int l = 0; l < 64; ++l)
    for (int e = 0; e < 8; ++e) {
      r.t[0][l][e] = l < 48 ? slot_code(l >> 2, e >> 1, l & 3, e & 1) : 0x8000;
      r.t[1][l][e] = l < 48 ? slot_code(2 * (l >> 3) + ((l >> 1) & 1), 2 * ((e >> 1) & 1) + ((l >> 2) & 1), 2 * (l & 1) + (e >> 2), e & 1) : 0x8000;
    }
  int tot = 0, dem = 0, acc = 0, k = 15, b = 0;
  for (int i = 0; i < 256; ++i) { tot += 16 - KP[kind(i)]; dem += WANT[kind(i)] - 10 * KP[kind(i)]; }
  for (int q = 0, n = 0; q < 16; ++q) {
    for (int i = 0; i < 256; ++i) acc += group(i) == q ? WANT[kind(i)] - 10 * KP[kind(i)] : 0;
    const int len = (acc * tot + dem / 2) / dem - n;
    n += len;
    r.pool[q][0] = len;
    for (int i = 1; i <= len; ++i) {
      while (k < KP[kind(b)]) { ++b; k = 15; }
      r.pool[q][i] = ((b & 7) * 32 + (b >> 3)) * 16 + k--;
    }
  }
  return r;
}
constexpr bool pool_ok() {
  const GT t = make_gt();
  for (int q = 0; q < 16; ++q) {
    int n = 0;
    for (int i = 0; i < 256; ++i) n += group(i) != q ? 0 : NSLOT - KP[kind(i)] < NCL ? NSLOT - KP[kind(i)] : NCL;
    if (t.pool[q][0] > 62 || t.pool[q][0] > n) return false;
    const int fb = 16 * q < 24 ? 0 : 16 * q - 24 > 192 ? 192 : 16 * q - 24;
    for (int i = 1; i <= t.pool[q][0]; ++i) {
      const int b = ((t.pool[q][i] >> 4) & 31) * 8 + (t.pool[q][i] >> 9);
      if (b < fb || b >= fb + 64) return false;
    }
  }
  for (int k = 0; k < 5; ++k) if (KP[k] < 8 || KP[k] > 16) return false;
  return true;
}
static_assert(pool_ok(), "pools");
static __device__ const __attribute__((aligned(16))) GT g_tab = make_gt();

__global__ __launch_bounds__(512, 2) void dnn_kernel(const int* __restrict__ idx, const u16* __restrict__ table,
                                                     const u16* __restrict__ ws_cin, const u16* __restrict__ w1,
                                                     const u16* __restrict__ b1, const u16* __restrict__ w2,
                                                     const u16* __restrict__ wout, const u16* __restrict__ b_out,
                                                     char* __restrict__ ws, u16* __restrict__ out) {
  __shared__ __attribute__((aligned(1024))) char shm[G_LDS];
  u32x4 ka1;
  u32x8 gs;
  u32x16 ka0;
  unsigned tch, blk = blockIdx.x;
  int lane;
  asm volatile(
      "s_load_dwordx16 s[4:19], %[ka], 0\ns_load_dwordx4 s[20:23], %[ka], 64\nv_readfirstlane_b32 s24, %[tid]\ns_lshr_b32 s37, s2, 3\ns_cmp_lt_u32 s24, 448\ns_cbranch_scc1 .Lf3\n"
      "s_getpc_b64 s[42:43]\n"
      ".Lxd_pc0:\nv_and_b32 v245, 15, %[tid]\nv_lshl_add_u32 v245, s37, 4, v245\nv_lshlrev_b32 v245, 7, v245\nv_min_u32 v245, .Lxd_end-.Lxd_pc0-4, v245\nglobal_load_dword v253, v245, s[42:43]\n"
      ".Lf3:\ns_and_b32 s36, s2, 7\ns_lshl_b32 s30, s36, 5\ns_add_u32 s30, s30, s37\ns_lshr_b32 s24, s24, 6\ns_mul_i32 s38, s30, 79\ns_lshr_b32 s38, s38, 10\ns_mul_i32 s28, s38, 13\n"
      "s_sub_u32 s28, s30, s28\nv_and_b32 v240, 63, %[tid]\ns_lshr_b32 s27, s36, 1\ns_add_u32 s27, s27, s36\ns_and_b32 s39, s28, 3\ns_mul_i32 s39, s39, 3\ns_add_u32 s27, s27, s39\n"
      "s_sub_u32 s39, s27, 12\ns_min_u32 s27, s27, s39\ns_lshl_b32 s27, s27, 1\ns_cmp_ge_u32 s24, 4\ns_cbranch_scc1 .Lf1\ns_movk_i32 s39, 13798\nv_mul_u32_u24 v245, 9363, v240\n"
      "s_cmp_lt_u32 s28, 9\ns_cselect_b32 s39, 13108, s39\ns_or_b32 s41, s27, 2\ns_mul_i32 s40, s28, 247\nv_lshrrev_b32 v245, 16, v245\ns_and_b32 s40, s40, 1023\n"
      "v_mad_i32_i24 v246, v245, -7, v240\ns_mul_i32 s38, s38, s39\nv_lshl_add_u32 v246, v246, 2, s41\ns_add_u32 s38, s38, s40\nv_subrev_u32 v247, 26, v246\ns_lshr_b32 s29, s38, 10\n"
      "v_mul_u32_u24 v245, 26, v245\ns_add_u32 s38, s38, s39\nv_min_u32 v246, v246, v247\ns_lshr_b32 s38, s38, 10\nv_add_lshl_u32 v241, v246, v245, 2\ns_sub_u32 s38, s38, s29\n"
      "v_add_u32 v244, 192, v241\ns_lshl_b32 s38, s38, 1\nv_add_u32 v243, 128, v241\ns_sub_u32 s25, s38, 24\nv_add_u32 v242, 64, v241\ns_mul_i32 s38, s24, 6272\ns_add_u32 m0, s38, 139648\n"
      "s_lshl1_add_u32 s38, s29, s24\ns_mul_i32 s38, s38, 832\ns_mul_i32 s40, s24, 3328\ns_add_u32 s38, s38, 16640\ns_sub_u32 s40, 16896, s40\ns_mov_b32 exec_hi, 0xffffff\n"
      "v_add_u32 v245, 256, v241\nv_add_u32 v246, 2432, v241\ns_waitcnt lgkmcnt(0)\ns_add_u32 s38, s4, s38\ns_addc_u32 s39, s5, 0\ns_cmp_ge_u32 s24, s25\nglobal_load_lds_dwordx4 v241, s[38:39]\n"
      "s_cbranch_scc1 .Li0\nglobal_load_lds_dwordx4 v246, s[38:39] offset:896\n"
      ".Li0:\ns_sub_u32 m0, m0, 4480\ns_sub_u32 s38, s38, s40\ns_subb_u32 s39, s39, 0\nglobal_load_lds_dwordx4 v245, s[38:39] offset:0\nglobal_load_lds_dwordx4 v244, s[38:39] offset:896\n"
      "global_load_lds_dwordx4 v243, s[38:39] offset:1792\nglobal_load_lds_dwordx4 v242, s[38:39] offset:2688\nglobal_load_lds_dwordx4 v241, s[38:39] offset:3584\ns_mov_b32 exec_hi, -1\n"
      "s_add_u32 s27, s27, 2\ns_mul_i32 s36, s36, 5\ns_mov_b32 s38, %[bkl]\ns_mov_b32 s39, %[bkh]\ns_lshr_b64 s[38:39], s[38:39], s36\ns_and_b32 s38, s38, 31\ns_xor_b32 s31, s38, s37\n"
      "s_cbranch_scc0 .Lbld\n"
      ".Lbp:\ns_mov_b32 s36, s24\ns_mul_i32 s42, s36, 6272\ns_cmp_ge_u32 s36, s25\ns_cselect_b64 vcc, 0, -1\ns_and_b32 s44, s27, 2\ns_xor_b32 s44, s44, 2\ns_lshl_b32 s44, s44, 2\n"
      "s_add_u32 s42, s42, s44\ns_add_u32 s42, s42, 135168\nv_lshrrev_b32 v22, 3, v240\nv_mul_u32_u24 v6, 112, v22\nv_add_u32 v6, s42, v6\nv_lshrrev_b32 v22, 4, v240\ns_lshl_b32 s42, s36, 2\n"
      "v_xor_b32 v22, v22, v240\nv_xor_b32 v22, s42, v22\nv_and_b32 v22, 7, v22\nv_lshlrev_b32 v4, 4, v22\nv_xor_b32 v5, 64, v4\ns_sub_u32 s44, s6, 1024\ns_subb_u32 s45, s7, 0\n"
      "s_sub_u32 s46, s6, 2048\ns_subb_u32 s47, s7, 0\ns_sub_u32 s48, s6, 3072\ns_subb_u32 s49, s7, 0\ns_mul_i32 s53, s36, 5120\ns_lshl_b32 s26, s36, 10\ns_lshl_b32 s54, s36, 12\n"
      "s_add_u32 s54, s54, 28672\nv_add_u32 v7, 1792, v6\nv_add_u32 v8, 3584, v6\nv_add_u32 v9, 5376, v6\ns_waitcnt vmcnt(0)\nds_read2_b64 v[196:199], v8 offset0:112 offset1:113\n"
      "ds_read2_b64 v[200:203], v9 offset0:0 offset1:1\nds_read2_b64 v[176:179], v6 offset0:0 offset1:1\nds_read2_b64 v[180:183], v6 offset0:112 offset1:113\n"
      "ds_read2_b64 v[184:187], v7 offset0:0 offset1:1\nds_read2_b64 v[188:191], v7 offset0:112 offset1:113\nds_read2_b64 v[192:195], v8 offset0:0 offset1:1\ns_waitcnt lgkmcnt(0)\n"
      "v_lshl_add_u32 v22, v176, 7, v4\nv_lshl_add_u32 v23, v180, 7, v5\nv_lshl_add_u32 v24, v184, 7, v4\nv_lshl_add_u32 v25, v188, 7, v5\nv_lshl_add_u32 v26, v192, 7, v4\n"
      "v_lshl_add_u32 v27, v196, 7, v4\nv_lshl_add_u32 v28, v200, 7, v4\ns_add_u32 m0, s26, 65536\ns_nop 0\nglobal_load_lds_dwordx4 v27, s[6:7] offset:0\nv_lshl_add_u32 v27, v197, 7, v4\n"
      "ds_read2_b64 v[196:199], v8 offset0:113 offset1:114\ns_cbranch_vccz .La1001\ns_add_u32 m0, s26, 69632\ns_nop 0\nglobal_load_lds_dwordx4 v28, s[6:7] offset:0\n"
      ".La1001:\nv_lshl_add_u32 v28, v201, 7, v4\ns_add_u32 m0, s53, 45056\ns_nop 0\nglobal_load_lds_dwordx4 v22, s[6:7] offset:0\nv_lshl_add_u32 v22, v177, 7, v4\n"
      "global_load_lds_dwordx4 v23, s[44:45] offset:1024\nv_lshl_add_u32 v23, v181, 7, v5\nds_read2_b64 v[180:183], v6 offset0:113 offset1:114\nglobal_load_lds_dwordx4 v24, s[46:47] offset:2048\n"
      "v_lshl_add_u32 v24, v185, 7, v4\nglobal_load_lds_dwordx4 v25, s[48:49] offset:3072\nv_lshl_add_u32 v25, v189, 7, v5\nds_read2_b64 v[188:191], v7 offset0:113 offset1:114\n"
      "s_add_u32 m0, s53, 49152\ns_nop 0\nglobal_load_lds_dwordx4 v26, s[6:7] offset:0\nv_lshl_add_u32 v26, v193, 7, v4\ns_waitcnt lgkmcnt(0)\ns_setprio 3\ns_barrier\ns_add_u32 m0, s26, 110592\n"
      "s_nop 0\nglobal_load_lds_dwordx4 v27, s[6:7] offset:0\nv_lshl_add_u32 v27, v196, 7, v4\ns_cbranch_vccz .La1002\ns_add_u32 m0, s26, 114688\ns_nop 0\n"
      "global_load_lds_dwordx4 v28, s[6:7] offset:0\n"
      ".La1002:\nv_lshl_add_u32 v28, v202, 7, v4\ns_add_u32 m0, s53, 90112\ns_nop 0\nglobal_load_lds_dwordx4 v22, s[6:7] offset:0\nv_lshl_add_u32 v22, v178, 7, v4\n"
      "global_load_lds_dwordx4 v23, s[44:45] offset:1024\nv_lshl_add_u32 v23, v180, 7, v5\nglobal_load_lds_dwordx4 v24, s[46:47] offset:2048\nv_lshl_add_u32 v24, v186, 7, v4\n"
      "global_load_lds_dwordx4 v25, s[48:49] offset:3072\nv_lshl_add_u32 v25, v188, 7, v5\ns_add_u32 m0, s53, 94208\ns_nop 0\nglobal_load_lds_dwordx4 v26, s[6:7] offset:0\n"
      "v_lshl_add_u32 v26, v194, 7, v4\nv_lshrrev_b32 v30, 2, v240\nv_bfe_u32 v31, v240, 1, 1\ns_lshl_b32 s42, s36, 1\nv_lshrrev_b32 v32, 3, v240\nv_or_b32 v31, s42, v31\ns_sub_u32 s33, s30, s29\n"
      "s_lshl_b32 s42, s33, 11\nv_xor_b32 v31, v31, v32\nv_lshl_add_u32 v30, v30, 7, s42\nv_and_b32 v32, 1, v240\nv_lshl_add_u32 v30, v31, 4, v30\nv_lshl_add_u32 v30, v32, 3, v30\n"
      "v_add_u32 v31, 90112, v30\nv_mov_b32 v32, 0\nv_mov_b32 v33, 0x3d800000\ns_mov_b32 s55, 0x80008000\ns_and_b32 s41, s24, 3\nv_lshrrev_b32 v245, 4, v240\nv_and_b32 v246, 15, v240\n"
      "s_mul_i32 s40, s41, 53248\nv_lshlrev_b32 v238, 1, v245\nv_mul_u32_u24 v245, 3328, v245\nv_xor_b32 v247, v246, v238\nv_add_u32 v245, s40, v245\nv_lshl_add_u32 v236, v247, 4, v245\n"
      "v_xor_b32 v238, 128, v236\nv_add_u32 v237, 12288, v236\nv_add_u32 v239, 36864, v238\nv_add_u32 v238, 24576, v238\ns_lshl_b32 s40, s28, 8\ns_add_u32 s10, s10, s40\ns_addc_u32 s11, s11, 0\n"
      "s_add_u32 s52, s27, 1\ns_mul_i32 s52, s52, 212992\ns_add_u32 s50, s10, s52\ns_addc_u32 s51, s11, 0\ns_add_u32 s50, s50, 212992\ns_addc_u32 s51, s51, 0\ns_cmp_eq_u32 s27, 24\n"
      "s_cselect_b64 s[50:51], s[10:11], s[50:51]\ns_lshl_b32 s42, s2, 2\ns_load_dword s37, s[20:21], s42\ns_load_dword s38, s[18:19], 0\ns_bfe_u32 s42, s2, 0x10002\ns_add_u32 s42, s42, 3\n"
      "s_lshr_b32 s43, s25, 1\ns_sub_u32 s43, 2, s43\ns_cmp_eq_u32 s31, 0\ns_cselect_b32 s42, s42, s43\ns_mul_i32 s42, s42, 5\ns_lshr_b32 s64, %[kp], s42\ns_and_b32 s64, s64, 31\ns_branch .Lfeed\n"
      ".Lf1:\ns_and_b32 s41, s24, 3\nv_lshrrev_b32 v245, 4, v240\nv_and_b32 v246, 15, v240\ns_mul_i32 s40, s41, 53248\nv_lshlrev_b32 v238, 1, v245\nv_mul_u32_u24 v245, 3328, v245\n"
      "v_xor_b32 v247, v246, v238\nv_add_u32 v245, s40, v245\nv_lshl_add_u32 v236, v247, 4, v245\nv_xor_b32 v238, 128, v236\nv_add_u32 v237, 12288, v236\nv_add_u32 v239, 36864, v238\n"
      "v_add_u32 v238, 24576, v238\ns_lshl_b32 s41, s41, 12\ns_mov_b32 m0, 160512\ns_lshl_b32 s40, s28, 8\ns_add_u32 s39, s27, 2\ns_mul_i32 s39, s39, 212992\ns_add_u32 s39, s39, s40\n"
      "v_lshl_add_u32 v244, v240, 4, s40\nv_lshl_add_u32 v243, v240, 2, s40\ns_mov_b64 exec, 0xffff\ns_cmp_lg_u32 s24, 5\ns_waitcnt lgkmcnt(0)\nglobal_load_lds_dwordx4 v244, s[12:13]\n"
      "s_mov_b64 exec, -1\ns_cbranch_scc1 .Lf4\nglobal_load_dword v253, v243, s[14:15]\n"
      ".Lf4:\ns_add_u32 m0, s41, 73728\ns_add_u32 s42, s10, s39\ns_addc_u32 s43, s11, 0\nglobal_load_lds_dwordx4 v236, s[42:43] offset:0\nglobal_load_lds_dwordx4 v237, s[42:43] offset:1024\n"
      "global_load_lds_dwordx4 v238, s[42:43] offset:2048\nglobal_load_lds_dwordx4 v239, s[42:43] offset:3072\ns_add_u32 m0, s41, 118784\ns_add_u32 s42, s42, 212992\ns_addc_u32 s43, s43, 0\n"
      "global_load_lds_dwordx4 v236, s[42:43] offset:0\nglobal_load_lds_dwordx4 v237, s[42:43] offset:1024\nglobal_load_lds_dwordx4 v238, s[42:43] offset:2048\n"
      "global_load_lds_dwordx4 v239, s[42:43] offset:3072\ns_movk_i32 s39, 13798\ns_cmp_lt_u32 s28, 9\ns_cselect_b32 s39, 13108, s39\ns_mul_i32 s40, s28, 247\ns_and_b32 s40, s40, 1023\n"
      "s_mul_i32 s38, s38, s39\ns_add_u32 s38, s38, s40\ns_lshr_b32 s29, s38, 10\ns_add_u32 s38, s38, s39\ns_lshr_b32 s38, s38, 10\ns_sub_u32 s38, s38, s29\ns_lshl_b32 s38, s38, 1\n"
      "s_sub_u32 s25, s38, 24\ns_add_u32 s27, s27, 2\ns_mul_i32 s36, s36, 5\ns_mov_b32 s38, %[bkl]\ns_mov_b32 s39, %[bkh]\ns_lshr_b64 s[38:39], s[38:39], s36\ns_and_b32 s38, s38, 31\n"
      "s_xor_b32 s31, s38, s37\ns_cbranch_scc0 .Lbld\n"
      ".Lbg:\ns_and_b32 s36, s24, 3\ns_lshl_b32 s42, s2, 2\ns_load_dword s37, s[20:21], s42\ns_load_dword s38, s[18:19], 0\ns_bfe_u32 s42, s2, 0x10002\ns_add_u32 s42, s42, 3\n"
      "s_lshr_b32 s43, s25, 1\ns_sub_u32 s43, 2, s43\ns_cmp_eq_u32 s31, 0\ns_cselect_b32 s42, s42, s43\ns_mul_i32 s42, s42, 5\ns_lshr_b32 s64, %[kp], s42\ns_and_b32 s64, s64, 31\ns_setprio 2\n"
      "s_and_b32 s56, s36, 1\ns_lshr_b32 s57, s36, 1\nv_lshrrev_b32 v1, 4, v240\nv_and_b32 v2, 15, v240\nv_lshlrev_b32 v3, 2, v2\nv_add_u32 v3, 162048, v3\nv_mov_b32 v4, 0\nds_write_b32 v3, v4\n"
      "s_mul_i32 s42, s56, 10240\nv_lshrrev_b32 v3, 1, v2\nv_xor_b32 v3, v3, v1\nv_lshl_add_u32 v4, v2, 7, s42\nv_lshl_add_u32 v96, v3, 4, v4\nv_xor_b32 v97, 64, v96\nv_add_u32 v98, 90112, v96\n"
      "v_add_u32 v99, 90112, v97\ns_cmp_eq_u32 s25, 4\ns_cselect_b32 s42, 4, 5\ns_mul_i32 s42, s42, s56\ns_sub_u32 s42, 12, s42\ns_lshl_b32 s42, s42, 11\ns_lshl_b32 s43, s56, 13\n"
      "s_sub_u32 s43, 20480, s43\nv_add_u32 v100, s42, v96\nv_add_u32 v120, s43, v96\nv_add_u32 v101, s42, v97\nv_add_u32 v121, s43, v97\nv_add_u32 v102, s42, v98\nv_add_u32 v122, s43, v98\n"
      "v_add_u32 v103, s42, v99\nv_add_u32 v123, s43, v99\nv_lshrrev_b32 v3, 2, v2\nv_lshl_add_u32 v4, v1, 3, v3\nv_and_b32 v5, 1, v1\nv_lshl_add_u32 v3, v5, 2, v3\ns_lshl_b32 s42, s36, 1\n"
      "v_xor_b32 v3, s42, v3\nv_and_b32 v5, 3, v2\nv_lshlrev_b32 v5, 3, v5\nv_lshl_add_u32 v4, v4, 8, v5\nv_lshl_add_u32 v4, v3, 5, v4\nv_add_u32 v104, 28672, v4\nv_xor_b32 v105, 32, v104\n"
      "v_xor_b32 v106, 64, v104\nv_xor_b32 v107, 96, v104\nv_add_u32 v108, 90112, v104\nv_add_u32 v109, 90112, v105\nv_add_u32 v110, 90112, v106\nv_add_u32 v111, 90112, v107\n"
      "s_lshl_b32 s42, s57, 10\ns_add_u32 s42, s42, 43008\ns_mul_i32 s43, s56, 320\ns_add_u32 s43, s43, s42\nv_lshl_add_u32 v254, v240, 2, s43\nv_cmp_gt_u32 vcc, 16, v240\ns_lshl_b32 s43, s56, 6\n"
      "s_add_u32 s42, s42, s43\ns_add_u32 s42, s42, 256\nv_min_u32 v5, 2, v1\nv_lshl_add_u32 v255, v2, 2, s42\nv_lshl_add_u32 v255, v5, 7, v255\nv_cndmask_b32 v255, v255, v254, vcc\n"
      "s_lshl_b32 s42, s36, 6\ns_add_u32 s42, s42, 160512\nv_lshl_add_u32 v3, v1, 3, s42\nv_xor_b32 v4, 64, v3\ns_lshl_b32 s42, s36, 7\ns_add_u32 s42, s42, 162816\nv_lshl_add_u32 v236, v1, 4, s42\n"
      "v_xor_b32 v237, 128, v236\ns_waitcnt lgkmcnt(0)\ns_mul_i32 s65, s36, 22272\ns_add_u32 s65, s65, 45056\ns_add_u32 s68, s64, 7\ns_min_u32 s68, s68, 21\ns_lshl_b32 s38, s38, 16\n"
      "s_lshr_b32 s42, s24, 2\ns_mul_i32 s43, s42, 3\ns_xor_b32 s43, s43, s36\ns_add_u32 s42, s42, s64\ns_add_u32 s42, s42, 8\ns_and_b32 s42, s42, 1\ns_lshl1_add_u32 s67, s43, s42\n"
      "s_add_u32 s66, s64, s67\ns_cmp_lt_u32 s66, s68\ns_cselect_b32 s66, s66, 127\ns_sub_u32 s66, s66, 7\ns_max_i32 s66, s66, 0\ns_getpc_b64 s[34:35]\n"
      ".Lk1063:\ns_add_u32 s34, s34, .Lt_g-.Lk1063\ns_addc_u32 s35, s35, 0\ns_lshr_b32 s42, s2, 3\ns_lshl1_add_u32 s42, s2, s42\ns_and_b32 s69, s42, 15\ns_lshl_b32 s43, s69, 7\n"
      "s_add_u32 s43, s43, 32768\ns_add_u32 s8, s20, s43\ns_addc_u32 s9, s21, 0\ns_add_u32 s12, s20, 65536\ns_addc_u32 s13, s21, 0\ns_add_u32 s16, s20, 2097152\ns_addc_u32 s17, s21, 0\n"
      "s_lshl_b32 s42, s69, 4\ns_max_u32 s42, s42, 24\ns_sub_u32 s42, s42, 24\ns_min_u32 s69, s42, 192\ns_lshl_b32 s42, s69, 3\ns_add_u32 s42, s42, 24576\ns_add_u32 s14, s20, s42\n"
      "s_addc_u32 s15, s21, 0\ns_mov_b32 s59, 0xc0135761\ns_mov_b32 s60, 0xc1e00000\ns_add_u32 s49, s65, 1856\ns_mov_b32 s45, 2\ns_mov_b32 s46, 0\ns_cmp_lg_u32 s24, 5\ns_cbranch_scc1 .Lj1064\n"
      "s_waitcnt vmcnt(8)\nv_lshlrev_b32 v16, 16, v253\nv_and_b32 v17, 0xffff0000, v253\ns_mov_b32 s42, 0xf000000\nv_max3_f32 v18, |v16|, |v17|, s42\ns_nop 1\n"
      "v_max_u32_dpp v18, v18, v18 quad_perm:[1,0,3,2] row_mask:0xf bank_mask:0xf\ns_nop 1\nv_max_u32_dpp v18, v18, v18 quad_perm:[2,3,0,1] row_mask:0xf bank_mask:0xf\ns_nop 1\n"
      "v_max_u32_dpp v18, v18, v18 row_half_mirror row_mask:0xf bank_mask:0xf\ns_nop 1\nv_max_u32_dpp v18, v18, v18 row_mirror row_mask:0xf bank_mask:0xf\nv_mov_b32 v19, v18\n"
      "s_mov_b32 s42, 162816\nv_lshl_add_u32 v20, v240, 3, s42\nv_permlane16_swap_b32 v18, v19\nv_max_u32 v18, v18, v19\nv_mov_b32 v19, v18\nv_mov_b32 v21, 162812\ns_nop 0\n"
      "v_permlane32_swap_b32 v18, v19\nv_max_u32 v18, v18, v19\nv_and_b32 v18, 0x7f800000, v18\nv_subrev_u32 v19, 0xc000000, v18\nds_write_b32 v21, v19\nv_lshrrev_b32 v18, 23, v18\n"
      "v_sub_u32 v18, 123, v18\nv_ldexp_f32 v16, v16, v18\nv_ldexp_f32 v17, v17, v18\ns_mov_b32 s42, 0x32000000\nv_max_f32 v18, |v16|, s42\nv_max_f32 v19, |v17|, s42\ns_brev_b32 s42, -2\n"
      "v_bfi_b32 v16, s42, v18, v16\nv_bfi_b32 v17, s42, v19, v17\nv_rcp_f32 v16, v16\nv_rcp_f32 v17, v17\ns_nop 0\nv_mul_f32 v18, 0x31800000, v16\nv_mul_f32 v19, 0x31800000, v17\n"
      "ds_write2st64_b64 v20, v[16:17], v[18:19] offset1:1\n"
      ".Lj1064:\ns_barrier\ns_waitcnt vmcnt(8)\nds_read_b64 v[8:9], v3 offset:0\nds_read_b64 v[10:11], v3 offset:32\nds_read_b64 v[12:13], v4 offset:0\nds_read_b64 v[14:15], v4 offset:32\n"
      "s_waitcnt lgkmcnt(0)\nv_lshlrev_b32 v124, 16, v8\nv_and_b32 v125, 0xffff0000, v8\nv_lshlrev_b32 v126, 16, v9\nv_and_b32 v127, 0xffff0000, v9\nv_lshlrev_b32 v128, 16, v10\n"
      "v_and_b32 v129, 0xffff0000, v10\nv_lshlrev_b32 v130, 16, v11\nv_and_b32 v131, 0xffff0000, v11\nv_lshlrev_b32 v132, 16, v12\nv_and_b32 v133, 0xffff0000, v12\nv_lshlrev_b32 v134, 16, v13\n"
      "v_and_b32 v135, 0xffff0000, v13\nv_lshlrev_b32 v136, 16, v14\nv_and_b32 v137, 0xffff0000, v14\nv_lshlrev_b32 v138, 16, v15\nv_and_b32 v139, 0xffff0000, v15\n"
      "v_mov_b64 v[140:141], v[124:125]\nv_mov_b64 v[142:143], v[126:127]\nv_mov_b64 v[144:145], v[128:129]\nv_mov_b64 v[146:147], v[130:131]\nv_mov_b64 v[148:149], v[132:133]\n"
      "v_mov_b64 v[150:151], v[134:135]\nv_mov_b64 v[152:153], v[136:137]\nv_mov_b64 v[154:155], v[138:139]\nv_mov_b64 v[156:157], v[124:125]\nv_mov_b64 v[158:159], v[126:127]\n"
      "v_mov_b64 v[160:161], v[128:129]\nv_mov_b64 v[162:163], v[130:131]\nv_mov_b64 v[164:165], v[132:133]\nv_mov_b64 v[166:167], v[134:135]\nv_mov_b64 v[168:169], v[136:137]\n"
      "v_mov_b64 v[170:171], v[138:139]\nv_mov_b64 v[172:173], v[124:125]\nv_mov_b64 v[174:175], v[126:127]\nv_mov_b64 v[176:177], v[128:129]\nv_mov_b64 v[178:179], v[130:131]\n"
      "v_mov_b64 v[180:181], v[132:133]\nv_mov_b64 v[182:183], v[134:135]\nv_mov_b64 v[184:185], v[136:137]\nv_mov_b64 v[186:187], v[138:139]\nv_mov_b64 v[188:189], v[124:125]\n"
      "v_mov_b64 v[190:191], v[126:127]\nv_mov_b64 v[192:193], v[128:129]\nv_mov_b64 v[194:195], v[130:131]\nv_mov_b64 v[196:197], v[132:133]\nv_mov_b64 v[198:199], v[134:135]\n"
      "v_mov_b64 v[200:201], v[136:137]\nv_mov_b64 v[202:203], v[138:139]\nv_mov_b64 v[204:205], v[124:125]\nv_mov_b64 v[206:207], v[126:127]\nv_mov_b64 v[208:209], v[128:129]\n"
      "v_mov_b64 v[210:211], v[130:131]\nv_mov_b64 v[212:213], v[132:133]\nv_mov_b64 v[214:215], v[134:135]\nv_mov_b64 v[216:217], v[136:137]\nv_mov_b64 v[218:219], v[138:139]\n"
      "v_mov_b64 v[220:221], v[124:125]\nv_mov_b64 v[222:223], v[126:127]\nv_mov_b64 v[224:225], v[128:129]\nv_mov_b64 v[226:227], v[130:131]\nv_mov_b64 v[228:229], v[132:133]\n"
      "v_mov_b64 v[230:231], v[134:135]\nv_mov_b64 v[232:233], v[136:137]\nv_mov_b64 v[234:235], v[138:139]\ns_mov_b32 s58, 4\ns_cmp_eq_u32 s25, 2\ns_cbranch_scc1 .Lg13\ns_cmp_eq_u32 s25, 0\n"
      "s_cbranch_scc1 .Lg12\ns_branch .Lg14\n"
      ".Lbld:\n"
      : "=&{v240}"(lane), "=&{v253}"(tch), "=&{s[4:19]}"(ka0), "=&{s[20:23]}"(ka1), "=&{s[24:31]}"(gs), "+{s2}"(blk)
      : [ka] "s"(__builtin_amdgcn_kernarg_segment_ptr()), [tid] "v"(threadIdx.x), [bkl] "i"((unsigned)BK), [bkh] "i"((unsigned)(BK >> 32)), [kp] "i"(KP[0] | KP[1] << 5 | KP[2] << 10 | KP[3] << 15 | KP[4] << 20)
      : "memory", "scc", "s36", "s37", "s38", "s39", "s40", "s41", "s42", "s43", "v236", "v237", "v238", "v239", "v241", "v242", "v243", "v244", "v245", "v246", "v247");
#define PIN(x) asm volatile("" : "+v"(x))
  const int w = gs[0], tid = w * 64 + lane;
  ws_cin = (const u16*)((unsigned long long)ka0[5] << 32 | ka0[4]);
  wout = (const u16*)((unsigned long long)ka0[13] << 32 | ka0[12]);
  ws = (char*)((unsigned long long)ka1[1] << 32 | ka1[0]);
  const unsigned sb = (unsigned)(size_t)(__attribute__((address_space(3))) char*)shm;
  const int g = lane >> 4, fr = lane & 15, hf = lane >> 5, l31 = lane & 31;
  constexpr unsigned TX = 0xA5C30F96u;
  auto chk = [](unsigned t) -> unsigned { return (t * 0x9E3779B1u) >> 24; };
  const int bid = blk & 7;
  const bool rblk = bid >= 4;
  const int fi = (bid & 3) * 8 + w, mi_ = min(fi, NF - 1);
  const unsigned tc = sb + (g & 1) * 32 + (fr & 3) * 8, h0 = 8 * (g >> 1) + (fr >> 2);
  u32x4 mrow;
  u32x2 rsp;
  unsigned msc;
  {
    unsigned ma_, ur[2], uw[6], fa[2][2];
    if (rblk) {
      const unsigned o3 = min(w * 4480, NF * NF * NF * 2 - 4480);
      asm volatile("s_mov_b32 m0, %2\n\ts_nop 0\n\tglobal_load_lds_dwordx4 %0, %1\n\tglobal_load_lds_dwordx4 %0, %1 offset:1024\n\tglobal_load_lds_dwordx4 %0, %1 offset:2048\n\t"
                   "global_load_lds_dwordx4 %0, %1 offset:3072\n\tglobal_load_lds_dwordx4 %0, %1 offset:3456"
                   :: "v"(lane * 16), "s"((const char*)(ws_cin + 2 * NF * NF * NF) + o3), "s"(sb + W3S + o3) : "memory");
      const unsigned rr = (bid & 3) * 8 + ((lane >> 2) & 7);
      const unsigned vo = (hf * NF * NF + min(rr, 25u) * NF) * 2 + (lane & 3) * 16;
#pragma unroll
      for (int m = 0; m < 2; ++m) {
        const unsigned p = min(w + 8 * m, 12);
        asm volatile("s_mov_b32 m0, %2\n\ts_nop 0\n\tglobal_load_lds_dwordx4 %0, %1" :: "v"(vo), "s"(ws_cin + (NF + 2 * p) * NF * NF), "s"(sb + W2S + p * 1024) : "memory");
      }
      asm volatile("ds_write_b32 %0, %1" :: "v"(sb + U_OFF + tid * 4), "v"(0) : "memory");
    } else {
#pragma unroll
      for (int s = 0; s < 2; ++s)
        asm volatile("s_mov_b32 m0, %2\n\ts_nop 0\n\tglobal_load_lds_dwordx4 %0, %1" :: "v"(min(l31, NF - 1) * NF * 2 + hf * 16), "s"(ws_cin + mi_ * NF * NF + s * 16), "s"(sb + W3S + w * DP + s * 1024) : "memory");
    }
    if (w < 3) asm volatile("s_mov_b32 m0, %2\n\ts_nop 0\n\tglobal_load_lds_dwordx4 %0, %1 sc1" :: "v"(lane * 16), "s"(w ? (const char*)&g_tab + (w - 1) * 1024 : ws), "s"(sb + NC_OFF + w * 1024) : "memory");
    if (w == 3) asm volatile("s_mov_b32 m0, %2\n\ts_nop 0\n\tglobal_load_lds_dword %0, %1" :: "v"(min(lane, 38) * 4), "s"(wout), "s"(sb + WO_OFF) : "memory");
    {
      const int ty = bid >> 2, f = lane >> 2;
      const bool sp = fi >= 24;
      int c0 = sp ? 5 : fi < 16 ? ty - 1 : 1, c1 = sp ? 16 : 3072, rho = sp ? 4 * (fi - 24) + 2 * ty : fi < 16 ? fi : 2 * (fi & 6) + (fi & 1) + 2 * ty;
      asm volatile("" : "+s"(c0), "+s"(c1), "+s"(rho));
      int t = (7 * (f >> 1) + c0) * 1024 + (f & 1) * c1 + ((lane & 3) * 16 + rho) * 16;
      PIN(t);
      ma_ = min((unsigned)t | (fi < 28 ? 0u : ~0u), 41u * 1024 + 1024);
      ma_ = lane < 48 ? ma_ : 43008;
    }
#pragma unroll
    for (int p = 0; p < 2; ++p) {
      ur[p] = sb + W3S + min(8 * g + 4 * p + (fr >> 2), NF - 1) * (NF * NF * 2) + w * 32 + (fr & 3) * 8;
      fa[0][p] = tc + W2S + ((h0 + 4 * p) * 8 + w) * 64; fa[1][p] = tc + W2S + (min(h0 + 16 + 4 * p, 25u) * 8 + w) * 64;
      PIN(ur[p]); PIN(fa[0][p]); PIN(fa[1][p]);
    }
#pragma unroll
    for (int m = 0; m < 6; ++m) {
      const unsigned e = (w + 8 * m) * 16 + fr;
      uw[m] = sb + U_OFF + e * 2 + ((e * 2521u) >> 16) * 12;
      PIN(uw[m]);
      if (m == 5) uw[m] = e < NF * NF ? uw[m] : 0x40000u;
    }
    PIN(uw[5]); PIN(ma_);
    asm volatile("s_waitcnt vmcnt(0) lgkmcnt(0)\n\ts_barrier" :: "{v253}"(tch) : "memory");
    const unsigned wov = *(const unsigned*)(shm + WO_OFF + lane * 4);
    const u32x4 tb = *(const u32x4*)(shm + GT_OFF + (fi >= 24) * 1024 + lane * 16);
    bf16x8 P[2], Q[2];
    if (rblk) {
      s16x4 wl[6][2];
      u32x4 wa;
      unsigned wo2;
      asm volatile("ds_read2_b64 %0, %1 offset0:13 offset1:14" : "=v"(wa) : "v"(sb + WO_OFF + g * 16));
      asm volatile("ds_read_u16 %0, %1 offset:52" : "=v"(wo2) : "v"(sb + WO_OFF + lane * 2));
#pragma unroll
      for (int m = 0; m < 6; ++m)
#pragma unroll
        for (int p = 0; p < 2; ++p) asm volatile("ds_read_b64_tr_b16 %0, %1 offset:%2" : "=v"(wl[m][p]) : "v"(ur[p]), "i"(m * 256));
      u32x2 fl[2][2];
#pragma unroll
      for (int s = 0; s < 2; ++s)
#pragma unroll
        for (int p = 0; p < 2; ++p) asm volatile("ds_read_b64_tr_b16 %0, %1" : "=v"(fl[s][p]) : "v"(fa[s][p]));
      asm volatile("s_waitcnt lgkmcnt(0)" : "+v"(wa), "+v"(wo2), "+v"(wl[0][0]), "+v"(wl[0][1]), "+v"(wl[1][0]), "+v"(wl[1][1]), "+v"(wl[2][0]), "+v"(wl[2][1]), "+v"(wl[3][0]), "+v"(wl[3][1]),
                   "+v"(wl[4][0]), "+v"(wl[4][1]), "+v"(wl[5][0]), "+v"(wl[5][1]), "+v"(fl[0][0]), "+v"(fl[0][1]), "+v"(fl[1][0]), "+v"(fl[1][1]));
      const unsigned km = g < 3 ? ~0u : 0u;
      const u32x4 wm = {wa[0], wa[1] & km, wa[2] & km, wa[3] & km};
      f32x4 du[6];
#pragma unroll
      for (int m = 0; m < 6; ++m)
        du[m] = __builtin_amdgcn_mfma_f32_16x16x32_bf16(__builtin_bit_cast(bf16x8, wm), __builtin_shufflevector(wl[m][0], wl[m][1], 0, 1, 2, 3, 4, 5, 6, 7), (f32x4){0.f, 0.f, 0.f, 0.f}, 0, 0, 0);
      asm volatile("ds_write_b16 %0, %1" :: "v"(lane < NF ? sb + U_OFF + lane * 64 + 52 : 0x40000u), "v"(wo2) : "memory");
#pragma unroll
      for (int m = 0; m < 6; ++m) {
        asm volatile("ds_write_b16 %0, %1" :: "v"(uw[m]), "v"(pk2(du[m][0], du[m][0])) : "memory");
        asm volatile("" :: "v"(du[m]));
      }
      asm volatile("s_waitcnt lgkmcnt(0)\n\ts_barrier" ::: "memory");
      s16x4 ul[2][2];
#pragma unroll
      for (int s = 0; s < 2; ++s)
#pragma unroll
        for (int p = 0; p < 2; ++p)
          asm volatile("ds_read_b64_tr_b16 %0, %1 offset:%2" : "=v"(ul[s][p]) : "v"(tc + U_OFF + h0 * 64), "i"(s * 1024 + p * 256));
      const unsigned fmask = l31 < NF ? ~0u : 0u;
#pragma unroll
      for (int s = 0; s < 2; ++s) {
        u32x4 q4 = {fl[s][0][0] & fmask, fl[s][0][1] & fmask, fl[s][1][0] & fmask, fl[s][1][1] & fmask};
        Q[s] = __builtin_bit_cast(bf16x8, q4);
      }
      asm volatile("s_waitcnt lgkmcnt(0)" : "+v"(ul[0][0]), "+v"(ul[0][1]), "+v"(ul[1][0]), "+v"(ul[1][1]));
#pragma unroll
      for (int s = 0; s < 2; ++s) P[s] = __builtin_shufflevector(ul[s][0], ul[s][1], 0, 1, 2, 3, 4, 5, 6, 7);
    } else {
#pragma unroll
      for (int s = 0; s < 2; ++s) {
        u32x4 xf = *(const u32x4*)(shm + W3S + w * DP + s * 1024 + lane * 16);
        if (s == 1) { const unsigned m = hf ? 0u : ~0u; xf[1] &= m; xf[2] &= m; xf[3] &= m; }
        Q[s] = __builtin_bit_cast(bf16x8, xf);
        const unsigned e = (l31 >> 1) - 8 * s - 4 * hf, v = 0x3f80u << ((lane & 1) * 16);
        u32x4 q4 = {e == 0 ? v : 0u, e == 1 ? v : 0u, e == 2 ? v : 0u, e == 3 ? v : 0u};
        P[s] = __builtin_bit_cast(bf16x8, q4);
      }
    }
    f32x16 D = {};
#pragma unroll
    for (int s = 0; s < 2; ++s) {
      D = __builtin_amdgcn_mfma_f32_32x32x16_bf16(P[s], Q[s], D, 0, 0, 0);
      D = __builtin_amdgcn_mfma_f32_32x32x16_bf16(Q[s], P[s], D, 0, 0, 0);
    }
    float* dl = (float*)(shm + W3S + w * DP);
    D[14] = lane == NF ? __uint_as_float(((unsigned)__builtin_amdgcn_readlane(wov, mi_ >> 1) >> ((mi_ & 1) * 16)) << 16) : D[14];
#pragma unroll
    for (int r = 0; r < 15; ++r) dl[((r & 3) + 8 * (r >> 2) + 4 * hf) * 33 + l31] = D[r];
    __builtin_amdgcn_wave_barrier();
    float v[8], mx = 0.f;
    const unsigned zm = fi >= NF ? 0xffff : rblk ? 0x8000 : 0xc000;
#pragma unroll
    for (int e = 0; e < 8; ++e) v[e] = dl[(tb[e >> 1] >> (16 * (e & 1))) & 0x3ff];
#pragma unroll
    for (int e = 0; e < 8; ++e) {
      const unsigned c = tb[e >> 1] >> (16 * (e & 1));
      float x = __builtin_amdgcn_ldexpf(v[e], ((c >> 10) & 15) - 2);
      PIN(x);
      v[e] = c & zm ? 0.f : x;
      mx = fmaxf(mx, fabsf(v[e]));
    }
    mx = fmaxf(mx, dpp<0xB1>(mx)); mx = fmaxf(mx, dpp<0x4E>(mx)); mx = fmaxf(mx, dpp<0x141>(mx)); mx = fmaxf(mx, dpp<0x140>(mx));
    const unsigned mxu = max(max((unsigned)__builtin_amdgcn_readlane(__float_as_int(mx), 0), (unsigned)__builtin_amdgcn_readlane(__float_as_int(mx), 16)), (unsigned)__builtin_amdgcn_readlane(__float_as_int(mx), 32));
    const unsigned ex = min(max(mxu >> 23, 40u), 250u);
    const float al = __uint_as_float((268 - ex) << 23);
#pragma unroll
    for (int e = 0; e < 4; ++e) {
      const unsigned q = pk2(v[2 * e] * al, v[2 * e + 1] * al);
      mrow[e] = __builtin_bit_cast(unsigned, __builtin_amdgcn_cvt_pkrtz(__uint_as_float(q << 16), __uint_as_float(q & 0xffff0000u)));
    }
    msc = fi < NF ? (ex - 14) << 23 : 0u;
    PIN(msc);
    asm volatile("global_store_dwordx4 %0, %1, %2 sc1" :: "v"(ma_), "{v[248:251]}"(mrow), "s"(ws + WS_M) : "memory");
    asm volatile("global_store_dword %0, %1, %2 sc1" :: "v"((fi < 16 ? (fi >> 2) * 64 + (fi & 3) * 4 : fi < 24 ? ((fi & 6) << 5) + (fi & 1) * 4 + 16 : (fi & 3) * 64 + (fi & 4) + 24) + (bid & 4) * 8), "{v252}"(msc), "s"(ws + WS_M + MC) : "memory");
    if (fi == 31) asm volatile("global_store_dword %0, %1, %2 sc1\n\ts_waitcnt vmcnt(0)" :: "v"((lane & 15) * 128), "{v252}"(msc), "s"(ws + WS_POOL) : "memory");
    const unsigned mval = *(const unsigned*)(shm + NC_OFF + (tid & 255) * 4), qval = *(const unsigned*)(shm + NC_OFF);
    rsp = (u32x2){qval << 8 | chk(mval), mval ^ TX};
    PIN(rsp);
    asm volatile("s_waitcnt lgkmcnt(0)\n\ts_barrier" ::: "memory");
  }
  static_assert(STAGE == 45056 && MC == 41984 && NSLOT == 21 && WS_RESP == 4096 && WS_POOL == 32768 && WS_M == 1048576 && NCL == 7 && sizeof(g_tab.t) == 2048, "constants of the code below");
  asm volatile(
      "s_cmp_lt_u32 s24, 4\ns_cbranch_scc1 .Lbp\ns_branch .Lbg\n"
      ".Lg14:\ns_waitcnt vmcnt(4)\ns_barrier\nds_read_b64_tr_b16 v[64:65], v104 offset:45056\nds_read_b64_tr_b16 v[66:67], v104 offset:46080\nds_read_b64_tr_b16 v[68:69], v104 offset:53248\n"
      "ds_read_b64_tr_b16 v[70:71], v104 offset:54272\nds_read_b64_tr_b16 v[72:73], v105 offset:45056\nds_read_b64_tr_b16 v[74:75], v105 offset:46080\n"
      "ds_read_b64_tr_b16 v[76:77], v105 offset:53248\nds_read_b64_tr_b16 v[78:79], v105 offset:54272\nds_read_b64_tr_b16 v[80:81], v106 offset:45056\n"
      "ds_read_b64_tr_b16 v[82:83], v106 offset:46080\nds_read_b64_tr_b16 v[84:85], v106 offset:53248\nds_read_b64_tr_b16 v[86:87], v106 offset:54272\n"
      "ds_read_b64_tr_b16 v[88:89], v107 offset:45056\nds_read_b64_tr_b16 v[90:91], v107 offset:46080\nds_read_b64_tr_b16 v[92:93], v107 offset:53248\n"
      "ds_read_b64_tr_b16 v[94:95], v107 offset:54272\nds_read_b128 v[0:3], v100 offset:45056\nds_read_b128 v[4:7], v101 offset:45056\nds_read_b128 v[8:11], v120 offset:45056\n"
      "ds_read_b128 v[12:15], v121 offset:45056\nds_read_b128 v[16:19], v96 offset:45056\ns_waitcnt lgkmcnt(1)\n.p2align 3\n"
      ".Lgl14:\nv_mfma_f32_16x16x32_bf16 v[124:127], v[64:67], v[0:3], v[124:127]\nds_read_b128 v[20:23], v97 offset:45056\nv_mfma_f32_16x16x32_bf16 v[124:127], v[68:71], v[4:7], v[124:127]\n"
      "ds_read_b128 v[24:27], v96 offset:47104\nv_mfma_f32_16x16x32_bf16 v[128:131], v[76:79], v[4:7], v[128:131]\nds_read_b128 v[28:31], v97 offset:47104\n"
      "v_mfma_f32_16x16x32_bf16 v[128:131], v[72:75], v[0:3], v[128:131]\nv_mfma_f32_16x16x32_bf16 v[132:135], v[80:83], v[0:3], v[132:135]\n"
      "v_mfma_f32_16x16x32_bf16 v[132:135], v[84:87], v[4:7], v[132:135]\nv_mfma_f32_16x16x32_bf16 v[136:139], v[92:95], v[4:7], v[136:139]\n"
      "v_mfma_f32_16x16x32_bf16 v[136:139], v[88:91], v[0:3], v[136:139]\nv_mfma_f32_16x16x32_bf16 v[152:155], v[88:91], v[8:11], v[152:155]\nds_read_b128 v[0:3], v96 offset:49152\n"
      "v_mfma_f32_16x16x32_bf16 v[152:155], v[92:95], v[12:15], v[152:155]\nds_read_b128 v[4:7], v97 offset:49152\nv_mfma_f32_16x16x32_bf16 v[148:151], v[84:87], v[12:15], v[148:151]\n"
      "v_mfma_f32_16x16x32_bf16 v[148:151], v[80:83], v[8:11], v[148:151]\nv_mfma_f32_16x16x32_bf16 v[144:147], v[72:75], v[8:11], v[144:147]\n"
      "v_mfma_f32_16x16x32_bf16 v[144:147], v[76:79], v[12:15], v[144:147]\nv_mfma_f32_16x16x32_bf16 v[140:143], v[68:71], v[12:15], v[140:143]\ns_waitcnt lgkmcnt(4)\n"
      "v_mfma_f32_16x16x32_bf16 v[140:143], v[64:67], v[8:11], v[140:143]\nv_mfma_f32_16x16x32_bf16 v[156:159], v[64:67], v[16:19], v[156:159]\nds_read_b128 v[8:11], v96 offset:51200\n"
      "v_mfma_f32_16x16x32_bf16 v[156:159], v[68:71], v[20:23], v[156:159]\nds_read_b128 v[12:15], v97 offset:51200\nv_mfma_f32_16x16x32_bf16 v[160:163], v[76:79], v[20:23], v[160:163]\n"
      "v_mfma_f32_16x16x32_bf16 v[160:163], v[72:75], v[16:19], v[160:163]\nv_mfma_f32_16x16x32_bf16 v[164:167], v[80:83], v[16:19], v[164:167]\n"
      "v_mfma_f32_16x16x32_bf16 v[164:167], v[84:87], v[20:23], v[164:167]\nv_mfma_f32_16x16x32_bf16 v[168:171], v[92:95], v[20:23], v[168:171]\ns_waitcnt lgkmcnt(2)\n"
      "v_mfma_f32_16x16x32_bf16 v[168:171], v[88:91], v[16:19], v[168:171]\nv_mfma_f32_16x16x32_bf16 v[184:187], v[88:91], v[24:27], v[184:187]\nds_read_b128 v[16:19], v96 offset:53248\n"
      "v_mfma_f32_16x16x32_bf16 v[184:187], v[92:95], v[28:31], v[184:187]\nds_read_b128 v[20:23], v97 offset:53248\nv_mfma_f32_16x16x32_bf16 v[180:183], v[84:87], v[28:31], v[180:183]\n"
      "v_mfma_f32_16x16x32_bf16 v[180:183], v[80:83], v[24:27], v[180:183]\nv_mfma_f32_16x16x32_bf16 v[176:179], v[72:75], v[24:27], v[176:179]\n"
      "v_mfma_f32_16x16x32_bf16 v[176:179], v[76:79], v[28:31], v[176:179]\ns_waitcnt vmcnt(0) lgkmcnt(0)\nv_mfma_f32_16x16x32_bf16 v[172:175], v[68:71], v[28:31], v[172:175]\ns_barrier\n"
      "v_mfma_f32_16x16x32_bf16 v[172:175], v[64:67], v[24:27], v[172:175]\nds_read_b64_tr_b16 v[32:33], v108 offset:0\nv_mfma_f32_16x16x32_bf16 v[188:191], v[64:67], v[0:3], v[188:191]\n"
      "ds_read_b64_tr_b16 v[34:35], v108 offset:1024\nv_mfma_f32_16x16x32_bf16 v[188:191], v[68:71], v[4:7], v[188:191]\nds_read_b64_tr_b16 v[36:37], v108 offset:8192\n"
      "v_mfma_f32_16x16x32_bf16 v[192:195], v[76:79], v[4:7], v[192:195]\nds_read_b64_tr_b16 v[38:39], v108 offset:9216\nv_mfma_f32_16x16x32_bf16 v[192:195], v[72:75], v[0:3], v[192:195]\n"
      "ds_read_b64_tr_b16 v[40:41], v109 offset:0\nv_mfma_f32_16x16x32_bf16 v[196:199], v[80:83], v[0:3], v[196:199]\nds_read_b64_tr_b16 v[42:43], v109 offset:1024\n"
      "v_mfma_f32_16x16x32_bf16 v[196:199], v[84:87], v[4:7], v[196:199]\nds_read_b64_tr_b16 v[44:45], v109 offset:8192\nv_mfma_f32_16x16x32_bf16 v[200:203], v[92:95], v[4:7], v[200:203]\n"
      "ds_read_b64_tr_b16 v[46:47], v109 offset:9216\nv_mfma_f32_16x16x32_bf16 v[200:203], v[88:91], v[0:3], v[200:203]\nds_read_b64_tr_b16 v[48:49], v110 offset:0\n"
      "v_mfma_f32_16x16x32_bf16 v[216:219], v[88:91], v[8:11], v[216:219]\nds_read_b128 v[0:3], v102 offset:0\nv_mfma_f32_16x16x32_bf16 v[216:219], v[92:95], v[12:15], v[216:219]\n"
      "ds_read_b128 v[4:7], v103 offset:0\nv_mfma_f32_16x16x32_bf16 v[212:215], v[84:87], v[12:15], v[212:215]\nds_read_b64_tr_b16 v[50:51], v110 offset:1024\n"
      "v_mfma_f32_16x16x32_bf16 v[212:215], v[80:83], v[8:11], v[212:215]\nds_read_b64_tr_b16 v[52:53], v110 offset:8192\nv_mfma_f32_16x16x32_bf16 v[208:211], v[72:75], v[8:11], v[208:211]\n"
      "ds_read_b64_tr_b16 v[54:55], v110 offset:9216\nv_mfma_f32_16x16x32_bf16 v[208:211], v[76:79], v[12:15], v[208:211]\nds_read_b64_tr_b16 v[56:57], v111 offset:0\n"
      "v_mfma_f32_16x16x32_bf16 v[204:207], v[68:71], v[12:15], v[204:207]\nds_read_b64_tr_b16 v[58:59], v111 offset:1024\nv_mfma_f32_16x16x32_bf16 v[204:207], v[64:67], v[8:11], v[204:207]\n"
      "ds_read_b64_tr_b16 v[60:61], v111 offset:8192\nv_mfma_f32_16x16x32_bf16 v[220:223], v[64:67], v[16:19], v[220:223]\nds_read_b128 v[8:11], v122 offset:0\n"
      "v_mfma_f32_16x16x32_bf16 v[220:223], v[68:71], v[20:23], v[220:223]\nds_read_b128 v[12:15], v123 offset:0\nv_mfma_f32_16x16x32_bf16 v[224:227], v[76:79], v[20:23], v[224:227]\n"
      "ds_read_b64_tr_b16 v[62:63], v111 offset:9216\nv_mfma_f32_16x16x32_bf16 v[224:227], v[72:75], v[16:19], v[224:227]\nv_mfma_f32_16x16x32_bf16 v[228:231], v[80:83], v[16:19], v[228:231]\n"
      "v_mfma_f32_16x16x32_bf16 v[228:231], v[84:87], v[20:23], v[228:231]\nv_mfma_f32_16x16x32_bf16 v[232:235], v[92:95], v[20:23], v[232:235]\ns_waitcnt lgkmcnt(0)\n"
      "v_mfma_f32_16x16x32_bf16 v[232:235], v[88:91], v[16:19], v[232:235]\nds_read_b128 v[16:19], v98 offset:0\nv_mfma_f32_16x16x32_bf16 v[124:127], v[32:35], v[0:3], v[124:127]\n"
      "ds_read_b128 v[20:23], v99 offset:0\nv_mfma_f32_16x16x32_bf16 v[124:127], v[36:39], v[4:7], v[124:127]\nds_read_b128 v[24:27], v98 offset:2048\n"
      "v_mfma_f32_16x16x32_bf16 v[128:131], v[44:47], v[4:7], v[128:131]\nds_read_b128 v[28:31], v99 offset:2048\nv_mfma_f32_16x16x32_bf16 v[128:131], v[40:43], v[0:3], v[128:131]\n"
      "v_mfma_f32_16x16x32_bf16 v[132:135], v[48:51], v[0:3], v[132:135]\nv_mfma_f32_16x16x32_bf16 v[132:135], v[52:55], v[4:7], v[132:135]\n"
      "v_mfma_f32_16x16x32_bf16 v[136:139], v[60:63], v[4:7], v[136:139]\nv_mfma_f32_16x16x32_bf16 v[136:139], v[56:59], v[0:3], v[136:139]\n"
      "v_mfma_f32_16x16x32_bf16 v[152:155], v[56:59], v[8:11], v[152:155]\nds_read_b128 v[0:3], v98 offset:4096\nv_mfma_f32_16x16x32_bf16 v[152:155], v[60:63], v[12:15], v[152:155]\n"
      "ds_read_b128 v[4:7], v99 offset:4096\nv_mfma_f32_16x16x32_bf16 v[148:151], v[52:55], v[12:15], v[148:151]\nv_mfma_f32_16x16x32_bf16 v[148:151], v[48:51], v[8:11], v[148:151]\n"
      "v_mfma_f32_16x16x32_bf16 v[144:147], v[40:43], v[8:11], v[144:147]\nv_mfma_f32_16x16x32_bf16 v[144:147], v[44:47], v[12:15], v[144:147]\n"
      "v_mfma_f32_16x16x32_bf16 v[140:143], v[36:39], v[12:15], v[140:143]\ns_waitcnt lgkmcnt(4)\nv_mfma_f32_16x16x32_bf16 v[140:143], v[32:35], v[8:11], v[140:143]\n"
      "v_mfma_f32_16x16x32_bf16 v[156:159], v[32:35], v[16:19], v[156:159]\nds_read_b128 v[8:11], v98 offset:6144\nv_mfma_f32_16x16x32_bf16 v[156:159], v[36:39], v[20:23], v[156:159]\n"
      "ds_read_b128 v[12:15], v99 offset:6144\nv_mfma_f32_16x16x32_bf16 v[160:163], v[44:47], v[20:23], v[160:163]\nv_mfma_f32_16x16x32_bf16 v[160:163], v[40:43], v[16:19], v[160:163]\n"
      "v_mfma_f32_16x16x32_bf16 v[164:167], v[48:51], v[16:19], v[164:167]\nv_mfma_f32_16x16x32_bf16 v[164:167], v[52:55], v[20:23], v[164:167]\n"
      "v_mfma_f32_16x16x32_bf16 v[168:171], v[60:63], v[20:23], v[168:171]\ns_waitcnt lgkmcnt(2)\nv_mfma_f32_16x16x32_bf16 v[168:171], v[56:59], v[16:19], v[168:171]\n"
      "v_mfma_f32_16x16x32_bf16 v[184:187], v[56:59], v[24:27], v[184:187]\nds_read_b128 v[16:19], v98 offset:8192\nv_mfma_f32_16x16x32_bf16 v[184:187], v[60:63], v[28:31], v[184:187]\n"
      "ds_read_b128 v[20:23], v99 offset:8192\nv_mfma_f32_16x16x32_bf16 v[180:183], v[52:55], v[28:31], v[180:183]\nv_mfma_f32_16x16x32_bf16 v[180:183], v[48:51], v[24:27], v[180:183]\n"
      "v_mfma_f32_16x16x32_bf16 v[176:179], v[40:43], v[24:27], v[176:179]\nv_mfma_f32_16x16x32_bf16 v[176:179], v[44:47], v[28:31], v[176:179]\ns_waitcnt vmcnt(0) lgkmcnt(0)\n"
      "v_mfma_f32_16x16x32_bf16 v[172:175], v[36:39], v[28:31], v[172:175]\ns_barrier\nv_mfma_f32_16x16x32_bf16 v[172:175], v[32:35], v[24:27], v[172:175]\n"
      "ds_read_b64_tr_b16 v[64:65], v104 offset:0\nv_mfma_f32_16x16x32_bf16 v[188:191], v[32:35], v[0:3], v[188:191]\nds_read_b64_tr_b16 v[66:67], v104 offset:1024\n"
      "v_mfma_f32_16x16x32_bf16 v[188:191], v[36:39], v[4:7], v[188:191]\nds_read_b64_tr_b16 v[68:69], v104 offset:8192\nv_mfma_f32_16x16x32_bf16 v[192:195], v[44:47], v[4:7], v[192:195]\n"
      "ds_read_b64_tr_b16 v[70:71], v104 offset:9216\nv_mfma_f32_16x16x32_bf16 v[192:195], v[40:43], v[0:3], v[192:195]\nds_read_b64_tr_b16 v[72:73], v105 offset:0\n"
      "v_mfma_f32_16x16x32_bf16 v[196:199], v[48:51], v[0:3], v[196:199]\nds_read_b64_tr_b16 v[74:75], v105 offset:1024\nv_mfma_f32_16x16x32_bf16 v[196:199], v[52:55], v[4:7], v[196:199]\n"
      "ds_read_b64_tr_b16 v[76:77], v105 offset:8192\nv_mfma_f32_16x16x32_bf16 v[200:203], v[60:63], v[4:7], v[200:203]\nds_read_b64_tr_b16 v[78:79], v105 offset:9216\n"
      "v_mfma_f32_16x16x32_bf16 v[200:203], v[56:59], v[0:3], v[200:203]\nds_read_b64_tr_b16 v[80:81], v106 offset:0\nv_mfma_f32_16x16x32_bf16 v[216:219], v[56:59], v[8:11], v[216:219]\n"
      "ds_read_b128 v[0:3], v100 offset:0\nv_mfma_f32_16x16x32_bf16 v[216:219], v[60:63], v[12:15], v[216:219]\nds_read_b128 v[4:7], v101 offset:0\n"
      "v_mfma_f32_16x16x32_bf16 v[212:215], v[52:55], v[12:15], v[212:215]\nds_read_b64_tr_b16 v[82:83], v106 offset:1024\nv_mfma_f32_16x16x32_bf16 v[212:215], v[48:51], v[8:11], v[212:215]\n"
      "ds_read_b64_tr_b16 v[84:85], v106 offset:8192\nv_mfma_f32_16x16x32_bf16 v[208:211], v[40:43], v[8:11], v[208:211]\nds_read_b64_tr_b16 v[86:87], v106 offset:9216\n"
      "v_mfma_f32_16x16x32_bf16 v[208:211], v[44:47], v[12:15], v[208:211]\nds_read_b64_tr_b16 v[88:89], v107 offset:0\nv_mfma_f32_16x16x32_bf16 v[204:207], v[36:39], v[12:15], v[204:207]\n"
      "ds_read_b64_tr_b16 v[90:91], v107 offset:1024\nv_mfma_f32_16x16x32_bf16 v[204:207], v[32:35], v[8:11], v[204:207]\nds_read_b64_tr_b16 v[92:93], v107 offset:8192\n"
      "v_mfma_f32_16x16x32_bf16 v[220:223], v[32:35], v[16:19], v[220:223]\nds_read_b128 v[8:11], v120 offset:0\nv_mfma_f32_16x16x32_bf16 v[220:223], v[36:39], v[20:23], v[220:223]\n"
      "ds_read_b128 v[12:15], v121 offset:0\nv_mfma_f32_16x16x32_bf16 v[224:227], v[44:47], v[20:23], v[224:227]\nds_read_b64_tr_b16 v[94:95], v107 offset:9216\n"
      "v_mfma_f32_16x16x32_bf16 v[224:227], v[40:43], v[16:19], v[224:227]\nv_mfma_f32_16x16x32_bf16 v[228:231], v[48:51], v[16:19], v[228:231]\n"
      "v_mfma_f32_16x16x32_bf16 v[228:231], v[52:55], v[20:23], v[228:231]\nv_mfma_f32_16x16x32_bf16 v[232:235], v[60:63], v[20:23], v[232:235]\ns_waitcnt lgkmcnt(0)\n"
      "v_mfma_f32_16x16x32_bf16 v[232:235], v[56:59], v[16:19], v[232:235]\nds_read_b128 v[16:19], v96 offset:0\nv_mfma_f32_16x16x32_bf16 v[124:127], v[64:67], v[0:3], v[124:127]\n"
      "ds_read_b128 v[20:23], v97 offset:0\nv_mfma_f32_16x16x32_bf16 v[124:127], v[68:71], v[4:7], v[124:127]\nds_read_b128 v[24:27], v96 offset:2048\n"
      "v_mfma_f32_16x16x32_bf16 v[128:131], v[76:79], v[4:7], v[128:131]\nds_read_b128 v[28:31], v97 offset:2048\nv_mfma_f32_16x16x32_bf16 v[128:131], v[72:75], v[0:3], v[128:131]\n"
      "v_mfma_f32_16x16x32_bf16 v[132:135], v[80:83], v[0:3], v[132:135]\nv_mfma_f32_16x16x32_bf16 v[132:135], v[84:87], v[4:7], v[132:135]\n"
      "v_mfma_f32_16x16x32_bf16 v[136:139], v[92:95], v[4:7], v[136:139]\nv_mfma_f32_16x16x32_bf16 v[136:139], v[88:91], v[0:3], v[136:139]\n"
      "v_mfma_f32_16x16x32_bf16 v[152:155], v[88:91], v[8:11], v[152:155]\nds_read_b128 v[0:3], v96 offset:4096\nv_mfma_f32_16x16x32_bf16 v[152:155], v[92:95], v[12:15], v[152:155]\n"
      "ds_read_b128 v[4:7], v97 offset:4096\nv_mfma_f32_16x16x32_bf16 v[148:151], v[84:87], v[12:15], v[148:151]\nv_mfma_f32_16x16x32_bf16 v[148:151], v[80:83], v[8:11], v[148:151]\n"
      "v_mfma_f32_16x16x32_bf16 v[144:147], v[72:75], v[8:11], v[144:147]\nv_mfma_f32_16x16x32_bf16 v[144:147], v[76:79], v[12:15], v[144:147]\n"
      "v_mfma_f32_16x16x32_bf16 v[140:143], v[68:71], v[12:15], v[140:143]\ns_waitcnt lgkmcnt(4)\nv_mfma_f32_16x16x32_bf16 v[140:143], v[64:67], v[8:11], v[140:143]\n"
      "v_mfma_f32_16x16x32_bf16 v[156:159], v[64:67], v[16:19], v[156:159]\nds_read_b128 v[8:11], v96 offset:6144\nv_mfma_f32_16x16x32_bf16 v[156:159], v[68:71], v[20:23], v[156:159]\n"
      "ds_read_b128 v[12:15], v97 offset:6144\nv_mfma_f32_16x16x32_bf16 v[160:163], v[76:79], v[20:23], v[160:163]\nv_mfma_f32_16x16x32_bf16 v[160:163], v[72:75], v[16:19], v[160:163]\n"
      "v_mfma_f32_16x16x32_bf16 v[164:167], v[80:83], v[16:19], v[164:167]\nv_mfma_f32_16x16x32_bf16 v[164:167], v[84:87], v[20:23], v[164:167]\n"
      "v_mfma_f32_16x16x32_bf16 v[168:171], v[92:95], v[20:23], v[168:171]\ns_waitcnt lgkmcnt(2)\nv_mfma_f32_16x16x32_bf16 v[168:171], v[88:91], v[16:19], v[168:171]\n"
      "v_mfma_f32_16x16x32_bf16 v[184:187], v[88:91], v[24:27], v[184:187]\nds_read_b128 v[16:19], v96 offset:8192\nv_mfma_f32_16x16x32_bf16 v[184:187], v[92:95], v[28:31], v[184:187]\n"
      "ds_read_b128 v[20:23], v97 offset:8192\nv_mfma_f32_16x16x32_bf16 v[180:183], v[84:87], v[28:31], v[180:183]\nv_mfma_f32_16x16x32_bf16 v[180:183], v[80:83], v[24:27], v[180:183]\n"
      "v_mfma_f32_16x16x32_bf16 v[176:179], v[72:75], v[24:27], v[176:179]\nv_mfma_f32_16x16x32_bf16 v[176:179], v[76:79], v[28:31], v[176:179]\ns_waitcnt vmcnt(0) lgkmcnt(0)\n"
      "v_mfma_f32_16x16x32_bf16 v[172:175], v[68:71], v[28:31], v[172:175]\ns_barrier\nv_mfma_f32_16x16x32_bf16 v[172:175], v[64:67], v[24:27], v[172:175]\n"
      "ds_read_b64_tr_b16 v[32:33], v104 offset:45056\nv_mfma_f32_16x16x32_bf16 v[188:191], v[64:67], v[0:3], v[188:191]\nds_read_b64_tr_b16 v[34:35], v104 offset:46080\n"
      "v_mfma_f32_16x16x32_bf16 v[188:191], v[68:71], v[4:7], v[188:191]\nds_read_b64_tr_b16 v[36:37], v104 offset:53248\nv_mfma_f32_16x16x32_bf16 v[192:195], v[76:79], v[4:7], v[192:195]\n"
      "ds_read_b64_tr_b16 v[38:39], v104 offset:54272\nv_mfma_f32_16x16x32_bf16 v[192:195], v[72:75], v[0:3], v[192:195]\nds_read_b64_tr_b16 v[40:41], v105 offset:45056\n"
      "v_mfma_f32_16x16x32_bf16 v[196:199], v[80:83], v[0:3], v[196:199]\nds_read_b64_tr_b16 v[42:43], v105 offset:46080\nv_mfma_f32_16x16x32_bf16 v[196:199], v[84:87], v[4:7], v[196:199]\n"
      "ds_read_b64_tr_b16 v[44:45], v105 offset:53248\nv_mfma_f32_16x16x32_bf16 v[200:203], v[92:95], v[4:7], v[200:203]\nds_read_b64_tr_b16 v[46:47], v105 offset:54272\n"
      "v_mfma_f32_16x16x32_bf16 v[200:203], v[88:91], v[0:3], v[200:203]\nds_read_b64_tr_b16 v[48:49], v106 offset:45056\nv_mfma_f32_16x16x32_bf16 v[216:219], v[88:91], v[8:11], v[216:219]\n"
      "ds_read_b128 v[0:3], v100 offset:45056\nv_mfma_f32_16x16x32_bf16 v[216:219], v[92:95], v[12:15], v[216:219]\nds_read_b128 v[4:7], v101 offset:45056\n"
      "v_mfma_f32_16x16x32_bf16 v[212:215], v[84:87], v[12:15], v[212:215]\nds_read_b64_tr_b16 v[50:51], v106 offset:46080\nv_mfma_f32_16x16x32_bf16 v[212:215], v[80:83], v[8:11], v[212:215]\n"
      "ds_read_b64_tr_b16 v[52:53], v106 offset:53248\nv_mfma_f32_16x16x32_bf16 v[208:211], v[72:75], v[8:11], v[208:211]\nds_read_b64_tr_b16 v[54:55], v106 offset:54272\n"
      "v_mfma_f32_16x16x32_bf16 v[208:211], v[76:79], v[12:15], v[208:211]\nds_read_b64_tr_b16 v[56:57], v107 offset:45056\nv_mfma_f32_16x16x32_bf16 v[204:207], v[68:71], v[12:15], v[204:207]\n"
      "ds_read_b64_tr_b16 v[58:59], v107 offset:46080\nv_mfma_f32_16x16x32_bf16 v[204:207], v[64:67], v[8:11], v[204:207]\nds_read_b64_tr_b16 v[60:61], v107 offset:53248\n"
      "v_mfma_f32_16x16x32_bf16 v[220:223], v[64:67], v[16:19], v[220:223]\nds_read_b128 v[8:11], v120 offset:45056\nv_mfma_f32_16x16x32_bf16 v[220:223], v[68:71], v[20:23], v[220:223]\n"
      "ds_read_b128 v[12:15], v121 offset:45056\nv_mfma_f32_16x16x32_bf16 v[224:227], v[76:79], v[20:23], v[224:227]\nds_read_b64_tr_b16 v[62:63], v107 offset:54272\n"
      "v_mfma_f32_16x16x32_bf16 v[224:227], v[72:75], v[16:19], v[224:227]\nv_mfma_f32_16x16x32_bf16 v[228:231], v[80:83], v[16:19], v[228:231]\n"
      "v_mfma_f32_16x16x32_bf16 v[228:231], v[84:87], v[20:23], v[228:231]\nv_mfma_f32_16x16x32_bf16 v[232:235], v[92:95], v[20:23], v[232:235]\ns_waitcnt lgkmcnt(0)\n"
      "v_mfma_f32_16x16x32_bf16 v[232:235], v[88:91], v[16:19], v[232:235]\nds_read_b128 v[16:19], v96 offset:45056\nv_mfma_f32_16x16x32_bf16 v[124:127], v[32:35], v[0:3], v[124:127]\n"
      "ds_read_b128 v[20:23], v97 offset:45056\nv_mfma_f32_16x16x32_bf16 v[124:127], v[36:39], v[4:7], v[124:127]\nds_read_b128 v[24:27], v96 offset:47104\n"
      "v_mfma_f32_16x16x32_bf16 v[128:131], v[44:47], v[4:7], v[128:131]\nds_read_b128 v[28:31], v97 offset:47104\nv_mfma_f32_16x16x32_bf16 v[128:131], v[40:43], v[0:3], v[128:131]\n"
      "v_mfma_f32_16x16x32_bf16 v[132:135], v[48:51], v[0:3], v[132:135]\nv_mfma_f32_16x16x32_bf16 v[132:135], v[52:55], v[4:7], v[132:135]\n"
      "v_mfma_f32_16x16x32_bf16 v[136:139], v[60:63], v[4:7], v[136:139]\nv_mfma_f32_16x16x32_bf16 v[136:139], v[56:59], v[0:3], v[136:139]\n"
      "v_mfma_f32_16x16x32_bf16 v[152:155], v[56:59], v[8:11], v[152:155]\nds_read_b128 v[0:3], v96 offset:49152\nv_mfma_f32_16x16x32_bf16 v[152:155], v[60:63], v[12:15], v[152:155]\n"
      "ds_read_b128 v[4:7], v97 offset:49152\nv_mfma_f32_16x16x32_bf16 v[148:151], v[52:55], v[12:15], v[148:151]\nv_mfma_f32_16x16x32_bf16 v[148:151], v[48:51], v[8:11], v[148:151]\n"
      "v_mfma_f32_16x16x32_bf16 v[144:147], v[40:43], v[8:11], v[144:147]\nv_mfma_f32_16x16x32_bf16 v[144:147], v[44:47], v[12:15], v[144:147]\n"
      "v_mfma_f32_16x16x32_bf16 v[140:143], v[36:39], v[12:15], v[140:143]\ns_waitcnt lgkmcnt(4)\nv_mfma_f32_16x16x32_bf16 v[140:143], v[32:35], v[8:11], v[140:143]\n"
      "v_mfma_f32_16x16x32_bf16 v[156:159], v[32:35], v[16:19], v[156:159]\nds_read_b128 v[8:11], v96 offset:51200\nv_mfma_f32_16x16x32_bf16 v[156:159], v[36:39], v[20:23], v[156:159]\n"
      "ds_read_b128 v[12:15], v97 offset:51200\nv_mfma_f32_16x16x32_bf16 v[160:163], v[44:47], v[20:23], v[160:163]\nv_mfma_f32_16x16x32_bf16 v[160:163], v[40:43], v[16:19], v[160:163]\n"
      "v_mfma_f32_16x16x32_bf16 v[164:167], v[48:51], v[16:19], v[164:167]\nv_mfma_f32_16x16x32_bf16 v[164:167], v[52:55], v[20:23], v[164:167]\n"
      "v_mfma_f32_16x16x32_bf16 v[168:171], v[60:63], v[20:23], v[168:171]\ns_waitcnt lgkmcnt(2)\nv_mfma_f32_16x16x32_bf16 v[168:171], v[56:59], v[16:19], v[168:171]\n"
      "v_mfma_f32_16x16x32_bf16 v[184:187], v[56:59], v[24:27], v[184:187]\nds_read_b128 v[16:19], v96 offset:53248\nv_mfma_f32_16x16x32_bf16 v[184:187], v[60:63], v[28:31], v[184:187]\n"
      "ds_read_b128 v[20:23], v97 offset:53248\nv_mfma_f32_16x16x32_bf16 v[180:183], v[52:55], v[28:31], v[180:183]\nv_mfma_f32_16x16x32_bf16 v[180:183], v[48:51], v[24:27], v[180:183]\n"
      "v_mfma_f32_16x16x32_bf16 v[176:179], v[40:43], v[24:27], v[176:179]\nv_mfma_f32_16x16x32_bf16 v[176:179], v[44:47], v[28:31], v[176:179]\ns_waitcnt vmcnt(0) lgkmcnt(0)\n"
      "v_mfma_f32_16x16x32_bf16 v[172:175], v[36:39], v[28:31], v[172:175]\ns_barrier\nv_mfma_f32_16x16x32_bf16 v[172:175], v[32:35], v[24:27], v[172:175]\n"
      "ds_read_b64_tr_b16 v[64:65], v108 offset:0\nv_mfma_f32_16x16x32_bf16 v[188:191], v[32:35], v[0:3], v[188:191]\nds_read_b64_tr_b16 v[66:67], v108 offset:1024\n"
      "v_mfma_f32_16x16x32_bf16 v[188:191], v[36:39], v[4:7], v[188:191]\nds_read_b64_tr_b16 v[68:69], v108 offset:8192\nv_mfma_f32_16x16x32_bf16 v[192:195], v[44:47], v[4:7], v[192:195]\n"
      "ds_read_b64_tr_b16 v[70:71], v108 offset:9216\nv_mfma_f32_16x16x32_bf16 v[192:195], v[40:43], v[0:3], v[192:195]\nds_read_b64_tr_b16 v[72:73], v109 offset:0\n"
      "v_mfma_f32_16x16x32_bf16 v[196:199], v[48:51], v[0:3], v[196:199]\nds_read_b64_tr_b16 v[74:75], v109 offset:1024\nv_mfma_f32_16x16x32_bf16 v[196:199], v[52:55], v[4:7], v[196:199]\n"
      "ds_read_b64_tr_b16 v[76:77], v109 offset:8192\nv_mfma_f32_16x16x32_bf16 v[200:203], v[60:63], v[4:7], v[200:203]\nds_read_b64_tr_b16 v[78:79], v109 offset:9216\n"
      "v_mfma_f32_16x16x32_bf16 v[200:203], v[56:59], v[0:3], v[200:203]\nds_read_b64_tr_b16 v[80:81], v110 offset:0\nv_mfma_f32_16x16x32_bf16 v[216:219], v[56:59], v[8:11], v[216:219]\n"
      "ds_read_b128 v[0:3], v102 offset:0\nv_mfma_f32_16x16x32_bf16 v[216:219], v[60:63], v[12:15], v[216:219]\nds_read_b128 v[4:7], v103 offset:0\n"
      "v_mfma_f32_16x16x32_bf16 v[212:215], v[52:55], v[12:15], v[212:215]\nds_read_b64_tr_b16 v[82:83], v110 offset:1024\nv_mfma_f32_16x16x32_bf16 v[212:215], v[48:51], v[8:11], v[212:215]\n"
      "ds_read_b64_tr_b16 v[84:85], v110 offset:8192\nv_mfma_f32_16x16x32_bf16 v[208:211], v[40:43], v[8:11], v[208:211]\nds_read_b64_tr_b16 v[86:87], v110 offset:9216\n"
      "v_mfma_f32_16x16x32_bf16 v[208:211], v[44:47], v[12:15], v[208:211]\nds_read_b64_tr_b16 v[88:89], v111 offset:0\nv_mfma_f32_16x16x32_bf16 v[204:207], v[36:39], v[12:15], v[204:207]\n"
      "ds_read_b64_tr_b16 v[90:91], v111 offset:1024\nv_mfma_f32_16x16x32_bf16 v[204:207], v[32:35], v[8:11], v[204:207]\nds_read_b64_tr_b16 v[92:93], v111 offset:8192\n"
      "v_mfma_f32_16x16x32_bf16 v[220:223], v[32:35], v[16:19], v[220:223]\nds_read_b128 v[8:11], v122 offset:0\nv_mfma_f32_16x16x32_bf16 v[220:223], v[36:39], v[20:23], v[220:223]\n"
      "ds_read_b128 v[12:15], v123 offset:0\nv_mfma_f32_16x16x32_bf16 v[224:227], v[44:47], v[20:23], v[224:227]\nds_read_b64_tr_b16 v[94:95], v111 offset:9216\n"
      "v_mfma_f32_16x16x32_bf16 v[224:227], v[40:43], v[16:19], v[224:227]\nv_mfma_f32_16x16x32_bf16 v[228:231], v[48:51], v[16:19], v[228:231]\n"
      "v_mfma_f32_16x16x32_bf16 v[228:231], v[52:55], v[20:23], v[228:231]\nv_mfma_f32_16x16x32_bf16 v[232:235], v[60:63], v[20:23], v[232:235]\ns_waitcnt lgkmcnt(0)\n"
      "v_mfma_f32_16x16x32_bf16 v[232:235], v[56:59], v[16:19], v[232:235]\nds_read_b128 v[16:19], v98 offset:0\nv_mfma_f32_16x16x32_bf16 v[124:127], v[64:67], v[0:3], v[124:127]\n"
      "ds_read_b128 v[20:23], v99 offset:0\nv_mfma_f32_16x16x32_bf16 v[124:127], v[68:71], v[4:7], v[124:127]\nds_read_b128 v[24:27], v98 offset:2048\n"
      "v_mfma_f32_16x16x32_bf16 v[128:131], v[76:79], v[4:7], v[128:131]\nds_read_b128 v[28:31], v99 offset:2048\nv_mfma_f32_16x16x32_bf16 v[128:131], v[72:75], v[0:3], v[128:131]\n"
      "v_mfma_f32_16x16x32_bf16 v[132:135], v[80:83], v[0:3], v[132:135]\nv_mfma_f32_16x16x32_bf16 v[132:135], v[84:87], v[4:7], v[132:135]\n"
      "v_mfma_f32_16x16x32_bf16 v[136:139], v[92:95], v[4:7], v[136:139]\nv_mfma_f32_16x16x32_bf16 v[136:139], v[88:91], v[0:3], v[136:139]\n"
      "v_mfma_f32_16x16x32_bf16 v[152:155], v[88:91], v[8:11], v[152:155]\nds_read_b128 v[0:3], v98 offset:4096\nv_mfma_f32_16x16x32_bf16 v[152:155], v[92:95], v[12:15], v[152:155]\n"
      "ds_read_b128 v[4:7], v99 offset:4096\nv_mfma_f32_16x16x32_bf16 v[148:151], v[84:87], v[12:15], v[148:151]\nv_mfma_f32_16x16x32_bf16 v[148:151], v[80:83], v[8:11], v[148:151]\n"
      "v_mfma_f32_16x16x32_bf16 v[144:147], v[72:75], v[8:11], v[144:147]\nv_mfma_f32_16x16x32_bf16 v[144:147], v[76:79], v[12:15], v[144:147]\n"
      "v_mfma_f32_16x16x32_bf16 v[140:143], v[68:71], v[12:15], v[140:143]\ns_waitcnt lgkmcnt(4)\nv_mfma_f32_16x16x32_bf16 v[140:143], v[64:67], v[8:11], v[140:143]\n"
      "v_mfma_f32_16x16x32_bf16 v[156:159], v[64:67], v[16:19], v[156:159]\nds_read_b128 v[8:11], v98 offset:6144\nv_mfma_f32_16x16x32_bf16 v[156:159], v[68:71], v[20:23], v[156:159]\n"
      "ds_read_b128 v[12:15], v99 offset:6144\nv_mfma_f32_16x16x32_bf16 v[160:163], v[76:79], v[20:23], v[160:163]\nv_mfma_f32_16x16x32_bf16 v[160:163], v[72:75], v[16:19], v[160:163]\n"
      "v_mfma_f32_16x16x32_bf16 v[164:167], v[80:83], v[16:19], v[164:167]\nv_mfma_f32_16x16x32_bf16 v[164:167], v[84:87], v[20:23], v[164:167]\n"
      "v_mfma_f32_16x16x32_bf16 v[168:171], v[92:95], v[20:23], v[168:171]\ns_waitcnt lgkmcnt(2)\nv_mfma_f32_16x16x32_bf16 v[168:171], v[88:91], v[16:19], v[168:171]\n"
      "v_mfma_f32_16x16x32_bf16 v[184:187], v[88:91], v[24:27], v[184:187]\nds_read_b128 v[16:19], v98 offset:8192\nv_mfma_f32_16x16x32_bf16 v[184:187], v[92:95], v[28:31], v[184:187]\n"
      "ds_read_b128 v[20:23], v99 offset:8192\nv_mfma_f32_16x16x32_bf16 v[180:183], v[84:87], v[28:31], v[180:183]\nv_mfma_f32_16x16x32_bf16 v[180:183], v[80:83], v[24:27], v[180:183]\n"
      "v_mfma_f32_16x16x32_bf16 v[176:179], v[72:75], v[24:27], v[176:179]\nv_mfma_f32_16x16x32_bf16 v[176:179], v[76:79], v[28:31], v[176:179]\ns_waitcnt vmcnt(0) lgkmcnt(0)\n"
      "v_mfma_f32_16x16x32_bf16 v[172:175], v[68:71], v[28:31], v[172:175]\ns_barrier\nv_mfma_f32_16x16x32_bf16 v[172:175], v[64:67], v[24:27], v[172:175]\n"
      "ds_read_b64_tr_b16 v[32:33], v104 offset:0\nv_mfma_f32_16x16x32_bf16 v[188:191], v[64:67], v[0:3], v[188:191]\nds_read_b64_tr_b16 v[34:35], v104 offset:1024\n"
      "v_mfma_f32_16x16x32_bf16 v[188:191], v[68:71], v[4:7], v[188:191]\nds_read_b64_tr_b16 v[36:37], v104 offset:8192\nv_mfma_f32_16x16x32_bf16 v[192:195], v[76:79], v[4:7], v[192:195]\n"
      "ds_read_b64_tr_b16 v[38:39], v104 offset:9216\nv_mfma_f32_16x16x32_bf16 v[192:195], v[72:75], v[0:3], v[192:195]\nds_read_b64_tr_b16 v[40:41], v105 offset:0\n"
      "v_mfma_f32_16x16x32_bf16 v[196:199], v[80:83], v[0:3], v[196:199]\nds_read_b64_tr_b16 v[42:43], v105 offset:1024\nv_mfma_f32_16x16x32_bf16 v[196:199], v[84:87], v[4:7], v[196:199]\n"
      "ds_read_b64_tr_b16 v[44:45], v105 offset:8192\nv_mfma_f32_16x16x32_bf16 v[200:203], v[92:95], v[4:7], v[200:203]\nds_read_b64_tr_b16 v[46:47], v105 offset:9216\n"
      "v_mfma_f32_16x16x32_bf16 v[200:203], v[88:91], v[0:3], v[200:203]\nds_read_b64_tr_b16 v[48:49], v106 offset:0\nv_mfma_f32_16x16x32_bf16 v[216:219], v[88:91], v[8:11], v[216:219]\n"
      "ds_read_b128 v[0:3], v100 offset:0\nv_mfma_f32_16x16x32_bf16 v[216:219], v[92:95], v[12:15], v[216:219]\nds_read_b128 v[4:7], v101 offset:0\n"
      "v_mfma_f32_16x16x32_bf16 v[212:215], v[84:87], v[12:15], v[212:215]\nds_read_b64_tr_b16 v[50:51], v106 offset:1024\nv_mfma_f32_16x16x32_bf16 v[212:215], v[80:83], v[8:11], v[212:215]\n"
      "ds_read_b64_tr_b16 v[52:53], v106 offset:8192\nv_mfma_f32_16x16x32_bf16 v[208:211], v[72:75], v[8:11], v[208:211]\nds_read_b64_tr_b16 v[54:55], v106 offset:9216\n"
      "v_mfma_f32_16x16x32_bf16 v[208:211], v[76:79], v[12:15], v[208:211]\nds_read_b64_tr_b16 v[56:57], v107 offset:0\nv_mfma_f32_16x16x32_bf16 v[204:207], v[68:71], v[12:15], v[204:207]\n"
      "ds_read_b64_tr_b16 v[58:59], v107 offset:1024\nv_mfma_f32_16x16x32_bf16 v[204:207], v[64:67], v[8:11], v[204:207]\nds_read_b64_tr_b16 v[60:61], v107 offset:8192\n"
      "v_mfma_f32_16x16x32_bf16 v[220:223], v[64:67], v[16:19], v[220:223]\nds_read_b128 v[8:11], v120 offset:0\nv_mfma_f32_16x16x32_bf16 v[220:223], v[68:71], v[20:23], v[220:223]\n"
      "ds_read_b128 v[12:15], v121 offset:0\nv_mfma_f32_16x16x32_bf16 v[224:227], v[76:79], v[20:23], v[224:227]\nds_read_b64_tr_b16 v[62:63], v107 offset:9216\n"
      "v_mfma_f32_16x16x32_bf16 v[224:227], v[72:75], v[16:19], v[224:227]\nv_mfma_f32_16x16x32_bf16 v[228:231], v[80:83], v[16:19], v[228:231]\n"
      "v_mfma_f32_16x16x32_bf16 v[228:231], v[84:87], v[20:23], v[228:231]\nv_mfma_f32_16x16x32_bf16 v[232:235], v[92:95], v[20:23], v[232:235]\ns_waitcnt lgkmcnt(0)\n"
      "v_mfma_f32_16x16x32_bf16 v[232:235], v[88:91], v[16:19], v[232:235]\nds_read_b128 v[16:19], v96 offset:0\nv_mfma_f32_16x16x32_bf16 v[124:127], v[32:35], v[0:3], v[124:127]\n"
      "ds_read_b128 v[20:23], v97 offset:0\nv_mfma_f32_16x16x32_bf16 v[124:127], v[36:39], v[4:7], v[124:127]\nds_read_b128 v[24:27], v96 offset:2048\n"
      "v_mfma_f32_16x16x32_bf16 v[128:131], v[44:47], v[4:7], v[128:131]\nds_read_b128 v[28:31], v97 offset:2048\nv_mfma_f32_16x16x32_bf16 v[128:131], v[40:43], v[0:3], v[128:131]\n"
      "v_mfma_f32_16x16x32_bf16 v[132:135], v[48:51], v[0:3], v[132:135]\nv_mfma_f32_16x16x32_bf16 v[132:135], v[52:55], v[4:7], v[132:135]\n"
      "v_mfma_f32_16x16x32_bf16 v[136:139], v[60:63], v[4:7], v[136:139]\nv_mfma_f32_16x16x32_bf16 v[136:139], v[56:59], v[0:3], v[136:139]\n"
      "v_mfma_f32_16x16x32_bf16 v[152:155], v[56:59], v[8:11], v[152:155]\nds_read_b128 v[0:3], v96 offset:4096\nv_mfma_f32_16x16x32_bf16 v[152:155], v[60:63], v[12:15], v[152:155]\n"
      "ds_read_b128 v[4:7], v97 offset:4096\nv_mfma_f32_16x16x32_bf16 v[148:151], v[52:55], v[12:15], v[148:151]\nv_mfma_f32_16x16x32_bf16 v[148:151], v[48:51], v[8:11], v[148:151]\n"
      "v_mfma_f32_16x16x32_bf16 v[144:147], v[40:43], v[8:11], v[144:147]\nv_mfma_f32_16x16x32_bf16 v[144:147], v[44:47], v[12:15], v[144:147]\n"
      "v_mfma_f32_16x16x32_bf16 v[140:143], v[36:39], v[12:15], v[140:143]\ns_waitcnt lgkmcnt(4)\nv_mfma_f32_16x16x32_bf16 v[140:143], v[32:35], v[8:11], v[140:143]\n"
      "v_mfma_f32_16x16x32_bf16 v[156:159], v[32:35], v[16:19], v[156:159]\nds_read_b128 v[8:11], v96 offset:6144\nv_mfma_f32_16x16x32_bf16 v[156:159], v[36:39], v[20:23], v[156:159]\n"
      "ds_read_b128 v[12:15], v97 offset:6144\nv_mfma_f32_16x16x32_bf16 v[160:163], v[44:47], v[20:23], v[160:163]\nv_mfma_f32_16x16x32_bf16 v[160:163], v[40:43], v[16:19], v[160:163]\n"
      "v_mfma_f32_16x16x32_bf16 v[164:167], v[48:51], v[16:19], v[164:167]\nv_mfma_f32_16x16x32_bf16 v[164:167], v[52:55], v[20:23], v[164:167]\n"
      "v_mfma_f32_16x16x32_bf16 v[168:171], v[60:63], v[20:23], v[168:171]\ns_waitcnt lgkmcnt(2)\nv_mfma_f32_16x16x32_bf16 v[168:171], v[56:59], v[16:19], v[168:171]\n"
      "v_mfma_f32_16x16x32_bf16 v[184:187], v[56:59], v[24:27], v[184:187]\nds_read_b128 v[16:19], v96 offset:8192\nv_mfma_f32_16x16x32_bf16 v[184:187], v[60:63], v[28:31], v[184:187]\n"
      "ds_read_b128 v[20:23], v97 offset:8192\nv_mfma_f32_16x16x32_bf16 v[180:183], v[52:55], v[28:31], v[180:183]\nv_mfma_f32_16x16x32_bf16 v[180:183], v[48:51], v[24:27], v[180:183]\n"
      "v_mfma_f32_16x16x32_bf16 v[176:179], v[40:43], v[24:27], v[176:179]\nv_mfma_f32_16x16x32_bf16 v[176:179], v[44:47], v[28:31], v[176:179]\ns_waitcnt vmcnt(0) lgkmcnt(0)\n"
      "v_mfma_f32_16x16x32_bf16 v[172:175], v[36:39], v[28:31], v[172:175]\ns_barrier\nv_mfma_f32_16x16x32_bf16 v[172:175], v[32:35], v[24:27], v[172:175]\n"
      "ds_read_b64_tr_b16 v[64:65], v104 offset:45056\nv_mfma_f32_16x16x32_bf16 v[188:191], v[32:35], v[0:3], v[188:191]\nds_read_b64_tr_b16 v[66:67], v104 offset:46080\n"
      "v_mfma_f32_16x16x32_bf16 v[188:191], v[36:39], v[4:7], v[188:191]\nds_read_b64_tr_b16 v[68:69], v104 offset:53248\nv_mfma_f32_16x16x32_bf16 v[192:195], v[44:47], v[4:7], v[192:195]\n"
      "ds_read_b64_tr_b16 v[70:71], v104 offset:54272\nv_mfma_f32_16x16x32_bf16 v[192:195], v[40:43], v[0:3], v[192:195]\nds_read_b64_tr_b16 v[72:73], v105 offset:45056\n"
      "v_mfma_f32_16x16x32_bf16 v[196:199], v[48:51], v[0:3], v[196:199]\nds_read_b64_tr_b16 v[74:75], v105 offset:46080\nv_mfma_f32_16x16x32_bf16 v[196:199], v[52:55], v[4:7], v[196:199]\n"
      "ds_read_b64_tr_b16 v[76:77], v105 offset:53248\nv_mfma_f32_16x16x32_bf16 v[200:203], v[60:63], v[4:7], v[200:203]\nds_read_b64_tr_b16 v[78:79], v105 offset:54272\n"
      "v_mfma_f32_16x16x32_bf16 v[200:203], v[56:59], v[0:3], v[200:203]\nds_read_b64_tr_b16 v[80:81], v106 offset:45056\nv_mfma_f32_16x16x32_bf16 v[216:219], v[56:59], v[8:11], v[216:219]\n"
      "ds_read_b128 v[0:3], v100 offset:45056\nv_mfma_f32_16x16x32_bf16 v[216:219], v[60:63], v[12:15], v[216:219]\nds_read_b128 v[4:7], v101 offset:45056\n"
      "v_mfma_f32_16x16x32_bf16 v[212:215], v[52:55], v[12:15], v[212:215]\nds_read_b64_tr_b16 v[82:83], v106 offset:46080\nv_mfma_f32_16x16x32_bf16 v[212:215], v[48:51], v[8:11], v[212:215]\n"
      "ds_read_b64_tr_b16 v[84:85], v106 offset:53248\nv_mfma_f32_16x16x32_bf16 v[208:211], v[40:43], v[8:11], v[208:211]\nds_read_b64_tr_b16 v[86:87], v106 offset:54272\n"
      "v_mfma_f32_16x16x32_bf16 v[208:211], v[44:47], v[12:15], v[208:211]\nds_read_b64_tr_b16 v[88:89], v107 offset:45056\nv_mfma_f32_16x16x32_bf16 v[204:207], v[36:39], v[12:15], v[204:207]\n"
      "ds_read_b64_tr_b16 v[90:91], v107 offset:46080\nv_mfma_f32_16x16x32_bf16 v[204:207], v[32:35], v[8:11], v[204:207]\nds_read_b64_tr_b16 v[92:93], v107 offset:53248\n"
      "v_mfma_f32_16x16x32_bf16 v[220:223], v[32:35], v[16:19], v[220:223]\nds_read_b128 v[8:11], v120 offset:45056\nv_mfma_f32_16x16x32_bf16 v[220:223], v[36:39], v[20:23], v[220:223]\n"
      "ds_read_b128 v[12:15], v121 offset:45056\nv_mfma_f32_16x16x32_bf16 v[224:227], v[44:47], v[20:23], v[224:227]\nds_read_b64_tr_b16 v[94:95], v107 offset:54272\n"
      "v_mfma_f32_16x16x32_bf16 v[224:227], v[40:43], v[16:19], v[224:227]\nv_mfma_f32_16x16x32_bf16 v[228:231], v[48:51], v[16:19], v[228:231]\ns_sub_u32 s58, s58, 1\ns_cmp_lg_u32 s58, 0\n"
      "v_mfma_f32_16x16x32_bf16 v[228:231], v[52:55], v[20:23], v[228:231]\nv_mfma_f32_16x16x32_bf16 v[232:235], v[60:63], v[20:23], v[232:235]\ns_waitcnt lgkmcnt(0)\n"
      "v_mfma_f32_16x16x32_bf16 v[232:235], v[56:59], v[16:19], v[232:235]\nds_read_b128 v[16:19], v96 offset:45056\ns_cbranch_scc1 .Lgl14\n"
      "v_mfma_f32_16x16x32_bf16 v[124:127], v[64:67], v[0:3], v[124:127]\nds_read_b128 v[20:23], v97 offset:45056\nv_cmp_eq_u32 s[62:63], 0, v240\n"
      "v_mfma_f32_16x16x32_bf16 v[124:127], v[68:71], v[4:7], v[124:127]\nds_read_b128 v[24:27], v96 offset:47104\nv_mov_b32 v253, 262144\n"
      "v_mfma_f32_16x16x32_bf16 v[128:131], v[76:79], v[4:7], v[128:131]\nds_read_b128 v[28:31], v97 offset:47104\nv_mov_b32 v248, 162048\n"
      "v_mfma_f32_16x16x32_bf16 v[128:131], v[72:75], v[0:3], v[128:131]\nv_mov_b32 v241, 1\nv_cndmask_b32 v253, v253, v248, s[62:63]\n"
      "v_mfma_f32_16x16x32_bf16 v[132:135], v[80:83], v[0:3], v[132:135]\nv_lshrrev_b32 v112, 4, v240\nv_bfe_u32 v113, v240, 2, 2\n"
      "v_mfma_f32_16x16x32_bf16 v[132:135], v[84:87], v[4:7], v[132:135]\nv_and_b32 v114, 3, v240\nv_mul_u32_u24 v114, 0x80008, v114\n"
      "v_mfma_f32_16x16x32_bf16 v[136:139], v[92:95], v[4:7], v[136:139]\nv_add_u32 v244, v112, v113\nv_and_b32 v244, 3, v244\nv_mfma_f32_16x16x32_bf16 v[136:139], v[88:91], v[0:3], v[136:139]\n"
      "v_add_u32 v116, 22, v113\nv_cmp_gt_u32 s[62:63], 2, v113\nv_mfma_f32_16x16x32_bf16 v[152:155], v[88:91], v[8:11], v[152:155]\nds_read_b128 v[0:3], v96 offset:49152\n"
      "v_and_b32 v113, 60, v240\nv_mfma_f32_16x16x32_bf16 v[152:155], v[92:95], v[12:15], v[152:155]\nds_read_b128 v[4:7], v97 offset:49152\ns_mov_b32 s42, 0\n"
      "v_mfma_f32_16x16x32_bf16 v[148:151], v[84:87], v[12:15], v[148:151]\nv_cndmask_b32 v116, v116, v244, s[62:63]\nv_lshl_or_b32 v244, v244, 16, v116\n"
      "v_mfma_f32_16x16x32_bf16 v[148:151], v[80:83], v[8:11], v[148:151]\nv_lshl_add_u32 v244, v244, 5, v114\nv_lshl_add_u32 v252, v240, 4, s42\n"
      "v_mfma_f32_16x16x32_bf16 v[144:147], v[72:75], v[8:11], v[144:147]\ns_mov_b32 s42, 0x76325410\ns_mov_b32 s43, 0x21005410\n"
      "v_mfma_f32_16x16x32_bf16 v[144:147], v[76:79], v[12:15], v[144:147]\nv_lshrrev_b64 v[116:117], v113, s[42:43]\nv_and_b32 v116, 7, v116\n"
      "v_mfma_f32_16x16x32_bf16 v[140:143], v[68:71], v[12:15], v[140:143]\ns_waitcnt lgkmcnt(4)\nv_lshl_add_u32 v116, v112, 3, v116\n"
      "v_mfma_f32_16x16x32_bf16 v[140:143], v[64:67], v[8:11], v[140:143]\ns_mov_b32 s42, 0x54107632\ns_mov_b32 s43, 0x21107632\n"
      "v_mfma_f32_16x16x32_bf16 v[156:159], v[64:67], v[16:19], v[156:159]\nds_read_b128 v[8:11], v96 offset:51200\nv_lshrrev_b64 v[118:119], v113, s[42:43]\n"
      "v_mfma_f32_16x16x32_bf16 v[156:159], v[68:71], v[20:23], v[156:159]\nds_read_b128 v[12:15], v97 offset:51200\nv_and_b32 v118, 7, v118\n"
      "v_mfma_f32_16x16x32_bf16 v[160:163], v[76:79], v[20:23], v[160:163]\nv_lshl_add_u32 v118, v112, 3, v118\nv_lshl_or_b32 v245, v118, 16, v116\n"
      "v_mfma_f32_16x16x32_bf16 v[160:163], v[72:75], v[16:19], v[160:163]\nv_lshl_add_u32 v245, v245, 5, v114\nv_and_b32 v113, 1, v240\n"
      "v_mfma_f32_16x16x32_bf16 v[164:167], v[80:83], v[16:19], v[164:167]\nv_mul_u32_u24 v113, 0xaaaa, v113\nv_add_u32 v242, 0x4444, v113\n"
      "v_mfma_f32_16x16x32_bf16 v[164:167], v[84:87], v[20:23], v[164:167]\ns_lshl_b32 s42, s36, 4\ns_add_u32 s42, s42, 160640\nv_mfma_f32_16x16x32_bf16 v[168:171], v[92:95], v[20:23], v[168:171]\n"
      "s_waitcnt lgkmcnt(2)\nv_lshl_add_u32 v243, v112, 2, s42\nv_mfma_f32_16x16x32_bf16 v[168:171], v[88:91], v[16:19], v[168:171]\nv_and_b32 v112, 1, v112\nv_lshl_add_u32 v112, v112, 3, s24\n"
      "v_mfma_f32_16x16x32_bf16 v[184:187], v[88:91], v[24:27], v[184:187]\nds_read_b128 v[16:19], v96 offset:53248\ns_sub_u32 s42, s64, 1\n"
      "v_mfma_f32_16x16x32_bf16 v[184:187], v[92:95], v[28:31], v[184:187]\nds_read_b128 v[20:23], v97 offset:53248\nv_min_u32 v112, s42, v112\n"
      "v_mfma_f32_16x16x32_bf16 v[180:183], v[84:87], v[28:31], v[180:183]\nv_lshlrev_b32 v112, 3, v112\nv_and_b32 v113, 15, v240\n"
      "v_mfma_f32_16x16x32_bf16 v[180:183], v[80:83], v[24:27], v[180:183]\nv_lshl_add_u32 v250, v112, 1, v113\ns_mov_b32 s42, 136192\n"
      "v_mfma_f32_16x16x32_bf16 v[176:179], v[72:75], v[24:27], v[176:179]\nv_lshl_add_u32 v250, v250, 2, s42\nv_min_u32 v113, 12, v113\n"
      "v_mfma_f32_16x16x32_bf16 v[176:179], v[76:79], v[28:31], v[176:179]\ns_waitcnt vmcnt(0) lgkmcnt(0)\ns_lshl_b32 s42, s30, 7\n"
      "v_mfma_f32_16x16x32_bf16 v[172:175], v[68:71], v[28:31], v[172:175]\ns_barrier\nv_lshl_add_u32 v113, v113, 15, s42\nv_mfma_f32_16x16x32_bf16 v[172:175], v[64:67], v[24:27], v[172:175]\n"
      "ds_read_b64_tr_b16 v[32:33], v108 offset:0\nv_add_u32 v249, v113, v112\nv_mfma_f32_16x16x32_bf16 v[188:191], v[64:67], v[0:3], v[188:191]\nds_read_b64_tr_b16 v[34:35], v108 offset:1024\n"
      "v_mfma_f32_16x16x32_bf16 v[188:191], v[68:71], v[4:7], v[188:191]\nds_read_b64_tr_b16 v[36:37], v108 offset:8192\nv_mfma_f32_16x16x32_bf16 v[192:195], v[76:79], v[4:7], v[192:195]\n"
      "ds_read_b64_tr_b16 v[38:39], v108 offset:9216\nv_mfma_f32_16x16x32_bf16 v[192:195], v[72:75], v[0:3], v[192:195]\nds_read_b64_tr_b16 v[40:41], v109 offset:0\n"
      "v_mfma_f32_16x16x32_bf16 v[196:199], v[80:83], v[0:3], v[196:199]\nds_read_b64_tr_b16 v[42:43], v109 offset:1024\nv_mfma_f32_16x16x32_bf16 v[196:199], v[84:87], v[4:7], v[196:199]\n"
      "ds_read_b64_tr_b16 v[44:45], v109 offset:8192\nv_mfma_f32_16x16x32_bf16 v[200:203], v[92:95], v[4:7], v[200:203]\nds_read_b64_tr_b16 v[46:47], v109 offset:9216\n"
      "v_mfma_f32_16x16x32_bf16 v[200:203], v[88:91], v[0:3], v[200:203]\nds_read_b64_tr_b16 v[48:49], v110 offset:0\nv_mfma_f32_16x16x32_bf16 v[216:219], v[88:91], v[8:11], v[216:219]\n"
      "ds_read_b128 v[0:3], v102 offset:0\nv_mfma_f32_16x16x32_bf16 v[216:219], v[92:95], v[12:15], v[216:219]\nds_read_b128 v[4:7], v103 offset:0\n"
      "v_mfma_f32_16x16x32_bf16 v[212:215], v[84:87], v[12:15], v[212:215]\nds_read_b64_tr_b16 v[50:51], v110 offset:1024\nv_mfma_f32_16x16x32_bf16 v[212:215], v[80:83], v[8:11], v[212:215]\n"
      "ds_read_b64_tr_b16 v[52:53], v110 offset:8192\nv_mfma_f32_16x16x32_bf16 v[208:211], v[72:75], v[8:11], v[208:211]\nds_read_b64_tr_b16 v[54:55], v110 offset:9216\n"
      "v_mfma_f32_16x16x32_bf16 v[208:211], v[76:79], v[12:15], v[208:211]\nds_read_b64_tr_b16 v[56:57], v111 offset:0\nv_mfma_f32_16x16x32_bf16 v[204:207], v[68:71], v[12:15], v[204:207]\n"
      "ds_read_b64_tr_b16 v[58:59], v111 offset:1024\nv_mfma_f32_16x16x32_bf16 v[204:207], v[64:67], v[8:11], v[204:207]\nds_read_b64_tr_b16 v[60:61], v111 offset:8192\n"
      "v_mfma_f32_16x16x32_bf16 v[220:223], v[64:67], v[16:19], v[220:223]\nds_read_b128 v[8:11], v122 offset:0\nv_mfma_f32_16x16x32_bf16 v[220:223], v[68:71], v[20:23], v[220:223]\n"
      "ds_read_b128 v[12:15], v123 offset:0\nv_mfma_f32_16x16x32_bf16 v[224:227], v[76:79], v[20:23], v[224:227]\nds_read_b64_tr_b16 v[62:63], v111 offset:9216\n"
      "v_mfma_f32_16x16x32_bf16 v[224:227], v[72:75], v[16:19], v[224:227]\nv_mfma_f32_16x16x32_bf16 v[228:231], v[80:83], v[16:19], v[228:231]\n"
      "v_mfma_f32_16x16x32_bf16 v[228:231], v[84:87], v[20:23], v[228:231]\nv_mfma_f32_16x16x32_bf16 v[232:235], v[92:95], v[20:23], v[232:235]\ns_waitcnt lgkmcnt(0)\n"
      "v_mfma_f32_16x16x32_bf16 v[232:235], v[88:91], v[16:19], v[232:235]\nds_read_b128 v[16:19], v98 offset:0\nv_mfma_f32_16x16x32_bf16 v[124:127], v[32:35], v[0:3], v[124:127]\n"
      "ds_read_b128 v[20:23], v99 offset:0\nv_mfma_f32_16x16x32_bf16 v[124:127], v[36:39], v[4:7], v[124:127]\nds_read_b128 v[24:27], v98 offset:2048\n"
      "v_mfma_f32_16x16x32_bf16 v[128:131], v[44:47], v[4:7], v[128:131]\nds_read_b128 v[28:31], v99 offset:2048\nv_mfma_f32_16x16x32_bf16 v[128:131], v[40:43], v[0:3], v[128:131]\n"
      "v_mfma_f32_16x16x32_bf16 v[132:135], v[48:51], v[0:3], v[132:135]\nv_mfma_f32_16x16x32_bf16 v[132:135], v[52:55], v[4:7], v[132:135]\n"
      "v_mfma_f32_16x16x32_bf16 v[136:139], v[60:63], v[4:7], v[136:139]\nv_mfma_f32_16x16x32_bf16 v[136:139], v[56:59], v[0:3], v[136:139]\n"
      "v_mfma_f32_16x16x32_bf16 v[152:155], v[56:59], v[8:11], v[152:155]\nds_read_b128 v[0:3], v98 offset:4096\nv_mfma_f32_16x16x32_bf16 v[152:155], v[60:63], v[12:15], v[152:155]\n"
      "ds_read_b128 v[4:7], v99 offset:4096\nv_mfma_f32_16x16x32_bf16 v[148:151], v[52:55], v[12:15], v[148:151]\nv_mfma_f32_16x16x32_bf16 v[148:151], v[48:51], v[8:11], v[148:151]\n"
      "v_mfma_f32_16x16x32_bf16 v[144:147], v[40:43], v[8:11], v[144:147]\nv_mfma_f32_16x16x32_bf16 v[144:147], v[44:47], v[12:15], v[144:147]\n"
      "v_mfma_f32_16x16x32_bf16 v[140:143], v[36:39], v[12:15], v[140:143]\ns_waitcnt lgkmcnt(4)\nv_mfma_f32_16x16x32_bf16 v[140:143], v[32:35], v[8:11], v[140:143]\n"
      "v_mfma_f32_16x16x32_bf16 v[156:159], v[32:35], v[16:19], v[156:159]\nds_read_b128 v[8:11], v98 offset:6144\nv_mfma_f32_16x16x32_bf16 v[156:159], v[36:39], v[20:23], v[156:159]\n"
      "ds_read_b128 v[12:15], v99 offset:6144\nv_mfma_f32_16x16x32_bf16 v[160:163], v[44:47], v[20:23], v[160:163]\nv_mfma_f32_16x16x32_bf16 v[160:163], v[40:43], v[16:19], v[160:163]\n"
      "v_mfma_f32_16x16x32_bf16 v[164:167], v[48:51], v[16:19], v[164:167]\nv_mfma_f32_16x16x32_bf16 v[164:167], v[52:55], v[20:23], v[164:167]\n"
      "v_mfma_f32_16x16x32_bf16 v[168:171], v[60:63], v[20:23], v[168:171]\ns_waitcnt lgkmcnt(2)\nv_mfma_f32_16x16x32_bf16 v[168:171], v[56:59], v[16:19], v[168:171]\n"
      "v_mfma_f32_16x16x32_bf16 v[184:187], v[56:59], v[24:27], v[184:187]\nds_read_b128 v[16:19], v98 offset:8192\nv_mfma_f32_16x16x32_bf16 v[184:187], v[60:63], v[28:31], v[184:187]\n"
      "ds_read_b128 v[20:23], v99 offset:8192\nv_mfma_f32_16x16x32_bf16 v[180:183], v[52:55], v[28:31], v[180:183]\nv_mfma_f32_16x16x32_bf16 v[180:183], v[48:51], v[24:27], v[180:183]\n"
      "v_mfma_f32_16x16x32_bf16 v[176:179], v[40:43], v[24:27], v[176:179]\nv_mfma_f32_16x16x32_bf16 v[176:179], v[44:47], v[28:31], v[176:179]\ns_waitcnt vmcnt(0) lgkmcnt(0)\n"
      "v_mfma_f32_16x16x32_bf16 v[172:175], v[36:39], v[28:31], v[172:175]\ns_barrier\nv_mfma_f32_16x16x32_bf16 v[172:175], v[32:35], v[24:27], v[172:175]\nds_read_b128 v[72:75], v252 offset:0\n"
      "v_lshl_add_u32 v246, v240, 1, v248\nv_mfma_f32_16x16x32_bf16 v[188:191], v[32:35], v[0:3], v[188:191]\nds_read_b128 v[24:27], v236 offset:512\nds_read_u16 v251, v246 offset:64\n"
      "v_mfma_f32_16x16x32_bf16 v[188:191], v[36:39], v[4:7], v[188:191]\nds_read_b128 v[28:31], v236 offset:576\nds_read_b32 v247, v248 offset:24\n"
      "v_mfma_f32_16x16x32_bf16 v[192:195], v[44:47], v[4:7], v[192:195]\nds_read_b128 v[76:79], v252 offset:1024\nv_and_b32 v246, 48, v240\n"
      "v_mfma_f32_16x16x32_bf16 v[192:195], v[40:43], v[0:3], v[192:195]\nds_read_b128 v[80:83], v252 offset:2048\ns_mov_b32 s42, 42240\n"
      "v_mfma_f32_16x16x32_bf16 v[196:199], v[48:51], v[0:3], v[196:199]\nds_read_b128 v[84:87], v252 offset:3072\nv_lshl_add_u32 v246, v246, 2, s42\n"
      "v_mfma_f32_16x16x32_bf16 v[196:199], v[52:55], v[4:7], v[196:199]\nds_read_b128 v[88:91], v252 offset:4096\ns_mov_b32 s58, 0xbdd2d3e7\n"
      "v_mfma_f32_16x16x32_bf16 v[200:203], v[60:63], v[4:7], v[200:203]\nds_read_b128 v[92:95], v252 offset:5120\ns_waitcnt lgkmcnt(4)\n"
      "v_mfma_f32_16x16x32_bf16 v[200:203], v[56:59], v[0:3], v[200:203]\nds_read_b128 v[96:99], v252 offset:6144\nv_readfirstlane_b32 s39, v247\n"
      "v_mfma_f32_16x16x32_bf16 v[216:219], v[56:59], v[8:11], v[216:219]\nds_read_b128 v[0:3], v236 offset:0\nv_readlane_b32 s55, v251, 0\n"
      "v_mfma_f32_16x16x32_bf16 v[216:219], v[60:63], v[12:15], v[216:219]\nds_read_b128 v[4:7], v236 offset:64\ns_setprio 3\nv_mfma_f32_16x16x32_bf16 v[212:215], v[52:55], v[12:15], v[212:215]\n"
      "ds_read_b128 v[100:103], v252 offset:7168\ns_xor_b32 s40, s39, 0xa5c30f96\nv_mfma_f32_16x16x32_bf16 v[212:215], v[48:51], v[8:11], v[212:215]\nds_read_b128 v[104:107], v252 offset:8192\n"
      "s_mul_i32 s41, s39, 0x9E3779B1\nv_mfma_f32_16x16x32_bf16 v[208:211], v[40:43], v[8:11], v[208:211]\nds_read_b128 v[108:111], v252 offset:9216\ns_lshr_b32 s41, s41, 24\n"
      "v_mfma_f32_16x16x32_bf16 v[208:211], v[44:47], v[12:15], v[208:211]\nds_read_b128 v[64:67], v237 offset:512\nv_mfma_f32_16x16x32_bf16 v[204:207], v[36:39], v[12:15], v[204:207]\n"
      "ds_read_b128 v[68:71], v237 offset:576\nv_mfma_f32_16x16x32_bf16 v[204:207], v[32:35], v[8:11], v[204:207]\nds_read_b128 v[112:115], v252 offset:10240\n"
      "v_mfma_f32_16x16x32_bf16 v[220:223], v[32:35], v[16:19], v[220:223]\nds_read_b128 v[8:11], v237 offset:0\nv_mfma_f32_16x16x32_bf16 v[220:223], v[36:39], v[20:23], v[220:223]\n"
      "ds_read_b128 v[12:15], v237 offset:64\nv_mfma_f32_16x16x32_bf16 v[224:227], v[44:47], v[20:23], v[224:227]\nds_read_b128 v[116:119], v252 offset:11264\n"
      "v_mfma_f32_16x16x32_bf16 v[224:227], v[40:43], v[16:19], v[224:227]\nds_read_b128 v[120:123], v252 offset:12288\n"
      "v_add_u32_sdwa v239, s49, v245 dst_sel:DWORD dst_unused:UNUSED_PAD src0_sel:DWORD src1_sel:WORD_1\nv_mfma_f32_16x16x32_bf16 v[228:231], v[48:51], v[16:19], v[228:231]\n"
      "v_add_u32_sdwa v236, s49, v244 dst_sel:DWORD dst_unused:UNUSED_PAD src0_sel:DWORD src1_sel:WORD_0\nv_mfma_f32_16x16x32_bf16 v[228:231], v[52:55], v[20:23], v[228:231]\n"
      "v_add_u32_sdwa v237, s49, v244 dst_sel:DWORD dst_unused:UNUSED_PAD src0_sel:DWORD src1_sel:WORD_1\nv_mfma_f32_16x16x32_bf16 v[232:235], v[60:63], v[20:23], v[232:235]\n"
      "v_add_u32_sdwa v238, s49, v245 dst_sel:DWORD dst_unused:UNUSED_PAD src0_sel:DWORD src1_sel:WORD_0\ns_waitcnt lgkmcnt(0)\nv_mfma_f32_16x16x32_bf16 v[232:235], v[56:59], v[16:19], v[232:235]\n"
      "v_pk_mul_f32 v[44:45], v[124:125], v[124:125]\nv_pk_mul_f32 v[46:47], v[126:127], v[126:127]\nv_pk_mul_f32 v[48:49], v[128:129], v[128:129]\nv_pk_mul_f32 v[50:51], v[130:131], v[130:131]\n"
      "v_pk_fma_f32 v[44:45], v[44:45], s[58:59], s[58:59] op_sel:[0,0,1] op_sel_hi:[1,0,1]\nv_pk_fma_f32 v[46:47], v[46:47], s[58:59], s[58:59] op_sel:[0,0,1] op_sel_hi:[1,0,1]\n"
      "v_pk_fma_f32 v[48:49], v[48:49], s[58:59], s[58:59] op_sel:[0,0,1] op_sel_hi:[1,0,1]\nv_pk_fma_f32 v[50:51], v[50:51], s[58:59], s[58:59] op_sel:[0,0,1] op_sel_hi:[1,0,1]\n"
      "v_pk_fma_f32 v[44:45], v[44:45], v[124:125], s[60:61] op_sel_hi:[1,1,0]\nv_pk_fma_f32 v[46:47], v[46:47], v[126:127], s[60:61] op_sel_hi:[1,1,0]\n"
      "v_pk_fma_f32 v[48:49], v[48:49], v[128:129], s[60:61] op_sel_hi:[1,1,0]\nv_pk_fma_f32 v[50:51], v[50:51], v[130:131], s[60:61] op_sel_hi:[1,1,0]\nv_exp_f32 v44, v44 clamp\n"
      "v_pk_mul_f32 v[52:53], v[132:133], v[132:133]\nv_exp_f32 v45, v45 clamp\nv_pk_mul_f32 v[54:55], v[134:135], v[134:135]\nv_pk_mul_f32 v[56:57], v[136:137], v[136:137]\n"
      "v_exp_f32 v46, v46 clamp\nv_pk_mul_f32 v[58:59], v[138:139], v[138:139]\nv_exp_f32 v47, v47 clamp\nv_pk_fma_f32 v[52:53], v[52:53], s[58:59], s[58:59] op_sel:[0,0,1] op_sel_hi:[1,0,1]\n"
      "v_pk_fma_f32 v[54:55], v[54:55], s[58:59], s[58:59] op_sel:[0,0,1] op_sel_hi:[1,0,1]\nv_exp_f32 v48, v48 clamp\n"
      "v_pk_fma_f32 v[56:57], v[56:57], s[58:59], s[58:59] op_sel:[0,0,1] op_sel_hi:[1,0,1]\nv_exp_f32 v49, v49 clamp\n"
      "v_pk_fma_f32 v[58:59], v[58:59], s[58:59], s[58:59] op_sel:[0,0,1] op_sel_hi:[1,0,1]\nv_pk_fma_f32 v[52:53], v[52:53], v[132:133], s[60:61] op_sel_hi:[1,1,0]\nv_exp_f32 v50, v50 clamp\n"
      "v_pk_fma_f32 v[54:55], v[54:55], v[134:135], s[60:61] op_sel_hi:[1,1,0]\nv_exp_f32 v51, v51 clamp\nv_pk_fma_f32 v[56:57], v[56:57], v[136:137], s[60:61] op_sel_hi:[1,1,0]\n"
      "v_pk_fma_f32 v[58:59], v[58:59], v[138:139], s[60:61] op_sel_hi:[1,1,0]\nv_exp_f32 v52, v52 clamp\nv_pk_fma_f32 v[46:47], v[46:47], v[2:3], v[26:27]\n"
      "v_pk_fma_f32 v[44:45], v[44:45], v[0:1], v[24:25]\nv_pk_fma_f32 v[48:49], v[48:49], v[4:5], v[28:29]\nv_exp_f32 v53, v53 clamp\nv_pk_fma_f32 v[50:51], v[50:51], v[6:7], v[30:31]\n"
      "v_pk_mul_f32 v[124:125], v[124:125], v[46:47]\nv_pk_mul_f32 v[128:129], v[128:129], v[50:51]\nv_exp_f32 v54, v54 clamp\nv_pk_fma_f32 v[124:125], v[126:127], v[44:45], v[124:125]\n"
      "v_pk_fma_f32 v[128:129], v[130:131], v[48:49], v[128:129]\nv_pk_mul_f32 v[44:45], v[44:45], v[46:47]\nv_exp_f32 v55, v55 clamp\nv_pk_mul_f32 v[48:49], v[48:49], v[50:51]\n"
      "v_pk_mul_f32 v[46:47], v[44:45], v[48:49]\nv_rcp_f32 v46, v46\nv_pk_mul_f32 v[124:125], v[124:125], v[48:49]\nv_rcp_f32 v47, v47\nv_pk_fma_f32 v[124:125], v[128:129], v[44:45], v[124:125]\n"
      "v_pk_mul_f32 v[32:33], v[124:125], v[46:47]\nv_pk_mul_f32 v[16:17], v[140:141], v[140:141]\nv_pk_mul_f32 v[18:19], v[142:143], v[142:143]\nv_exp_f32 v56, v56 clamp\n"
      "v_pk_mul_f32 v[20:21], v[144:145], v[144:145]\nv_pk_mul_f32 v[22:23], v[146:147], v[146:147]\nv_exp_f32 v57, v57 clamp\n"
      "v_pk_fma_f32 v[16:17], v[16:17], s[58:59], s[58:59] op_sel:[0,0,1] op_sel_hi:[1,0,1]\nv_pk_fma_f32 v[18:19], v[18:19], s[58:59], s[58:59] op_sel:[0,0,1] op_sel_hi:[1,0,1]\n"
      "v_pk_fma_f32 v[20:21], v[20:21], s[58:59], s[58:59] op_sel:[0,0,1] op_sel_hi:[1,0,1]\nv_exp_f32 v58, v58 clamp\n"
      "v_pk_fma_f32 v[22:23], v[22:23], s[58:59], s[58:59] op_sel:[0,0,1] op_sel_hi:[1,0,1]\nv_pk_fma_f32 v[16:17], v[16:17], v[140:141], s[60:61] op_sel_hi:[1,1,0]\n"
      "v_pk_fma_f32 v[18:19], v[18:19], v[142:143], s[60:61] op_sel_hi:[1,1,0]\nv_exp_f32 v59, v59 clamp\nv_pk_fma_f32 v[20:21], v[20:21], v[144:145], s[60:61] op_sel_hi:[1,1,0]\n"
      "v_pk_fma_f32 v[22:23], v[22:23], v[146:147], s[60:61] op_sel_hi:[1,1,0]\nv_exp_f32 v16, v16 clamp\nv_pk_fma_f32 v[54:55], v[54:55], v[10:11], v[66:67]\n"
      "v_pk_fma_f32 v[52:53], v[52:53], v[8:9], v[64:65]\nv_pk_fma_f32 v[56:57], v[56:57], v[12:13], v[68:69]\nv_exp_f32 v17, v17 clamp\nv_pk_fma_f32 v[58:59], v[58:59], v[14:15], v[70:71]\n"
      "v_pk_mul_f32 v[132:133], v[132:133], v[54:55]\nv_pk_mul_f32 v[136:137], v[136:137], v[58:59]\nv_exp_f32 v18, v18 clamp\nv_pk_fma_f32 v[132:133], v[134:135], v[52:53], v[132:133]\n"
      "v_pk_fma_f32 v[136:137], v[138:139], v[56:57], v[136:137]\nv_pk_mul_f32 v[52:53], v[52:53], v[54:55]\nv_exp_f32 v19, v19 clamp\nv_pk_mul_f32 v[56:57], v[56:57], v[58:59]\n"
      "v_pk_mul_f32 v[54:55], v[52:53], v[56:57]\nv_rcp_f32 v54, v54\nv_pk_mul_f32 v[132:133], v[132:133], v[56:57]\nv_rcp_f32 v55, v55\nv_pk_fma_f32 v[132:133], v[136:137], v[52:53], v[132:133]\n"
      "v_pk_fma_f32 v[32:33], v[132:133], v[54:55], v[32:33]\nv_add_f32 v42, v32, v33\nv_pk_mul_f32 v[44:45], v[148:149], v[148:149]\nv_pk_mul_f32 v[46:47], v[150:151], v[150:151]\n"
      "v_exp_f32 v20, v20 clamp\nv_pk_mul_f32 v[48:49], v[152:153], v[152:153]\nv_pk_mul_f32 v[50:51], v[154:155], v[154:155]\nv_exp_f32 v21, v21 clamp\n"
      "v_pk_fma_f32 v[44:45], v[44:45], s[58:59], s[58:59] op_sel:[0,0,1] op_sel_hi:[1,0,1]\nv_pk_fma_f32 v[46:47], v[46:47], s[58:59], s[58:59] op_sel:[0,0,1] op_sel_hi:[1,0,1]\n"
      "v_pk_fma_f32 v[48:49], v[48:49], s[58:59], s[58:59] op_sel:[0,0,1] op_sel_hi:[1,0,1]\nv_exp_f32 v22, v22 clamp\n"
      "v_pk_fma_f32 v[50:51], v[50:51], s[58:59], s[58:59] op_sel:[0,0,1] op_sel_hi:[1,0,1]\nv_pk_fma_f32 v[44:45], v[44:45], v[148:149], s[60:61] op_sel_hi:[1,1,0]\n"
      "v_pk_fma_f32 v[46:47], v[46:47], v[150:151], s[60:61] op_sel_hi:[1,1,0]\nv_exp_f32 v23, v23 clamp\nv_pk_fma_f32 v[48:49], v[48:49], v[152:153], s[60:61] op_sel_hi:[1,1,0]\n"
      "v_pk_fma_f32 v[50:51], v[50:51], v[154:155], s[60:61] op_sel_hi:[1,1,0]\nv_exp_f32 v44, v44 clamp\nv_pk_fma_f32 v[18:19], v[18:19], v[2:3], v[26:27]\n"
      "v_pk_fma_f32 v[16:17], v[16:17], v[0:1], v[24:25]\nv_pk_fma_f32 v[20:21], v[20:21], v[4:5], v[28:29]\nv_exp_f32 v45, v45 clamp\nv_pk_fma_f32 v[22:23], v[22:23], v[6:7], v[30:31]\n"
      "v_pk_mul_f32 v[140:141], v[140:141], v[18:19]\nv_pk_mul_f32 v[144:145], v[144:145], v[22:23]\nv_exp_f32 v46, v46 clamp\nv_pk_fma_f32 v[140:141], v[142:143], v[16:17], v[140:141]\n"
      "v_pk_fma_f32 v[144:145], v[146:147], v[20:21], v[144:145]\nv_pk_mul_f32 v[16:17], v[16:17], v[18:19]\nv_exp_f32 v47, v47 clamp\nv_pk_mul_f32 v[20:21], v[20:21], v[22:23]\n"
      "v_pk_mul_f32 v[18:19], v[16:17], v[20:21]\nv_rcp_f32 v18, v18\nv_pk_mul_f32 v[140:141], v[140:141], v[20:21]\nv_rcp_f32 v19, v19\nv_pk_fma_f32 v[140:141], v[144:145], v[16:17], v[140:141]\n"
      "v_pk_mul_f32 v[32:33], v[140:141], v[18:19]\nv_pk_mul_f32 v[52:53], v[156:157], v[156:157]\nv_pk_mul_f32 v[54:55], v[158:159], v[158:159]\nv_exp_f32 v48, v48 clamp\n"
      "v_pk_mul_f32 v[56:57], v[160:161], v[160:161]\nv_pk_mul_f32 v[58:59], v[162:163], v[162:163]\nv_exp_f32 v49, v49 clamp\n"
      "v_pk_fma_f32 v[52:53], v[52:53], s[58:59], s[58:59] op_sel:[0,0,1] op_sel_hi:[1,0,1]\nv_pk_fma_f32 v[54:55], v[54:55], s[58:59], s[58:59] op_sel:[0,0,1] op_sel_hi:[1,0,1]\n"
      "v_pk_fma_f32 v[56:57], v[56:57], s[58:59], s[58:59] op_sel:[0,0,1] op_sel_hi:[1,0,1]\nv_exp_f32 v50, v50 clamp\n"
      "v_pk_fma_f32 v[58:59], v[58:59], s[58:59], s[58:59] op_sel:[0,0,1] op_sel_hi:[1,0,1]\nv_pk_fma_f32 v[52:53], v[52:53], v[156:157], s[60:61] op_sel_hi:[1,1,0]\n"
      "v_pk_fma_f32 v[54:55], v[54:55], v[158:159], s[60:61] op_sel_hi:[1,1,0]\nv_exp_f32 v51, v51 clamp\nv_pk_fma_f32 v[56:57], v[56:57], v[160:161], s[60:61] op_sel_hi:[1,1,0]\n"
      "v_pk_fma_f32 v[58:59], v[58:59], v[162:163], s[60:61] op_sel_hi:[1,1,0]\nv_exp_f32 v52, v52 clamp\nv_pk_fma_f32 v[46:47], v[46:47], v[10:11], v[66:67]\n"
      "v_pk_fma_f32 v[44:45], v[44:45], v[8:9], v[64:65]\nv_pk_fma_f32 v[48:49], v[48:49], v[12:13], v[68:69]\nv_exp_f32 v53, v53 clamp\nv_pk_fma_f32 v[50:51], v[50:51], v[14:15], v[70:71]\n"
      "v_pk_mul_f32 v[148:149], v[148:149], v[46:47]\nv_pk_mul_f32 v[152:153], v[152:153], v[50:51]\nv_exp_f32 v54, v54 clamp\nv_pk_fma_f32 v[148:149], v[150:151], v[44:45], v[148:149]\n"
      "v_pk_fma_f32 v[152:153], v[154:155], v[48:49], v[152:153]\nv_pk_mul_f32 v[44:45], v[44:45], v[46:47]\nv_exp_f32 v55, v55 clamp\nv_pk_mul_f32 v[48:49], v[48:49], v[50:51]\n"
      "v_pk_mul_f32 v[46:47], v[44:45], v[48:49]\nv_rcp_f32 v46, v46\nv_pk_mul_f32 v[148:149], v[148:149], v[48:49]\nv_rcp_f32 v47, v47\nv_pk_fma_f32 v[148:149], v[152:153], v[44:45], v[148:149]\n"
      "v_pk_fma_f32 v[32:33], v[148:149], v[46:47], v[32:33]\nv_add_f32 v41, v32, v33\nv_pk_mul_f32 v[16:17], v[164:165], v[164:165]\nv_pk_mul_f32 v[18:19], v[166:167], v[166:167]\n"
      "v_exp_f32 v56, v56 clamp\nv_pk_mul_f32 v[20:21], v[168:169], v[168:169]\nv_pk_mul_f32 v[22:23], v[170:171], v[170:171]\nv_exp_f32 v57, v57 clamp\n"
      "v_pk_fma_f32 v[16:17], v[16:17], s[58:59], s[58:59] op_sel:[0,0,1] op_sel_hi:[1,0,1]\nv_pk_fma_f32 v[18:19], v[18:19], s[58:59], s[58:59] op_sel:[0,0,1] op_sel_hi:[1,0,1]\n"
      "v_pk_fma_f32 v[20:21], v[20:21], s[58:59], s[58:59] op_sel:[0,0,1] op_sel_hi:[1,0,1]\nv_exp_f32 v58, v58 clamp\n"
      "v_pk_fma_f32 v[22:23], v[22:23], s[58:59], s[58:59] op_sel:[0,0,1] op_sel_hi:[1,0,1]\nv_pk_fma_f32 v[16:17], v[16:17], v[164:165], s[60:61] op_sel_hi:[1,1,0]\n"
      "v_pk_fma_f32 v[18:19], v[18:19], v[166:167], s[60:61] op_sel_hi:[1,1,0]\nv_exp_f32 v59, v59 clamp\nv_pk_fma_f32 v[20:21], v[20:21], v[168:169], s[60:61] op_sel_hi:[1,1,0]\n"
      "v_pk_fma_f32 v[22:23], v[22:23], v[170:171], s[60:61] op_sel_hi:[1,1,0]\nv_exp_f32 v16, v16 clamp\nv_pk_fma_f32 v[54:55], v[54:55], v[2:3], v[26:27]\n"
      "v_pk_fma_f32 v[52:53], v[52:53], v[0:1], v[24:25]\nv_pk_fma_f32 v[56:57], v[56:57], v[4:5], v[28:29]\nv_exp_f32 v17, v17 clamp\nv_pk_fma_f32 v[58:59], v[58:59], v[6:7], v[30:31]\n"
      "v_pk_mul_f32 v[156:157], v[156:157], v[54:55]\nv_pk_mul_f32 v[160:161], v[160:161], v[58:59]\nv_exp_f32 v18, v18 clamp\nv_pk_fma_f32 v[156:157], v[158:159], v[52:53], v[156:157]\n"
      "v_pk_fma_f32 v[160:161], v[162:163], v[56:57], v[160:161]\nv_pk_mul_f32 v[52:53], v[52:53], v[54:55]\nv_exp_f32 v19, v19 clamp\nv_pk_mul_f32 v[56:57], v[56:57], v[58:59]\n"
      "v_pk_mul_f32 v[54:55], v[52:53], v[56:57]\nv_rcp_f32 v54, v54\nv_pk_mul_f32 v[156:157], v[156:157], v[56:57]\nv_rcp_f32 v55, v55\nv_pk_fma_f32 v[156:157], v[160:161], v[52:53], v[156:157]\n"
      "v_pk_mul_f32 v[32:33], v[156:157], v[54:55]\nv_pk_mul_f32 v[44:45], v[172:173], v[172:173]\nv_pk_mul_f32 v[46:47], v[174:175], v[174:175]\nv_exp_f32 v20, v20 clamp\n"
      "v_pk_mul_f32 v[48:49], v[176:177], v[176:177]\nv_pk_mul_f32 v[50:51], v[178:179], v[178:179]\nv_exp_f32 v21, v21 clamp\n"
      "v_pk_fma_f32 v[44:45], v[44:45], s[58:59], s[58:59] op_sel:[0,0,1] op_sel_hi:[1,0,1]\nv_pk_fma_f32 v[46:47], v[46:47], s[58:59], s[58:59] op_sel:[0,0,1] op_sel_hi:[1,0,1]\n"
      "v_pk_fma_f32 v[48:49], v[48:49], s[58:59], s[58:59] op_sel:[0,0,1] op_sel_hi:[1,0,1]\nv_exp_f32 v22, v22 clamp\n"
      "v_pk_fma_f32 v[50:51], v[50:51], s[58:59], s[58:59] op_sel:[0,0,1] op_sel_hi:[1,0,1]\nv_pk_fma_f32 v[44:45], v[44:45], v[172:173], s[60:61] op_sel_hi:[1,1,0]\n"
      "v_pk_fma_f32 v[46:47], v[46:47], v[174:175], s[60:61] op_sel_hi:[1,1,0]\nv_exp_f32 v23, v23 clamp\nv_pk_fma_f32 v[48:49], v[48:49], v[176:177], s[60:61] op_sel_hi:[1,1,0]\n"
      "v_pk_fma_f32 v[50:51], v[50:51], v[178:179], s[60:61] op_sel_hi:[1,1,0]\nv_exp_f32 v44, v44 clamp\nv_pk_fma_f32 v[18:19], v[18:19], v[10:11], v[66:67]\n"
      "v_pk_fma_f32 v[16:17], v[16:17], v[8:9], v[64:65]\nv_pk_fma_f32 v[20:21], v[20:21], v[12:13], v[68:69]\nv_exp_f32 v45, v45 clamp\nv_pk_fma_f32 v[22:23], v[22:23], v[14:15], v[70:71]\n"
      "v_pk_mul_f32 v[164:165], v[164:165], v[18:19]\nv_pk_mul_f32 v[168:169], v[168:169], v[22:23]\nv_exp_f32 v46, v46 clamp\nv_pk_fma_f32 v[164:165], v[166:167], v[16:17], v[164:165]\n"
      "v_pk_fma_f32 v[168:169], v[170:171], v[20:21], v[168:169]\nv_pk_mul_f32 v[16:17], v[16:17], v[18:19]\nv_exp_f32 v47, v47 clamp\nv_pk_mul_f32 v[20:21], v[20:21], v[22:23]\n"
      "v_pk_mul_f32 v[18:19], v[16:17], v[20:21]\nv_rcp_f32 v18, v18\nv_pk_mul_f32 v[164:165], v[164:165], v[20:21]\nv_rcp_f32 v19, v19\nv_pk_fma_f32 v[164:165], v[168:169], v[16:17], v[164:165]\n"
      "v_pk_fma_f32 v[32:33], v[164:165], v[18:19], v[32:33]\nv_add_f32 v36, v32, v33\nv_pk_mul_f32 v[52:53], v[180:181], v[180:181]\nv_pk_mul_f32 v[54:55], v[182:183], v[182:183]\n"
      "v_exp_f32 v48, v48 clamp\nv_pk_mul_f32 v[56:57], v[184:185], v[184:185]\nv_pk_mul_f32 v[58:59], v[186:187], v[186:187]\nv_exp_f32 v49, v49 clamp\n"
      "v_pk_fma_f32 v[52:53], v[52:53], s[58:59], s[58:59] op_sel:[0,0,1] op_sel_hi:[1,0,1]\nv_pk_fma_f32 v[54:55], v[54:55], s[58:59], s[58:59] op_sel:[0,0,1] op_sel_hi:[1,0,1]\n"
      "v_pk_fma_f32 v[56:57], v[56:57], s[58:59], s[58:59] op_sel:[0,0,1] op_sel_hi:[1,0,1]\nv_exp_f32 v50, v50 clamp\n"
      "v_pk_fma_f32 v[58:59], v[58:59], s[58:59], s[58:59] op_sel:[0,0,1] op_sel_hi:[1,0,1]\nv_pk_fma_f32 v[52:53], v[52:53], v[180:181], s[60:61] op_sel_hi:[1,1,0]\n"
      "v_pk_fma_f32 v[54:55], v[54:55], v[182:183], s[60:61] op_sel_hi:[1,1,0]\nv_exp_f32 v51, v51 clamp\nv_pk_fma_f32 v[56:57], v[56:57], v[184:185], s[60:61] op_sel_hi:[1,1,0]\n"
      "v_pk_fma_f32 v[58:59], v[58:59], v[186:187], s[60:61] op_sel_hi:[1,1,0]\nv_exp_f32 v52, v52 clamp\nv_pk_fma_f32 v[46:47], v[46:47], v[2:3], v[26:27]\n"
      "v_pk_fma_f32 v[44:45], v[44:45], v[0:1], v[24:25]\nv_pk_fma_f32 v[48:49], v[48:49], v[4:5], v[28:29]\nv_exp_f32 v53, v53 clamp\nv_pk_fma_f32 v[50:51], v[50:51], v[6:7], v[30:31]\n"
      "v_pk_mul_f32 v[172:173], v[172:173], v[46:47]\nv_pk_mul_f32 v[176:177], v[176:177], v[50:51]\nv_exp_f32 v54, v54 clamp\nv_pk_fma_f32 v[172:173], v[174:175], v[44:45], v[172:173]\n"
      "v_pk_fma_f32 v[176:177], v[178:179], v[48:49], v[176:177]\nv_pk_mul_f32 v[44:45], v[44:45], v[46:47]\nv_exp_f32 v55, v55 clamp\nv_pk_mul_f32 v[48:49], v[48:49], v[50:51]\n"
      "v_pk_mul_f32 v[46:47], v[44:45], v[48:49]\nv_rcp_f32 v46, v46\nv_pk_mul_f32 v[172:173], v[172:173], v[48:49]\nv_rcp_f32 v47, v47\nv_pk_fma_f32 v[172:173], v[176:177], v[44:45], v[172:173]\n"
      "v_pk_mul_f32 v[32:33], v[172:173], v[46:47]\nv_pk_mul_f32 v[16:17], v[188:189], v[188:189]\nv_pk_mul_f32 v[18:19], v[190:191], v[190:191]\nv_exp_f32 v56, v56 clamp\n"
      "v_pk_mul_f32 v[20:21], v[192:193], v[192:193]\nv_pk_mul_f32 v[22:23], v[194:195], v[194:195]\nv_exp_f32 v57, v57 clamp\n"
      "v_pk_fma_f32 v[16:17], v[16:17], s[58:59], s[58:59] op_sel:[0,0,1] op_sel_hi:[1,0,1]\nv_pk_fma_f32 v[18:19], v[18:19], s[58:59], s[58:59] op_sel:[0,0,1] op_sel_hi:[1,0,1]\n"
      "v_pk_fma_f32 v[20:21], v[20:21], s[58:59], s[58:59] op_sel:[0,0,1] op_sel_hi:[1,0,1]\nv_exp_f32 v58, v58 clamp\n"
      "v_pk_fma_f32 v[22:23], v[22:23], s[58:59], s[58:59] op_sel:[0,0,1] op_sel_hi:[1,0,1]\nv_pk_fma_f32 v[16:17], v[16:17], v[188:189], s[60:61] op_sel_hi:[1,1,0]\n"
      "v_pk_fma_f32 v[18:19], v[18:19], v[190:191], s[60:61] op_sel_hi:[1,1,0]\nv_exp_f32 v59, v59 clamp\nv_pk_fma_f32 v[20:21], v[20:21], v[192:193], s[60:61] op_sel_hi:[1,1,0]\n"
      "v_pk_fma_f32 v[22:23], v[22:23], v[194:195], s[60:61] op_sel_hi:[1,1,0]\nv_exp_f32 v16, v16 clamp\nv_pk_fma_f32 v[54:55], v[54:55], v[10:11], v[66:67]\n"
      "v_pk_fma_f32 v[52:53], v[52:53], v[8:9], v[64:65]\nv_pk_fma_f32 v[56:57], v[56:57], v[12:13], v[68:69]\nv_exp_f32 v17, v17 clamp\nv_pk_fma_f32 v[58:59], v[58:59], v[14:15], v[70:71]\n"
      "v_pk_mul_f32 v[180:181], v[180:181], v[54:55]\nv_pk_mul_f32 v[184:185], v[184:185], v[58:59]\nv_exp_f32 v18, v18 clamp\nv_pk_fma_f32 v[180:181], v[182:183], v[52:53], v[180:181]\n"
      "v_pk_fma_f32 v[184:185], v[186:187], v[56:57], v[184:185]\nv_pk_mul_f32 v[52:53], v[52:53], v[54:55]\nv_exp_f32 v19, v19 clamp\nv_pk_mul_f32 v[56:57], v[56:57], v[58:59]\n"
      "v_pk_mul_f32 v[54:55], v[52:53], v[56:57]\nv_rcp_f32 v54, v54\nv_pk_mul_f32 v[180:181], v[180:181], v[56:57]\nv_rcp_f32 v55, v55\nv_pk_fma_f32 v[180:181], v[184:185], v[52:53], v[180:181]\n"
      "v_pk_fma_f32 v[32:33], v[180:181], v[54:55], v[32:33]\nv_add_f32 v37, v32, v33\nv_pk_mul_f32 v[44:45], v[196:197], v[196:197]\nv_pk_mul_f32 v[46:47], v[198:199], v[198:199]\n"
      "v_exp_f32 v20, v20 clamp\nv_pk_mul_f32 v[48:49], v[200:201], v[200:201]\nv_pk_mul_f32 v[50:51], v[202:203], v[202:203]\nv_exp_f32 v21, v21 clamp\n"
      "v_pk_fma_f32 v[44:45], v[44:45], s[58:59], s[58:59] op_sel:[0,0,1] op_sel_hi:[1,0,1]\nv_pk_fma_f32 v[46:47], v[46:47], s[58:59], s[58:59] op_sel:[0,0,1] op_sel_hi:[1,0,1]\n"
      "v_pk_fma_f32 v[48:49], v[48:49], s[58:59], s[58:59] op_sel:[0,0,1] op_sel_hi:[1,0,1]\nv_exp_f32 v22, v22 clamp\n"
      "v_pk_fma_f32 v[50:51], v[50:51], s[58:59], s[58:59] op_sel:[0,0,1] op_sel_hi:[1,0,1]\nv_pk_fma_f32 v[44:45], v[44:45], v[196:197], s[60:61] op_sel_hi:[1,1,0]\n"
      "v_pk_fma_f32 v[46:47], v[46:47], v[198:199], s[60:61] op_sel_hi:[1,1,0]\nv_exp_f32 v23, v23 clamp\nv_pk_fma_f32 v[48:49], v[48:49], v[200:201], s[60:61] op_sel_hi:[1,1,0]\n"
      "v_pk_fma_f32 v[50:51], v[50:51], v[202:203], s[60:61] op_sel_hi:[1,1,0]\nv_exp_f32 v44, v44 clamp\nv_pk_fma_f32 v[18:19], v[18:19], v[2:3], v[26:27]\n"
      "v_pk_fma_f32 v[16:17], v[16:17], v[0:1], v[24:25]\nv_pk_fma_f32 v[20:21], v[20:21], v[4:5], v[28:29]\nv_exp_f32 v45, v45 clamp\nv_pk_fma_f32 v[22:23], v[22:23], v[6:7], v[30:31]\n"
      "v_pk_mul_f32 v[188:189], v[188:189], v[18:19]\nv_pk_mul_f32 v[192:193], v[192:193], v[22:23]\nv_exp_f32 v46, v46 clamp\nv_pk_fma_f32 v[188:189], v[190:191], v[16:17], v[188:189]\n"
      "v_pk_fma_f32 v[192:193], v[194:195], v[20:21], v[192:193]\nv_pk_mul_f32 v[16:17], v[16:17], v[18:19]\nv_exp_f32 v47, v47 clamp\nv_pk_mul_f32 v[20:21], v[20:21], v[22:23]\n"
      "v_pk_mul_f32 v[18:19], v[16:17], v[20:21]\nv_rcp_f32 v18, v18\nv_pk_mul_f32 v[188:189], v[188:189], v[20:21]\nv_rcp_f32 v19, v19\nv_pk_fma_f32 v[188:189], v[192:193], v[16:17], v[188:189]\n"
      "v_pk_mul_f32 v[32:33], v[188:189], v[18:19]\nv_pk_mul_f32 v[52:53], v[204:205], v[204:205]\nv_pk_mul_f32 v[54:55], v[206:207], v[206:207]\nv_exp_f32 v48, v48 clamp\n"
      "v_pk_mul_f32 v[56:57], v[208:209], v[208:209]\nv_pk_mul_f32 v[58:59], v[210:211], v[210:211]\nv_exp_f32 v49, v49 clamp\n"
      "v_pk_fma_f32 v[52:53], v[52:53], s[58:59], s[58:59] op_sel:[0,0,1] op_sel_hi:[1,0,1]\nv_pk_fma_f32 v[54:55], v[54:55], s[58:59], s[58:59] op_sel:[0,0,1] op_sel_hi:[1,0,1]\n"
      "v_pk_fma_f32 v[56:57], v[56:57], s[58:59], s[58:59] op_sel:[0,0,1] op_sel_hi:[1,0,1]\nv_exp_f32 v50, v50 clamp\n"
      "v_pk_fma_f32 v[58:59], v[58:59], s[58:59], s[58:59] op_sel:[0,0,1] op_sel_hi:[1,0,1]\nv_pk_fma_f32 v[52:53], v[52:53], v[204:205], s[60:61] op_sel_hi:[1,1,0]\n"
      "v_pk_fma_f32 v[54:55], v[54:55], v[206:207], s[60:61] op_sel_hi:[1,1,0]\nv_exp_f32 v51, v51 clamp\nv_pk_fma_f32 v[56:57], v[56:57], v[208:209], s[60:61] op_sel_hi:[1,1,0]\n"
      "v_pk_fma_f32 v[58:59], v[58:59], v[210:211], s[60:61] op_sel_hi:[1,1,0]\nv_exp_f32 v52, v52 clamp\nv_pk_fma_f32 v[46:47], v[46:47], v[10:11], v[66:67]\n"
      "v_pk_fma_f32 v[44:45], v[44:45], v[8:9], v[64:65]\nv_pk_fma_f32 v[48:49], v[48:49], v[12:13], v[68:69]\nv_exp_f32 v53, v53 clamp\nv_pk_fma_f32 v[50:51], v[50:51], v[14:15], v[70:71]\n"
      "v_pk_mul_f32 v[196:197], v[196:197], v[46:47]\nv_pk_mul_f32 v[200:201], v[200:201], v[50:51]\nv_exp_f32 v54, v54 clamp\nv_pk_fma_f32 v[196:197], v[198:199], v[44:45], v[196:197]\n"
      "v_pk_fma_f32 v[200:201], v[202:203], v[48:49], v[200:201]\nv_pk_mul_f32 v[44:45], v[44:45], v[46:47]\nv_exp_f32 v55, v55 clamp\nv_pk_mul_f32 v[48:49], v[48:49], v[50:51]\n"
      "v_pk_mul_f32 v[46:47], v[44:45], v[48:49]\nv_rcp_f32 v46, v46\nv_pk_mul_f32 v[196:197], v[196:197], v[48:49]\nv_rcp_f32 v47, v47\nv_pk_fma_f32 v[196:197], v[200:201], v[44:45], v[196:197]\n"
      "v_pk_fma_f32 v[32:33], v[196:197], v[46:47], v[32:33]\nv_add_f32 v38, v32, v33\nv_pk_mul_f32 v[16:17], v[212:213], v[212:213]\nv_pk_mul_f32 v[18:19], v[214:215], v[214:215]\n"
      "v_exp_f32 v56, v56 clamp\nv_pk_mul_f32 v[20:21], v[216:217], v[216:217]\nv_pk_mul_f32 v[22:23], v[218:219], v[218:219]\nv_exp_f32 v57, v57 clamp\n"
      "v_pk_fma_f32 v[16:17], v[16:17], s[58:59], s[58:59] op_sel:[0,0,1] op_sel_hi:[1,0,1]\nv_pk_fma_f32 v[18:19], v[18:19], s[58:59], s[58:59] op_sel:[0,0,1] op_sel_hi:[1,0,1]\n"
      "v_pk_fma_f32 v[20:21], v[20:21], s[58:59], s[58:59] op_sel:[0,0,1] op_sel_hi:[1,0,1]\nv_exp_f32 v58, v58 clamp\n"
      "v_pk_fma_f32 v[22:23], v[22:23], s[58:59], s[58:59] op_sel:[0,0,1] op_sel_hi:[1,0,1]\nv_pk_fma_f32 v[16:17], v[16:17], v[212:213], s[60:61] op_sel_hi:[1,1,0]\n"
      "v_pk_fma_f32 v[18:19], v[18:19], v[214:215], s[60:61] op_sel_hi:[1,1,0]\nv_exp_f32 v59, v59 clamp\nv_pk_fma_f32 v[20:21], v[20:21], v[216:217], s[60:61] op_sel_hi:[1,1,0]\n"
      "v_pk_fma_f32 v[22:23], v[22:23], v[218:219], s[60:61] op_sel_hi:[1,1,0]\nv_exp_f32 v16, v16 clamp\nv_pk_fma_f32 v[54:55], v[54:55], v[2:3], v[26:27]\n"
      "v_pk_fma_f32 v[52:53], v[52:53], v[0:1], v[24:25]\nv_pk_fma_f32 v[56:57], v[56:57], v[4:5], v[28:29]\nv_exp_f32 v17, v17 clamp\nv_pk_fma_f32 v[58:59], v[58:59], v[6:7], v[30:31]\n"
      "v_pk_mul_f32 v[204:205], v[204:205], v[54:55]\nv_pk_mul_f32 v[208:209], v[208:209], v[58:59]\nv_exp_f32 v18, v18 clamp\nv_pk_fma_f32 v[204:205], v[206:207], v[52:53], v[204:205]\n"
      "v_pk_fma_f32 v[208:209], v[210:211], v[56:57], v[208:209]\nv_pk_mul_f32 v[52:53], v[52:53], v[54:55]\nv_exp_f32 v19, v19 clamp\nv_pk_mul_f32 v[56:57], v[56:57], v[58:59]\n"
      "v_pk_mul_f32 v[54:55], v[52:53], v[56:57]\nv_rcp_f32 v54, v54\nv_pk_mul_f32 v[204:205], v[204:205], v[56:57]\nv_rcp_f32 v55, v55\nv_pk_fma_f32 v[204:205], v[208:209], v[52:53], v[204:205]\n"
      "v_pk_mul_f32 v[32:33], v[204:205], v[54:55]\nv_pk_mul_f32 v[44:45], v[220:221], v[220:221]\nv_pk_mul_f32 v[46:47], v[222:223], v[222:223]\nv_exp_f32 v20, v20 clamp\n"
      "v_pk_mul_f32 v[48:49], v[224:225], v[224:225]\nv_pk_mul_f32 v[50:51], v[226:227], v[226:227]\nv_exp_f32 v21, v21 clamp\n"
      "v_pk_fma_f32 v[44:45], v[44:45], s[58:59], s[58:59] op_sel:[0,0,1] op_sel_hi:[1,0,1]\nv_pk_fma_f32 v[46:47], v[46:47], s[58:59], s[58:59] op_sel:[0,0,1] op_sel_hi:[1,0,1]\n"
      "v_pk_fma_f32 v[48:49], v[48:49], s[58:59], s[58:59] op_sel:[0,0,1] op_sel_hi:[1,0,1]\nv_exp_f32 v22, v22 clamp\n"
      "v_pk_fma_f32 v[50:51], v[50:51], s[58:59], s[58:59] op_sel:[0,0,1] op_sel_hi:[1,0,1]\nv_pk_fma_f32 v[44:45], v[44:45], v[220:221], s[60:61] op_sel_hi:[1,1,0]\n"
      "v_pk_fma_f32 v[46:47], v[46:47], v[222:223], s[60:61] op_sel_hi:[1,1,0]\nv_exp_f32 v23, v23 clamp\nv_pk_fma_f32 v[48:49], v[48:49], v[224:225], s[60:61] op_sel_hi:[1,1,0]\n"
      "v_pk_fma_f32 v[50:51], v[50:51], v[226:227], s[60:61] op_sel_hi:[1,1,0]\nv_exp_f32 v44, v44 clamp\nv_pk_fma_f32 v[18:19], v[18:19], v[10:11], v[66:67]\n"
      "v_pk_fma_f32 v[16:17], v[16:17], v[8:9], v[64:65]\nv_pk_fma_f32 v[20:21], v[20:21], v[12:13], v[68:69]\nv_exp_f32 v45, v45 clamp\nv_pk_fma_f32 v[22:23], v[22:23], v[14:15], v[70:71]\n"
      "v_pk_mul_f32 v[212:213], v[212:213], v[18:19]\nv_pk_mul_f32 v[216:217], v[216:217], v[22:23]\nv_exp_f32 v46, v46 clamp\nv_pk_fma_f32 v[212:213], v[214:215], v[16:17], v[212:213]\n"
      "v_pk_fma_f32 v[216:217], v[218:219], v[20:21], v[216:217]\nv_pk_mul_f32 v[16:17], v[16:17], v[18:19]\nv_exp_f32 v47, v47 clamp\nv_pk_mul_f32 v[20:21], v[20:21], v[22:23]\n"
      "v_pk_mul_f32 v[18:19], v[16:17], v[20:21]\nv_rcp_f32 v18, v18\nv_pk_mul_f32 v[212:213], v[212:213], v[20:21]\nv_rcp_f32 v19, v19\nv_pk_fma_f32 v[212:213], v[216:217], v[16:17], v[212:213]\n"
      "v_pk_fma_f32 v[32:33], v[212:213], v[18:19], v[32:33]\nv_add_f32 v39, v32, v33\nv_pk_mul_f32 v[52:53], v[228:229], v[228:229]\nv_pk_mul_f32 v[54:55], v[230:231], v[230:231]\n"
      "v_exp_f32 v48, v48 clamp\nv_pk_mul_f32 v[56:57], v[232:233], v[232:233]\nv_pk_mul_f32 v[58:59], v[234:235], v[234:235]\nv_exp_f32 v49, v49 clamp\n"
      "v_pk_fma_f32 v[52:53], v[52:53], s[58:59], s[58:59] op_sel:[0,0,1] op_sel_hi:[1,0,1]\nv_pk_fma_f32 v[54:55], v[54:55], s[58:59], s[58:59] op_sel:[0,0,1] op_sel_hi:[1,0,1]\n"
      "v_pk_fma_f32 v[56:57], v[56:57], s[58:59], s[58:59] op_sel:[0,0,1] op_sel_hi:[1,0,1]\nv_exp_f32 v50, v50 clamp\n"
      "v_pk_fma_f32 v[58:59], v[58:59], s[58:59], s[58:59] op_sel:[0,0,1] op_sel_hi:[1,0,1]\nv_pk_fma_f32 v[52:53], v[52:53], v[228:229], s[60:61] op_sel_hi:[1,1,0]\n"
      "v_pk_fma_f32 v[54:55], v[54:55], v[230:231], s[60:61] op_sel_hi:[1,1,0]\nv_exp_f32 v51, v51 clamp\nv_pk_fma_f32 v[56:57], v[56:57], v[232:233], s[60:61] op_sel_hi:[1,1,0]\n"
      "v_pk_fma_f32 v[58:59], v[58:59], v[234:235], s[60:61] op_sel_hi:[1,1,0]\nv_exp_f32 v52, v52 clamp\nv_pk_fma_f32 v[46:47], v[46:47], v[2:3], v[26:27]\nv_exp_f32 v53, v53 clamp\n"
      "v_pk_fma_f32 v[44:45], v[44:45], v[0:1], v[24:25]\nv_pk_fma_f32 v[48:49], v[48:49], v[4:5], v[28:29]\nv_exp_f32 v54, v54 clamp\nv_pk_fma_f32 v[50:51], v[50:51], v[6:7], v[30:31]\n"
      "v_pk_mul_f32 v[220:221], v[220:221], v[46:47]\nv_exp_f32 v55, v55 clamp\nv_pk_mul_f32 v[224:225], v[224:225], v[50:51]\nv_pk_fma_f32 v[220:221], v[222:223], v[44:45], v[220:221]\n"
      "v_exp_f32 v56, v56 clamp\nv_pk_fma_f32 v[224:225], v[226:227], v[48:49], v[224:225]\nv_exp_f32 v57, v57 clamp\nv_pk_mul_f32 v[44:45], v[44:45], v[46:47]\n"
      "v_pk_mul_f32 v[48:49], v[48:49], v[50:51]\nv_exp_f32 v58, v58 clamp\nv_pk_mul_f32 v[46:47], v[44:45], v[48:49]\nv_rcp_f32 v46, v46\nv_pk_mul_f32 v[220:221], v[220:221], v[48:49]\n"
      "v_exp_f32 v59, v59 clamp\nv_rcp_f32 v47, v47\nv_pk_fma_f32 v[220:221], v[224:225], v[44:45], v[220:221]\nv_pk_mul_f32 v[32:33], v[220:221], v[46:47]\n"
      "v_pk_fma_f32 v[54:55], v[54:55], v[10:11], v[66:67]\nv_pk_fma_f32 v[52:53], v[52:53], v[8:9], v[64:65]\nv_pk_fma_f32 v[56:57], v[56:57], v[12:13], v[68:69]\n"
      "v_pk_fma_f32 v[58:59], v[58:59], v[14:15], v[70:71]\nv_pk_mul_f32 v[228:229], v[228:229], v[54:55]\nv_pk_mul_f32 v[232:233], v[232:233], v[58:59]\n"
      "v_pk_fma_f32 v[228:229], v[230:231], v[52:53], v[228:229]\nv_pk_fma_f32 v[232:233], v[234:235], v[56:57], v[232:233]\nv_pk_mul_f32 v[52:53], v[52:53], v[54:55]\n"
      "v_pk_mul_f32 v[56:57], v[56:57], v[58:59]\nv_pk_mul_f32 v[54:55], v[52:53], v[56:57]\nv_rcp_f32 v54, v54\nv_pk_mul_f32 v[228:229], v[228:229], v[56:57]\nv_rcp_f32 v55, v55\n"
      "v_pk_fma_f32 v[228:229], v[232:233], v[52:53], v[228:229]\nv_pk_fma_f32 v[32:33], v[228:229], v[54:55], v[32:33]\nv_add_f32 v40, v32, v33\ns_barrier\ns_branch .Lge\n"
      ".Lg12:\ns_waitcnt vmcnt(4)\ns_barrier\nds_read_b64_tr_b16 v[64:65], v104 offset:45056\nds_read_b64_tr_b16 v[66:67], v104 offset:46080\nds_read_b64_tr_b16 v[68:69], v104 offset:53248\n"
      "ds_read_b64_tr_b16 v[70:71], v104 offset:54272\nds_read_b64_tr_b16 v[72:73], v105 offset:45056\nds_read_b64_tr_b16 v[74:75], v105 offset:46080\n"
      "ds_read_b64_tr_b16 v[76:77], v105 offset:53248\nds_read_b64_tr_b16 v[78:79], v105 offset:54272\nds_read_b64_tr_b16 v[80:81], v106 offset:45056\n"
      "ds_read_b64_tr_b16 v[82:83], v106 offset:46080\nds_read_b64_tr_b16 v[84:85], v106 offset:53248\nds_read_b64_tr_b16 v[86:87], v106 offset:54272\n"
      "ds_read_b64_tr_b16 v[88:89], v107 offset:45056\nds_read_b64_tr_b16 v[90:91], v107 offset:46080\nds_read_b64_tr_b16 v[92:93], v107 offset:53248\n"
      "ds_read_b64_tr_b16 v[94:95], v107 offset:54272\nds_read_b128 v[8:11], v120 offset:45056\nds_read_b128 v[12:15], v121 offset:45056\nds_read_b128 v[16:19], v96 offset:45056\n"
      "s_waitcnt lgkmcnt(1)\n.p2align 3\n"
      ".Lgl12:\nv_mfma_f32_16x16x32_bf16 v[152:155], v[88:91], v[8:11], v[152:155]\nds_read_b128 v[20:23], v97 offset:45056\nv_mfma_f32_16x16x32_bf16 v[152:155], v[92:95], v[12:15], v[152:155]\n"
      "ds_read_b128 v[24:27], v96 offset:47104\nv_mfma_f32_16x16x32_bf16 v[148:151], v[84:87], v[12:15], v[148:151]\nds_read_b128 v[28:31], v97 offset:47104\nds_read_b128 v[0:3], v96 offset:49152\n"
      "v_mfma_f32_16x16x32_bf16 v[148:151], v[80:83], v[8:11], v[148:151]\nds_read_b128 v[4:7], v97 offset:49152\nv_mfma_f32_16x16x32_bf16 v[144:147], v[72:75], v[8:11], v[144:147]\n"
      "v_mfma_f32_16x16x32_bf16 v[144:147], v[76:79], v[12:15], v[144:147]\nv_mfma_f32_16x16x32_bf16 v[140:143], v[68:71], v[12:15], v[140:143]\ns_waitcnt lgkmcnt(4)\n"
      "v_mfma_f32_16x16x32_bf16 v[140:143], v[64:67], v[8:11], v[140:143]\nv_mfma_f32_16x16x32_bf16 v[156:159], v[64:67], v[16:19], v[156:159]\nds_read_b128 v[8:11], v96 offset:51200\n"
      "v_mfma_f32_16x16x32_bf16 v[156:159], v[68:71], v[20:23], v[156:159]\nds_read_b128 v[12:15], v97 offset:51200\nv_mfma_f32_16x16x32_bf16 v[160:163], v[76:79], v[20:23], v[160:163]\n"
      "v_mfma_f32_16x16x32_bf16 v[160:163], v[72:75], v[16:19], v[160:163]\nv_mfma_f32_16x16x32_bf16 v[164:167], v[80:83], v[16:19], v[164:167]\n"
      "v_mfma_f32_16x16x32_bf16 v[164:167], v[84:87], v[20:23], v[164:167]\nv_mfma_f32_16x16x32_bf16 v[168:171], v[92:95], v[20:23], v[168:171]\ns_waitcnt lgkmcnt(2)\n"
      "v_mfma_f32_16x16x32_bf16 v[168:171], v[88:91], v[16:19], v[168:171]\nv_mfma_f32_16x16x32_bf16 v[184:187], v[88:91], v[24:27], v[184:187]\nds_read_b128 v[16:19], v96 offset:53248\n"
      "v_mfma_f32_16x16x32_bf16 v[184:187], v[92:95], v[28:31], v[184:187]\nds_read_b128 v[20:23], v97 offset:53248\nv_mfma_f32_16x16x32_bf16 v[180:183], v[84:87], v[28:31], v[180:183]\n"
      "v_mfma_f32_16x16x32_bf16 v[180:183], v[80:83], v[24:27], v[180:183]\nv_mfma_f32_16x16x32_bf16 v[176:179], v[72:75], v[24:27], v[176:179]\n"
      "v_mfma_f32_16x16x32_bf16 v[176:179], v[76:79], v[28:31], v[176:179]\ns_waitcnt vmcnt(0) lgkmcnt(0)\nv_mfma_f32_16x16x32_bf16 v[172:175], v[68:71], v[28:31], v[172:175]\ns_barrier\n"
      "v_mfma_f32_16x16x32_bf16 v[172:175], v[64:67], v[24:27], v[172:175]\nds_read_b64_tr_b16 v[32:33], v108 offset:0\nv_mfma_f32_16x16x32_bf16 v[188:191], v[64:67], v[0:3], v[188:191]\n"
      "ds_read_b64_tr_b16 v[34:35], v108 offset:1024\nv_mfma_f32_16x16x32_bf16 v[188:191], v[68:71], v[4:7], v[188:191]\nds_read_b64_tr_b16 v[36:37], v108 offset:8192\n"
      "v_mfma_f32_16x16x32_bf16 v[192:195], v[76:79], v[4:7], v[192:195]\nds_read_b64_tr_b16 v[38:39], v108 offset:9216\nv_mfma_f32_16x16x32_bf16 v[192:195], v[72:75], v[0:3], v[192:195]\n"
      "ds_read_b64_tr_b16 v[40:41], v109 offset:0\nv_mfma_f32_16x16x32_bf16 v[196:199], v[80:83], v[0:3], v[196:199]\nds_read_b64_tr_b16 v[42:43], v109 offset:1024\n"
      "v_mfma_f32_16x16x32_bf16 v[196:199], v[84:87], v[4:7], v[196:199]\nds_read_b64_tr_b16 v[44:45], v109 offset:8192\nv_mfma_f32_16x16x32_bf16 v[200:203], v[92:95], v[4:7], v[200:203]\n"
      "ds_read_b64_tr_b16 v[46:47], v109 offset:9216\nv_mfma_f32_16x16x32_bf16 v[200:203], v[88:91], v[0:3], v[200:203]\nds_read_b64_tr_b16 v[48:49], v110 offset:0\n"
      "v_mfma_f32_16x16x32_bf16 v[216:219], v[88:91], v[8:11], v[216:219]\nv_mfma_f32_16x16x32_bf16 v[216:219], v[92:95], v[12:15], v[216:219]\n"
      "v_mfma_f32_16x16x32_bf16 v[212:215], v[84:87], v[12:15], v[212:215]\nds_read_b64_tr_b16 v[50:51], v110 offset:1024\nv_mfma_f32_16x16x32_bf16 v[212:215], v[80:83], v[8:11], v[212:215]\n"
      "ds_read_b64_tr_b16 v[52:53], v110 offset:8192\nv_mfma_f32_16x16x32_bf16 v[208:211], v[72:75], v[8:11], v[208:211]\nds_read_b64_tr_b16 v[54:55], v110 offset:9216\n"
      "v_mfma_f32_16x16x32_bf16 v[208:211], v[76:79], v[12:15], v[208:211]\nds_read_b64_tr_b16 v[56:57], v111 offset:0\nv_mfma_f32_16x16x32_bf16 v[204:207], v[68:71], v[12:15], v[204:207]\n"
      "ds_read_b64_tr_b16 v[58:59], v111 offset:1024\nv_mfma_f32_16x16x32_bf16 v[204:207], v[64:67], v[8:11], v[204:207]\nds_read_b64_tr_b16 v[60:61], v111 offset:8192\n"
      "v_mfma_f32_16x16x32_bf16 v[220:223], v[64:67], v[16:19], v[220:223]\nds_read_b128 v[8:11], v122 offset:0\nv_mfma_f32_16x16x32_bf16 v[220:223], v[68:71], v[20:23], v[220:223]\n"
      "ds_read_b128 v[12:15], v123 offset:0\nv_mfma_f32_16x16x32_bf16 v[224:227], v[76:79], v[20:23], v[224:227]\nds_read_b64_tr_b16 v[62:63], v111 offset:9216\n"
      "v_mfma_f32_16x16x32_bf16 v[224:227], v[72:75], v[16:19], v[224:227]\nv_mfma_f32_16x16x32_bf16 v[228:231], v[80:83], v[16:19], v[228:231]\n"
      "v_mfma_f32_16x16x32_bf16 v[228:231], v[84:87], v[20:23], v[228:231]\nv_mfma_f32_16x16x32_bf16 v[232:235], v[92:95], v[20:23], v[232:235]\ns_waitcnt lgkmcnt(0)\n"
      "v_mfma_f32_16x16x32_bf16 v[232:235], v[88:91], v[16:19], v[232:235]\nds_read_b128 v[16:19], v98 offset:0\nv_mfma_f32_16x16x32_bf16 v[152:155], v[56:59], v[8:11], v[152:155]\n"
      "ds_read_b128 v[20:23], v99 offset:0\nv_mfma_f32_16x16x32_bf16 v[152:155], v[60:63], v[12:15], v[152:155]\nds_read_b128 v[24:27], v98 offset:2048\n"
      "v_mfma_f32_16x16x32_bf16 v[148:151], v[52:55], v[12:15], v[148:151]\nds_read_b128 v[28:31], v99 offset:2048\nds_read_b128 v[0:3], v98 offset:4096\n"
      "v_mfma_f32_16x16x32_bf16 v[148:151], v[48:51], v[8:11], v[148:151]\nds_read_b128 v[4:7], v99 offset:4096\nv_mfma_f32_16x16x32_bf16 v[144:147], v[40:43], v[8:11], v[144:147]\n"
      "v_mfma_f32_16x16x32_bf16 v[144:147], v[44:47], v[12:15], v[144:147]\nv_mfma_f32_16x16x32_bf16 v[140:143], v[36:39], v[12:15], v[140:143]\ns_waitcnt lgkmcnt(4)\n"
      "v_mfma_f32_16x16x32_bf16 v[140:143], v[32:35], v[8:11], v[140:143]\nv_mfma_f32_16x16x32_bf16 v[156:159], v[32:35], v[16:19], v[156:159]\nds_read_b128 v[8:11], v98 offset:6144\n"
      "v_mfma_f32_16x16x32_bf16 v[156:159], v[36:39], v[20:23], v[156:159]\nds_read_b128 v[12:15], v99 offset:6144\nv_mfma_f32_16x16x32_bf16 v[160:163], v[44:47], v[20:23], v[160:163]\n"
      "v_mfma_f32_16x16x32_bf16 v[160:163], v[40:43], v[16:19], v[160:163]\nv_mfma_f32_16x16x32_bf16 v[164:167], v[48:51], v[16:19], v[164:167]\n"
      "v_mfma_f32_16x16x32_bf16 v[164:167], v[52:55], v[20:23], v[164:167]\nv_mfma_f32_16x16x32_bf16 v[168:171], v[60:63], v[20:23], v[168:171]\ns_waitcnt lgkmcnt(2)\n"
      "v_mfma_f32_16x16x32_bf16 v[168:171], v[56:59], v[16:19], v[168:171]\nv_mfma_f32_16x16x32_bf16 v[184:187], v[56:59], v[24:27], v[184:187]\nds_read_b128 v[16:19], v98 offset:8192\n"
      "v_mfma_f32_16x16x32_bf16 v[184:187], v[60:63], v[28:31], v[184:187]\nds_read_b128 v[20:23], v99 offset:8192\nv_mfma_f32_16x16x32_bf16 v[180:183], v[52:55], v[28:31], v[180:183]\n"
      "v_mfma_f32_16x16x32_bf16 v[180:183], v[48:51], v[24:27], v[180:183]\nv_mfma_f32_16x16x32_bf16 v[176:179], v[40:43], v[24:27], v[176:179]\n"
      "v_mfma_f32_16x16x32_bf16 v[176:179], v[44:47], v[28:31], v[176:179]\ns_waitcnt vmcnt(0) lgkmcnt(0)\nv_mfma_f32_16x16x32_bf16 v[172:175], v[36:39], v[28:31], v[172:175]\ns_barrier\n"
      "v_mfma_f32_16x16x32_bf16 v[172:175], v[32:35], v[24:27], v[172:175]\nds_read_b64_tr_b16 v[64:65], v104 offset:0\nv_mfma_f32_16x16x32_bf16 v[188:191], v[32:35], v[0:3], v[188:191]\n"
      "ds_read_b64_tr_b16 v[66:67], v104 offset:1024\nv_mfma_f32_16x16x32_bf16 v[188:191], v[36:39], v[4:7], v[188:191]\nds_read_b64_tr_b16 v[68:69], v104 offset:8192\n"
      "v_mfma_f32_16x16x32_bf16 v[192:195], v[44:47], v[4:7], v[192:195]\nds_read_b64_tr_b16 v[70:71], v104 offset:9216\nv_mfma_f32_16x16x32_bf16 v[192:195], v[40:43], v[0:3], v[192:195]\n"
      "ds_read_b64_tr_b16 v[72:73], v105 offset:0\nv_mfma_f32_16x16x32_bf16 v[196:199], v[48:51], v[0:3], v[196:199]\nds_read_b64_tr_b16 v[74:75], v105 offset:1024\n"
      "v_mfma_f32_16x16x32_bf16 v[196:199], v[52:55], v[4:7], v[196:199]\nds_read_b64_tr_b16 v[76:77], v105 offset:8192\nv_mfma_f32_16x16x32_bf16 v[200:203], v[60:63], v[4:7], v[200:203]\n"
      "ds_read_b64_tr_b16 v[78:79], v105 offset:9216\nv_mfma_f32_16x16x32_bf16 v[200:203], v[56:59], v[0:3], v[200:203]\nds_read_b64_tr_b16 v[80:81], v106 offset:0\n"
      "v_mfma_f32_16x16x32_bf16 v[216:219], v[56:59], v[8:11], v[216:219]\nv_mfma_f32_16x16x32_bf16 v[216:219], v[60:63], v[12:15], v[216:219]\n"
      "v_mfma_f32_16x16x32_bf16 v[212:215], v[52:55], v[12:15], v[212:215]\nds_read_b64_tr_b16 v[82:83], v106 offset:1024\nv_mfma_f32_16x16x32_bf16 v[212:215], v[48:51], v[8:11], v[212:215]\n"
      "ds_read_b64_tr_b16 v[84:85], v106 offset:8192\nv_mfma_f32_16x16x32_bf16 v[208:211], v[40:43], v[8:11], v[208:211]\nds_read_b64_tr_b16 v[86:87], v106 offset:9216\n"
      "v_mfma_f32_16x16x32_bf16 v[208:211], v[44:47], v[12:15], v[208:211]\nds_read_b64_tr_b16 v[88:89], v107 offset:0\nv_mfma_f32_16x16x32_bf16 v[204:207], v[36:39], v[12:15], v[204:207]\n"
      "ds_read_b64_tr_b16 v[90:91], v107 offset:1024\nv_mfma_f32_16x16x32_bf16 v[204:207], v[32:35], v[8:11], v[204:207]\nds_read_b64_tr_b16 v[92:93], v107 offset:8192\n"
      "v_mfma_f32_16x16x32_bf16 v[220:223], v[32:35], v[16:19], v[220:223]\nds_read_b128 v[8:11], v120 offset:0\nv_mfma_f32_16x16x32_bf16 v[220:223], v[36:39], v[20:23], v[220:223]\n"
      "ds_read_b128 v[12:15], v121 offset:0\nv_mfma_f32_16x16x32_bf16 v[224:227], v[44:47], v[20:23], v[224:227]\nds_read_b64_tr_b16 v[94:95], v107 offset:9216\n"
      "v_mfma_f32_16x16x32_bf16 v[224:227], v[40:43], v[16:19], v[224:227]\nv_mfma_f32_16x16x32_bf16 v[228:231], v[48:51], v[16:19], v[228:231]\n"
      "v_mfma_f32_16x16x32_bf16 v[228:231], v[52:55], v[20:23], v[228:231]\nv_mfma_f32_16x16x32_bf16 v[232:235], v[60:63], v[20:23], v[232:235]\ns_waitcnt lgkmcnt(0)\n"
      "v_mfma_f32_16x16x32_bf16 v[232:235], v[56:59], v[16:19], v[232:235]\nds_read_b128 v[16:19], v96 offset:0\nv_mfma_f32_16x16x32_bf16 v[152:155], v[88:91], v[8:11], v[152:155]\n"
      "ds_read_b128 v[20:23], v97 offset:0\nv_mfma_f32_16x16x32_bf16 v[152:155], v[92:95], v[12:15], v[152:155]\nds_read_b128 v[24:27], v96 offset:2048\n"
      "v_mfma_f32_16x16x32_bf16 v[148:151], v[84:87], v[12:15], v[148:151]\nds_read_b128 v[28:31], v97 offset:2048\nds_read_b128 v[0:3], v96 offset:4096\n"
      "v_mfma_f32_16x16x32_bf16 v[148:151], v[80:83], v[8:11], v[148:151]\nds_read_b128 v[4:7], v97 offset:4096\nv_mfma_f32_16x16x32_bf16 v[144:147], v[72:75], v[8:11], v[144:147]\n"
      "v_mfma_f32_16x16x32_bf16 v[144:147], v[76:79], v[12:15], v[144:147]\nv_mfma_f32_16x16x32_bf16 v[140:143], v[68:71], v[12:15], v[140:143]\ns_waitcnt lgkmcnt(4)\n"
      "v_mfma_f32_16x16x32_bf16 v[140:143], v[64:67], v[8:11], v[140:143]\nv_mfma_f32_16x16x32_bf16 v[156:159], v[64:67], v[16:19], v[156:159]\nds_read_b128 v[8:11], v96 offset:6144\n"
      "v_mfma_f32_16x16x32_bf16 v[156:159], v[68:71], v[20:23], v[156:159]\nds_read_b128 v[12:15], v97 offset:6144\nv_mfma_f32_16x16x32_bf16 v[160:163], v[76:79], v[20:23], v[160:163]\n"
      "v_mfma_f32_16x16x32_bf16 v[160:163], v[72:75], v[16:19], v[160:163]\nv_mfma_f32_16x16x32_bf16 v[164:167], v[80:83], v[16:19], v[164:167]\n"
      "v_mfma_f32_16x16x32_bf16 v[164:167], v[84:87], v[20:23], v[164:167]\nv_mfma_f32_16x16x32_bf16 v[168:171], v[92:95], v[20:23], v[168:171]\ns_waitcnt lgkmcnt(2)\n"
      "v_mfma_f32_16x16x32_bf16 v[168:171], v[88:91], v[16:19], v[168:171]\nv_mfma_f32_16x16x32_bf16 v[184:187], v[88:91], v[24:27], v[184:187]\nds_read_b128 v[16:19], v96 offset:8192\n"
      "v_mfma_f32_16x16x32_bf16 v[184:187], v[92:95], v[28:31], v[184:187]\nds_read_b128 v[20:23], v97 offset:8192\nv_mfma_f32_16x16x32_bf16 v[180:183], v[84:87], v[28:31], v[180:183]\n"
      "v_mfma_f32_16x16x32_bf16 v[180:183], v[80:83], v[24:27], v[180:183]\nv_mfma_f32_16x16x32_bf16 v[176:179], v[72:75], v[24:27], v[176:179]\n"
      "v_mfma_f32_16x16x32_bf16 v[176:179], v[76:79], v[28:31], v[176:179]\ns_waitcnt vmcnt(0) lgkmcnt(0)\nv_mfma_f32_16x16x32_bf16 v[172:175], v[68:71], v[28:31], v[172:175]\ns_barrier\n"
      "v_mfma_f32_16x16x32_bf16 v[172:175], v[64:67], v[24:27], v[172:175]\nds_read_b64_tr_b16 v[32:33], v104 offset:45056\nv_mfma_f32_16x16x32_bf16 v[188:191], v[64:67], v[0:3], v[188:191]\n"
      "ds_read_b64_tr_b16 v[34:35], v104 offset:46080\nv_mfma_f32_16x16x32_bf16 v[188:191], v[68:71], v[4:7], v[188:191]\nds_read_b64_tr_b16 v[36:37], v104 offset:53248\n"
      "v_mfma_f32_16x16x32_bf16 v[192:195], v[76:79], v[4:7], v[192:195]\nds_read_b64_tr_b16 v[38:39], v104 offset:54272\nv_mfma_f32_16x16x32_bf16 v[192:195], v[72:75], v[0:3], v[192:195]\n"
      "ds_read_b64_tr_b16 v[40:41], v105 offset:45056\nv_mfma_f32_16x16x32_bf16 v[196:199], v[80:83], v[0:3], v[196:199]\nds_read_b64_tr_b16 v[42:43], v105 offset:46080\n"
      "v_mfma_f32_16x16x32_bf16 v[196:199], v[84:87], v[4:7], v[196:199]\nds_read_b64_tr_b16 v[44:45], v105 offset:53248\nv_mfma_f32_16x16x32_bf16 v[200:203], v[92:95], v[4:7], v[200:203]\n"
      "ds_read_b64_tr_b16 v[46:47], v105 offset:54272\nv_mfma_f32_16x16x32_bf16 v[200:203], v[88:91], v[0:3], v[200:203]\nds_read_b64_tr_b16 v[48:49], v106 offset:45056\n"
      "v_mfma_f32_16x16x32_bf16 v[216:219], v[88:91], v[8:11], v[216:219]\nv_mfma_f32_16x16x32_bf16 v[216:219], v[92:95], v[12:15], v[216:219]\n"
      "v_mfma_f32_16x16x32_bf16 v[212:215], v[84:87], v[12:15], v[212:215]\nds_read_b64_tr_b16 v[50:51], v106 offset:46080\nv_mfma_f32_16x16x32_bf16 v[212:215], v[80:83], v[8:11], v[212:215]\n"
      "ds_read_b64_tr_b16 v[52:53], v106 offset:53248\nv_mfma_f32_16x16x32_bf16 v[208:211], v[72:75], v[8:11], v[208:211]\nds_read_b64_tr_b16 v[54:55], v106 offset:54272\n"
      "v_mfma_f32_16x16x32_bf16 v[208:211], v[76:79], v[12:15], v[208:211]\nds_read_b64_tr_b16 v[56:57], v107 offset:45056\nv_mfma_f32_16x16x32_bf16 v[204:207], v[68:71], v[12:15], v[204:207]\n"
      "ds_read_b64_tr_b16 v[58:59], v107 offset:46080\nv_mfma_f32_16x16x32_bf16 v[204:207], v[64:67], v[8:11], v[204:207]\nds_read_b64_tr_b16 v[60:61], v107 offset:53248\n"
      "v_mfma_f32_16x16x32_bf16 v[220:223], v[64:67], v[16:19], v[220:223]\nds_read_b128 v[8:11], v120 offset:45056\nv_mfma_f32_16x16x32_bf16 v[220:223], v[68:71], v[20:23], v[220:223]\n"
      "ds_read_b128 v[12:15], v121 offset:45056\nv_mfma_f32_16x16x32_bf16 v[224:227], v[76:79], v[20:23], v[224:227]\nds_read_b64_tr_b16 v[62:63], v107 offset:54272\n"
      "v_mfma_f32_16x16x32_bf16 v[224:227], v[72:75], v[16:19], v[224:227]\nv_mfma_f32_16x16x32_bf16 v[228:231], v[80:83], v[16:19], v[228:231]\n"
      "v_mfma_f32_16x16x32_bf16 v[228:231], v[84:87], v[20:23], v[228:231]\nv_mfma_f32_16x16x32_bf16 v[232:235], v[92:95], v[20:23], v[232:235]\ns_waitcnt lgkmcnt(0)\n"
      "v_mfma_f32_16x16x32_bf16 v[232:235], v[88:91], v[16:19], v[232:235]\nds_read_b128 v[16:19], v96 offset:45056\nv_mfma_f32_16x16x32_bf16 v[152:155], v[56:59], v[8:11], v[152:155]\n"
      "ds_read_b128 v[20:23], v97 offset:45056\nv_mfma_f32_16x16x32_bf16 v[152:155], v[60:63], v[12:15], v[152:155]\nds_read_b128 v[24:27], v96 offset:47104\n"
      "v_mfma_f32_16x16x32_bf16 v[148:151], v[52:55], v[12:15], v[148:151]\nds_read_b128 v[28:31], v97 offset:47104\nds_read_b128 v[0:3], v96 offset:49152\n"
      "v_mfma_f32_16x16x32_bf16 v[148:151], v[48:51], v[8:11], v[148:151]\nds_read_b128 v[4:7], v97 offset:49152\nv_mfma_f32_16x16x32_bf16 v[144:147], v[40:43], v[8:11], v[144:147]\n"
      "v_mfma_f32_16x16x32_bf16 v[144:147], v[44:47], v[12:15], v[144:147]\nv_mfma_f32_16x16x32_bf16 v[140:143], v[36:39], v[12:15], v[140:143]\ns_waitcnt lgkmcnt(4)\n"
      "v_mfma_f32_16x16x32_bf16 v[140:143], v[32:35], v[8:11], v[140:143]\nv_mfma_f32_16x16x32_bf16 v[156:159], v[32:35], v[16:19], v[156:159]\nds_read_b128 v[8:11], v96 offset:51200\n"
      "v_mfma_f32_16x16x32_bf16 v[156:159], v[36:39], v[20:23], v[156:159]\nds_read_b128 v[12:15], v97 offset:51200\nv_mfma_f32_16x16x32_bf16 v[160:163], v[44:47], v[20:23], v[160:163]\n"
      "v_mfma_f32_16x16x32_bf16 v[160:163], v[40:43], v[16:19], v[160:163]\nv_mfma_f32_16x16x32_bf16 v[164:167], v[48:51], v[16:19], v[164:167]\n"
      "v_mfma_f32_16x16x32_bf16 v[164:167], v[52:55], v[20:23], v[164:167]\nv_mfma_f32_16x16x32_bf16 v[168:171], v[60:63], v[20:23], v[168:171]\ns_waitcnt lgkmcnt(2)\n"
      "v_mfma_f32_16x16x32_bf16 v[168:171], v[56:59], v[16:19], v[168:171]\nv_mfma_f32_16x16x32_bf16 v[184:187], v[56:59], v[24:27], v[184:187]\nds_read_b128 v[16:19], v96 offset:53248\n"
      "v_mfma_f32_16x16x32_bf16 v[184:187], v[60:63], v[28:31], v[184:187]\nds_read_b128 v[20:23], v97 offset:53248\nv_mfma_f32_16x16x32_bf16 v[180:183], v[52:55], v[28:31], v[180:183]\n"
      "v_mfma_f32_16x16x32_bf16 v[180:183], v[48:51], v[24:27], v[180:183]\nv_mfma_f32_16x16x32_bf16 v[176:179], v[40:43], v[24:27], v[176:179]\n"
      "v_mfma_f32_16x16x32_bf16 v[176:179], v[44:47], v[28:31], v[176:179]\ns_waitcnt vmcnt(0) lgkmcnt(0)\nv_mfma_f32_16x16x32_bf16 v[172:175], v[36:39], v[28:31], v[172:175]\ns_barrier\n"
      "v_mfma_f32_16x16x32_bf16 v[172:175], v[32:35], v[24:27], v[172:175]\nds_read_b64_tr_b16 v[64:65], v108 offset:0\nv_mfma_f32_16x16x32_bf16 v[188:191], v[32:35], v[0:3], v[188:191]\n"
      "ds_read_b64_tr_b16 v[66:67], v108 offset:1024\nv_mfma_f32_16x16x32_bf16 v[188:191], v[36:39], v[4:7], v[188:191]\nds_read_b64_tr_b16 v[68:69], v108 offset:8192\n"
      "v_mfma_f32_16x16x32_bf16 v[192:195], v[44:47], v[4:7], v[192:195]\nds_read_b64_tr_b16 v[70:71], v108 offset:9216\nv_mfma_f32_16x16x32_bf16 v[192:195], v[40:43], v[0:3], v[192:195]\n"
      "ds_read_b64_tr_b16 v[72:73], v109 offset:0\nv_mfma_f32_16x16x32_bf16 v[196:199], v[48:51], v[0:3], v[196:199]\nds_read_b64_tr_b16 v[74:75], v109 offset:1024\n"
      "v_mfma_f32_16x16x32_bf16 v[196:199], v[52:55], v[4:7], v[196:199]\nds_read_b64_tr_b16 v[76:77], v109 offset:8192\nv_mfma_f32_16x16x32_bf16 v[200:203], v[60:63], v[4:7], v[200:203]\n"
      "ds_read_b64_tr_b16 v[78:79], v109 offset:9216\nv_mfma_f32_16x16x32_bf16 v[200:203], v[56:59], v[0:3], v[200:203]\nds_read_b64_tr_b16 v[80:81], v110 offset:0\n"
      "v_mfma_f32_16x16x32_bf16 v[216:219], v[56:59], v[8:11], v[216:219]\nv_mfma_f32_16x16x32_bf16 v[216:219], v[60:63], v[12:15], v[216:219]\n"
      "v_mfma_f32_16x16x32_bf16 v[212:215], v[52:55], v[12:15], v[212:215]\nds_read_b64_tr_b16 v[82:83], v110 offset:1024\nv_mfma_f32_16x16x32_bf16 v[212:215], v[48:51], v[8:11], v[212:215]\n"
      "ds_read_b64_tr_b16 v[84:85], v110 offset:8192\nv_mfma_f32_16x16x32_bf16 v[208:211], v[40:43], v[8:11], v[208:211]\nds_read_b64_tr_b16 v[86:87], v110 offset:9216\n"
      "v_mfma_f32_16x16x32_bf16 v[208:211], v[44:47], v[12:15], v[208:211]\nds_read_b64_tr_b16 v[88:89], v111 offset:0\nv_mfma_f32_16x16x32_bf16 v[204:207], v[36:39], v[12:15], v[204:207]\n"
      "ds_read_b64_tr_b16 v[90:91], v111 offset:1024\nv_mfma_f32_16x16x32_bf16 v[204:207], v[32:35], v[8:11], v[204:207]\nds_read_b64_tr_b16 v[92:93], v111 offset:8192\n"
      "v_mfma_f32_16x16x32_bf16 v[220:223], v[32:35], v[16:19], v[220:223]\nds_read_b128 v[8:11], v122 offset:0\nv_mfma_f32_16x16x32_bf16 v[220:223], v[36:39], v[20:23], v[220:223]\n"
      "ds_read_b128 v[12:15], v123 offset:0\nv_mfma_f32_16x16x32_bf16 v[224:227], v[44:47], v[20:23], v[224:227]\nds_read_b64_tr_b16 v[94:95], v111 offset:9216\n"
      "v_mfma_f32_16x16x32_bf16 v[224:227], v[40:43], v[16:19], v[224:227]\nv_mfma_f32_16x16x32_bf16 v[228:231], v[48:51], v[16:19], v[228:231]\n"
      "v_mfma_f32_16x16x32_bf16 v[228:231], v[52:55], v[20:23], v[228:231]\nv_mfma_f32_16x16x32_bf16 v[232:235], v[60:63], v[20:23], v[232:235]\ns_waitcnt lgkmcnt(0)\n"
      "v_mfma_f32_16x16x32_bf16 v[232:235], v[56:59], v[16:19], v[232:235]\nds_read_b128 v[16:19], v98 offset:0\nv_mfma_f32_16x16x32_bf16 v[152:155], v[88:91], v[8:11], v[152:155]\n"
      "ds_read_b128 v[20:23], v99 offset:0\nv_mfma_f32_16x16x32_bf16 v[152:155], v[92:95], v[12:15], v[152:155]\nds_read_b128 v[24:27], v98 offset:2048\n"
      "v_mfma_f32_16x16x32_bf16 v[148:151], v[84:87], v[12:15], v[148:151]\nds_read_b128 v[28:31], v99 offset:2048\nds_read_b128 v[0:3], v98 offset:4096\n"
      "v_mfma_f32_16x16x32_bf16 v[148:151], v[80:83], v[8:11], v[148:151]\nds_read_b128 v[4:7], v99 offset:4096\nv_mfma_f32_16x16x32_bf16 v[144:147], v[72:75], v[8:11], v[144:147]\n"
      "v_mfma_f32_16x16x32_bf16 v[144:147], v[76:79], v[12:15], v[144:147]\nv_mfma_f32_16x16x32_bf16 v[140:143], v[68:71], v[12:15], v[140:143]\ns_waitcnt lgkmcnt(4)\n"
      "v_mfma_f32_16x16x32_bf16 v[140:143], v[64:67], v[8:11], v[140:143]\nv_mfma_f32_16x16x32_bf16 v[156:159], v[64:67], v[16:19], v[156:159]\nds_read_b128 v[8:11], v98 offset:6144\n"
      "v_mfma_f32_16x16x32_bf16 v[156:159], v[68:71], v[20:23], v[156:159]\nds_read_b128 v[12:15], v99 offset:6144\nv_mfma_f32_16x16x32_bf16 v[160:163], v[76:79], v[20:23], v[160:163]\n"
      "v_mfma_f32_16x16x32_bf16 v[160:163], v[72:75], v[16:19], v[160:163]\nv_mfma_f32_16x16x32_bf16 v[164:167], v[80:83], v[16:19], v[164:167]\n"
      "v_mfma_f32_16x16x32_bf16 v[164:167], v[84:87], v[20:23], v[164:167]\nv_mfma_f32_16x16x32_bf16 v[168:171], v[92:95], v[20:23], v[168:171]\ns_waitcnt lgkmcnt(2)\n"
      "v_mfma_f32_16x16x32_bf16 v[168:171], v[88:91], v[16:19], v[168:171]\nv_mfma_f32_16x16x32_bf16 v[184:187], v[88:91], v[24:27], v[184:187]\nds_read_b128 v[16:19], v98 offset:8192\n"
      "v_mfma_f32_16x16x32_bf16 v[184:187], v[92:95], v[28:31], v[184:187]\nds_read_b128 v[20:23], v99 offset:8192\nv_mfma_f32_16x16x32_bf16 v[180:183], v[84:87], v[28:31], v[180:183]\n"
      "v_mfma_f32_16x16x32_bf16 v[180:183], v[80:83], v[24:27], v[180:183]\nv_mfma_f32_16x16x32_bf16 v[176:179], v[72:75], v[24:27], v[176:179]\n"
      "v_mfma_f32_16x16x32_bf16 v[176:179], v[76:79], v[28:31], v[176:179]\ns_waitcnt vmcnt(0) lgkmcnt(0)\nv_mfma_f32_16x16x32_bf16 v[172:175], v[68:71], v[28:31], v[172:175]\ns_barrier\n"
      "v_mfma_f32_16x16x32_bf16 v[172:175], v[64:67], v[24:27], v[172:175]\nds_read_b64_tr_b16 v[32:33], v104 offset:0\nv_mfma_f32_16x16x32_bf16 v[188:191], v[64:67], v[0:3], v[188:191]\n"
      "ds_read_b64_tr_b16 v[34:35], v104 offset:1024\nv_mfma_f32_16x16x32_bf16 v[188:191], v[68:71], v[4:7], v[188:191]\nds_read_b64_tr_b16 v[36:37], v104 offset:8192\n"
      "v_mfma_f32_16x16x32_bf16 v[192:195], v[76:79], v[4:7], v[192:195]\nds_read_b64_tr_b16 v[38:39], v104 offset:9216\nv_mfma_f32_16x16x32_bf16 v[192:195], v[72:75], v[0:3], v[192:195]\n"
      "ds_read_b64_tr_b16 v[40:41], v105 offset:0\nv_mfma_f32_16x16x32_bf16 v[196:199], v[80:83], v[0:3], v[196:199]\nds_read_b64_tr_b16 v[42:43], v105 offset:1024\n"
      "v_mfma_f32_16x16x32_bf16 v[196:199], v[84:87], v[4:7], v[196:199]\nds_read_b64_tr_b16 v[44:45], v105 offset:8192\nv_mfma_f32_16x16x32_bf16 v[200:203], v[92:95], v[4:7], v[200:203]\n"
      "ds_read_b64_tr_b16 v[46:47], v105 offset:9216\nv_mfma_f32_16x16x32_bf16 v[200:203], v[88:91], v[0:3], v[200:203]\nds_read_b64_tr_b16 v[48:49], v106 offset:0\n"
      "v_mfma_f32_16x16x32_bf16 v[216:219], v[88:91], v[8:11], v[216:219]\nv_mfma_f32_16x16x32_bf16 v[216:219], v[92:95], v[12:15], v[216:219]\n"
      "v_mfma_f32_16x16x32_bf16 v[212:215], v[84:87], v[12:15], v[212:215]\nds_read_b64_tr_b16 v[50:51], v106 offset:1024\nv_mfma_f32_16x16x32_bf16 v[212:215], v[80:83], v[8:11], v[212:215]\n"
      "ds_read_b64_tr_b16 v[52:53], v106 offset:8192\nv_mfma_f32_16x16x32_bf16 v[208:211], v[72:75], v[8:11], v[208:211]\nds_read_b64_tr_b16 v[54:55], v106 offset:9216\n"
      "v_mfma_f32_16x16x32_bf16 v[208:211], v[76:79], v[12:15], v[208:211]\nds_read_b64_tr_b16 v[56:57], v107 offset:0\nv_mfma_f32_16x16x32_bf16 v[204:207], v[68:71], v[12:15], v[204:207]\n"
      "ds_read_b64_tr_b16 v[58:59], v107 offset:1024\nv_mfma_f32_16x16x32_bf16 v[204:207], v[64:67], v[8:11], v[204:207]\nds_read_b64_tr_b16 v[60:61], v107 offset:8192\n"
      "v_mfma_f32_16x16x32_bf16 v[220:223], v[64:67], v[16:19], v[220:223]\nds_read_b128 v[8:11], v120 offset:0\nv_mfma_f32_16x16x32_bf16 v[220:223], v[68:71], v[20:23], v[220:223]\n"
      "ds_read_b128 v[12:15], v121 offset:0\nv_mfma_f32_16x16x32_bf16 v[224:227], v[76:79], v[20:23], v[224:227]\nds_read_b64_tr_b16 v[62:63], v107 offset:9216\n"
      "v_mfma_f32_16x16x32_bf16 v[224:227], v[72:75], v[16:19], v[224:227]\nv_mfma_f32_16x16x32_bf16 v[228:231], v[80:83], v[16:19], v[228:231]\n"
      "v_mfma_f32_16x16x32_bf16 v[228:231], v[84:87], v[20:23], v[228:231]\nv_mfma_f32_16x16x32_bf16 v[232:235], v[92:95], v[20:23], v[232:235]\ns_waitcnt lgkmcnt(0)\n"
      "v_mfma_f32_16x16x32_bf16 v[232:235], v[88:91], v[16:19], v[232:235]\nds_read_b128 v[16:19], v96 offset:0\nv_mfma_f32_16x16x32_bf16 v[152:155], v[56:59], v[8:11], v[152:155]\n"
      "ds_read_b128 v[20:23], v97 offset:0\nv_mfma_f32_16x16x32_bf16 v[152:155], v[60:63], v[12:15], v[152:155]\nds_read_b128 v[24:27], v96 offset:2048\n"
      "v_mfma_f32_16x16x32_bf16 v[148:151], v[52:55], v[12:15], v[148:151]\nds_read_b128 v[28:31], v97 offset:2048\nds_read_b128 v[0:3], v96 offset:4096\n"
      "v_mfma_f32_16x16x32_bf16 v[148:151], v[48:51], v[8:11], v[148:151]\nds_read_b128 v[4:7], v97 offset:4096\nv_mfma_f32_16x16x32_bf16 v[144:147], v[40:43], v[8:11], v[144:147]\n"
      "v_mfma_f32_16x16x32_bf16 v[144:147], v[44:47], v[12:15], v[144:147]\nv_mfma_f32_16x16x32_bf16 v[140:143], v[36:39], v[12:15], v[140:143]\ns_waitcnt lgkmcnt(4)\n"
      "v_mfma_f32_16x16x32_bf16 v[140:143], v[32:35], v[8:11], v[140:143]\nv_mfma_f32_16x16x32_bf16 v[156:159], v[32:35], v[16:19], v[156:159]\nds_read_b128 v[8:11], v96 offset:6144\n"
      "v_mfma_f32_16x16x32_bf16 v[156:159], v[36:39], v[20:23], v[156:159]\nds_read_b128 v[12:15], v97 offset:6144\nv_mfma_f32_16x16x32_bf16 v[160:163], v[44:47], v[20:23], v[160:163]\n"
      "v_mfma_f32_16x16x32_bf16 v[160:163], v[40:43], v[16:19], v[160:163]\nv_mfma_f32_16x16x32_bf16 v[164:167], v[48:51], v[16:19], v[164:167]\n"
      "v_mfma_f32_16x16x32_bf16 v[164:167], v[52:55], v[20:23], v[164:167]\nv_mfma_f32_16x16x32_bf16 v[168:171], v[60:63], v[20:23], v[168:171]\ns_waitcnt lgkmcnt(2)\n"
      "v_mfma_f32_16x16x32_bf16 v[168:171], v[56:59], v[16:19], v[168:171]\nv_mfma_f32_16x16x32_bf16 v[184:187], v[56:59], v[24:27], v[184:187]\nds_read_b128 v[16:19], v96 offset:8192\n"
      "v_mfma_f32_16x16x32_bf16 v[184:187], v[60:63], v[28:31], v[184:187]\nds_read_b128 v[20:23], v97 offset:8192\nv_mfma_f32_16x16x32_bf16 v[180:183], v[52:55], v[28:31], v[180:183]\n"
      "v_mfma_f32_16x16x32_bf16 v[180:183], v[48:51], v[24:27], v[180:183]\nv_mfma_f32_16x16x32_bf16 v[176:179], v[40:43], v[24:27], v[176:179]\n"
      "v_mfma_f32_16x16x32_bf16 v[176:179], v[44:47], v[28:31], v[176:179]\ns_waitcnt vmcnt(0) lgkmcnt(0)\nv_mfma_f32_16x16x32_bf16 v[172:175], v[36:39], v[28:31], v[172:175]\ns_barrier\n"
      "v_mfma_f32_16x16x32_bf16 v[172:175], v[32:35], v[24:27], v[172:175]\nds_read_b64_tr_b16 v[64:65], v104 offset:45056\nv_mfma_f32_16x16x32_bf16 v[188:191], v[32:35], v[0:3], v[188:191]\n"
      "ds_read_b64_tr_b16 v[66:67], v104 offset:46080\nv_mfma_f32_16x16x32_bf16 v[188:191], v[36:39], v[4:7], v[188:191]\nds_read_b64_tr_b16 v[68:69], v104 offset:53248\n"
      "v_mfma_f32_16x16x32_bf16 v[192:195], v[44:47], v[4:7], v[192:195]\nds_read_b64_tr_b16 v[70:71], v104 offset:54272\nv_mfma_f32_16x16x32_bf16 v[192:195], v[40:43], v[0:3], v[192:195]\n"
      "ds_read_b64_tr_b16 v[72:73], v105 offset:45056\nv_mfma_f32_16x16x32_bf16 v[196:199], v[48:51], v[0:3], v[196:199]\nds_read_b64_tr_b16 v[74:75], v105 offset:46080\n"
      "v_mfma_f32_16x16x32_bf16 v[196:199], v[52:55], v[4:7], v[196:199]\nds_read_b64_tr_b16 v[76:77], v105 offset:53248\nv_mfma_f32_16x16x32_bf16 v[200:203], v[60:63], v[4:7], v[200:203]\n"
      "ds_read_b64_tr_b16 v[78:79], v105 offset:54272\nv_mfma_f32_16x16x32_bf16 v[200:203], v[56:59], v[0:3], v[200:203]\nds_read_b64_tr_b16 v[80:81], v106 offset:45056\n"
      "v_mfma_f32_16x16x32_bf16 v[216:219], v[56:59], v[8:11], v[216:219]\nv_mfma_f32_16x16x32_bf16 v[216:219], v[60:63], v[12:15], v[216:219]\n"
      "v_mfma_f32_16x16x32_bf16 v[212:215], v[52:55], v[12:15], v[212:215]\nds_read_b64_tr_b16 v[82:83], v106 offset:46080\nv_mfma_f32_16x16x32_bf16 v[212:215], v[48:51], v[8:11], v[212:215]\n"
      "ds_read_b64_tr_b16 v[84:85], v106 offset:53248\nv_mfma_f32_16x16x32_bf16 v[208:211], v[40:43], v[8:11], v[208:211]\nds_read_b64_tr_b16 v[86:87], v106 offset:54272\n"
      "v_mfma_f32_16x16x32_bf16 v[208:211], v[44:47], v[12:15], v[208:211]\nds_read_b64_tr_b16 v[88:89], v107 offset:45056\nv_mfma_f32_16x16x32_bf16 v[204:207], v[36:39], v[12:15], v[204:207]\n"
      "ds_read_b64_tr_b16 v[90:91], v107 offset:46080\nv_mfma_f32_16x16x32_bf16 v[204:207], v[32:35], v[8:11], v[204:207]\nds_read_b64_tr_b16 v[92:93], v107 offset:53248\n"
      "v_mfma_f32_16x16x32_bf16 v[220:223], v[32:35], v[16:19], v[220:223]\nds_read_b128 v[8:11], v120 offset:45056\nv_mfma_f32_16x16x32_bf16 v[220:223], v[36:39], v[20:23], v[220:223]\n"
      "ds_read_b128 v[12:15], v121 offset:45056\nv_mfma_f32_16x16x32_bf16 v[224:227], v[44:47], v[20:23], v[224:227]\nds_read_b64_tr_b16 v[94:95], v107 offset:54272\n"
      "v_mfma_f32_16x16x32_bf16 v[224:227], v[40:43], v[16:19], v[224:227]\nv_mfma_f32_16x16x32_bf16 v[228:231], v[48:51], v[16:19], v[228:231]\ns_sub_u32 s58, s58, 1\ns_cmp_lg_u32 s58, 0\n"
      "v_mfma_f32_16x16x32_bf16 v[228:231], v[52:55], v[20:23], v[228:231]\nv_mfma_f32_16x16x32_bf16 v[232:235], v[60:63], v[20:23], v[232:235]\ns_waitcnt lgkmcnt(0)\n"
      "v_mfma_f32_16x16x32_bf16 v[232:235], v[56:59], v[16:19], v[232:235]\nds_read_b128 v[16:19], v96 offset:45056\ns_cbranch_scc1 .Lgl12\n"
      "v_mfma_f32_16x16x32_bf16 v[152:155], v[88:91], v[8:11], v[152:155]\nds_read_b128 v[20:23], v97 offset:45056\nv_cmp_eq_u32 s[62:63], 0, v240\n"
      "v_mfma_f32_16x16x32_bf16 v[152:155], v[92:95], v[12:15], v[152:155]\nds_read_b128 v[24:27], v96 offset:47104\nv_mov_b32 v253, 262144\n"
      "v_mfma_f32_16x16x32_bf16 v[148:151], v[84:87], v[12:15], v[148:151]\nds_read_b128 v[28:31], v97 offset:47104\nds_read_b128 v[0:3], v96 offset:49152\n"
      "v_mfma_f32_16x16x32_bf16 v[148:151], v[80:83], v[8:11], v[148:151]\nds_read_b128 v[4:7], v97 offset:49152\nv_mov_b32 v248, 162048\n"
      "v_mfma_f32_16x16x32_bf16 v[144:147], v[72:75], v[8:11], v[144:147]\nv_mov_b32 v241, 1\nv_cndmask_b32 v253, v253, v248, s[62:63]\n"
      "v_mfma_f32_16x16x32_bf16 v[144:147], v[76:79], v[12:15], v[144:147]\nv_lshrrev_b32 v112, 4, v240\nv_bfe_u32 v113, v240, 2, 2\n"
      "v_mfma_f32_16x16x32_bf16 v[140:143], v[68:71], v[12:15], v[140:143]\ns_waitcnt lgkmcnt(4)\nv_and_b32 v114, 3, v240\nv_mfma_f32_16x16x32_bf16 v[140:143], v[64:67], v[8:11], v[140:143]\n"
      "v_mul_u32_u24 v114, 0x80008, v114\nv_add_u32 v244, v112, v113\nv_mfma_f32_16x16x32_bf16 v[156:159], v[64:67], v[16:19], v[156:159]\nds_read_b128 v[8:11], v96 offset:51200\n"
      "v_and_b32 v244, 3, v244\nv_mfma_f32_16x16x32_bf16 v[156:159], v[68:71], v[20:23], v[156:159]\nds_read_b128 v[12:15], v97 offset:51200\nv_add_u32 v116, 22, v113\n"
      "v_mfma_f32_16x16x32_bf16 v[160:163], v[76:79], v[20:23], v[160:163]\nv_cmp_gt_u32 s[62:63], 2, v113\nv_and_b32 v113, 60, v240\n"
      "v_mfma_f32_16x16x32_bf16 v[160:163], v[72:75], v[16:19], v[160:163]\ns_mov_b32 s42, 0\nv_cndmask_b32 v116, v116, v244, s[62:63]\n"
      "v_mfma_f32_16x16x32_bf16 v[164:167], v[80:83], v[16:19], v[164:167]\nv_lshl_or_b32 v244, v244, 16, v116\nv_lshl_add_u32 v244, v244, 5, v114\n"
      "v_mfma_f32_16x16x32_bf16 v[164:167], v[84:87], v[20:23], v[164:167]\nv_lshl_add_u32 v252, v240, 4, s42\ns_mov_b32 s42, 0x76325410\n"
      "v_mfma_f32_16x16x32_bf16 v[168:171], v[92:95], v[20:23], v[168:171]\ns_waitcnt lgkmcnt(2)\ns_mov_b32 s43, 0x21005410\nv_mfma_f32_16x16x32_bf16 v[168:171], v[88:91], v[16:19], v[168:171]\n"
      "v_lshrrev_b64 v[116:117], v113, s[42:43]\nv_and_b32 v116, 7, v116\nv_mfma_f32_16x16x32_bf16 v[184:187], v[88:91], v[24:27], v[184:187]\nds_read_b128 v[16:19], v96 offset:53248\n"
      "v_lshl_add_u32 v116, v112, 3, v116\nv_mfma_f32_16x16x32_bf16 v[184:187], v[92:95], v[28:31], v[184:187]\nds_read_b128 v[20:23], v97 offset:53248\ns_mov_b32 s42, 0x54107632\n"
      "v_mfma_f32_16x16x32_bf16 v[180:183], v[84:87], v[28:31], v[180:183]\ns_mov_b32 s43, 0x21107632\nv_lshrrev_b64 v[118:119], v113, s[42:43]\n"
      "v_mfma_f32_16x16x32_bf16 v[180:183], v[80:83], v[24:27], v[180:183]\nv_and_b32 v118, 7, v118\nv_lshl_add_u32 v118, v112, 3, v118\n"
      "v_mfma_f32_16x16x32_bf16 v[176:179], v[72:75], v[24:27], v[176:179]\nv_lshl_or_b32 v245, v118, 16, v116\nv_lshl_add_u32 v245, v245, 5, v114\n"
      "v_mfma_f32_16x16x32_bf16 v[176:179], v[76:79], v[28:31], v[176:179]\ns_waitcnt vmcnt(0) lgkmcnt(0)\nv_and_b32 v113, 1, v240\n"
      "v_mfma_f32_16x16x32_bf16 v[172:175], v[68:71], v[28:31], v[172:175]\ns_barrier\nv_mul_u32_u24 v113, 0xaaaa, v113\nv_mfma_f32_16x16x32_bf16 v[172:175], v[64:67], v[24:27], v[172:175]\n"
      "ds_read_b64_tr_b16 v[32:33], v108 offset:0\nv_add_u32 v242, 0x4444, v113\nv_mfma_f32_16x16x32_bf16 v[188:191], v[64:67], v[0:3], v[188:191]\nds_read_b64_tr_b16 v[34:35], v108 offset:1024\n"
      "s_lshl_b32 s42, s36, 4\nv_mfma_f32_16x16x32_bf16 v[188:191], v[68:71], v[4:7], v[188:191]\nds_read_b64_tr_b16 v[36:37], v108 offset:8192\ns_add_u32 s42, s42, 160640\n"
      "v_mfma_f32_16x16x32_bf16 v[192:195], v[76:79], v[4:7], v[192:195]\nds_read_b64_tr_b16 v[38:39], v108 offset:9216\nv_lshl_add_u32 v243, v112, 2, s42\n"
      "v_mfma_f32_16x16x32_bf16 v[192:195], v[72:75], v[0:3], v[192:195]\nds_read_b64_tr_b16 v[40:41], v109 offset:0\nv_and_b32 v112, 1, v112\n"
      "v_mfma_f32_16x16x32_bf16 v[196:199], v[80:83], v[0:3], v[196:199]\nds_read_b64_tr_b16 v[42:43], v109 offset:1024\nv_lshl_add_u32 v112, v112, 3, s24\n"
      "v_mfma_f32_16x16x32_bf16 v[196:199], v[84:87], v[4:7], v[196:199]\nds_read_b64_tr_b16 v[44:45], v109 offset:8192\ns_sub_u32 s42, s64, 1\n"
      "v_mfma_f32_16x16x32_bf16 v[200:203], v[92:95], v[4:7], v[200:203]\nds_read_b64_tr_b16 v[46:47], v109 offset:9216\nv_min_u32 v112, s42, v112\n"
      "v_mfma_f32_16x16x32_bf16 v[200:203], v[88:91], v[0:3], v[200:203]\nds_read_b64_tr_b16 v[48:49], v110 offset:0\nv_lshlrev_b32 v112, 3, v112\n"
      "v_mfma_f32_16x16x32_bf16 v[216:219], v[88:91], v[8:11], v[216:219]\nv_and_b32 v113, 15, v240\nv_lshl_add_u32 v250, v112, 1, v113\n"
      "v_mfma_f32_16x16x32_bf16 v[216:219], v[92:95], v[12:15], v[216:219]\ns_mov_b32 s42, 136192\nv_lshl_add_u32 v250, v250, 2, s42\n"
      "v_mfma_f32_16x16x32_bf16 v[212:215], v[84:87], v[12:15], v[212:215]\nds_read_b64_tr_b16 v[50:51], v110 offset:1024\nv_min_u32 v113, 12, v113\n"
      "v_mfma_f32_16x16x32_bf16 v[212:215], v[80:83], v[8:11], v[212:215]\nds_read_b64_tr_b16 v[52:53], v110 offset:8192\ns_lshl_b32 s42, s30, 7\n"
      "v_mfma_f32_16x16x32_bf16 v[208:211], v[72:75], v[8:11], v[208:211]\nds_read_b64_tr_b16 v[54:55], v110 offset:9216\nv_lshl_add_u32 v113, v113, 15, s42\n"
      "v_mfma_f32_16x16x32_bf16 v[208:211], v[76:79], v[12:15], v[208:211]\nds_read_b64_tr_b16 v[56:57], v111 offset:0\nv_add_u32 v249, v113, v112\n"
      "v_mfma_f32_16x16x32_bf16 v[204:207], v[68:71], v[12:15], v[204:207]\nds_read_b64_tr_b16 v[58:59], v111 offset:1024\nv_mfma_f32_16x16x32_bf16 v[204:207], v[64:67], v[8:11], v[204:207]\n"
      "ds_read_b64_tr_b16 v[60:61], v111 offset:8192\nv_mfma_f32_16x16x32_bf16 v[220:223], v[64:67], v[16:19], v[220:223]\nds_read_b128 v[8:11], v122 offset:0\n"
      "v_mfma_f32_16x16x32_bf16 v[220:223], v[68:71], v[20:23], v[220:223]\nds_read_b128 v[12:15], v123 offset:0\nv_mfma_f32_16x16x32_bf16 v[224:227], v[76:79], v[20:23], v[224:227]\n"
      "ds_read_b64_tr_b16 v[62:63], v111 offset:9216\nv_mfma_f32_16x16x32_bf16 v[224:227], v[72:75], v[16:19], v[224:227]\nv_mfma_f32_16x16x32_bf16 v[228:231], v[80:83], v[16:19], v[228:231]\n"
      "v_mfma_f32_16x16x32_bf16 v[228:231], v[84:87], v[20:23], v[228:231]\nv_mfma_f32_16x16x32_bf16 v[232:235], v[92:95], v[20:23], v[232:235]\ns_waitcnt lgkmcnt(0)\n"
      "v_mfma_f32_16x16x32_bf16 v[232:235], v[88:91], v[16:19], v[232:235]\nds_read_b128 v[16:19], v98 offset:0\nv_mfma_f32_16x16x32_bf16 v[152:155], v[56:59], v[8:11], v[152:155]\n"
      "ds_read_b128 v[20:23], v99 offset:0\nv_mfma_f32_16x16x32_bf16 v[152:155], v[60:63], v[12:15], v[152:155]\nds_read_b128 v[24:27], v98 offset:2048\n"
      "v_mfma_f32_16x16x32_bf16 v[148:151], v[52:55], v[12:15], v[148:151]\nds_read_b128 v[28:31], v99 offset:2048\nds_read_b128 v[0:3], v98 offset:4096\n"
      "v_mfma_f32_16x16x32_bf16 v[148:151], v[48:51], v[8:11], v[148:151]\nds_read_b128 v[4:7], v99 offset:4096\nv_mfma_f32_16x16x32_bf16 v[144:147], v[40:43], v[8:11], v[144:147]\n"
      "v_mfma_f32_16x16x32_bf16 v[144:147], v[44:47], v[12:15], v[144:147]\nv_mfma_f32_16x16x32_bf16 v[140:143], v[36:39], v[12:15], v[140:143]\ns_waitcnt lgkmcnt(4)\n"
      "v_mfma_f32_16x16x32_bf16 v[140:143], v[32:35], v[8:11], v[140:143]\nv_mfma_f32_16x16x32_bf16 v[156:159], v[32:35], v[16:19], v[156:159]\nds_read_b128 v[8:11], v98 offset:6144\n"
      "v_mfma_f32_16x16x32_bf16 v[156:159], v[36:39], v[20:23], v[156:159]\nds_read_b128 v[12:15], v99 offset:6144\nv_mfma_f32_16x16x32_bf16 v[160:163], v[44:47], v[20:23], v[160:163]\n"
      "v_mfma_f32_16x16x32_bf16 v[160:163], v[40:43], v[16:19], v[160:163]\nv_mfma_f32_16x16x32_bf16 v[164:167], v[48:51], v[16:19], v[164:167]\n"
      "v_mfma_f32_16x16x32_bf16 v[164:167], v[52:55], v[20:23], v[164:167]\nv_mfma_f32_16x16x32_bf16 v[168:171], v[60:63], v[20:23], v[168:171]\ns_waitcnt lgkmcnt(2)\n"
      "v_mfma_f32_16x16x32_bf16 v[168:171], v[56:59], v[16:19], v[168:171]\nv_mfma_f32_16x16x32_bf16 v[184:187], v[56:59], v[24:27], v[184:187]\nds_read_b128 v[16:19], v98 offset:8192\n"
      "v_mfma_f32_16x16x32_bf16 v[184:187], v[60:63], v[28:31], v[184:187]\nds_read_b128 v[20:23], v99 offset:8192\nv_mfma_f32_16x16x32_bf16 v[180:183], v[52:55], v[28:31], v[180:183]\n"
      "v_mfma_f32_16x16x32_bf16 v[180:183], v[48:51], v[24:27], v[180:183]\nv_mfma_f32_16x16x32_bf16 v[176:179], v[40:43], v[24:27], v[176:179]\n"
      "v_mfma_f32_16x16x32_bf16 v[176:179], v[44:47], v[28:31], v[176:179]\ns_waitcnt vmcnt(0) lgkmcnt(0)\nv_mfma_f32_16x16x32_bf16 v[172:175], v[36:39], v[28:31], v[172:175]\ns_barrier\n"
      "v_mfma_f32_16x16x32_bf16 v[172:175], v[32:35], v[24:27], v[172:175]\nds_read_b128 v[72:75], v252 offset:0\nv_lshl_add_u32 v246, v240, 1, v248\n"
      "v_mfma_f32_16x16x32_bf16 v[188:191], v[32:35], v[0:3], v[188:191]\nds_read_b128 v[24:27], v236 offset:512\nds_read_u16 v251, v246 offset:64\n"
      "v_mfma_f32_16x16x32_bf16 v[188:191], v[36:39], v[4:7], v[188:191]\nds_read_b128 v[28:31], v236 offset:576\nds_read_b32 v247, v248 offset:24\n"
      "v_mfma_f32_16x16x32_bf16 v[192:195], v[44:47], v[4:7], v[192:195]\nds_read_b128 v[76:79], v252 offset:1024\nv_and_b32 v246, 48, v240\n"
      "v_mfma_f32_16x16x32_bf16 v[192:195], v[40:43], v[0:3], v[192:195]\nds_read_b128 v[80:83], v252 offset:2048\ns_mov_b32 s42, 42240\n"
      "v_mfma_f32_16x16x32_bf16 v[196:199], v[48:51], v[0:3], v[196:199]\nds_read_b128 v[84:87], v252 offset:3072\nv_lshl_add_u32 v246, v246, 2, s42\n"
      "v_mfma_f32_16x16x32_bf16 v[196:199], v[52:55], v[4:7], v[196:199]\nds_read_b128 v[88:91], v252 offset:4096\ns_mov_b32 s58, 0xbdd2d3e7\n"
      "v_mfma_f32_16x16x32_bf16 v[200:203], v[60:63], v[4:7], v[200:203]\nds_read_b128 v[92:95], v252 offset:5120\ns_waitcnt lgkmcnt(4)\n"
      "v_mfma_f32_16x16x32_bf16 v[200:203], v[56:59], v[0:3], v[200:203]\nds_read_b128 v[96:99], v252 offset:6144\nv_readfirstlane_b32 s39, v247\n"
      "v_mfma_f32_16x16x32_bf16 v[216:219], v[56:59], v[8:11], v[216:219]\nds_read_b128 v[0:3], v236 offset:0\nv_readlane_b32 s55, v251, 0\n"
      "v_mfma_f32_16x16x32_bf16 v[216:219], v[60:63], v[12:15], v[216:219]\nds_read_b128 v[4:7], v236 offset:64\ns_setprio 3\nv_mfma_f32_16x16x32_bf16 v[212:215], v[52:55], v[12:15], v[212:215]\n"
      "ds_read_b128 v[100:103], v252 offset:7168\ns_xor_b32 s40, s39, 0xa5c30f96\nv_mfma_f32_16x16x32_bf16 v[212:215], v[48:51], v[8:11], v[212:215]\nds_read_b128 v[104:107], v252 offset:8192\n"
      "s_mul_i32 s41, s39, 0x9E3779B1\nv_mfma_f32_16x16x32_bf16 v[208:211], v[40:43], v[8:11], v[208:211]\nds_read_b128 v[108:111], v252 offset:9216\ns_lshr_b32 s41, s41, 24\n"
      "v_mfma_f32_16x16x32_bf16 v[208:211], v[44:47], v[12:15], v[208:211]\nds_read_b128 v[64:67], v237 offset:512\nv_mfma_f32_16x16x32_bf16 v[204:207], v[36:39], v[12:15], v[204:207]\n"
      "ds_read_b128 v[68:71], v237 offset:576\nv_mfma_f32_16x16x32_bf16 v[204:207], v[32:35], v[8:11], v[204:207]\nds_read_b128 v[112:115], v252 offset:10240\n"
      "v_mfma_f32_16x16x32_bf16 v[220:223], v[32:35], v[16:19], v[220:223]\nds_read_b128 v[8:11], v237 offset:0\nv_mfma_f32_16x16x32_bf16 v[220:223], v[36:39], v[20:23], v[220:223]\n"
      "ds_read_b128 v[12:15], v237 offset:64\nv_mfma_f32_16x16x32_bf16 v[224:227], v[44:47], v[20:23], v[224:227]\nds_read_b128 v[116:119], v252 offset:11264\n"
      "v_mfma_f32_16x16x32_bf16 v[224:227], v[40:43], v[16:19], v[224:227]\nds_read_b128 v[120:123], v252 offset:12288\n"
      "v_add_u32_sdwa v239, s49, v245 dst_sel:DWORD dst_unused:UNUSED_PAD src0_sel:DWORD src1_sel:WORD_1\nv_mfma_f32_16x16x32_bf16 v[228:231], v[48:51], v[16:19], v[228:231]\n"
      "ds_read_b128 v[124:127], v252 offset:13312\nv_add_u32_sdwa v236, s49, v244 dst_sel:DWORD dst_unused:UNUSED_PAD src0_sel:DWORD src1_sel:WORD_0\n"
      "v_mfma_f32_16x16x32_bf16 v[228:231], v[52:55], v[20:23], v[228:231]\nds_read_b128 v[128:131], v252 offset:14336\n"
      "v_add_u32_sdwa v237, s49, v244 dst_sel:DWORD dst_unused:UNUSED_PAD src0_sel:DWORD src1_sel:WORD_1\nv_mfma_f32_16x16x32_bf16 v[232:235], v[60:63], v[20:23], v[232:235]\n"
      "ds_read_b128 v[132:135], v252 offset:15360\nv_add_u32_sdwa v238, s49, v245 dst_sel:DWORD dst_unused:UNUSED_PAD src0_sel:DWORD src1_sel:WORD_0\ns_waitcnt lgkmcnt(0)\n"
      "v_mfma_f32_16x16x32_bf16 v[232:235], v[56:59], v[16:19], v[232:235]\nds_read_b128 v[136:139], v252 offset:16384\nv_mov_b32 v42, 0\nv_pk_mul_f32 v[44:45], v[140:141], v[140:141]\n"
      "v_pk_mul_f32 v[46:47], v[142:143], v[142:143]\nv_pk_mul_f32 v[48:49], v[144:145], v[144:145]\nv_pk_mul_f32 v[50:51], v[146:147], v[146:147]\n"
      "v_pk_fma_f32 v[44:45], v[44:45], s[58:59], s[58:59] op_sel:[0,0,1] op_sel_hi:[1,0,1]\nv_pk_fma_f32 v[46:47], v[46:47], s[58:59], s[58:59] op_sel:[0,0,1] op_sel_hi:[1,0,1]\n"
      "v_pk_fma_f32 v[48:49], v[48:49], s[58:59], s[58:59] op_sel:[0,0,1] op_sel_hi:[1,0,1]\nv_pk_fma_f32 v[50:51], v[50:51], s[58:59], s[58:59] op_sel:[0,0,1] op_sel_hi:[1,0,1]\n"
      "v_pk_fma_f32 v[44:45], v[44:45], v[140:141], s[60:61] op_sel_hi:[1,1,0]\nv_pk_fma_f32 v[46:47], v[46:47], v[142:143], s[60:61] op_sel_hi:[1,1,0]\n"
      "v_pk_fma_f32 v[48:49], v[48:49], v[144:145], s[60:61] op_sel_hi:[1,1,0]\nv_pk_fma_f32 v[50:51], v[50:51], v[146:147], s[60:61] op_sel_hi:[1,1,0]\nv_exp_f32 v44, v44 clamp\n"
      "v_pk_mul_f32 v[52:53], v[148:149], v[148:149]\nv_exp_f32 v45, v45 clamp\nv_pk_mul_f32 v[54:55], v[150:151], v[150:151]\nv_pk_mul_f32 v[56:57], v[152:153], v[152:153]\n"
      "v_exp_f32 v46, v46 clamp\nv_pk_mul_f32 v[58:59], v[154:155], v[154:155]\nv_exp_f32 v47, v47 clamp\nv_pk_fma_f32 v[52:53], v[52:53], s[58:59], s[58:59] op_sel:[0,0,1] op_sel_hi:[1,0,1]\n"
      "v_pk_fma_f32 v[54:55], v[54:55], s[58:59], s[58:59] op_sel:[0,0,1] op_sel_hi:[1,0,1]\nv_exp_f32 v48, v48 clamp\n"
      "v_pk_fma_f32 v[56:57], v[56:57], s[58:59], s[58:59] op_sel:[0,0,1] op_sel_hi:[1,0,1]\nv_exp_f32 v49, v49 clamp\n"
      "v_pk_fma_f32 v[58:59], v[58:59], s[58:59], s[58:59] op_sel:[0,0,1] op_sel_hi:[1,0,1]\nv_pk_fma_f32 v[52:53], v[52:53], v[148:149], s[60:61] op_sel_hi:[1,1,0]\nv_exp_f32 v50, v50 clamp\n"
      "v_pk_fma_f32 v[54:55], v[54:55], v[150:151], s[60:61] op_sel_hi:[1,1,0]\nv_exp_f32 v51, v51 clamp\nv_pk_fma_f32 v[56:57], v[56:57], v[152:153], s[60:61] op_sel_hi:[1,1,0]\n"
      "v_pk_fma_f32 v[58:59], v[58:59], v[154:155], s[60:61] op_sel_hi:[1,1,0]\nv_exp_f32 v52, v52 clamp\nv_pk_fma_f32 v[46:47], v[46:47], v[2:3], v[26:27]\n"
      "v_pk_fma_f32 v[44:45], v[44:45], v[0:1], v[24:25]\nv_pk_fma_f32 v[48:49], v[48:49], v[4:5], v[28:29]\nv_exp_f32 v53, v53 clamp\nv_pk_fma_f32 v[50:51], v[50:51], v[6:7], v[30:31]\n"
      "v_pk_mul_f32 v[140:141], v[140:141], v[46:47]\nv_pk_mul_f32 v[144:145], v[144:145], v[50:51]\nv_exp_f32 v54, v54 clamp\nv_pk_fma_f32 v[140:141], v[142:143], v[44:45], v[140:141]\n"
      "v_pk_fma_f32 v[144:145], v[146:147], v[48:49], v[144:145]\nv_pk_mul_f32 v[44:45], v[44:45], v[46:47]\nv_exp_f32 v55, v55 clamp\nv_pk_mul_f32 v[48:49], v[48:49], v[50:51]\n"
      "v_pk_mul_f32 v[46:47], v[44:45], v[48:49]\nv_rcp_f32 v46, v46\nv_pk_mul_f32 v[140:141], v[140:141], v[48:49]\nv_rcp_f32 v47, v47\nv_pk_fma_f32 v[140:141], v[144:145], v[44:45], v[140:141]\n"
      "v_pk_mul_f32 v[32:33], v[140:141], v[46:47]\nv_pk_mul_f32 v[16:17], v[156:157], v[156:157]\nv_pk_mul_f32 v[18:19], v[158:159], v[158:159]\nv_exp_f32 v56, v56 clamp\n"
      "v_pk_mul_f32 v[20:21], v[160:161], v[160:161]\nv_pk_mul_f32 v[22:23], v[162:163], v[162:163]\nv_exp_f32 v57, v57 clamp\n"
      "v_pk_fma_f32 v[16:17], v[16:17], s[58:59], s[58:59] op_sel:[0,0,1] op_sel_hi:[1,0,1]\nv_pk_fma_f32 v[18:19], v[18:19], s[58:59], s[58:59] op_sel:[0,0,1] op_sel_hi:[1,0,1]\n"
      "v_pk_fma_f32 v[20:21], v[20:21], s[58:59], s[58:59] op_sel:[0,0,1] op_sel_hi:[1,0,1]\nv_exp_f32 v58, v58 clamp\n"
      "v_pk_fma_f32 v[22:23], v[22:23], s[58:59], s[58:59] op_sel:[0,0,1] op_sel_hi:[1,0,1]\nv_pk_fma_f32 v[16:17], v[16:17], v[156:157], s[60:61] op_sel_hi:[1,1,0]\n"
      "v_pk_fma_f32 v[18:19], v[18:19], v[158:159], s[60:61] op_sel_hi:[1,1,0]\nv_exp_f32 v59, v59 clamp\nv_pk_fma_f32 v[20:21], v[20:21], v[160:161], s[60:61] op_sel_hi:[1,1,0]\n"
      "v_pk_fma_f32 v[22:23], v[22:23], v[162:163], s[60:61] op_sel_hi:[1,1,0]\nv_exp_f32 v16, v16 clamp\nv_pk_fma_f32 v[54:55], v[54:55], v[10:11], v[66:67]\n"
      "v_pk_fma_f32 v[52:53], v[52:53], v[8:9], v[64:65]\nv_pk_fma_f32 v[56:57], v[56:57], v[12:13], v[68:69]\nv_exp_f32 v17, v17 clamp\nv_pk_fma_f32 v[58:59], v[58:59], v[14:15], v[70:71]\n"
      "v_pk_mul_f32 v[148:149], v[148:149], v[54:55]\nv_pk_mul_f32 v[152:153], v[152:153], v[58:59]\nv_exp_f32 v18, v18 clamp\nv_pk_fma_f32 v[148:149], v[150:151], v[52:53], v[148:149]\n"
      "v_pk_fma_f32 v[152:153], v[154:155], v[56:57], v[152:153]\nv_pk_mul_f32 v[52:53], v[52:53], v[54:55]\nv_exp_f32 v19, v19 clamp\nv_pk_mul_f32 v[56:57], v[56:57], v[58:59]\n"
      "v_pk_mul_f32 v[54:55], v[52:53], v[56:57]\nv_rcp_f32 v54, v54\nv_pk_mul_f32 v[148:149], v[148:149], v[56:57]\nv_rcp_f32 v55, v55\nv_pk_fma_f32 v[148:149], v[152:153], v[52:53], v[148:149]\n"
      "v_pk_fma_f32 v[32:33], v[148:149], v[54:55], v[32:33]\nv_add_f32 v41, v32, v33\nv_pk_mul_f32 v[44:45], v[164:165], v[164:165]\nv_pk_mul_f32 v[46:47], v[166:167], v[166:167]\n"
      "v_exp_f32 v20, v20 clamp\nv_pk_mul_f32 v[48:49], v[168:169], v[168:169]\nv_pk_mul_f32 v[50:51], v[170:171], v[170:171]\nv_exp_f32 v21, v21 clamp\n"
      "v_pk_fma_f32 v[44:45], v[44:45], s[58:59], s[58:59] op_sel:[0,0,1] op_sel_hi:[1,0,1]\nv_pk_fma_f32 v[46:47], v[46:47], s[58:59], s[58:59] op_sel:[0,0,1] op_sel_hi:[1,0,1]\n"
      "v_pk_fma_f32 v[48:49], v[48:49], s[58:59], s[58:59] op_sel:[0,0,1] op_sel_hi:[1,0,1]\nv_exp_f32 v22, v22 clamp\n"
      "v_pk_fma_f32 v[50:51], v[50:51], s[58:59], s[58:59] op_sel:[0,0,1] op_sel_hi:[1,0,1]\nv_pk_fma_f32 v[44:45], v[44:45], v[164:165], s[60:61] op_sel_hi:[1,1,0]\n"
      "v_pk_fma_f32 v[46:47], v[46:47], v[166:167], s[60:61] op_sel_hi:[1,1,0]\nv_exp_f32 v23, v23 clamp\nv_pk_fma_f32 v[48:49], v[48:49], v[168:169], s[60:61] op_sel_hi:[1,1,0]\n"
      "v_pk_fma_f32 v[50:51], v[50:51], v[170:171], s[60:61] op_sel_hi:[1,1,0]\nv_exp_f32 v44, v44 clamp\nv_pk_fma_f32 v[18:19], v[18:19], v[2:3], v[26:27]\n"
      "v_pk_fma_f32 v[16:17], v[16:17], v[0:1], v[24:25]\nv_pk_fma_f32 v[20:21], v[20:21], v[4:5], v[28:29]\nv_exp_f32 v45, v45 clamp\nv_pk_fma_f32 v[22:23], v[22:23], v[6:7], v[30:31]\n"
      "v_pk_mul_f32 v[156:157], v[156:157], v[18:19]\nv_pk_mul_f32 v[160:161], v[160:161], v[22:23]\nv_exp_f32 v46, v46 clamp\nv_pk_fma_f32 v[156:157], v[158:159], v[16:17], v[156:157]\n"
      "v_pk_fma_f32 v[160:161], v[162:163], v[20:21], v[160:161]\nv_pk_mul_f32 v[16:17], v[16:17], v[18:19]\nv_exp_f32 v47, v47 clamp\nv_pk_mul_f32 v[20:21], v[20:21], v[22:23]\n"
      "v_pk_mul_f32 v[18:19], v[16:17], v[20:21]\nv_rcp_f32 v18, v18\nv_pk_mul_f32 v[156:157], v[156:157], v[20:21]\nv_rcp_f32 v19, v19\nv_pk_fma_f32 v[156:157], v[160:161], v[16:17], v[156:157]\n"
      "v_pk_mul_f32 v[32:33], v[156:157], v[18:19]\nv_pk_mul_f32 v[52:53], v[172:173], v[172:173]\nv_pk_mul_f32 v[54:55], v[174:175], v[174:175]\nv_exp_f32 v48, v48 clamp\n"
      "v_pk_mul_f32 v[56:57], v[176:177], v[176:177]\nv_pk_mul_f32 v[58:59], v[178:179], v[178:179]\nv_exp_f32 v49, v49 clamp\n"
      "v_pk_fma_f32 v[52:53], v[52:53], s[58:59], s[58:59] op_sel:[0,0,1] op_sel_hi:[1,0,1]\nv_pk_fma_f32 v[54:55], v[54:55], s[58:59], s[58:59] op_sel:[0,0,1] op_sel_hi:[1,0,1]\n"
      "v_pk_fma_f32 v[56:57], v[56:57], s[58:59], s[58:59] op_sel:[0,0,1] op_sel_hi:[1,0,1]\nv_exp_f32 v50, v50 clamp\n"
      "v_pk_fma_f32 v[58:59], v[58:59], s[58:59], s[58:59] op_sel:[0,0,1] op_sel_hi:[1,0,1]\nv_pk_fma_f32 v[52:53], v[52:53], v[172:173], s[60:61] op_sel_hi:[1,1,0]\n"
      "v_pk_fma_f32 v[54:55], v[54:55], v[174:175], s[60:61] op_sel_hi:[1,1,0]\nv_exp_f32 v51, v51 clamp\nv_pk_fma_f32 v[56:57], v[56:57], v[176:177], s[60:61] op_sel_hi:[1,1,0]\n"
      "v_pk_fma_f32 v[58:59], v[58:59], v[178:179], s[60:61] op_sel_hi:[1,1,0]\nv_exp_f32 v52, v52 clamp\nv_pk_fma_f32 v[46:47], v[46:47], v[10:11], v[66:67]\n"
      "v_pk_fma_f32 v[44:45], v[44:45], v[8:9], v[64:65]\nv_pk_fma_f32 v[48:49], v[48:49], v[12:13], v[68:69]\nv_exp_f32 v53, v53 clamp\nv_pk_fma_f32 v[50:51], v[50:51], v[14:15], v[70:71]\n"
      "v_pk_mul_f32 v[164:165], v[164:165], v[46:47]\nv_pk_mul_f32 v[168:169], v[168:169], v[50:51]\nv_exp_f32 v54, v54 clamp\nv_pk_fma_f32 v[164:165], v[166:167], v[44:45], v[164:165]\n"
      "v_pk_fma_f32 v[168:169], v[170:171], v[48:49], v[168:169]\nv_pk_mul_f32 v[44:45], v[44:45], v[46:47]\nv_exp_f32 v55, v55 clamp\nv_pk_mul_f32 v[48:49], v[48:49], v[50:51]\n"
      "v_pk_mul_f32 v[46:47], v[44:45], v[48:49]\nv_rcp_f32 v46, v46\nv_pk_mul_f32 v[164:165], v[164:165], v[48:49]\nv_rcp_f32 v47, v47\nv_pk_fma_f32 v[164:165], v[168:169], v[44:45], v[164:165]\n"
      "v_pk_fma_f32 v[32:33], v[164:165], v[46:47], v[32:33]\nv_add_f32 v36, v32, v33\nv_pk_mul_f32 v[16:17], v[180:181], v[180:181]\nv_pk_mul_f32 v[18:19], v[182:183], v[182:183]\n"
      "v_exp_f32 v56, v56 clamp\nv_pk_mul_f32 v[20:21], v[184:185], v[184:185]\nv_pk_mul_f32 v[22:23], v[186:187], v[186:187]\nv_exp_f32 v57, v57 clamp\n"
      "v_pk_fma_f32 v[16:17], v[16:17], s[58:59], s[58:59] op_sel:[0,0,1] op_sel_hi:[1,0,1]\nv_pk_fma_f32 v[18:19], v[18:19], s[58:59], s[58:59] op_sel:[0,0,1] op_sel_hi:[1,0,1]\n"
      "v_pk_fma_f32 v[20:21], v[20:21], s[58:59], s[58:59] op_sel:[0,0,1] op_sel_hi:[1,0,1]\nv_exp_f32 v58, v58 clamp\n"
      "v_pk_fma_f32 v[22:23], v[22:23], s[58:59], s[58:59] op_sel:[0,0,1] op_sel_hi:[1,0,1]\nv_pk_fma_f32 v[16:17], v[16:17], v[180:181], s[60:61] op_sel_hi:[1,1,0]\n"
      "v_pk_fma_f32 v[18:19], v[18:19], v[182:183], s[60:61] op_sel_hi:[1,1,0]\nv_exp_f32 v59, v59 clamp\nv_pk_fma_f32 v[20:21], v[20:21], v[184:185], s[60:61] op_sel_hi:[1,1,0]\n"
      "v_pk_fma_f32 v[22:23], v[22:23], v[186:187], s[60:61] op_sel_hi:[1,1,0]\nv_exp_f32 v16, v16 clamp\nv_pk_fma_f32 v[54:55], v[54:55], v[2:3], v[26:27]\n"
      "v_pk_fma_f32 v[52:53], v[52:53], v[0:1], v[24:25]\nv_pk_fma_f32 v[56:57], v[56:57], v[4:5], v[28:29]\nv_exp_f32 v17, v17 clamp\nv_pk_fma_f32 v[58:59], v[58:59], v[6:7], v[30:31]\n"
      "v_pk_mul_f32 v[172:173], v[172:173], v[54:55]\nv_pk_mul_f32 v[176:177], v[176:177], v[58:59]\nv_exp_f32 v18, v18 clamp\nv_pk_fma_f32 v[172:173], v[174:175], v[52:53], v[172:173]\n"
      "v_pk_fma_f32 v[176:177], v[178:179], v[56:57], v[176:177]\nv_pk_mul_f32 v[52:53], v[52:53], v[54:55]\nv_exp_f32 v19, v19 clamp\nv_pk_mul_f32 v[56:57], v[56:57], v[58:59]\n"
      "v_pk_mul_f32 v[54:55], v[52:53], v[56:57]\nv_rcp_f32 v54, v54\nv_pk_mul_f32 v[172:173], v[172:173], v[56:57]\nv_rcp_f32 v55, v55\nv_pk_fma_f32 v[172:173], v[176:177], v[52:53], v[172:173]\n"
      "v_pk_mul_f32 v[32:33], v[172:173], v[54:55]\nv_pk_mul_f32 v[44:45], v[188:189], v[188:189]\nv_pk_mul_f32 v[46:47], v[190:191], v[190:191]\nv_exp_f32 v20, v20 clamp\n"
      "v_pk_mul_f32 v[48:49], v[192:193], v[192:193]\nv_pk_mul_f32 v[50:51], v[194:195], v[194:195]\nv_exp_f32 v21, v21 clamp\n"
      "v_pk_fma_f32 v[44:45], v[44:45], s[58:59], s[58:59] op_sel:[0,0,1] op_sel_hi:[1,0,1]\nv_pk_fma_f32 v[46:47], v[46:47], s[58:59], s[58:59] op_sel:[0,0,1] op_sel_hi:[1,0,1]\n"
      "v_pk_fma_f32 v[48:49], v[48:49], s[58:59], s[58:59] op_sel:[0,0,1] op_sel_hi:[1,0,1]\nv_exp_f32 v22, v22 clamp\n"
      "v_pk_fma_f32 v[50:51], v[50:51], s[58:59], s[58:59] op_sel:[0,0,1] op_sel_hi:[1,0,1]\nv_pk_fma_f32 v[44:45], v[44:45], v[188:189], s[60:61] op_sel_hi:[1,1,0]\n"
      "v_pk_fma_f32 v[46:47], v[46:47], v[190:191], s[60:61] op_sel_hi:[1,1,0]\nv_exp_f32 v23, v23 clamp\nv_pk_fma_f32 v[48:49], v[48:49], v[192:193], s[60:61] op_sel_hi:[1,1,0]\n"
      "v_pk_fma_f32 v[50:51], v[50:51], v[194:195], s[60:61] op_sel_hi:[1,1,0]\nv_exp_f32 v44, v44 clamp\nv_pk_fma_f32 v[18:19], v[18:19], v[10:11], v[66:67]\n"
      "v_pk_fma_f32 v[16:17], v[16:17], v[8:9], v[64:65]\nv_pk_fma_f32 v[20:21], v[20:21], v[12:13], v[68:69]\nv_exp_f32 v45, v45 clamp\nv_pk_fma_f32 v[22:23], v[22:23], v[14:15], v[70:71]\n"
      "v_pk_mul_f32 v[180:181], v[180:181], v[18:19]\nv_pk_mul_f32 v[184:185], v[184:185], v[22:23]\nv_exp_f32 v46, v46 clamp\nv_pk_fma_f32 v[180:181], v[182:183], v[16:17], v[180:181]\n"
      "v_pk_fma_f32 v[184:185], v[186:187], v[20:21], v[184:185]\nv_pk_mul_f32 v[16:17], v[16:17], v[18:19]\nv_exp_f32 v47, v47 clamp\nv_pk_mul_f32 v[20:21], v[20:21], v[22:23]\n"
      "v_pk_mul_f32 v[18:19], v[16:17], v[20:21]\nv_rcp_f32 v18, v18\nv_pk_mul_f32 v[180:181], v[180:181], v[20:21]\nv_rcp_f32 v19, v19\nv_pk_fma_f32 v[180:181], v[184:185], v[16:17], v[180:181]\n"
      "v_pk_fma_f32 v[32:33], v[180:181], v[18:19], v[32:33]\nv_add_f32 v37, v32, v33\nv_pk_mul_f32 v[52:53], v[196:197], v[196:197]\nv_pk_mul_f32 v[54:55], v[198:199], v[198:199]\n"
      "v_exp_f32 v48, v48 clamp\nv_pk_mul_f32 v[56:57], v[200:201], v[200:201]\nv_pk_mul_f32 v[58:59], v[202:203], v[202:203]\nv_exp_f32 v49, v49 clamp\n"
      "v_pk_fma_f32 v[52:53], v[52:53], s[58:59], s[58:59] op_sel:[0,0,1] op_sel_hi:[1,0,1]\nv_pk_fma_f32 v[54:55], v[54:55], s[58:59], s[58:59] op_sel:[0,0,1] op_sel_hi:[1,0,1]\n"
      "v_pk_fma_f32 v[56:57], v[56:57], s[58:59], s[58:59] op_sel:[0,0,1] op_sel_hi:[1,0,1]\nv_exp_f32 v50, v50 clamp\n"
      "v_pk_fma_f32 v[58:59], v[58:59], s[58:59], s[58:59] op_sel:[0,0,1] op_sel_hi:[1,0,1]\nv_pk_fma_f32 v[52:53], v[52:53], v[196:197], s[60:61] op_sel_hi:[1,1,0]\n"
      "v_pk_fma_f32 v[54:55], v[54:55], v[198:199], s[60:61] op_sel_hi:[1,1,0]\nv_exp_f32 v51, v51 clamp\nv_pk_fma_f32 v[56:57], v[56:57], v[200:201], s[60:61] op_sel_hi:[1,1,0]\n"
      "v_pk_fma_f32 v[58:59], v[58:59], v[202:203], s[60:61] op_sel_hi:[1,1,0]\nv_exp_f32 v52, v52 clamp\nv_pk_fma_f32 v[46:47], v[46:47], v[2:3], v[26:27]\n"
      "v_pk_fma_f32 v[44:45], v[44:45], v[0:1], v[24:25]\nv_pk_fma_f32 v[48:49], v[48:49], v[4:5], v[28:29]\nv_exp_f32 v53, v53 clamp\nv_pk_fma_f32 v[50:51], v[50:51], v[6:7], v[30:31]\n"
      "v_pk_mul_f32 v[188:189], v[188:189], v[46:47]\nv_pk_mul_f32 v[192:193], v[192:193], v[50:51]\nv_exp_f32 v54, v54 clamp\nv_pk_fma_f32 v[188:189], v[190:191], v[44:45], v[188:189]\n"
      "v_pk_fma_f32 v[192:193], v[194:195], v[48:49], v[192:193]\nv_pk_mul_f32 v[44:45], v[44:45], v[46:47]\nv_exp_f32 v55, v55 clamp\nv_pk_mul_f32 v[48:49], v[48:49], v[50:51]\n"
      "v_pk_mul_f32 v[46:47], v[44:45], v[48:49]\nv_rcp_f32 v46, v46\nv_pk_mul_f32 v[188:189], v[188:189], v[48:49]\nv_rcp_f32 v47, v47\nv_pk_fma_f32 v[188:189], v[192:193], v[44:45], v[188:189]\n"
      "v_pk_mul_f32 v[32:33], v[188:189], v[46:47]\nv_pk_mul_f32 v[16:17], v[204:205], v[204:205]\nv_pk_mul_f32 v[18:19], v[206:207], v[206:207]\nv_exp_f32 v56, v56 clamp\n"
      "v_pk_mul_f32 v[20:21], v[208:209], v[208:209]\nv_pk_mul_f32 v[22:23], v[210:211], v[210:211]\nv_exp_f32 v57, v57 clamp\n"
      "v_pk_fma_f32 v[16:17], v[16:17], s[58:59], s[58:59] op_sel:[0,0,1] op_sel_hi:[1,0,1]\nv_pk_fma_f32 v[18:19], v[18:19], s[58:59], s[58:59] op_sel:[0,0,1] op_sel_hi:[1,0,1]\n"
      "v_pk_fma_f32 v[20:21], v[20:21], s[58:59], s[58:59] op_sel:[0,0,1] op_sel_hi:[1,0,1]\nv_exp_f32 v58, v58 clamp\n"
      "v_pk_fma_f32 v[22:23], v[22:23], s[58:59], s[58:59] op_sel:[0,0,1] op_sel_hi:[1,0,1]\nv_pk_fma_f32 v[16:17], v[16:17], v[204:205], s[60:61] op_sel_hi:[1,1,0]\n"
      "v_pk_fma_f32 v[18:19], v[18:19], v[206:207], s[60:61] op_sel_hi:[1,1,0]\nv_exp_f32 v59, v59 clamp\nv_pk_fma_f32 v[20:21], v[20:21], v[208:209], s[60:61] op_sel_hi:[1,1,0]\n"
      "v_pk_fma_f32 v[22:23], v[22:23], v[210:211], s[60:61] op_sel_hi:[1,1,0]\nv_exp_f32 v16, v16 clamp\nv_pk_fma_f32 v[54:55], v[54:55], v[10:11], v[66:67]\n"
      "v_pk_fma_f32 v[52:53], v[52:53], v[8:9], v[64:65]\nv_pk_fma_f32 v[56:57], v[56:57], v[12:13], v[68:69]\nv_exp_f32 v17, v17 clamp\nv_pk_fma_f32 v[58:59], v[58:59], v[14:15], v[70:71]\n"
      "v_pk_mul_f32 v[196:197], v[196:197], v[54:55]\nv_pk_mul_f32 v[200:201], v[200:201], v[58:59]\nv_exp_f32 v18, v18 clamp\nv_pk_fma_f32 v[196:197], v[198:199], v[52:53], v[196:197]\n"
      "v_pk_fma_f32 v[200:201], v[202:203], v[56:57], v[200:201]\nv_pk_mul_f32 v[52:53], v[52:53], v[54:55]\nv_exp_f32 v19, v19 clamp\nv_pk_mul_f32 v[56:57], v[56:57], v[58:59]\n"
      "v_pk_mul_f32 v[54:55], v[52:53], v[56:57]\nv_rcp_f32 v54, v54\nv_pk_mul_f32 v[196:197], v[196:197], v[56:57]\nv_rcp_f32 v55, v55\nv_pk_fma_f32 v[196:197], v[200:201], v[52:53], v[196:197]\n"
      "v_pk_fma_f32 v[32:33], v[196:197], v[54:55], v[32:33]\nv_add_f32 v38, v32, v33\nv_pk_mul_f32 v[44:45], v[212:213], v[212:213]\nv_pk_mul_f32 v[46:47], v[214:215], v[214:215]\n"
      "v_exp_f32 v20, v20 clamp\nv_pk_mul_f32 v[48:49], v[216:217], v[216:217]\nv_pk_mul_f32 v[50:51], v[218:219], v[218:219]\nv_exp_f32 v21, v21 clamp\n"
      "v_pk_fma_f32 v[44:45], v[44:45], s[58:59], s[58:59] op_sel:[0,0,1] op_sel_hi:[1,0,1]\nv_pk_fma_f32 v[46:47], v[46:47], s[58:59], s[58:59] op_sel:[0,0,1] op_sel_hi:[1,0,1]\n"
      "v_pk_fma_f32 v[48:49], v[48:49], s[58:59], s[58:59] op_sel:[0,0,1] op_sel_hi:[1,0,1]\nv_exp_f32 v22, v22 clamp\n"
      "v_pk_fma_f32 v[50:51], v[50:51], s[58:59], s[58:59] op_sel:[0,0,1] op_sel_hi:[1,0,1]\nv_pk_fma_f32 v[44:45], v[44:45], v[212:213], s[60:61] op_sel_hi:[1,1,0]\n"
      "v_pk_fma_f32 v[46:47], v[46:47], v[214:215], s[60:61] op_sel_hi:[1,1,0]\nv_exp_f32 v23, v23 clamp\nv_pk_fma_f32 v[48:49], v[48:49], v[216:217], s[60:61] op_sel_hi:[1,1,0]\n"
      "v_pk_fma_f32 v[50:51], v[50:51], v[218:219], s[60:61] op_sel_hi:[1,1,0]\nv_exp_f32 v44, v44 clamp\nv_pk_fma_f32 v[18:19], v[18:19], v[2:3], v[26:27]\n"
      "v_pk_fma_f32 v[16:17], v[16:17], v[0:1], v[24:25]\nv_pk_fma_f32 v[20:21], v[20:21], v[4:5], v[28:29]\nv_exp_f32 v45, v45 clamp\nv_pk_fma_f32 v[22:23], v[22:23], v[6:7], v[30:31]\n"
      "v_pk_mul_f32 v[204:205], v[204:205], v[18:19]\nv_pk_mul_f32 v[208:209], v[208:209], v[22:23]\nv_exp_f32 v46, v46 clamp\nv_pk_fma_f32 v[204:205], v[206:207], v[16:17], v[204:205]\n"
      "v_pk_fma_f32 v[208:209], v[210:211], v[20:21], v[208:209]\nv_pk_mul_f32 v[16:17], v[16:17], v[18:19]\nv_exp_f32 v47, v47 clamp\nv_pk_mul_f32 v[20:21], v[20:21], v[22:23]\n"
      "v_pk_mul_f32 v[18:19], v[16:17], v[20:21]\nv_rcp_f32 v18, v18\nv_pk_mul_f32 v[204:205], v[204:205], v[20:21]\nv_rcp_f32 v19, v19\nv_pk_fma_f32 v[204:205], v[208:209], v[16:17], v[204:205]\n"
      "v_pk_mul_f32 v[32:33], v[204:205], v[18:19]\nv_pk_mul_f32 v[52:53], v[220:221], v[220:221]\nv_pk_mul_f32 v[54:55], v[222:223], v[222:223]\nv_exp_f32 v48, v48 clamp\n"
      "v_pk_mul_f32 v[56:57], v[224:225], v[224:225]\nv_pk_mul_f32 v[58:59], v[226:227], v[226:227]\nv_exp_f32 v49, v49 clamp\n"
      "v_pk_fma_f32 v[52:53], v[52:53], s[58:59], s[58:59] op_sel:[0,0,1] op_sel_hi:[1,0,1]\nv_pk_fma_f32 v[54:55], v[54:55], s[58:59], s[58:59] op_sel:[0,0,1] op_sel_hi:[1,0,1]\n"
      "v_pk_fma_f32 v[56:57], v[56:57], s[58:59], s[58:59] op_sel:[0,0,1] op_sel_hi:[1,0,1]\nv_exp_f32 v50, v50 clamp\n"
      "v_pk_fma_f32 v[58:59], v[58:59], s[58:59], s[58:59] op_sel:[0,0,1] op_sel_hi:[1,0,1]\nv_pk_fma_f32 v[52:53], v[52:53], v[220:221], s[60:61] op_sel_hi:[1,1,0]\n"
      "v_pk_fma_f32 v[54:55], v[54:55], v[222:223], s[60:61] op_sel_hi:[1,1,0]\nv_exp_f32 v51, v51 clamp\nv_pk_fma_f32 v[56:57], v[56:57], v[224:225], s[60:61] op_sel_hi:[1,1,0]\n"
      "v_pk_fma_f32 v[58:59], v[58:59], v[226:227], s[60:61] op_sel_hi:[1,1,0]\nv_exp_f32 v52, v52 clamp\nv_pk_fma_f32 v[46:47], v[46:47], v[10:11], v[66:67]\n"
      "v_pk_fma_f32 v[44:45], v[44:45], v[8:9], v[64:65]\nv_pk_fma_f32 v[48:49], v[48:49], v[12:13], v[68:69]\nv_exp_f32 v53, v53 clamp\nv_pk_fma_f32 v[50:51], v[50:51], v[14:15], v[70:71]\n"
      "v_pk_mul_f32 v[212:213], v[212:213], v[46:47]\nv_pk_mul_f32 v[216:217], v[216:217], v[50:51]\nv_exp_f32 v54, v54 clamp\nv_pk_fma_f32 v[212:213], v[214:215], v[44:45], v[212:213]\n"
      "v_pk_fma_f32 v[216:217], v[218:219], v[48:49], v[216:217]\nv_pk_mul_f32 v[44:45], v[44:45], v[46:47]\nv_exp_f32 v55, v55 clamp\nv_pk_mul_f32 v[48:49], v[48:49], v[50:51]\n"
      "v_pk_mul_f32 v[46:47], v[44:45], v[48:49]\nv_rcp_f32 v46, v46\nv_pk_mul_f32 v[212:213], v[212:213], v[48:49]\nv_rcp_f32 v47, v47\nv_pk_fma_f32 v[212:213], v[216:217], v[44:45], v[212:213]\n"
      "v_pk_fma_f32 v[32:33], v[212:213], v[46:47], v[32:33]\nv_add_f32 v39, v32, v33\nv_pk_mul_f32 v[16:17], v[228:229], v[228:229]\nv_pk_mul_f32 v[18:19], v[230:231], v[230:231]\n"
      "v_exp_f32 v56, v56 clamp\nv_pk_mul_f32 v[20:21], v[232:233], v[232:233]\nv_pk_mul_f32 v[22:23], v[234:235], v[234:235]\nv_exp_f32 v57, v57 clamp\n"
      "v_pk_fma_f32 v[16:17], v[16:17], s[58:59], s[58:59] op_sel:[0,0,1] op_sel_hi:[1,0,1]\nv_pk_fma_f32 v[18:19], v[18:19], s[58:59], s[58:59] op_sel:[0,0,1] op_sel_hi:[1,0,1]\n"
      "v_pk_fma_f32 v[20:21], v[20:21], s[58:59], s[58:59] op_sel:[0,0,1] op_sel_hi:[1,0,1]\nv_exp_f32 v58, v58 clamp\n"
      "v_pk_fma_f32 v[22:23], v[22:23], s[58:59], s[58:59] op_sel:[0,0,1] op_sel_hi:[1,0,1]\nv_pk_fma_f32 v[16:17], v[16:17], v[228:229], s[60:61] op_sel_hi:[1,1,0]\n"
      "v_pk_fma_f32 v[18:19], v[18:19], v[230:231], s[60:61] op_sel_hi:[1,1,0]\nv_exp_f32 v59, v59 clamp\nv_pk_fma_f32 v[20:21], v[20:21], v[232:233], s[60:61] op_sel_hi:[1,1,0]\n"
      "v_pk_fma_f32 v[22:23], v[22:23], v[234:235], s[60:61] op_sel_hi:[1,1,0]\nv_exp_f32 v16, v16 clamp\nv_pk_fma_f32 v[54:55], v[54:55], v[2:3], v[26:27]\nv_exp_f32 v17, v17 clamp\n"
      "v_pk_fma_f32 v[52:53], v[52:53], v[0:1], v[24:25]\nv_pk_fma_f32 v[56:57], v[56:57], v[4:5], v[28:29]\nv_exp_f32 v18, v18 clamp\nv_pk_fma_f32 v[58:59], v[58:59], v[6:7], v[30:31]\n"
      "v_pk_mul_f32 v[220:221], v[220:221], v[54:55]\nv_exp_f32 v19, v19 clamp\nv_pk_mul_f32 v[224:225], v[224:225], v[58:59]\nv_pk_fma_f32 v[220:221], v[222:223], v[52:53], v[220:221]\n"
      "v_exp_f32 v20, v20 clamp\nv_pk_fma_f32 v[224:225], v[226:227], v[56:57], v[224:225]\nv_exp_f32 v21, v21 clamp\nv_pk_mul_f32 v[52:53], v[52:53], v[54:55]\n"
      "v_pk_mul_f32 v[56:57], v[56:57], v[58:59]\nv_exp_f32 v22, v22 clamp\nv_pk_mul_f32 v[54:55], v[52:53], v[56:57]\nv_rcp_f32 v54, v54\nv_pk_mul_f32 v[220:221], v[220:221], v[56:57]\n"
      "v_exp_f32 v23, v23 clamp\nv_rcp_f32 v55, v55\nv_pk_fma_f32 v[220:221], v[224:225], v[52:53], v[220:221]\nv_pk_mul_f32 v[32:33], v[220:221], v[54:55]\n"
      "v_pk_fma_f32 v[18:19], v[18:19], v[10:11], v[66:67]\nv_pk_fma_f32 v[16:17], v[16:17], v[8:9], v[64:65]\nv_pk_fma_f32 v[20:21], v[20:21], v[12:13], v[68:69]\n"
      "v_pk_fma_f32 v[22:23], v[22:23], v[14:15], v[70:71]\nv_pk_mul_f32 v[228:229], v[228:229], v[18:19]\nv_pk_mul_f32 v[232:233], v[232:233], v[22:23]\n"
      "v_pk_fma_f32 v[228:229], v[230:231], v[16:17], v[228:229]\nv_pk_fma_f32 v[232:233], v[234:235], v[20:21], v[232:233]\nv_pk_mul_f32 v[16:17], v[16:17], v[18:19]\n"
      "v_pk_mul_f32 v[20:21], v[20:21], v[22:23]\nv_pk_mul_f32 v[18:19], v[16:17], v[20:21]\nv_rcp_f32 v18, v18\nv_pk_mul_f32 v[228:229], v[228:229], v[20:21]\nv_rcp_f32 v19, v19\n"
      "v_pk_fma_f32 v[228:229], v[232:233], v[16:17], v[228:229]\nv_pk_fma_f32 v[32:33], v[228:229], v[18:19], v[32:33]\nv_add_f32 v40, v32, v33\ns_barrier\ns_branch .Lge\n"
      ".Lg13:\ns_waitcnt vmcnt(4)\ns_barrier\nds_read_b64_tr_b16 v[64:65], v104 offset:45056\nds_read_b64_tr_b16 v[66:67], v104 offset:46080\nds_read_b64_tr_b16 v[68:69], v104 offset:53248\n"
      "ds_read_b64_tr_b16 v[70:71], v104 offset:54272\nds_read_b64_tr_b16 v[72:73], v105 offset:45056\nds_read_b64_tr_b16 v[74:75], v105 offset:46080\n"
      "ds_read_b64_tr_b16 v[76:77], v105 offset:53248\nds_read_b64_tr_b16 v[78:79], v105 offset:54272\nds_read_b64_tr_b16 v[80:81], v106 offset:45056\n"
      "ds_read_b64_tr_b16 v[82:83], v106 offset:46080\nds_read_b64_tr_b16 v[84:85], v106 offset:53248\nds_read_b64_tr_b16 v[86:87], v106 offset:54272\n"
      "ds_read_b64_tr_b16 v[88:89], v107 offset:45056\nds_read_b64_tr_b16 v[90:91], v107 offset:46080\nds_read_b64_tr_b16 v[92:93], v107 offset:53248\n"
      "ds_read_b64_tr_b16 v[94:95], v107 offset:54272\nds_read_b128 v[0:3], v100 offset:45056\nds_read_b128 v[4:7], v101 offset:45056\nds_read_b128 v[8:11], v120 offset:45056\n"
      "ds_read_b128 v[12:15], v121 offset:45056\nds_read_b128 v[16:19], v96 offset:45056\ns_waitcnt lgkmcnt(1)\n.p2align 3\n"
      ".Lgl13:\nv_mfma_f32_16x16x32_bf16 v[124:127], v[64:67], v[0:3], v[124:127]\nds_read_b128 v[20:23], v97 offset:45056\nv_mfma_f32_16x16x32_bf16 v[124:127], v[68:71], v[4:7], v[124:127]\n"
      "ds_read_b128 v[24:27], v96 offset:47104\nv_mfma_f32_16x16x32_bf16 v[128:131], v[76:79], v[4:7], v[128:131]\nds_read_b128 v[28:31], v97 offset:47104\n"
      "v_mfma_f32_16x16x32_bf16 v[128:131], v[72:75], v[0:3], v[128:131]\nv_mfma_f32_16x16x32_bf16 v[152:155], v[88:91], v[8:11], v[152:155]\nds_read_b128 v[0:3], v96 offset:49152\n"
      "v_mfma_f32_16x16x32_bf16 v[152:155], v[92:95], v[12:15], v[152:155]\nds_read_b128 v[4:7], v97 offset:49152\nv_mfma_f32_16x16x32_bf16 v[148:151], v[84:87], v[12:15], v[148:151]\n"
      "v_mfma_f32_16x16x32_bf16 v[148:151], v[80:83], v[8:11], v[148:151]\nv_mfma_f32_16x16x32_bf16 v[144:147], v[72:75], v[8:11], v[144:147]\n"
      "v_mfma_f32_16x16x32_bf16 v[144:147], v[76:79], v[12:15], v[144:147]\nv_mfma_f32_16x16x32_bf16 v[140:143], v[68:71], v[12:15], v[140:143]\ns_waitcnt lgkmcnt(4)\n"
      "v_mfma_f32_16x16x32_bf16 v[140:143], v[64:67], v[8:11], v[140:143]\nv_mfma_f32_16x16x32_bf16 v[156:159], v[64:67], v[16:19], v[156:159]\nds_read_b128 v[8:11], v96 offset:51200\n"
      "v_mfma_f32_16x16x32_bf16 v[156:159], v[68:71], v[20:23], v[156:159]\nds_read_b128 v[12:15], v97 offset:51200\nv_mfma_f32_16x16x32_bf16 v[160:163], v[76:79], v[20:23], v[160:163]\n"
      "v_mfma_f32_16x16x32_bf16 v[160:163], v[72:75], v[16:19], v[160:163]\nv_mfma_f32_16x16x32_bf16 v[164:167], v[80:83], v[16:19], v[164:167]\n"
      "v_mfma_f32_16x16x32_bf16 v[164:167], v[84:87], v[20:23], v[164:167]\nv_mfma_f32_16x16x32_bf16 v[168:171], v[92:95], v[20:23], v[168:171]\ns_waitcnt lgkmcnt(2)\n"
      "v_mfma_f32_16x16x32_bf16 v[168:171], v[88:91], v[16:19], v[168:171]\nv_mfma_f32_16x16x32_bf16 v[184:187], v[88:91], v[24:27], v[184:187]\nds_read_b128 v[16:19], v96 offset:53248\n"
      "v_mfma_f32_16x16x32_bf16 v[184:187], v[92:95], v[28:31], v[184:187]\nds_read_b128 v[20:23], v97 offset:53248\nv_mfma_f32_16x16x32_bf16 v[180:183], v[84:87], v[28:31], v[180:183]\n"
      "v_mfma_f32_16x16x32_bf16 v[180:183], v[80:83], v[24:27], v[180:183]\nv_mfma_f32_16x16x32_bf16 v[176:179], v[72:75], v[24:27], v[176:179]\n"
      "v_mfma_f32_16x16x32_bf16 v[176:179], v[76:79], v[28:31], v[176:179]\ns_waitcnt vmcnt(0) lgkmcnt(0)\nv_mfma_f32_16x16x32_bf16 v[172:175], v[68:71], v[28:31], v[172:175]\ns_barrier\n"
      "v_mfma_f32_16x16x32_bf16 v[172:175], v[64:67], v[24:27], v[172:175]\nds_read_b64_tr_b16 v[32:33], v108 offset:0\nv_mfma_f32_16x16x32_bf16 v[188:191], v[64:67], v[0:3], v[188:191]\n"
      "ds_read_b64_tr_b16 v[34:35], v108 offset:1024\nv_mfma_f32_16x16x32_bf16 v[188:191], v[68:71], v[4:7], v[188:191]\nds_read_b64_tr_b16 v[36:37], v108 offset:8192\n"
      "v_mfma_f32_16x16x32_bf16 v[192:195], v[76:79], v[4:7], v[192:195]\nds_read_b64_tr_b16 v[38:39], v108 offset:9216\nv_mfma_f32_16x16x32_bf16 v[192:195], v[72:75], v[0:3], v[192:195]\n"
      "ds_read_b64_tr_b16 v[40:41], v109 offset:0\nv_mfma_f32_16x16x32_bf16 v[196:199], v[80:83], v[0:3], v[196:199]\nds_read_b64_tr_b16 v[42:43], v109 offset:1024\n"
      "v_mfma_f32_16x16x32_bf16 v[196:199], v[84:87], v[4:7], v[196:199]\nds_read_b64_tr_b16 v[44:45], v109 offset:8192\nv_mfma_f32_16x16x32_bf16 v[200:203], v[92:95], v[4:7], v[200:203]\n"
      "ds_read_b64_tr_b16 v[46:47], v109 offset:9216\nv_mfma_f32_16x16x32_bf16 v[200:203], v[88:91], v[0:3], v[200:203]\nds_read_b64_tr_b16 v[48:49], v110 offset:0\n"
      "v_mfma_f32_16x16x32_bf16 v[216:219], v[88:91], v[8:11], v[216:219]\nds_read_b128 v[0:3], v102 offset:0\nv_mfma_f32_16x16x32_bf16 v[216:219], v[92:95], v[12:15], v[216:219]\n"
      "ds_read_b128 v[4:7], v103 offset:0\nv_mfma_f32_16x16x32_bf16 v[212:215], v[84:87], v[12:15], v[212:215]\nds_read_b64_tr_b16 v[50:51], v110 offset:1024\n"
      "v_mfma_f32_16x16x32_bf16 v[212:215], v[80:83], v[8:11], v[212:215]\nds_read_b64_tr_b16 v[52:53], v110 offset:8192\nv_mfma_f32_16x16x32_bf16 v[208:211], v[72:75], v[8:11], v[208:211]\n"
      "ds_read_b64_tr_b16 v[54:55], v110 offset:9216\nv_mfma_f32_16x16x32_bf16 v[208:211], v[76:79], v[12:15], v[208:211]\nds_read_b64_tr_b16 v[56:57], v111 offset:0\n"
      "v_mfma_f32_16x16x32_bf16 v[204:207], v[68:71], v[12:15], v[204:207]\nds_read_b64_tr_b16 v[58:59], v111 offset:1024\nv_mfma_f32_16x16x32_bf16 v[204:207], v[64:67], v[8:11], v[204:207]\n"
      "ds_read_b64_tr_b16 v[60:61], v111 offset:8192\nv_mfma_f32_16x16x32_bf16 v[220:223], v[64:67], v[16:19], v[220:223]\nds_read_b128 v[8:11], v122 offset:0\n"
      "v_mfma_f32_16x16x32_bf16 v[220:223], v[68:71], v[20:23], v[220:223]\nds_read_b128 v[12:15], v123 offset:0\nv_mfma_f32_16x16x32_bf16 v[224:227], v[76:79], v[20:23], v[224:227]\n"
      "ds_read_b64_tr_b16 v[62:63], v111 offset:9216\nv_mfma_f32_16x16x32_bf16 v[224:227], v[72:75], v[16:19], v[224:227]\nv_mfma_f32_16x16x32_bf16 v[228:231], v[80:83], v[16:19], v[228:231]\n"
      "v_mfma_f32_16x16x32_bf16 v[228:231], v[84:87], v[20:23], v[228:231]\nv_mfma_f32_16x16x32_bf16 v[232:235], v[92:95], v[20:23], v[232:235]\ns_waitcnt lgkmcnt(0)\n"
      "v_mfma_f32_16x16x32_bf16 v[232:235], v[88:91], v[16:19], v[232:235]\nds_read_b128 v[16:19], v98 offset:0\nv_mfma_f32_16x16x32_bf16 v[124:127], v[32:35], v[0:3], v[124:127]\n"
      "ds_read_b128 v[20:23], v99 offset:0\nv_mfma_f32_16x16x32_bf16 v[124:127], v[36:39], v[4:7], v[124:127]\nds_read_b128 v[24:27], v98 offset:2048\n"
      "v_mfma_f32_16x16x32_bf16 v[128:131], v[44:47], v[4:7], v[128:131]\nds_read_b128 v[28:31], v99 offset:2048\nv_mfma_f32_16x16x32_bf16 v[128:131], v[40:43], v[0:3], v[128:131]\n"
      "v_mfma_f32_16x16x32_bf16 v[152:155], v[56:59], v[8:11], v[152:155]\nds_read_b128 v[0:3], v98 offset:4096\nv_mfma_f32_16x16x32_bf16 v[152:155], v[60:63], v[12:15], v[152:155]\n"
      "ds_read_b128 v[4:7], v99 offset:4096\nv_mfma_f32_16x16x32_bf16 v[148:151], v[52:55], v[12:15], v[148:151]\nv_mfma_f32_16x16x32_bf16 v[148:151], v[48:51], v[8:11], v[148:151]\n"
      "v_mfma_f32_16x16x32_bf16 v[144:147], v[40:43], v[8:11], v[144:147]\nv_mfma_f32_16x16x32_bf16 v[144:147], v[44:47], v[12:15], v[144:147]\n"
      "v_mfma_f32_16x16x32_bf16 v[140:143], v[36:39], v[12:15], v[140:143]\ns_waitcnt lgkmcnt(4)\nv_mfma_f32_16x16x32_bf16 v[140:143], v[32:35], v[8:11], v[140:143]\n"
      "v_mfma_f32_16x16x32_bf16 v[156:159], v[32:35], v[16:19], v[156:159]\nds_read_b128 v[8:11], v98 offset:6144\nv_mfma_f32_16x16x32_bf16 v[156:159], v[36:39], v[20:23], v[156:159]\n"
      "ds_read_b128 v[12:15], v99 offset:6144\nv_mfma_f32_16x16x32_bf16 v[160:163], v[44:47], v[20:23], v[160:163]\nv_mfma_f32_16x16x32_bf16 v[160:163], v[40:43], v[16:19], v[160:163]\n"
      "v_mfma_f32_16x16x32_bf16 v[164:167], v[48:51], v[16:19], v[164:167]\nv_mfma_f32_16x16x32_bf16 v[164:167], v[52:55], v[20:23], v[164:167]\n"
      "v_mfma_f32_16x16x32_bf16 v[168:171], v[60:63], v[20:23], v[168:171]\ns_waitcnt lgkmcnt(2)\nv_mfma_f32_16x16x32_bf16 v[168:171], v[56:59], v[16:19], v[168:171]\n"
      "v_mfma_f32_16x16x32_bf16 v[184:187], v[56:59], v[24:27], v[184:187]\nds_read_b128 v[16:19], v98 offset:8192\nv_mfma_f32_16x16x32_bf16 v[184:187], v[60:63], v[28:31], v[184:187]\n"
      "ds_read_b128 v[20:23], v99 offset:8192\nv_mfma_f32_16x16x32_bf16 v[180:183], v[52:55], v[28:31], v[180:183]\nv_mfma_f32_16x16x32_bf16 v[180:183], v[48:51], v[24:27], v[180:183]\n"
      "v_mfma_f32_16x16x32_bf16 v[176:179], v[40:43], v[24:27], v[176:179]\nv_mfma_f32_16x16x32_bf16 v[176:179], v[44:47], v[28:31], v[176:179]\ns_waitcnt vmcnt(0) lgkmcnt(0)\n"
      "v_mfma_f32_16x16x32_bf16 v[172:175], v[36:39], v[28:31], v[172:175]\ns_barrier\nv_mfma_f32_16x16x32_bf16 v[172:175], v[32:35], v[24:27], v[172:175]\n"
      "ds_read_b64_tr_b16 v[64:65], v104 offset:0\nv_mfma_f32_16x16x32_bf16 v[188:191], v[32:35], v[0:3], v[188:191]\nds_read_b64_tr_b16 v[66:67], v104 offset:1024\n"
      "v_mfma_f32_16x16x32_bf16 v[188:191], v[36:39], v[4:7], v[188:191]\nds_read_b64_tr_b16 v[68:69], v104 offset:8192\nv_mfma_f32_16x16x32_bf16 v[192:195], v[44:47], v[4:7], v[192:195]\n"
      "ds_read_b64_tr_b16 v[70:71], v104 offset:9216\nv_mfma_f32_16x16x32_bf16 v[192:195], v[40:43], v[0:3], v[192:195]\nds_read_b64_tr_b16 v[72:73], v105 offset:0\n"
      "v_mfma_f32_16x16x32_bf16 v[196:199], v[48:51], v[0:3], v[196:199]\nds_read_b64_tr_b16 v[74:75], v105 offset:1024\nv_mfma_f32_16x16x32_bf16 v[196:199], v[52:55], v[4:7], v[196:199]\n"
      "ds_read_b64_tr_b16 v[76:77], v105 offset:8192\nv_mfma_f32_16x16x32_bf16 v[200:203], v[60:63], v[4:7], v[200:203]\nds_read_b64_tr_b16 v[78:79], v105 offset:9216\n"
      "v_mfma_f32_16x16x32_bf16 v[200:203], v[56:59], v[0:3], v[200:203]\nds_read_b64_tr_b16 v[80:81], v106 offset:0\nv_mfma_f32_16x16x32_bf16 v[216:219], v[56:59], v[8:11], v[216:219]\n"
      "ds_read_b128 v[0:3], v100 offset:0\nv_mfma_f32_16x16x32_bf16 v[216:219], v[60:63], v[12:15], v[216:219]\nds_read_b128 v[4:7], v101 offset:0\n"
      "v_mfma_f32_16x16x32_bf16 v[212:215], v[52:55], v[12:15], v[212:215]\nds_read_b64_tr_b16 v[82:83], v106 offset:1024\nv_mfma_f32_16x16x32_bf16 v[212:215], v[48:51], v[8:11], v[212:215]\n"
      "ds_read_b64_tr_b16 v[84:85], v106 offset:8192\nv_mfma_f32_16x16x32_bf16 v[208:211], v[40:43], v[8:11], v[208:211]\nds_read_b64_tr_b16 v[86:87], v106 offset:9216\n"
      "v_mfma_f32_16x16x32_bf16 v[208:211], v[44:47], v[12:15], v[208:211]\nds_read_b64_tr_b16 v[88:89], v107 offset:0\nv_mfma_f32_16x16x32_bf16 v[204:207], v[36:39], v[12:15], v[204:207]\n"
      "ds_read_b64_tr_b16 v[90:91], v107 offset:1024\nv_mfma_f32_16x16x32_bf16 v[204:207], v[32:35], v[8:11], v[204:207]\nds_read_b64_tr_b16 v[92:93], v107 offset:8192\n"
      "v_mfma_f32_16x16x32_bf16 v[220:223], v[32:35], v[16:19], v[220:223]\nds_read_b128 v[8:11], v120 offset:0\nv_mfma_f32_16x16x32_bf16 v[220:223], v[36:39], v[20:23], v[220:223]\n"
      "ds_read_b128 v[12:15], v121 offset:0\nv_mfma_f32_16x16x32_bf16 v[224:227], v[44:47], v[20:23], v[224:227]\nds_read_b64_tr_b16 v[94:95], v107 offset:9216\n"
      "v_mfma_f32_16x16x32_bf16 v[224:227], v[40:43], v[16:19], v[224:227]\nv_mfma_f32_16x16x32_bf16 v[228:231], v[48:51], v[16:19], v[228:231]\n"
      "v_mfma_f32_16x16x32_bf16 v[228:231], v[52:55], v[20:23], v[228:231]\nv_mfma_f32_16x16x32_bf16 v[232:235], v[60:63], v[20:23], v[232:235]\ns_waitcnt lgkmcnt(0)\n"
      "v_mfma_f32_16x16x32_bf16 v[232:235], v[56:59], v[16:19], v[232:235]\nds_read_b128 v[16:19], v96 offset:0\nv_mfma_f32_16x16x32_bf16 v[124:127], v[64:67], v[0:3], v[124:127]\n"
      "ds_read_b128 v[20:23], v97 offset:0\nv_mfma_f32_16x16x32_bf16 v[124:127], v[68:71], v[4:7], v[124:127]\nds_read_b128 v[24:27], v96 offset:2048\n"
      "v_mfma_f32_16x16x32_bf16 v[128:131], v[76:79], v[4:7], v[128:131]\nds_read_b128 v[28:31], v97 offset:2048\nv_mfma_f32_16x16x32_bf16 v[128:131], v[72:75], v[0:3], v[128:131]\n"
      "v_mfma_f32_16x16x32_bf16 v[152:155], v[88:91], v[8:11], v[152:155]\nds_read_b128 v[0:3], v96 offset:4096\nv_mfma_f32_16x16x32_bf16 v[152:155], v[92:95], v[12:15], v[152:155]\n"
      "ds_read_b128 v[4:7], v97 offset:4096\nv_mfma_f32_16x16x32_bf16 v[148:151], v[84:87], v[12:15], v[148:151]\nv_mfma_f32_16x16x32_bf16 v[148:151], v[80:83], v[8:11], v[148:151]\n"
      "v_mfma_f32_16x16x32_bf16 v[144:147], v[72:75], v[8:11], v[144:147]\nv_mfma_f32_16x16x32_bf16 v[144:147], v[76:79], v[12:15], v[144:147]\n"
      "v_mfma_f32_16x16x32_bf16 v[140:143], v[68:71], v[12:15], v[140:143]\ns_waitcnt lgkmcnt(4)\nv_mfma_f32_16x16x32_bf16 v[140:143], v[64:67], v[8:11], v[140:143]\n"
      "v_mfma_f32_16x16x32_bf16 v[156:159], v[64:67], v[16:19], v[156:159]\nds_read_b128 v[8:11], v96 offset:6144\nv_mfma_f32_16x16x32_bf16 v[156:159], v[68:71], v[20:23], v[156:159]\n"
      "ds_read_b128 v[12:15], v97 offset:6144\nv_mfma_f32_16x16x32_bf16 v[160:163], v[76:79], v[20:23], v[160:163]\nv_mfma_f32_16x16x32_bf16 v[160:163], v[72:75], v[16:19], v[160:163]\n"
      "v_mfma_f32_16x16x32_bf16 v[164:167], v[80:83], v[16:19], v[164:167]\nv_mfma_f32_16x16x32_bf16 v[164:167], v[84:87], v[20:23], v[164:167]\n"
      "v_mfma_f32_16x16x32_bf16 v[168:171], v[92:95], v[20:23], v[168:171]\ns_waitcnt lgkmcnt(2)\nv_mfma_f32_16x16x32_bf16 v[168:171], v[88:91], v[16:19], v[168:171]\n"
      "v_mfma_f32_16x16x32_bf16 v[184:187], v[88:91], v[24:27], v[184:187]\nds_read_b128 v[16:19], v96 offset:8192\nv_mfma_f32_16x16x32_bf16 v[184:187], v[92:95], v[28:31], v[184:187]\n"
      "ds_read_b128 v[20:23], v97 offset:8192\nv_mfma_f32_16x16x32_bf16 v[180:183], v[84:87], v[28:31], v[180:183]\nv_mfma_f32_16x16x32_bf16 v[180:183], v[80:83], v[24:27], v[180:183]\n"
      "v_mfma_f32_16x16x32_bf16 v[176:179], v[72:75], v[24:27], v[176:179]\nv_mfma_f32_16x16x32_bf16 v[176:179], v[76:79], v[28:31], v[176:179]\ns_waitcnt vmcnt(0) lgkmcnt(0)\n"
      "v_mfma_f32_16x16x32_bf16 v[172:175], v[68:71], v[28:31], v[172:175]\ns_barrier\nv_mfma_f32_16x16x32_bf16 v[172:175], v[64:67], v[24:27], v[172:175]\n"
      "ds_read_b64_tr_b16 v[32:33], v104 offset:45056\nv_mfma_f32_16x16x32_bf16 v[188:191], v[64:67], v[0:3], v[188:191]\nds_read_b64_tr_b16 v[34:35], v104 offset:46080\n"
      "v_mfma_f32_16x16x32_bf16 v[188:191], v[68:71], v[4:7], v[188:191]\nds_read_b64_tr_b16 v[36:37], v104 offset:53248\nv_mfma_f32_16x16x32_bf16 v[192:195], v[76:79], v[4:7], v[192:195]\n"
      "ds_read_b64_tr_b16 v[38:39], v104 offset:54272\nv_mfma_f32_16x16x32_bf16 v[192:195], v[72:75], v[0:3], v[192:195]\nds_read_b64_tr_b16 v[40:41], v105 offset:45056\n"
      "v_mfma_f32_16x16x32_bf16 v[196:199], v[80:83], v[0:3], v[196:199]\nds_read_b64_tr_b16 v[42:43], v105 offset:46080\nv_mfma_f32_16x16x32_bf16 v[196:199], v[84:87], v[4:7], v[196:199]\n"
      "ds_read_b64_tr_b16 v[44:45], v105 offset:53248\nv_mfma_f32_16x16x32_bf16 v[200:203], v[92:95], v[4:7], v[200:203]\nds_read_b64_tr_b16 v[46:47], v105 offset:54272\n"
      "v_mfma_f32_16x16x32_bf16 v[200:203], v[88:91], v[0:3], v[200:203]\nds_read_b64_tr_b16 v[48:49], v106 offset:45056\nv_mfma_f32_16x16x32_bf16 v[216:219], v[88:91], v[8:11], v[216:219]\n"
      "ds_read_b128 v[0:3], v100 offset:45056\nv_mfma_f32_16x16x32_bf16 v[216:219], v[92:95], v[12:15], v[216:219]\nds_read_b128 v[4:7], v101 offset:45056\n"
      "v_mfma_f32_16x16x32_bf16 v[212:215], v[84:87], v[12:15], v[212:215]\nds_read_b64_tr_b16 v[50:51], v106 offset:46080\nv_mfma_f32_16x16x32_bf16 v[212:215], v[80:83], v[8:11], v[212:215]\n"
      "ds_read_b64_tr_b16 v[52:53], v106 offset:53248\nv_mfma_f32_16x16x32_bf16 v[208:211], v[72:75], v[8:11], v[208:211]\nds_read_b64_tr_b16 v[54:55], v106 offset:54272\n"
      "v_mfma_f32_16x16x32_bf16 v[208:211], v[76:79], v[12:15], v[208:211]\nds_read_b64_tr_b16 v[56:57], v107 offset:45056\nv_mfma_f32_16x16x32_bf16 v[204:207], v[68:71], v[12:15], v[204:207]\n"
      "ds_read_b64_tr_b16 v[58:59], v107 offset:46080\nv_mfma_f32_16x16x32_bf16 v[204:207], v[64:67], v[8:11], v[204:207]\nds_read_b64_tr_b16 v[60:61], v107 offset:53248\n"
      "v_mfma_f32_16x16x32_bf16 v[220:223], v[64:67], v[16:19], v[220:223]\nds_read_b128 v[8:11], v120 offset:45056\nv_mfma_f32_16x16x32_bf16 v[220:223], v[68:71], v[20:23], v[220:223]\n"
      "ds_read_b128 v[12:15], v121 offset:45056\nv_mfma_f32_16x16x32_bf16 v[224:227], v[76:79], v[20:23], v[224:227]\nds_read_b64_tr_b16 v[62:63], v107 offset:54272\n"
      "v_mfma_f32_16x16x32_bf16 v[224:227], v[72:75], v[16:19], v[224:227]\nv_mfma_f32_16x16x32_bf16 v[228:231], v[80:83], v[16:19], v[228:231]\n"
      "v_mfma_f32_16x16x32_bf16 v[228:231], v[84:87], v[20:23], v[228:231]\nv_mfma_f32_16x16x32_bf16 v[232:235], v[92:95], v[20:23], v[232:235]\ns_waitcnt lgkmcnt(0)\n"
      "v_mfma_f32_16x16x32_bf16 v[232:235], v[88:91], v[16:19], v[232:235]\nds_read_b128 v[16:19], v96 offset:45056\nv_mfma_f32_16x16x32_bf16 v[124:127], v[32:35], v[0:3], v[124:127]\n"
      "ds_read_b128 v[20:23], v97 offset:45056\nv_mfma_f32_16x16x32_bf16 v[124:127], v[36:39], v[4:7], v[124:127]\nds_read_b128 v[24:27], v96 offset:47104\n"
      "v_mfma_f32_16x16x32_bf16 v[128:131], v[44:47], v[4:7], v[128:131]\nds_read_b128 v[28:31], v97 offset:47104\nv_mfma_f32_16x16x32_bf16 v[128:131], v[40:43], v[0:3], v[128:131]\n"
      "v_mfma_f32_16x16x32_bf16 v[152:155], v[56:59], v[8:11], v[152:155]\nds_read_b128 v[0:3], v96 offset:49152\nv_mfma_f32_16x16x32_bf16 v[152:155], v[60:63], v[12:15], v[152:155]\n"
      "ds_read_b128 v[4:7], v97 offset:49152\nv_mfma_f32_16x16x32_bf16 v[148:151], v[52:55], v[12:15], v[148:151]\nv_mfma_f32_16x16x32_bf16 v[148:151], v[48:51], v[8:11], v[148:151]\n"
      "v_mfma_f32_16x16x32_bf16 v[144:147], v[40:43], v[8:11], v[144:147]\nv_mfma_f32_16x16x32_bf16 v[144:147], v[44:47], v[12:15], v[144:147]\n"
      "v_mfma_f32_16x16x32_bf16 v[140:143], v[36:39], v[12:15], v[140:143]\ns_waitcnt lgkmcnt(4)\nv_mfma_f32_16x16x32_bf16 v[140:143], v[32:35], v[8:11], v[140:143]\n"
      "v_mfma_f32_16x16x32_bf16 v[156:159], v[32:35], v[16:19], v[156:159]\nds_read_b128 v[8:11], v96 offset:51200\nv_mfma_f32_16x16x32_bf16 v[156:159], v[36:39], v[20:23], v[156:159]\n"
      "ds_read_b128 v[12:15], v97 offset:51200\nv_mfma_f32_16x16x32_bf16 v[160:163], v[44:47], v[20:23], v[160:163]\nv_mfma_f32_16x16x32_bf16 v[160:163], v[40:43], v[16:19], v[160:163]\n"
      "v_mfma_f32_16x16x32_bf16 v[164:167], v[48:51], v[16:19], v[164:167]\nv_mfma_f32_16x16x32_bf16 v[164:167], v[52:55], v[20:23], v[164:167]\n"
      "v_mfma_f32_16x16x32_bf16 v[168:171], v[60:63], v[20:23], v[168:171]\ns_waitcnt lgkmcnt(2)\nv_mfma_f32_16x16x32_bf16 v[168:171], v[56:59], v[16:19], v[168:171]\n"
      "v_mfma_f32_16x16x32_bf16 v[184:187], v[56:59], v[24:27], v[184:187]\nds_read_b128 v[16:19], v96 offset:53248\nv_mfma_f32_16x16x32_bf16 v[184:187], v[60:63], v[28:31], v[184:187]\n"
      "ds_read_b128 v[20:23], v97 offset:53248\nv_mfma_f32_16x16x32_bf16 v[180:183], v[52:55], v[28:31], v[180:183]\nv_mfma_f32_16x16x32_bf16 v[180:183], v[48:51], v[24:27], v[180:183]\n"
      "v_mfma_f32_16x16x32_bf16 v[176:179], v[40:43], v[24:27], v[176:179]\nv_mfma_f32_16x16x32_bf16 v[176:179], v[44:47], v[28:31], v[176:179]\ns_waitcnt vmcnt(0) lgkmcnt(0)\n"
      "v_mfma_f32_16x16x32_bf16 v[172:175], v[36:39], v[28:31], v[172:175]\ns_barrier\nv_mfma_f32_16x16x32_bf16 v[172:175], v[32:35], v[24:27], v[172:175]\n"
      "ds_read_b64_tr_b16 v[64:65], v108 offset:0\nv_mfma_f32_16x16x32_bf16 v[188:191], v[32:35], v[0:3], v[188:191]\nds_read_b64_tr_b16 v[66:67], v108 offset:1024\n"
      "v_mfma_f32_16x16x32_bf16 v[188:191], v[36:39], v[4:7], v[188:191]\nds_read_b64_tr_b16 v[68:69], v108 offset:8192\nv_mfma_f32_16x16x32_bf16 v[192:195], v[44:47], v[4:7], v[192:195]\n"
      "ds_read_b64_tr_b16 v[70:71], v108 offset:9216\nv_mfma_f32_16x16x32_bf16 v[192:195], v[40:43], v[0:3], v[192:195]\nds_read_b64_tr_b16 v[72:73], v109 offset:0\n"
      "v_mfma_f32_16x16x32_bf16 v[196:199], v[48:51], v[0:3], v[196:199]\nds_read_b64_tr_b16 v[74:75], v109 offset:1024\nv_mfma_f32_16x16x32_bf16 v[196:199], v[52:55], v[4:7], v[196:199]\n"
      "ds_read_b64_tr_b16 v[76:77], v109 offset:8192\nv_mfma_f32_16x16x32_bf16 v[200:203], v[60:63], v[4:7], v[200:203]\nds_read_b64_tr_b16 v[78:79], v109 offset:9216\n"
      "v_mfma_f32_16x16x32_bf16 v[200:203], v[56:59], v[0:3], v[200:203]\nds_read_b64_tr_b16 v[80:81], v110 offset:0\nv_mfma_f32_16x16x32_bf16 v[216:219], v[56:59], v[8:11], v[216:219]\n"
      "ds_read_b128 v[0:3], v102 offset:0\nv_mfma_f32_16x16x32_bf16 v[216:219], v[60:63], v[12:15], v[216:219]\nds_read_b128 v[4:7], v103 offset:0\n"
      "v_mfma_f32_16x16x32_bf16 v[212:215], v[52:55], v[12:15], v[212:215]\nds_read_b64_tr_b16 v[82:83], v110 offset:1024\nv_mfma_f32_16x16x32_bf16 v[212:215], v[48:51], v[8:11], v[212:215]\n"
      "ds_read_b64_tr_b16 v[84:85], v110 offset:8192\nv_mfma_f32_16x16x32_bf16 v[208:211], v[40:43], v[8:11], v[208:211]\nds_read_b64_tr_b16 v[86:87], v110 offset:9216\n"
      "v_mfma_f32_16x16x32_bf16 v[208:211], v[44:47], v[12:15], v[208:211]\nds_read_b64_tr_b16 v[88:89], v111 offset:0\nv_mfma_f32_16x16x32_bf16 v[204:207], v[36:39], v[12:15], v[204:207]\n"
      "ds_read_b64_tr_b16 v[90:91], v111 offset:1024\nv_mfma_f32_16x16x32_bf16 v[204:207], v[32:35], v[8:11], v[204:207]\nds_read_b64_tr_b16 v[92:93], v111 offset:8192\n"
      "v_mfma_f32_16x16x32_bf16 v[220:223], v[32:35], v[16:19], v[220:223]\nds_read_b128 v[8:11], v122 offset:0\nv_mfma_f32_16x16x32_bf16 v[220:223], v[36:39], v[20:23], v[220:223]\n"
      "ds_read_b128 v[12:15], v123 offset:0\nv_mfma_f32_16x16x32_bf16 v[224:227], v[44:47], v[20:23], v[224:227]\nds_read_b64_tr_b16 v[94:95], v111 offset:9216\n"
      "v_mfma_f32_16x16x32_bf16 v[224:227], v[40:43], v[16:19], v[224:227]\nv_mfma_f32_16x16x32_bf16 v[228:231], v[48:51], v[16:19], v[228:231]\n"
      "v_mfma_f32_16x16x32_bf16 v[228:231], v[52:55], v[20:23], v[228:231]\nv_mfma_f32_16x16x32_bf16 v[232:235], v[60:63], v[20:23], v[232:235]\ns_waitcnt lgkmcnt(0)\n"
      "v_mfma_f32_16x16x32_bf16 v[232:235], v[56:59], v[16:19], v[232:235]\nds_read_b128 v[16:19], v98 offset:0\nv_mfma_f32_16x16x32_bf16 v[124:127], v[64:67], v[0:3], v[124:127]\n"
      "ds_read_b128 v[20:23], v99 offset:0\nv_mfma_f32_16x16x32_bf16 v[124:127], v[68:71], v[4:7], v[124:127]\nds_read_b128 v[24:27], v98 offset:2048\n"
      "v_mfma_f32_16x16x32_bf16 v[128:131], v[76:79], v[4:7], v[128:131]\nds_read_b128 v[28:31], v99 offset:2048\nv_mfma_f32_16x16x32_bf16 v[128:131], v[72:75], v[0:3], v[128:131]\n"
      "v_mfma_f32_16x16x32_bf16 v[152:155], v[88:91], v[8:11], v[152:155]\nds_read_b128 v[0:3], v98 offset:4096\nv_mfma_f32_16x16x32_bf16 v[152:155], v[92:95], v[12:15], v[152:155]\n"
      "ds_read_b128 v[4:7], v99 offset:4096\nv_mfma_f32_16x16x32_bf16 v[148:151], v[84:87], v[12:15], v[148:151]\nv_mfma_f32_16x16x32_bf16 v[148:151], v[80:83], v[8:11], v[148:151]\n"
      "v_mfma_f32_16x16x32_bf16 v[144:147], v[72:75], v[8:11], v[144:147]\nv_mfma_f32_16x16x32_bf16 v[144:147], v[76:79], v[12:15], v[144:147]\n"
      "v_mfma_f32_16x16x32_bf16 v[140:143], v[68:71], v[12:15], v[140:143]\ns_waitcnt lgkmcnt(4)\nv_mfma_f32_16x16x32_bf16 v[140:143], v[64:67], v[8:11], v[140:143]\n"
      "v_mfma_f32_16x16x32_bf16 v[156:159], v[64:67], v[16:19], v[156:159]\nds_read_b128 v[8:11], v98 offset:6144\nv_mfma_f32_16x16x32_bf16 v[156:159], v[68:71], v[20:23], v[156:159]\n"
      "ds_read_b128 v[12:15], v99 offset:6144\nv_mfma_f32_16x16x32_bf16 v[160:163], v[76:79], v[20:23], v[160:163]\nv_mfma_f32_16x16x32_bf16 v[160:163], v[72:75], v[16:19], v[160:163]\n"
      "v_mfma_f32_16x16x32_bf16 v[164:167], v[80:83], v[16:19], v[164:167]\nv_mfma_f32_16x16x32_bf16 v[164:167], v[84:87], v[20:23], v[164:167]\n"
      "v_mfma_f32_16x16x32_bf16 v[168:171], v[92:95], v[20:23], v[168:171]\ns_waitcnt lgkmcnt(2)\nv_mfma_f32_16x16x32_bf16 v[168:171], v[88:91], v[16:19], v[168:171]\n"
      "v_mfma_f32_16x16x32_bf16 v[184:187], v[88:91], v[24:27], v[184:187]\nds_read_b128 v[16:19], v98 offset:8192\nv_mfma_f32_16x16x32_bf16 v[184:187], v[92:95], v[28:31], v[184:187]\n"
      "ds_read_b128 v[20:23], v99 offset:8192\nv_mfma_f32_16x16x32_bf16 v[180:183], v[84:87], v[28:31], v[180:183]\nv_mfma_f32_16x16x32_bf16 v[180:183], v[80:83], v[24:27], v[180:183]\n"
      "v_mfma_f32_16x16x32_bf16 v[176:179], v[72:75], v[24:27], v[176:179]\nv_mfma_f32_16x16x32_bf16 v[176:179], v[76:79], v[28:31], v[176:179]\ns_waitcnt vmcnt(0) lgkmcnt(0)\n"
      "v_mfma_f32_16x16x32_bf16 v[172:175], v[68:71], v[28:31], v[172:175]\ns_barrier\nv_mfma_f32_16x16x32_bf16 v[172:175], v[64:67], v[24:27], v[172:175]\n"
      "ds_read_b64_tr_b16 v[32:33], v104 offset:0\nv_mfma_f32_16x16x32_bf16 v[188:191], v[64:67], v[0:3], v[188:191]\nds_read_b64_tr_b16 v[34:35], v104 offset:1024\n"
      "v_mfma_f32_16x16x32_bf16 v[188:191], v[68:71], v[4:7], v[188:191]\nds_read_b64_tr_b16 v[36:37], v104 offset:8192\nv_mfma_f32_16x16x32_bf16 v[192:195], v[76:79], v[4:7], v[192:195]\n"
      "ds_read_b64_tr_b16 v[38:39], v104 offset:9216\nv_mfma_f32_16x16x32_bf16 v[192:195], v[72:75], v[0:3], v[192:195]\nds_read_b64_tr_b16 v[40:41], v105 offset:0\n"
      "v_mfma_f32_16x16x32_bf16 v[196:199], v[80:83], v[0:3], v[196:199]\nds_read_b64_tr_b16 v[42:43], v105 offset:1024\nv_mfma_f32_16x16x32_bf16 v[196:199], v[84:87], v[4:7], v[196:199]\n"
      "ds_read_b64_tr_b16 v[44:45], v105 offset:8192\nv_mfma_f32_16x16x32_bf16 v[200:203], v[92:95], v[4:7], v[200:203]\nds_read_b64_tr_b16 v[46:47], v105 offset:9216\n"
      "v_mfma_f32_16x16x32_bf16 v[200:203], v[88:91], v[0:3], v[200:203]\nds_read_b64_tr_b16 v[48:49], v106 offset:0\nv_mfma_f32_16x16x32_bf16 v[216:219], v[88:91], v[8:11], v[216:219]\n"
      "ds_read_b128 v[0:3], v100 offset:0\nv_mfma_f32_16x16x32_bf16 v[216:219], v[92:95], v[12:15], v[216:219]\nds_read_b128 v[4:7], v101 offset:0\n"
      "v_mfma_f32_16x16x32_bf16 v[212:215], v[84:87], v[12:15], v[212:215]\nds_read_b64_tr_b16 v[50:51], v106 offset:1024\nv_mfma_f32_16x16x32_bf16 v[212:215], v[80:83], v[8:11], v[212:215]\n"
      "ds_read_b64_tr_b16 v[52:53], v106 offset:8192\nv_mfma_f32_16x16x32_bf16 v[208:211], v[72:75], v[8:11], v[208:211]\nds_read_b64_tr_b16 v[54:55], v106 offset:9216\n"
      "v_mfma_f32_16x16x32_bf16 v[208:211], v[76:79], v[12:15], v[208:211]\nds_read_b64_tr_b16 v[56:57], v107 offset:0\nv_mfma_f32_16x16x32_bf16 v[204:207], v[68:71], v[12:15], v[204:207]\n"
      "ds_read_b64_tr_b16 v[58:59], v107 offset:1024\nv_mfma_f32_16x16x32_bf16 v[204:207], v[64:67], v[8:11], v[204:207]\nds_read_b64_tr_b16 v[60:61], v107 offset:8192\n"
      "v_mfma_f32_16x16x32_bf16 v[220:223], v[64:67], v[16:19], v[220:223]\nds_read_b128 v[8:11], v120 offset:0\nv_mfma_f32_16x16x32_bf16 v[220:223], v[68:71], v[20:23], v[220:223]\n"
      "ds_read_b128 v[12:15], v121 offset:0\nv_mfma_f32_16x16x32_bf16 v[224:227], v[76:79], v[20:23], v[224:227]\nds_read_b64_tr_b16 v[62:63], v107 offset:9216\n"
      "v_mfma_f32_16x16x32_bf16 v[224:227], v[72:75], v[16:19], v[224:227]\nv_mfma_f32_16x16x32_bf16 v[228:231], v[80:83], v[16:19], v[228:231]\n"
      "v_mfma_f32_16x16x32_bf16 v[228:231], v[84:87], v[20:23], v[228:231]\nv_mfma_f32_16x16x32_bf16 v[232:235], v[92:95], v[20:23], v[232:235]\ns_waitcnt lgkmcnt(0)\n"
      "v_mfma_f32_16x16x32_bf16 v[232:235], v[88:91], v[16:19], v[232:235]\nds_read_b128 v[16:19], v96 offset:0\nv_mfma_f32_16x16x32_bf16 v[124:127], v[32:35], v[0:3], v[124:127]\n"
      "ds_read_b128 v[20:23], v97 offset:0\nv_mfma_f32_16x16x32_bf16 v[124:127], v[36:39], v[4:7], v[124:127]\nds_read_b128 v[24:27], v96 offset:2048\n"
      "v_mfma_f32_16x16x32_bf16 v[128:131], v[44:47], v[4:7], v[128:131]\nds_read_b128 v[28:31], v97 offset:2048\nv_mfma_f32_16x16x32_bf16 v[128:131], v[40:43], v[0:3], v[128:131]\n"
      "v_mfma_f32_16x16x32_bf16 v[152:155], v[56:59], v[8:11], v[152:155]\nds_read_b128 v[0:3], v96 offset:4096\nv_mfma_f32_16x16x32_bf16 v[152:155], v[60:63], v[12:15], v[152:155]\n"
      "ds_read_b128 v[4:7], v97 offset:4096\nv_mfma_f32_16x16x32_bf16 v[148:151], v[52:55], v[12:15], v[148:151]\nv_mfma_f32_16x16x32_bf16 v[148:151], v[48:51], v[8:11], v[148:151]\n"
      "v_mfma_f32_16x16x32_bf16 v[144:147], v[40:43], v[8:11], v[144:147]\nv_mfma_f32_16x16x32_bf16 v[144:147], v[44:47], v[12:15], v[144:147]\n"
      "v_mfma_f32_16x16x32_bf16 v[140:143], v[36:39], v[12:15], v[140:143]\ns_waitcnt lgkmcnt(4)\nv_mfma_f32_16x16x32_bf16 v[140:143], v[32:35], v[8:11], v[140:143]\n"
      "v_mfma_f32_16x16x32_bf16 v[156:159], v[32:35], v[16:19], v[156:159]\nds_read_b128 v[8:11], v96 offset:6144\nv_mfma_f32_16x16x32_bf16 v[156:159], v[36:39], v[20:23], v[156:159]\n"
      "ds_read_b128 v[12:15], v97 offset:6144\nv_mfma_f32_16x16x32_bf16 v[160:163], v[44:47], v[20:23], v[160:163]\nv_mfma_f32_16x16x32_bf16 v[160:163], v[40:43], v[16:19], v[160:163]\n"
      "v_mfma_f32_16x16x32_bf16 v[164:167], v[48:51], v[16:19], v[164:167]\nv_mfma_f32_16x16x32_bf16 v[164:167], v[52:55], v[20:23], v[164:167]\n"
      "v_mfma_f32_16x16x32_bf16 v[168:171], v[60:63], v[20:23], v[168:171]\ns_waitcnt lgkmcnt(2)\nv_mfma_f32_16x16x32_bf16 v[168:171], v[56:59], v[16:19], v[168:171]\n"
      "v_mfma_f32_16x16x32_bf16 v[184:187], v[56:59], v[24:27], v[184:187]\nds_read_b128 v[16:19], v96 offset:8192\nv_mfma_f32_16x16x32_bf16 v[184:187], v[60:63], v[28:31], v[184:187]\n"
      "ds_read_b128 v[20:23], v97 offset:8192\nv_mfma_f32_16x16x32_bf16 v[180:183], v[52:55], v[28:31], v[180:183]\nv_mfma_f32_16x16x32_bf16 v[180:183], v[48:51], v[24:27], v[180:183]\n"
      "v_mfma_f32_16x16x32_bf16 v[176:179], v[40:43], v[24:27], v[176:179]\nv_mfma_f32_16x16x32_bf16 v[176:179], v[44:47], v[28:31], v[176:179]\ns_waitcnt vmcnt(0) lgkmcnt(0)\n"
      "v_mfma_f32_16x16x32_bf16 v[172:175], v[36:39], v[28:31], v[172:175]\ns_barrier\nv_mfma_f32_16x16x32_bf16 v[172:175], v[32:35], v[24:27], v[172:175]\n"
      "ds_read_b64_tr_b16 v[64:65], v104 offset:45056\nv_mfma_f32_16x16x32_bf16 v[188:191], v[32:35], v[0:3], v[188:191]\nds_read_b64_tr_b16 v[66:67], v104 offset:46080\n"
      "v_mfma_f32_16x16x32_bf16 v[188:191], v[36:39], v[4:7], v[188:191]\nds_read_b64_tr_b16 v[68:69], v104 offset:53248\nv_mfma_f32_16x16x32_bf16 v[192:195], v[44:47], v[4:7], v[192:195]\n"
      "ds_read_b64_tr_b16 v[70:71], v104 offset:54272\nv_mfma_f32_16x16x32_bf16 v[192:195], v[40:43], v[0:3], v[192:195]\nds_read_b64_tr_b16 v[72:73], v105 offset:45056\n"
      "v_mfma_f32_16x16x32_bf16 v[196:199], v[48:51], v[0:3], v[196:199]\nds_read_b64_tr_b16 v[74:75], v105 offset:46080\nv_mfma_f32_16x16x32_bf16 v[196:199], v[52:55], v[4:7], v[196:199]\n"
      "ds_read_b64_tr_b16 v[76:77], v105 offset:53248\nv_mfma_f32_16x16x32_bf16 v[200:203], v[60:63], v[4:7], v[200:203]\nds_read_b64_tr_b16 v[78:79], v105 offset:54272\n"
      "v_mfma_f32_16x16x32_bf16 v[200:203], v[56:59], v[0:3], v[200:203]\nds_read_b64_tr_b16 v[80:81], v106 offset:45056\nv_mfma_f32_16x16x32_bf16 v[216:219], v[56:59], v[8:11], v[216:219]\n"
      "ds_read_b128 v[0:3], v100 offset:45056\nv_mfma_f32_16x16x32_bf16 v[216:219], v[60:63], v[12:15], v[216:219]\nds_read_b128 v[4:7], v101 offset:45056\n"
      "v_mfma_f32_16x16x32_bf16 v[212:215], v[52:55], v[12:15], v[212:215]\nds_read_b64_tr_b16 v[82:83], v106 offset:46080\nv_mfma_f32_16x16x32_bf16 v[212:215], v[48:51], v[8:11], v[212:215]\n"
      "ds_read_b64_tr_b16 v[84:85], v106 offset:53248\nv_mfma_f32_16x16x32_bf16 v[208:211], v[40:43], v[8:11], v[208:211]\nds_read_b64_tr_b16 v[86:87], v106 offset:54272\n"
      "v_mfma_f32_16x16x32_bf16 v[208:211], v[44:47], v[12:15], v[208:211]\nds_read_b64_tr_b16 v[88:89], v107 offset:45056\nv_mfma_f32_16x16x32_bf16 v[204:207], v[36:39], v[12:15], v[204:207]\n"
      "ds_read_b64_tr_b16 v[90:91], v107 offset:46080\nv_mfma_f32_16x16x32_bf16 v[204:207], v[32:35], v[8:11], v[204:207]\nds_read_b64_tr_b16 v[92:93], v107 offset:53248\n"
      "v_mfma_f32_16x16x32_bf16 v[220:223], v[32:35], v[16:19], v[220:223]\nds_read_b128 v[8:11], v120 offset:45056\nv_mfma_f32_16x16x32_bf16 v[220:223], v[36:39], v[20:23], v[220:223]\n"
      "ds_read_b128 v[12:15], v121 offset:45056\nv_mfma_f32_16x16x32_bf16 v[224:227], v[44:47], v[20:23], v[224:227]\nds_read_b64_tr_b16 v[94:95], v107 offset:54272\n"
      "v_mfma_f32_16x16x32_bf16 v[224:227], v[40:43], v[16:19], v[224:227]\nv_mfma_f32_16x16x32_bf16 v[228:231], v[48:51], v[16:19], v[228:231]\ns_sub_u32 s58, s58, 1\ns_cmp_lg_u32 s58, 0\n"
      "v_mfma_f32_16x16x32_bf16 v[228:231], v[52:55], v[20:23], v[228:231]\nv_mfma_f32_16x16x32_bf16 v[232:235], v[60:63], v[20:23], v[232:235]\ns_waitcnt lgkmcnt(0)\n"
      "v_mfma_f32_16x16x32_bf16 v[232:235], v[56:59], v[16:19], v[232:235]\nds_read_b128 v[16:19], v96 offset:45056\ns_cbranch_scc1 .Lgl13\n"
      "v_mfma_f32_16x16x32_bf16 v[124:127], v[64:67], v[0:3], v[124:127]\nds_read_b128 v[20:23], v97 offset:45056\nv_cmp_eq_u32 s[62:63], 0, v240\n"
      "v_mfma_f32_16x16x32_bf16 v[124:127], v[68:71], v[4:7], v[124:127]\nds_read_b128 v[24:27], v96 offset:47104\nv_mov_b32 v253, 262144\n"
      "v_mfma_f32_16x16x32_bf16 v[128:131], v[76:79], v[4:7], v[128:131]\nds_read_b128 v[28:31], v97 offset:47104\nv_mov_b32 v248, 162048\n"
      "v_mfma_f32_16x16x32_bf16 v[128:131], v[72:75], v[0:3], v[128:131]\nv_mov_b32 v241, 1\nv_cndmask_b32 v253, v253, v248, s[62:63]\n"
      "v_mfma_f32_16x16x32_bf16 v[152:155], v[88:91], v[8:11], v[152:155]\nds_read_b128 v[0:3], v96 offset:49152\nv_lshrrev_b32 v112, 4, v240\n"
      "v_mfma_f32_16x16x32_bf16 v[152:155], v[92:95], v[12:15], v[152:155]\nds_read_b128 v[4:7], v97 offset:49152\nv_bfe_u32 v113, v240, 2, 2\n"
      "v_mfma_f32_16x16x32_bf16 v[148:151], v[84:87], v[12:15], v[148:151]\nv_and_b32 v114, 3, v240\nv_mul_u32_u24 v114, 0x80008, v114\n"
      "v_mfma_f32_16x16x32_bf16 v[148:151], v[80:83], v[8:11], v[148:151]\nv_add_u32 v244, v112, v113\nv_and_b32 v244, 3, v244\nv_mfma_f32_16x16x32_bf16 v[144:147], v[72:75], v[8:11], v[144:147]\n"
      "v_add_u32 v116, 22, v113\nv_cmp_gt_u32 s[62:63], 2, v113\nv_mfma_f32_16x16x32_bf16 v[144:147], v[76:79], v[12:15], v[144:147]\nv_and_b32 v113, 60, v240\ns_mov_b32 s42, 0\n"
      "v_mfma_f32_16x16x32_bf16 v[140:143], v[68:71], v[12:15], v[140:143]\ns_waitcnt lgkmcnt(4)\nv_cndmask_b32 v116, v116, v244, s[62:63]\n"
      "v_mfma_f32_16x16x32_bf16 v[140:143], v[64:67], v[8:11], v[140:143]\nv_lshl_or_b32 v244, v244, 16, v116\nv_lshl_add_u32 v244, v244, 5, v114\n"
      "v_mfma_f32_16x16x32_bf16 v[156:159], v[64:67], v[16:19], v[156:159]\nds_read_b128 v[8:11], v96 offset:51200\nv_lshl_add_u32 v252, v240, 4, s42\n"
      "v_mfma_f32_16x16x32_bf16 v[156:159], v[68:71], v[20:23], v[156:159]\nds_read_b128 v[12:15], v97 offset:51200\ns_mov_b32 s42, 0x76325410\n"
      "v_mfma_f32_16x16x32_bf16 v[160:163], v[76:79], v[20:23], v[160:163]\ns_mov_b32 s43, 0x21005410\nv_lshrrev_b64 v[116:117], v113, s[42:43]\n"
      "v_mfma_f32_16x16x32_bf16 v[160:163], v[72:75], v[16:19], v[160:163]\nv_and_b32 v116, 7, v116\nv_lshl_add_u32 v116, v112, 3, v116\n"
      "v_mfma_f32_16x16x32_bf16 v[164:167], v[80:83], v[16:19], v[164:167]\ns_mov_b32 s42, 0x54107632\ns_mov_b32 s43, 0x21107632\n"
      "v_mfma_f32_16x16x32_bf16 v[164:167], v[84:87], v[20:23], v[164:167]\nv_lshrrev_b64 v[118:119], v113, s[42:43]\nv_and_b32 v118, 7, v118\n"
      "v_mfma_f32_16x16x32_bf16 v[168:171], v[92:95], v[20:23], v[168:171]\ns_waitcnt lgkmcnt(2)\nv_lshl_add_u32 v118, v112, 3, v118\n"
      "v_mfma_f32_16x16x32_bf16 v[168:171], v[88:91], v[16:19], v[168:171]\nv_lshl_or_b32 v245, v118, 16, v116\nv_lshl_add_u32 v245, v245, 5, v114\n"
      "v_mfma_f32_16x16x32_bf16 v[184:187], v[88:91], v[24:27], v[184:187]\nds_read_b128 v[16:19], v96 offset:53248\nv_and_b32 v113, 1, v240\n"
      "v_mfma_f32_16x16x32_bf16 v[184:187], v[92:95], v[28:31], v[184:187]\nds_read_b128 v[20:23], v97 offset:53248\nv_mul_u32_u24 v113, 0xaaaa, v113\n"
      "v_mfma_f32_16x16x32_bf16 v[180:183], v[84:87], v[28:31], v[180:183]\nv_add_u32 v242, 0x4444, v113\ns_lshl_b32 s42, s36, 4\n"
      "v_mfma_f32_16x16x32_bf16 v[180:183], v[80:83], v[24:27], v[180:183]\ns_add_u32 s42, s42, 160640\nv_lshl_add_u32 v243, v112, 2, s42\n"
      "v_mfma_f32_16x16x32_bf16 v[176:179], v[72:75], v[24:27], v[176:179]\nv_and_b32 v112, 1, v112\nv_lshl_add_u32 v112, v112, 3, s24\n"
      "v_mfma_f32_16x16x32_bf16 v[176:179], v[76:79], v[28:31], v[176:179]\ns_waitcnt vmcnt(0) lgkmcnt(0)\ns_sub_u32 s42, s64, 1\n"
      "v_mfma_f32_16x16x32_bf16 v[172:175], v[68:71], v[28:31], v[172:175]\ns_barrier\nv_min_u32 v112, s42, v112\nv_mfma_f32_16x16x32_bf16 v[172:175], v[64:67], v[24:27], v[172:175]\n"
      "ds_read_b64_tr_b16 v[32:33], v108 offset:0\nv_lshlrev_b32 v112, 3, v112\nv_mfma_f32_16x16x32_bf16 v[188:191], v[64:67], v[0:3], v[188:191]\nds_read_b64_tr_b16 v[34:35], v108 offset:1024\n"
      "v_and_b32 v113, 15, v240\nv_mfma_f32_16x16x32_bf16 v[188:191], v[68:71], v[4:7], v[188:191]\nds_read_b64_tr_b16 v[36:37], v108 offset:8192\nv_lshl_add_u32 v250, v112, 1, v113\n"
      "v_mfma_f32_16x16x32_bf16 v[192:195], v[76:79], v[4:7], v[192:195]\nds_read_b64_tr_b16 v[38:39], v108 offset:9216\ns_mov_b32 s42, 136192\n"
      "v_mfma_f32_16x16x32_bf16 v[192:195], v[72:75], v[0:3], v[192:195]\nds_read_b64_tr_b16 v[40:41], v109 offset:0\nv_lshl_add_u32 v250, v250, 2, s42\n"
      "v_mfma_f32_16x16x32_bf16 v[196:199], v[80:83], v[0:3], v[196:199]\nds_read_b64_tr_b16 v[42:43], v109 offset:1024\nv_min_u32 v113, 12, v113\n"
      "v_mfma_f32_16x16x32_bf16 v[196:199], v[84:87], v[4:7], v[196:199]\nds_read_b64_tr_b16 v[44:45], v109 offset:8192\ns_lshl_b32 s42, s30, 7\n"
      "v_mfma_f32_16x16x32_bf16 v[200:203], v[92:95], v[4:7], v[200:203]\nds_read_b64_tr_b16 v[46:47], v109 offset:9216\nv_lshl_add_u32 v113, v113, 15, s42\n"
      "v_mfma_f32_16x16x32_bf16 v[200:203], v[88:91], v[0:3], v[200:203]\nds_read_b64_tr_b16 v[48:49], v110 offset:0\nv_add_u32 v249, v113, v112\n"
      "v_mfma_f32_16x16x32_bf16 v[216:219], v[88:91], v[8:11], v[216:219]\nds_read_b128 v[0:3], v102 offset:0\nv_mfma_f32_16x16x32_bf16 v[216:219], v[92:95], v[12:15], v[216:219]\n"
      "ds_read_b128 v[4:7], v103 offset:0\nv_mfma_f32_16x16x32_bf16 v[212:215], v[84:87], v[12:15], v[212:215]\nds_read_b64_tr_b16 v[50:51], v110 offset:1024\n"
      "v_mfma_f32_16x16x32_bf16 v[212:215], v[80:83], v[8:11], v[212:215]\nds_read_b64_tr_b16 v[52:53], v110 offset:8192\nv_mfma_f32_16x16x32_bf16 v[208:211], v[72:75], v[8:11], v[208:211]\n"
      "ds_read_b64_tr_b16 v[54:55], v110 offset:9216\nv_mfma_f32_16x16x32_bf16 v[208:211], v[76:79], v[12:15], v[208:211]\nds_read_b64_tr_b16 v[56:57], v111 offset:0\n"
      "v_mfma_f32_16x16x32_bf16 v[204:207], v[68:71], v[12:15], v[204:207]\nds_read_b64_tr_b16 v[58:59], v111 offset:1024\nv_mfma_f32_16x16x32_bf16 v[204:207], v[64:67], v[8:11], v[204:207]\n"
      "ds_read_b64_tr_b16 v[60:61], v111 offset:8192\nv_mfma_f32_16x16x32_bf16 v[220:223], v[64:67], v[16:19], v[220:223]\nds_read_b128 v[8:11], v122 offset:0\n"
      "v_mfma_f32_16x16x32_bf16 v[220:223], v[68:71], v[20:23], v[220:223]\nds_read_b128 v[12:15], v123 offset:0\nv_mfma_f32_16x16x32_bf16 v[224:227], v[76:79], v[20:23], v[224:227]\n"
      "ds_read_b64_tr_b16 v[62:63], v111 offset:9216\nv_mfma_f32_16x16x32_bf16 v[224:227], v[72:75], v[16:19], v[224:227]\nv_mfma_f32_16x16x32_bf16 v[228:231], v[80:83], v[16:19], v[228:231]\n"
      "v_mfma_f32_16x16x32_bf16 v[228:231], v[84:87], v[20:23], v[228:231]\nv_mfma_f32_16x16x32_bf16 v[232:235], v[92:95], v[20:23], v[232:235]\ns_waitcnt lgkmcnt(0)\n"
      "v_mfma_f32_16x16x32_bf16 v[232:235], v[88:91], v[16:19], v[232:235]\nds_read_b128 v[16:19], v98 offset:0\nv_mfma_f32_16x16x32_bf16 v[124:127], v[32:35], v[0:3], v[124:127]\n"
      "ds_read_b128 v[20:23], v99 offset:0\nv_mfma_f32_16x16x32_bf16 v[124:127], v[36:39], v[4:7], v[124:127]\nds_read_b128 v[24:27], v98 offset:2048\n"
      "v_mfma_f32_16x16x32_bf16 v[128:131], v[44:47], v[4:7], v[128:131]\nds_read_b128 v[28:31], v99 offset:2048\nv_mfma_f32_16x16x32_bf16 v[128:131], v[40:43], v[0:3], v[128:131]\n"
      "v_mfma_f32_16x16x32_bf16 v[152:155], v[56:59], v[8:11], v[152:155]\nds_read_b128 v[0:3], v98 offset:4096\nv_mfma_f32_16x16x32_bf16 v[152:155], v[60:63], v[12:15], v[152:155]\n"
      "ds_read_b128 v[4:7], v99 offset:4096\nv_mfma_f32_16x16x32_bf16 v[148:151], v[52:55], v[12:15], v[148:151]\nv_mfma_f32_16x16x32_bf16 v[148:151], v[48:51], v[8:11], v[148:151]\n"
      "v_mfma_f32_16x16x32_bf16 v[144:147], v[40:43], v[8:11], v[144:147]\nv_mfma_f32_16x16x32_bf16 v[144:147], v[44:47], v[12:15], v[144:147]\n"
      "v_mfma_f32_16x16x32_bf16 v[140:143], v[36:39], v[12:15], v[140:143]\ns_waitcnt lgkmcnt(4)\nv_mfma_f32_16x16x32_bf16 v[140:143], v[32:35], v[8:11], v[140:143]\n"
      "v_mfma_f32_16x16x32_bf16 v[156:159], v[32:35], v[16:19], v[156:159]\nds_read_b128 v[8:11], v98 offset:6144\nv_mfma_f32_16x16x32_bf16 v[156:159], v[36:39], v[20:23], v[156:159]\n"
      "ds_read_b128 v[12:15], v99 offset:6144\nv_mfma_f32_16x16x32_bf16 v[160:163], v[44:47], v[20:23], v[160:163]\nv_mfma_f32_16x16x32_bf16 v[160:163], v[40:43], v[16:19], v[160:163]\n"
      "v_mfma_f32_16x16x32_bf16 v[164:167], v[48:51], v[16:19], v[164:167]\nv_mfma_f32_16x16x32_bf16 v[164:167], v[52:55], v[20:23], v[164:167]\n"
      "v_mfma_f32_16x16x32_bf16 v[168:171], v[60:63], v[20:23], v[168:171]\ns_waitcnt lgkmcnt(2)\nv_mfma_f32_16x16x32_bf16 v[168:171], v[56:59], v[16:19], v[168:171]\n"
      "v_mfma_f32_16x16x32_bf16 v[184:187], v[56:59], v[24:27], v[184:187]\nds_read_b128 v[16:19], v98 offset:8192\nv_mfma_f32_16x16x32_bf16 v[184:187], v[60:63], v[28:31], v[184:187]\n"
      "ds_read_b128 v[20:23], v99 offset:8192\nv_mfma_f32_16x16x32_bf16 v[180:183], v[52:55], v[28:31], v[180:183]\nv_mfma_f32_16x16x32_bf16 v[180:183], v[48:51], v[24:27], v[180:183]\n"
      "v_mfma_f32_16x16x32_bf16 v[176:179], v[40:43], v[24:27], v[176:179]\nv_mfma_f32_16x16x32_bf16 v[176:179], v[44:47], v[28:31], v[176:179]\ns_waitcnt vmcnt(0) lgkmcnt(0)\n"
      "v_mfma_f32_16x16x32_bf16 v[172:175], v[36:39], v[28:31], v[172:175]\ns_barrier\nv_mfma_f32_16x16x32_bf16 v[172:175], v[32:35], v[24:27], v[172:175]\nds_read_b128 v[72:75], v252 offset:0\n"
      "v_lshl_add_u32 v246, v240, 1, v248\nv_mfma_f32_16x16x32_bf16 v[188:191], v[32:35], v[0:3], v[188:191]\nds_read_b128 v[24:27], v236 offset:512\nds_read_u16 v251, v246 offset:64\n"
      "v_mfma_f32_16x16x32_bf16 v[188:191], v[36:39], v[4:7], v[188:191]\nds_read_b128 v[28:31], v236 offset:576\nds_read_b32 v247, v248 offset:24\n"
      "v_mfma_f32_16x16x32_bf16 v[192:195], v[44:47], v[4:7], v[192:195]\nds_read_b128 v[76:79], v252 offset:1024\nv_and_b32 v246, 48, v240\n"
      "v_mfma_f32_16x16x32_bf16 v[192:195], v[40:43], v[0:3], v[192:195]\nds_read_b128 v[80:83], v252 offset:2048\ns_mov_b32 s42, 42240\n"
      "v_mfma_f32_16x16x32_bf16 v[196:199], v[48:51], v[0:3], v[196:199]\nds_read_b128 v[84:87], v252 offset:3072\nv_lshl_add_u32 v246, v246, 2, s42\n"
      "v_mfma_f32_16x16x32_bf16 v[196:199], v[52:55], v[4:7], v[196:199]\nds_read_b128 v[88:91], v252 offset:4096\ns_mov_b32 s58, 0xbdd2d3e7\n"
      "v_mfma_f32_16x16x32_bf16 v[200:203], v[60:63], v[4:7], v[200:203]\nds_read_b128 v[92:95], v252 offset:5120\ns_waitcnt lgkmcnt(4)\n"
      "v_mfma_f32_16x16x32_bf16 v[200:203], v[56:59], v[0:3], v[200:203]\nds_read_b128 v[96:99], v252 offset:6144\nv_readfirstlane_b32 s39, v247\n"
      "v_mfma_f32_16x16x32_bf16 v[216:219], v[56:59], v[8:11], v[216:219]\nds_read_b128 v[0:3], v236 offset:0\nv_readlane_b32 s55, v251, 0\n"
      "v_mfma_f32_16x16x32_bf16 v[216:219], v[60:63], v[12:15], v[216:219]\nds_read_b128 v[4:7], v236 offset:64\ns_setprio 3\nv_mfma_f32_16x16x32_bf16 v[212:215], v[52:55], v[12:15], v[212:215]\n"
      "ds_read_b128 v[100:103], v252 offset:7168\ns_xor_b32 s40, s39, 0xa5c30f96\nv_mfma_f32_16x16x32_bf16 v[212:215], v[48:51], v[8:11], v[212:215]\nds_read_b128 v[104:107], v252 offset:8192\n"
      "s_mul_i32 s41, s39, 0x9E3779B1\nv_mfma_f32_16x16x32_bf16 v[208:211], v[40:43], v[8:11], v[208:211]\nds_read_b128 v[108:111], v252 offset:9216\ns_lshr_b32 s41, s41, 24\n"
      "v_mfma_f32_16x16x32_bf16 v[208:211], v[44:47], v[12:15], v[208:211]\nds_read_b128 v[64:67], v237 offset:512\nv_mfma_f32_16x16x32_bf16 v[204:207], v[36:39], v[12:15], v[204:207]\n"
      "ds_read_b128 v[68:71], v237 offset:576\nv_mfma_f32_16x16x32_bf16 v[204:207], v[32:35], v[8:11], v[204:207]\nds_read_b128 v[112:115], v252 offset:10240\n"
      "v_mfma_f32_16x16x32_bf16 v[220:223], v[32:35], v[16:19], v[220:223]\nds_read_b128 v[8:11], v237 offset:0\nv_mfma_f32_16x16x32_bf16 v[220:223], v[36:39], v[20:23], v[220:223]\n"
      "ds_read_b128 v[12:15], v237 offset:64\nv_mfma_f32_16x16x32_bf16 v[224:227], v[44:47], v[20:23], v[224:227]\nds_read_b128 v[116:119], v252 offset:11264\n"
      "v_mfma_f32_16x16x32_bf16 v[224:227], v[40:43], v[16:19], v[224:227]\nds_read_b128 v[120:123], v252 offset:12288\n"
      "v_add_u32_sdwa v239, s49, v245 dst_sel:DWORD dst_unused:UNUSED_PAD src0_sel:DWORD src1_sel:WORD_1\nv_mfma_f32_16x16x32_bf16 v[228:231], v[48:51], v[16:19], v[228:231]\n"
      "ds_read_b128 v[132:135], v252 offset:15360\nv_add_u32_sdwa v236, s49, v244 dst_sel:DWORD dst_unused:UNUSED_PAD src0_sel:DWORD src1_sel:WORD_0\n"
      "v_mfma_f32_16x16x32_bf16 v[228:231], v[52:55], v[20:23], v[228:231]\nds_read_b128 v[136:139], v252 offset:16384\n"
      "v_add_u32_sdwa v237, s49, v244 dst_sel:DWORD dst_unused:UNUSED_PAD src0_sel:DWORD src1_sel:WORD_1\nv_mfma_f32_16x16x32_bf16 v[232:235], v[60:63], v[20:23], v[232:235]\n"
      "v_add_u32_sdwa v238, s49, v245 dst_sel:DWORD dst_unused:UNUSED_PAD src0_sel:DWORD src1_sel:WORD_0\ns_waitcnt lgkmcnt(0)\nv_mfma_f32_16x16x32_bf16 v[232:235], v[56:59], v[16:19], v[232:235]\n"
      "v_pk_mul_f32 v[44:45], v[124:125], v[124:125]\nv_pk_mul_f32 v[46:47], v[126:127], v[126:127]\nv_pk_mul_f32 v[48:49], v[128:129], v[128:129]\nv_pk_mul_f32 v[50:51], v[130:131], v[130:131]\n"
      "v_pk_fma_f32 v[44:45], v[44:45], s[58:59], s[58:59] op_sel:[0,0,1] op_sel_hi:[1,0,1]\nv_pk_fma_f32 v[46:47], v[46:47], s[58:59], s[58:59] op_sel:[0,0,1] op_sel_hi:[1,0,1]\n"
      "v_pk_fma_f32 v[48:49], v[48:49], s[58:59], s[58:59] op_sel:[0,0,1] op_sel_hi:[1,0,1]\nv_pk_fma_f32 v[50:51], v[50:51], s[58:59], s[58:59] op_sel:[0,0,1] op_sel_hi:[1,0,1]\n"
      "v_pk_fma_f32 v[44:45], v[44:45], v[124:125], s[60:61] op_sel_hi:[1,1,0]\nv_pk_fma_f32 v[46:47], v[46:47], v[126:127], s[60:61] op_sel_hi:[1,1,0]\n"
      "v_pk_fma_f32 v[48:49], v[48:49], v[128:129], s[60:61] op_sel_hi:[1,1,0]\nv_pk_fma_f32 v[50:51], v[50:51], v[130:131], s[60:61] op_sel_hi:[1,1,0]\nv_exp_f32 v44, v44 clamp\n"
      "v_pk_mul_f32 v[52:53], v[140:141], v[140:141]\nv_exp_f32 v45, v45 clamp\nv_pk_mul_f32 v[54:55], v[142:143], v[142:143]\nv_pk_mul_f32 v[56:57], v[144:145], v[144:145]\n"
      "v_exp_f32 v46, v46 clamp\nv_pk_mul_f32 v[58:59], v[146:147], v[146:147]\nv_exp_f32 v47, v47 clamp\nv_pk_fma_f32 v[52:53], v[52:53], s[58:59], s[58:59] op_sel:[0,0,1] op_sel_hi:[1,0,1]\n"
      "v_pk_fma_f32 v[54:55], v[54:55], s[58:59], s[58:59] op_sel:[0,0,1] op_sel_hi:[1,0,1]\nv_exp_f32 v48, v48 clamp\n"
      "v_pk_fma_f32 v[56:57], v[56:57], s[58:59], s[58:59] op_sel:[0,0,1] op_sel_hi:[1,0,1]\nv_exp_f32 v49, v49 clamp\n"
      "v_pk_fma_f32 v[58:59], v[58:59], s[58:59], s[58:59] op_sel:[0,0,1] op_sel_hi:[1,0,1]\nv_pk_fma_f32 v[52:53], v[52:53], v[140:141], s[60:61] op_sel_hi:[1,1,0]\nv_exp_f32 v50, v50 clamp\n"
      "v_pk_fma_f32 v[54:55], v[54:55], v[142:143], s[60:61] op_sel_hi:[1,1,0]\nv_exp_f32 v51, v51 clamp\nv_pk_fma_f32 v[56:57], v[56:57], v[144:145], s[60:61] op_sel_hi:[1,1,0]\n"
      "v_pk_fma_f32 v[58:59], v[58:59], v[146:147], s[60:61] op_sel_hi:[1,1,0]\nv_exp_f32 v52, v52 clamp\nv_pk_fma_f32 v[46:47], v[46:47], v[2:3], v[26:27]\n"
      "v_pk_fma_f32 v[44:45], v[44:45], v[0:1], v[24:25]\nv_pk_fma_f32 v[48:49], v[48:49], v[4:5], v[28:29]\nv_exp_f32 v53, v53 clamp\nv_pk_fma_f32 v[50:51], v[50:51], v[6:7], v[30:31]\n"
      "v_pk_mul_f32 v[124:125], v[124:125], v[46:47]\nv_pk_mul_f32 v[128:129], v[128:129], v[50:51]\nv_exp_f32 v54, v54 clamp\nv_pk_fma_f32 v[124:125], v[126:127], v[44:45], v[124:125]\n"
      "v_pk_fma_f32 v[128:129], v[130:131], v[48:49], v[128:129]\nv_pk_mul_f32 v[44:45], v[44:45], v[46:47]\nv_exp_f32 v55, v55 clamp\nv_pk_mul_f32 v[48:49], v[48:49], v[50:51]\n"
      "v_pk_mul_f32 v[46:47], v[44:45], v[48:49]\nv_rcp_f32 v46, v46\nv_pk_mul_f32 v[124:125], v[124:125], v[48:49]\nv_rcp_f32 v47, v47\nv_pk_fma_f32 v[124:125], v[128:129], v[44:45], v[124:125]\n"
      "v_pk_mul_f32 v[32:33], v[124:125], v[46:47]\nv_add_f32 v42, v32, v33\nv_pk_mul_f32 v[16:17], v[148:149], v[148:149]\nv_pk_mul_f32 v[18:19], v[150:151], v[150:151]\nv_exp_f32 v56, v56 clamp\n"
      "v_pk_mul_f32 v[20:21], v[152:153], v[152:153]\nv_pk_mul_f32 v[22:23], v[154:155], v[154:155]\nv_exp_f32 v57, v57 clamp\n"
      "v_pk_fma_f32 v[16:17], v[16:17], s[58:59], s[58:59] op_sel:[0,0,1] op_sel_hi:[1,0,1]\nv_pk_fma_f32 v[18:19], v[18:19], s[58:59], s[58:59] op_sel:[0,0,1] op_sel_hi:[1,0,1]\n"
      "v_pk_fma_f32 v[20:21], v[20:21], s[58:59], s[58:59] op_sel:[0,0,1] op_sel_hi:[1,0,1]\nv_exp_f32 v58, v58 clamp\n"
      "v_pk_fma_f32 v[22:23], v[22:23], s[58:59], s[58:59] op_sel:[0,0,1] op_sel_hi:[1,0,1]\nv_pk_fma_f32 v[16:17], v[16:17], v[148:149], s[60:61] op_sel_hi:[1,1,0]\n"
      "v_pk_fma_f32 v[18:19], v[18:19], v[150:151], s[60:61] op_sel_hi:[1,1,0]\nv_exp_f32 v59, v59 clamp\nv_pk_fma_f32 v[20:21], v[20:21], v[152:153], s[60:61] op_sel_hi:[1,1,0]\n"
      "v_pk_fma_f32 v[22:23], v[22:23], v[154:155], s[60:61] op_sel_hi:[1,1,0]\nv_exp_f32 v16, v16 clamp\nv_pk_fma_f32 v[54:55], v[54:55], v[2:3], v[26:27]\n"
      "v_pk_fma_f32 v[52:53], v[52:53], v[0:1], v[24:25]\nv_pk_fma_f32 v[56:57], v[56:57], v[4:5], v[28:29]\nv_exp_f32 v17, v17 clamp\nv_pk_fma_f32 v[58:59], v[58:59], v[6:7], v[30:31]\n"
      "v_pk_mul_f32 v[140:141], v[140:141], v[54:55]\nv_pk_mul_f32 v[144:145], v[144:145], v[58:59]\nv_exp_f32 v18, v18 clamp\nv_pk_fma_f32 v[140:141], v[142:143], v[52:53], v[140:141]\n"
      "v_pk_fma_f32 v[144:145], v[146:147], v[56:57], v[144:145]\nv_pk_mul_f32 v[52:53], v[52:53], v[54:55]\nv_exp_f32 v19, v19 clamp\nv_pk_mul_f32 v[56:57], v[56:57], v[58:59]\n"
      "v_pk_mul_f32 v[54:55], v[52:53], v[56:57]\nv_rcp_f32 v54, v54\nv_pk_mul_f32 v[140:141], v[140:141], v[56:57]\nv_rcp_f32 v55, v55\nv_pk_fma_f32 v[140:141], v[144:145], v[52:53], v[140:141]\n"
      "v_pk_mul_f32 v[32:33], v[140:141], v[54:55]\nv_pk_mul_f32 v[44:45], v[156:157], v[156:157]\nv_pk_mul_f32 v[46:47], v[158:159], v[158:159]\nv_exp_f32 v20, v20 clamp\n"
      "v_pk_mul_f32 v[48:49], v[160:161], v[160:161]\nv_pk_mul_f32 v[50:51], v[162:163], v[162:163]\nv_exp_f32 v21, v21 clamp\n"
      "v_pk_fma_f32 v[44:45], v[44:45], s[58:59], s[58:59] op_sel:[0,0,1] op_sel_hi:[1,0,1]\nv_pk_fma_f32 v[46:47], v[46:47], s[58:59], s[58:59] op_sel:[0,0,1] op_sel_hi:[1,0,1]\n"
      "v_pk_fma_f32 v[48:49], v[48:49], s[58:59], s[58:59] op_sel:[0,0,1] op_sel_hi:[1,0,1]\nv_exp_f32 v22, v22 clamp\n"
      "v_pk_fma_f32 v[50:51], v[50:51], s[58:59], s[58:59] op_sel:[0,0,1] op_sel_hi:[1,0,1]\nv_pk_fma_f32 v[44:45], v[44:45], v[156:157], s[60:61] op_sel_hi:[1,1,0]\n"
      "v_pk_fma_f32 v[46:47], v[46:47], v[158:159], s[60:61] op_sel_hi:[1,1,0]\nv_exp_f32 v23, v23 clamp\nv_pk_fma_f32 v[48:49], v[48:49], v[160:161], s[60:61] op_sel_hi:[1,1,0]\n"
      "v_pk_fma_f32 v[50:51], v[50:51], v[162:163], s[60:61] op_sel_hi:[1,1,0]\nv_exp_f32 v44, v44 clamp\nv_pk_fma_f32 v[18:19], v[18:19], v[10:11], v[66:67]\n"
      "v_pk_fma_f32 v[16:17], v[16:17], v[8:9], v[64:65]\nv_pk_fma_f32 v[20:21], v[20:21], v[12:13], v[68:69]\nv_exp_f32 v45, v45 clamp\nv_pk_fma_f32 v[22:23], v[22:23], v[14:15], v[70:71]\n"
      "v_pk_mul_f32 v[148:149], v[148:149], v[18:19]\nv_pk_mul_f32 v[152:153], v[152:153], v[22:23]\nv_exp_f32 v46, v46 clamp\nv_pk_fma_f32 v[148:149], v[150:151], v[16:17], v[148:149]\n"
      "v_pk_fma_f32 v[152:153], v[154:155], v[20:21], v[152:153]\nv_pk_mul_f32 v[16:17], v[16:17], v[18:19]\nv_exp_f32 v47, v47 clamp\nv_pk_mul_f32 v[20:21], v[20:21], v[22:23]\n"
      "v_pk_mul_f32 v[18:19], v[16:17], v[20:21]\nv_rcp_f32 v18, v18\nv_pk_mul_f32 v[148:149], v[148:149], v[20:21]\nv_rcp_f32 v19, v19\nv_pk_fma_f32 v[148:149], v[152:153], v[16:17], v[148:149]\n"
      "v_pk_fma_f32 v[32:33], v[148:149], v[18:19], v[32:33]\nv_add_f32 v41, v32, v33\nv_pk_mul_f32 v[52:53], v[164:165], v[164:165]\nv_pk_mul_f32 v[54:55], v[166:167], v[166:167]\n"
      "v_exp_f32 v48, v48 clamp\nv_pk_mul_f32 v[56:57], v[168:169], v[168:169]\nv_pk_mul_f32 v[58:59], v[170:171], v[170:171]\nv_exp_f32 v49, v49 clamp\n"
      "v_pk_fma_f32 v[52:53], v[52:53], s[58:59], s[58:59] op_sel:[0,0,1] op_sel_hi:[1,0,1]\nv_pk_fma_f32 v[54:55], v[54:55], s[58:59], s[58:59] op_sel:[0,0,1] op_sel_hi:[1,0,1]\n"
      "v_pk_fma_f32 v[56:57], v[56:57], s[58:59], s[58:59] op_sel:[0,0,1] op_sel_hi:[1,0,1]\nv_exp_f32 v50, v50 clamp\n"
      "v_pk_fma_f32 v[58:59], v[58:59], s[58:59], s[58:59] op_sel:[0,0,1] op_sel_hi:[1,0,1]\nv_pk_fma_f32 v[52:53], v[52:53], v[164:165], s[60:61] op_sel_hi:[1,1,0]\n"
      "v_pk_fma_f32 v[54:55], v[54:55], v[166:167], s[60:61] op_sel_hi:[1,1,0]\nv_exp_f32 v51, v51 clamp\nv_pk_fma_f32 v[56:57], v[56:57], v[168:169], s[60:61] op_sel_hi:[1,1,0]\n"
      "v_pk_fma_f32 v[58:59], v[58:59], v[170:171], s[60:61] op_sel_hi:[1,1,0]\nv_exp_f32 v52, v52 clamp\nv_pk_fma_f32 v[46:47], v[46:47], v[2:3], v[26:27]\n"
      "v_pk_fma_f32 v[44:45], v[44:45], v[0:1], v[24:25]\nv_pk_fma_f32 v[48:49], v[48:49], v[4:5], v[28:29]\nv_exp_f32 v53, v53 clamp\nv_pk_fma_f32 v[50:51], v[50:51], v[6:7], v[30:31]\n"
      "v_pk_mul_f32 v[156:157], v[156:157], v[46:47]\nv_pk_mul_f32 v[160:161], v[160:161], v[50:51]\nv_exp_f32 v54, v54 clamp\nv_pk_fma_f32 v[156:157], v[158:159], v[44:45], v[156:157]\n"
      "v_pk_fma_f32 v[160:161], v[162:163], v[48:49], v[160:161]\nv_pk_mul_f32 v[44:45], v[44:45], v[46:47]\nv_exp_f32 v55, v55 clamp\nv_pk_mul_f32 v[48:49], v[48:49], v[50:51]\n"
      "v_pk_mul_f32 v[46:47], v[44:45], v[48:49]\nv_rcp_f32 v46, v46\nv_pk_mul_f32 v[156:157], v[156:157], v[48:49]\nv_rcp_f32 v47, v47\nv_pk_fma_f32 v[156:157], v[160:161], v[44:45], v[156:157]\n"
      "v_pk_mul_f32 v[32:33], v[156:157], v[46:47]\nv_pk_mul_f32 v[16:17], v[172:173], v[172:173]\nv_pk_mul_f32 v[18:19], v[174:175], v[174:175]\nv_exp_f32 v56, v56 clamp\n"
      "v_pk_mul_f32 v[20:21], v[176:177], v[176:177]\nv_pk_mul_f32 v[22:23], v[178:179], v[178:179]\nv_exp_f32 v57, v57 clamp\n"
      "v_pk_fma_f32 v[16:17], v[16:17], s[58:59], s[58:59] op_sel:[0,0,1] op_sel_hi:[1,0,1]\nv_pk_fma_f32 v[18:19], v[18:19], s[58:59], s[58:59] op_sel:[0,0,1] op_sel_hi:[1,0,1]\n"
      "v_pk_fma_f32 v[20:21], v[20:21], s[58:59], s[58:59] op_sel:[0,0,1] op_sel_hi:[1,0,1]\nv_exp_f32 v58, v58 clamp\n"
      "v_pk_fma_f32 v[22:23], v[22:23], s[58:59], s[58:59] op_sel:[0,0,1] op_sel_hi:[1,0,1]\nv_pk_fma_f32 v[16:17], v[16:17], v[172:173], s[60:61] op_sel_hi:[1,1,0]\n"
      "v_pk_fma_f32 v[18:19], v[18:19], v[174:175], s[60:61] op_sel_hi:[1,1,0]\nv_exp_f32 v59, v59 clamp\nv_pk_fma_f32 v[20:21], v[20:21], v[176:177], s[60:61] op_sel_hi:[1,1,0]\n"
      "v_pk_fma_f32 v[22:23], v[22:23], v[178:179], s[60:61] op_sel_hi:[1,1,0]\nv_exp_f32 v16, v16 clamp\nv_pk_fma_f32 v[54:55], v[54:55], v[10:11], v[66:67]\n"
      "v_pk_fma_f32 v[52:53], v[52:53], v[8:9], v[64:65]\nv_pk_fma_f32 v[56:57], v[56:57], v[12:13], v[68:69]\nv_exp_f32 v17, v17 clamp\nv_pk_fma_f32 v[58:59], v[58:59], v[14:15], v[70:71]\n"
      "v_pk_mul_f32 v[164:165], v[164:165], v[54:55]\nv_pk_mul_f32 v[168:169], v[168:169], v[58:59]\nv_exp_f32 v18, v18 clamp\nv_pk_fma_f32 v[164:165], v[166:167], v[52:53], v[164:165]\n"
      "v_pk_fma_f32 v[168:169], v[170:171], v[56:57], v[168:169]\nv_pk_mul_f32 v[52:53], v[52:53], v[54:55]\nv_exp_f32 v19, v19 clamp\nv_pk_mul_f32 v[56:57], v[56:57], v[58:59]\n"
      "v_pk_mul_f32 v[54:55], v[52:53], v[56:57]\nv_rcp_f32 v54, v54\nv_pk_mul_f32 v[164:165], v[164:165], v[56:57]\nv_rcp_f32 v55, v55\nv_pk_fma_f32 v[164:165], v[168:169], v[52:53], v[164:165]\n"
      "v_pk_fma_f32 v[32:33], v[164:165], v[54:55], v[32:33]\nv_add_f32 v36, v32, v33\nv_pk_mul_f32 v[44:45], v[180:181], v[180:181]\nv_pk_mul_f32 v[46:47], v[182:183], v[182:183]\n"
      "v_exp_f32 v20, v20 clamp\nv_pk_mul_f32 v[48:49], v[184:185], v[184:185]\nv_pk_mul_f32 v[50:51], v[186:187], v[186:187]\nv_exp_f32 v21, v21 clamp\n"
      "v_pk_fma_f32 v[44:45], v[44:45], s[58:59], s[58:59] op_sel:[0,0,1] op_sel_hi:[1,0,1]\nv_pk_fma_f32 v[46:47], v[46:47], s[58:59], s[58:59] op_sel:[0,0,1] op_sel_hi:[1,0,1]\n"
      "v_pk_fma_f32 v[48:49], v[48:49], s[58:59], s[58:59] op_sel:[0,0,1] op_sel_hi:[1,0,1]\nv_exp_f32 v22, v22 clamp\n"
      "v_pk_fma_f32 v[50:51], v[50:51], s[58:59], s[58:59] op_sel:[0,0,1] op_sel_hi:[1,0,1]\nv_pk_fma_f32 v[44:45], v[44:45], v[180:181], s[60:61] op_sel_hi:[1,1,0]\n"
      "v_pk_fma_f32 v[46:47], v[46:47], v[182:183], s[60:61] op_sel_hi:[1,1,0]\nv_exp_f32 v23, v23 clamp\nv_pk_fma_f32 v[48:49], v[48:49], v[184:185], s[60:61] op_sel_hi:[1,1,0]\n"
      "v_pk_fma_f32 v[50:51], v[50:51], v[186:187], s[60:61] op_sel_hi:[1,1,0]\nv_exp_f32 v44, v44 clamp\nv_pk_fma_f32 v[18:19], v[18:19], v[2:3], v[26:27]\n"
      "v_pk_fma_f32 v[16:17], v[16:17], v[0:1], v[24:25]\nv_pk_fma_f32 v[20:21], v[20:21], v[4:5], v[28:29]\nv_exp_f32 v45, v45 clamp\nv_pk_fma_f32 v[22:23], v[22:23], v[6:7], v[30:31]\n"
      "v_pk_mul_f32 v[172:173], v[172:173], v[18:19]\nv_pk_mul_f32 v[176:177], v[176:177], v[22:23]\nv_exp_f32 v46, v46 clamp\nv_pk_fma_f32 v[172:173], v[174:175], v[16:17], v[172:173]\n"
      "v_pk_fma_f32 v[176:177], v[178:179], v[20:21], v[176:177]\nv_pk_mul_f32 v[16:17], v[16:17], v[18:19]\nv_exp_f32 v47, v47 clamp\nv_pk_mul_f32 v[20:21], v[20:21], v[22:23]\n"
      "v_pk_mul_f32 v[18:19], v[16:17], v[20:21]\nv_rcp_f32 v18, v18\nv_pk_mul_f32 v[172:173], v[172:173], v[20:21]\nv_rcp_f32 v19, v19\nv_pk_fma_f32 v[172:173], v[176:177], v[16:17], v[172:173]\n"
      "v_pk_mul_f32 v[32:33], v[172:173], v[18:19]\nv_pk_mul_f32 v[52:53], v[188:189], v[188:189]\nv_pk_mul_f32 v[54:55], v[190:191], v[190:191]\nv_exp_f32 v48, v48 clamp\n"
      "v_pk_mul_f32 v[56:57], v[192:193], v[192:193]\nv_pk_mul_f32 v[58:59], v[194:195], v[194:195]\nv_exp_f32 v49, v49 clamp\n"
      "v_pk_fma_f32 v[52:53], v[52:53], s[58:59], s[58:59] op_sel:[0,0,1] op_sel_hi:[1,0,1]\nv_pk_fma_f32 v[54:55], v[54:55], s[58:59], s[58:59] op_sel:[0,0,1] op_sel_hi:[1,0,1]\n"
      "v_pk_fma_f32 v[56:57], v[56:57], s[58:59], s[58:59] op_sel:[0,0,1] op_sel_hi:[1,0,1]\nv_exp_f32 v50, v50 clamp\n"
      "v_pk_fma_f32 v[58:59], v[58:59], s[58:59], s[58:59] op_sel:[0,0,1] op_sel_hi:[1,0,1]\nv_pk_fma_f32 v[52:53], v[52:53], v[188:189], s[60:61] op_sel_hi:[1,1,0]\n"
      "v_pk_fma_f32 v[54:55], v[54:55], v[190:191], s[60:61] op_sel_hi:[1,1,0]\nv_exp_f32 v51, v51 clamp\nv_pk_fma_f32 v[56:57], v[56:57], v[192:193], s[60:61] op_sel_hi:[1,1,0]\n"
      "v_pk_fma_f32 v[58:59], v[58:59], v[194:195], s[60:61] op_sel_hi:[1,1,0]\nv_exp_f32 v52, v52 clamp\nv_pk_fma_f32 v[46:47], v[46:47], v[10:11], v[66:67]\n"
      "v_pk_fma_f32 v[44:45], v[44:45], v[8:9], v[64:65]\nv_pk_fma_f32 v[48:49], v[48:49], v[12:13], v[68:69]\nv_exp_f32 v53, v53 clamp\nv_pk_fma_f32 v[50:51], v[50:51], v[14:15], v[70:71]\n"
      "v_pk_mul_f32 v[180:181], v[180:181], v[46:47]\nv_pk_mul_f32 v[184:185], v[184:185], v[50:51]\nv_exp_f32 v54, v54 clamp\nv_pk_fma_f32 v[180:181], v[182:183], v[44:45], v[180:181]\n"
      "v_pk_fma_f32 v[184:185], v[186:187], v[48:49], v[184:185]\nv_pk_mul_f32 v[44:45], v[44:45], v[46:47]\nv_exp_f32 v55, v55 clamp\nv_pk_mul_f32 v[48:49], v[48:49], v[50:51]\n"
      "v_pk_mul_f32 v[46:47], v[44:45], v[48:49]\nv_rcp_f32 v46, v46\nv_pk_mul_f32 v[180:181], v[180:181], v[48:49]\nv_rcp_f32 v47, v47\nv_pk_fma_f32 v[180:181], v[184:185], v[44:45], v[180:181]\n"
      "v_pk_fma_f32 v[32:33], v[180:181], v[46:47], v[32:33]\nv_add_f32 v37, v32, v33\nv_pk_mul_f32 v[16:17], v[196:197], v[196:197]\nv_pk_mul_f32 v[18:19], v[198:199], v[198:199]\n"
      "v_exp_f32 v56, v56 clamp\nv_pk_mul_f32 v[20:21], v[200:201], v[200:201]\nv_pk_mul_f32 v[22:23], v[202:203], v[202:203]\nv_exp_f32 v57, v57 clamp\n"
      "v_pk_fma_f32 v[16:17], v[16:17], s[58:59], s[58:59] op_sel:[0,0,1] op_sel_hi:[1,0,1]\nv_pk_fma_f32 v[18:19], v[18:19], s[58:59], s[58:59] op_sel:[0,0,1] op_sel_hi:[1,0,1]\n"
      "v_pk_fma_f32 v[20:21], v[20:21], s[58:59], s[58:59] op_sel:[0,0,1] op_sel_hi:[1,0,1]\nv_exp_f32 v58, v58 clamp\n"
      "v_pk_fma_f32 v[22:23], v[22:23], s[58:59], s[58:59] op_sel:[0,0,1] op_sel_hi:[1,0,1]\nv_pk_fma_f32 v[16:17], v[16:17], v[196:197], s[60:61] op_sel_hi:[1,1,0]\n"
      "v_pk_fma_f32 v[18:19], v[18:19], v[198:199], s[60:61] op_sel_hi:[1,1,0]\nv_exp_f32 v59, v59 clamp\nv_pk_fma_f32 v[20:21], v[20:21], v[200:201], s[60:61] op_sel_hi:[1,1,0]\n"
      "v_pk_fma_f32 v[22:23], v[22:23], v[202:203], s[60:61] op_sel_hi:[1,1,0]\nv_exp_f32 v16, v16 clamp\nv_pk_fma_f32 v[54:55], v[54:55], v[2:3], v[26:27]\n"
      "v_pk_fma_f32 v[52:53], v[52:53], v[0:1], v[24:25]\nv_pk_fma_f32 v[56:57], v[56:57], v[4:5], v[28:29]\nv_exp_f32 v17, v17 clamp\nv_pk_fma_f32 v[58:59], v[58:59], v[6:7], v[30:31]\n"
      "v_pk_mul_f32 v[188:189], v[188:189], v[54:55]\nv_pk_mul_f32 v[192:193], v[192:193], v[58:59]\nv_exp_f32 v18, v18 clamp\nv_pk_fma_f32 v[188:189], v[190:191], v[52:53], v[188:189]\n"
      "v_pk_fma_f32 v[192:193], v[194:195], v[56:57], v[192:193]\nv_pk_mul_f32 v[52:53], v[52:53], v[54:55]\nv_exp_f32 v19, v19 clamp\nv_pk_mul_f32 v[56:57], v[56:57], v[58:59]\n"
      "v_pk_mul_f32 v[54:55], v[52:53], v[56:57]\nv_rcp_f32 v54, v54\nv_pk_mul_f32 v[188:189], v[188:189], v[56:57]\nv_rcp_f32 v55, v55\nv_pk_fma_f32 v[188:189], v[192:193], v[52:53], v[188:189]\n"
      "v_pk_mul_f32 v[32:33], v[188:189], v[54:55]\nv_pk_mul_f32 v[44:45], v[204:205], v[204:205]\nv_pk_mul_f32 v[46:47], v[206:207], v[206:207]\nv_exp_f32 v20, v20 clamp\n"
      "v_pk_mul_f32 v[48:49], v[208:209], v[208:209]\nv_pk_mul_f32 v[50:51], v[210:211], v[210:211]\nv_exp_f32 v21, v21 clamp\n"
      "v_pk_fma_f32 v[44:45], v[44:45], s[58:59], s[58:59] op_sel:[0,0,1] op_sel_hi:[1,0,1]\nv_pk_fma_f32 v[46:47], v[46:47], s[58:59], s[58:59] op_sel:[0,0,1] op_sel_hi:[1,0,1]\n"
      "v_pk_fma_f32 v[48:49], v[48:49], s[58:59], s[58:59] op_sel:[0,0,1] op_sel_hi:[1,0,1]\nv_exp_f32 v22, v22 clamp\n"
      "v_pk_fma_f32 v[50:51], v[50:51], s[58:59], s[58:59] op_sel:[0,0,1] op_sel_hi:[1,0,1]\nv_pk_fma_f32 v[44:45], v[44:45], v[204:205], s[60:61] op_sel_hi:[1,1,0]\n"
      "v_pk_fma_f32 v[46:47], v[46:47], v[206:207], s[60:61] op_sel_hi:[1,1,0]\nv_exp_f32 v23, v23 clamp\nv_pk_fma_f32 v[48:49], v[48:49], v[208:209], s[60:61] op_sel_hi:[1,1,0]\n"
      "v_pk_fma_f32 v[50:51], v[50:51], v[210:211], s[60:61] op_sel_hi:[1,1,0]\nv_exp_f32 v44, v44 clamp\nv_pk_fma_f32 v[18:19], v[18:19], v[10:11], v[66:67]\n"
      "v_pk_fma_f32 v[16:17], v[16:17], v[8:9], v[64:65]\nv_pk_fma_f32 v[20:21], v[20:21], v[12:13], v[68:69]\nv_exp_f32 v45, v45 clamp\nv_pk_fma_f32 v[22:23], v[22:23], v[14:15], v[70:71]\n"
      "v_pk_mul_f32 v[196:197], v[196:197], v[18:19]\nv_pk_mul_f32 v[200:201], v[200:201], v[22:23]\nv_exp_f32 v46, v46 clamp\nv_pk_fma_f32 v[196:197], v[198:199], v[16:17], v[196:197]\n"
      "v_pk_fma_f32 v[200:201], v[202:203], v[20:21], v[200:201]\nv_pk_mul_f32 v[16:17], v[16:17], v[18:19]\nv_exp_f32 v47, v47 clamp\nv_pk_mul_f32 v[20:21], v[20:21], v[22:23]\n"
      "v_pk_mul_f32 v[18:19], v[16:17], v[20:21]\nv_rcp_f32 v18, v18\nv_pk_mul_f32 v[196:197], v[196:197], v[20:21]\nv_rcp_f32 v19, v19\nv_pk_fma_f32 v[196:197], v[200:201], v[16:17], v[196:197]\n"
      "v_pk_fma_f32 v[32:33], v[196:197], v[18:19], v[32:33]\nv_add_f32 v38, v32, v33\nv_pk_mul_f32 v[52:53], v[212:213], v[212:213]\nv_pk_mul_f32 v[54:55], v[214:215], v[214:215]\n"
      "v_exp_f32 v48, v48 clamp\nv_pk_mul_f32 v[56:57], v[216:217], v[216:217]\nv_pk_mul_f32 v[58:59], v[218:219], v[218:219]\nv_exp_f32 v49, v49 clamp\n"
      "v_pk_fma_f32 v[52:53], v[52:53], s[58:59], s[58:59] op_sel:[0,0,1] op_sel_hi:[1,0,1]\nv_pk_fma_f32 v[54:55], v[54:55], s[58:59], s[58:59] op_sel:[0,0,1] op_sel_hi:[1,0,1]\n"
      "v_pk_fma_f32 v[56:57], v[56:57], s[58:59], s[58:59] op_sel:[0,0,1] op_sel_hi:[1,0,1]\nv_exp_f32 v50, v50 clamp\n"
      "v_pk_fma_f32 v[58:59], v[58:59], s[58:59], s[58:59] op_sel:[0,0,1] op_sel_hi:[1,0,1]\nv_pk_fma_f32 v[52:53], v[52:53], v[212:213], s[60:61] op_sel_hi:[1,1,0]\n"
      "v_pk_fma_f32 v[54:55], v[54:55], v[214:215], s[60:61] op_sel_hi:[1,1,0]\nv_exp_f32 v51, v51 clamp\nv_pk_fma_f32 v[56:57], v[56:57], v[216:217], s[60:61] op_sel_hi:[1,1,0]\n"
      "v_pk_fma_f32 v[58:59], v[58:59], v[218:219], s[60:61] op_sel_hi:[1,1,0]\nv_exp_f32 v52, v52 clamp\nv_pk_fma_f32 v[46:47], v[46:47], v[2:3], v[26:27]\n"
      "v_pk_fma_f32 v[44:45], v[44:45], v[0:1], v[24:25]\nv_pk_fma_f32 v[48:49], v[48:49], v[4:5], v[28:29]\nv_exp_f32 v53, v53 clamp\nv_pk_fma_f32 v[50:51], v[50:51], v[6:7], v[30:31]\n"
      "v_pk_mul_f32 v[204:205], v[204:205], v[46:47]\nv_pk_mul_f32 v[208:209], v[208:209], v[50:51]\nv_exp_f32 v54, v54 clamp\nv_pk_fma_f32 v[204:205], v[206:207], v[44:45], v[204:205]\n"
      "v_pk_fma_f32 v[208:209], v[210:211], v[48:49], v[208:209]\nv_pk_mul_f32 v[44:45], v[44:45], v[46:47]\nv_exp_f32 v55, v55 clamp\nv_pk_mul_f32 v[48:49], v[48:49], v[50:51]\n"
      "v_pk_mul_f32 v[46:47], v[44:45], v[48:49]\nv_rcp_f32 v46, v46\nv_pk_mul_f32 v[204:205], v[204:205], v[48:49]\nv_rcp_f32 v47, v47\nv_pk_fma_f32 v[204:205], v[208:209], v[44:45], v[204:205]\n"
      "v_pk_mul_f32 v[32:33], v[204:205], v[46:47]\nv_pk_mul_f32 v[16:17], v[220:221], v[220:221]\nv_pk_mul_f32 v[18:19], v[222:223], v[222:223]\nv_exp_f32 v56, v56 clamp\n"
      "v_pk_mul_f32 v[20:21], v[224:225], v[224:225]\nv_pk_mul_f32 v[22:23], v[226:227], v[226:227]\nv_exp_f32 v57, v57 clamp\n"
      "v_pk_fma_f32 v[16:17], v[16:17], s[58:59], s[58:59] op_sel:[0,0,1] op_sel_hi:[1,0,1]\nv_pk_fma_f32 v[18:19], v[18:19], s[58:59], s[58:59] op_sel:[0,0,1] op_sel_hi:[1,0,1]\n"
      "v_pk_fma_f32 v[20:21], v[20:21], s[58:59], s[58:59] op_sel:[0,0,1] op_sel_hi:[1,0,1]\nv_exp_f32 v58, v58 clamp\n"
      "v_pk_fma_f32 v[22:23], v[22:23], s[58:59], s[58:59] op_sel:[0,0,1] op_sel_hi:[1,0,1]\nv_pk_fma_f32 v[16:17], v[16:17], v[220:221], s[60:61] op_sel_hi:[1,1,0]\n"
      "v_pk_fma_f32 v[18:19], v[18:19], v[222:223], s[60:61] op_sel_hi:[1,1,0]\nv_exp_f32 v59, v59 clamp\nv_pk_fma_f32 v[20:21], v[20:21], v[224:225], s[60:61] op_sel_hi:[1,1,0]\n"
      "v_pk_fma_f32 v[22:23], v[22:23], v[226:227], s[60:61] op_sel_hi:[1,1,0]\nv_exp_f32 v16, v16 clamp\nv_pk_fma_f32 v[54:55], v[54:55], v[10:11], v[66:67]\n"
      "v_pk_fma_f32 v[52:53], v[52:53], v[8:9], v[64:65]\nv_pk_fma_f32 v[56:57], v[56:57], v[12:13], v[68:69]\nv_exp_f32 v17, v17 clamp\nv_pk_fma_f32 v[58:59], v[58:59], v[14:15], v[70:71]\n"
      "v_pk_mul_f32 v[212:213], v[212:213], v[54:55]\nv_pk_mul_f32 v[216:217], v[216:217], v[58:59]\nv_exp_f32 v18, v18 clamp\nv_pk_fma_f32 v[212:213], v[214:215], v[52:53], v[212:213]\n"
      "v_pk_fma_f32 v[216:217], v[218:219], v[56:57], v[216:217]\nv_pk_mul_f32 v[52:53], v[52:53], v[54:55]\nv_exp_f32 v19, v19 clamp\nv_pk_mul_f32 v[56:57], v[56:57], v[58:59]\n"
      "v_pk_mul_f32 v[54:55], v[52:53], v[56:57]\nv_rcp_f32 v54, v54\nv_pk_mul_f32 v[212:213], v[212:213], v[56:57]\nv_rcp_f32 v55, v55\nv_pk_fma_f32 v[212:213], v[216:217], v[52:53], v[212:213]\n"
      "v_pk_fma_f32 v[32:33], v[212:213], v[54:55], v[32:33]\nv_add_f32 v39, v32, v33\nv_pk_mul_f32 v[44:45], v[228:229], v[228:229]\nv_pk_mul_f32 v[46:47], v[230:231], v[230:231]\n"
      "v_exp_f32 v20, v20 clamp\nv_pk_mul_f32 v[48:49], v[232:233], v[232:233]\nv_pk_mul_f32 v[50:51], v[234:235], v[234:235]\nv_exp_f32 v21, v21 clamp\n"
      "v_pk_fma_f32 v[44:45], v[44:45], s[58:59], s[58:59] op_sel:[0,0,1] op_sel_hi:[1,0,1]\nv_pk_fma_f32 v[46:47], v[46:47], s[58:59], s[58:59] op_sel:[0,0,1] op_sel_hi:[1,0,1]\n"
      "v_pk_fma_f32 v[48:49], v[48:49], s[58:59], s[58:59] op_sel:[0,0,1] op_sel_hi:[1,0,1]\nv_exp_f32 v22, v22 clamp\n"
      "v_pk_fma_f32 v[50:51], v[50:51], s[58:59], s[58:59] op_sel:[0,0,1] op_sel_hi:[1,0,1]\nv_pk_fma_f32 v[44:45], v[44:45], v[228:229], s[60:61] op_sel_hi:[1,1,0]\n"
      "v_pk_fma_f32 v[46:47], v[46:47], v[230:231], s[60:61] op_sel_hi:[1,1,0]\nv_exp_f32 v23, v23 clamp\nv_pk_fma_f32 v[48:49], v[48:49], v[232:233], s[60:61] op_sel_hi:[1,1,0]\n"
      "v_pk_fma_f32 v[50:51], v[50:51], v[234:235], s[60:61] op_sel_hi:[1,1,0]\nv_exp_f32 v44, v44 clamp\nv_pk_fma_f32 v[18:19], v[18:19], v[2:3], v[26:27]\nv_exp_f32 v45, v45 clamp\n"
      "v_pk_fma_f32 v[16:17], v[16:17], v[0:1], v[24:25]\nv_pk_fma_f32 v[20:21], v[20:21], v[4:5], v[28:29]\nv_exp_f32 v46, v46 clamp\nv_pk_fma_f32 v[22:23], v[22:23], v[6:7], v[30:31]\n"
      "v_pk_mul_f32 v[220:221], v[220:221], v[18:19]\nv_exp_f32 v47, v47 clamp\nv_pk_mul_f32 v[224:225], v[224:225], v[22:23]\nv_pk_fma_f32 v[220:221], v[222:223], v[16:17], v[220:221]\n"
      "v_exp_f32 v48, v48 clamp\nv_pk_fma_f32 v[224:225], v[226:227], v[20:21], v[224:225]\nv_exp_f32 v49, v49 clamp\nv_pk_mul_f32 v[16:17], v[16:17], v[18:19]\n"
      "v_pk_mul_f32 v[20:21], v[20:21], v[22:23]\nv_exp_f32 v50, v50 clamp\nv_pk_mul_f32 v[18:19], v[16:17], v[20:21]\nv_rcp_f32 v18, v18\nv_pk_mul_f32 v[220:221], v[220:221], v[20:21]\n"
      "v_exp_f32 v51, v51 clamp\nv_rcp_f32 v19, v19\nv_pk_fma_f32 v[220:221], v[224:225], v[16:17], v[220:221]\nv_pk_mul_f32 v[32:33], v[220:221], v[18:19]\n"
      "v_pk_fma_f32 v[46:47], v[46:47], v[10:11], v[66:67]\nv_pk_fma_f32 v[44:45], v[44:45], v[8:9], v[64:65]\nv_pk_fma_f32 v[48:49], v[48:49], v[12:13], v[68:69]\n"
      "v_pk_fma_f32 v[50:51], v[50:51], v[14:15], v[70:71]\nv_pk_mul_f32 v[228:229], v[228:229], v[46:47]\nv_pk_mul_f32 v[232:233], v[232:233], v[50:51]\n"
      "v_pk_fma_f32 v[228:229], v[230:231], v[44:45], v[228:229]\nv_pk_fma_f32 v[232:233], v[234:235], v[48:49], v[232:233]\nv_pk_mul_f32 v[44:45], v[44:45], v[46:47]\n"
      "v_pk_mul_f32 v[48:49], v[48:49], v[50:51]\nv_pk_mul_f32 v[46:47], v[44:45], v[48:49]\nv_rcp_f32 v46, v46\nv_pk_mul_f32 v[228:229], v[228:229], v[48:49]\nv_rcp_f32 v47, v47\n"
      "v_pk_fma_f32 v[228:229], v[232:233], v[44:45], v[228:229]\nv_pk_fma_f32 v[32:33], v[228:229], v[46:47], v[32:33]\nv_add_f32 v40, v32, v33\ns_barrier\n"
      ".Lge:\nv_mov_b32 v43, v42\nds_read_b128 v[48:51], v246\nds_read_b128 v[52:55], v246 offset:16\nds_read_b64_tr_b16 v[68:69], v238 offset:0\nds_read_b64_tr_b16 v[70:71], v239 offset:0\n"
      "ds_read_b64_tr_b16 v[56:57], v236 offset:0\nds_read_b64_tr_b16 v[58:59], v237 offset:128\nds_read_b64_tr_b16 v[60:61], v237 offset:256\nds_read_b64_tr_b16 v[62:63], v237 offset:384\n"
      "ds_read_b64_tr_b16 v[64:65], v237 offset:512\nds_read_b64_tr_b16 v[66:67], v237 offset:640\nds_read_b128 v[44:47], v248 offset:48\nv_permlane32_swap_b32 v36, v38\n"
      "v_permlane32_swap_b32 v37, v39\nv_permlane32_swap_b32 v40, v42\nv_permlane32_swap_b32 v41, v43\nv_add_f32 v36, v36, v38\nv_add_f32 v37, v37, v39\nv_add_f32 v40, v40, v42\n"
      "v_add_f32 v41, v41, v43\ns_nop 0\nv_permlane16_swap_b32 v36, v37\nv_permlane16_swap_b32 v40, v41\nv_add_f32 v36, v36, v37\nv_add_f32 v40, v40, v41\nds_write_b32 v254, v36\n"
      "ds_write_b32 v255, v40 offset:256\nds_add_rtn_u32 v2, v253, v241 offset:20\ns_waitcnt lgkmcnt(11)\nv_readlane_b32 s48, v69, 48\ns_mov_b32 s56, 0\ns_mov_b32 s44, 0\ns_mov_b32 s33, s45\n"
      "s_waitcnt lgkmcnt(10)\nv_pk_mul_f16 v20, v68, v70\nv_pk_mul_f16 v21, v69, v71\ns_waitcnt lgkmcnt(9)\nv_pk_mul_f16 v22, v56, v56 op_sel_hi:[0,1]\nv_pk_mul_f16 v23, v56, v57 op_sel_hi:[0,1]\n"
      "v_pk_mul_f16 v16, v68, s48 op_sel:[0,1]\nv_pk_mul_f16 v17, v69, s48 op_sel:[0,1]\nv_pk_mul_f16 v18, v70, s48 op_sel:[0,1]\nv_pk_mul_f16 v19, v71, s48 op_sel:[0,1]\ns_nop 1\n"
      "v_mfma_f32_16x16x32_f16 v[40:43], v[76:79], v[16:19], 0\nv_mfma_f32_16x16x32_f16 v[40:43], v[88:91], v[20:23], v[40:43]\ns_add_u32 s45, s45, 2\ns_cmp_ge_u32 s45, s46\ns_cbranch_scc0 .Lc_bg\n"
      "s_swappc_b64 s[4:5], s[34:35]\n"
      ".Lc_bg:\nv_mfma_f32_16x16x32_f16 v[36:39], v[84:87], v[20:23], 0\ns_waitcnt lgkmcnt(4)\nv_pk_mul_f16 v24, v66, v66 op_sel_hi:[0,1]\nv_pk_mul_f16 v25, v66, v57 op_sel_hi:[0,1]\n"
      "ds_read_b128 v[124:127], v252 offset:13312\nv_mfma_f32_16x16x32_f16 v[36:39], v[72:75], v[16:19], v[36:39]\nv_pk_mul_f16 v26, v56, v66 op_sel_hi:[0,1]\n"
      "v_pk_mul_f16 v27, v56, v67 op_sel_hi:[0,1]\nds_read_b128 v[128:131], v252 offset:14336\nv_mfma_f32_16x16x32_f16 v[32:35], v[80:83], v[20:23], 0\nv_pk_mul_f16 v28, v58, v66 op_sel_hi:[0,1]\n"
      "v_pk_mul_f16 v29, v58, v67 op_sel_hi:[0,1]\nds_read_b128 v[132:135], v252 offset:15360\ns_waitcnt lgkmcnt(6)\nv_smfmac_f32_16x16x64_f16 v[44:47], v[92:95], v[16:23], v242\n"
      "v_pk_mul_f16 v30, v60, v66 op_sel_hi:[0,1]\nv_pk_mul_f16 v31, v60, v67 op_sel_hi:[0,1]\nds_read_b128 v[136:139], v252 offset:16384\n"
      "v_mfma_f32_16x16x32_f16 v[32:35], v[96:99], v[24:27], v[32:35]\nv_pk_mul_f16 v16, v62, v66 op_sel_hi:[0,1]\nv_pk_mul_f16 v17, v62, v67 op_sel_hi:[0,1]\n"
      "ds_read_b128 v[140:143], v252 offset:17408\nv_mfma_f32_16x16x32_f16 v[32:35], v[108:111], v[28:31], v[32:35]\nv_pk_mul_f16 v18, v64, v66 op_sel_hi:[0,1]\n"
      "ds_read_b128 v[144:147], v252 offset:18432\nv_mfma_f32_16x16x32_f16 v[36:39], v[112:115], v[28:31], v[36:39]\nv_pk_mul_f16 v19, v64, v67 op_sel_hi:[0,1]\ns_cmp_ge_u32 s45, s46\n"
      "s_cbranch_scc0 .Lc_bh\ns_swappc_b64 s[4:5], s[34:35]\n"
      ".Lc_bh:\nds_read_b128 v[148:151], v252 offset:19456\nv_mfma_f32_16x16x32_f16 v[36:39], v[100:103], v[24:27], v[36:39]\nv_pk_mul_f16 v20, v64, v64 op_sel_hi:[0,1]\n"
      "ds_read_b128 v[152:155], v252 offset:20480\nv_mfma_f32_16x16x32_f16 v[40:43], v[104:107], v[24:27], v[40:43]\nv_pk_mul_f16 v21, v64, v57 op_sel_hi:[0,1]\n"
      "ds_read_b128 v[156:159], v252 offset:21504\nv_mfma_f32_16x16x32_f16 v[40:43], v[116:119], v[28:31], v[40:43]\nv_pk_mul_f16 v22, v56, v64 op_sel_hi:[0,1]\n"
      "ds_read_b128 v[160:163], v252 offset:22528\nv_smfmac_f32_16x16x64_f16 v[44:47], v[120:123], v[24:31], v242\nv_pk_mul_f16 v23, v56, v65 op_sel_hi:[0,1]\n"
      "ds_read_b128 v[164:167], v252 offset:23552\ns_waitcnt lgkmcnt(10)\nv_mfma_f32_16x16x32_f16 v[32:35], v[124:127], v[16:19], v[32:35]\nv_pk_mul_f16 v24, v58, v64 op_sel_hi:[0,1]\n"
      "v_pk_mul_f16 v25, v58, v65 op_sel_hi:[0,1]\nds_read_b64_tr_b16 v[70:71], v239 offset:1856\nds_read_b128 v[168:171], v252 offset:24576\ns_waitcnt lgkmcnt(9)\n"
      "v_mfma_f32_16x16x32_f16 v[32:35], v[136:139], v[20:23], v[32:35]\nv_pk_mul_f16 v26, v60, v64 op_sel_hi:[0,1]\nds_read_b64_tr_b16 v[68:69], v238 offset:1856\n"
      "ds_read_b128 v[172:175], v252 offset:25600\ns_waitcnt lgkmcnt(10)\nv_mfma_f32_16x16x32_f16 v[36:39], v[140:143], v[20:23], v[36:39]\nv_pk_mul_f16 v27, v60, v65 op_sel_hi:[0,1]\n"
      "ds_read_b64_tr_b16 v[66:67], v237 offset:2496\nds_read_b128 v[176:179], v252 offset:26624\nv_mfma_f32_16x16x32_f16 v[36:39], v[128:131], v[16:19], v[36:39]\n"
      "v_pk_mul_f16 v28, v62, v64 op_sel_hi:[0,1]\nds_read_b128 v[180:183], v252 offset:27648\nv_mfma_f32_16x16x32_f16 v[40:43], v[132:135], v[16:19], v[40:43]\n"
      "v_pk_mul_f16 v29, v62, v65 op_sel_hi:[0,1]\nds_read_b128 v[184:187], v252 offset:28672\ns_waitcnt lgkmcnt(13)\nv_mfma_f32_16x16x32_f16 v[40:43], v[144:147], v[20:23], v[40:43]\n"
      "v_pk_mul_f16 v30, v62, v62 op_sel_hi:[0,1]\nds_read_b128 v[188:191], v252 offset:29696\ns_waitcnt lgkmcnt(13)\nv_smfmac_f32_16x16x64_f16 v[44:47], v[148:151], v[16:23], v242\n"
      "v_pk_mul_f16 v31, v62, v57 op_sel_hi:[0,1]\nds_read_b128 v[192:195], v252 offset:30720\ns_waitcnt lgkmcnt(13)\nv_mfma_f32_16x16x32_f16 v[32:35], v[152:155], v[24:27], v[32:35]\n"
      "v_pk_mul_f16 v16, v56, v62 op_sel_hi:[0,1]\nv_pk_mul_f16 v17, v56, v63 op_sel_hi:[0,1]\nds_read_b128 v[196:199], v252 offset:31744\ns_waitcnt lgkmcnt(11)\n"
      "v_mfma_f32_16x16x32_f16 v[32:35], v[164:167], v[28:31], v[32:35]\nv_pk_mul_f16 v18, v58, v62 op_sel_hi:[0,1]\nds_read_b128 v[200:203], v252 offset:32768\n"
      "ds_read_b64_tr_b16 v[64:65], v237 offset:2368\ns_waitcnt lgkmcnt(11)\nv_mfma_f32_16x16x32_f16 v[36:39], v[168:171], v[28:31], v[36:39]\nv_pk_mul_f16 v19, v58, v63 op_sel_hi:[0,1]\n"
      "ds_read_b128 v[204:207], v252 offset:33792\nv_mfma_f32_16x16x32_f16 v[36:39], v[156:159], v[24:27], v[36:39]\nv_pk_mul_f16 v20, v60, v62 op_sel_hi:[0,1]\n"
      "ds_read_b128 v[208:211], v252 offset:34816\nv_mfma_f32_16x16x32_f16 v[40:43], v[160:163], v[24:27], v[40:43]\nv_pk_mul_f16 v21, v60, v63 op_sel_hi:[0,1]\n"
      "ds_read_b128 v[212:215], v252 offset:35840\ns_waitcnt lgkmcnt(13)\nv_readlane_b32 s48, v69, 48\ns_waitcnt lgkmcnt(12)\nv_mfma_f32_16x16x32_f16 v[40:43], v[172:175], v[28:31], v[40:43]\n"
      "v_pk_mul_f16 v22, v60, v60 op_sel_hi:[0,1]\nds_read_b128 v[216:219], v252 offset:36864\ns_waitcnt lgkmcnt(11)\nv_smfmac_f32_16x16x64_f16 v[44:47], v[176:179], v[24:31], v242\n"
      "v_pk_mul_f16 v23, v60, v57 op_sel_hi:[0,1]\nds_read_b128 v[220:223], v252 offset:37888\ns_waitcnt lgkmcnt(11)\nv_mfma_f32_16x16x32_f16 v[32:35], v[180:183], v[16:19], v[32:35]\n"
      "v_pk_mul_f16 v24, v56, v60 op_sel_hi:[0,1]\nv_pk_mul_f16 v25, v56, v61 op_sel_hi:[0,1]\nds_read_b128 v[224:227], v252 offset:38912\ns_waitcnt lgkmcnt(9)\n"
      "v_mfma_f32_16x16x32_f16 v[32:35], v[192:195], v[20:23], v[32:35]\nv_pk_mul_f16 v26, v58, v60 op_sel_hi:[0,1]\nv_pk_mul_f16 v27, v58, v61 op_sel_hi:[0,1]\n"
      "ds_read_b128 v[228:231], v252 offset:39936\ns_waitcnt lgkmcnt(9)\nv_mfma_f32_16x16x32_f16 v[36:39], v[196:199], v[20:23], v[36:39]\nv_pk_mul_f16 v28, v58, v58 op_sel_hi:[0,1]\n"
      "ds_read_b128 v[232:235], v252 offset:40960\nds_read_b64_tr_b16 v[62:63], v237 offset:2240\nv_mfma_f32_16x16x32_f16 v[36:39], v[184:187], v[16:19], v[36:39]\n"
      "v_pk_mul_f16 v29, v58, v57 op_sel_hi:[0,1]\nds_read_b64_tr_b16 v[60:61], v237 offset:2112\nv_mfma_f32_16x16x32_f16 v[40:43], v[188:191], v[16:19], v[40:43]\n"
      "v_pk_mul_f16 v30, v56, v58 op_sel_hi:[0,1]\ns_waitcnt lgkmcnt(11)\nv_mfma_f32_16x16x32_f16 v[40:43], v[200:203], v[20:23], v[40:43]\nv_pk_mul_f16 v31, v56, v59 op_sel_hi:[0,1]\n"
      "s_waitcnt lgkmcnt(9)\nv_smfmac_f32_16x16x64_f16 v[44:47], v[204:207], v[16:23], v242\nds_read_b64_tr_b16 v[56:57], v236 offset:1856\nds_read_b64_tr_b16 v[58:59], v237 offset:1984\n"
      "s_waitcnt lgkmcnt(4)\nv_smfmac_f32_16x16x64_f16 v[44:47], v[232:235], v[24:31], v242\nv_pk_mul_f16 v16, v68, s48 op_sel:[0,1]\nv_pk_mul_f16 v17, v69, s48 op_sel:[0,1]\n"
      "v_mfma_f32_16x16x32_f16 v[32:35], v[208:211], v[24:27], v[32:35]\nv_pk_mul_f16 v18, v70, s48 op_sel:[0,1]\nv_pk_mul_f16 v19, v71, s48 op_sel:[0,1]\n"
      "v_mfma_f32_16x16x32_f16 v[32:35], v[220:223], v[28:31], v[32:35]\nv_pk_mul_f16 v20, v68, v70\nv_pk_mul_f16 v21, v69, v71\nv_mfma_f32_16x16x32_f16 v[36:39], v[224:227], v[28:31], v[36:39]\n"
      "v_add_f32 v44, v44, v45\nv_add_f32 v46, v46, v47\nv_mfma_f32_16x16x32_f16 v[36:39], v[212:215], v[24:27], v[36:39]\nv_mul_f32 v55, v44, v54\nv_mul_f32 v55, v55, v46\n"
      "ds_read_b128 v[44:47], v248 offset:48\nv_mfma_f32_16x16x32_f16 v[40:43], v[216:219], v[24:27], v[40:43]\ns_waitcnt lgkmcnt(2)\nv_pk_mul_f16 v22, v56, v56 op_sel_hi:[0,1]\n"
      "v_pk_mul_f16 v23, v56, v57 op_sel_hi:[0,1]\nv_mfma_f32_16x16x32_f16 v[40:43], v[228:231], v[28:31], v[40:43]\nv_mul_f32 v32, v32, v48\nv_mul_f32 v33, v33, v49\nv_mul_f32 v34, v34, v50\n"
      "s_waitcnt lgkmcnt(1)\ns_branch .Lc_u1\n"
      ".Lfeed:\ns_waitcnt vmcnt(7) lgkmcnt(0)\ns_cbranch_vccnz .Lw1004\ns_waitcnt vmcnt(6)\n"
      ".Lw1004:\ns_barrier\nds_read_b64 v[44:45], v30 offset:45056\ns_add_u32 m0, s54, 0\ns_nop 0\nglobal_load_lds_dwordx4 v236, s[50:51] offset:0\n"
      "global_load_lds_dwordx4 v237, s[50:51] offset:1024\nglobal_load_lds_dwordx4 v238, s[50:51] offset:2048\nglobal_load_lds_dwordx4 v239, s[50:51] offset:3072\ns_add_u32 s50, s50, 212992\n"
      "s_addc_u32 s51, s51, 0\ns_add_u32 m0, s26, 20480\ns_nop 0\nglobal_load_lds_dwordx4 v27, s[6:7] offset:0\nv_lshl_add_u32 v27, v197, 7, v4\ns_cbranch_vccz .La1003\ns_add_u32 m0, s26, 24576\n"
      "s_nop 0\nglobal_load_lds_dwordx4 v28, s[6:7] offset:0\n"
      ".La1003:\nv_lshl_add_u32 v28, v203, 7, v4\nds_read2_b64 v[200:203], v9 offset0:2 offset1:3\ns_add_u32 m0, s53, 0\ns_nop 0\nglobal_load_lds_dwordx4 v22, s[6:7] offset:0\n"
      "v_lshl_add_u32 v22, v179, 7, v4\nds_read2_b64 v[176:179], v6 offset0:2 offset1:3\nglobal_load_lds_dwordx4 v23, s[44:45] offset:1024\nv_lshl_add_u32 v23, v181, 7, v5\n"
      "global_load_lds_dwordx4 v24, s[46:47] offset:2048\nv_lshl_add_u32 v24, v187, 7, v4\nds_read2_b64 v[184:187], v7 offset0:2 offset1:3\nglobal_load_lds_dwordx4 v25, s[48:49] offset:3072\n"
      "v_lshl_add_u32 v25, v189, 7, v5\ns_add_u32 m0, s53, 4096\ns_nop 0\nglobal_load_lds_dwordx4 v26, s[6:7] offset:0\nv_lshl_add_u32 v26, v195, 7, v4\n"
      "ds_read2_b64 v[192:195], v8 offset0:2 offset1:3\ns_waitcnt vmcnt(11) lgkmcnt(0)\ns_cbranch_vccnz .Lw1006\ns_waitcnt vmcnt(10)\n"
      ".Lw1006:\ns_barrier\ns_add_u32 m0, s54, 45056\ns_nop 0\nglobal_load_lds_dwordx4 v236, s[50:51] offset:0\nds_read_b64 v[46:47], v31 offset:0\n"
      "global_load_lds_dwordx4 v237, s[50:51] offset:1024\nglobal_load_lds_dwordx4 v238, s[50:51] offset:2048\nglobal_load_lds_dwordx4 v239, s[50:51] offset:3072\ns_add_u32 s50, s50, 212992\n"
      "s_addc_u32 s51, s51, 0\ns_cmp_eq_u32 s27, 22\ns_cselect_b64 s[50:51], s[10:11], s[50:51]\ns_add_u32 m0, s26, 65536\nv_max3_f16 v32, |v44|, |v45|, v32\n"
      "global_load_lds_dwordx4 v27, s[6:7] offset:0\nv_lshl_add_u32 v27, v198, 7, v4\nv_max3_f32 v33, |v44|, |v45|, v33\ns_cbranch_vccz .La1007\ns_add_u32 m0, s26, 69632\ns_nop 0\n"
      "global_load_lds_dwordx4 v28, s[6:7] offset:0\n"
      ".La1007:\nv_lshl_add_u32 v28, v200, 7, v4\ns_add_u32 m0, s53, 45056\nv_and_b32 v96, s55, v44\nglobal_load_lds_dwordx4 v22, s[6:7] offset:0\nv_lshl_add_u32 v22, v176, 7, v4\n"
      "v_xor_b32 v44, v44, v96\nglobal_load_lds_dwordx4 v23, s[44:45] offset:1024\nv_lshl_add_u32 v23, v182, 7, v5\nv_and_b32 v97, s55, v45\nglobal_load_lds_dwordx4 v24, s[46:47] offset:2048\n"
      "v_lshl_add_u32 v24, v184, 7, v4\nv_xor_b32 v45, v45, v97\nglobal_load_lds_dwordx4 v25, s[48:49] offset:3072\ns_add_u32 m0, s53, 49152\nv_lshl_add_u32 v25, v190, 7, v5\n"
      "global_load_lds_dwordx4 v26, s[6:7] offset:0\nv_lshl_add_u32 v26, v192, 7, v4\ns_cmp_lg_u32 s31, 0\ns_cbranch_scc1 .Lb1008\ns_lshl_b32 s42, s24, 9\nv_lshl_add_u32 v39, v240, 3, s42\n"
      "s_and_b32 s42, s2, 7\ns_lshl_b32 s42, s42, 11\ns_add_u32 s42, s42, 4096\ns_add_u32 s58, s20, s42\ns_addc_u32 s59, s21, 0\nglobal_store_dwordx2 v39, v[254:255], s[58:59] sc1\n"
      ".Lb1008:\ns_waitcnt vmcnt(11) lgkmcnt(0)\ns_cbranch_vccnz .Lw1009\ns_waitcnt vmcnt(10)\n"
      ".Lw1009:\ns_barrier\ns_add_u32 m0, s54, 90112\ns_nop 0\nglobal_load_lds_dwordx4 v236, s[50:51] offset:0\nds_read_b64 v[48:49], v30 offset:0\n"
      "global_load_lds_dwordx4 v237, s[50:51] offset:1024\nglobal_load_lds_dwordx4 v238, s[50:51] offset:2048\nglobal_load_lds_dwordx4 v239, s[50:51] offset:3072\ns_add_u32 s50, s50, 212992\n"
      "s_addc_u32 s51, s51, 0\ns_add_u32 m0, s26, 110592\nv_max3_f16 v32, |v46|, |v47|, v32\nglobal_load_lds_dwordx4 v27, s[6:7] offset:0\nv_lshl_add_u32 v27, v199, 7, v4\n"
      "ds_read2_b64 v[196:199], v8 offset0:115 offset1:116\nv_max3_f32 v33, |v46|, |v47|, v33\ns_cbranch_vccz .La1010\ns_add_u32 m0, s26, 114688\ns_nop 0\n"
      "global_load_lds_dwordx4 v28, s[6:7] offset:0\n"
      ".La1010:\nv_lshl_add_u32 v28, v201, 7, v4\ns_add_u32 m0, s53, 90112\nv_and_b32 v98, s55, v46\nglobal_load_lds_dwordx4 v22, s[6:7] offset:0\nv_lshl_add_u32 v22, v177, 7, v4\n"
      "v_xor_b32 v46, v46, v98\nglobal_load_lds_dwordx4 v23, s[44:45] offset:1024\nv_lshl_add_u32 v23, v183, 7, v5\nds_read2_b64 v[180:183], v6 offset0:115 offset1:116\nv_and_b32 v99, s55, v47\n"
      "global_load_lds_dwordx4 v24, s[46:47] offset:2048\nv_lshl_add_u32 v24, v185, 7, v4\nv_xor_b32 v47, v47, v99\nglobal_load_lds_dwordx4 v25, s[48:49] offset:3072\n"
      "v_lshl_add_u32 v25, v191, 7, v5\ns_add_u32 m0, s53, 94208\nds_read2_b64 v[188:191], v7 offset0:115 offset1:116\nglobal_load_lds_dwordx4 v26, s[6:7] offset:0\n"
      "v_lshl_add_u32 v26, v193, 7, v4\ns_cmp_lg_u32 s24, 2\ns_cbranch_scc1 .Lo1011\ns_getpc_b64 s[62:63]\ns_add_u32 s62, s62, _ZL5g_tab@rel32@lo+2052\n"
      "s_addc_u32 s63, s63, _ZL5g_tab@rel32@hi+2060\ns_lshr_b32 s42, s2, 3\ns_lshl1_add_u32 s42, s2, s42\ns_and_b32 s42, s42, 15\ns_lshl_b32 s42, s42, 7\nv_lshl_add_u32 v40, v240, 4, s42\n"
      "s_mov_b32 m0, 162112\ns_mov_b64 exec, 0xff\nglobal_load_lds_dwordx4 v40, s[62:63]\ns_mov_b64 exec, -1\n"
      ".Lo1011:\ns_waitcnt vmcnt(11) lgkmcnt(0)\ns_cbranch_vccnz .Lw1012\ns_waitcnt vmcnt(10)\n"
      ".Lw1012:\ns_barrier\ns_add_u32 m0, s54, 0\ns_nop 0\nglobal_load_lds_dwordx4 v236, s[50:51] offset:0\nds_read_b64 v[50:51], v30 offset:45056\n"
      "global_load_lds_dwordx4 v237, s[50:51] offset:1024\nglobal_load_lds_dwordx4 v238, s[50:51] offset:2048\nglobal_load_lds_dwordx4 v239, s[50:51] offset:3072\ns_add_u32 s50, s50, 212992\n"
      "s_addc_u32 s51, s51, 0\ns_cmp_eq_u32 s27, 20\ns_cselect_b64 s[50:51], s[10:11], s[50:51]\ns_add_u32 m0, s26, 20480\nv_max3_f16 v32, |v48|, |v49|, v32\n"
      "global_load_lds_dwordx4 v27, s[6:7] offset:0\nv_lshl_add_u32 v27, v196, 7, v4\nv_max3_f32 v33, |v48|, |v49|, v33\ns_cbranch_vccz .La1013\ns_add_u32 m0, s26, 24576\ns_nop 0\n"
      "global_load_lds_dwordx4 v28, s[6:7] offset:0\n"
      ".La1013:\nv_lshl_add_u32 v28, v202, 7, v4\ns_add_u32 m0, s53, 0\nv_and_b32 v100, s55, v48\nglobal_load_lds_dwordx4 v22, s[6:7] offset:0\nv_lshl_add_u32 v22, v178, 7, v4\n"
      "v_xor_b32 v48, v48, v100\nglobal_load_lds_dwordx4 v23, s[44:45] offset:1024\nv_lshl_add_u32 v23, v180, 7, v5\nv_and_b32 v101, s55, v49\nglobal_load_lds_dwordx4 v24, s[46:47] offset:2048\n"
      "v_lshl_add_u32 v24, v186, 7, v4\nv_xor_b32 v49, v49, v101\nglobal_load_lds_dwordx4 v25, s[48:49] offset:3072\ns_add_u32 m0, s53, 4096\nv_lshl_add_u32 v25, v188, 7, v5\n"
      "global_load_lds_dwordx4 v26, s[6:7] offset:0\nv_lshl_add_u32 v26, v194, 7, v4\ns_waitcnt vmcnt(11) lgkmcnt(0)\ns_cbranch_vccnz .Lw1014\ns_waitcnt vmcnt(10)\n"
      ".Lw1014:\ns_barrier\ns_add_u32 m0, s54, 45056\ns_nop 0\nglobal_load_lds_dwordx4 v236, s[50:51] offset:0\nds_read_b64 v[52:53], v31 offset:0\n"
      "global_load_lds_dwordx4 v237, s[50:51] offset:1024\nglobal_load_lds_dwordx4 v238, s[50:51] offset:2048\nglobal_load_lds_dwordx4 v239, s[50:51] offset:3072\ns_add_u32 s50, s50, 212992\n"
      "s_addc_u32 s51, s51, 0\ns_add_u32 m0, s26, 65536\nv_max3_f16 v32, |v50|, |v51|, v32\nglobal_load_lds_dwordx4 v27, s[6:7] offset:0\nv_lshl_add_u32 v27, v197, 7, v4\n"
      "v_max3_f32 v33, |v50|, |v51|, v33\ns_cbranch_vccz .La1015\ns_add_u32 m0, s26, 69632\ns_nop 0\nglobal_load_lds_dwordx4 v28, s[6:7] offset:0\n"
      ".La1015:\nv_lshl_add_u32 v28, v203, 7, v4\nds_read2_b64 v[200:203], v9 offset0:4 offset1:5\ns_add_u32 m0, s53, 45056\nv_and_b32 v102, s55, v50\nglobal_load_lds_dwordx4 v22, s[6:7] offset:0\n"
      "v_lshl_add_u32 v22, v179, 7, v4\nds_read2_b64 v[176:179], v6 offset0:4 offset1:5\nv_xor_b32 v50, v50, v102\nglobal_load_lds_dwordx4 v23, s[44:45] offset:1024\n"
      "v_lshl_add_u32 v23, v181, 7, v5\nv_and_b32 v103, s55, v51\nglobal_load_lds_dwordx4 v24, s[46:47] offset:2048\nv_lshl_add_u32 v24, v187, 7, v4\n"
      "ds_read2_b64 v[184:187], v7 offset0:4 offset1:5\nv_xor_b32 v51, v51, v103\nglobal_load_lds_dwordx4 v25, s[48:49] offset:3072\ns_add_u32 m0, s53, 49152\nv_lshl_add_u32 v25, v189, 7, v5\n"
      "global_load_lds_dwordx4 v26, s[6:7] offset:0\nv_lshl_add_u32 v26, v195, 7, v4\nds_read2_b64 v[192:195], v8 offset0:4 offset1:5\ns_waitcnt vmcnt(11) lgkmcnt(0)\ns_cbranch_vccnz .Lw1016\n"
      "s_waitcnt vmcnt(10)\n"
      ".Lw1016:\ns_barrier\ns_add_u32 m0, s54, 90112\ns_nop 0\nglobal_load_lds_dwordx4 v236, s[50:51] offset:0\nds_read_b64 v[54:55], v30 offset:0\n"
      "global_load_lds_dwordx4 v237, s[50:51] offset:1024\nglobal_load_lds_dwordx4 v238, s[50:51] offset:2048\nglobal_load_lds_dwordx4 v239, s[50:51] offset:3072\ns_add_u32 s50, s50, 212992\n"
      "s_addc_u32 s51, s51, 0\ns_cmp_eq_u32 s27, 18\ns_cselect_b64 s[50:51], s[10:11], s[50:51]\ns_add_u32 m0, s26, 110592\nv_max3_f16 v32, |v52|, |v53|, v32\n"
      "global_load_lds_dwordx4 v27, s[6:7] offset:0\nv_lshl_add_u32 v27, v198, 7, v4\nv_max3_f32 v33, |v52|, |v53|, v33\ns_cbranch_vccz .La1017\ns_add_u32 m0, s26, 114688\ns_nop 0\n"
      "global_load_lds_dwordx4 v28, s[6:7] offset:0\n"
      ".La1017:\nv_lshl_add_u32 v28, v200, 7, v4\ns_add_u32 m0, s53, 90112\nv_and_b32 v104, s55, v52\nglobal_load_lds_dwordx4 v22, s[6:7] offset:0\nv_lshl_add_u32 v22, v176, 7, v4\n"
      "v_xor_b32 v52, v52, v104\nglobal_load_lds_dwordx4 v23, s[44:45] offset:1024\nv_lshl_add_u32 v23, v182, 7, v5\nv_and_b32 v105, s55, v53\nglobal_load_lds_dwordx4 v24, s[46:47] offset:2048\n"
      "v_lshl_add_u32 v24, v184, 7, v4\nv_xor_b32 v53, v53, v105\nglobal_load_lds_dwordx4 v25, s[48:49] offset:3072\ns_add_u32 m0, s53, 94208\nv_lshl_add_u32 v25, v190, 7, v5\n"
      "global_load_lds_dwordx4 v26, s[6:7] offset:0\nv_lshl_add_u32 v26, v192, 7, v4\ns_waitcnt vmcnt(11) lgkmcnt(0)\ns_cbranch_vccnz .Lw1018\ns_waitcnt vmcnt(10)\n"
      ".Lw1018:\ns_barrier\ns_add_u32 m0, s54, 0\ns_nop 0\nglobal_load_lds_dwordx4 v236, s[50:51] offset:0\nds_read_b64 v[56:57], v30 offset:45056\n"
      "global_load_lds_dwordx4 v237, s[50:51] offset:1024\nglobal_load_lds_dwordx4 v238, s[50:51] offset:2048\nglobal_load_lds_dwordx4 v239, s[50:51] offset:3072\ns_add_u32 s50, s50, 212992\n"
      "s_addc_u32 s51, s51, 0\nv_max3_f16 v32, |v54|, |v55|, v32\nv_max3_f32 v33, |v54|, |v55|, v33\ns_add_u32 m0, s26, 20480\nv_cmp_eq_u32 s[62:63], 0, v240\n"
      "global_load_lds_dwordx4 v27, s[6:7] offset:0\nv_lshl_add_u32 v27, v199, 7, v4\nds_read2_b64 v[196:199], v8 offset0:117 offset1:118\nv_mov_b32 v253, 262144\nv_mov_b32 v248, 162048\n"
      "v_mov_b32 v241, 1\ns_cbranch_vccz .La1019\ns_add_u32 m0, s26, 24576\ns_nop 0\nglobal_load_lds_dwordx4 v28, s[6:7] offset:0\n"
      ".La1019:\nv_lshl_add_u32 v28, v201, 7, v4\nv_cndmask_b32 v253, v253, v248, s[62:63]\ns_add_u32 m0, s53, 0\nv_and_b32 v106, s55, v54\nglobal_load_lds_dwordx4 v22, s[6:7] offset:0\n"
      "v_lshl_add_u32 v22, v177, 7, v4\nv_xor_b32 v54, v54, v106\nv_and_b32 v107, s55, v55\nglobal_load_lds_dwordx4 v23, s[44:45] offset:1024\nv_lshl_add_u32 v23, v183, 7, v5\n"
      "ds_read2_b64 v[180:183], v6 offset0:117 offset1:118\nv_xor_b32 v55, v55, v107\nv_lshrrev_b32 v164, 4, v240\nglobal_load_lds_dwordx4 v24, s[46:47] offset:2048\n"
      "v_lshl_add_u32 v24, v185, 7, v4\nv_bfe_u32 v165, v240, 2, 2\nv_and_b32 v166, 3, v240\nglobal_load_lds_dwordx4 v25, s[48:49] offset:3072\nv_lshl_add_u32 v25, v191, 7, v5\n"
      "ds_read2_b64 v[188:191], v7 offset0:117 offset1:118\nv_mul_u32_u24 v166, 0x80008, v166\ns_add_u32 m0, s53, 4096\nv_add_u32 v244, v164, v165\nglobal_load_lds_dwordx4 v26, s[6:7] offset:0\n"
      "v_lshl_add_u32 v26, v193, 7, v4\nv_and_b32 v244, 3, v244\nv_add_u32 v168, 22, v165\ns_waitcnt vmcnt(11) lgkmcnt(0)\ns_cbranch_vccnz .Lw1020\ns_waitcnt vmcnt(10)\n"
      ".Lw1020:\ns_barrier\ns_add_u32 m0, s54, 45056\ns_nop 0\nglobal_load_lds_dwordx4 v236, s[50:51] offset:0\nds_read_b64 v[58:59], v31 offset:0\n"
      "global_load_lds_dwordx4 v237, s[50:51] offset:1024\nglobal_load_lds_dwordx4 v238, s[50:51] offset:2048\nglobal_load_lds_dwordx4 v239, s[50:51] offset:3072\ns_add_u32 s50, s50, 212992\n"
      "s_addc_u32 s51, s51, 0\ns_cmp_eq_u32 s27, 16\ns_cselect_b64 s[50:51], s[10:11], s[50:51]\nv_max3_f16 v32, |v56|, |v57|, v32\nv_max3_f32 v33, |v56|, |v57|, v33\ns_add_u32 m0, s26, 65536\n"
      "v_cmp_gt_u32 s[62:63], 2, v165\nglobal_load_lds_dwordx4 v27, s[6:7] offset:0\nv_lshl_add_u32 v27, v196, 7, v4\nv_and_b32 v165, 60, v240\ns_mov_b32 s42, 0\n"
      "v_cndmask_b32 v168, v168, v244, s[62:63]\ns_cbranch_vccz .La1021\ns_add_u32 m0, s26, 69632\ns_nop 0\nglobal_load_lds_dwordx4 v28, s[6:7] offset:0\n"
      ".La1021:\nv_lshl_add_u32 v28, v202, 7, v4\nv_lshl_or_b32 v244, v244, 16, v168\nv_lshl_add_u32 v244, v244, 5, v166\ns_add_u32 m0, s53, 45056\nv_lshl_add_u32 v252, v240, 4, s42\n"
      "global_load_lds_dwordx4 v22, s[6:7] offset:0\nv_lshl_add_u32 v22, v178, 7, v4\nv_and_b32 v108, s55, v56\nv_xor_b32 v56, v56, v108\nv_and_b32 v109, s55, v57\n"
      "global_load_lds_dwordx4 v23, s[44:45] offset:1024\nv_lshl_add_u32 v23, v180, 7, v5\nv_xor_b32 v57, v57, v109\ns_mov_b32 s42, 0x76325410\ns_mov_b32 s43, 0x21005410\n"
      "global_load_lds_dwordx4 v24, s[46:47] offset:2048\nv_lshl_add_u32 v24, v186, 7, v4\nv_lshrrev_b64 v[168:169], v165, s[42:43]\nv_and_b32 v168, 7, v168\nv_lshl_add_u32 v168, v164, 3, v168\n"
      "global_load_lds_dwordx4 v25, s[48:49] offset:3072\nv_lshl_add_u32 v25, v188, 7, v5\ns_mov_b32 s42, 0x54107632\ns_mov_b32 s43, 0x21107632\ns_add_u32 m0, s53, 49152\n"
      "v_lshrrev_b64 v[170:171], v165, s[42:43]\nglobal_load_lds_dwordx4 v26, s[6:7] offset:0\nv_lshl_add_u32 v26, v194, 7, v4\nv_and_b32 v170, 7, v170\nv_lshl_add_u32 v170, v164, 3, v170\n"
      "s_waitcnt vmcnt(11) lgkmcnt(0)\ns_cbranch_vccnz .Lw1022\ns_waitcnt vmcnt(10)\n"
      ".Lw1022:\ns_barrier\ns_add_u32 m0, s54, 90112\ns_nop 0\nglobal_load_lds_dwordx4 v236, s[50:51] offset:0\nds_read_b64 v[60:61], v30 offset:0\n"
      "global_load_lds_dwordx4 v237, s[50:51] offset:1024\nglobal_load_lds_dwordx4 v238, s[50:51] offset:2048\nglobal_load_lds_dwordx4 v239, s[50:51] offset:3072\ns_add_u32 s50, s50, 212992\n"
      "s_addc_u32 s51, s51, 0\nv_max3_f16 v32, |v58|, |v59|, v32\ns_add_u32 m0, s26, 110592\nv_max3_f32 v33, |v58|, |v59|, v33\nglobal_load_lds_dwordx4 v27, s[6:7] offset:0\n"
      "v_lshl_add_u32 v27, v197, 7, v4\nv_lshl_or_b32 v245, v170, 16, v168\nv_lshl_add_u32 v245, v245, 5, v166\ns_cbranch_vccz .La1023\ns_add_u32 m0, s26, 114688\ns_nop 0\n"
      "global_load_lds_dwordx4 v28, s[6:7] offset:0\n"
      ".La1023:\nv_lshl_add_u32 v28, v203, 7, v4\nds_read2_b64 v[200:203], v9 offset0:6 offset1:7\nv_and_b32 v165, 1, v240\ns_add_u32 m0, s53, 90112\nv_mul_u32_u24 v165, 0xaaaa, v165\n"
      "global_load_lds_dwordx4 v22, s[6:7] offset:0\nv_lshl_add_u32 v22, v179, 7, v4\nds_read2_b64 v[176:179], v6 offset0:6 offset1:7\nv_add_u32 v242, 0x4444, v165\ns_lshl_b32 s42, s36, 4\n"
      "global_load_lds_dwordx4 v23, s[44:45] offset:1024\nv_lshl_add_u32 v23, v181, 7, v5\ns_add_u32 s42, s42, 160448\nv_lshl_add_u32 v243, v164, 2, s42\n"
      "global_load_lds_dwordx4 v24, s[46:47] offset:2048\nv_lshl_add_u32 v24, v187, 7, v4\nds_read2_b64 v[184:187], v7 offset0:6 offset1:7\nv_and_b32 v110, s55, v58\nv_xor_b32 v58, v58, v110\n"
      "global_load_lds_dwordx4 v25, s[48:49] offset:3072\nv_lshl_add_u32 v25, v189, 7, v5\ns_add_u32 m0, s53, 94208\nv_and_b32 v111, s55, v59\nglobal_load_lds_dwordx4 v26, s[6:7] offset:0\n"
      "v_lshl_add_u32 v26, v195, 7, v4\nds_read2_b64 v[192:195], v8 offset0:6 offset1:7\nv_xor_b32 v59, v59, v111\ns_waitcnt vmcnt(11) lgkmcnt(0)\ns_cbranch_vccnz .Lw1024\ns_waitcnt vmcnt(10)\n"
      ".Lw1024:\ns_barrier\ns_add_u32 m0, s54, 0\ns_nop 0\nglobal_load_lds_dwordx4 v236, s[50:51] offset:0\nds_read_b64 v[62:63], v30 offset:45056\n"
      "global_load_lds_dwordx4 v237, s[50:51] offset:1024\nglobal_load_lds_dwordx4 v238, s[50:51] offset:2048\nglobal_load_lds_dwordx4 v239, s[50:51] offset:3072\ns_add_u32 s50, s50, 212992\n"
      "s_addc_u32 s51, s51, 0\ns_cmp_eq_u32 s27, 14\ns_cselect_b64 s[50:51], s[10:11], s[50:51]\nv_max3_f16 v32, |v60|, |v61|, v32\nv_max3_f32 v33, |v60|, |v61|, v33\ns_add_u32 m0, s26, 20480\n"
      "v_and_b32 v164, 1, v164\nglobal_load_lds_dwordx4 v27, s[6:7] offset:0\nv_lshl_add_u32 v27, v198, 7, v4\nv_lshl_add_u32 v164, v164, 3, s24\ns_sub_u32 s42, s64, 1\nv_min_u32 v164, s42, v164\n"
      "s_cbranch_vccz .La1025\ns_add_u32 m0, s26, 24576\ns_nop 0\nglobal_load_lds_dwordx4 v28, s[6:7] offset:0\n"
      ".La1025:\nv_lshl_add_u32 v28, v200, 7, v4\nv_lshlrev_b32 v164, 3, v164\nv_and_b32 v165, 15, v240\ns_add_u32 m0, s53, 0\nv_lshl_add_u32 v250, v164, 1, v165\n"
      "global_load_lds_dwordx4 v22, s[6:7] offset:0\nv_lshl_add_u32 v22, v176, 7, v4\ns_mov_b32 s42, 136192\nv_lshl_add_u32 v250, v250, 2, s42\nglobal_load_lds_dwordx4 v23, s[44:45] offset:1024\n"
      "v_lshl_add_u32 v23, v182, 7, v5\nv_min_u32 v165, 12, v165\ns_lshl_b32 s42, s30, 7\nglobal_load_lds_dwordx4 v24, s[46:47] offset:2048\nv_lshl_add_u32 v24, v184, 7, v4\n"
      "v_lshl_add_u32 v165, v165, 15, s42\nv_add_u32 v249, v165, v164\nglobal_load_lds_dwordx4 v25, s[48:49] offset:3072\nv_lshl_add_u32 v25, v190, 7, v5\nv_and_b32 v112, s55, v60\n"
      "s_add_u32 m0, s53, 4096\nv_xor_b32 v60, v60, v112\nglobal_load_lds_dwordx4 v26, s[6:7] offset:0\nv_lshl_add_u32 v26, v192, 7, v4\nv_and_b32 v113, s55, v61\nv_xor_b32 v61, v61, v113\n"
      "s_waitcnt vmcnt(11) lgkmcnt(0)\ns_cbranch_vccnz .Lw1026\ns_waitcnt vmcnt(10)\n"
      ".Lw1026:\ns_barrier\ns_add_u32 m0, s54, 45056\ns_nop 0\nglobal_load_lds_dwordx4 v236, s[50:51] offset:0\nds_read_b64 v[64:65], v31 offset:0\n"
      "global_load_lds_dwordx4 v237, s[50:51] offset:1024\nglobal_load_lds_dwordx4 v238, s[50:51] offset:2048\nglobal_load_lds_dwordx4 v239, s[50:51] offset:3072\ns_add_u32 s50, s50, 212992\n"
      "s_addc_u32 s51, s51, 0\nv_max3_f16 v32, |v62|, |v63|, v32\ns_add_u32 m0, s26, 65536\nv_max3_f32 v33, |v62|, |v63|, v33\nglobal_load_lds_dwordx4 v27, s[6:7] offset:0\n"
      "v_lshl_add_u32 v27, v199, 7, v4\nds_read2_b64 v[196:199], v8 offset0:119 offset1:120\ns_mul_i32 s65, s36, 22272\ns_add_u32 s65, s65, 45056\ns_cbranch_vccz .La1027\ns_add_u32 m0, s26, 69632\n"
      "s_nop 0\nglobal_load_lds_dwordx4 v28, s[6:7] offset:0\n"
      ".La1027:\nv_lshl_add_u32 v28, v201, 7, v4\ns_add_u32 s68, s64, 7\ns_min_u32 s68, s68, 21\ns_add_u32 m0, s53, 45056\ns_nop 0\nglobal_load_lds_dwordx4 v22, s[6:7] offset:0\n"
      "v_lshl_add_u32 v22, v177, 7, v4\ns_lshl_b32 s38, s38, 16\nglobal_load_lds_dwordx4 v23, s[44:45] offset:1024\nv_lshl_add_u32 v23, v183, 7, v5\n"
      "ds_read2_b64 v[180:183], v6 offset0:119 offset1:120\nv_and_b32 v114, s55, v62\nglobal_load_lds_dwordx4 v24, s[46:47] offset:2048\nv_lshl_add_u32 v24, v185, 7, v4\nv_xor_b32 v62, v62, v114\n"
      "global_load_lds_dwordx4 v25, s[48:49] offset:3072\nv_lshl_add_u32 v25, v191, 7, v5\nds_read2_b64 v[188:191], v7 offset0:119 offset1:120\ns_add_u32 m0, s53, 49152\nv_and_b32 v115, s55, v63\n"
      "global_load_lds_dwordx4 v26, s[6:7] offset:0\nv_lshl_add_u32 v26, v193, 7, v4\nv_xor_b32 v63, v63, v115\ns_waitcnt vmcnt(11) lgkmcnt(0)\ns_cbranch_vccnz .Lw1028\ns_waitcnt vmcnt(10)\n"
      ".Lw1028:\ns_barrier\ns_add_u32 m0, s54, 90112\ns_nop 0\nglobal_load_lds_dwordx4 v236, s[50:51] offset:0\nds_read_b64 v[66:67], v30 offset:0\n"
      "global_load_lds_dwordx4 v237, s[50:51] offset:1024\nglobal_load_lds_dwordx4 v238, s[50:51] offset:2048\nglobal_load_lds_dwordx4 v239, s[50:51] offset:3072\ns_add_u32 s50, s50, 212992\n"
      "s_addc_u32 s51, s51, 0\ns_cmp_eq_u32 s27, 12\ns_cselect_b64 s[50:51], s[10:11], s[50:51]\nv_max3_f16 v32, |v64|, |v65|, v32\nv_max3_f32 v33, |v64|, |v65|, v33\ns_lshr_b32 s42, s24, 2\n"
      "s_add_u32 m0, s26, 110592\ns_nop 0\nglobal_load_lds_dwordx4 v27, s[6:7] offset:0\nv_lshl_add_u32 v27, v196, 7, v4\ns_mul_i32 s43, s42, 3\ns_xor_b32 s43, s43, s36\ns_cbranch_vccz .La1029\n"
      "s_add_u32 m0, s26, 114688\ns_nop 0\nglobal_load_lds_dwordx4 v28, s[6:7] offset:0\n"
      ".La1029:\nv_lshl_add_u32 v28, v202, 7, v4\ns_add_u32 s42, s42, s64\ns_add_u32 s42, s42, 8\ns_add_u32 m0, s53, 90112\ns_nop 0\nglobal_load_lds_dwordx4 v22, s[6:7] offset:0\n"
      "v_lshl_add_u32 v22, v178, 7, v4\ns_and_b32 s42, s42, 1\ns_lshl1_add_u32 s67, s43, s42\nglobal_load_lds_dwordx4 v23, s[44:45] offset:1024\nv_lshl_add_u32 v23, v180, 7, v5\n"
      "s_add_u32 s66, s64, s67\ns_cmp_lt_u32 s66, s68\ns_cselect_b32 s66, s66, 127\nglobal_load_lds_dwordx4 v24, s[46:47] offset:2048\nv_lshl_add_u32 v24, v186, 7, v4\ns_sub_u32 s66, s66, 7\n"
      "s_max_i32 s66, s66, 0\nglobal_load_lds_dwordx4 v25, s[48:49] offset:3072\nv_lshl_add_u32 v25, v188, 7, v5\nv_and_b32 v116, s55, v64\ns_add_u32 m0, s53, 94208\nv_xor_b32 v64, v64, v116\n"
      "global_load_lds_dwordx4 v26, s[6:7] offset:0\nv_lshl_add_u32 v26, v194, 7, v4\nv_and_b32 v117, s55, v65\nv_xor_b32 v65, v65, v117\ns_waitcnt vmcnt(11) lgkmcnt(0)\ns_cbranch_vccnz .Lw1030\n"
      "s_waitcnt vmcnt(10)\n"
      ".Lw1030:\ns_barrier\ns_add_u32 m0, s54, 0\ns_nop 0\nglobal_load_lds_dwordx4 v236, s[50:51] offset:0\nds_read_b64 v[68:69], v30 offset:45056\n"
      "global_load_lds_dwordx4 v237, s[50:51] offset:1024\nglobal_load_lds_dwordx4 v238, s[50:51] offset:2048\nglobal_load_lds_dwordx4 v239, s[50:51] offset:3072\ns_add_u32 s50, s50, 212992\n"
      "s_addc_u32 s51, s51, 0\ns_add_u32 m0, s26, 20480\nv_max3_f16 v32, |v66|, |v67|, v32\nglobal_load_lds_dwordx4 v27, s[6:7] offset:0\nv_lshl_add_u32 v27, v197, 7, v4\n"
      "v_max3_f32 v33, |v66|, |v67|, v33\ns_cbranch_vccz .La1031\ns_add_u32 m0, s26, 24576\ns_nop 0\nglobal_load_lds_dwordx4 v28, s[6:7] offset:0\n"
      ".La1031:\nv_lshl_add_u32 v28, v203, 7, v4\nds_read2_b64 v[200:203], v9 offset0:8 offset1:9\ns_getpc_b64 s[34:35]\n"
      ".Lk1005:\ns_add_u32 m0, s53, 0\ns_nop 0\nglobal_load_lds_dwordx4 v22, s[6:7] offset:0\nv_lshl_add_u32 v22, v179, 7, v4\nds_read2_b64 v[176:179], v6 offset0:8 offset1:9\n"
      "s_add_u32 s34, s34, .Lt_p-.Lk1005\ns_addc_u32 s35, s35, 0\nglobal_load_lds_dwordx4 v23, s[44:45] offset:1024\nv_lshl_add_u32 v23, v181, 7, v5\nv_and_b32 v118, s55, v66\n"
      "global_load_lds_dwordx4 v24, s[46:47] offset:2048\nv_lshl_add_u32 v24, v187, 7, v4\nds_read2_b64 v[184:187], v7 offset0:8 offset1:9\nv_xor_b32 v66, v66, v118\n"
      "global_load_lds_dwordx4 v25, s[48:49] offset:3072\nv_lshl_add_u32 v25, v189, 7, v5\ns_add_u32 m0, s53, 4096\nv_and_b32 v119, s55, v67\nglobal_load_lds_dwordx4 v26, s[6:7] offset:0\n"
      "v_lshl_add_u32 v26, v195, 7, v4\nds_read2_b64 v[192:195], v8 offset0:8 offset1:9\nv_xor_b32 v67, v67, v119\ns_waitcnt vmcnt(11) lgkmcnt(0)\ns_cbranch_vccnz .Lw1032\ns_waitcnt vmcnt(10)\n"
      ".Lw1032:\ns_barrier\ns_add_u32 m0, s54, 45056\ns_nop 0\nglobal_load_lds_dwordx4 v236, s[50:51] offset:0\nds_read_b64 v[70:71], v31 offset:0\n"
      "global_load_lds_dwordx4 v237, s[50:51] offset:1024\nglobal_load_lds_dwordx4 v238, s[50:51] offset:2048\nglobal_load_lds_dwordx4 v239, s[50:51] offset:3072\ns_add_u32 s50, s50, 212992\n"
      "s_addc_u32 s51, s51, 0\ns_cmp_eq_u32 s27, 10\ns_cselect_b64 s[50:51], s[10:11], s[50:51]\nv_max3_f16 v32, |v68|, |v69|, v32\ns_add_u32 m0, s26, 65536\nv_max3_f32 v33, |v68|, |v69|, v33\n"
      "global_load_lds_dwordx4 v27, s[6:7] offset:0\nv_lshl_add_u32 v27, v198, 7, v4\ns_lshr_b32 s42, s2, 3\ns_lshl1_add_u32 s42, s2, s42\ns_cbranch_vccz .La1033\ns_add_u32 m0, s26, 69632\n"
      "s_nop 0\nglobal_load_lds_dwordx4 v28, s[6:7] offset:0\n"
      ".La1033:\nv_lshl_add_u32 v28, v200, 7, v4\ns_and_b32 s69, s42, 15\ns_lshl_b32 s43, s69, 7\ns_add_u32 m0, s53, 45056\ns_nop 0\nglobal_load_lds_dwordx4 v22, s[6:7] offset:0\n"
      "v_lshl_add_u32 v22, v176, 7, v4\ns_add_u32 s43, s43, 32768\ns_add_u32 s8, s20, s43\ns_addc_u32 s9, s21, 0\nglobal_load_lds_dwordx4 v23, s[44:45] offset:1024\n"
      "v_lshl_add_u32 v23, v182, 7, v5\ns_add_u32 s12, s20, 65536\ns_addc_u32 s13, s21, 0\ns_add_u32 s16, s20, 2097152\ns_addc_u32 s17, s21, 0\nglobal_load_lds_dwordx4 v24, s[46:47] offset:2048\n"
      "v_lshl_add_u32 v24, v184, 7, v4\nv_and_b32 v120, s55, v68\nv_xor_b32 v68, v68, v120\nglobal_load_lds_dwordx4 v25, s[48:49] offset:3072\nv_lshl_add_u32 v25, v190, 7, v5\n"
      "s_add_u32 m0, s53, 49152\nv_and_b32 v121, s55, v69\nglobal_load_lds_dwordx4 v26, s[6:7] offset:0\nv_lshl_add_u32 v26, v192, 7, v4\nv_xor_b32 v69, v69, v121\ns_waitcnt vmcnt(11) lgkmcnt(0)\n"
      "s_cbranch_vccnz .Lw1034\ns_waitcnt vmcnt(10)\n"
      ".Lw1034:\ns_barrier\ns_add_u32 m0, s54, 90112\ns_nop 0\nglobal_load_lds_dwordx4 v236, s[50:51] offset:0\nds_read_b64 v[72:73], v30 offset:0\n"
      "global_load_lds_dwordx4 v237, s[50:51] offset:1024\nglobal_load_lds_dwordx4 v238, s[50:51] offset:2048\nglobal_load_lds_dwordx4 v239, s[50:51] offset:3072\ns_add_u32 s50, s50, 212992\n"
      "s_addc_u32 s51, s51, 0\nv_max3_f16 v32, |v70|, |v71|, v32\ns_add_u32 m0, s26, 110592\nv_max3_f32 v33, |v70|, |v71|, v33\nglobal_load_lds_dwordx4 v27, s[6:7] offset:0\n"
      "v_lshl_add_u32 v27, v199, 7, v4\nds_read2_b64 v[196:199], v8 offset0:121 offset1:122\ns_lshl_b32 s42, s69, 4\ns_max_u32 s42, s42, 24\ns_cbranch_vccz .La1035\ns_add_u32 m0, s26, 114688\n"
      "s_nop 0\nglobal_load_lds_dwordx4 v28, s[6:7] offset:0\n"
      ".La1035:\nv_lshl_add_u32 v28, v201, 7, v4\ns_sub_u32 s42, s42, 24\ns_min_u32 s69, s42, 192\ns_add_u32 m0, s53, 90112\ns_nop 0\nglobal_load_lds_dwordx4 v22, s[6:7] offset:0\n"
      "v_lshl_add_u32 v22, v177, 7, v4\ns_lshl_b32 s42, s69, 3\ns_add_u32 s42, s42, 24576\nglobal_load_lds_dwordx4 v23, s[44:45] offset:1024\nv_lshl_add_u32 v23, v183, 7, v5\n"
      "ds_read2_b64 v[180:183], v6 offset0:121 offset1:122\ns_add_u32 s14, s20, s42\ns_addc_u32 s15, s21, 0\nv_and_b32 v122, s55, v70\nglobal_load_lds_dwordx4 v24, s[46:47] offset:2048\n"
      "v_lshl_add_u32 v24, v185, 7, v4\nv_xor_b32 v70, v70, v122\nglobal_load_lds_dwordx4 v25, s[48:49] offset:3072\nv_lshl_add_u32 v25, v191, 7, v5\n"
      "ds_read2_b64 v[188:191], v7 offset0:121 offset1:122\ns_add_u32 m0, s53, 94208\nv_and_b32 v123, s55, v71\nglobal_load_lds_dwordx4 v26, s[6:7] offset:0\nv_lshl_add_u32 v26, v193, 7, v4\n"
      "v_xor_b32 v71, v71, v123\ns_waitcnt vmcnt(11) lgkmcnt(0)\ns_cbranch_vccnz .Lw1036\ns_waitcnt vmcnt(10)\n"
      ".Lw1036:\ns_barrier\ns_add_u32 m0, s54, 0\ns_nop 0\nglobal_load_lds_dwordx4 v236, s[50:51] offset:0\nds_read_b64 v[74:75], v30 offset:45056\n"
      "global_load_lds_dwordx4 v237, s[50:51] offset:1024\nglobal_load_lds_dwordx4 v238, s[50:51] offset:2048\nglobal_load_lds_dwordx4 v239, s[50:51] offset:3072\ns_add_u32 s50, s50, 212992\n"
      "s_addc_u32 s51, s51, 0\ns_cmp_eq_u32 s27, 8\ns_cselect_b64 s[50:51], s[10:11], s[50:51]\nv_max3_f16 v32, |v72|, |v73|, v32\ns_add_u32 m0, s26, 20480\nv_max3_f32 v33, |v72|, |v73|, v33\n"
      "global_load_lds_dwordx4 v27, s[6:7] offset:0\nv_lshl_add_u32 v27, v196, 7, v4\ns_lshl_b32 s42, s2, 3\ns_add_u32 s42, s42, 4096\ns_cbranch_vccz .La1037\ns_add_u32 m0, s26, 24576\ns_nop 0\n"
      "global_load_lds_dwordx4 v28, s[6:7] offset:0\n"
      ".La1037:\nv_lshl_add_u32 v28, v202, 7, v4\ns_add_u32 s58, s20, s42\ns_addc_u32 s59, s21, 0\ns_add_u32 m0, s53, 0\nv_and_b32 v38, 7, v240\nglobal_load_lds_dwordx4 v22, s[6:7] offset:0\n"
      "v_lshl_add_u32 v22, v178, 7, v4\nv_lshlrev_b32 v38, 11, v38\ns_xor_b32 s40, s37, 0xa5c30f96\nglobal_load_lds_dwordx4 v23, s[44:45] offset:1024\nv_lshl_add_u32 v23, v180, 7, v5\n"
      "s_mul_i32 s41, s37, 0x9E3779B1\ns_lshr_b32 s41, s41, 24\nglobal_load_lds_dwordx4 v24, s[46:47] offset:2048\nv_lshl_add_u32 v24, v186, 7, v4\nv_and_b32 v124, s55, v72\n"
      "v_xor_b32 v72, v72, v124\nglobal_load_lds_dwordx4 v25, s[48:49] offset:3072\nv_lshl_add_u32 v25, v188, 7, v5\ns_add_u32 m0, s53, 4096\nv_and_b32 v125, s55, v73\n"
      "global_load_lds_dwordx4 v26, s[6:7] offset:0\nv_lshl_add_u32 v26, v194, 7, v4\nv_xor_b32 v73, v73, v125\ns_waitcnt vmcnt(11) lgkmcnt(0)\ns_cbranch_vccnz .Lw1038\ns_waitcnt vmcnt(10)\n"
      ".Lw1038:\ns_barrier\ns_add_u32 m0, s54, 45056\ns_nop 0\nglobal_load_lds_dwordx4 v236, s[50:51] offset:0\nds_read_b64 v[76:77], v31 offset:0\n"
      "global_load_lds_dwordx4 v237, s[50:51] offset:1024\nglobal_load_lds_dwordx4 v238, s[50:51] offset:2048\nglobal_load_lds_dwordx4 v239, s[50:51] offset:3072\ns_add_u32 s50, s50, 212992\n"
      "s_addc_u32 s51, s51, 0\nv_max3_f16 v32, |v74|, |v75|, v32\ns_add_u32 m0, s26, 65536\nv_max3_f32 v33, |v74|, |v75|, v33\nglobal_load_lds_dwordx4 v27, s[6:7] offset:0\n"
      "v_lshl_add_u32 v27, v197, 7, v4\nv_lshrrev_b32 v164, 2, v240\nv_and_b32 v148, 3, v240\ns_cbranch_vccz .La1039\ns_add_u32 m0, s26, 69632\ns_nop 0\n"
      "global_load_lds_dwordx4 v28, s[6:7] offset:0\n"
      ".La1039:\nv_lshl_add_u32 v28, v203, 7, v4\nds_read2_b64 v[200:203], v9 offset0:10 offset1:11\nv_lshl_add_u32 v148, v148, 3, s65\ns_add_u32 m0, s53, 45056\nv_mul_u32_u24 v164, 928, v164\n"
      "global_load_lds_dwordx4 v22, s[6:7] offset:0\nv_lshl_add_u32 v22, v179, 7, v4\nds_read2_b64 v[176:179], v6 offset0:10 offset1:11\nv_add_u32 v149, s65, v164\nv_add_u32 v148, v148, v164\n"
      "global_load_lds_dwordx4 v23, s[44:45] offset:1024\nv_lshl_add_u32 v23, v181, 7, v5\ns_add_u32 s42, s65, 7424\nv_add_u32 v163, s42, v252\nglobal_load_lds_dwordx4 v24, s[46:47] offset:2048\n"
      "v_lshl_add_u32 v24, v187, 7, v4\nds_read2_b64 v[184:187], v7 offset0:10 offset1:11\nv_and_b32 v126, s55, v74\nv_xor_b32 v74, v74, v126\nglobal_load_lds_dwordx4 v25, s[48:49] offset:3072\n"
      "v_lshl_add_u32 v25, v189, 7, v5\ns_add_u32 m0, s53, 49152\nv_and_b32 v127, s55, v75\nglobal_load_lds_dwordx4 v26, s[6:7] offset:0\nv_lshl_add_u32 v26, v195, 7, v4\n"
      "ds_read2_b64 v[192:195], v8 offset0:10 offset1:11\nv_xor_b32 v75, v75, v127\ns_waitcnt vmcnt(11) lgkmcnt(0)\ns_cbranch_vccnz .Lw1040\ns_waitcnt vmcnt(10)\n"
      ".Lw1040:\ns_barrier\ns_add_u32 m0, s54, 90112\ns_nop 0\nglobal_load_lds_dwordx4 v236, s[50:51] offset:0\nds_read_b64 v[78:79], v30 offset:0\n"
      "global_load_lds_dwordx4 v237, s[50:51] offset:1024\nglobal_load_lds_dwordx4 v238, s[50:51] offset:2048\nglobal_load_lds_dwordx4 v239, s[50:51] offset:3072\ns_add_u32 s50, s50, 212992\n"
      "s_addc_u32 s51, s51, 0\ns_cmp_eq_u32 s27, 6\ns_cselect_b64 s[50:51], s[10:11], s[50:51]\nv_max3_f16 v32, |v76|, |v77|, v32\ns_add_u32 m0, s26, 110592\nv_max3_f32 v33, |v76|, |v77|, v33\n"
      "global_load_lds_dwordx4 v27, s[6:7] offset:0\nv_lshl_add_u32 v27, v198, 7, v4\ns_mul_i32 s42, s36, 10752\ns_add_u32 s56, s42, 0\ns_cbranch_vccz .La1041\ns_add_u32 m0, s26, 114688\ns_nop 0\n"
      "global_load_lds_dwordx4 v28, s[6:7] offset:0\n"
      ".La1041:\nv_lshl_add_u32 v28, v200, 7, v4\ns_add_u32 s42, s42, 1048576\ns_add_u32 m0, s53, 90112\ns_nop 0\nglobal_load_lds_dwordx4 v22, s[6:7] offset:0\nv_lshl_add_u32 v22, v176, 7, v4\n"
      "s_add_u32 s60, s20, s42\ns_addc_u32 s61, s21, 0\nglobal_load_lds_dwordx4 v23, s[44:45] offset:1024\nv_lshl_add_u32 v23, v182, 7, v5\nv_and_b32 v128, s55, v76\n"
      "global_load_lds_dwordx4 v24, s[46:47] offset:2048\nv_lshl_add_u32 v24, v184, 7, v4\nv_xor_b32 v76, v76, v128\nglobal_load_lds_dwordx4 v25, s[48:49] offset:3072\n"
      "v_lshl_add_u32 v25, v190, 7, v5\ns_add_u32 m0, s53, 94208\nv_and_b32 v129, s55, v77\nglobal_load_lds_dwordx4 v26, s[6:7] offset:0\nv_lshl_add_u32 v26, v192, 7, v4\nv_xor_b32 v77, v77, v129\n"
      "s_waitcnt vmcnt(11) lgkmcnt(0)\ns_cbranch_vccnz .Lw1042\ns_waitcnt vmcnt(10)\n"
      ".Lw1042:\ns_barrier\ns_add_u32 m0, s54, 0\ns_nop 0\nglobal_load_lds_dwordx4 v236, s[50:51] offset:0\nds_read_b64 v[80:81], v30 offset:45056\n"
      "global_load_lds_dwordx4 v237, s[50:51] offset:1024\nglobal_load_lds_dwordx4 v238, s[50:51] offset:2048\nglobal_load_lds_dwordx4 v239, s[50:51] offset:3072\ns_add_u32 s50, s50, 212992\n"
      "s_addc_u32 s51, s51, 0\nv_max3_f16 v32, |v78|, |v79|, v32\nv_max3_f32 v33, |v78|, |v79|, v33\ns_add_u32 s42, s27, 0\ns_add_u32 m0, s26, 20480\ns_nop 0\n"
      "global_load_lds_dwordx4 v27, s[6:7] offset:0\nv_lshl_add_u32 v27, v199, 7, v4\nds_read2_b64 v[196:199], v8 offset0:123 offset1:124\ns_sub_u32 s43, s42, 26\ns_min_u32 s42, s42, s43\n"
      "v_lshl_add_u32 v150, s42, 5, v148\ns_cbranch_vccz .La1043\ns_add_u32 m0, s26, 24576\ns_nop 0\nglobal_load_lds_dwordx4 v28, s[6:7] offset:0\n"
      ".La1043:\nv_lshl_add_u32 v28, v201, 7, v4\ns_add_u32 s42, s27, 2\ns_sub_u32 s43, s42, 26\ns_add_u32 m0, s53, 0\ns_nop 0\nglobal_load_lds_dwordx4 v22, s[6:7] offset:0\n"
      "v_lshl_add_u32 v22, v177, 7, v4\ns_min_u32 s42, s42, s43\nv_lshl_add_u32 v151, s42, 5, v148\nglobal_load_lds_dwordx4 v23, s[44:45] offset:1024\nv_lshl_add_u32 v23, v183, 7, v5\n"
      "ds_read2_b64 v[180:183], v6 offset0:123 offset1:124\ns_add_u32 s42, s27, 4\ns_sub_u32 s43, s42, 26\nglobal_load_lds_dwordx4 v24, s[46:47] offset:2048\nv_lshl_add_u32 v24, v185, 7, v4\n"
      "s_min_u32 s42, s42, s43\nv_lshl_add_u32 v152, s42, 5, v148\nglobal_load_lds_dwordx4 v25, s[48:49] offset:3072\nv_lshl_add_u32 v25, v191, 7, v5\n"
      "ds_read2_b64 v[188:191], v7 offset0:123 offset1:124\nv_and_b32 v130, s55, v78\ns_add_u32 m0, s53, 4096\nv_xor_b32 v78, v78, v130\nglobal_load_lds_dwordx4 v26, s[6:7] offset:0\n"
      "v_lshl_add_u32 v26, v193, 7, v4\nv_and_b32 v131, s55, v79\nv_xor_b32 v79, v79, v131\ns_waitcnt vmcnt(11) lgkmcnt(0)\ns_cbranch_vccnz .Lw1044\ns_waitcnt vmcnt(10)\n"
      ".Lw1044:\ns_barrier\ns_add_u32 m0, s54, 45056\ns_nop 0\nglobal_load_lds_dwordx4 v236, s[50:51] offset:0\nds_read_b64 v[82:83], v31 offset:0\n"
      "global_load_lds_dwordx4 v237, s[50:51] offset:1024\nglobal_load_lds_dwordx4 v238, s[50:51] offset:2048\nglobal_load_lds_dwordx4 v239, s[50:51] offset:3072\ns_add_u32 s50, s50, 212992\n"
      "s_addc_u32 s51, s51, 0\ns_cmp_eq_u32 s27, 4\ns_cselect_b64 s[50:51], s[10:11], s[50:51]\nv_max3_f16 v32, |v80|, |v81|, v32\nv_max3_f32 v33, |v80|, |v81|, v33\ns_add_u32 s42, s27, 6\n"
      "s_add_u32 m0, s26, 65536\ns_nop 0\nglobal_load_lds_dwordx4 v27, s[6:7] offset:0\nv_lshl_add_u32 v27, v196, 7, v4\ns_sub_u32 s43, s42, 26\ns_min_u32 s42, s42, s43\n"
      "v_lshl_add_u32 v153, s42, 5, v148\ns_cbranch_vccz .La1045\ns_add_u32 m0, s26, 69632\ns_nop 0\nglobal_load_lds_dwordx4 v28, s[6:7] offset:0\n"
      ".La1045:\nv_lshl_add_u32 v28, v202, 7, v4\ns_add_u32 s42, s27, 8\ns_sub_u32 s43, s42, 26\ns_add_u32 m0, s53, 45056\ns_nop 0\nglobal_load_lds_dwordx4 v22, s[6:7] offset:0\n"
      "v_lshl_add_u32 v22, v178, 7, v4\ns_min_u32 s42, s42, s43\nv_lshl_add_u32 v154, s42, 5, v148\nglobal_load_lds_dwordx4 v23, s[44:45] offset:1024\nv_lshl_add_u32 v23, v180, 7, v5\n"
      "s_add_u32 s42, s27, 10\ns_sub_u32 s43, s42, 26\nglobal_load_lds_dwordx4 v24, s[46:47] offset:2048\nv_lshl_add_u32 v24, v186, 7, v4\ns_min_u32 s42, s42, s43\n"
      "v_lshl_add_u32 v155, s42, 5, v148\nglobal_load_lds_dwordx4 v25, s[48:49] offset:3072\nv_lshl_add_u32 v25, v188, 7, v5\nv_and_b32 v132, s55, v80\ns_add_u32 m0, s53, 49152\n"
      "v_xor_b32 v80, v80, v132\nglobal_load_lds_dwordx4 v26, s[6:7] offset:0\nv_lshl_add_u32 v26, v194, 7, v4\nv_and_b32 v133, s55, v81\nv_xor_b32 v81, v81, v133\ns_cmp_lg_u32 s24, 3\n"
      "s_cbranch_scc1 .Lo1046\nglobal_load_dwordx2 v[36:37], v38, s[58:59] sc1\n"
      ".Lo1046:\ns_waitcnt vmcnt(11) lgkmcnt(0)\ns_cbranch_vccnz .Lw1047\ns_waitcnt vmcnt(10)\n"
      ".Lw1047:\ns_barrier\ns_add_u32 m0, s54, 90112\ns_nop 0\nglobal_load_lds_dwordx4 v236, s[50:51] offset:0\nds_read_b64 v[84:85], v30 offset:0\n"
      "global_load_lds_dwordx4 v237, s[50:51] offset:1024\nglobal_load_lds_dwordx4 v238, s[50:51] offset:2048\nglobal_load_lds_dwordx4 v239, s[50:51] offset:3072\ns_add_u32 s50, s50, 212992\n"
      "s_addc_u32 s51, s51, 0\nv_max3_f16 v32, |v82|, |v83|, v32\nv_max3_f32 v33, |v82|, |v83|, v33\ns_add_u32 s42, s27, 12\ns_add_u32 m0, s26, 110592\ns_nop 0\n"
      "global_load_lds_dwordx4 v27, s[6:7] offset:0\nv_lshl_add_u32 v27, v197, 7, v4\ns_sub_u32 s43, s42, 26\ns_min_u32 s42, s42, s43\nv_lshl_add_u32 v156, s42, 5, v148\ns_cbranch_vccz .La1048\n"
      "s_add_u32 m0, s26, 114688\ns_nop 0\nglobal_load_lds_dwordx4 v28, s[6:7] offset:0\n"
      ".La1048:\nv_lshl_add_u32 v28, v203, 7, v4\nds_read2_b64 v[200:203], v9 offset0:12 offset1:13\ns_add_u32 s42, s27, 14\ns_sub_u32 s43, s42, 26\ns_add_u32 m0, s53, 90112\ns_nop 0\n"
      "global_load_lds_dwordx4 v22, s[6:7] offset:0\nv_lshl_add_u32 v22, v179, 7, v4\nds_read2_b64 v[176:179], v6 offset0:12 offset1:13\ns_min_u32 s42, s42, s43\nv_lshl_add_u32 v157, s42, 5, v148\n"
      "global_load_lds_dwordx4 v23, s[44:45] offset:1024\nv_lshl_add_u32 v23, v181, 7, v5\ns_add_u32 s42, s27, 16\ns_sub_u32 s43, s42, 26\nglobal_load_lds_dwordx4 v24, s[46:47] offset:2048\n"
      "v_lshl_add_u32 v24, v187, 7, v4\nds_read2_b64 v[184:187], v7 offset0:12 offset1:13\ns_min_u32 s42, s42, s43\nv_lshl_add_u32 v158, s42, 5, v148\n"
      "global_load_lds_dwordx4 v25, s[48:49] offset:3072\nv_lshl_add_u32 v25, v189, 7, v5\nv_and_b32 v134, s55, v82\ns_add_u32 m0, s53, 94208\nv_xor_b32 v82, v82, v134\n"
      "global_load_lds_dwordx4 v26, s[6:7] offset:0\nv_lshl_add_u32 v26, v195, 7, v4\nds_read2_b64 v[192:195], v8 offset0:12 offset1:13\nv_and_b32 v135, s55, v83\nv_xor_b32 v83, v83, v135\n"
      "s_waitcnt vmcnt(11) lgkmcnt(0)\ns_cbranch_vccnz .Lw1049\ns_waitcnt vmcnt(10)\n"
      ".Lw1049:\ns_barrier\ns_add_u32 m0, s54, 0\ns_nop 0\nglobal_load_lds_dwordx4 v236, s[50:51] offset:0\nds_read_b64 v[86:87], v30 offset:45056\n"
      "global_load_lds_dwordx4 v237, s[50:51] offset:1024\nglobal_load_lds_dwordx4 v238, s[50:51] offset:2048\nglobal_load_lds_dwordx4 v239, s[50:51] offset:3072\ns_add_u32 s50, s50, 212992\n"
      "s_addc_u32 s51, s51, 0\ns_cmp_eq_u32 s27, 2\ns_cselect_b64 s[50:51], s[10:11], s[50:51]\nv_max3_f16 v32, |v84|, |v85|, v32\nv_max3_f32 v33, |v84|, |v85|, v33\ns_add_u32 s42, s27, 18\n"
      "s_add_u32 m0, s26, 20480\ns_nop 0\nglobal_load_lds_dwordx4 v27, s[6:7] offset:0\nv_lshl_add_u32 v27, v198, 7, v4\ns_sub_u32 s43, s42, 26\ns_min_u32 s42, s42, s43\n"
      "v_lshl_add_u32 v159, s42, 5, v148\ns_cbranch_vccz .La1050\ns_add_u32 m0, s26, 24576\ns_nop 0\nglobal_load_lds_dwordx4 v28, s[6:7] offset:0\n"
      ".La1050:\nv_lshl_add_u32 v28, v200, 7, v4\ns_add_u32 s42, s27, 20\ns_sub_u32 s43, s42, 26\ns_add_u32 m0, s53, 0\ns_nop 0\nglobal_load_lds_dwordx4 v22, s[6:7] offset:0\n"
      "v_lshl_add_u32 v22, v176, 7, v4\ns_min_u32 s42, s42, s43\nv_lshl_add_u32 v160, s42, 5, v148\nglobal_load_lds_dwordx4 v23, s[44:45] offset:1024\nv_lshl_add_u32 v23, v182, 7, v5\n"
      "s_add_u32 s42, s27, 22\ns_sub_u32 s43, s42, 26\nglobal_load_lds_dwordx4 v24, s[46:47] offset:2048\nv_lshl_add_u32 v24, v184, 7, v4\ns_min_u32 s42, s42, s43\n"
      "v_lshl_add_u32 v161, s42, 5, v148\nglobal_load_lds_dwordx4 v25, s[48:49] offset:3072\nv_lshl_add_u32 v25, v190, 7, v5\nv_and_b32 v136, s55, v84\ns_add_u32 m0, s53, 4096\n"
      "v_xor_b32 v84, v84, v136\nglobal_load_lds_dwordx4 v26, s[6:7] offset:0\nv_lshl_add_u32 v26, v192, 7, v4\nv_and_b32 v137, s55, v85\nv_xor_b32 v85, v85, v137\ns_waitcnt vmcnt(11) lgkmcnt(0)\n"
      "s_cbranch_vccnz .Lw1051\ns_waitcnt vmcnt(10)\n"
      ".Lw1051:\ns_barrier\ns_add_u32 m0, s54, 45056\ns_nop 0\nglobal_load_lds_dwordx4 v236, s[50:51] offset:0\nds_read_b64 v[88:89], v31 offset:0\n"
      "global_load_lds_dwordx4 v237, s[50:51] offset:1024\nglobal_load_lds_dwordx4 v238, s[50:51] offset:2048\nglobal_load_lds_dwordx4 v239, s[50:51] offset:3072\ns_add_u32 s50, s50, 212992\n"
      "s_addc_u32 s51, s51, 0\nv_max3_f16 v32, |v86|, |v87|, v32\ns_add_u32 m0, s26, 65536\nv_max3_f32 v33, |v86|, |v87|, v33\nglobal_load_lds_dwordx4 v27, s[6:7] offset:0\n"
      "v_lshl_add_u32 v27, v199, 7, v4\ns_add_u32 s42, s27, 24\ns_sub_u32 s43, s42, 26\ns_cbranch_vccz .La1052\ns_add_u32 m0, s26, 69632\ns_nop 0\nglobal_load_lds_dwordx4 v28, s[6:7] offset:0\n"
      ".La1052:\nv_lshl_add_u32 v28, v201, 7, v4\ns_min_u32 s42, s42, s43\ns_add_u32 m0, s53, 45056\ns_nop 0\nglobal_load_lds_dwordx4 v22, s[6:7] offset:0\nv_lshl_add_u32 v22, v177, 7, v4\n"
      "v_lshl_add_u32 v162, s42, 5, v148\nglobal_load_lds_dwordx4 v23, s[44:45] offset:1024\nv_lshl_add_u32 v23, v183, 7, v5\nv_and_b32 v138, s55, v86\n"
      "global_load_lds_dwordx4 v24, s[46:47] offset:2048\nv_lshl_add_u32 v24, v185, 7, v4\nv_xor_b32 v86, v86, v138\nglobal_load_lds_dwordx4 v25, s[48:49] offset:3072\n"
      "v_lshl_add_u32 v25, v191, 7, v5\ns_add_u32 m0, s53, 49152\nv_and_b32 v139, s55, v87\nglobal_load_lds_dwordx4 v26, s[6:7] offset:0\nv_lshl_add_u32 v26, v193, 7, v4\nv_xor_b32 v87, v87, v139\n"
      "s_cmp_lg_u32 s24, 3\ns_cbranch_scc1 .Lo1055\ns_mov_b32 s42, 0\n"
      ".Lq1053:\nv_and_b32 v164, 0xff, v36\nv_xor_b32 v164, s41, v164\nv_xor_b32 v165, s40, v37\nv_or_b32 v164, v164, v165\nv_cmp_ne_u32 s[62:63], 0, v164\nv_lshrrev_b32 v164, 8, v36\n"
      "s_cmp_eq_u64 s[62:63], 0\ns_cbranch_scc1 .Lr1054\ns_sleep 1\nglobal_load_dwordx2 v[36:37], v38, s[58:59] sc1\ns_add_u32 s42, s42, 1\ns_bitcmp0_b32 s42, 22\ns_waitcnt vmcnt(0)\n"
      "s_cbranch_scc1 .Lq1053\n"
      ".Lr1054:\nds_write_b32 v253, v164 offset:24\n"
      ".Lo1055:\ns_waitcnt vmcnt(11) lgkmcnt(0)\ns_cbranch_vccnz .Lw1056\ns_waitcnt vmcnt(10)\n"
      ".Lw1056:\ns_barrier\ns_add_u32 m0, s54, 90112\ns_nop 0\nglobal_load_lds_dwordx4 v236, s[50:51] offset:0\nds_read_b64 v[90:91], v30 offset:0\n"
      "global_load_lds_dwordx4 v237, s[50:51] offset:1024\nglobal_load_lds_dwordx4 v238, s[50:51] offset:2048\nglobal_load_lds_dwordx4 v239, s[50:51] offset:3072\n"
      "v_max3_f16 v32, |v88|, |v89|, v32\nv_max3_f32 v33, |v88|, |v89|, v33\nv_and_b32 v140, s55, v88\ns_add_u32 m0, s26, 110592\nv_xor_b32 v88, v88, v140\n"
      "global_load_lds_dwordx4 v27, s[6:7] offset:0\nv_and_b32 v141, s55, v89\nv_xor_b32 v89, v89, v141\ns_lshl_b32 s42, s24, 6\nv_add_u32 v10, s42, v240\ns_cbranch_vccz .La1057\n"
      "s_add_u32 m0, s26, 114688\ns_nop 0\nglobal_load_lds_dwordx4 v28, s[6:7] offset:0\n"
      ".La1057:\nv_min_u32 v10, 83, v10\nv_and_b32 v11, 3, v10\nv_lshrrev_b32 v12, 2, v10\ns_add_u32 m0, s53, 90112\nv_mul_u32_u24 v11, 22272, v11\nglobal_load_lds_dwordx4 v22, s[6:7] offset:0\n"
      "v_mul_u32_u24 v13, 928, v12\ns_mov_b32 s42, 45920\nv_add3_u32 v11, v11, v13, s42\nglobal_load_lds_dwordx4 v23, s[44:45] offset:1024\ns_mov_b32 s42, 136192\nv_lshl_add_u32 v13, v10, 4, s42\n"
      "s_mov_b32 s42, 160448\nglobal_load_lds_dwordx4 v24, s[46:47] offset:2048\nv_lshl_add_u32 v10, v10, 4, s42\nv_min_u32 v14, 1, v12\nv_lshl_add_u32 v10, v14, 6, v10\n"
      "global_load_lds_dwordx4 v25, s[48:49] offset:3072\ns_bfm_b32 s42, s64, 0\nv_lshrrev_b32 v15, v12, s42\ns_add_u32 m0, s53, 94208\nv_subrev_u32 v14, s64, v12\n"
      "global_load_lds_dwordx4 v26, s[6:7] offset:0\nv_lshl_add_u32 v12, v240, 1, v248\nds_read_u16 v251, v12 offset:64\nds_read_b32 v9, v248 offset:24\ns_waitcnt vmcnt(11) lgkmcnt(0)\n"
      "s_cbranch_vccnz .Lw1058\ns_waitcnt vmcnt(10)\n"
      ".Lw1058:\ns_barrier\ns_add_u32 m0, s56, 0\ns_nop 0\nglobal_load_lds_dwordx4 v252, s[60:61] offset:0 sc1\nds_read_b64 v[92:93], v30 offset:45056\n"
      "global_load_lds_dwordx4 v252, s[60:61] offset:1024 sc1\nglobal_load_lds_dwordx4 v252, s[60:61] offset:2048 sc1\nglobal_load_lds_dwordx4 v252, s[60:61] offset:3072 sc1\n"
      "s_add_u32 s60, s60, 4096\ns_addc_u32 s61, s61, 0\nv_max3_f16 v32, |v90|, |v91|, v32\nv_max3_f32 v33, |v90|, |v91|, v33\nv_and_b32 v142, s55, v90\ns_add_u32 m0, s56, 4096\n"
      "v_xor_b32 v90, v90, v142\nglobal_load_lds_dwordx4 v252, s[60:61] offset:0 sc1\nv_and_b32 v143, s55, v91\nv_xor_b32 v91, v91, v143\nv_readfirstlane_b32 s39, v9\nv_readlane_b32 s57, v251, 0\n"
      "global_load_lds_dwordx4 v252, s[60:61] offset:1024 sc1\ns_xor_b32 s40, s39, 0xa5c30f96\ns_mul_i32 s41, s39, 0x9E3779B1\ns_lshr_b32 s41, s41, 24\ns_lshl_b32 s42, s2, 3\n"
      "global_load_lds_dwordx4 v252, s[60:61] offset:2048 sc1\ns_add_u32 s42, s42, 24576\nv_mov_b32 v7, s42\nv_mov_b32 v8, s41\nv_mov_b32 v9, s40\n"
      "global_load_lds_dwordx4 v252, s[60:61] offset:3072 sc1\ns_add_u32 s60, s60, 4096\ns_addc_u32 s61, s61, 0\ns_lshl_b32 s42, s2, 2\nv_mov_b32 v12, s42\ns_add_u32 s42, s37, 1\n"
      "s_add_u32 m0, s56, 8192\ns_nop 0\nglobal_load_lds_dwordx4 v252, s[60:61] offset:0 sc1\nv_mov_b32 v6, s42\nv_and_b32 v1, 48, v240\ns_mov_b32 s42, 41984\n"
      "global_load_lds_dwordx4 v252, s[60:61] offset:1024 sc1\nv_lshl_add_u32 v1, v1, 2, s42\ns_sub_u32 s49, s65, 928\n"
      "v_add_u32_sdwa v236, s49, v244 dst_sel:DWORD dst_unused:UNUSED_PAD src0_sel:DWORD src1_sel:WORD_0\nglobal_load_lds_dwordx4 v252, s[60:61] offset:1536 sc1\n"
      "v_add_u32_sdwa v237, s49, v244 dst_sel:DWORD dst_unused:UNUSED_PAD src0_sel:DWORD src1_sel:WORD_1\n"
      "v_add_u32_sdwa v238, s49, v245 dst_sel:DWORD dst_unused:UNUSED_PAD src0_sel:DWORD src1_sel:WORD_0\n"
      "v_add_u32_sdwa v239, s49, v245 dst_sel:DWORD dst_unused:UNUSED_PAD src0_sel:DWORD src1_sel:WORD_1\ns_waitcnt vmcnt(11) lgkmcnt(0)\ns_barrier\nds_read_b64 v[94:95], v31 offset:0\n"
      "v_max3_f16 v32, |v92|, |v93|, v32\nv_max3_f32 v33, |v92|, |v93|, v33\ns_nop 1\nv_and_b32 v144, s55, v92\nv_xor_b32 v92, v92, v144\ns_nop 1\nv_and_b32 v145, s55, v93\n"
      "v_xor_b32 v93, v93, v145\ns_lshl_b32 s42, s30, 6\ns_add_u32 s42, s42, s36\ns_add_u32 s42, s42, 60\ns_mulk_i32 s42, 896\ns_add_u32 s44, s16, s42\ns_addc_u32 s45, s17, 0\n"
      "s_waitcnt lgkmcnt(0)\nv_max3_f16 v32, |v94|, |v95|, v32\nv_max3_f32 v33, |v94|, |v95|, v33\ns_nop 1\nv_and_b32 v146, s55, v94\nv_xor_b32 v94, v94, v146\ns_nop 1\nv_and_b32 v147, s55, v95\n"
      "v_xor_b32 v95, v95, v147\ns_nop 1\nv_lshlrev_b32 v32, 16, v32\nv_max_u32 v33, v32, v33\ns_nop 1\nv_max_u32_dpp v33, v33, v33 quad_perm:[1,0,3,2] row_mask:0xf bank_mask:0xf\ns_nop 1\n"
      "v_max_u32_dpp v33, v33, v33 quad_perm:[2,3,0,1] row_mask:0xf bank_mask:0xf\nv_lshrrev_b32 v33, 23, v33\ns_nop 1\nv_subrev_u32 v32, 22, v33\nv_mul_u32_u24 v32, 0x800080, v32\ns_nop 1\n"
      "v_pk_sub_u16 v44, v44, v32 clamp\nv_pk_sub_u16 v45, v45, v32 clamp\ns_nop 1\nv_pk_sub_u16 v46, v46, v32 clamp\nv_pk_sub_u16 v47, v47, v32 clamp\ns_nop 1\nv_lshl_or_b32 v44, v44, 3, v96\n"
      "v_lshl_or_b32 v45, v45, 3, v97\ns_nop 1\nv_lshl_or_b32 v46, v46, 3, v98\nv_lshl_or_b32 v47, v47, 3, v99\ns_nop 1\nv_pk_sub_u16 v48, v48, v32 clamp\nv_pk_sub_u16 v49, v49, v32 clamp\n"
      "s_nop 1\nv_pk_sub_u16 v50, v50, v32 clamp\nv_pk_sub_u16 v51, v51, v32 clamp\ns_nop 1\nv_lshl_or_b32 v48, v48, 3, v100\nv_lshl_or_b32 v49, v49, 3, v101\ns_nop 1\n"
      "v_lshl_or_b32 v50, v50, 3, v102\nv_lshl_or_b32 v51, v51, 3, v103\ns_nop 1\nv_pk_sub_u16 v52, v52, v32 clamp\nv_pk_sub_u16 v53, v53, v32 clamp\ns_nop 1\nv_pk_sub_u16 v54, v54, v32 clamp\n"
      "v_pk_sub_u16 v55, v55, v32 clamp\ns_nop 1\nv_lshl_or_b32 v52, v52, 3, v104\nv_lshl_or_b32 v53, v53, 3, v105\ns_nop 1\nv_lshl_or_b32 v54, v54, 3, v106\nv_lshl_or_b32 v55, v55, 3, v107\n"
      "s_nop 1\nv_pk_sub_u16 v56, v56, v32 clamp\nv_pk_sub_u16 v57, v57, v32 clamp\ns_nop 1\nv_pk_sub_u16 v58, v58, v32 clamp\nv_pk_sub_u16 v59, v59, v32 clamp\ns_nop 1\n"
      "v_lshl_or_b32 v56, v56, 3, v108\nv_lshl_or_b32 v57, v57, 3, v109\ns_nop 1\nv_lshl_or_b32 v58, v58, 3, v110\nv_lshl_or_b32 v59, v59, 3, v111\ns_nop 1\nv_pk_sub_u16 v60, v60, v32 clamp\n"
      "v_pk_sub_u16 v61, v61, v32 clamp\ns_nop 1\nv_pk_sub_u16 v62, v62, v32 clamp\nv_pk_sub_u16 v63, v63, v32 clamp\ns_nop 1\nv_lshl_or_b32 v60, v60, 3, v112\nv_lshl_or_b32 v61, v61, 3, v113\n"
      "s_nop 1\nv_lshl_or_b32 v62, v62, 3, v114\nv_lshl_or_b32 v63, v63, 3, v115\ns_nop 1\nv_pk_sub_u16 v64, v64, v32 clamp\nv_pk_sub_u16 v65, v65, v32 clamp\ns_nop 1\n"
      "v_pk_sub_u16 v66, v66, v32 clamp\nv_pk_sub_u16 v67, v67, v32 clamp\ns_nop 1\nv_lshl_or_b32 v64, v64, 3, v116\nv_lshl_or_b32 v65, v65, 3, v117\ns_nop 1\nv_lshl_or_b32 v66, v66, 3, v118\n"
      "v_lshl_or_b32 v67, v67, 3, v119\ns_nop 1\nv_pk_sub_u16 v68, v68, v32 clamp\nv_pk_sub_u16 v69, v69, v32 clamp\ns_nop 1\nv_pk_sub_u16 v70, v70, v32 clamp\nv_pk_sub_u16 v71, v71, v32 clamp\n"
      "s_nop 1\nv_lshl_or_b32 v68, v68, 3, v120\nv_lshl_or_b32 v69, v69, 3, v121\ns_nop 1\nv_lshl_or_b32 v70, v70, 3, v122\nv_lshl_or_b32 v71, v71, 3, v123\ns_nop 1\n"
      "v_pk_sub_u16 v72, v72, v32 clamp\nv_pk_sub_u16 v73, v73, v32 clamp\ns_nop 1\nv_pk_sub_u16 v74, v74, v32 clamp\nv_pk_sub_u16 v75, v75, v32 clamp\ns_nop 1\nv_lshl_or_b32 v72, v72, 3, v124\n"
      "v_lshl_or_b32 v73, v73, 3, v125\ns_nop 1\nv_lshl_or_b32 v74, v74, 3, v126\nv_lshl_or_b32 v75, v75, 3, v127\ns_waitcnt vmcnt(0)\ns_barrier\nv_pk_sub_u16 v76, v76, v32 clamp\n"
      "v_pk_sub_u16 v77, v77, v32 clamp\ns_nop 1\nv_pk_sub_u16 v78, v78, v32 clamp\nv_pk_sub_u16 v79, v79, v32 clamp\ns_nop 1\nv_lshl_or_b32 v76, v76, 3, v128\nv_lshl_or_b32 v77, v77, 3, v129\n"
      "s_nop 1\nv_lshl_or_b32 v78, v78, 3, v130\nv_lshl_or_b32 v79, v79, 3, v131\ns_nop 1\nv_pk_sub_u16 v80, v80, v32 clamp\nv_pk_sub_u16 v81, v81, v32 clamp\ns_nop 1\n"
      "v_pk_sub_u16 v82, v82, v32 clamp\nv_pk_sub_u16 v83, v83, v32 clamp\ns_nop 1\nv_lshl_or_b32 v80, v80, 3, v132\nv_lshl_or_b32 v81, v81, 3, v133\ns_nop 1\nv_lshl_or_b32 v82, v82, 3, v134\n"
      "v_lshl_or_b32 v83, v83, 3, v135\ns_nop 1\nv_pk_sub_u16 v84, v84, v32 clamp\nv_pk_sub_u16 v85, v85, v32 clamp\ns_nop 1\nv_pk_sub_u16 v86, v86, v32 clamp\nv_pk_sub_u16 v87, v87, v32 clamp\n"
      "s_nop 1\nv_lshl_or_b32 v84, v84, 3, v136\nv_lshl_or_b32 v85, v85, 3, v137\ns_nop 1\nv_lshl_or_b32 v86, v86, 3, v138\nv_lshl_or_b32 v87, v87, 3, v139\ns_nop 1\n"
      "v_pk_sub_u16 v88, v88, v32 clamp\nv_pk_sub_u16 v89, v89, v32 clamp\ns_nop 1\nv_pk_sub_u16 v90, v90, v32 clamp\nv_pk_sub_u16 v91, v91, v32 clamp\ns_nop 1\nv_lshl_or_b32 v88, v88, 3, v140\n"
      "v_lshl_or_b32 v89, v89, 3, v141\ns_nop 1\nv_lshl_or_b32 v90, v90, 3, v142\nv_lshl_or_b32 v91, v91, 3, v143\ns_nop 1\nv_pk_sub_u16 v92, v92, v32 clamp\nv_pk_sub_u16 v93, v93, v32 clamp\n"
      "s_nop 1\nv_pk_sub_u16 v94, v94, v32 clamp\nv_pk_sub_u16 v95, v95, v32 clamp\ns_nop 1\nv_lshl_or_b32 v92, v92, 3, v144\nv_lshl_or_b32 v93, v93, 3, v145\ns_nop 1\n"
      "v_lshl_or_b32 v94, v94, 3, v146\nv_lshl_or_b32 v95, v95, 3, v147\ns_nop 1\nv_sub_u32 v4, 257, v33\nv_lshlrev_b32 v4, 23, v4\ns_nop 1\nv_cvt_pkrtz_f16_f32 v4, v4, v4\n"
      "v_lshlrev_b32 v33, 2, v33\ns_nop 1\nv_mov_b32 v5, v4\nv_subrev_u32 v33, 536, v33\ns_setprio 0\nds_write_b64 v148, v[4:5] offset:832\nds_write_b32 v149, v33 offset:864\n"
      "ds_write2_b64 v150, v[44:45], v[46:47] offset1:4\nds_write2_b64 v151, v[48:49], v[50:51] offset1:4\nds_write2_b64 v152, v[52:53], v[54:55] offset1:4\n"
      "ds_write2_b64 v153, v[56:57], v[58:59] offset1:4\nds_write2_b64 v154, v[60:61], v[62:63] offset1:4\nds_write2_b64 v155, v[64:65], v[66:67] offset1:4\n"
      "ds_write2_b64 v156, v[68:69], v[70:71] offset1:4\nds_write2_b64 v157, v[72:73], v[74:75] offset1:4\nds_write2_b64 v158, v[76:77], v[78:79] offset1:4\n"
      "ds_write2_b64 v159, v[80:81], v[82:83] offset1:4\nds_write2_b64 v160, v[84:85], v[86:87] offset1:4\nds_write2_b64 v161, v[88:89], v[90:91] offset1:4\n"
      "ds_write2_b64 v162, v[92:93], v[94:95] offset1:4\nds_read_b128 v[16:19], v163 offset:6496\nds_read_b128 v[20:23], v163 offset:5568\nds_read_b128 v[24:27], v163 offset:4640\n"
      "s_cmp_gt_u32 s64, 12\ns_cbranch_scc1 .Ld1060\nds_read_b128 v[28:31], v163 offset:3712\nds_read_b128 v[32:35], v163 offset:2784\nds_read_b128 v[36:39], v163 offset:1856\n"
      "ds_read_b128 v[40:43], v163 offset:928\nds_read_b128 v[44:47], v163 offset:0\n"
      ".Ld1060:\ns_mov_b32 exec_hi, 0x7fffff\ns_waitcnt lgkmcnt(0)\ns_cmp_gt_u32 s64, 15\ns_cbranch_scc1 .Ld1061\nglobal_store_dwordx4 v252, v[16:19], s[44:45] sc1\ns_sub_u32 s44, s44, 3584\n"
      "s_subb_u32 s45, s45, 0\ns_cmp_gt_u32 s64, 14\ns_cbranch_scc1 .Ld1061\nglobal_store_dwordx4 v252, v[20:23], s[44:45] sc1\ns_sub_u32 s44, s44, 3584\ns_subb_u32 s45, s45, 0\n"
      "s_cmp_gt_u32 s64, 13\ns_cbranch_scc1 .Ld1061\nglobal_store_dwordx4 v252, v[24:27], s[44:45] sc1\ns_sub_u32 s44, s44, 3584\ns_subb_u32 s45, s45, 0\ns_cmp_gt_u32 s64, 12\n"
      "s_cbranch_scc1 .Ld1061\nglobal_store_dwordx4 v252, v[28:31], s[44:45] sc1\ns_sub_u32 s44, s44, 3584\ns_subb_u32 s45, s45, 0\ns_cmp_gt_u32 s64, 11\ns_cbranch_scc1 .Ld1061\n"
      "global_store_dwordx4 v252, v[32:35], s[44:45] sc1\ns_sub_u32 s44, s44, 3584\ns_subb_u32 s45, s45, 0\ns_cmp_gt_u32 s64, 10\ns_cbranch_scc1 .Ld1061\n"
      "global_store_dwordx4 v252, v[36:39], s[44:45] sc1\ns_sub_u32 s44, s44, 3584\ns_subb_u32 s45, s45, 0\ns_cmp_gt_u32 s64, 9\ns_cbranch_scc1 .Ld1061\n"
      "global_store_dwordx4 v252, v[40:43], s[44:45] sc1\ns_sub_u32 s44, s44, 3584\ns_subb_u32 s45, s45, 0\ns_cmp_gt_u32 s64, 8\ns_cbranch_scc1 .Ld1061\n"
      "global_store_dwordx4 v252, v[44:47], s[44:45] sc1\ns_sub_u32 s44, s44, 3584\ns_subb_u32 s45, s45, 0\n"
      ".Ld1061:\ns_mov_b32 exec_hi, -1\nds_read_b128 v[48:51], v1 offset:0\nds_read_b128 v[52:55], v1 offset:16\nds_read_b128 v[56:59], v1 offset:32\nds_read_b128 v[60:63], v1 offset:48\n"
      "ds_read_b128 v[72:75], v252 offset:0\nds_read_b128 v[76:79], v252 offset:1024\nds_read_b128 v[80:83], v252 offset:2048\nds_read_b128 v[84:87], v252 offset:3072\n"
      "ds_read_b128 v[88:91], v252 offset:4096\nds_read_b128 v[92:95], v252 offset:5120\nds_read_b128 v[96:99], v252 offset:6144\nds_read_b128 v[100:103], v252 offset:7168\n"
      "ds_read_b128 v[104:107], v252 offset:8192\nds_read_b128 v[108:111], v252 offset:9216\nds_read_b128 v[112:115], v252 offset:10240\nds_read_b128 v[116:119], v252 offset:11264\n"
      "ds_read_b128 v[120:123], v252 offset:12288\nds_read_b128 v[124:127], v252 offset:13312\nds_read_b128 v[128:131], v252 offset:14336\nds_read_b128 v[132:135], v252 offset:15360\n"
      "ds_read_b128 v[136:139], v252 offset:16384\nds_read_b128 v[140:143], v252 offset:17408\nds_read_b128 v[144:147], v252 offset:18432\nds_read_b128 v[148:151], v252 offset:19456\n"
      "ds_read_b128 v[152:155], v252 offset:20480\nds_read_b128 v[156:159], v252 offset:21504\nds_read_b128 v[160:163], v252 offset:22528\nds_read_b128 v[164:167], v252 offset:23552\n"
      "ds_read_b128 v[168:171], v252 offset:24576\nds_read_b128 v[172:175], v252 offset:25600\nds_read_b128 v[176:179], v252 offset:26624\nds_read_b128 v[180:183], v252 offset:27648\n"
      "ds_read_b128 v[184:187], v252 offset:28672\nds_read_b128 v[188:191], v252 offset:29696\nds_read_b128 v[192:195], v252 offset:30720\nds_read_b128 v[196:199], v252 offset:31744\n"
      "ds_read_b128 v[200:203], v252 offset:32768\nds_read_b128 v[204:207], v252 offset:33792\nds_read_b128 v[208:211], v252 offset:34816\nds_read_b128 v[212:215], v252 offset:35840\n"
      "ds_read_b128 v[216:219], v252 offset:36864\nds_read_b128 v[220:223], v252 offset:37888\nds_read_b128 v[224:227], v252 offset:38912\nds_read_b128 v[228:231], v252 offset:39936\n"
      "ds_read_b128 v[232:235], v252 offset:40960\ns_mov_b32 s55, s57\ns_mov_b32 s45, -1\ns_mov_b32 s46, 0\ns_setprio 3\ns_waitcnt lgkmcnt(15)\nv_mul_f32 v48, v48, v56\nv_mul_f32 v49, v49, v57\n"
      "v_mul_f32 v50, v50, v58\nv_mul_f32 v51, v51, v59\nv_mul_f32 v52, v52, v60\nv_mul_f32 v53, v53, v61\nv_mul_f32 v54, v54, v62\nds_write_b128 v1, v[48:51] offset:256\n"
      "ds_write_b128 v1, v[52:55] offset:272\nds_read_b64_tr_b16 v[68:69], v238 offset:928\nds_read_b64_tr_b16 v[70:71], v239 offset:928\nds_read_b64_tr_b16 v[56:57], v236 offset:928\n"
      "ds_read_b64_tr_b16 v[58:59], v237 offset:1056\nds_read_b64_tr_b16 v[60:61], v237 offset:1184\nds_read_b64_tr_b16 v[62:63], v237 offset:1312\nds_read_b64_tr_b16 v[64:65], v237 offset:1440\n"
      "ds_read_b64_tr_b16 v[66:67], v237 offset:1568\ns_lshl_b32 s42, s30, 5\nv_mov_b32 v2, s42\ns_mov_b64 exec, 1\nglobal_load_dword v3, v2, s[22:23] sc1\ns_mov_b64 exec, -1\n"
      "s_swappc_b64 s[4:5], s[34:35]\ns_waitcnt vmcnt(0)\nds_read_b128 v[44:47], v248 offset:48\nds_add_rtn_u32 v0, v253, v241 offset:32\ns_waitcnt lgkmcnt(0)\nv_readfirstlane_b32 s42, v0\n"
      "s_cmp_lg_u32 s42, 3\ns_cbranch_scc1 .Lf1062\ns_mov_b64 exec, 1\nglobal_store_dwordx2 v7, v[8:9], s[20:21] sc1\nglobal_store_dword v12, v6, s[20:21] sc1\ns_mov_b64 exec, -1\n"
      ".Lf1062:\nv_readlane_b32 s48, v69, 48\ns_mov_b32 s56, 0\ns_mov_b32 s44, 0\ns_mov_b32 s33, s45\nv_pk_mul_f16 v20, v68, v70\nv_pk_mul_f16 v21, v69, v71\n"
      "v_pk_mul_f16 v22, v56, v56 op_sel_hi:[0,1]\nv_pk_mul_f16 v23, v56, v57 op_sel_hi:[0,1]\nv_pk_mul_f16 v16, v68, s48 op_sel:[0,1]\nv_pk_mul_f16 v17, v69, s48 op_sel:[0,1]\n"
      "v_pk_mul_f16 v18, v70, s48 op_sel:[0,1]\nv_pk_mul_f16 v19, v71, s48 op_sel:[0,1]\n.p2align 3\ns_nop 0\ns_barrier\nv_mfma_f32_16x16x32_f16 v[40:43], v[76:79], v[16:19], 0\n"
      "v_mfma_f32_16x16x32_f16 v[40:43], v[88:91], v[20:23], v[40:43]\ns_add_u32 s45, s45, 2\ns_cmp_ge_u32 s45, s46\ns_cbranch_scc0 .Lc_b0\ns_swappc_b64 s[4:5], s[34:35]\n"
      ".Lc_b0:\nv_mfma_f32_16x16x32_f16 v[36:39], v[84:87], v[20:23], 0\nv_pk_mul_f16 v24, v66, v66 op_sel_hi:[0,1]\nv_pk_mul_f16 v25, v66, v57 op_sel_hi:[0,1]\n"
      "v_mfma_f32_16x16x32_f16 v[36:39], v[72:75], v[16:19], v[36:39]\nv_pk_mul_f16 v26, v56, v66 op_sel_hi:[0,1]\nv_pk_mul_f16 v27, v56, v67 op_sel_hi:[0,1]\ns_setprio 0\n"
      "v_mfma_f32_16x16x32_f16 v[32:35], v[80:83], v[20:23], 0\nv_pk_mul_f16 v28, v58, v66 op_sel_hi:[0,1]\nv_pk_mul_f16 v29, v58, v67 op_sel_hi:[0,1]\ns_waitcnt lgkmcnt(0)\n"
      "v_smfmac_f32_16x16x64_f16 v[44:47], v[92:95], v[16:23], v242\nv_pk_mul_f16 v30, v60, v66 op_sel_hi:[0,1]\nv_pk_mul_f16 v31, v60, v67 op_sel_hi:[0,1]\n"
      "v_mfma_f32_16x16x32_f16 v[32:35], v[96:99], v[24:27], v[32:35]\nv_pk_mul_f16 v16, v62, v66 op_sel_hi:[0,1]\nv_pk_mul_f16 v17, v62, v67 op_sel_hi:[0,1]\ns_setprio 1\n"
      "v_mfma_f32_16x16x32_f16 v[32:35], v[108:111], v[28:31], v[32:35]\nv_pk_mul_f16 v18, v64, v66 op_sel_hi:[0,1]\nv_mfma_f32_16x16x32_f16 v[36:39], v[112:115], v[28:31], v[36:39]\n"
      "v_pk_mul_f16 v19, v64, v67 op_sel_hi:[0,1]\nv_mfma_f32_16x16x32_f16 v[36:39], v[100:103], v[24:27], v[36:39]\nv_pk_mul_f16 v20, v64, v64 op_sel_hi:[0,1]\n"
      "v_mfma_f32_16x16x32_f16 v[40:43], v[104:107], v[24:27], v[40:43]\nv_pk_mul_f16 v21, v64, v57 op_sel_hi:[0,1]\nv_mfma_f32_16x16x32_f16 v[40:43], v[116:119], v[28:31], v[40:43]\n"
      "v_pk_mul_f16 v22, v56, v64 op_sel_hi:[0,1]\nv_smfmac_f32_16x16x64_f16 v[44:47], v[120:123], v[24:31], v242\nv_pk_mul_f16 v23, v56, v65 op_sel_hi:[0,1]\n"
      "v_mfma_f32_16x16x32_f16 v[32:35], v[124:127], v[16:19], v[32:35]\nv_pk_mul_f16 v24, v58, v64 op_sel_hi:[0,1]\nv_pk_mul_f16 v25, v58, v65 op_sel_hi:[0,1]\ns_setprio 2\n"
      "ds_read_b64_tr_b16 v[70:71], v239 offset:1856\nv_mfma_f32_16x16x32_f16 v[32:35], v[136:139], v[20:23], v[32:35]\nv_pk_mul_f16 v26, v60, v64 op_sel_hi:[0,1]\n"
      "ds_read_b64_tr_b16 v[68:69], v238 offset:1856\nv_mfma_f32_16x16x32_f16 v[36:39], v[140:143], v[20:23], v[36:39]\nv_pk_mul_f16 v27, v60, v65 op_sel_hi:[0,1]\n"
      "ds_read_b64_tr_b16 v[66:67], v237 offset:2496\nv_mfma_f32_16x16x32_f16 v[36:39], v[128:131], v[16:19], v[36:39]\nv_pk_mul_f16 v28, v62, v64 op_sel_hi:[0,1]\n"
      "v_mfma_f32_16x16x32_f16 v[40:43], v[132:135], v[16:19], v[40:43]\nv_pk_mul_f16 v29, v62, v65 op_sel_hi:[0,1]\nv_mfma_f32_16x16x32_f16 v[40:43], v[144:147], v[20:23], v[40:43]\n"
      "v_pk_mul_f16 v30, v62, v62 op_sel_hi:[0,1]\nv_smfmac_f32_16x16x64_f16 v[44:47], v[148:151], v[16:23], v242\nv_pk_mul_f16 v31, v62, v57 op_sel_hi:[0,1]\n"
      "v_mfma_f32_16x16x32_f16 v[32:35], v[152:155], v[24:27], v[32:35]\nv_pk_mul_f16 v16, v56, v62 op_sel_hi:[0,1]\nv_pk_mul_f16 v17, v56, v63 op_sel_hi:[0,1]\n"
      "v_mfma_f32_16x16x32_f16 v[32:35], v[164:167], v[28:31], v[32:35]\nv_pk_mul_f16 v18, v58, v62 op_sel_hi:[0,1]\nds_read_b64_tr_b16 v[64:65], v237 offset:2368\n"
      "v_mfma_f32_16x16x32_f16 v[36:39], v[168:171], v[28:31], v[36:39]\nv_pk_mul_f16 v19, v58, v63 op_sel_hi:[0,1]\nv_mfma_f32_16x16x32_f16 v[36:39], v[156:159], v[24:27], v[36:39]\n"
      "v_pk_mul_f16 v20, v60, v62 op_sel_hi:[0,1]\ns_setprio 3\nv_mfma_f32_16x16x32_f16 v[40:43], v[160:163], v[24:27], v[40:43]\nv_pk_mul_f16 v21, v60, v63 op_sel_hi:[0,1]\ns_waitcnt lgkmcnt(1)\n"
      "v_readlane_b32 s48, v69, 48\nv_mfma_f32_16x16x32_f16 v[40:43], v[172:175], v[28:31], v[40:43]\nv_pk_mul_f16 v22, v60, v60 op_sel_hi:[0,1]\n"
      "v_smfmac_f32_16x16x64_f16 v[44:47], v[176:179], v[24:31], v242\nv_pk_mul_f16 v23, v60, v57 op_sel_hi:[0,1]\nv_mfma_f32_16x16x32_f16 v[32:35], v[180:183], v[16:19], v[32:35]\n"
      "v_pk_mul_f16 v24, v56, v60 op_sel_hi:[0,1]\nv_pk_mul_f16 v25, v56, v61 op_sel_hi:[0,1]\nv_mfma_f32_16x16x32_f16 v[32:35], v[192:195], v[20:23], v[32:35]\n"
      "v_pk_mul_f16 v26, v58, v60 op_sel_hi:[0,1]\nv_pk_mul_f16 v27, v58, v61 op_sel_hi:[0,1]\nv_mfma_f32_16x16x32_f16 v[36:39], v[196:199], v[20:23], v[36:39]\n"
      "v_pk_mul_f16 v28, v58, v58 op_sel_hi:[0,1]\nds_read_b64_tr_b16 v[62:63], v237 offset:2240\nv_mfma_f32_16x16x32_f16 v[36:39], v[184:187], v[16:19], v[36:39]\n"
      "v_pk_mul_f16 v29, v58, v57 op_sel_hi:[0,1]\nds_read_b64_tr_b16 v[60:61], v237 offset:2112\nv_mfma_f32_16x16x32_f16 v[40:43], v[188:191], v[16:19], v[40:43]\n"
      "v_pk_mul_f16 v30, v56, v58 op_sel_hi:[0,1]\nv_mfma_f32_16x16x32_f16 v[40:43], v[200:203], v[20:23], v[40:43]\nv_pk_mul_f16 v31, v56, v59 op_sel_hi:[0,1]\n"
      "v_smfmac_f32_16x16x64_f16 v[44:47], v[204:207], v[16:23], v242\nds_read_b64_tr_b16 v[56:57], v236 offset:1856\nds_read_b64_tr_b16 v[58:59], v237 offset:1984\n"
      "v_smfmac_f32_16x16x64_f16 v[44:47], v[232:235], v[24:31], v242\nv_pk_mul_f16 v16, v68, s48 op_sel:[0,1]\nv_pk_mul_f16 v17, v69, s48 op_sel:[0,1]\n"
      "v_mfma_f32_16x16x32_f16 v[32:35], v[208:211], v[24:27], v[32:35]\nv_pk_mul_f16 v18, v70, s48 op_sel:[0,1]\nv_pk_mul_f16 v19, v71, s48 op_sel:[0,1]\n"
      "v_mfma_f32_16x16x32_f16 v[32:35], v[220:223], v[28:31], v[32:35]\nv_pk_mul_f16 v20, v68, v70\nv_pk_mul_f16 v21, v69, v71\nv_mfma_f32_16x16x32_f16 v[36:39], v[224:227], v[28:31], v[36:39]\n"
      "v_add_f32 v44, v44, v45\nv_add_f32 v46, v46, v47\nv_mfma_f32_16x16x32_f16 v[36:39], v[212:215], v[24:27], v[36:39]\nv_mul_f32 v55, v44, v54\nv_mul_f32 v55, v55, v46\n"
      "ds_read_b128 v[44:47], v248 offset:48\nv_mfma_f32_16x16x32_f16 v[40:43], v[216:219], v[24:27], v[40:43]\ns_waitcnt lgkmcnt(1)\nv_pk_mul_f16 v22, v56, v56 op_sel_hi:[0,1]\n"
      "v_pk_mul_f16 v23, v56, v57 op_sel_hi:[0,1]\nv_mfma_f32_16x16x32_f16 v[40:43], v[228:231], v[28:31], v[40:43]\nv_mul_f32 v32, v32, v48\nv_mul_f32 v33, v33, v49\nv_mul_f32 v34, v34, v50\n"
      ".Lc_u1:\nv_mfma_f32_16x16x32_f16 v[244:247], v[76:79], v[16:19], 0\nv_fmac_f32 v55, v32, v36\nv_mul_f32 v35, v35, v51\nv_fmac_f32 v55, v33, v37\n"
      "v_mfma_f32_16x16x32_f16 v[244:247], v[88:91], v[20:23], v[244:247]\nv_fmac_f32 v55, v34, v38\nv_fmac_f32 v55, v35, v39\ns_add_u32 s45, s45, 2\ns_cmp_ge_u32 s45, s46\ns_cbranch_scc0 .Lc_b1\n"
      "s_swappc_b64 s[4:5], s[34:35]\n"
      ".Lc_b1:\nv_mfma_f32_16x16x32_f16 v[36:39], v[84:87], v[20:23], 0\nv_pk_mul_f16 v24, v66, v66 op_sel_hi:[0,1]\nv_pk_mul_f16 v25, v66, v57 op_sel_hi:[0,1]\n"
      "v_mfma_f32_16x16x32_f16 v[36:39], v[72:75], v[16:19], v[36:39]\nv_pk_mul_f16 v26, v56, v66 op_sel_hi:[0,1]\nv_pk_mul_f16 v27, v56, v67 op_sel_hi:[0,1]\ns_setprio 0\n"
      "v_mfma_f32_16x16x32_f16 v[32:35], v[80:83], v[20:23], 0\nv_pk_mul_f16 v28, v58, v66 op_sel_hi:[0,1]\nv_pk_mul_f16 v29, v58, v67 op_sel_hi:[0,1]\ns_waitcnt lgkmcnt(0)\n"
      "v_smfmac_f32_16x16x64_f16 v[44:47], v[92:95], v[16:23], v242\nv_pk_mul_f16 v30, v60, v66 op_sel_hi:[0,1]\nv_pk_mul_f16 v31, v60, v67 op_sel_hi:[0,1]\n"
      "v_mfma_f32_16x16x32_f16 v[32:35], v[96:99], v[24:27], v[32:35]\nv_pk_mul_f16 v16, v62, v66 op_sel_hi:[0,1]\nv_pk_mul_f16 v17, v62, v67 op_sel_hi:[0,1]\ns_setprio 1\n"
      "v_mfma_f32_16x16x32_f16 v[32:35], v[108:111], v[28:31], v[32:35]\nv_pk_mul_f16 v18, v64, v66 op_sel_hi:[0,1]\nv_mul_f32 v40, v40, v52\n"
      "v_mfma_f32_16x16x32_f16 v[36:39], v[112:115], v[28:31], v[36:39]\nv_pk_mul_f16 v19, v64, v67 op_sel_hi:[0,1]\nv_mul_f32 v41, v41, v53\n"
      "v_mfma_f32_16x16x32_f16 v[36:39], v[100:103], v[24:27], v[36:39]\nv_pk_mul_f16 v20, v64, v64 op_sel_hi:[0,1]\nv_fmac_f32 v55, v40, v42\n"
      "v_mfma_f32_16x16x32_f16 v[244:247], v[104:107], v[24:27], v[244:247]\nv_pk_mul_f16 v21, v64, v57 op_sel_hi:[0,1]\nv_fmac_f32 v55, v41, v43\n"
      "v_mfma_f32_16x16x32_f16 v[244:247], v[116:119], v[28:31], v[244:247]\nv_pk_mul_f16 v22, v56, v64 op_sel_hi:[0,1]\nv_smfmac_f32_16x16x64_f16 v[44:47], v[120:123], v[24:31], v242\n"
      "v_pk_mul_f16 v23, v56, v65 op_sel_hi:[0,1]\nv_mfma_f32_16x16x32_f16 v[32:35], v[124:127], v[16:19], v[32:35]\nv_pk_mul_f16 v24, v58, v64 op_sel_hi:[0,1]\n"
      "v_pk_mul_f16 v25, v58, v65 op_sel_hi:[0,1]\ns_setprio 2\nds_read_b64_tr_b16 v[70:71], v239 offset:3712\nv_mfma_f32_16x16x32_f16 v[32:35], v[136:139], v[20:23], v[32:35]\n"
      "v_pk_mul_f16 v26, v60, v64 op_sel_hi:[0,1]\nds_read_b64_tr_b16 v[68:69], v238 offset:3712\nv_mfma_f32_16x16x32_f16 v[36:39], v[140:143], v[20:23], v[36:39]\n"
      "v_pk_mul_f16 v27, v60, v65 op_sel_hi:[0,1]\nds_read_b64_tr_b16 v[66:67], v237 offset:4352\nv_mfma_f32_16x16x32_f16 v[36:39], v[128:131], v[16:19], v[36:39]\n"
      "v_pk_mul_f16 v28, v62, v64 op_sel_hi:[0,1]\nv_add_f32_dpp v55, v55, v55 quad_perm:[1,0,3,2] row_mask:0xf bank_mask:0xf\nv_mfma_f32_16x16x32_f16 v[244:247], v[132:135], v[16:19], v[244:247]\n"
      "v_pk_mul_f16 v29, v62, v65 op_sel_hi:[0,1]\nv_add_f32_dpp v55, v55, v55 quad_perm:[2,3,0,1] row_mask:0xf bank_mask:0xf\nv_mfma_f32_16x16x32_f16 v[244:247], v[144:147], v[20:23], v[244:247]\n"
      "v_pk_mul_f16 v30, v62, v62 op_sel_hi:[0,1]\nv_add_f32_dpp v55, v55, v55 row_half_mirror row_mask:0xf bank_mask:0xf\nv_smfmac_f32_16x16x64_f16 v[44:47], v[148:151], v[16:23], v242\n"
      "v_pk_mul_f16 v31, v62, v57 op_sel_hi:[0,1]\nv_add_f32_dpp v55, v55, v55 row_mirror row_mask:0xf bank_mask:0xf\nv_mfma_f32_16x16x32_f16 v[32:35], v[152:155], v[24:27], v[32:35]\n"
      "v_pk_mul_f16 v16, v56, v62 op_sel_hi:[0,1]\nv_pk_mul_f16 v17, v56, v63 op_sel_hi:[0,1]\nds_write_b32 v243, v55 offset:0\nv_mfma_f32_16x16x32_f16 v[32:35], v[164:167], v[28:31], v[32:35]\n"
      "v_pk_mul_f16 v18, v58, v62 op_sel_hi:[0,1]\nds_read_b64_tr_b16 v[64:65], v237 offset:4224\nv_mfma_f32_16x16x32_f16 v[36:39], v[168:171], v[28:31], v[36:39]\n"
      "v_pk_mul_f16 v19, v58, v63 op_sel_hi:[0,1]\nv_mfma_f32_16x16x32_f16 v[36:39], v[156:159], v[24:27], v[36:39]\nv_pk_mul_f16 v20, v60, v62 op_sel_hi:[0,1]\ns_setprio 3\n"
      "v_mfma_f32_16x16x32_f16 v[244:247], v[160:163], v[24:27], v[244:247]\nv_pk_mul_f16 v21, v60, v63 op_sel_hi:[0,1]\ns_waitcnt lgkmcnt(1)\nv_readlane_b32 s48, v69, 48\n"
      "v_mfma_f32_16x16x32_f16 v[244:247], v[172:175], v[28:31], v[244:247]\nv_pk_mul_f16 v22, v60, v60 op_sel_hi:[0,1]\nv_smfmac_f32_16x16x64_f16 v[44:47], v[176:179], v[24:31], v242\n"
      "v_pk_mul_f16 v23, v60, v57 op_sel_hi:[0,1]\nv_mfma_f32_16x16x32_f16 v[32:35], v[180:183], v[16:19], v[32:35]\nv_pk_mul_f16 v24, v56, v60 op_sel_hi:[0,1]\n"
      "v_pk_mul_f16 v25, v56, v61 op_sel_hi:[0,1]\nv_mfma_f32_16x16x32_f16 v[32:35], v[192:195], v[20:23], v[32:35]\nv_pk_mul_f16 v26, v58, v60 op_sel_hi:[0,1]\n"
      "v_pk_mul_f16 v27, v58, v61 op_sel_hi:[0,1]\nv_mfma_f32_16x16x32_f16 v[36:39], v[196:199], v[20:23], v[36:39]\nv_pk_mul_f16 v28, v58, v58 op_sel_hi:[0,1]\n"
      "ds_read_b64_tr_b16 v[62:63], v237 offset:4096\nv_mfma_f32_16x16x32_f16 v[36:39], v[184:187], v[16:19], v[36:39]\nv_pk_mul_f16 v29, v58, v57 op_sel_hi:[0,1]\n"
      "ds_read_b64_tr_b16 v[60:61], v237 offset:3968\nv_mfma_f32_16x16x32_f16 v[244:247], v[188:191], v[16:19], v[244:247]\nv_pk_mul_f16 v30, v56, v58 op_sel_hi:[0,1]\n"
      "v_mfma_f32_16x16x32_f16 v[244:247], v[200:203], v[20:23], v[244:247]\nv_pk_mul_f16 v31, v56, v59 op_sel_hi:[0,1]\nv_smfmac_f32_16x16x64_f16 v[44:47], v[204:207], v[16:23], v242\n"
      "ds_read_b64_tr_b16 v[56:57], v236 offset:3712\nds_read_b64_tr_b16 v[58:59], v237 offset:3840\nv_smfmac_f32_16x16x64_f16 v[44:47], v[232:235], v[24:31], v242\n"
      "v_pk_mul_f16 v16, v68, s48 op_sel:[0,1]\nv_pk_mul_f16 v17, v69, s48 op_sel:[0,1]\nv_mfma_f32_16x16x32_f16 v[32:35], v[208:211], v[24:27], v[32:35]\nv_pk_mul_f16 v18, v70, s48 op_sel:[0,1]\n"
      "v_pk_mul_f16 v19, v71, s48 op_sel:[0,1]\nv_mfma_f32_16x16x32_f16 v[32:35], v[220:223], v[28:31], v[32:35]\nv_pk_mul_f16 v20, v68, v70\nv_pk_mul_f16 v21, v69, v71\n"
      "v_mfma_f32_16x16x32_f16 v[36:39], v[224:227], v[28:31], v[36:39]\nv_add_f32 v44, v44, v45\nv_add_f32 v46, v46, v47\nv_mfma_f32_16x16x32_f16 v[36:39], v[212:215], v[24:27], v[36:39]\n"
      "v_mul_f32 v55, v44, v54\nv_mul_f32 v55, v55, v46\nds_read_b128 v[44:47], v248 offset:48\nv_mfma_f32_16x16x32_f16 v[244:247], v[216:219], v[24:27], v[244:247]\ns_waitcnt lgkmcnt(1)\n"
      "v_pk_mul_f16 v22, v56, v56 op_sel_hi:[0,1]\nv_pk_mul_f16 v23, v56, v57 op_sel_hi:[0,1]\nv_mfma_f32_16x16x32_f16 v[244:247], v[228:231], v[28:31], v[244:247]\nv_mul_f32 v32, v32, v48\n"
      "v_mul_f32 v33, v33, v49\nv_mul_f32 v34, v34, v50\nv_mfma_f32_16x16x32_f16 v[40:43], v[76:79], v[16:19], 0\nds_read_b32 v4, v248\nv_fmac_f32 v55, v32, v36\nv_mul_f32 v35, v35, v51\n"
      "v_fmac_f32 v55, v33, v37\nv_mfma_f32_16x16x32_f16 v[40:43], v[88:91], v[20:23], v[40:43]\nv_fmac_f32 v55, v34, v38\nv_fmac_f32 v55, v35, v39\ns_add_u32 s45, s45, 2\ns_cmp_ge_u32 s45, s46\n"
      "s_cbranch_scc0 .Lc_b2\ns_swappc_b64 s[4:5], s[34:35]\n"
      ".Lc_b2:\nv_mfma_f32_16x16x32_f16 v[36:39], v[84:87], v[20:23], 0\nv_pk_mul_f16 v24, v66, v66 op_sel_hi:[0,1]\nv_pk_mul_f16 v25, v66, v57 op_sel_hi:[0,1]\n"
      "v_mfma_f32_16x16x32_f16 v[36:39], v[72:75], v[16:19], v[36:39]\nv_pk_mul_f16 v26, v56, v66 op_sel_hi:[0,1]\nv_pk_mul_f16 v27, v56, v67 op_sel_hi:[0,1]\ns_setprio 0\n"
      "v_mfma_f32_16x16x32_f16 v[32:35], v[80:83], v[20:23], 0\nv_pk_mul_f16 v28, v58, v66 op_sel_hi:[0,1]\nv_pk_mul_f16 v29, v58, v67 op_sel_hi:[0,1]\ns_waitcnt lgkmcnt(0)\n"
      "v_smfmac_f32_16x16x64_f16 v[44:47], v[92:95], v[16:23], v242\nv_pk_mul_f16 v30, v60, v66 op_sel_hi:[0,1]\nv_pk_mul_f16 v31, v60, v67 op_sel_hi:[0,1]\n"
      "v_mfma_f32_16x16x32_f16 v[32:35], v[96:99], v[24:27], v[32:35]\nv_pk_mul_f16 v16, v62, v66 op_sel_hi:[0,1]\nv_pk_mul_f16 v17, v62, v67 op_sel_hi:[0,1]\ns_setprio 1\n"
      "v_mfma_f32_16x16x32_f16 v[32:35], v[108:111], v[28:31], v[32:35]\nv_pk_mul_f16 v18, v64, v66 op_sel_hi:[0,1]\nv_mul_f32 v244, v244, v52\n"
      "v_mfma_f32_16x16x32_f16 v[36:39], v[112:115], v[28:31], v[36:39]\nv_pk_mul_f16 v19, v64, v67 op_sel_hi:[0,1]\nv_mul_f32 v245, v245, v53\n"
      "v_mfma_f32_16x16x32_f16 v[36:39], v[100:103], v[24:27], v[36:39]\nv_pk_mul_f16 v20, v64, v64 op_sel_hi:[0,1]\nv_fmac_f32 v55, v244, v246\n"
      "v_mfma_f32_16x16x32_f16 v[40:43], v[104:107], v[24:27], v[40:43]\nv_pk_mul_f16 v21, v64, v57 op_sel_hi:[0,1]\nv_fmac_f32 v55, v245, v247\n"
      "v_mfma_f32_16x16x32_f16 v[40:43], v[116:119], v[28:31], v[40:43]\nv_pk_mul_f16 v22, v56, v64 op_sel_hi:[0,1]\nv_readfirstlane_b32 s44, v4\n"
      "v_smfmac_f32_16x16x64_f16 v[44:47], v[120:123], v[24:31], v242\nv_pk_mul_f16 v23, v56, v65 op_sel_hi:[0,1]\nv_mfma_f32_16x16x32_f16 v[32:35], v[124:127], v[16:19], v[32:35]\n"
      "v_pk_mul_f16 v24, v58, v64 op_sel_hi:[0,1]\nv_pk_mul_f16 v25, v58, v65 op_sel_hi:[0,1]\ns_setprio 2\nds_read_b64_tr_b16 v[70:71], v239 offset:5568\n"
      "v_mfma_f32_16x16x32_f16 v[32:35], v[136:139], v[20:23], v[32:35]\nv_pk_mul_f16 v26, v60, v64 op_sel_hi:[0,1]\nds_read_b64_tr_b16 v[68:69], v238 offset:5568\n"
      "v_mfma_f32_16x16x32_f16 v[36:39], v[140:143], v[20:23], v[36:39]\nv_pk_mul_f16 v27, v60, v65 op_sel_hi:[0,1]\nds_read_b64_tr_b16 v[66:67], v237 offset:6208\n"
      "v_mfma_f32_16x16x32_f16 v[36:39], v[128:131], v[16:19], v[36:39]\nv_pk_mul_f16 v28, v62, v64 op_sel_hi:[0,1]\nv_add_f32_dpp v55, v55, v55 quad_perm:[1,0,3,2] row_mask:0xf bank_mask:0xf\n"
      "v_mfma_f32_16x16x32_f16 v[40:43], v[132:135], v[16:19], v[40:43]\nv_pk_mul_f16 v29, v62, v65 op_sel_hi:[0,1]\nv_add_f32_dpp v55, v55, v55 quad_perm:[2,3,0,1] row_mask:0xf bank_mask:0xf\n"
      "v_mfma_f32_16x16x32_f16 v[40:43], v[144:147], v[20:23], v[40:43]\nv_pk_mul_f16 v30, v62, v62 op_sel_hi:[0,1]\nv_add_f32_dpp v55, v55, v55 row_half_mirror row_mask:0xf bank_mask:0xf\n"
      "v_smfmac_f32_16x16x64_f16 v[44:47], v[148:151], v[16:23], v242\nv_pk_mul_f16 v31, v62, v57 op_sel_hi:[0,1]\nv_add_f32_dpp v55, v55, v55 row_mirror row_mask:0xf bank_mask:0xf\n"
      "v_mfma_f32_16x16x32_f16 v[32:35], v[152:155], v[24:27], v[32:35]\nv_pk_mul_f16 v16, v56, v62 op_sel_hi:[0,1]\nv_pk_mul_f16 v17, v56, v63 op_sel_hi:[0,1]\nds_write_b32 v243, v55 offset:128\n"
      "v_mfma_f32_16x16x32_f16 v[32:35], v[164:167], v[28:31], v[32:35]\nv_pk_mul_f16 v18, v58, v62 op_sel_hi:[0,1]\nds_read_b64_tr_b16 v[64:65], v237 offset:6080\n"
      "v_mfma_f32_16x16x32_f16 v[36:39], v[168:171], v[28:31], v[36:39]\nv_pk_mul_f16 v19, v58, v63 op_sel_hi:[0,1]\nv_mfma_f32_16x16x32_f16 v[36:39], v[156:159], v[24:27], v[36:39]\n"
      "v_pk_mul_f16 v20, v60, v62 op_sel_hi:[0,1]\ns_setprio 3\ns_ff0_i32_b32 s53, s44\nv_mfma_f32_16x16x32_f16 v[40:43], v[160:163], v[24:27], v[40:43]\n"
      "v_pk_mul_f16 v21, v60, v63 op_sel_hi:[0,1]\ns_waitcnt lgkmcnt(1)\nv_readlane_b32 s48, v69, 48\nv_mfma_f32_16x16x32_f16 v[40:43], v[172:175], v[28:31], v[40:43]\n"
      "v_pk_mul_f16 v22, v60, v60 op_sel_hi:[0,1]\ns_add_u32 s53, s53, s64\nv_smfmac_f32_16x16x64_f16 v[44:47], v[176:179], v[24:31], v242\nv_pk_mul_f16 v23, v60, v57 op_sel_hi:[0,1]\n"
      "v_mfma_f32_16x16x32_f16 v[32:35], v[180:183], v[16:19], v[32:35]\nv_pk_mul_f16 v24, v56, v60 op_sel_hi:[0,1]\nv_pk_mul_f16 v25, v56, v61 op_sel_hi:[0,1]\n"
      "v_mfma_f32_16x16x32_f16 v[32:35], v[192:195], v[20:23], v[32:35]\nv_pk_mul_f16 v26, v58, v60 op_sel_hi:[0,1]\nv_pk_mul_f16 v27, v58, v61 op_sel_hi:[0,1]\n"
      "v_mfma_f32_16x16x32_f16 v[36:39], v[196:199], v[20:23], v[36:39]\nv_pk_mul_f16 v28, v58, v58 op_sel_hi:[0,1]\nds_read_b64_tr_b16 v[62:63], v237 offset:5952\n"
      "v_mfma_f32_16x16x32_f16 v[36:39], v[184:187], v[16:19], v[36:39]\nv_pk_mul_f16 v29, v58, v57 op_sel_hi:[0,1]\nds_read_b64_tr_b16 v[60:61], v237 offset:5824\n"
      "v_mfma_f32_16x16x32_f16 v[40:43], v[188:191], v[16:19], v[40:43]\nv_pk_mul_f16 v30, v56, v58 op_sel_hi:[0,1]\nv_mfma_f32_16x16x32_f16 v[40:43], v[200:203], v[20:23], v[40:43]\n"
      "v_pk_mul_f16 v31, v56, v59 op_sel_hi:[0,1]\nv_smfmac_f32_16x16x64_f16 v[44:47], v[204:207], v[16:23], v242\nds_read_b64_tr_b16 v[56:57], v236 offset:5568\n"
      "ds_read_b64_tr_b16 v[58:59], v237 offset:5696\nv_smfmac_f32_16x16x64_f16 v[44:47], v[232:235], v[24:31], v242\nv_pk_mul_f16 v16, v68, s48 op_sel:[0,1]\n"
      "v_pk_mul_f16 v17, v69, s48 op_sel:[0,1]\nv_mfma_f32_16x16x32_f16 v[32:35], v[208:211], v[24:27], v[32:35]\nv_pk_mul_f16 v18, v70, s48 op_sel:[0,1]\nv_pk_mul_f16 v19, v71, s48 op_sel:[0,1]\n"
      "v_mfma_f32_16x16x32_f16 v[32:35], v[220:223], v[28:31], v[32:35]\nv_pk_mul_f16 v20, v68, v70\nv_pk_mul_f16 v21, v69, v71\nv_mfma_f32_16x16x32_f16 v[36:39], v[224:227], v[28:31], v[36:39]\n"
      "v_add_f32 v44, v44, v45\nv_add_f32 v46, v46, v47\nv_mfma_f32_16x16x32_f16 v[36:39], v[212:215], v[24:27], v[36:39]\nv_mul_f32 v55, v44, v54\nv_mul_f32 v55, v55, v46\n"
      "ds_read_b128 v[44:47], v248 offset:48\nv_mfma_f32_16x16x32_f16 v[40:43], v[216:219], v[24:27], v[40:43]\ns_waitcnt lgkmcnt(1)\nv_pk_mul_f16 v22, v56, v56 op_sel_hi:[0,1]\n"
      "v_pk_mul_f16 v23, v56, v57 op_sel_hi:[0,1]\nv_mfma_f32_16x16x32_f16 v[40:43], v[228:231], v[28:31], v[40:43]\ns_cmp_lt_u32 s45, s53\nv_mul_f32 v32, v32, v48\nv_mul_f32 v33, v33, v49\n"
      "v_mul_f32 v34, v34, v50\ns_cbranch_scc0 .Lc_s2\n"
      ".Lc_u3:\nv_mfma_f32_16x16x32_f16 v[244:247], v[76:79], v[16:19], 0\nds_read_b32 v4, v248\nv_fmac_f32 v55, v32, v36\nv_mul_f32 v35, v35, v51\nv_fmac_f32 v55, v33, v37\n"
      "v_mfma_f32_16x16x32_f16 v[244:247], v[88:91], v[20:23], v[244:247]\nv_fmac_f32 v55, v34, v38\nv_fmac_f32 v55, v35, v39\ns_add_u32 s45, s45, 2\ns_cmp_ge_u32 s45, s46\ns_cbranch_scc0 .Lc_b3\n"
      "s_swappc_b64 s[4:5], s[34:35]\n"
      ".Lc_b3:\nv_mfma_f32_16x16x32_f16 v[36:39], v[84:87], v[20:23], 0\nv_pk_mul_f16 v24, v66, v66 op_sel_hi:[0,1]\nv_pk_mul_f16 v25, v66, v57 op_sel_hi:[0,1]\n"
      "v_mfma_f32_16x16x32_f16 v[36:39], v[72:75], v[16:19], v[36:39]\nv_pk_mul_f16 v26, v56, v66 op_sel_hi:[0,1]\nv_pk_mul_f16 v27, v56, v67 op_sel_hi:[0,1]\ns_setprio 0\n"
      "v_mfma_f32_16x16x32_f16 v[32:35], v[80:83], v[20:23], 0\nv_pk_mul_f16 v28, v58, v66 op_sel_hi:[0,1]\nv_pk_mul_f16 v29, v58, v67 op_sel_hi:[0,1]\ns_waitcnt lgkmcnt(0)\n"
      "v_smfmac_f32_16x16x64_f16 v[44:47], v[92:95], v[16:23], v242\nv_pk_mul_f16 v30, v60, v66 op_sel_hi:[0,1]\nv_pk_mul_f16 v31, v60, v67 op_sel_hi:[0,1]\n"
      "v_mfma_f32_16x16x32_f16 v[32:35], v[96:99], v[24:27], v[32:35]\nv_pk_mul_f16 v16, v62, v66 op_sel_hi:[0,1]\nv_pk_mul_f16 v17, v62, v67 op_sel_hi:[0,1]\ns_setprio 1\n"
      "v_mfma_f32_16x16x32_f16 v[32:35], v[108:111], v[28:31], v[32:35]\nv_pk_mul_f16 v18, v64, v66 op_sel_hi:[0,1]\nv_mul_f32 v40, v40, v52\n"
      "v_mfma_f32_16x16x32_f16 v[36:39], v[112:115], v[28:31], v[36:39]\nv_pk_mul_f16 v19, v64, v67 op_sel_hi:[0,1]\nv_mul_f32 v41, v41, v53\n"
      "v_mfma_f32_16x16x32_f16 v[36:39], v[100:103], v[24:27], v[36:39]\nv_pk_mul_f16 v20, v64, v64 op_sel_hi:[0,1]\nv_fmac_f32 v55, v40, v42\n"
      "v_mfma_f32_16x16x32_f16 v[244:247], v[104:107], v[24:27], v[244:247]\nv_pk_mul_f16 v21, v64, v57 op_sel_hi:[0,1]\nv_fmac_f32 v55, v41, v43\n"
      "v_mfma_f32_16x16x32_f16 v[244:247], v[116:119], v[28:31], v[244:247]\nv_pk_mul_f16 v22, v56, v64 op_sel_hi:[0,1]\nv_readfirstlane_b32 s44, v4\n"
      "v_smfmac_f32_16x16x64_f16 v[44:47], v[120:123], v[24:31], v242\nv_pk_mul_f16 v23, v56, v65 op_sel_hi:[0,1]\nv_mfma_f32_16x16x32_f16 v[32:35], v[124:127], v[16:19], v[32:35]\n"
      "v_pk_mul_f16 v24, v58, v64 op_sel_hi:[0,1]\nv_pk_mul_f16 v25, v58, v65 op_sel_hi:[0,1]\ns_setprio 2\nds_read_b64_tr_b16 v[70:71], v239 offset:7424\n"
      "v_mfma_f32_16x16x32_f16 v[32:35], v[136:139], v[20:23], v[32:35]\nv_pk_mul_f16 v26, v60, v64 op_sel_hi:[0,1]\nds_read_b64_tr_b16 v[68:69], v238 offset:7424\n"
      "v_mfma_f32_16x16x32_f16 v[36:39], v[140:143], v[20:23], v[36:39]\nv_pk_mul_f16 v27, v60, v65 op_sel_hi:[0,1]\nds_read_b64_tr_b16 v[66:67], v237 offset:8064\n"
      "v_mfma_f32_16x16x32_f16 v[36:39], v[128:131], v[16:19], v[36:39]\nv_pk_mul_f16 v28, v62, v64 op_sel_hi:[0,1]\nv_add_f32_dpp v55, v55, v55 quad_perm:[1,0,3,2] row_mask:0xf bank_mask:0xf\n"
      "v_mfma_f32_16x16x32_f16 v[244:247], v[132:135], v[16:19], v[244:247]\nv_pk_mul_f16 v29, v62, v65 op_sel_hi:[0,1]\nv_add_f32_dpp v55, v55, v55 quad_perm:[2,3,0,1] row_mask:0xf bank_mask:0xf\n"
      "v_mfma_f32_16x16x32_f16 v[244:247], v[144:147], v[20:23], v[244:247]\nv_pk_mul_f16 v30, v62, v62 op_sel_hi:[0,1]\nv_add_f32_dpp v55, v55, v55 row_half_mirror row_mask:0xf bank_mask:0xf\n"
      "v_smfmac_f32_16x16x64_f16 v[44:47], v[148:151], v[16:23], v242\nv_pk_mul_f16 v31, v62, v57 op_sel_hi:[0,1]\nv_add_f32_dpp v55, v55, v55 row_mirror row_mask:0xf bank_mask:0xf\n"
      "v_mfma_f32_16x16x32_f16 v[32:35], v[152:155], v[24:27], v[32:35]\nv_pk_mul_f16 v16, v56, v62 op_sel_hi:[0,1]\nv_pk_mul_f16 v17, v56, v63 op_sel_hi:[0,1]\nds_write_b32 v243, v55 offset:256\n"
      "v_mfma_f32_16x16x32_f16 v[32:35], v[164:167], v[28:31], v[32:35]\nv_pk_mul_f16 v18, v58, v62 op_sel_hi:[0,1]\nds_read_b64_tr_b16 v[64:65], v237 offset:7936\n"
      "v_mfma_f32_16x16x32_f16 v[36:39], v[168:171], v[28:31], v[36:39]\nv_pk_mul_f16 v19, v58, v63 op_sel_hi:[0,1]\nv_mfma_f32_16x16x32_f16 v[36:39], v[156:159], v[24:27], v[36:39]\n"
      "v_pk_mul_f16 v20, v60, v62 op_sel_hi:[0,1]\ns_setprio 3\ns_ff0_i32_b32 s53, s44\nv_mfma_f32_16x16x32_f16 v[244:247], v[160:163], v[24:27], v[244:247]\n"
      "v_pk_mul_f16 v21, v60, v63 op_sel_hi:[0,1]\ns_waitcnt lgkmcnt(1)\nv_readlane_b32 s48, v69, 48\nv_mfma_f32_16x16x32_f16 v[244:247], v[172:175], v[28:31], v[244:247]\n"
      "v_pk_mul_f16 v22, v60, v60 op_sel_hi:[0,1]\ns_add_u32 s53, s53, s64\nv_smfmac_f32_16x16x64_f16 v[44:47], v[176:179], v[24:31], v242\nv_pk_mul_f16 v23, v60, v57 op_sel_hi:[0,1]\n"
      "v_mfma_f32_16x16x32_f16 v[32:35], v[180:183], v[16:19], v[32:35]\nv_pk_mul_f16 v24, v56, v60 op_sel_hi:[0,1]\nv_pk_mul_f16 v25, v56, v61 op_sel_hi:[0,1]\n"
      "v_mfma_f32_16x16x32_f16 v[32:35], v[192:195], v[20:23], v[32:35]\nv_pk_mul_f16 v26, v58, v60 op_sel_hi:[0,1]\nv_pk_mul_f16 v27, v58, v61 op_sel_hi:[0,1]\n"
      "v_mfma_f32_16x16x32_f16 v[36:39], v[196:199], v[20:23], v[36:39]\nv_pk_mul_f16 v28, v58, v58 op_sel_hi:[0,1]\nds_read_b64_tr_b16 v[62:63], v237 offset:7808\n"
      "v_mfma_f32_16x16x32_f16 v[36:39], v[184:187], v[16:19], v[36:39]\nv_pk_mul_f16 v29, v58, v57 op_sel_hi:[0,1]\nds_read_b64_tr_b16 v[60:61], v237 offset:7680\n"
      "v_mfma_f32_16x16x32_f16 v[244:247], v[188:191], v[16:19], v[244:247]\nv_pk_mul_f16 v30, v56, v58 op_sel_hi:[0,1]\nv_mfma_f32_16x16x32_f16 v[244:247], v[200:203], v[20:23], v[244:247]\n"
      "v_pk_mul_f16 v31, v56, v59 op_sel_hi:[0,1]\nv_smfmac_f32_16x16x64_f16 v[44:47], v[204:207], v[16:23], v242\nds_read_b64_tr_b16 v[56:57], v236 offset:7424\n"
      "ds_read_b64_tr_b16 v[58:59], v237 offset:7552\nv_smfmac_f32_16x16x64_f16 v[44:47], v[232:235], v[24:31], v242\nv_pk_mul_f16 v16, v68, s48 op_sel:[0,1]\n"
      "v_pk_mul_f16 v17, v69, s48 op_sel:[0,1]\nv_mfma_f32_16x16x32_f16 v[32:35], v[208:211], v[24:27], v[32:35]\nv_pk_mul_f16 v18, v70, s48 op_sel:[0,1]\nv_pk_mul_f16 v19, v71, s48 op_sel:[0,1]\n"
      "v_mfma_f32_16x16x32_f16 v[32:35], v[220:223], v[28:31], v[32:35]\nv_pk_mul_f16 v20, v68, v70\nv_pk_mul_f16 v21, v69, v71\nv_mfma_f32_16x16x32_f16 v[36:39], v[224:227], v[28:31], v[36:39]\n"
      "v_add_f32 v44, v44, v45\nv_add_f32 v46, v46, v47\nv_mfma_f32_16x16x32_f16 v[36:39], v[212:215], v[24:27], v[36:39]\nv_mul_f32 v55, v44, v54\nv_mul_f32 v55, v55, v46\n"
      "ds_read_b128 v[44:47], v248 offset:48\nv_mfma_f32_16x16x32_f16 v[244:247], v[216:219], v[24:27], v[244:247]\ns_waitcnt lgkmcnt(1)\nv_pk_mul_f16 v22, v56, v56 op_sel_hi:[0,1]\n"
      "v_pk_mul_f16 v23, v56, v57 op_sel_hi:[0,1]\nv_mfma_f32_16x16x32_f16 v[244:247], v[228:231], v[28:31], v[244:247]\ns_cmp_lt_u32 s45, s53\nv_mul_f32 v32, v32, v48\nv_mul_f32 v33, v33, v49\n"
      "v_mul_f32 v34, v34, v50\ns_cbranch_scc0 .Lc_s3\n"
      ".Lc_u4:\nv_mfma_f32_16x16x32_f16 v[40:43], v[76:79], v[16:19], 0\nds_read_b32 v4, v248\nv_fmac_f32 v55, v32, v36\nv_mul_f32 v35, v35, v51\nv_fmac_f32 v55, v33, v37\n"
      "v_mfma_f32_16x16x32_f16 v[40:43], v[88:91], v[20:23], v[40:43]\nv_fmac_f32 v55, v34, v38\nv_fmac_f32 v55, v35, v39\ns_add_u32 s45, s45, 2\ns_cmp_ge_u32 s45, s46\ns_cbranch_scc0 .Lc_b4\n"
      "s_swappc_b64 s[4:5], s[34:35]\n"
      ".Lc_b4:\nv_mfma_f32_16x16x32_f16 v[36:39], v[84:87], v[20:23], 0\nv_pk_mul_f16 v24, v66, v66 op_sel_hi:[0,1]\nv_pk_mul_f16 v25, v66, v57 op_sel_hi:[0,1]\n"
      "v_mfma_f32_16x16x32_f16 v[36:39], v[72:75], v[16:19], v[36:39]\nv_pk_mul_f16 v26, v56, v66 op_sel_hi:[0,1]\nv_pk_mul_f16 v27, v56, v67 op_sel_hi:[0,1]\ns_setprio 0\n"
      "v_mfma_f32_16x16x32_f16 v[32:35], v[80:83], v[20:23], 0\nv_pk_mul_f16 v28, v58, v66 op_sel_hi:[0,1]\nv_pk_mul_f16 v29, v58, v67 op_sel_hi:[0,1]\ns_waitcnt lgkmcnt(0)\n"
      "v_smfmac_f32_16x16x64_f16 v[44:47], v[92:95], v[16:23], v242\nv_pk_mul_f16 v30, v60, v66 op_sel_hi:[0,1]\nv_pk_mul_f16 v31, v60, v67 op_sel_hi:[0,1]\n"
      "v_mfma_f32_16x16x32_f16 v[32:35], v[96:99], v[24:27], v[32:35]\nv_pk_mul_f16 v16, v62, v66 op_sel_hi:[0,1]\nv_pk_mul_f16 v17, v62, v67 op_sel_hi:[0,1]\ns_setprio 1\n"
      "v_mfma_f32_16x16x32_f16 v[32:35], v[108:111], v[28:31], v[32:35]\nv_pk_mul_f16 v18, v64, v66 op_sel_hi:[0,1]\nv_mul_f32 v244, v244, v52\n"
      "v_mfma_f32_16x16x32_f16 v[36:39], v[112:115], v[28:31], v[36:39]\nv_pk_mul_f16 v19, v64, v67 op_sel_hi:[0,1]\nv_mul_f32 v245, v245, v53\n"
      "v_mfma_f32_16x16x32_f16 v[36:39], v[100:103], v[24:27], v[36:39]\nv_pk_mul_f16 v20, v64, v64 op_sel_hi:[0,1]\nv_fmac_f32 v55, v244, v246\n"
      "v_mfma_f32_16x16x32_f16 v[40:43], v[104:107], v[24:27], v[40:43]\nv_pk_mul_f16 v21, v64, v57 op_sel_hi:[0,1]\nv_fmac_f32 v55, v245, v247\n"
      "v_mfma_f32_16x16x32_f16 v[40:43], v[116:119], v[28:31], v[40:43]\nv_pk_mul_f16 v22, v56, v64 op_sel_hi:[0,1]\nv_readfirstlane_b32 s44, v4\n"
      "v_smfmac_f32_16x16x64_f16 v[44:47], v[120:123], v[24:31], v242\nv_pk_mul_f16 v23, v56, v65 op_sel_hi:[0,1]\nv_mfma_f32_16x16x32_f16 v[32:35], v[124:127], v[16:19], v[32:35]\n"
      "v_pk_mul_f16 v24, v58, v64 op_sel_hi:[0,1]\nv_pk_mul_f16 v25, v58, v65 op_sel_hi:[0,1]\ns_setprio 2\nds_read_b64_tr_b16 v[70:71], v239 offset:9280\n"
      "v_mfma_f32_16x16x32_f16 v[32:35], v[136:139], v[20:23], v[32:35]\nv_pk_mul_f16 v26, v60, v64 op_sel_hi:[0,1]\nds_read_b64_tr_b16 v[68:69], v238 offset:9280\n"
      "v_mfma_f32_16x16x32_f16 v[36:39], v[140:143], v[20:23], v[36:39]\nv_pk_mul_f16 v27, v60, v65 op_sel_hi:[0,1]\nds_read_b64_tr_b16 v[66:67], v237 offset:9920\n"
      "v_mfma_f32_16x16x32_f16 v[36:39], v[128:131], v[16:19], v[36:39]\nv_pk_mul_f16 v28, v62, v64 op_sel_hi:[0,1]\nv_add_f32_dpp v55, v55, v55 quad_perm:[1,0,3,2] row_mask:0xf bank_mask:0xf\n"
      "v_mfma_f32_16x16x32_f16 v[40:43], v[132:135], v[16:19], v[40:43]\nv_pk_mul_f16 v29, v62, v65 op_sel_hi:[0,1]\nv_add_f32_dpp v55, v55, v55 quad_perm:[2,3,0,1] row_mask:0xf bank_mask:0xf\n"
      "v_mfma_f32_16x16x32_f16 v[40:43], v[144:147], v[20:23], v[40:43]\nv_pk_mul_f16 v30, v62, v62 op_sel_hi:[0,1]\nv_add_f32_dpp v55, v55, v55 row_half_mirror row_mask:0xf bank_mask:0xf\n"
      "v_smfmac_f32_16x16x64_f16 v[44:47], v[148:151], v[16:23], v242\nv_pk_mul_f16 v31, v62, v57 op_sel_hi:[0,1]\nv_add_f32_dpp v55, v55, v55 row_mirror row_mask:0xf bank_mask:0xf\n"
      "v_mfma_f32_16x16x32_f16 v[32:35], v[152:155], v[24:27], v[32:35]\nv_pk_mul_f16 v16, v56, v62 op_sel_hi:[0,1]\nv_pk_mul_f16 v17, v56, v63 op_sel_hi:[0,1]\nds_write_b32 v243, v55 offset:384\n"
      "v_mfma_f32_16x16x32_f16 v[32:35], v[164:167], v[28:31], v[32:35]\nv_pk_mul_f16 v18, v58, v62 op_sel_hi:[0,1]\nds_read_b64_tr_b16 v[64:65], v237 offset:9792\n"
      "v_mfma_f32_16x16x32_f16 v[36:39], v[168:171], v[28:31], v[36:39]\nv_pk_mul_f16 v19, v58, v63 op_sel_hi:[0,1]\nv_mfma_f32_16x16x32_f16 v[36:39], v[156:159], v[24:27], v[36:39]\n"
      "v_pk_mul_f16 v20, v60, v62 op_sel_hi:[0,1]\ns_setprio 3\ns_ff0_i32_b32 s53, s44\nv_mfma_f32_16x16x32_f16 v[40:43], v[160:163], v[24:27], v[40:43]\n"
      "v_pk_mul_f16 v21, v60, v63 op_sel_hi:[0,1]\ns_waitcnt lgkmcnt(1)\nv_readlane_b32 s48, v69, 48\nv_mfma_f32_16x16x32_f16 v[40:43], v[172:175], v[28:31], v[40:43]\n"
      "v_pk_mul_f16 v22, v60, v60 op_sel_hi:[0,1]\ns_add_u32 s53, s53, s64\nv_smfmac_f32_16x16x64_f16 v[44:47], v[176:179], v[24:31], v242\nv_pk_mul_f16 v23, v60, v57 op_sel_hi:[0,1]\n"
      "v_mfma_f32_16x16x32_f16 v[32:35], v[180:183], v[16:19], v[32:35]\nv_pk_mul_f16 v24, v56, v60 op_sel_hi:[0,1]\nv_pk_mul_f16 v25, v56, v61 op_sel_hi:[0,1]\n"
      "v_mfma_f32_16x16x32_f16 v[32:35], v[192:195], v[20:23], v[32:35]\nv_pk_mul_f16 v26, v58, v60 op_sel_hi:[0,1]\nv_pk_mul_f16 v27, v58, v61 op_sel_hi:[0,1]\n"
      "v_mfma_f32_16x16x32_f16 v[36:39], v[196:199], v[20:23], v[36:39]\nv_pk_mul_f16 v28, v58, v58 op_sel_hi:[0,1]\nds_read_b64_tr_b16 v[62:63], v237 offset:9664\n"
      "v_mfma_f32_16x16x32_f16 v[36:39], v[184:187], v[16:19], v[36:39]\nv_pk_mul_f16 v29, v58, v57 op_sel_hi:[0,1]\nds_read_b64_tr_b16 v[60:61], v237 offset:9536\n"
      "v_mfma_f32_16x16x32_f16 v[40:43], v[188:191], v[16:19], v[40:43]\nv_pk_mul_f16 v30, v56, v58 op_sel_hi:[0,1]\nv_mfma_f32_16x16x32_f16 v[40:43], v[200:203], v[20:23], v[40:43]\n"
      "v_pk_mul_f16 v31, v56, v59 op_sel_hi:[0,1]\nv_smfmac_f32_16x16x64_f16 v[44:47], v[204:207], v[16:23], v242\nds_read_b64_tr_b16 v[56:57], v236 offset:9280\n"
      "ds_read_b64_tr_b16 v[58:59], v237 offset:9408\nv_smfmac_f32_16x16x64_f16 v[44:47], v[232:235], v[24:31], v242\nv_pk_mul_f16 v16, v68, s48 op_sel:[0,1]\n"
      "v_pk_mul_f16 v17, v69, s48 op_sel:[0,1]\nv_mfma_f32_16x16x32_f16 v[32:35], v[208:211], v[24:27], v[32:35]\nv_pk_mul_f16 v18, v70, s48 op_sel:[0,1]\nv_pk_mul_f16 v19, v71, s48 op_sel:[0,1]\n"
      "v_mfma_f32_16x16x32_f16 v[32:35], v[220:223], v[28:31], v[32:35]\nv_pk_mul_f16 v20, v68, v70\nv_pk_mul_f16 v21, v69, v71\nv_mfma_f32_16x16x32_f16 v[36:39], v[224:227], v[28:31], v[36:39]\n"
      "v_add_f32 v44, v44, v45\nv_add_f32 v46, v46, v47\nv_mfma_f32_16x16x32_f16 v[36:39], v[212:215], v[24:27], v[36:39]\nv_mul_f32 v55, v44, v54\nv_mul_f32 v55, v55, v46\n"
      "ds_read_b128 v[44:47], v248 offset:48\nv_mfma_f32_16x16x32_f16 v[40:43], v[216:219], v[24:27], v[40:43]\ns_waitcnt lgkmcnt(1)\nv_pk_mul_f16 v22, v56, v56 op_sel_hi:[0,1]\n"
      "v_pk_mul_f16 v23, v56, v57 op_sel_hi:[0,1]\nv_mfma_f32_16x16x32_f16 v[40:43], v[228:231], v[28:31], v[40:43]\ns_cmp_lt_u32 s45, s53\nv_mul_f32 v32, v32, v48\nv_mul_f32 v33, v33, v49\n"
      "v_mul_f32 v34, v34, v50\ns_cbranch_scc0 .Lc_s4\n"
      ".Lc_u5:\nv_mfma_f32_16x16x32_f16 v[244:247], v[76:79], v[16:19], 0\nds_read_b32 v4, v248\nv_fmac_f32 v55, v32, v36\nv_mul_f32 v35, v35, v51\nv_fmac_f32 v55, v33, v37\n"
      "v_mfma_f32_16x16x32_f16 v[244:247], v[88:91], v[20:23], v[244:247]\nv_fmac_f32 v55, v34, v38\nv_fmac_f32 v55, v35, v39\ns_add_u32 s45, s45, 2\ns_cmp_ge_u32 s45, s46\ns_cbranch_scc0 .Lc_b5\n"
      "s_swappc_b64 s[4:5], s[34:35]\n"
      ".Lc_b5:\nv_mfma_f32_16x16x32_f16 v[36:39], v[84:87], v[20:23], 0\nv_pk_mul_f16 v24, v66, v66 op_sel_hi:[0,1]\nv_pk_mul_f16 v25, v66, v57 op_sel_hi:[0,1]\n"
      "v_mfma_f32_16x16x32_f16 v[36:39], v[72:75], v[16:19], v[36:39]\nv_pk_mul_f16 v26, v56, v66 op_sel_hi:[0,1]\nv_pk_mul_f16 v27, v56, v67 op_sel_hi:[0,1]\ns_setprio 0\n"
      "v_mfma_f32_16x16x32_f16 v[32:35], v[80:83], v[20:23], 0\nv_pk_mul_f16 v28, v58, v66 op_sel_hi:[0,1]\nv_pk_mul_f16 v29, v58, v67 op_sel_hi:[0,1]\ns_waitcnt lgkmcnt(0)\n"
      "v_smfmac_f32_16x16x64_f16 v[44:47], v[92:95], v[16:23], v242\nv_pk_mul_f16 v30, v60, v66 op_sel_hi:[0,1]\nv_pk_mul_f16 v31, v60, v67 op_sel_hi:[0,1]\n"
      "v_mfma_f32_16x16x32_f16 v[32:35], v[96:99], v[24:27], v[32:35]\nv_pk_mul_f16 v16, v62, v66 op_sel_hi:[0,1]\nv_pk_mul_f16 v17, v62, v67 op_sel_hi:[0,1]\ns_setprio 1\n"
      "v_mfma_f32_16x16x32_f16 v[32:35], v[108:111], v[28:31], v[32:35]\nv_pk_mul_f16 v18, v64, v66 op_sel_hi:[0,1]\nv_mul_f32 v40, v40, v52\n"
      "v_mfma_f32_16x16x32_f16 v[36:39], v[112:115], v[28:31], v[36:39]\nv_pk_mul_f16 v19, v64, v67 op_sel_hi:[0,1]\nv_mul_f32 v41, v41, v53\n"
      "v_mfma_f32_16x16x32_f16 v[36:39], v[100:103], v[24:27], v[36:39]\nv_pk_mul_f16 v20, v64, v64 op_sel_hi:[0,1]\nv_fmac_f32 v55, v40, v42\n"
      "v_mfma_f32_16x16x32_f16 v[244:247], v[104:107], v[24:27], v[244:247]\nv_pk_mul_f16 v21, v64, v57 op_sel_hi:[0,1]\nv_fmac_f32 v55, v41, v43\n"
      "v_mfma_f32_16x16x32_f16 v[244:247], v[116:119], v[28:31], v[244:247]\nv_pk_mul_f16 v22, v56, v64 op_sel_hi:[0,1]\nv_readfirstlane_b32 s44, v4\n"
      "v_smfmac_f32_16x16x64_f16 v[44:47], v[120:123], v[24:31], v242\nv_pk_mul_f16 v23, v56, v65 op_sel_hi:[0,1]\nv_mfma_f32_16x16x32_f16 v[32:35], v[124:127], v[16:19], v[32:35]\n"
      "v_pk_mul_f16 v24, v58, v64 op_sel_hi:[0,1]\nv_pk_mul_f16 v25, v58, v65 op_sel_hi:[0,1]\ns_setprio 2\nds_read_b64_tr_b16 v[70:71], v239 offset:11136\n"
      "v_mfma_f32_16x16x32_f16 v[32:35], v[136:139], v[20:23], v[32:35]\nv_pk_mul_f16 v26, v60, v64 op_sel_hi:[0,1]\nds_read_b64_tr_b16 v[68:69], v238 offset:11136\n"
      "v_mfma_f32_16x16x32_f16 v[36:39], v[140:143], v[20:23], v[36:39]\nv_pk_mul_f16 v27, v60, v65 op_sel_hi:[0,1]\nds_read_b64_tr_b16 v[66:67], v237 offset:11776\n"
      "v_mfma_f32_16x16x32_f16 v[36:39], v[128:131], v[16:19], v[36:39]\nv_pk_mul_f16 v28, v62, v64 op_sel_hi:[0,1]\nv_add_f32_dpp v55, v55, v55 quad_perm:[1,0,3,2] row_mask:0xf bank_mask:0xf\n"
      "v_mfma_f32_16x16x32_f16 v[244:247], v[132:135], v[16:19], v[244:247]\nv_pk_mul_f16 v29, v62, v65 op_sel_hi:[0,1]\nv_add_f32_dpp v55, v55, v55 quad_perm:[2,3,0,1] row_mask:0xf bank_mask:0xf\n"
      "v_mfma_f32_16x16x32_f16 v[244:247], v[144:147], v[20:23], v[244:247]\nv_pk_mul_f16 v30, v62, v62 op_sel_hi:[0,1]\nv_add_f32_dpp v55, v55, v55 row_half_mirror row_mask:0xf bank_mask:0xf\n"
      "v_smfmac_f32_16x16x64_f16 v[44:47], v[148:151], v[16:23], v242\nv_pk_mul_f16 v31, v62, v57 op_sel_hi:[0,1]\nv_add_f32_dpp v55, v55, v55 row_mirror row_mask:0xf bank_mask:0xf\n"
      "v_mfma_f32_16x16x32_f16 v[32:35], v[152:155], v[24:27], v[32:35]\nv_pk_mul_f16 v16, v56, v62 op_sel_hi:[0,1]\nv_pk_mul_f16 v17, v56, v63 op_sel_hi:[0,1]\nds_write_b32 v243, v55 offset:512\n"
      "v_mfma_f32_16x16x32_f16 v[32:35], v[164:167], v[28:31], v[32:35]\nv_pk_mul_f16 v18, v58, v62 op_sel_hi:[0,1]\nds_read_b64_tr_b16 v[64:65], v237 offset:11648\n"
      "v_mfma_f32_16x16x32_f16 v[36:39], v[168:171], v[28:31], v[36:39]\nv_pk_mul_f16 v19, v58, v63 op_sel_hi:[0,1]\nv_mfma_f32_16x16x32_f16 v[36:39], v[156:159], v[24:27], v[36:39]\n"
      "v_pk_mul_f16 v20, v60, v62 op_sel_hi:[0,1]\ns_setprio 3\ns_ff0_i32_b32 s53, s44\nv_mfma_f32_16x16x32_f16 v[244:247], v[160:163], v[24:27], v[244:247]\n"
      "v_pk_mul_f16 v21, v60, v63 op_sel_hi:[0,1]\ns_waitcnt lgkmcnt(1)\nv_readlane_b32 s48, v69, 48\nv_mfma_f32_16x16x32_f16 v[244:247], v[172:175], v[28:31], v[244:247]\n"
      "v_pk_mul_f16 v22, v60, v60 op_sel_hi:[0,1]\ns_add_u32 s53, s53, s64\nv_smfmac_f32_16x16x64_f16 v[44:47], v[176:179], v[24:31], v242\nv_pk_mul_f16 v23, v60, v57 op_sel_hi:[0,1]\n"
      "v_mfma_f32_16x16x32_f16 v[32:35], v[180:183], v[16:19], v[32:35]\nv_pk_mul_f16 v24, v56, v60 op_sel_hi:[0,1]\nv_pk_mul_f16 v25, v56, v61 op_sel_hi:[0,1]\n"
      "v_mfma_f32_16x16x32_f16 v[32:35], v[192:195], v[20:23], v[32:35]\nv_pk_mul_f16 v26, v58, v60 op_sel_hi:[0,1]\nv_pk_mul_f16 v27, v58, v61 op_sel_hi:[0,1]\n"
      "v_mfma_f32_16x16x32_f16 v[36:39], v[196:199], v[20:23], v[36:39]\nv_pk_mul_f16 v28, v58, v58 op_sel_hi:[0,1]\nds_read_b64_tr_b16 v[62:63], v237 offset:11520\n"
      "v_mfma_f32_16x16x32_f16 v[36:39], v[184:187], v[16:19], v[36:39]\nv_pk_mul_f16 v29, v58, v57 op_sel_hi:[0,1]\nds_read_b64_tr_b16 v[60:61], v237 offset:11392\n"
      "v_mfma_f32_16x16x32_f16 v[244:247], v[188:191], v[16:19], v[244:247]\nv_pk_mul_f16 v30, v56, v58 op_sel_hi:[0,1]\nv_mfma_f32_16x16x32_f16 v[244:247], v[200:203], v[20:23], v[244:247]\n"
      "v_pk_mul_f16 v31, v56, v59 op_sel_hi:[0,1]\nv_smfmac_f32_16x16x64_f16 v[44:47], v[204:207], v[16:23], v242\nds_read_b64_tr_b16 v[56:57], v236 offset:11136\n"
      "ds_read_b64_tr_b16 v[58:59], v237 offset:11264\nv_smfmac_f32_16x16x64_f16 v[44:47], v[232:235], v[24:31], v242\nv_pk_mul_f16 v16, v68, s48 op_sel:[0,1]\n"
      "v_pk_mul_f16 v17, v69, s48 op_sel:[0,1]\nv_mfma_f32_16x16x32_f16 v[32:35], v[208:211], v[24:27], v[32:35]\nv_pk_mul_f16 v18, v70, s48 op_sel:[0,1]\nv_pk_mul_f16 v19, v71, s48 op_sel:[0,1]\n"
      "v_mfma_f32_16x16x32_f16 v[32:35], v[220:223], v[28:31], v[32:35]\nv_pk_mul_f16 v20, v68, v70\nv_pk_mul_f16 v21, v69, v71\nv_mfma_f32_16x16x32_f16 v[36:39], v[224:227], v[28:31], v[36:39]\n"
      "v_add_f32 v44, v44, v45\nv_add_f32 v46, v46, v47\nv_mfma_f32_16x16x32_f16 v[36:39], v[212:215], v[24:27], v[36:39]\nv_mul_f32 v55, v44, v54\nv_mul_f32 v55, v55, v46\n"
      "ds_read_b128 v[44:47], v248 offset:48\nv_mfma_f32_16x16x32_f16 v[244:247], v[216:219], v[24:27], v[244:247]\ns_waitcnt lgkmcnt(1)\nv_pk_mul_f16 v22, v56, v56 op_sel_hi:[0,1]\n"
      "v_pk_mul_f16 v23, v56, v57 op_sel_hi:[0,1]\nv_mfma_f32_16x16x32_f16 v[244:247], v[228:231], v[28:31], v[244:247]\ns_cmp_lt_u32 s45, s53\nv_mul_f32 v32, v32, v48\nv_mul_f32 v33, v33, v49\n"
      "v_mul_f32 v34, v34, v50\ns_cbranch_scc0 .Lc_s5\n"
      ".Lc_u6:\nv_mfma_f32_16x16x32_f16 v[40:43], v[76:79], v[16:19], 0\nds_read_b32 v4, v248\nv_fmac_f32 v55, v32, v36\nv_mul_f32 v35, v35, v51\nv_fmac_f32 v55, v33, v37\n"
      "v_mfma_f32_16x16x32_f16 v[40:43], v[88:91], v[20:23], v[40:43]\nv_fmac_f32 v55, v34, v38\nv_fmac_f32 v55, v35, v39\ns_add_u32 s45, s45, 2\ns_cmp_ge_u32 s45, s46\ns_cbranch_scc0 .Lc_b6\n"
      "s_swappc_b64 s[4:5], s[34:35]\n"
      ".Lc_b6:\nv_mfma_f32_16x16x32_f16 v[36:39], v[84:87], v[20:23], 0\nv_pk_mul_f16 v24, v66, v66 op_sel_hi:[0,1]\nv_pk_mul_f16 v25, v66, v57 op_sel_hi:[0,1]\n"
      "v_mfma_f32_16x16x32_f16 v[36:39], v[72:75], v[16:19], v[36:39]\nv_pk_mul_f16 v26, v56, v66 op_sel_hi:[0,1]\nv_pk_mul_f16 v27, v56, v67 op_sel_hi:[0,1]\ns_setprio 0\n"
      "v_mfma_f32_16x16x32_f16 v[32:35], v[80:83], v[20:23], 0\nv_pk_mul_f16 v28, v58, v66 op_sel_hi:[0,1]\nv_pk_mul_f16 v29, v58, v67 op_sel_hi:[0,1]\ns_waitcnt lgkmcnt(0)\n"
      "v_smfmac_f32_16x16x64_f16 v[44:47], v[92:95], v[16:23], v242\nv_pk_mul_f16 v30, v60, v66 op_sel_hi:[0,1]\nv_pk_mul_f16 v31, v60, v67 op_sel_hi:[0,1]\n"
      "v_mfma_f32_16x16x32_f16 v[32:35], v[96:99], v[24:27], v[32:35]\nv_pk_mul_f16 v16, v62, v66 op_sel_hi:[0,1]\nv_pk_mul_f16 v17, v62, v67 op_sel_hi:[0,1]\ns_setprio 1\n"
      "v_mfma_f32_16x16x32_f16 v[32:35], v[108:111], v[28:31], v[32:35]\nv_pk_mul_f16 v18, v64, v66 op_sel_hi:[0,1]\nv_mul_f32 v244, v244, v52\n"
      "v_mfma_f32_16x16x32_f16 v[36:39], v[112:115], v[28:31], v[36:39]\nv_pk_mul_f16 v19, v64, v67 op_sel_hi:[0,1]\nv_mul_f32 v245, v245, v53\n"
      "v_mfma_f32_16x16x32_f16 v[36:39], v[100:103], v[24:27], v[36:39]\nv_pk_mul_f16 v20, v64, v64 op_sel_hi:[0,1]\nv_fmac_f32 v55, v244, v246\n"
      "v_mfma_f32_16x16x32_f16 v[40:43], v[104:107], v[24:27], v[40:43]\nv_pk_mul_f16 v21, v64, v57 op_sel_hi:[0,1]\nv_fmac_f32 v55, v245, v247\n"
      "v_mfma_f32_16x16x32_f16 v[40:43], v[116:119], v[28:31], v[40:43]\nv_pk_mul_f16 v22, v56, v64 op_sel_hi:[0,1]\nv_readfirstlane_b32 s44, v4\n"
      "v_smfmac_f32_16x16x64_f16 v[44:47], v[120:123], v[24:31], v242\nv_pk_mul_f16 v23, v56, v65 op_sel_hi:[0,1]\nv_mfma_f32_16x16x32_f16 v[32:35], v[124:127], v[16:19], v[32:35]\n"
      "v_pk_mul_f16 v24, v58, v64 op_sel_hi:[0,1]\nv_pk_mul_f16 v25, v58, v65 op_sel_hi:[0,1]\ns_setprio 2\nds_read_b64_tr_b16 v[70:71], v239 offset:12992\n"
      "v_mfma_f32_16x16x32_f16 v[32:35], v[136:139], v[20:23], v[32:35]\nv_pk_mul_f16 v26, v60, v64 op_sel_hi:[0,1]\nds_read_b64_tr_b16 v[68:69], v238 offset:12992\n"
      "v_mfma_f32_16x16x32_f16 v[36:39], v[140:143], v[20:23], v[36:39]\nv_pk_mul_f16 v27, v60, v65 op_sel_hi:[0,1]\nds_read_b64_tr_b16 v[66:67], v237 offset:13632\n"
      "v_mfma_f32_16x16x32_f16 v[36:39], v[128:131], v[16:19], v[36:39]\nv_pk_mul_f16 v28, v62, v64 op_sel_hi:[0,1]\nv_add_f32_dpp v55, v55, v55 quad_perm:[1,0,3,2] row_mask:0xf bank_mask:0xf\n"
      "v_mfma_f32_16x16x32_f16 v[40:43], v[132:135], v[16:19], v[40:43]\nv_pk_mul_f16 v29, v62, v65 op_sel_hi:[0,1]\nv_add_f32_dpp v55, v55, v55 quad_perm:[2,3,0,1] row_mask:0xf bank_mask:0xf\n"
      "v_mfma_f32_16x16x32_f16 v[40:43], v[144:147], v[20:23], v[40:43]\nv_pk_mul_f16 v30, v62, v62 op_sel_hi:[0,1]\nv_add_f32_dpp v55, v55, v55 row_half_mirror row_mask:0xf bank_mask:0xf\n"
      "v_smfmac_f32_16x16x64_f16 v[44:47], v[148:151], v[16:23], v242\nv_pk_mul_f16 v31, v62, v57 op_sel_hi:[0,1]\nv_add_f32_dpp v55, v55, v55 row_mirror row_mask:0xf bank_mask:0xf\n"
      "v_mfma_f32_16x16x32_f16 v[32:35], v[152:155], v[24:27], v[32:35]\nv_pk_mul_f16 v16, v56, v62 op_sel_hi:[0,1]\nv_pk_mul_f16 v17, v56, v63 op_sel_hi:[0,1]\nds_write_b32 v243, v55 offset:640\n"
      "v_mfma_f32_16x16x32_f16 v[32:35], v[164:167], v[28:31], v[32:35]\nv_pk_mul_f16 v18, v58, v62 op_sel_hi:[0,1]\nds_read_b64_tr_b16 v[64:65], v237 offset:13504\n"
      "v_mfma_f32_16x16x32_f16 v[36:39], v[168:171], v[28:31], v[36:39]\nv_pk_mul_f16 v19, v58, v63 op_sel_hi:[0,1]\nv_mfma_f32_16x16x32_f16 v[36:39], v[156:159], v[24:27], v[36:39]\n"
      "v_pk_mul_f16 v20, v60, v62 op_sel_hi:[0,1]\ns_setprio 3\ns_ff0_i32_b32 s53, s44\nv_mfma_f32_16x16x32_f16 v[40:43], v[160:163], v[24:27], v[40:43]\n"
      "v_pk_mul_f16 v21, v60, v63 op_sel_hi:[0,1]\ns_waitcnt lgkmcnt(1)\nv_readlane_b32 s48, v69, 48\nv_mfma_f32_16x16x32_f16 v[40:43], v[172:175], v[28:31], v[40:43]\n"
      "v_pk_mul_f16 v22, v60, v60 op_sel_hi:[0,1]\ns_add_u32 s53, s53, s64\nv_smfmac_f32_16x16x64_f16 v[44:47], v[176:179], v[24:31], v242\nv_pk_mul_f16 v23, v60, v57 op_sel_hi:[0,1]\n"
      "v_mfma_f32_16x16x32_f16 v[32:35], v[180:183], v[16:19], v[32:35]\nv_pk_mul_f16 v24, v56, v60 op_sel_hi:[0,1]\nv_pk_mul_f16 v25, v56, v61 op_sel_hi:[0,1]\n"
      "v_mfma_f32_16x16x32_f16 v[32:35], v[192:195], v[20:23], v[32:35]\nv_pk_mul_f16 v26, v58, v60 op_sel_hi:[0,1]\nv_pk_mul_f16 v27, v58, v61 op_sel_hi:[0,1]\n"
      "v_mfma_f32_16x16x32_f16 v[36:39], v[196:199], v[20:23], v[36:39]\nv_pk_mul_f16 v28, v58, v58 op_sel_hi:[0,1]\nds_read_b64_tr_b16 v[62:63], v237 offset:13376\n"
      "v_mfma_f32_16x16x32_f16 v[36:39], v[184:187], v[16:19], v[36:39]\nv_pk_mul_f16 v29, v58, v57 op_sel_hi:[0,1]\nds_read_b64_tr_b16 v[60:61], v237 offset:13248\n"
      "v_mfma_f32_16x16x32_f16 v[40:43], v[188:191], v[16:19], v[40:43]\nv_pk_mul_f16 v30, v56, v58 op_sel_hi:[0,1]\nv_mfma_f32_16x16x32_f16 v[40:43], v[200:203], v[20:23], v[40:43]\n"
      "v_pk_mul_f16 v31, v56, v59 op_sel_hi:[0,1]\nv_smfmac_f32_16x16x64_f16 v[44:47], v[204:207], v[16:23], v242\nds_read_b64_tr_b16 v[56:57], v236 offset:12992\n"
      "ds_read_b64_tr_b16 v[58:59], v237 offset:13120\nv_smfmac_f32_16x16x64_f16 v[44:47], v[232:235], v[24:31], v242\nv_pk_mul_f16 v16, v68, s48 op_sel:[0,1]\n"
      "v_pk_mul_f16 v17, v69, s48 op_sel:[0,1]\nv_mfma_f32_16x16x32_f16 v[32:35], v[208:211], v[24:27], v[32:35]\nv_pk_mul_f16 v18, v70, s48 op_sel:[0,1]\nv_pk_mul_f16 v19, v71, s48 op_sel:[0,1]\n"
      "v_mfma_f32_16x16x32_f16 v[32:35], v[220:223], v[28:31], v[32:35]\nv_pk_mul_f16 v20, v68, v70\nv_pk_mul_f16 v21, v69, v71\nv_mfma_f32_16x16x32_f16 v[36:39], v[224:227], v[28:31], v[36:39]\n"
      "v_add_f32 v44, v44, v45\nv_add_f32 v46, v46, v47\nv_mfma_f32_16x16x32_f16 v[36:39], v[212:215], v[24:27], v[36:39]\nv_mul_f32 v55, v44, v54\nv_mul_f32 v55, v55, v46\n"
      "ds_read_b128 v[44:47], v248 offset:48\nv_mfma_f32_16x16x32_f16 v[40:43], v[216:219], v[24:27], v[40:43]\ns_waitcnt lgkmcnt(1)\nv_pk_mul_f16 v22, v56, v56 op_sel_hi:[0,1]\n"
      "v_pk_mul_f16 v23, v56, v57 op_sel_hi:[0,1]\nv_mfma_f32_16x16x32_f16 v[40:43], v[228:231], v[28:31], v[40:43]\ns_cmp_lt_u32 s45, s53\nv_mul_f32 v32, v32, v48\nv_mul_f32 v33, v33, v49\n"
      "v_mul_f32 v34, v34, v50\ns_cbranch_scc0 .Lc_s6\n"
      ".Lc_u7:\nv_mfma_f32_16x16x32_f16 v[244:247], v[76:79], v[16:19], 0\nds_read_b32 v4, v248\nv_fmac_f32 v55, v32, v36\nv_mul_f32 v35, v35, v51\nv_fmac_f32 v55, v33, v37\n"
      "v_mfma_f32_16x16x32_f16 v[244:247], v[88:91], v[20:23], v[244:247]\nv_fmac_f32 v55, v34, v38\nv_fmac_f32 v55, v35, v39\ns_add_u32 s45, s45, 2\ns_cmp_ge_u32 s45, s46\ns_cbranch_scc0 .Lc_b7\n"
      "s_swappc_b64 s[4:5], s[34:35]\n"
      ".Lc_b7:\nv_mfma_f32_16x16x32_f16 v[36:39], v[84:87], v[20:23], 0\nv_pk_mul_f16 v24, v66, v66 op_sel_hi:[0,1]\nv_pk_mul_f16 v25, v66, v57 op_sel_hi:[0,1]\n"
      "v_mfma_f32_16x16x32_f16 v[36:39], v[72:75], v[16:19], v[36:39]\nv_pk_mul_f16 v26, v56, v66 op_sel_hi:[0,1]\nv_pk_mul_f16 v27, v56, v67 op_sel_hi:[0,1]\ns_setprio 0\n"
      "v_mfma_f32_16x16x32_f16 v[32:35], v[80:83], v[20:23], 0\nv_pk_mul_f16 v28, v58, v66 op_sel_hi:[0,1]\nv_pk_mul_f16 v29, v58, v67 op_sel_hi:[0,1]\ns_waitcnt lgkmcnt(0)\n"
      "v_smfmac_f32_16x16x64_f16 v[44:47], v[92:95], v[16:23], v242\nv_pk_mul_f16 v30, v60, v66 op_sel_hi:[0,1]\nv_pk_mul_f16 v31, v60, v67 op_sel_hi:[0,1]\n"
      "v_mfma_f32_16x16x32_f16 v[32:35], v[96:99], v[24:27], v[32:35]\nv_pk_mul_f16 v16, v62, v66 op_sel_hi:[0,1]\nv_pk_mul_f16 v17, v62, v67 op_sel_hi:[0,1]\ns_setprio 1\n"
      "v_mfma_f32_16x16x32_f16 v[32:35], v[108:111], v[28:31], v[32:35]\nv_pk_mul_f16 v18, v64, v66 op_sel_hi:[0,1]\nv_mul_f32 v40, v40, v52\n"
      "v_mfma_f32_16x16x32_f16 v[36:39], v[112:115], v[28:31], v[36:39]\nv_pk_mul_f16 v19, v64, v67 op_sel_hi:[0,1]\nv_mul_f32 v41, v41, v53\n"
      "v_mfma_f32_16x16x32_f16 v[36:39], v[100:103], v[24:27], v[36:39]\nv_pk_mul_f16 v20, v64, v64 op_sel_hi:[0,1]\nv_fmac_f32 v55, v40, v42\n"
      "v_mfma_f32_16x16x32_f16 v[244:247], v[104:107], v[24:27], v[244:247]\nv_pk_mul_f16 v21, v64, v57 op_sel_hi:[0,1]\nv_fmac_f32 v55, v41, v43\n"
      "v_mfma_f32_16x16x32_f16 v[244:247], v[116:119], v[28:31], v[244:247]\nv_pk_mul_f16 v22, v56, v64 op_sel_hi:[0,1]\nv_readfirstlane_b32 s44, v4\n"
      "v_smfmac_f32_16x16x64_f16 v[44:47], v[120:123], v[24:31], v242\nv_pk_mul_f16 v23, v56, v65 op_sel_hi:[0,1]\nv_mfma_f32_16x16x32_f16 v[32:35], v[124:127], v[16:19], v[32:35]\n"
      "v_pk_mul_f16 v24, v58, v64 op_sel_hi:[0,1]\nv_pk_mul_f16 v25, v58, v65 op_sel_hi:[0,1]\ns_setprio 2\nds_read_b64_tr_b16 v[70:71], v239 offset:14848\n"
      "v_mfma_f32_16x16x32_f16 v[32:35], v[136:139], v[20:23], v[32:35]\nv_pk_mul_f16 v26, v60, v64 op_sel_hi:[0,1]\nds_read_b64_tr_b16 v[68:69], v238 offset:14848\n"
      "v_mfma_f32_16x16x32_f16 v[36:39], v[140:143], v[20:23], v[36:39]\nv_pk_mul_f16 v27, v60, v65 op_sel_hi:[0,1]\nds_read_b64_tr_b16 v[66:67], v237 offset:15488\n"
      "v_mfma_f32_16x16x32_f16 v[36:39], v[128:131], v[16:19], v[36:39]\nv_pk_mul_f16 v28, v62, v64 op_sel_hi:[0,1]\nv_add_f32_dpp v55, v55, v55 quad_perm:[1,0,3,2] row_mask:0xf bank_mask:0xf\n"
      "v_mfma_f32_16x16x32_f16 v[244:247], v[132:135], v[16:19], v[244:247]\nv_pk_mul_f16 v29, v62, v65 op_sel_hi:[0,1]\nv_add_f32_dpp v55, v55, v55 quad_perm:[2,3,0,1] row_mask:0xf bank_mask:0xf\n"
      "v_mfma_f32_16x16x32_f16 v[244:247], v[144:147], v[20:23], v[244:247]\nv_pk_mul_f16 v30, v62, v62 op_sel_hi:[0,1]\nv_add_f32_dpp v55, v55, v55 row_half_mirror row_mask:0xf bank_mask:0xf\n"
      "v_smfmac_f32_16x16x64_f16 v[44:47], v[148:151], v[16:23], v242\nv_pk_mul_f16 v31, v62, v57 op_sel_hi:[0,1]\nv_add_f32_dpp v55, v55, v55 row_mirror row_mask:0xf bank_mask:0xf\n"
      "v_mfma_f32_16x16x32_f16 v[32:35], v[152:155], v[24:27], v[32:35]\nv_pk_mul_f16 v16, v56, v62 op_sel_hi:[0,1]\nv_pk_mul_f16 v17, v56, v63 op_sel_hi:[0,1]\nds_write_b32 v243, v55 offset:768\n"
      "v_mfma_f32_16x16x32_f16 v[32:35], v[164:167], v[28:31], v[32:35]\nv_pk_mul_f16 v18, v58, v62 op_sel_hi:[0,1]\nds_read_b64_tr_b16 v[64:65], v237 offset:15360\n"
      "v_mfma_f32_16x16x32_f16 v[36:39], v[168:171], v[28:31], v[36:39]\nv_pk_mul_f16 v19, v58, v63 op_sel_hi:[0,1]\nv_mfma_f32_16x16x32_f16 v[36:39], v[156:159], v[24:27], v[36:39]\n"
      "v_pk_mul_f16 v20, v60, v62 op_sel_hi:[0,1]\ns_setprio 3\ns_ff0_i32_b32 s53, s44\nv_mfma_f32_16x16x32_f16 v[244:247], v[160:163], v[24:27], v[244:247]\n"
      "v_pk_mul_f16 v21, v60, v63 op_sel_hi:[0,1]\ns_waitcnt lgkmcnt(1)\nv_readlane_b32 s48, v69, 48\nv_mfma_f32_16x16x32_f16 v[244:247], v[172:175], v[28:31], v[244:247]\n"
      "v_pk_mul_f16 v22, v60, v60 op_sel_hi:[0,1]\ns_add_u32 s53, s53, s64\nv_smfmac_f32_16x16x64_f16 v[44:47], v[176:179], v[24:31], v242\nv_pk_mul_f16 v23, v60, v57 op_sel_hi:[0,1]\n"
      "v_mfma_f32_16x16x32_f16 v[32:35], v[180:183], v[16:19], v[32:35]\nv_pk_mul_f16 v24, v56, v60 op_sel_hi:[0,1]\nv_pk_mul_f16 v25, v56, v61 op_sel_hi:[0,1]\n"
      "v_mfma_f32_16x16x32_f16 v[32:35], v[192:195], v[20:23], v[32:35]\nv_pk_mul_f16 v26, v58, v60 op_sel_hi:[0,1]\nv_pk_mul_f16 v27, v58, v61 op_sel_hi:[0,1]\n"
      "v_mfma_f32_16x16x32_f16 v[36:39], v[196:199], v[20:23], v[36:39]\nv_pk_mul_f16 v28, v58, v58 op_sel_hi:[0,1]\nds_read_b64_tr_b16 v[62:63], v237 offset:15232\n"
      "v_mfma_f32_16x16x32_f16 v[36:39], v[184:187], v[16:19], v[36:39]\nv_pk_mul_f16 v29, v58, v57 op_sel_hi:[0,1]\nds_read_b64_tr_b16 v[60:61], v237 offset:15104\n"
      "v_mfma_f32_16x16x32_f16 v[244:247], v[188:191], v[16:19], v[244:247]\nv_pk_mul_f16 v30, v56, v58 op_sel_hi:[0,1]\nv_mfma_f32_16x16x32_f16 v[244:247], v[200:203], v[20:23], v[244:247]\n"
      "v_pk_mul_f16 v31, v56, v59 op_sel_hi:[0,1]\nv_smfmac_f32_16x16x64_f16 v[44:47], v[204:207], v[16:23], v242\nds_read_b64_tr_b16 v[56:57], v236 offset:14848\n"
      "ds_read_b64_tr_b16 v[58:59], v237 offset:14976\nv_smfmac_f32_16x16x64_f16 v[44:47], v[232:235], v[24:31], v242\nv_pk_mul_f16 v16, v68, s48 op_sel:[0,1]\n"
      "v_pk_mul_f16 v17, v69, s48 op_sel:[0,1]\nv_mfma_f32_16x16x32_f16 v[32:35], v[208:211], v[24:27], v[32:35]\nv_pk_mul_f16 v18, v70, s48 op_sel:[0,1]\nv_pk_mul_f16 v19, v71, s48 op_sel:[0,1]\n"
      "v_mfma_f32_16x16x32_f16 v[32:35], v[220:223], v[28:31], v[32:35]\nv_pk_mul_f16 v20, v68, v70\nv_pk_mul_f16 v21, v69, v71\nv_mfma_f32_16x16x32_f16 v[36:39], v[224:227], v[28:31], v[36:39]\n"
      "v_add_f32 v44, v44, v45\nv_add_f32 v46, v46, v47\nv_mfma_f32_16x16x32_f16 v[36:39], v[212:215], v[24:27], v[36:39]\nv_mul_f32 v55, v44, v54\nv_mul_f32 v55, v55, v46\n"
      "ds_read_b128 v[44:47], v248 offset:48\nv_mfma_f32_16x16x32_f16 v[244:247], v[216:219], v[24:27], v[244:247]\ns_waitcnt lgkmcnt(1)\nv_pk_mul_f16 v22, v56, v56 op_sel_hi:[0,1]\n"
      "v_pk_mul_f16 v23, v56, v57 op_sel_hi:[0,1]\nv_mfma_f32_16x16x32_f16 v[244:247], v[228:231], v[28:31], v[244:247]\ns_cmp_lt_u32 s45, s53\nv_mul_f32 v32, v32, v48\nv_mul_f32 v33, v33, v49\n"
      "v_mul_f32 v34, v34, v50\ns_cbranch_scc0 .Lc_s7\n"
      ".Lc_u8:\nv_mfma_f32_16x16x32_f16 v[40:43], v[76:79], v[16:19], 0\nds_read_b32 v4, v248\nv_fmac_f32 v55, v32, v36\nv_mul_f32 v35, v35, v51\nv_fmac_f32 v55, v33, v37\n"
      "v_mfma_f32_16x16x32_f16 v[40:43], v[88:91], v[20:23], v[40:43]\nv_fmac_f32 v55, v34, v38\nv_fmac_f32 v55, v35, v39\ns_add_u32 s45, s45, 2\ns_cmp_ge_u32 s45, s46\ns_cbranch_scc0 .Lc_b8\n"
      "s_swappc_b64 s[4:5], s[34:35]\n"
      ".Lc_b8:\nv_mfma_f32_16x16x32_f16 v[36:39], v[84:87], v[20:23], 0\nv_pk_mul_f16 v24, v66, v66 op_sel_hi:[0,1]\nv_pk_mul_f16 v25, v66, v57 op_sel_hi:[0,1]\n"
      "v_mfma_f32_16x16x32_f16 v[36:39], v[72:75], v[16:19], v[36:39]\nv_pk_mul_f16 v26, v56, v66 op_sel_hi:[0,1]\nv_pk_mul_f16 v27, v56, v67 op_sel_hi:[0,1]\ns_setprio 0\n"
      "v_mfma_f32_16x16x32_f16 v[32:35], v[80:83], v[20:23], 0\nv_pk_mul_f16 v28, v58, v66 op_sel_hi:[0,1]\nv_pk_mul_f16 v29, v58, v67 op_sel_hi:[0,1]\ns_waitcnt lgkmcnt(0)\n"
      "v_smfmac_f32_16x16x64_f16 v[44:47], v[92:95], v[16:23], v242\nv_pk_mul_f16 v30, v60, v66 op_sel_hi:[0,1]\nv_pk_mul_f16 v31, v60, v67 op_sel_hi:[0,1]\n"
      "v_mfma_f32_16x16x32_f16 v[32:35], v[96:99], v[24:27], v[32:35]\nv_pk_mul_f16 v16, v62, v66 op_sel_hi:[0,1]\nv_pk_mul_f16 v17, v62, v67 op_sel_hi:[0,1]\ns_setprio 1\n"
      "v_mfma_f32_16x16x32_f16 v[32:35], v[108:111], v[28:31], v[32:35]\nv_pk_mul_f16 v18, v64, v66 op_sel_hi:[0,1]\nv_mul_f32 v244, v244, v52\n"
      "v_mfma_f32_16x16x32_f16 v[36:39], v[112:115], v[28:31], v[36:39]\nv_pk_mul_f16 v19, v64, v67 op_sel_hi:[0,1]\nv_mul_f32 v245, v245, v53\n"
      "v_mfma_f32_16x16x32_f16 v[36:39], v[100:103], v[24:27], v[36:39]\nv_pk_mul_f16 v20, v64, v64 op_sel_hi:[0,1]\nv_fmac_f32 v55, v244, v246\n"
      "v_mfma_f32_16x16x32_f16 v[40:43], v[104:107], v[24:27], v[40:43]\nv_pk_mul_f16 v21, v64, v57 op_sel_hi:[0,1]\nv_fmac_f32 v55, v245, v247\n"
      "v_mfma_f32_16x16x32_f16 v[40:43], v[116:119], v[28:31], v[40:43]\nv_pk_mul_f16 v22, v56, v64 op_sel_hi:[0,1]\nv_readfirstlane_b32 s44, v4\n"
      "v_smfmac_f32_16x16x64_f16 v[44:47], v[120:123], v[24:31], v242\nv_pk_mul_f16 v23, v56, v65 op_sel_hi:[0,1]\nv_mfma_f32_16x16x32_f16 v[32:35], v[124:127], v[16:19], v[32:35]\n"
      "v_pk_mul_f16 v24, v58, v64 op_sel_hi:[0,1]\nv_pk_mul_f16 v25, v58, v65 op_sel_hi:[0,1]\ns_setprio 2\nds_read_b64_tr_b16 v[70:71], v239 offset:16704\n"
      "v_mfma_f32_16x16x32_f16 v[32:35], v[136:139], v[20:23], v[32:35]\nv_pk_mul_f16 v26, v60, v64 op_sel_hi:[0,1]\nds_read_b64_tr_b16 v[68:69], v238 offset:16704\n"
      "v_mfma_f32_16x16x32_f16 v[36:39], v[140:143], v[20:23], v[36:39]\nv_pk_mul_f16 v27, v60, v65 op_sel_hi:[0,1]\nds_read_b64_tr_b16 v[66:67], v237 offset:17344\n"
      "v_mfma_f32_16x16x32_f16 v[36:39], v[128:131], v[16:19], v[36:39]\nv_pk_mul_f16 v28, v62, v64 op_sel_hi:[0,1]\nv_add_f32_dpp v55, v55, v55 quad_perm:[1,0,3,2] row_mask:0xf bank_mask:0xf\n"
      "v_mfma_f32_16x16x32_f16 v[40:43], v[132:135], v[16:19], v[40:43]\nv_pk_mul_f16 v29, v62, v65 op_sel_hi:[0,1]\nv_add_f32_dpp v55, v55, v55 quad_perm:[2,3,0,1] row_mask:0xf bank_mask:0xf\n"
      "v_mfma_f32_16x16x32_f16 v[40:43], v[144:147], v[20:23], v[40:43]\nv_pk_mul_f16 v30, v62, v62 op_sel_hi:[0,1]\nv_add_f32_dpp v55, v55, v55 row_half_mirror row_mask:0xf bank_mask:0xf\n"
      "v_smfmac_f32_16x16x64_f16 v[44:47], v[148:151], v[16:23], v242\nv_pk_mul_f16 v31, v62, v57 op_sel_hi:[0,1]\nv_add_f32_dpp v55, v55, v55 row_mirror row_mask:0xf bank_mask:0xf\n"
      "v_mfma_f32_16x16x32_f16 v[32:35], v[152:155], v[24:27], v[32:35]\nv_pk_mul_f16 v16, v56, v62 op_sel_hi:[0,1]\nv_pk_mul_f16 v17, v56, v63 op_sel_hi:[0,1]\nds_write_b32 v243, v55 offset:896\n"
      "v_mfma_f32_16x16x32_f16 v[32:35], v[164:167], v[28:31], v[32:35]\nv_pk_mul_f16 v18, v58, v62 op_sel_hi:[0,1]\nds_read_b64_tr_b16 v[64:65], v237 offset:17216\n"
      "v_mfma_f32_16x16x32_f16 v[36:39], v[168:171], v[28:31], v[36:39]\nv_pk_mul_f16 v19, v58, v63 op_sel_hi:[0,1]\nv_mfma_f32_16x16x32_f16 v[36:39], v[156:159], v[24:27], v[36:39]\n"
      "v_pk_mul_f16 v20, v60, v62 op_sel_hi:[0,1]\ns_setprio 3\ns_ff0_i32_b32 s53, s44\nv_mfma_f32_16x16x32_f16 v[40:43], v[160:163], v[24:27], v[40:43]\n"
      "v_pk_mul_f16 v21, v60, v63 op_sel_hi:[0,1]\ns_waitcnt lgkmcnt(1)\nv_readlane_b32 s48, v69, 48\nv_mfma_f32_16x16x32_f16 v[40:43], v[172:175], v[28:31], v[40:43]\n"
      "v_pk_mul_f16 v22, v60, v60 op_sel_hi:[0,1]\ns_add_u32 s53, s53, s64\nv_smfmac_f32_16x16x64_f16 v[44:47], v[176:179], v[24:31], v242\nv_pk_mul_f16 v23, v60, v57 op_sel_hi:[0,1]\n"
      "v_mfma_f32_16x16x32_f16 v[32:35], v[180:183], v[16:19], v[32:35]\nv_pk_mul_f16 v24, v56, v60 op_sel_hi:[0,1]\nv_pk_mul_f16 v25, v56, v61 op_sel_hi:[0,1]\n"
      "v_mfma_f32_16x16x32_f16 v[32:35], v[192:195], v[20:23], v[32:35]\nv_pk_mul_f16 v26, v58, v60 op_sel_hi:[0,1]\nv_pk_mul_f16 v27, v58, v61 op_sel_hi:[0,1]\n"
      "v_mfma_f32_16x16x32_f16 v[36:39], v[196:199], v[20:23], v[36:39]\nv_pk_mul_f16 v28, v58, v58 op_sel_hi:[0,1]\nds_read_b64_tr_b16 v[62:63], v237 offset:17088\n"
      "v_mfma_f32_16x16x32_f16 v[36:39], v[184:187], v[16:19], v[36:39]\nv_pk_mul_f16 v29, v58, v57 op_sel_hi:[0,1]\nds_read_b64_tr_b16 v[60:61], v237 offset:16960\n"
      "v_mfma_f32_16x16x32_f16 v[40:43], v[188:191], v[16:19], v[40:43]\nv_pk_mul_f16 v30, v56, v58 op_sel_hi:[0,1]\nv_mfma_f32_16x16x32_f16 v[40:43], v[200:203], v[20:23], v[40:43]\n"
      "v_pk_mul_f16 v31, v56, v59 op_sel_hi:[0,1]\nv_smfmac_f32_16x16x64_f16 v[44:47], v[204:207], v[16:23], v242\nds_read_b64_tr_b16 v[56:57], v236 offset:16704\n"
      "ds_read_b64_tr_b16 v[58:59], v237 offset:16832\nv_smfmac_f32_16x16x64_f16 v[44:47], v[232:235], v[24:31], v242\nv_pk_mul_f16 v16, v68, s48 op_sel:[0,1]\n"
      "v_pk_mul_f16 v17, v69, s48 op_sel:[0,1]\nv_mfma_f32_16x16x32_f16 v[32:35], v[208:211], v[24:27], v[32:35]\nv_pk_mul_f16 v18, v70, s48 op_sel:[0,1]\nv_pk_mul_f16 v19, v71, s48 op_sel:[0,1]\n"
      "v_mfma_f32_16x16x32_f16 v[32:35], v[220:223], v[28:31], v[32:35]\nv_pk_mul_f16 v20, v68, v70\nv_pk_mul_f16 v21, v69, v71\nv_mfma_f32_16x16x32_f16 v[36:39], v[224:227], v[28:31], v[36:39]\n"
      "v_add_f32 v44, v44, v45\nv_add_f32 v46, v46, v47\nv_mfma_f32_16x16x32_f16 v[36:39], v[212:215], v[24:27], v[36:39]\nv_mul_f32 v55, v44, v54\nv_mul_f32 v55, v55, v46\n"
      "ds_read_b128 v[44:47], v248 offset:48\nv_mfma_f32_16x16x32_f16 v[40:43], v[216:219], v[24:27], v[40:43]\ns_waitcnt lgkmcnt(1)\nv_pk_mul_f16 v22, v56, v56 op_sel_hi:[0,1]\n"
      "v_pk_mul_f16 v23, v56, v57 op_sel_hi:[0,1]\nv_mfma_f32_16x16x32_f16 v[40:43], v[228:231], v[28:31], v[40:43]\ns_cmp_lt_u32 s45, s53\nv_mul_f32 v32, v32, v48\nv_mul_f32 v33, v33, v49\n"
      "v_mul_f32 v34, v34, v50\ns_cbranch_scc0 .Lc_s8\n"
      ".Lc_u9:\nv_mfma_f32_16x16x32_f16 v[244:247], v[76:79], v[16:19], 0\nds_read_b32 v4, v248\nv_fmac_f32 v55, v32, v36\nv_mul_f32 v35, v35, v51\nv_fmac_f32 v55, v33, v37\n"
      "v_mfma_f32_16x16x32_f16 v[244:247], v[88:91], v[20:23], v[244:247]\nv_fmac_f32 v55, v34, v38\nv_fmac_f32 v55, v35, v39\ns_add_u32 s45, s45, 2\ns_cmp_ge_u32 s45, s46\ns_cbranch_scc0 .Lc_b9\n"
      "s_swappc_b64 s[4:5], s[34:35]\n"
      ".Lc_b9:\nv_mfma_f32_16x16x32_f16 v[36:39], v[84:87], v[20:23], 0\nv_pk_mul_f16 v24, v66, v66 op_sel_hi:[0,1]\nv_pk_mul_f16 v25, v66, v57 op_sel_hi:[0,1]\n"
      "v_mfma_f32_16x16x32_f16 v[36:39], v[72:75], v[16:19], v[36:39]\nv_pk_mul_f16 v26, v56, v66 op_sel_hi:[0,1]\nv_pk_mul_f16 v27, v56, v67 op_sel_hi:[0,1]\ns_setprio 0\n"
      "v_mfma_f32_16x16x32_f16 v[32:35], v[80:83], v[20:23], 0\nv_pk_mul_f16 v28, v58, v66 op_sel_hi:[0,1]\nv_pk_mul_f16 v29, v58, v67 op_sel_hi:[0,1]\ns_waitcnt lgkmcnt(0)\n"
      "v_smfmac_f32_16x16x64_f16 v[44:47], v[92:95], v[16:23], v242\nv_pk_mul_f16 v30, v60, v66 op_sel_hi:[0,1]\nv_pk_mul_f16 v31, v60, v67 op_sel_hi:[0,1]\n"
      "v_mfma_f32_16x16x32_f16 v[32:35], v[96:99], v[24:27], v[32:35]\nv_pk_mul_f16 v16, v62, v66 op_sel_hi:[0,1]\nv_pk_mul_f16 v17, v62, v67 op_sel_hi:[0,1]\ns_setprio 1\n"
      "v_mfma_f32_16x16x32_f16 v[32:35], v[108:111], v[28:31], v[32:35]\nv_pk_mul_f16 v18, v64, v66 op_sel_hi:[0,1]\nv_mul_f32 v40, v40, v52\n"
      "v_mfma_f32_16x16x32_f16 v[36:39], v[112:115], v[28:31], v[36:39]\nv_pk_mul_f16 v19, v64, v67 op_sel_hi:[0,1]\nv_mul_f32 v41, v41, v53\n"
      "v_mfma_f32_16x16x32_f16 v[36:39], v[100:103], v[24:27], v[36:39]\nv_pk_mul_f16 v20, v64, v64 op_sel_hi:[0,1]\nv_fmac_f32 v55, v40, v42\n"
      "v_mfma_f32_16x16x32_f16 v[244:247], v[104:107], v[24:27], v[244:247]\nv_pk_mul_f16 v21, v64, v57 op_sel_hi:[0,1]\nv_fmac_f32 v55, v41, v43\n"
      "v_mfma_f32_16x16x32_f16 v[244:247], v[116:119], v[28:31], v[244:247]\nv_pk_mul_f16 v22, v56, v64 op_sel_hi:[0,1]\nv_readfirstlane_b32 s44, v4\n"
      "v_smfmac_f32_16x16x64_f16 v[44:47], v[120:123], v[24:31], v242\nv_pk_mul_f16 v23, v56, v65 op_sel_hi:[0,1]\nv_mfma_f32_16x16x32_f16 v[32:35], v[124:127], v[16:19], v[32:35]\n"
      "v_pk_mul_f16 v24, v58, v64 op_sel_hi:[0,1]\nv_pk_mul_f16 v25, v58, v65 op_sel_hi:[0,1]\ns_setprio 2\nds_read_b64_tr_b16 v[70:71], v239 offset:18560\n"
      "v_mfma_f32_16x16x32_f16 v[32:35], v[136:139], v[20:23], v[32:35]\nv_pk_mul_f16 v26, v60, v64 op_sel_hi:[0,1]\nds_read_b64_tr_b16 v[68:69], v238 offset:18560\n"
      "v_mfma_f32_16x16x32_f16 v[36:39], v[140:143], v[20:23], v[36:39]\nv_pk_mul_f16 v27, v60, v65 op_sel_hi:[0,1]\nds_read_b64_tr_b16 v[66:67], v237 offset:19200\n"
      "v_mfma_f32_16x16x32_f16 v[36:39], v[128:131], v[16:19], v[36:39]\nv_pk_mul_f16 v28, v62, v64 op_sel_hi:[0,1]\nv_add_f32_dpp v55, v55, v55 quad_perm:[1,0,3,2] row_mask:0xf bank_mask:0xf\n"
      "v_mfma_f32_16x16x32_f16 v[244:247], v[132:135], v[16:19], v[244:247]\nv_pk_mul_f16 v29, v62, v65 op_sel_hi:[0,1]\nv_add_f32_dpp v55, v55, v55 quad_perm:[2,3,0,1] row_mask:0xf bank_mask:0xf\n"
      "v_mfma_f32_16x16x32_f16 v[244:247], v[144:147], v[20:23], v[244:247]\nv_pk_mul_f16 v30, v62, v62 op_sel_hi:[0,1]\nv_add_f32_dpp v55, v55, v55 row_half_mirror row_mask:0xf bank_mask:0xf\n"
      "v_smfmac_f32_16x16x64_f16 v[44:47], v[148:151], v[16:23], v242\nv_pk_mul_f16 v31, v62, v57 op_sel_hi:[0,1]\nv_add_f32_dpp v55, v55, v55 row_mirror row_mask:0xf bank_mask:0xf\n"
      "v_mfma_f32_16x16x32_f16 v[32:35], v[152:155], v[24:27], v[32:35]\nv_pk_mul_f16 v16, v56, v62 op_sel_hi:[0,1]\nv_pk_mul_f16 v17, v56, v63 op_sel_hi:[0,1]\nds_write_b32 v243, v55 offset:1024\n"
      "v_mfma_f32_16x16x32_f16 v[32:35], v[164:167], v[28:31], v[32:35]\nv_pk_mul_f16 v18, v58, v62 op_sel_hi:[0,1]\nds_read_b64_tr_b16 v[64:65], v237 offset:19072\n"
      "v_mfma_f32_16x16x32_f16 v[36:39], v[168:171], v[28:31], v[36:39]\nv_pk_mul_f16 v19, v58, v63 op_sel_hi:[0,1]\nv_mfma_f32_16x16x32_f16 v[36:39], v[156:159], v[24:27], v[36:39]\n"
      "v_pk_mul_f16 v20, v60, v62 op_sel_hi:[0,1]\ns_setprio 3\ns_ff0_i32_b32 s53, s44\nv_mfma_f32_16x16x32_f16 v[244:247], v[160:163], v[24:27], v[244:247]\n"
      "v_pk_mul_f16 v21, v60, v63 op_sel_hi:[0,1]\ns_waitcnt lgkmcnt(1)\nv_readlane_b32 s48, v69, 48\nv_mfma_f32_16x16x32_f16 v[244:247], v[172:175], v[28:31], v[244:247]\n"
      "v_pk_mul_f16 v22, v60, v60 op_sel_hi:[0,1]\ns_add_u32 s53, s53, s64\nv_smfmac_f32_16x16x64_f16 v[44:47], v[176:179], v[24:31], v242\nv_pk_mul_f16 v23, v60, v57 op_sel_hi:[0,1]\n"
      "v_mfma_f32_16x16x32_f16 v[32:35], v[180:183], v[16:19], v[32:35]\nv_pk_mul_f16 v24, v56, v60 op_sel_hi:[0,1]\nv_pk_mul_f16 v25, v56, v61 op_sel_hi:[0,1]\n"
      "v_mfma_f32_16x16x32_f16 v[32:35], v[192:195], v[20:23], v[32:35]\nv_pk_mul_f16 v26, v58, v60 op_sel_hi:[0,1]\nv_pk_mul_f16 v27, v58, v61 op_sel_hi:[0,1]\n"
      "v_mfma_f32_16x16x32_f16 v[36:39], v[196:199], v[20:23], v[36:39]\nv_pk_mul_f16 v28, v58, v58 op_sel_hi:[0,1]\nds_read_b64_tr_b16 v[62:63], v237 offset:18944\n"
      "v_mfma_f32_16x16x32_f16 v[36:39], v[184:187], v[16:19], v[36:39]\nv_pk_mul_f16 v29, v58, v57 op_sel_hi:[0,1]\nds_read_b64_tr_b16 v[60:61], v237 offset:18816\n"
      "v_mfma_f32_16x16x32_f16 v[244:247], v[188:191], v[16:19], v[244:247]\nv_pk_mul_f16 v30, v56, v58 op_sel_hi:[0,1]\nv_mfma_f32_16x16x32_f16 v[244:247], v[200:203], v[20:23], v[244:247]\n"
      "v_pk_mul_f16 v31, v56, v59 op_sel_hi:[0,1]\nv_smfmac_f32_16x16x64_f16 v[44:47], v[204:207], v[16:23], v242\nds_read_b64_tr_b16 v[56:57], v236 offset:18560\n"
      "ds_read_b64_tr_b16 v[58:59], v237 offset:18688\nv_smfmac_f32_16x16x64_f16 v[44:47], v[232:235], v[24:31], v242\nv_pk_mul_f16 v16, v68, s48 op_sel:[0,1]\n"
      "v_pk_mul_f16 v17, v69, s48 op_sel:[0,1]\nv_mfma_f32_16x16x32_f16 v[32:35], v[208:211], v[24:27], v[32:35]\nv_pk_mul_f16 v18, v70, s48 op_sel:[0,1]\nv_pk_mul_f16 v19, v71, s48 op_sel:[0,1]\n"
      "v_mfma_f32_16x16x32_f16 v[32:35], v[220:223], v[28:31], v[32:35]\nv_pk_mul_f16 v20, v68, v70\nv_pk_mul_f16 v21, v69, v71\nv_mfma_f32_16x16x32_f16 v[36:39], v[224:227], v[28:31], v[36:39]\n"
      "v_add_f32 v44, v44, v45\nv_add_f32 v46, v46, v47\nv_mfma_f32_16x16x32_f16 v[36:39], v[212:215], v[24:27], v[36:39]\nv_mul_f32 v55, v44, v54\nv_mul_f32 v55, v55, v46\n"
      "ds_read_b128 v[44:47], v248 offset:48\nv_mfma_f32_16x16x32_f16 v[244:247], v[216:219], v[24:27], v[244:247]\ns_waitcnt lgkmcnt(1)\nv_pk_mul_f16 v22, v56, v56 op_sel_hi:[0,1]\n"
      "v_pk_mul_f16 v23, v56, v57 op_sel_hi:[0,1]\nv_mfma_f32_16x16x32_f16 v[244:247], v[228:231], v[28:31], v[244:247]\ns_cmp_lt_u32 s45, s53\nv_mul_f32 v32, v32, v48\nv_mul_f32 v33, v33, v49\n"
      "v_mul_f32 v34, v34, v50\ns_cbranch_scc0 .Lc_s9\n"
      ".Lc_u10:\nv_mfma_f32_16x16x32_f16 v[40:43], v[76:79], v[16:19], 0\nds_read_b32 v4, v248\nv_fmac_f32 v55, v32, v36\nv_mul_f32 v35, v35, v51\nv_fmac_f32 v55, v33, v37\n"
      "v_mfma_f32_16x16x32_f16 v[40:43], v[88:91], v[20:23], v[40:43]\nv_fmac_f32 v55, v34, v38\nv_fmac_f32 v55, v35, v39\ns_add_u32 s45, s45, 2\ns_cmp_ge_u32 s45, s46\ns_cbranch_scc0 .Lc_b10\n"
      "s_swappc_b64 s[4:5], s[34:35]\n"
      ".Lc_b10:\nv_mfma_f32_16x16x32_f16 v[36:39], v[84:87], v[20:23], 0\nv_pk_mul_f16 v24, v66, v66 op_sel_hi:[0,1]\nv_pk_mul_f16 v25, v66, v57 op_sel_hi:[0,1]\n"
      "v_mfma_f32_16x16x32_f16 v[36:39], v[72:75], v[16:19], v[36:39]\nv_pk_mul_f16 v26, v56, v66 op_sel_hi:[0,1]\nv_pk_mul_f16 v27, v56, v67 op_sel_hi:[0,1]\ns_setprio 0\n"
      "v_mfma_f32_16x16x32_f16 v[32:35], v[80:83], v[20:23], 0\nv_pk_mul_f16 v28, v58, v66 op_sel_hi:[0,1]\nv_pk_mul_f16 v29, v58, v67 op_sel_hi:[0,1]\ns_waitcnt lgkmcnt(0)\n"
      "v_smfmac_f32_16x16x64_f16 v[44:47], v[92:95], v[16:23], v242\nv_pk_mul_f16 v30, v60, v66 op_sel_hi:[0,1]\nv_pk_mul_f16 v31, v60, v67 op_sel_hi:[0,1]\n"
      "v_mfma_f32_16x16x32_f16 v[32:35], v[96:99], v[24:27], v[32:35]\nv_pk_mul_f16 v16, v62, v66 op_sel_hi:[0,1]\nv_pk_mul_f16 v17, v62, v67 op_sel_hi:[0,1]\ns_setprio 1\n"
      "v_mfma_f32_16x16x32_f16 v[32:35], v[108:111], v[28:31], v[32:35]\nv_pk_mul_f16 v18, v64, v66 op_sel_hi:[0,1]\nv_mul_f32 v244, v244, v52\n"
      "v_mfma_f32_16x16x32_f16 v[36:39], v[112:115], v[28:31], v[36:39]\nv_pk_mul_f16 v19, v64, v67 op_sel_hi:[0,1]\nv_mul_f32 v245, v245, v53\n"
      "v_mfma_f32_16x16x32_f16 v[36:39], v[100:103], v[24:27], v[36:39]\nv_pk_mul_f16 v20, v64, v64 op_sel_hi:[0,1]\nv_fmac_f32 v55, v244, v246\n"
      "v_mfma_f32_16x16x32_f16 v[40:43], v[104:107], v[24:27], v[40:43]\nv_pk_mul_f16 v21, v64, v57 op_sel_hi:[0,1]\nv_fmac_f32 v55, v245, v247\n"
      "v_mfma_f32_16x16x32_f16 v[40:43], v[116:119], v[28:31], v[40:43]\nv_pk_mul_f16 v22, v56, v64 op_sel_hi:[0,1]\nv_readfirstlane_b32 s44, v4\n"
      "v_smfmac_f32_16x16x64_f16 v[44:47], v[120:123], v[24:31], v242\nv_pk_mul_f16 v23, v56, v65 op_sel_hi:[0,1]\nv_mfma_f32_16x16x32_f16 v[32:35], v[124:127], v[16:19], v[32:35]\n"
      "v_pk_mul_f16 v24, v58, v64 op_sel_hi:[0,1]\nv_pk_mul_f16 v25, v58, v65 op_sel_hi:[0,1]\ns_setprio 2\nv_mfma_f32_16x16x32_f16 v[32:35], v[136:139], v[20:23], v[32:35]\n"
      "v_pk_mul_f16 v26, v60, v64 op_sel_hi:[0,1]\nv_mfma_f32_16x16x32_f16 v[36:39], v[140:143], v[20:23], v[36:39]\nv_pk_mul_f16 v27, v60, v65 op_sel_hi:[0,1]\n"
      "v_mfma_f32_16x16x32_f16 v[36:39], v[128:131], v[16:19], v[36:39]\nv_pk_mul_f16 v28, v62, v64 op_sel_hi:[0,1]\nv_add_f32_dpp v55, v55, v55 quad_perm:[1,0,3,2] row_mask:0xf bank_mask:0xf\n"
      "v_mfma_f32_16x16x32_f16 v[40:43], v[132:135], v[16:19], v[40:43]\nv_pk_mul_f16 v29, v62, v65 op_sel_hi:[0,1]\nv_add_f32_dpp v55, v55, v55 quad_perm:[2,3,0,1] row_mask:0xf bank_mask:0xf\n"
      "v_mfma_f32_16x16x32_f16 v[40:43], v[144:147], v[20:23], v[40:43]\nv_pk_mul_f16 v30, v62, v62 op_sel_hi:[0,1]\nv_add_f32_dpp v55, v55, v55 row_half_mirror row_mask:0xf bank_mask:0xf\n"
      "v_smfmac_f32_16x16x64_f16 v[44:47], v[148:151], v[16:23], v242\nv_pk_mul_f16 v31, v62, v57 op_sel_hi:[0,1]\nv_add_f32_dpp v55, v55, v55 row_mirror row_mask:0xf bank_mask:0xf\n"
      "v_mfma_f32_16x16x32_f16 v[32:35], v[152:155], v[24:27], v[32:35]\nv_pk_mul_f16 v16, v56, v62 op_sel_hi:[0,1]\nv_pk_mul_f16 v17, v56, v63 op_sel_hi:[0,1]\nds_write_b32 v243, v55 offset:1152\n"
      "v_mfma_f32_16x16x32_f16 v[32:35], v[164:167], v[28:31], v[32:35]\nv_pk_mul_f16 v18, v58, v62 op_sel_hi:[0,1]\nv_mfma_f32_16x16x32_f16 v[36:39], v[168:171], v[28:31], v[36:39]\n"
      "v_pk_mul_f16 v19, v58, v63 op_sel_hi:[0,1]\nv_mfma_f32_16x16x32_f16 v[36:39], v[156:159], v[24:27], v[36:39]\nv_pk_mul_f16 v20, v60, v62 op_sel_hi:[0,1]\ns_setprio 3\n"
      "s_ff0_i32_b32 s53, s44\nv_mfma_f32_16x16x32_f16 v[40:43], v[160:163], v[24:27], v[40:43]\nv_pk_mul_f16 v21, v60, v63 op_sel_hi:[0,1]\n"
      "v_mfma_f32_16x16x32_f16 v[40:43], v[172:175], v[28:31], v[40:43]\nv_pk_mul_f16 v22, v60, v60 op_sel_hi:[0,1]\ns_add_u32 s53, s53, s64\n"
      "v_smfmac_f32_16x16x64_f16 v[44:47], v[176:179], v[24:31], v242\nv_pk_mul_f16 v23, v60, v57 op_sel_hi:[0,1]\nv_mfma_f32_16x16x32_f16 v[32:35], v[180:183], v[16:19], v[32:35]\n"
      "v_pk_mul_f16 v24, v56, v60 op_sel_hi:[0,1]\nv_pk_mul_f16 v25, v56, v61 op_sel_hi:[0,1]\nv_mfma_f32_16x16x32_f16 v[32:35], v[192:195], v[20:23], v[32:35]\n"
      "v_pk_mul_f16 v26, v58, v60 op_sel_hi:[0,1]\nv_pk_mul_f16 v27, v58, v61 op_sel_hi:[0,1]\nv_mfma_f32_16x16x32_f16 v[36:39], v[196:199], v[20:23], v[36:39]\n"
      "v_pk_mul_f16 v28, v58, v58 op_sel_hi:[0,1]\nv_mfma_f32_16x16x32_f16 v[36:39], v[184:187], v[16:19], v[36:39]\nv_pk_mul_f16 v29, v58, v57 op_sel_hi:[0,1]\n"
      "v_mfma_f32_16x16x32_f16 v[40:43], v[188:191], v[16:19], v[40:43]\nv_pk_mul_f16 v30, v56, v58 op_sel_hi:[0,1]\nv_mfma_f32_16x16x32_f16 v[40:43], v[200:203], v[20:23], v[40:43]\n"
      "v_pk_mul_f16 v31, v56, v59 op_sel_hi:[0,1]\nv_smfmac_f32_16x16x64_f16 v[44:47], v[204:207], v[16:23], v242\ns_nop 0\nv_smfmac_f32_16x16x64_f16 v[44:47], v[232:235], v[24:31], v242\ns_nop 1\n"
      "v_mfma_f32_16x16x32_f16 v[32:35], v[208:211], v[24:27], v[32:35]\ns_nop 1\nv_mfma_f32_16x16x32_f16 v[32:35], v[220:223], v[28:31], v[32:35]\ns_nop 1\n"
      "v_mfma_f32_16x16x32_f16 v[36:39], v[224:227], v[28:31], v[36:39]\nv_add_f32 v44, v44, v45\nv_add_f32 v46, v46, v47\nv_mfma_f32_16x16x32_f16 v[36:39], v[212:215], v[24:27], v[36:39]\n"
      "v_mul_f32 v55, v44, v54\nv_mul_f32 v55, v55, v46\nds_read_b128 v[44:47], v248 offset:48\nv_mfma_f32_16x16x32_f16 v[40:43], v[216:219], v[24:27], v[40:43]\ns_nop 2\n"
      "v_mfma_f32_16x16x32_f16 v[40:43], v[228:231], v[28:31], v[40:43]\nv_mul_f32 v32, v32, v48\nv_mul_f32 v33, v33, v49\nv_mul_f32 v34, v34, v50\n"
      ".Lc_slow:\ns_sub_u32 s49, s45, s64\ns_lshr_b32 s50, s44, 16\ns_or_b32 s50, s50, s56\ns_ashr_i32 s50, s50, s49\ns_cmp_lg_u32 s50, -1\ns_cbranch_scc1 .Lc_more\n"
      ".Lc_fin:\ns_sub_u32 s50, s45, s33\ns_bitcmp1_b32 s50, 1\ns_cselect_b64 vcc, -1, 0\ns_lshl_b32 s50, s50, 6\ns_sub_u32 s50, s50, 128\nv_fmac_f32 v55, v32, v36\nv_mul_f32 v35, v35, v51\n"
      "v_fmac_f32 v55, v33, v37\nv_fmac_f32 v55, v34, v38\nv_fmac_f32 v55, v35, v39\nv_cndmask_b32 v40, v244, v40, vcc\nv_cndmask_b32 v41, v245, v41, vcc\nv_cndmask_b32 v42, v246, v42, vcc\n"
      "v_cndmask_b32 v43, v247, v43, vcc\nv_mul_f32 v40, v40, v52\nv_mul_f32 v41, v41, v53\nv_fmac_f32 v55, v40, v42\nv_fmac_f32 v55, v41, v43\nv_add_u32 v243, s50, v243\ns_nop 0\n"
      "v_add_f32_dpp v55, v55, v55 quad_perm:[1,0,3,2] row_mask:0xf bank_mask:0xf\ns_nop 1\nv_add_f32_dpp v55, v55, v55 quad_perm:[2,3,0,1] row_mask:0xf bank_mask:0xf\ns_nop 1\n"
      "v_add_f32_dpp v55, v55, v55 row_half_mirror row_mask:0xf bank_mask:0xf\ns_nop 1\nv_add_f32_dpp v55, v55, v55 row_mirror row_mask:0xf bank_mask:0xf\nds_write_b32 v243, v55\n"
      "s_cmp_gt_u32 s24, 1\ns_waitcnt lgkmcnt(0)\ns_barrier\ns_cbranch_scc1 .Lc_end\nds_read_b128 v[4:7], v10\nds_read_b32 v3, v11\nds_read_b128 v[16:19], v13\nds_read_b32 v2, v248\n"
      "s_mov_b32 vcc_lo, 0x88888888\ns_mov_b32 vcc_hi, vcc_lo\ns_cmp_eq_u32 s24, 0\ns_cselect_b32 s43, vcc_lo, 0\ns_cselect_b32 s42, vcc_lo, 0x88888\nv_mov_b32 v9, s38\ns_waitcnt lgkmcnt(2)\n"
      "v_add_f32 v4, v4, v5\nv_add_f32 v6, v6, v7\nv_add_f32 v4, v4, v6\nv_ldexp_f32 v4, v4, v3\ns_waitcnt lgkmcnt(1)\nv_add_f32 v17, v17, v18\nv_add_f32 v17, v17, v19\nv_add_f32 v4, v4, v16\n"
      "v_cndmask_b32 v17, v17, v9, vcc\nv_add_f32 v4, v4, v17\ns_waitcnt lgkmcnt(0)\nv_and_b32 v2, 0xff, v2\nv_add_f32_dpp v4, v4, v4 quad_perm:[1,0,3,2] row_mask:0xf bank_mask:0xf\n"
      "v_lshrrev_b32 v2, v14, v2\nv_and_or_b32 v2, v2, 1, v15\nv_add_f32_dpp v4, v4, v4 quad_perm:[2,3,0,1] row_mask:0xf bank_mask:0xf\nv_cmp_ne_u32 vcc, 0, v2\nv_bfe_u32 v8, v19, 3, 12\n"
      "v_lshlrev_b32 v8, 1, v8\nv_cvt_pk_bf16_f32 v4, v4, v4\ns_and_b64 exec, vcc, s[42:43]\ns_cbranch_execz .Lc_end\nglobal_store_short v8, v4, s[22:23] sc1\ns_endpgm\n"
      ".Lc_s2:\ns_getpc_b64 s[60:61]\ns_branch .Lc_slow\ns_branch .Lc_u3\n"
      ".Lc_s3:\ns_getpc_b64 s[60:61]\ns_branch .Lc_slow\ns_branch .Lc_u4\n"
      ".Lc_s4:\ns_getpc_b64 s[60:61]\ns_branch .Lc_slow\ns_branch .Lc_u5\n"
      ".Lc_s5:\ns_getpc_b64 s[60:61]\ns_branch .Lc_slow\ns_branch .Lc_u6\n"
      ".Lc_s6:\ns_getpc_b64 s[60:61]\ns_branch .Lc_slow\ns_branch .Lc_u7\n"
      ".Lc_s7:\ns_getpc_b64 s[60:61]\ns_branch .Lc_slow\ns_branch .Lc_u8\n"
      ".Lc_s8:\ns_getpc_b64 s[60:61]\ns_branch .Lc_slow\ns_branch .Lc_u9\n"
      ".Lc_s9:\ns_getpc_b64 s[60:61]\ns_branch .Lc_slow\ns_branch .Lc_u10\n"
      ".Lc_more:\ns_mov_b32 s51, 0\ns_add_u32 s60, s60, 4\ns_addc_u32 s61, s61, 0\n"
      ".Lc_chk:\ns_sub_u32 s49, s45, s64\ns_sub_u32 s53, s68, s64\ns_lshl_b32 s53, -1, s53\ns_lshr_b32 s50, s44, 16\ns_or_b32 s50, s50, s53\ns_ashr_i32 s50, s50, s49\ns_cmp_eq_u32 s50, -1\n"
      "s_cbranch_scc0 .Lc_c1\ns_cmp_lg_u32 s56, 0\ns_cbranch_scc1 .Lc_fin\ns_branch .Lc_c2\n"
      ".Lc_c1:\ns_lshr_b32 s50, s44, s49\ns_and_b32 s50, s50, 0x10001\ns_cbranch_scc1 .Lc_have\n"
      ".Lc_c2:\ns_add_u32 s51, s51, 1\ns_bitcmp1_b32 s51, 22\ns_cbranch_scc1 .Lc_fin\ns_cmp_lg_u32 s56, 0\ns_cbranch_scc1 .Lc_rd\ns_swappc_b64 s[4:5], s[34:35]\n"
      ".Lc_rd:\nds_read_b32 v4, v248\ns_waitcnt lgkmcnt(0)\nv_readfirstlane_b32 s44, v4\ns_branch .Lc_chk\n"
      ".Lc_have:\ns_cmp_eq_u32 s51, 0\ns_cbranch_scc1 .Lc_go\ns_sub_u32 s49, s45, s33\ns_mulk_i32 s49, 928\nv_add_u32 v24, s49, v236\nv_add_u32 v25, s49, v237\nv_add_u32 v26, s49, v238\n"
      "v_add_u32 v27, s49, v239\nds_read_b64_tr_b16 v[68:69], v26 offset:0\nds_read_b64_tr_b16 v[70:71], v27 offset:0\nds_read_b64_tr_b16 v[56:57], v24 offset:0\n"
      "ds_read_b64_tr_b16 v[58:59], v25 offset:128\nds_read_b64_tr_b16 v[60:61], v25 offset:256\nds_read_b64_tr_b16 v[62:63], v25 offset:384\nds_read_b64_tr_b16 v[64:65], v25 offset:512\n"
      "ds_read_b64_tr_b16 v[66:67], v25 offset:640\ns_waitcnt lgkmcnt(0)\nv_readlane_b32 s48, v69, 48\nv_pk_mul_f16 v20, v68, v70\nv_pk_mul_f16 v21, v69, v71\n"
      "v_pk_mul_f16 v22, v56, v56 op_sel_hi:[0,1]\nv_pk_mul_f16 v23, v56, v57 op_sel_hi:[0,1]\nv_pk_mul_f16 v16, v68, s48 op_sel:[0,1]\nv_pk_mul_f16 v17, v69, s48 op_sel:[0,1]\n"
      "v_pk_mul_f16 v18, v70, s48 op_sel:[0,1]\nv_pk_mul_f16 v19, v71, s48 op_sel:[0,1]\n"
      ".Lc_go:\ns_setpc_b64 s[60:61]\n"
      ".Lt_g:\ns_waitcnt lgkmcnt(0)\nv_readfirstlane_b32 s42, v2\ns_mov_b32 s43, 43008\nv_lshl_add_u32 v1, v240, 2, s43\ns_cmp_lg_u32 s42, 3\ns_cbranch_scc1 .Lt_p\nds_read_b32 v3, v248 offset:764\n"
      "ds_read2st64_b32 v[6:7], v1 offset0:0 offset1:4\nds_read2st64_b32 v[8:9], v1 offset0:1 offset1:5\nds_read2st64_b32 v[10:11], v1 offset0:2 offset1:6\n"
      "ds_read2st64_b32 v[12:13], v1 offset0:3 offset1:7\ns_lshl_b32 s42, s28, 8\ns_add_u32 s42, s42, s29\ns_lshl_b32 s42, s42, 7\ns_add_u32 s58, s12, s42\ns_addc_u32 s59, s13, 0\n"
      "v_lshlrev_b32 v14, 3, v240\ns_cmp_eq_u32 s25, 2\ns_cselect_b64 vcc, -1, 0\ns_movk_i32 s42, 0xff00\nv_mov_b32 v1, s41\ns_lshl_b32 s43, s25, 3\ns_waitcnt lgkmcnt(0)\nv_add_f32 v12, v12, v13\n"
      "v_add_f32 v6, v6, v7\nv_add_f32 v8, v8, v9\nv_add_f32 v10, v10, v11\nv_mov_b32 v13, v12\nv_mov_b32 v15, v12\ns_nop 1\nv_permlane16_swap_b32 v13, v15\nv_add_f32 v13, v13, v15\n"
      "v_cndmask_b32 v12, v12, v13, vcc\nv_mul_f32 v6, v3, v6\nv_mul_f32 v8, v3, v8\nv_mul_f32 v10, v3, v10\nv_mul_f32 v12, v3, v12\nv_add_u32 v6, 0x80, v6\nv_and_or_b32 v6, v6, s42, v1\n"
      "v_mov_b32 v7, s40\nv_add_u32 v8, 0x80, v8\nv_and_or_b32 v8, v8, s42, v1\nv_mov_b32 v9, s40\nv_add_u32 v10, 0x80, v10\nv_and_or_b32 v10, v10, s42, v1\nv_mov_b32 v11, s40\n"
      "v_add_u32 v12, 0x80, v12\nv_and_or_b32 v12, v12, s42, v1\nv_mov_b32 v13, s40\nglobal_store_dwordx2 v14, v[6:7], s[58:59] offset:0 sc1\n"
      "global_store_dwordx2 v14, v[8:9], s[58:59] offset:512 sc1\nglobal_store_dwordx2 v14, v[10:11], s[58:59] offset:1024 sc1\ns_cmp_eq_u32 s43, 0\ns_cbranch_scc1 .Lt_p\ns_bfm_b64 exec, s43, 0\n"
      "global_store_dwordx2 v14, v[12:13], s[58:59] offset:1536 sc1\ns_mov_b64 exec, -1\n"
      ".Lt_p:\ns_cmp_gt_u32 s66, 100\ns_cbranch_scc1 .Lt_d\ns_mov_b32 s46, s66\ns_swappc_b64 s[34:35], s[4:5]\ns_lshl_b32 s43, 0x10000, s67\ns_cmp_gt_u32 s44, 0xffff\ns_cbranch_scc1 .Lt_a\n"
      "v_lshlrev_b32 v1, 3, v240\ns_mov_b64 exec, 1\nglobal_atomic_add v5, v1, v241, s[8:9] sc0 sc1\ns_mov_b64 exec, -1\nglobal_load_dwordx2 v[254:255], v249, s[12:13] sc1\n"
      ".Lt_f:\nglobal_load_dwordx2 v[2:3], v1, s[14:15] sc1\ns_swappc_b64 s[34:35], s[4:5]\ns_waitcnt vmcnt(0)\nv_readfirstlane_b32 s50, v5\nv_cmp_eq_u32 vcc, s41, v2\n"
      "v_cmp_eq_u32 s[62:63], s40, v3\nv_mov_b32 v7, s43\ns_cmp_lt_u32 s50, s55\ns_cbranch_scc1 .Lt_r\n"
      ".Lt_e:\nds_add_u32 v253, v7\ns_branch .Lt_c\n"
      ".Lt_r:\ns_add_u32 s50, s50, 1\nv_readlane_b32 s54, v251, s50\ns_and_b64 s[62:63], s[62:63], vcc\ns_bfe_u32 s50, s54, 0x50004\ns_lshr_b32 s49, s54, 9\ns_lshl3_add_u32 s50, s50, s49\n"
      "s_sub_u32 s50, s50, s69\ns_bitcmp0_b64 s[62:63], s50\ns_cbranch_scc1 .Lt_f\ns_mul_i32 s50, s54, 3584\ns_add_u32 s62, s16, s50\ns_addc_u32 s63, s17, 0\ns_add_u32 s50, s64, s67\n"
      "s_mul_i32 s49, s50, 928\ns_add_u32 m0, s49, 45056\ns_mov_b32 exec_hi, 0x7fffff\nglobal_load_lds_dwordx4 v252, s[62:63] offset:0 sc1\ns_add_u32 m0, m0, 21376\ns_lshl_b32 s54, s54, 3\n"
      "global_load_lds_dwordx4 v252, s[62:63] offset:896 sc1\ns_add_u32 m0, m0, 21376\ns_lshl_b32 s50, s50, 6\nglobal_load_lds_dwordx4 v252, s[62:63] offset:1792 sc1\ns_add_u32 m0, m0, 21376\n"
      "s_lshl_b32 s43, 1, s67\nglobal_load_lds_dwordx4 v252, s[62:63] offset:2688 sc1\ns_mov_b32 exec_hi, -1\ns_mov_b32 exec_lo, 0\nv_and_b32 v249, 0xffff8000, v249\ns_add_u32 s50, s50, 136192\n"
      "v_or_b32 v249, s54, v249\nv_and_or_b32 v250, v250, 63, s50\ns_mov_b32 exec_lo, -1\nv_mov_b32 v7, s43\nglobal_load_dwordx2 v[254:255], v249, s[12:13] sc1\ns_swappc_b64 s[34:35], s[4:5]\n"
      "s_waitcnt vmcnt(0)\ns_branch .Lt_e\n"
      ".Lt_a:\nv_mov_b32 v0, s43\nds_add_u32 v253, v0\n"
      ".Lt_d:\ns_sub_u32 s46, s64, 4\ns_cmp_ge_i32 s45, s46\ns_cbranch_scc1 .Lt_l\ns_swappc_b64 s[34:35], s[4:5]\n"
      ".Lt_l:\nglobal_load_dwordx2 v[254:255], v249, s[12:13] sc1\ns_swappc_b64 s[34:35], s[4:5]\ns_waitcnt vmcnt(0)\n"
      ".Lt_c:\nv_and_b32 v6, 0xff, v254\nv_cmp_ne_u32 vcc, s40, v255\nv_cmp_ne_u32 s[62:63], s41, v6\nv_and_b32 v9, 0xffffff00, v254\ns_or_b64 vcc, vcc, s[62:63]\ns_cbranch_vccnz .Lt_d\n"
      "s_mov_b32 vcc_lo, 0x1fff1fff\ns_mov_b32 vcc_hi, vcc_lo\ns_sub_u32 s56, s68, s64\nv_cndmask_b32 v9, v249, v9, vcc\ns_lshl_b32 s56, -1, s56\ns_movk_i32 s46, 99\nds_write_b32 v250, v9\n"
      "s_setpc_b64 s[4:5]\n"
      ".Lc_end:\n"
      ".Lxd_end:"
      :: "{v240}"(lane), "{v253}"(tch), "{v[248:251]}"(mrow), "{v252}"(msc), "{v[254:255]}"(rsp), "{s[4:19]}"(ka0), "{s[20:23]}"(ka1), "{s[24:31]}"(gs), "{s2}"(blk)
      : "memory", "vcc", "scc", "s33", "s34", "s35", "s36", "s37", "s38", "s39", "s40", "s41", "s42", "s43", "s44", "s45", "s46", "s47", "s48", "s49", "s50", "s51", "s52", "s53", "s54", "s55", "s56", "s57", "s58", "s59", "s60", "s61", "s62", "s63", "s64", "s65", "s66", "s67", "s68", "s69",
        "v0", "v1", "v2", "v3", "v4", "v5", "v6", "v7", "v8", "v9", "v10", "v11", "v12", "v13", "v14", "v15", "v16", "v17", "v18", "v19", "v20", "v21", "v22", "v23", "v24", "v25", "v26", "v27",
        "v28", "v29", "v30", "v31", "v32", "v33", "v34", "v35", "v36", "v37", "v38", "v39", "v40", "v41", "v42", "v43", "v44", "v45", "v46", "v47", "v48", "v49", "v50", "v51", "v52", "v53", "v54",
        "v55", "v56", "v57", "v58", "v59", "v60", "v61", "v62", "v63", "v64", "v65", "v66", "v67", "v68", "v69", "v70", "v71", "v72", "v73", "v74", "v75", "v76", "v77", "v78", "v79", "v80", "v81",
        "v82", "v83", "v84", "v85", "v86", "v87", "v88", "v89", "v90", "v91", "v92", "v93", "v94", "v95", "v96", "v97", "v98", "v99", "v100", "v101", "v102", "v103", "v104", "v105", "v106", "v107",
        "v108", "v109", "v110", "v111", "v112", "v113", "v114", "v115", "v116", "v117", "v118", "v119", "v120", "v121", "v122", "v123", "v124", "v125", "v126", "v127", "v128", "v129", "v130",
        "v131", "v132", "v133", "v134", "v135", "v136", "v137", "v138", "v139", "v140", "v141", "v142", "v143", "v144", "v145", "v146", "v147", "v148", "v149", "v150", "v151", "v152", "v153",
        "v154", "v155", "v156", "v157", "v158", "v159", "v160", "v161", "v162", "v163", "v164", "v165", "v166", "v167", "v168", "v169", "v170", "v171", "v172", "v173", "v174", "v175", "v176",
        "v177", "v178", "v179", "v180", "v181", "v182", "v183", "v184", "v185", "v186", "v187", "v188", "v189", "v190", "v191", "v192", "v193", "v194", "v195", "v196", "v197", "v198", "v199",
        "v200", "v201", "v202", "v203", "v204", "v205", "v206", "v207", "v208", "v209", "v210", "v211", "v212", "v213", "v214", "v215", "v216", "v217", "v218", "v219", "v220", "v221", "v222",
        "v223", "v224", "v225", "v226", "v227", "v228", "v229", "v230", "v231", "v232", "v233", "v234", "v235", "v236", "v237", "v238", "v239", "v241", "v242", "v243", "v244", "v245", "v246", "v247");
}

extern "C" void kernel_launch(void* const* d_in, const int* in_sizes, int n_in, void* d_out, int out_size,
                              void* d_ws, size_t ws_size, hipStream_t stream) {
  const int* idx = (const int*)d_in[0];
  const u16* table = (const u16*)d_in[1];
  const u16* ws_cin = (const u16*)d_in[2];
  const u16* wout = (const u16*)d_in[3];
  const u16* w1 = (const u16*)d_in[4];
  const u16* b1 = (const u16*)d_in[5];
  const u16* w2 = (const u16*)d_in[6];
  const u16* b_out = (const u16*)d_in[7];
  dnn_kernel<<<256, 512, 0, stream>>>(idx, table, ws_cin, w1, b1, w2, wout, b_out, (char*)d_ws, (u16*)d_out);
}
```
